```python
import torch

def _softmax(x, axis=-1):
    return torch.softmax(x, dim=axis)

def _gelu(x):
    return torch.nn.functional.gelu(x, approximate='tanh')

def _silu(x):
    return torch.nn.functional.silu(x)

def _sigmoid(x):
    return torch.sigmoid(x)

def _layernorm(x, axis=-1, eps=1e-05):
    m = x.mean(dim=axis, keepdim=True)
    v = x.var(dim=axis, keepdim=True, unbiased=False)
    return (x - m) / torch.sqrt(v + eps)

def _scatter_add(tgt, idx, src, axis=0):
    return tgt.clone().index_add_(axis, idx, src)

def _segment_sum(src, seg, n):
    out = torch.zeros((n,) + src.shape[1:], dtype=src.dtype)
    return out.index_add_(0, seg, src)

def _pad(x, pw):
    flat = tuple((v for pair in reversed(pw) for v in pair))
    return torch.nn.functional.pad(x, flat)
kernel_name = 'standalone__swin_window_attn__vision'

def setup_inputs():

    _w = lambda a: (lambda t: t.to(torch.bfloat16) if t.is_floating_point() else t)(torch.as_tensor(a))
    H = W = 512 // 8 * 8
    C, win = (192, 8)
    return {'x': _w((lambda *s: torch.randn(*s, dtype=torch.bfloat16) * 0.2)(1, H, W, C)), 'wqkv': _w((lambda *s: torch.randn(*s, dtype=torch.bfloat16) * 0.2)(C, 3 * C)), 'wo': _w((lambda *s: torch.randn(*s, dtype=torch.bfloat16) * 0.05)(C, C))}

def reference(x, wqkv, wo):
    B, H, W, C = x.shape
    win = 8
    nh, nw = (H // win, W // win)
    xw = (lambda x, *d: x.permute(*d))(x.reshape(B, nh, win, nw, win, C), 0, 1, 3, 2, 4, 5)
    xw = xw.reshape(B * nh * nw, win * win, C)
    qkv = xw @ wqkv
    q, k, v = (qkv[..., :C].clone(), qkv[..., C:2 * C].clone(), qkv[..., 2 * C:].clone())
    scale = C ** (-0.5)
    p = _softmax(torch.einsum('bqc,bkc->bqk', q, k) * scale, axis=-1)
    o = torch.einsum('bqk,bkc->bqc', p, v) @ wo
    o = (lambda x, *d: x.permute(*d))(o.reshape(B, nh, nw, win, win, C), 0, 1, 3, 2, 4, 5)
    return o.reshape(B, H, W, C)
```

```cpp
#define XMAP
#include <hip/hip_runtime.h>
#include <hip/hip_bf16.h>
#include <stdint.h>

using u32x4 = __attribute__((ext_vector_type(4))) unsigned;
using u32x8 = __attribute__((ext_vector_type(8))) unsigned;
#define DI __device__ __forceinline__

static constexpr int ROWB = 512 * 384;
static constexpr int STEPB = 64 * ROWB;
static constexpr unsigned XBYTES = 512u * ROWB;
static constexpr int SLOT = 24576;
static constexpr int WQ = 36864;
static constexpr int PB = 6 * SLOT;
static constexpr int N_OFF = 0, F_OFF = 80 * 1024, A_OFF = 84 * 1024, E_OFF = 88 * 1024;
static constexpr unsigned WS_BYTES = 96 * 1024;

DI unsigned chan(unsigned b, unsigned p) { return 8 * (p >> 2) + 4 * b + (p & 3); }

#define R10(p, n) p #n "0", p #n "1", p #n "2", p #n "3", p #n "4", p #n "5", p #n "6", p #n "7", p #n "8", p #n "9"
#define R240(p) R10(p, 4), R10(p, 5), R10(p, 6), R10(p, 7), R10(p, 8), R10(p, 9), R10(p, 10), R10(p, 11), R10(p, 12), R10(p, 13), \
  R10(p, 14), R10(p, 15), R10(p, 16), R10(p, 17), R10(p, 18), R10(p, 19), R10(p, 20), R10(p, 21), R10(p, 22), R10(p, 23), R10(p, 24), \
  p "250", p "251", p "252", p "253", p "254", p "255"
#define ACLOB R10("a", ), R10("a", 1), R10("a", 2), R10("a", 3), R240("a")
#define VCLOB R10("v", ), R10("v", 1), "v20", "v21", "v22", "v23", "v27", "v28", "v29", "v30", "v32", "v33", "v34", "v35", "v36", "v37", "v38", "v39", R240("v")
#define SCLOB "s48", "s49", R10("s", 5), R10("s", 6), R10("s", 7), R10("s", 8), "s90", "s91", "s92", "s93", "s94", "s95", "vcc"

__global__ __launch_bounds__(256, 1) void attn_kernel(const unsigned char* __restrict__ x,
                                                      const unsigned char* __restrict__ wqkv,
                                                      const unsigned char* __restrict__ wo,
                                                      unsigned char* ws,
                                                      unsigned char* __restrict__ out
                                                      ) {
  __shared__ u32x4 lds[10240];
  u32x8 k8; u32x4 k4;
  asm volatile(
    "s_load_dwordx8 s[36:43], %2, 0\n\t"
    "s_load_dwordx2 s[44:45], %2, 32\n\t"
    : "={s[36:43]}"(k8), "={s[44:47]}"(k4) : "s"(__builtin_amdgcn_kernarg_segment_ptr()) : "memory", ACLOB);
  __builtin_amdgcn_sched_barrier(0);
  const unsigned lane = threadIdx.x & 63, wave = __builtin_amdgcn_readfirstlane(threadIdx.x >> 6);
  const unsigned role = wave >> 1, pair = wave & 1;
  const unsigned r3 = lane >> 3, ch = lane & 7;
  const unsigned ldsb = (unsigned)(size_t)lds;
  const unsigned bid = blockIdx.x;
  const unsigned vk0 = chan(0, r3) * 1152 + (ch ^ (r3 >> 1)) * 16, vk1 = (vk0 ^ 64) + 16 * 1152;
  const unsigned c0 = 3 * wave, c1 = c0 + 1;
  const unsigned k0 = (32 * (c0 >> 1) + 4 * (c0 & 1)) * 1152 + 384, k1 = (32 * (c1 >> 1) + 4 * (c1 & 1)) * 1152 + 384;
  const unsigned wid = (bid >> 3) * 4 + wave;
  const unsigned w0 = (bid & 7) * 64 + (bid >> 3) * 2 + pair;
  const unsigned wb = (w0 >> 6) * 8 * ROWB + (w0 & 63) * 8 * 384;
  const unsigned rbeg = ldsb + pair * 5 * SLOT, rd = SLOT - pair * 2 * SLOT;

  asm volatile(
    "s_mov_b32 s50, 0x36000\n\t"
    "s_mov_b32 s51, 0x20000\n\t"
    "s_mov_b32 s54, 0x12000\n\t"
    "s_mov_b32 s55, 0x20000\n\t"
    "s_mov_b32 s58, 0x18000\n\t"
    "s_mov_b32 s59, 0x20000\n\t"
    "s_mov_b32 s63, 0x20000\n\t"
    "s_cmp_lt_u32 %[tch], 96\n\t"
    "s_cselect_b32 s62, s50, s54\n\t"
    "s_cselect_b32 s64, 0, 96\n\t"
    "s_sub_u32 s64, %[tch], s64\n\t"
    "s_mul_i32 s64, s64, 2304\n\t"
    "s_cmp_lt_u32 %[tch], 96\n\t"
    "s_waitcnt lgkmcnt(0)\n\t"
    "s_cselect_b64 s[60:61], s[38:39], s[40:41]\n\t"
    "s_and_b32 s61, s61, 0xffff\n\t"
    "s_mov_b32 s48, s38\n\t"
    "s_and_b32 s49, s39, 0xffff\n\t"
    "s_cmp_lg_u32 %[prod], 0\n\t"
    "s_mov_b64 exec, 0x3ffff\n\t"
    "buffer_load_dword a255, v26, s[60:63], s64 offen\n\t"
    "s_mov_b64 exec, -1\n\t"
    "s_cbranch_scc1 .Lpl_%=\n\t"
    ".Lpb_%=:\n\t"
    "s_add_u32 m0, %[kl], 0\n\t"
    "s_nop 0\n\t"
    "buffer_load_dwordx4 v24, s[48:51], %[k0] offen offset:0 lds\n\t"
    "s_add_u32 m0, %[kl], 1024\n\t"
    "s_nop 0\n\t"
    "buffer_load_dwordx4 v25, s[48:51], %[k0] offen offset:0 lds\n\t"
    "s_add_u32 m0, %[kl], 1920\n\t"
    "s_nop 0\n\t"
    "buffer_load_dwordx4 v24, s[48:51], %[k0] offen offset:128 lds\n\t"
    "s_add_u32 m0, %[kl], 2944\n\t"
    "s_nop 0\n\t"
    "buffer_load_dwordx4 v25, s[48:51], %[k0] offen offset:128 lds\n\t"
    "s_add_u32 m0, %[kl], 3840\n\t"
    "s_nop 0\n\t"
    "buffer_load_dwordx4 v24, s[48:51], %[k0] offen offset:256 lds\n\t"
    "s_add_u32 m0, %[kl], 4864\n\t"
    "s_nop 0\n\t"
    "buffer_load_dwordx4 v25, s[48:51], %[k0] offen offset:256 lds\n\t"
    "s_add_u32 m0, %[kl], 6144\n\t"
    "s_nop 0\n\t"
    "buffer_load_dwordx4 v24, s[48:51], %[k1] offen offset:0 lds\n\t"
    "s_add_u32 m0, %[kl], 7168\n\t"
    "s_nop 0\n\t"
    "buffer_load_dwordx4 v25, s[48:51], %[k1] offen offset:0 lds\n\t"
    "s_add_u32 m0, %[kl], 8064\n\t"
    "s_nop 0\n\t"
    "buffer_load_dwordx4 v24, s[48:51], %[k1] offen offset:128 lds\n\t"
    "s_add_u32 m0, %[kl], 9088\n\t"
    "s_nop 0\n\t"
    "buffer_load_dwordx4 v25, s[48:51], %[k1] offen offset:128 lds\n\t"
    "s_mov_b32 s56, s42\n\t"
    "s_and_b32 s57, s43, 0xffff\n\t"
    "s_cmp_eq_u32 %[role], 0\n\t"
    "s_cbranch_scc1 .Lrolm_%=\n\t"
    "v_lshlrev_b32_e32 v30, 4, v31\n\t"
    "v_and_b32_e32 v28, 7, v31\n\t"
    "v_bfe_u32 v27, v31, 3, 1\n\t"
    "v_lshrrev_b32_e32 v29, 4, v31\n\t"
    "v_bfe_u32 v20, v31, 1, 3\n\t"
    "v_xor_b32_e32 v20, v20, v29\n\t"
    "v_lshlrev_b32_e32 v21, 7, v28\n\t"
    "v_lshl_or_b32 v21, v27, 11, v21\n\t"
    "v_lshl_or_b32 v21, v20, 4, v21\n\t"
    "v_add_u32_e32 v11, %[tb], v30\n\t"
    "v_lshlrev_b32_e32 v9, 10, v27\n\t"
    "v_sub_u32_e32 v9, v21, v9\n\t"
    "v_add_u32_e32 v9, %[kl], v9\n\t"
    "v_xor_b32_e32 v10, 64, v9\n\t"
    "v_add_u32_e32 v12, 0x9000, v21\n\t"
    "v_xor_b32_e32 v13, 64, v12\n\t"
    "v_add_u32_e32 v14, %[wn], v30\n\t"
    "v_add_u32_e32 v6, %[pl], v30\n\t"
    "v_mul_u32_u24_e32 v27, 0xc00, v27\n\t"
    "v_add_u32_e32 v27, %[rbeg], v27\n\t"
    "v_mad_u32_u24 v20, v28, 3, 0\n\t"
    "v_and_b32_e32 v21, 6, v20\n\t"
    "v_xor_b32_e32 v21, v21, v29\n\t"
    "v_lshl_add_u32 v20, v20, 7, v27\n\t"
    "v_lshl_add_u32 v0, v21, 4, v20\n\t"
    "v_xor_b32_e32 v1, 64, v0\n\t"
    "v_mad_u32_u24 v20, v28, 3, 1\n\t"
    "v_and_b32_e32 v21, 6, v20\n\t"
    "v_xor_b32_e32 v21, v21, v29\n\t"
    "v_lshl_add_u32 v20, v20, 7, v27\n\t"
    "v_lshl_add_u32 v2, v21, 4, v20\n\t"
    "v_xor_b32_e32 v3, 64, v2\n\t"
    "v_mad_u32_u24 v20, v28, 3, 2\n\t"
    "v_and_b32_e32 v21, 6, v20\n\t"
    "v_xor_b32_e32 v21, v21, v29\n\t"
    "v_lshl_add_u32 v20, v20, 7, v27\n\t"
    "v_lshl_add_u32 v4, v21, 4, v20\n\t"
    "v_xor_b32_e32 v5, 64, v4\n\t"
    "s_add_u32 m0, %[kl], 9984\n\t"
    "s_nop 0\n\t"
    "buffer_load_dwordx4 v24, s[48:51], %[k1] offen offset:256 lds\n\t"
    "s_add_u32 m0, %[kl], 11008\n\t"
    "s_nop 0\n\t"
    "buffer_load_dwordx4 v25, s[48:51], %[k1] offen offset:256 lds\n\t"
    "s_add_u32 m0, %[kl], 12288\n\t"
    "s_nop 0\n\t"
    "buffer_load_dwordx4 v24, s[48:51], %[k2] offen offset:0 lds\n\t"
    "s_add_u32 m0, %[kl], 13312\n\t"
    "s_nop 0\n\t"
    "buffer_load_dwordx4 v25, s[48:51], %[k2] offen offset:0 lds\n\t"
    "s_add_u32 m0, %[kl], 14208\n\t"
    "s_nop 0\n\t"
    "buffer_load_dwordx4 v24, s[48:51], %[k2] offen offset:128 lds\n\t"
    "s_add_u32 m0, %[kl], 15232\n\t"
    "s_nop 0\n\t"
    "buffer_load_dwordx4 v25, s[48:51], %[k2] offen offset:128 lds\n\t"
    "s_add_u32 m0, %[kl], 16128\n\t"
    "s_nop 0\n\t"
    "buffer_load_dwordx4 v24, s[48:51], %[k2] offen offset:256 lds\n\t"
    "s_add_u32 m0, %[kl], 17152\n\t"
    "s_nop 0\n\t"
    "buffer_load_dwordx4 v25, s[48:51], %[k2] offen offset:256 lds\n\t"
    "s_add_u32 m0, %[wql], 0\n\t"
    "s_add_u32 s61, %[wqg], 0\n\t"
    "buffer_load_dwordx4 v24, s[48:51], s61 offen offset:0 lds\n\t"
    "s_add_u32 m0, %[wql], 3968\n\t"
    "v_lshrrev_b32_e32 v20, 5, v31\n\t"
    "buffer_load_dwordx4 v24, s[48:51], s61 offen offset:128 lds\n\t"
    "s_add_u32 m0, %[wql], 7936\n\t"
    "v_mul_u32_u24_e32 v20, 0x30000, v20\n\t"
    "buffer_load_dwordx4 v24, s[48:51], s61 offen offset:256 lds\n\t"
    "s_add_u32 m0, %[wql], 2048\n\t"
    "v_and_b32_e32 v21, 1, v29\n\t"
    "buffer_load_dwordx4 v25, s[48:51], s61 offen offset:0 lds\n\t"
    "s_add_u32 m0, %[wql], 6016\n\t"
    "v_mul_u32_u24_e32 v21, 0x600, v21\n\t"
    "buffer_load_dwordx4 v25, s[48:51], s61 offen offset:128 lds\n\t"
    "s_add_u32 m0, %[wql], 9984\n\t"
    "v_add_u32_e32 v20, v20, v21\n\t"
    "buffer_load_dwordx4 v25, s[48:51], s61 offen offset:256 lds\n\t"
    "s_add_u32 m0, %[wql], 12288\n\t"
    "s_add_u32 s61, %[wqg], 36864\n\t"
    "buffer_load_dwordx4 v24, s[48:51], s61 offen offset:0 lds\n\t"
    "s_add_u32 m0, %[wql], 16256\n\t"
    "v_and_b32_e32 v21, 15, v31\n\t"
    "buffer_load_dwordx4 v24, s[48:51], s61 offen offset:128 lds\n\t"
    "s_add_u32 m0, %[wql], 20224\n\t"
    "v_lshl_add_u32 v7, v21, 4, v20\n\t"
    "buffer_load_dwordx4 v24, s[48:51], s61 offen offset:256 lds\n\t"
    "s_add_u32 m0, %[wql], 14336\n\t"
    "v_lshl_add_u32 v8, v21, 3, v20\n\t"
    "buffer_load_dwordx4 v25, s[48:51], s61 offen offset:0 lds\n\t"
    "s_add_u32 m0, %[wql], 18304\n\t"
    "v_add_u32_e32 v8, 0x100, v8\n\t"
    "buffer_load_dwordx4 v25, s[48:51], s61 offen offset:128 lds\n\t"
    "s_add_u32 m0, %[wql], 22272\n\t"
    "v_and_b32_e32 v20, 48, v30\n\t"
    "buffer_load_dwordx4 v25, s[48:51], s61 offen offset:256 lds\n\t"
    "s_add_u32 m0, %[wql], 24576\n\t"
    "s_add_u32 s61, %[wqg], 73728\n\t"
    "buffer_load_dwordx4 v24, s[48:51], s61 offen offset:0 lds\n\t"
    "s_add_u32 m0, %[wql], 28544\n\t"
    "v_lshl_or_b32 v20, v29, 6, v20\n\t"
    "buffer_load_dwordx4 v24, s[48:51], s61 offen offset:128 lds\n\t"
    "s_add_u32 m0, %[wql], 32512\n\t"
    "v_lshrrev_b32_e32 v21, 2, v21\n\t"
    "buffer_load_dwordx4 v24, s[48:51], s61 offen offset:256 lds\n\t"
    "s_add_u32 m0, %[wql], 26624\n\t"
    "v_lshl_or_b32 v15, v21, 8, v20\n\t"
    "buffer_load_dwordx4 v25, s[48:51], s61 offen offset:0 lds\n\t"
    "s_add_u32 m0, %[wql], 30592\n\t"
    "v_min_u32_e32 v21, 35, v31\n\t"
    "buffer_load_dwordx4 v25, s[48:51], s61 offen offset:128 lds\n\t"
    "s_add_u32 m0, %[wql], 34560\n\t"
    "v_lshlrev_b32_e32 v21, 4, v21\n\t"
    "buffer_load_dwordx4 v25, s[48:51], s61 offen offset:256 lds\n\t"
    "s_add_u32 m0, %[wql], 36864\n\t"
    "s_add_u32 s61, %[wqg], 110592\n\t"
    "buffer_load_dwordx4 v24, s[48:51], s61 offen offset:0 lds\n\t"
    "s_add_u32 m0, %[wql], 40832\n\t"
    "v_add_u32_e32 v21, 0x14000, v21\n\t"
    "buffer_load_dwordx4 v24, s[48:51], s61 offen offset:128 lds\n\t"
    "s_add_u32 m0, %[wql], 44800\n\t"
    "s_mov_b32 s52, s44\n\t"
    "buffer_load_dwordx4 v24, s[48:51], s61 offen offset:256 lds\n\t"
    "s_add_u32 m0, %[wql], 38912\n\t"
    "s_and_b32 s53, s45, 0xffff\n\t"
    "buffer_load_dwordx4 v25, s[48:51], s61 offen offset:0 lds\n\t"
    "s_add_u32 m0, %[wql], 42880\n\t"
    "s_mov_b32 s54, 0x6000000\n\t"
    "buffer_load_dwordx4 v25, s[48:51], s61 offen offset:128 lds\n\t"
    "s_add_u32 m0, %[wql], 46848\n\t"
    "s_mov_b32 s55, 0x20000\n\t"
    "buffer_load_dwordx4 v25, s[48:51], s61 offen offset:256 lds\n\t"
    "buffer_load_dwordx2 v[22:23], off, s[56:59], %[eoff] sc1\n\t"
    "s_waitcnt vmcnt(37)\n\t"
    "ds_read_b128 v[116:119], v9 offset:0\n\t"
    "ds_read_b128 v[120:123], v10 offset:0\n\t"
    "ds_read_b128 v[124:127], v9 offset:2048\n\t"
    "ds_read_b128 v[128:131], v10 offset:2048\n\t"
    "ds_read_b128 v[132:135], v9 offset:4096\n\t"
    "ds_read_b128 v[136:139], v10 offset:4096\n\t"
    "s_waitcnt vmcnt(31)\n\t"
    "ds_read_b128 v[140:143], v9 offset:6144\n\t"
    "ds_read_b128 v[144:147], v10 offset:6144\n\t"
    "ds_read_b128 v[148:151], v9 offset:8192\n\t"
    "ds_read_b128 v[152:155], v10 offset:8192\n\t"
    "ds_read_b128 v[156:159], v9 offset:10240\n\t"
    "ds_read_b128 v[160:163], v10 offset:10240\n\t"
    "s_waitcnt vmcnt(25)\n\t"
    "ds_read_b128 v[164:167], v9 offset:12288\n\t"
    "ds_read_b128 v[168:171], v10 offset:12288\n\t"
    "ds_read_b128 v[172:175], v9 offset:14336\n\t"
    "ds_read_b128 v[176:179], v10 offset:14336\n\t"
    "ds_read_b128 v[180:183], v9 offset:16384\n\t"
    "ds_read_b128 v[184:187], v10 offset:16384\n\t"
    "s_mov_b32 s67, 2\n\t"
    "s_waitcnt vmcnt(19) lgkmcnt(0)\n\t"
    "s_barrier\n\t"
    "ds_read_b128 v[32:35], v12 offset:0\n\t"
    "ds_read_b128 v[36:39], v13 offset:0\n\t"
    "ds_read_b128 v[40:43], v12 offset:4096\n\t"
    "ds_read_b128 v[44:47], v13 offset:4096\n\t"
    "ds_read_b128 v[48:51], v12 offset:8192\n\t"
    "ds_read_b128 v[52:55], v13 offset:8192\n\t"
    "s_mov_b32 s60, 6\n\t"
    "s_branch .Lf7_%=\n\t"
    ".Lfs1_%=:\n\t"
    "s_waitcnt vmcnt(0)\n\t"
    "v_add_co_u32_e32 v22, vcc, 0x7f4a7c15, v22\n\t"
    "v_mov_b32_e32 v16, 0x9e3779b9\n\t"
    "s_nop 0\n\t"
    "v_addc_co_u32_e32 v23, vcc, v16, v23, vcc\n\t"
    "buffer_load_dwordx4 v[16:19], v21, s[56:59], 0 offen sc1\n\t"
    "s_branch .Lfb1_%=\n\t"
    ".Lf7_%=:\n\t"
    "s_waitcnt lgkmcnt(3)\n\t"
    "v_mfma_f32_16x16x32_bf16 v[80:83], v[32:35], v[116:119], 0\n\t"
    "ds_write_b128 v11, v[108:111] offset:1024\n\t"
    "v_mfma_f32_16x16x32_bf16 v[80:83], v[36:39], v[120:123], v[80:83]\n\t"
    "ds_read_b128 v[56:59], v12 offset:1024\n\t"
    "v_mfma_f32_16x16x32_bf16 v[80:83], v[40:43], v[124:127], v[80:83]\n\t"
    "s_waitcnt lgkmcnt(2)\n\t"
    "v_mfma_f32_16x16x32_bf16 v[80:83], v[44:47], v[128:131], v[80:83]\n\t"
    "ds_read_b128 v[60:63], v13 offset:1024\n\t"
    "v_cvt_pk_bf16_f32 v114, v100, v101\n\t"
    "v_mfma_f32_16x16x32_bf16 v[80:83], v[48:51], v[132:135], v[80:83]\n\t"
    "v_cvt_pk_bf16_f32 v115, v102, v103\n\t"
    "v_mfma_f32_16x16x32_bf16 v[80:83], v[52:55], v[136:139], v[80:83]\n\t"
    "ds_read_b128 v[64:67], v12 offset:5120\n\t"
    "v_mfma_f32_16x16x32_bf16 v[88:91], v[32:35], v[140:143], 0\n\t"
    "ds_write_b128 v11, v[112:115] offset:2048\n\t"
    "v_mfma_f32_16x16x32_bf16 v[88:91], v[36:39], v[144:147], v[88:91]\n\t"
    "ds_read_b128 v[68:71], v13 offset:5120\n\t"
    "v_mfma_f32_16x16x32_bf16 v[88:91], v[40:43], v[148:151], v[88:91]\n\t"
    "v_mov_b32_e32 v11, v14\n\t"
    "v_add_u32_e32 v14, 0x3000, v14\n\t"
    "v_mfma_f32_16x16x32_bf16 v[88:91], v[44:47], v[152:155], v[88:91]\n\t"
    "ds_read_b128 v[72:75], v12 offset:9216\n\t"
    "v_cvt_pk_bf16_f32 v104, v80, v81\n\t"
    "v_mfma_f32_16x16x32_bf16 v[88:91], v[48:51], v[156:159], v[88:91]\n\t"
    "v_cvt_pk_bf16_f32 v105, v82, v83\n\t"
    "v_mfma_f32_16x16x32_bf16 v[88:91], v[52:55], v[160:163], v[88:91]\n\t"
    "ds_read_b128 v[76:79], v13 offset:9216\n\t"
    "v_mfma_f32_16x16x32_bf16 v[96:99], v[32:35], v[164:167], 0\n\t"
    "s_cmp_eq_u32 s60, s67\n\t"
    "s_cbranch_scc1 .Lfs1_%=\n\t"
    ".Lfb1_%=:\n\t"
    "v_mfma_f32_16x16x32_bf16 v[96:99], v[36:39], v[168:171], v[96:99]\n\t"
    "v_mfma_f32_16x16x32_bf16 v[96:99], v[40:43], v[172:175], v[96:99]\n\t"
    "v_add_u32_e32 v12, 0x3000, v12\n\t"
    "v_add_u32_e32 v13, 0x3000, v13\n\t"
    "v_mfma_f32_16x16x32_bf16 v[96:99], v[44:47], v[176:179], v[96:99]\n\t"
    "v_cvt_pk_bf16_f32 v108, v88, v89\n\t"
    "v_cvt_pk_bf16_f32 v109, v90, v91\n\t"
    "v_mfma_f32_16x16x32_bf16 v[96:99], v[48:51], v[180:183], v[96:99]\n\t"
    "v_mfma_f32_16x16x32_bf16 v[96:99], v[52:55], v[184:187], v[96:99]\n\t"
    "s_waitcnt lgkmcnt(4)\n\t"
    "v_mfma_f32_16x16x32_bf16 v[84:87], v[56:59], v[116:119], 0\n\t"
    "v_mfma_f32_16x16x32_bf16 v[84:87], v[60:63], v[120:123], v[84:87]\n\t"
    "v_mfma_f32_16x16x32_bf16 v[84:87], v[64:67], v[124:127], v[84:87]\n\t"
    "s_waitcnt lgkmcnt(0)\n\t"
    "v_mfma_f32_16x16x32_bf16 v[84:87], v[68:71], v[128:131], v[84:87]\n\t"
    "s_waitcnt vmcnt(1)\n\t"
    "s_barrier\n\t"
    "v_mfma_f32_16x16x32_bf16 v[84:87], v[72:75], v[132:135], v[84:87]\n\t"
    "ds_read_b128 v[32:35], v12 offset:0\n\t"
    "v_cvt_pk_bf16_f32 v112, v96, v97\n\t"
    "v_mfma_f32_16x16x32_bf16 v[84:87], v[76:79], v[136:139], v[84:87]\n\t"
    "ds_read_b128 v[36:39], v13 offset:0\n\t"
    "v_cvt_pk_bf16_f32 v113, v98, v99\n\t"
    "v_mfma_f32_16x16x32_bf16 v[92:95], v[56:59], v[140:143], 0\n\t"
    "ds_read_b128 v[40:43], v12 offset:4096\n\t"
    "v_mfma_f32_16x16x32_bf16 v[92:95], v[60:63], v[144:147], v[92:95]\n\t"
    "ds_read_b128 v[44:47], v13 offset:4096\n\t"
    "v_mfma_f32_16x16x32_bf16 v[92:95], v[64:67], v[148:151], v[92:95]\n\t"
    "ds_read_b128 v[48:51], v12 offset:8192\n\t"
    "v_mfma_f32_16x16x32_bf16 v[92:95], v[68:71], v[152:155], v[92:95]\n\t"
    "ds_read_b128 v[52:55], v13 offset:8192\n\t"
    "v_cvt_pk_bf16_f32 v106, v84, v85\n\t"
    "v_mfma_f32_16x16x32_bf16 v[92:95], v[72:75], v[156:159], v[92:95]\n\t"
    "v_cvt_pk_bf16_f32 v107, v86, v87\n\t"
    "v_mfma_f32_16x16x32_bf16 v[92:95], v[76:79], v[160:163], v[92:95]\n\t"
    "ds_write_b128 v11, v[104:107] offset:0\n\t"
    "v_mfma_f32_16x16x32_bf16 v[100:103], v[56:59], v[164:167], 0\n\t"
    "v_mfma_f32_16x16x32_bf16 v[100:103], v[60:63], v[168:171], v[100:103]\n\t"
    "v_mfma_f32_16x16x32_bf16 v[100:103], v[64:67], v[172:175], v[100:103]\n\t"
    "v_mfma_f32_16x16x32_bf16 v[100:103], v[68:71], v[176:179], v[100:103]\n\t"
    "v_cvt_pk_bf16_f32 v110, v92, v93\n\t"
    "v_cvt_pk_bf16_f32 v111, v94, v95\n\t"
    "v_mfma_f32_16x16x32_bf16 v[100:103], v[72:75], v[180:183], v[100:103]\n\t"
    "s_sub_u32 s60, s60, 1\n\t"
    "s_cmp_lg_u32 s60, 0\n\t"
    "v_mfma_f32_16x16x32_bf16 v[100:103], v[76:79], v[184:187], v[100:103]\n\t"
    "s_cbranch_scc1 .Lf7_%=\n\t"
    "ds_write_b128 v11, v[108:111] offset:1024\n\t"
    "s_nop 6\n\t"
    "v_cvt_pk_bf16_f32 v114, v100, v101\n\t"
    "v_cvt_pk_bf16_f32 v115, v102, v103\n\t"
    "ds_write_b128 v11, v[112:115] offset:2048\n\t"
    "s_waitcnt lgkmcnt(0)\n\t"
    "s_barrier\n\t"
    "s_mov_b32 s60, 0x10000\n\t"
    ".Lp8_%=:\n\t"
    "s_waitcnt vmcnt(0)\n\t"
    "v_cmp_eq_u64_e32 vcc, v[16:17], v[22:23]\n\t"
    "v_cmp_eq_u64_e64 s[66:67], v[18:19], v[22:23]\n\t"
    "s_and_b64 vcc, vcc, s[66:67]\n\t"
    "s_cmp_eq_u64 vcc, exec\n\t"
    "s_cbranch_scc1 .Lk9_%=\n\t"
    "s_sleep 1\n\t"
    "buffer_load_dwordx4 v[16:19], v21, s[56:59], 0 offen sc1\n\t"
    "s_sub_u32 s60, s60, 1\n\t"
    "s_cmp_lg_u32 s60, 0\n\t"
    "s_cbranch_scc1 .Lp8_%=\n\t"
    "s_waitcnt vmcnt(0)\n\t"
    ".Lk9_%=:\n\t"
    "s_mov_b32 s66, %[noff]\n\t"
    "s_mov_b64 exec, 0x1ff\n\t"
    "buffer_load_dword v10, v26, s[56:59], %[ntch] offen sc1\n\t"
    "s_mov_b64 exec, -1\n\t"
    "v_mov_b32_e32 v9, v15\n\t"
    "buffer_load_dwordx4 a[0:3], v9, s[56:59], s66 offen offset:0 sc1\n\t"
    "buffer_load_dwordx4 a[4:7], v9, s[56:59], s66 offen offset:1024 sc1\n\t"
    "buffer_load_dwordx4 a[8:11], v9, s[56:59], s66 offen offset:2048 sc1\n\t"
    "buffer_load_dwordx4 a[12:15], v9, s[56:59], s66 offen offset:3072 sc1\n\t"
    "s_add_u32 s66, s66, 4096\n\t"
    "buffer_load_dwordx4 a[16:19], v9, s[56:59], s66 offen offset:0 sc1\n\t"
    "buffer_load_dwordx4 a[20:23], v9, s[56:59], s66 offen offset:1024 sc1\n\t"
    "buffer_load_dwordx4 a[24:27], v9, s[56:59], s66 offen offset:2048 sc1\n\t"
    "buffer_load_dwordx4 a[28:31], v9, s[56:59], s66 offen offset:3072 sc1\n\t"
    "s_add_u32 s66, s66, 4096\n\t"
    "buffer_load_dwordx4 a[32:35], v9, s[56:59], s66 offen offset:0 sc1\n\t"
    "buffer_load_dwordx4 a[36:39], v9, s[56:59], s66 offen offset:1024 sc1\n\t"
    "buffer_load_dwordx4 a[40:43], v9, s[56:59], s66 offen offset:2048 sc1\n\t"
    "buffer_load_dwordx4 a[44:47], v9, s[56:59], s66 offen offset:3072 sc1\n\t"
    "s_add_u32 s66, s66, 4096\n\t"
    "buffer_load_dwordx4 a[48:51], v9, s[56:59], s66 offen offset:0 sc1\n\t"
    "buffer_load_dwordx4 a[52:55], v9, s[56:59], s66 offen offset:1024 sc1\n\t"
    "buffer_load_dwordx4 a[56:59], v9, s[56:59], s66 offen offset:2048 sc1\n\t"
    "buffer_load_dwordx4 a[60:63], v9, s[56:59], s66 offen offset:3072 sc1\n\t"
    "s_add_u32 s66, s66, 4096\n\t"
    "buffer_load_dwordx4 a[64:67], v9, s[56:59], s66 offen offset:0 sc1\n\t"
    "buffer_load_dwordx4 a[68:71], v9, s[56:59], s66 offen offset:1024 sc1\n\t"
    "buffer_load_dwordx4 a[72:75], v9, s[56:59], s66 offen offset:2048 sc1\n\t"
    "buffer_load_dwordx4 a[76:79], v9, s[56:59], s66 offen offset:3072 sc1\n\t"
    "s_add_u32 s66, s66, 4096\n\t"
    "buffer_load_dwordx4 a[80:83], v9, s[56:59], s66 offen offset:0 sc1\n\t"
    "buffer_load_dwordx4 a[84:87], v9, s[56:59], s66 offen offset:1024 sc1\n\t"
    "buffer_load_dwordx4 a[88:91], v9, s[56:59], s66 offen offset:2048 sc1\n\t"
    "buffer_load_dwordx4 a[92:95], v9, s[56:59], s66 offen offset:3072 sc1\n\t"
    "s_add_u32 s66, s66, 4096\n\t"
    "buffer_load_dwordx4 a[96:99], v9, s[56:59], s66 offen offset:0 sc1\n\t"
    "buffer_load_dwordx4 a[100:103], v9, s[56:59], s66 offen offset:1024 sc1\n\t"
    "buffer_load_dwordx4 a[104:107], v9, s[56:59], s66 offen offset:2048 sc1\n\t"
    "buffer_load_dwordx4 a[108:111], v9, s[56:59], s66 offen offset:3072 sc1\n\t"
    "s_add_u32 s66, s66, 4096\n\t"
    "buffer_load_dwordx4 a[112:115], v9, s[56:59], s66 offen offset:0 sc1\n\t"
    "buffer_load_dwordx4 a[116:119], v9, s[56:59], s66 offen offset:1024 sc1\n\t"
    "buffer_load_dwordx4 a[120:123], v9, s[56:59], s66 offen offset:2048 sc1\n\t"
    "buffer_load_dwordx4 a[124:127], v9, s[56:59], s66 offen offset:3072 sc1\n\t"
    "s_add_u32 s66, s66, 4096\n\t"
    "buffer_load_dwordx4 a[128:131], v9, s[56:59], s66 offen offset:0 sc1\n\t"
    "buffer_load_dwordx4 a[132:135], v9, s[56:59], s66 offen offset:1024 sc1\n\t"
    "buffer_load_dwordx4 a[136:139], v9, s[56:59], s66 offen offset:2048 sc1\n\t"
    "buffer_load_dwordx4 a[140:143], v9, s[56:59], s66 offen offset:3072 sc1\n\t"
    "s_add_u32 s66, s66, 4096\n\t"
    "buffer_load_dwordx4 a[144:147], v9, s[56:59], s66 offen offset:0 sc1\n\t"
    "buffer_load_dwordx4 a[148:151], v9, s[56:59], s66 offen offset:1024 sc1\n\t"
    "buffer_load_dwordx4 a[152:155], v9, s[56:59], s66 offen offset:2048 sc1\n\t"
    "buffer_load_dwordx4 a[156:159], v9, s[56:59], s66 offen offset:3072 sc1\n\t"
    "s_add_u32 s66, s66, 4096\n\t"
    "buffer_load_dwordx4 a[160:163], v9, s[56:59], s66 offen offset:0 sc1\n\t"
    "buffer_load_dwordx4 a[164:167], v9, s[56:59], s66 offen offset:1024 sc1\n\t"
    "buffer_load_dwordx4 a[168:171], v9, s[56:59], s66 offen offset:2048 sc1\n\t"
    "buffer_load_dwordx4 a[172:175], v9, s[56:59], s66 offen offset:3072 sc1\n\t"
    "s_add_u32 s66, s66, 4096\n\t"
    "buffer_load_dwordx4 a[176:179], v9, s[56:59], s66 offen offset:0 sc1\n\t"
    "buffer_load_dwordx4 a[180:183], v9, s[56:59], s66 offen offset:1024 sc1\n\t"
    "buffer_load_dwordx4 a[184:187], v9, s[56:59], s66 offen offset:2048 sc1\n\t"
    "buffer_load_dwordx4 a[188:191], v9, s[56:59], s66 offen offset:3072 sc1\n\t"
    "s_add_u32 s66, s66, 4096\n\t"
    "s_barrier\n\t"
    "s_mov_b64 exec, %[amask]\n\t"
    "buffer_store_dwordx2 v[22:23], off, s[56:59], %[aown] sc1\n\t"
    "s_mov_b64 exec, -1\n\t"
    "ds_read_b128 v[120:123], v0 offset:0\n\t"
    "ds_read_b128 v[140:143], v1 offset:0\n\t"
    "ds_read_b128 v[148:151], v2 offset:0\n\t"
    "ds_read_b128 v[152:155], v3 offset:0\n\t"
    "ds_read_b128 v[156:159], v4 offset:0\n\t"
    "ds_read_b128 v[160:163], v5 offset:0\n\t"
    "ds_read_b128 v[164:167], v0 offset:6144\n\t"
    "ds_read_b128 v[168:171], v1 offset:6144\n\t"
    "ds_read_b128 v[172:175], v2 offset:6144\n\t"
    "ds_read_b128 v[176:179], v3 offset:6144\n\t"
    "ds_read_b128 v[180:183], v4 offset:6144\n\t"
    "ds_read_b128 v[196:199], v5 offset:6144\n\t"
    "s_add_u32 s61, %[wb], 0xff400000\n\t"
    "s_mov_b32 s62, %[rbeg]\n\t"
    "s_mov_b64 vcc, -1\n\t"
    "s_waitcnt vmcnt(36)\n\t"
    "s_mov_b32 s60, 8\n\t"
    "s_branch .Lf10_%=\n\t"
    ".Lw2_%=:\n\t"
    "buffer_load_dwordx4 a[192:195], v9, s[56:59], s66 offen offset:0 sc1\n\t"
    "buffer_load_dwordx4 a[196:199], v9, s[56:59], s66 offen offset:1024 sc1\n\t"
    "buffer_load_dwordx4 a[200:203], v9, s[56:59], s66 offen offset:2048 sc1\n\t"
    "buffer_load_dwordx4 a[204:207], v9, s[56:59], s66 offen offset:3072 sc1\n\t"
    "s_add_u32 s66, s66, 4096\n\t"
    "buffer_load_dwordx4 a[208:211], v9, s[56:59], s66 offen offset:0 sc1\n\t"
    "buffer_load_dwordx4 a[212:215], v9, s[56:59], s66 offen offset:1024 sc1\n\t"
    "buffer_load_dwordx4 a[216:219], v9, s[56:59], s66 offen offset:2048 sc1\n\t"
    "buffer_load_dwordx4 a[220:223], v9, s[56:59], s66 offen offset:3072 sc1\n\t"
    "s_add_u32 s66, s66, 4096\n\t"
    "buffer_load_dwordx4 a[224:227], v9, s[56:59], s66 offen offset:0 sc1\n\t"
    "buffer_load_dwordx4 a[228:231], v9, s[56:59], s66 offen offset:1024 sc1\n\t"
    "buffer_load_dwordx4 a[232:235], v9, s[56:59], s66 offen offset:2048 sc1\n\t"
    "buffer_load_dwordx4 a[236:239], v9, s[56:59], s66 offen offset:3072 sc1\n\t"
    "s_add_u32 s66, s66, 4096\n\t"
    "s_waitcnt vmcnt(36)\n\t"
    "s_branch .Lv2_%=\n\t"
    ".Lw4_%=:\n\t"
    "buffer_load_dwordx4 a[240:243], v9, s[56:59], s66 offen offset:0 sc1\n\t"
    "buffer_load_dwordx4 a[244:247], v9, s[56:59], s66 offen offset:1024 sc1\n\t"
    "buffer_load_dwordx4 a[248:251], v9, s[56:59], s66 offen offset:2048 sc1\n\t"
    "buffer_load_dwordx4 a[252:255], v9, s[56:59], s66 offen offset:3072 sc1\n\t"
    "s_add_u32 s66, s66, 4096\n\t"
    "buffer_load_dwordx4 v[224:227], v9, s[56:59], s66 offen offset:0 sc1\n\t"
    "buffer_load_dwordx4 v[228:231], v9, s[56:59], s66 offen offset:1024 sc1\n\t"
    "buffer_load_dwordx4 v[232:235], v9, s[56:59], s66 offen offset:2048 sc1\n\t"
    "buffer_load_dwordx4 v[236:239], v9, s[56:59], s66 offen offset:3072 sc1\n\t"
    "s_add_u32 s66, s66, 4096\n\t"
    "buffer_load_dwordx4 v[240:243], v9, s[56:59], s66 offen offset:0 sc1\n\t"
    "buffer_load_dwordx4 v[244:247], v9, s[56:59], s66 offen offset:1024 sc1\n\t"
    "buffer_load_dwordx4 v[248:251], v9, s[56:59], s66 offen offset:2048 sc1\n\t"
    "buffer_load_dwordx4 v[252:255], v9, s[56:59], s66 offen offset:3072 sc1\n\t"
    "s_add_u32 s66, s66, 4096\n\t"
    "s_waitcnt vmcnt(36)\n\t"
    "s_branch .Lv4_%=\n\t"
    ".Lw6_%=:\n\t"
    "s_waitcnt vmcnt(24)\n\t"
    "s_branch .Lv6_%=\n\t"
    ".Lw8_%=:\n\t"
    "s_waitcnt vmcnt(12)\n\t"
    "s_branch .Lv8_%=\n\t"
    ".Lw10_%=:\n\t"
    "s_waitcnt vmcnt(0)\n\t"
    "s_mov_b64 vcc, 0\n\t"
    "s_branch .Lv10_%=\n\t"
    ".Lf10_%=:\n\t"
    "s_waitcnt lgkmcnt(7)\n\t"
    "v_mfma_f32_16x16x32_bf16 v[60:63], v[120:123], a[0:3], 0\n\t"
    "buffer_store_dwordx2 v[116:117], v8, s[52:55], s61 offen offset:768 nt\n\t"
    "s_waitcnt lgkmcnt(10)\n\t"
    "v_mfma_f32_16x16x32_bf16 v[60:63], v[140:143], a[4:7], v[60:63]\n\t"
    "v_cvt_pk_bf16_f32 v35, v67, v71\n\t"
    "v_cvt_pk_bf16_f32 v40, v72, v48\n\t"
    "s_waitcnt lgkmcnt(9)\n\t"
    "v_mfma_f32_16x16x32_bf16 v[60:63], v[148:151], a[8:11], v[60:63]\n\t"
    "v_cvt_pk_bf16_f32 v42, v73, v49\n\t"
    "v_cvt_pk_bf16_f32 v44, v74, v50\n\t"
    "s_waitcnt lgkmcnt(8)\n\t"
    "v_mfma_f32_16x16x32_bf16 v[60:63], v[152:155], a[12:15], v[60:63]\n\t"
    "v_cvt_pk_bf16_f32 v46, v75, v51\n\t"
    "v_cvt_pk_bf16_f32 v41, v52, v56\n\t"
    "s_waitcnt lgkmcnt(7)\n\t"
    "v_mfma_f32_16x16x32_bf16 v[60:63], v[156:159], a[16:19], v[60:63]\n\t"
    "v_cvt_pk_bf16_f32 v43, v53, v57\n\t"
    "s_waitcnt lgkmcnt(2)\n\t"
    "v_mfma_f32_16x16x32_bf16 v[60:63], v[160:163], a[20:23], v[60:63]\n\t"
    "v_cvt_pk_bf16_f32 v45, v54, v58\n\t"
    "v_cvt_pk_bf16_f32 v47, v55, v59\n\t"
    "s_waitcnt lgkmcnt(5)\n\t"
    "v_mfma_f32_16x16x32_bf16 v[64:67], v[164:167], a[0:3], 0\n\t"
    "s_waitcnt lgkmcnt(0)\n\t"
    "v_mfma_f32_16x16x32_bf16 v[64:67], v[168:171], a[4:7], v[64:67]\n\t"
    "v_mfma_f32_16x16x32_bf16 v[64:67], v[172:175], a[8:11], v[64:67]\n\t"
    "v_mfma_f32_16x16x32_bf16 v[64:67], v[176:179], a[12:15], v[64:67]\n\t"
    "v_cvt_pk_bf16_f32 v48, v60, v61\n\t"
    "s_waitcnt lgkmcnt(0)\n\t"
    "v_mfma_f32_16x16x32_bf16 v[64:67], v[180:183], a[16:19], v[64:67]\n\t"
    "v_cvt_pk_bf16_f32 v49, v62, v63\n\t"
    "v_mfma_f32_16x16x32_bf16 v[64:67], v[196:199], a[20:23], v[64:67]\n\t"
    "v_mfma_f32_16x16x32_bf16 v[56:59], v[120:123], a[24:27], 0\n\t"
    "buffer_store_dwordx2 v[118:119], v8, s[52:55], s61 offen offset:1152 nt\n\t"
    "v_mfma_f32_16x16x32_bf16 v[56:59], v[140:143], a[28:31], v[56:59]\n\t"
    "s_add_u32 s61, s61, 393216\n\t"
    "v_mfma_f32_16x16x32_bf16 v[56:59], v[148:151], a[32:35], v[56:59]\n\t"
    "v_mfma_f32_16x16x32_bf16 v[56:59], v[152:155], a[36:39], v[56:59]\n\t"
    "v_cvt_pk_bf16_f32 v50, v64, v65\n\t"
    "v_cvt_pk_bf16_f32 v51, v66, v67\n\t"
    "v_mfma_f32_16x16x32_bf16 v[56:59], v[156:159], a[40:43], v[56:59]\n\t"
    "v_mfma_f32_16x16x32_bf16 v[56:59], v[160:163], a[44:47], v[56:59]\n\t"
    "v_mfma_f32_16x16x32_bf16 v[60:63], v[164:167], a[24:27], 0\n\t"
    "v_mfma_f32_16x16x32_bf16 v[60:63], v[168:171], a[28:31], v[60:63]\n\t"
    "v_mfma_f32_16x16x32_bf16 v[60:63], v[172:175], a[32:35], v[60:63]\n\t"
    "v_mfma_f32_16x16x32_bf16 v[60:63], v[176:179], a[36:39], v[60:63]\n\t"
    "v_cvt_pk_bf16_f32 v52, v56, v57\n\t"
    "v_cvt_pk_bf16_f32 v53, v58, v59\n\t"
    "v_mfma_f32_16x16x32_bf16 v[60:63], v[180:183], a[40:43], v[60:63]\n\t"
    "v_mfma_f32_16x16x32_bf16 v[60:63], v[196:199], a[44:47], v[60:63]\n\t"
    "s_cbranch_vccnz .Lw2_%=\n\t"
    ".Lv2_%=:\n\t"
    "v_mfma_f32_16x16x32_bf16 v[64:67], v[120:123], a[48:51], 0\n\t"
    "buffer_store_dwordx4 v[124:127], v7, s[52:55], s61 offen offset:0 nt\n\t"
    "v_mfma_f32_16x16x32_bf16 v[64:67], v[140:143], a[52:55], v[64:67]\n\t"
    "v_mfma_f32_16x16x32_bf16 v[64:67], v[148:151], a[56:59], v[64:67]\n\t"
    "v_mfma_f32_16x16x32_bf16 v[64:67], v[152:155], a[60:63], v[64:67]\n\t"
    "v_cvt_pk_bf16_f32 v54, v60, v61\n\t"
    "v_cvt_pk_bf16_f32 v55, v62, v63\n\t"
    "v_mfma_f32_16x16x32_bf16 v[64:67], v[156:159], a[64:67], v[64:67]\n\t"
    "v_mfma_f32_16x16x32_bf16 v[64:67], v[160:163], a[68:71], v[64:67]\n\t"
    "v_mfma_f32_16x16x32_bf16 v[60:63], v[164:167], a[48:51], 0\n\t"
    "v_mfma_f32_16x16x32_bf16 v[60:63], v[168:171], a[52:55], v[60:63]\n\t"
    "v_mfma_f32_16x16x32_bf16 v[60:63], v[172:175], a[56:59], v[60:63]\n\t"
    "v_mfma_f32_16x16x32_bf16 v[60:63], v[176:179], a[60:63], v[60:63]\n\t"
    "v_cvt_pk_bf16_f32 v56, v64, v65\n\t"
    "v_cvt_pk_bf16_f32 v57, v66, v67\n\t"
    "v_mfma_f32_16x16x32_bf16 v[60:63], v[180:183], a[64:67], v[60:63]\n\t"
    "v_mfma_f32_16x16x32_bf16 v[60:63], v[196:199], a[68:71], v[60:63]\n\t"
    "v_mfma_f32_16x16x32_bf16 v[64:67], v[120:123], a[72:75], 0\n\t"
    "buffer_store_dwordx4 v[104:107], v7, s[52:55], s61 offen offset:384 nt\n\t"
    "v_mfma_f32_16x16x32_bf16 v[64:67], v[140:143], a[76:79], v[64:67]\n\t"
    "v_mfma_f32_16x16x32_bf16 v[64:67], v[148:151], a[80:83], v[64:67]\n\t"
    "v_mfma_f32_16x16x32_bf16 v[64:67], v[152:155], a[84:87], v[64:67]\n\t"
    "v_cvt_pk_bf16_f32 v58, v60, v61\n\t"
    "v_cvt_pk_bf16_f32 v59, v62, v63\n\t"
    "v_mfma_f32_16x16x32_bf16 v[64:67], v[156:159], a[88:91], v[64:67]\n\t"
    "v_mfma_f32_16x16x32_bf16 v[64:67], v[160:163], a[92:95], v[64:67]\n\t"
    "v_mfma_f32_16x16x32_bf16 v[68:71], v[164:167], a[72:75], 0\n\t"
    "v_mfma_f32_16x16x32_bf16 v[68:71], v[168:171], a[76:79], v[68:71]\n\t"
    "v_mfma_f32_16x16x32_bf16 v[68:71], v[172:175], a[80:83], v[68:71]\n\t"
    "v_mfma_f32_16x16x32_bf16 v[68:71], v[176:179], a[84:87], v[68:71]\n\t"
    "v_cvt_pk_bf16_f32 v60, v64, v65\n\t"
    "v_cvt_pk_bf16_f32 v61, v66, v67\n\t"
    "v_mfma_f32_16x16x32_bf16 v[68:71], v[180:183], a[88:91], v[68:71]\n\t"
    "v_mfma_f32_16x16x32_bf16 v[68:71], v[196:199], a[92:95], v[68:71]\n\t"
    "s_cbranch_vccnz .Lw4_%=\n\t"
    ".Lv4_%=:\n\t"
    "v_mfma_f32_16x16x32_bf16 v[72:75], v[120:123], a[96:99], 0\n\t"
    "buffer_store_dwordx4 v[136:139], v7, s[52:55], s61 offen offset:768 nt\n\t"
    "v_mfma_f32_16x16x32_bf16 v[72:75], v[140:143], a[100:103], v[72:75]\n\t"
    "v_mfma_f32_16x16x32_bf16 v[72:75], v[148:151], a[104:107], v[72:75]\n\t"
    "v_mfma_f32_16x16x32_bf16 v[72:75], v[152:155], a[108:111], v[72:75]\n\t"
    "v_cvt_pk_bf16_f32 v62, v68, v69\n\t"
    "v_cvt_pk_bf16_f32 v63, v70, v71\n\t"
    "v_mfma_f32_16x16x32_bf16 v[72:75], v[156:159], a[112:115], v[72:75]\n\t"
    "v_mfma_f32_16x16x32_bf16 v[72:75], v[160:163], a[116:119], v[72:75]\n\t"
    "v_mfma_f32_16x16x32_bf16 v[68:71], v[164:167], a[96:99], 0\n\t"
    "v_mfma_f32_16x16x32_bf16 v[68:71], v[168:171], a[100:103], v[68:71]\n\t"
    "v_mfma_f32_16x16x32_bf16 v[68:71], v[172:175], a[104:107], v[68:71]\n\t"
    "v_mfma_f32_16x16x32_bf16 v[68:71], v[176:179], a[108:111], v[68:71]\n\t"
    "v_cvt_pk_bf16_f32 v64, v72, v73\n\t"
    "v_cvt_pk_bf16_f32 v65, v74, v75\n\t"
    "v_mfma_f32_16x16x32_bf16 v[68:71], v[180:183], a[112:115], v[68:71]\n\t"
    "v_mfma_f32_16x16x32_bf16 v[68:71], v[196:199], a[116:119], v[68:71]\n\t"
    "v_mfma_f32_16x16x32_bf16 v[72:75], v[120:123], a[120:123], 0\n\t"
    "buffer_store_dwordx4 v[128:131], v7, s[52:55], s61 offen offset:1152 nt\n\t"
    "v_mfma_f32_16x16x32_bf16 v[72:75], v[140:143], a[124:127], v[72:75]\n\t"
    "v_mfma_f32_16x16x32_bf16 v[72:75], v[148:151], a[128:131], v[72:75]\n\t"
    "v_mfma_f32_16x16x32_bf16 v[72:75], v[152:155], a[132:135], v[72:75]\n\t"
    "v_cvt_pk_bf16_f32 v66, v68, v69\n\t"
    "v_cvt_pk_bf16_f32 v67, v70, v71\n\t"
    "v_mfma_f32_16x16x32_bf16 v[72:75], v[156:159], a[136:139], v[72:75]\n\t"
    "v_mfma_f32_16x16x32_bf16 v[72:75], v[160:163], a[140:143], v[72:75]\n\t"
    "v_mfma_f32_16x16x32_bf16 v[76:79], v[164:167], a[120:123], 0\n\t"
    "v_mfma_f32_16x16x32_bf16 v[76:79], v[168:171], a[124:127], v[76:79]\n\t"
    "v_mfma_f32_16x16x32_bf16 v[76:79], v[172:175], a[128:131], v[76:79]\n\t"
    "v_mfma_f32_16x16x32_bf16 v[76:79], v[176:179], a[132:135], v[76:79]\n\t"
    "v_cvt_pk_bf16_f32 v68, v72, v73\n\t"
    "v_cvt_pk_bf16_f32 v69, v74, v75\n\t"
    "v_mfma_f32_16x16x32_bf16 v[76:79], v[180:183], a[136:139], v[76:79]\n\t"
    "v_mfma_f32_16x16x32_bf16 v[76:79], v[196:199], a[140:143], v[76:79]\n\t"
    "s_cbranch_vccnz .Lw6_%=\n\t"
    ".Lv6_%=:\n\t"
    "v_mfma_f32_16x16x32_bf16 v[80:83], v[120:123], a[144:147], 0\n\t"
    "buffer_store_dwordx2 v[144:145], v8, s[52:55], s61 offen offset:0 nt\n\t"
    "v_mfma_f32_16x16x32_bf16 v[80:83], v[140:143], a[148:151], v[80:83]\n\t"
    "v_mfma_f32_16x16x32_bf16 v[80:83], v[148:151], a[152:155], v[80:83]\n\t"
    "v_mfma_f32_16x16x32_bf16 v[80:83], v[152:155], a[156:159], v[80:83]\n\t"
    "v_cvt_pk_bf16_f32 v70, v76, v77\n\t"
    "v_cvt_pk_bf16_f32 v71, v78, v79\n\t"
    "v_mfma_f32_16x16x32_bf16 v[80:83], v[156:159], a[160:163], v[80:83]\n\t"
    "v_mfma_f32_16x16x32_bf16 v[80:83], v[160:163], a[164:167], v[80:83]\n\t"
    "v_mfma_f32_16x16x32_bf16 v[76:79], v[164:167], a[144:147], 0\n\t"
    "v_mfma_f32_16x16x32_bf16 v[76:79], v[168:171], a[148:151], v[76:79]\n\t"
    "v_mfma_f32_16x16x32_bf16 v[76:79], v[172:175], a[152:155], v[76:79]\n\t"
    "v_mfma_f32_16x16x32_bf16 v[76:79], v[176:179], a[156:159], v[76:79]\n\t"
    "v_cvt_pk_bf16_f32 v72, v80, v81\n\t"
    "v_cvt_pk_bf16_f32 v73, v82, v83\n\t"
    "v_mfma_f32_16x16x32_bf16 v[76:79], v[180:183], a[160:163], v[76:79]\n\t"
    "v_mfma_f32_16x16x32_bf16 v[76:79], v[196:199], a[164:167], v[76:79]\n\t"
    "v_mfma_f32_16x16x32_bf16 v[80:83], v[120:123], a[168:171], 0\n\t"
    "buffer_store_dwordx2 v[146:147], v8, s[52:55], s61 offen offset:384 nt\n\t"
    "v_mfma_f32_16x16x32_bf16 v[80:83], v[140:143], a[172:175], v[80:83]\n\t"
    "v_mfma_f32_16x16x32_bf16 v[80:83], v[148:151], a[176:179], v[80:83]\n\t"
    "v_mfma_f32_16x16x32_bf16 v[80:83], v[152:155], a[180:183], v[80:83]\n\t"
    "v_cvt_pk_bf16_f32 v74, v76, v77\n\t"
    "v_cvt_pk_bf16_f32 v75, v78, v79\n\t"
    "v_mfma_f32_16x16x32_bf16 v[80:83], v[156:159], a[184:187], v[80:83]\n\t"
    "v_mfma_f32_16x16x32_bf16 v[80:83], v[160:163], a[188:191], v[80:83]\n\t"
    "v_mfma_f32_16x16x32_bf16 v[84:87], v[164:167], a[168:171], 0\n\t"
    "v_mfma_f32_16x16x32_bf16 v[84:87], v[168:171], a[172:175], v[84:87]\n\t"
    "v_mfma_f32_16x16x32_bf16 v[84:87], v[172:175], a[176:179], v[84:87]\n\t"
    "v_mfma_f32_16x16x32_bf16 v[84:87], v[176:179], a[180:183], v[84:87]\n\t"
    "v_cvt_pk_bf16_f32 v76, v80, v81\n\t"
    "v_cvt_pk_bf16_f32 v77, v82, v83\n\t"
    "v_mfma_f32_16x16x32_bf16 v[84:87], v[180:183], a[184:187], v[84:87]\n\t"
    "v_mfma_f32_16x16x32_bf16 v[84:87], v[196:199], a[188:191], v[84:87]\n\t"
    "s_cbranch_vccnz .Lw8_%=\n\t"
    ".Lv8_%=:\n\t"
    "v_mfma_f32_16x16x32_bf16 v[88:91], v[120:123], a[192:195], 0\n\t"
    "buffer_store_dwordx2 v[188:189], v8, s[52:55], s61 offen offset:768 nt\n\t"
    "v_mfma_f32_16x16x32_bf16 v[88:91], v[140:143], a[196:199], v[88:91]\n\t"
    "v_mfma_f32_16x16x32_bf16 v[88:91], v[148:151], a[200:203], v[88:91]\n\t"
    "v_mfma_f32_16x16x32_bf16 v[88:91], v[152:155], a[204:207], v[88:91]\n\t"
    "v_cvt_pk_bf16_f32 v78, v84, v85\n\t"
    "v_cvt_pk_bf16_f32 v79, v86, v87\n\t"
    "v_mfma_f32_16x16x32_bf16 v[88:91], v[156:159], a[208:211], v[88:91]\n\t"
    "v_mfma_f32_16x16x32_bf16 v[88:91], v[160:163], a[212:215], v[88:91]\n\t"
    "v_mfma_f32_16x16x32_bf16 v[84:87], v[164:167], a[192:195], 0\n\t"
    "v_mfma_f32_16x16x32_bf16 v[84:87], v[168:171], a[196:199], v[84:87]\n\t"
    "v_mfma_f32_16x16x32_bf16 v[84:87], v[172:175], a[200:203], v[84:87]\n\t"
    "v_mfma_f32_16x16x32_bf16 v[84:87], v[176:179], a[204:207], v[84:87]\n\t"
    "v_cvt_pk_bf16_f32 v80, v88, v89\n\t"
    "v_cvt_pk_bf16_f32 v81, v90, v91\n\t"
    "v_mfma_f32_16x16x32_bf16 v[84:87], v[180:183], a[208:211], v[84:87]\n\t"
    "v_mfma_f32_16x16x32_bf16 v[84:87], v[196:199], a[212:215], v[84:87]\n\t"
    "v_mfma_f32_16x16x32_bf16 v[88:91], v[120:123], a[216:219], 0\n\t"
    "buffer_store_dwordx2 v[190:191], v8, s[52:55], s61 offen offset:1152 nt\n\t"
    "v_mfma_f32_16x16x32_bf16 v[88:91], v[140:143], a[220:223], v[88:91]\n\t"
    "s_add_u32 s61, s61, 393216\n\t"
    "v_mfma_f32_16x16x32_bf16 v[88:91], v[148:151], a[224:227], v[88:91]\n\t"
    "v_mfma_f32_16x16x32_bf16 v[88:91], v[152:155], a[228:231], v[88:91]\n\t"
    "v_cvt_pk_bf16_f32 v82, v84, v85\n\t"
    "v_cvt_pk_bf16_f32 v83, v86, v87\n\t"
    "v_mfma_f32_16x16x32_bf16 v[88:91], v[156:159], a[232:235], v[88:91]\n\t"
    "v_mfma_f32_16x16x32_bf16 v[88:91], v[160:163], a[236:239], v[88:91]\n\t"
    "v_mfma_f32_16x16x32_bf16 v[92:95], v[164:167], a[216:219], 0\n\t"
    "v_mfma_f32_16x16x32_bf16 v[92:95], v[168:171], a[220:223], v[92:95]\n\t"
    "v_mfma_f32_16x16x32_bf16 v[92:95], v[172:175], a[224:227], v[92:95]\n\t"
    "v_mfma_f32_16x16x32_bf16 v[92:95], v[176:179], a[228:231], v[92:95]\n\t"
    "v_cvt_pk_bf16_f32 v84, v88, v89\n\t"
    "v_cvt_pk_bf16_f32 v85, v90, v91\n\t"
    "v_mfma_f32_16x16x32_bf16 v[92:95], v[180:183], a[232:235], v[92:95]\n\t"
    "v_mfma_f32_16x16x32_bf16 v[92:95], v[196:199], a[236:239], v[92:95]\n\t"
    "s_cbranch_vccnz .Lw10_%=\n\t"
    ".Lv10_%=:\n\t"
    "v_mfma_f32_16x16x32_bf16 v[96:99], v[120:123], a[240:243], 0\n\t"
    "buffer_store_dwordx4 v[100:103], v7, s[52:55], s61 offen offset:0 nt\n\t"
    "v_mfma_f32_16x16x32_bf16 v[96:99], v[140:143], a[244:247], v[96:99]\n\t"
    "v_mfma_f32_16x16x32_bf16 v[96:99], v[148:151], a[248:251], v[96:99]\n\t"
    "v_mfma_f32_16x16x32_bf16 v[96:99], v[152:155], a[252:255], v[96:99]\n\t"
    "v_cvt_pk_bf16_f32 v86, v92, v93\n\t"
    "v_cvt_pk_bf16_f32 v87, v94, v95\n\t"
    "v_mfma_f32_16x16x32_bf16 v[96:99], v[156:159], v[224:227], v[96:99]\n\t"
    "v_mfma_f32_16x16x32_bf16 v[96:99], v[160:163], v[228:231], v[96:99]\n\t"
    "v_mfma_f32_16x16x32_bf16 v[124:127], v[164:167], a[240:243], 0\n\t"
    "v_mfma_f32_16x16x32_bf16 v[124:127], v[168:171], a[244:247], v[124:127]\n\t"
    "v_mfma_f32_16x16x32_bf16 v[124:127], v[172:175], a[248:251], v[124:127]\n\t"
    "v_mfma_f32_16x16x32_bf16 v[124:127], v[176:179], a[252:255], v[124:127]\n\t"
    "v_cvt_pk_bf16_f32 v88, v96, v97\n\t"
    "v_cvt_pk_bf16_f32 v89, v98, v99\n\t"
    "v_mfma_f32_16x16x32_bf16 v[124:127], v[180:183], v[224:227], v[124:127]\n\t"
    "v_mfma_f32_16x16x32_bf16 v[124:127], v[196:199], v[228:231], v[124:127]\n\t"
    "v_mfma_f32_16x16x32_bf16 v[184:187], v[120:123], v[232:235], 0\n\t"
    "ds_read_b128 v[92:95], v0 offset:12288\n\t"
    "buffer_store_dwordx4 v[112:115], v7, s[52:55], s61 offen offset:384 nt\n\t"
    "v_mfma_f32_16x16x32_bf16 v[184:187], v[140:143], v[236:239], v[184:187]\n\t"
    "ds_read_b128 v[96:99], v1 offset:12288\n\t"
    "v_mfma_f32_16x16x32_bf16 v[184:187], v[148:151], v[240:243], v[184:187]\n\t"
    "ds_read_b128 v[100:103], v2 offset:12288\n\t"
    "v_mfma_f32_16x16x32_bf16 v[184:187], v[152:155], v[244:247], v[184:187]\n\t"
    "ds_read_b128 v[104:107], v3 offset:12288\n\t"
    "v_cvt_pk_bf16_f32 v90, v124, v125\n\t"
    "v_mfma_f32_16x16x32_bf16 v[184:187], v[156:159], v[248:251], v[184:187]\n\t"
    "ds_read_b128 v[108:111], v4 offset:12288\n\t"
    "v_cvt_pk_bf16_f32 v91, v126, v127\n\t"
    "v_mfma_f32_16x16x32_bf16 v[184:187], v[160:163], v[252:255], v[184:187]\n\t"
    "ds_read_b128 v[112:115], v5 offset:12288\n\t"
    "v_mfma_f32_16x16x32_bf16 v[120:123], v[164:167], v[232:235], 0\n\t"
    "ds_read_b128 v[116:119], v0 offset:18432\n\t"
    "v_mfma_f32_16x16x32_bf16 v[120:123], v[168:171], v[236:239], v[120:123]\n\t"
    "ds_read_b128 v[124:127], v1 offset:18432\n\t"
    "v_mfma_f32_16x16x32_bf16 v[120:123], v[172:175], v[240:243], v[120:123]\n\t"
    "ds_read_b128 v[128:131], v2 offset:18432\n\t"
    "v_mfma_f32_16x16x32_bf16 v[120:123], v[176:179], v[244:247], v[120:123]\n\t"
    "ds_read_b128 v[136:139], v3 offset:18432\n\t"
    "v_cvt_pk_bf16_f32 v132, v184, v185\n\t"
    "v_mfma_f32_16x16x32_bf16 v[120:123], v[180:183], v[248:251], v[120:123]\n\t"
    "ds_read_b128 v[140:143], v4 offset:18432\n\t"
    "v_cvt_pk_bf16_f32 v133, v186, v187\n\t"
    "v_mfma_f32_16x16x32_bf16 v[120:123], v[196:199], v[252:255], v[120:123]\n\t"
    "ds_read_b128 v[144:147], v5 offset:18432\n\t"
    "s_waitcnt lgkmcnt(7)\n\t"
    "v_mfma_f32_16x16x32_bf16 v[148:151], v[92:95], a[0:3], 0\n\t"
    "buffer_store_dwordx4 v[192:195], v7, s[52:55], s61 offen offset:768 nt\n\t"
    "v_mfma_f32_16x16x32_bf16 v[148:151], v[96:99], a[4:7], v[148:151]\n\t"
    "v_mfma_f32_16x16x32_bf16 v[148:151], v[100:103], a[8:11], v[148:151]\n\t"
    "v_mfma_f32_16x16x32_bf16 v[148:151], v[104:107], a[12:15], v[148:151]\n\t"
    "v_cvt_pk_bf16_f32 v134, v120, v121\n\t"
    "v_cvt_pk_bf16_f32 v135, v122, v123\n\t"
    "v_mfma_f32_16x16x32_bf16 v[148:151], v[108:111], a[16:19], v[148:151]\n\t"
    "s_waitcnt lgkmcnt(2)\n\t"
    "v_mfma_f32_16x16x32_bf16 v[148:151], v[112:115], a[20:23], v[148:151]\n\t"
    "v_mfma_f32_16x16x32_bf16 v[120:123], v[116:119], a[0:3], 0\n\t"
    "v_mfma_f32_16x16x32_bf16 v[120:123], v[124:127], a[4:7], v[120:123]\n\t"
    "v_mfma_f32_16x16x32_bf16 v[120:123], v[128:131], a[8:11], v[120:123]\n\t"
    "v_mfma_f32_16x16x32_bf16 v[120:123], v[136:139], a[12:15], v[120:123]\n\t"
    "v_cvt_pk_bf16_f32 v152, v148, v149\n\t"
    "s_waitcnt lgkmcnt(0)\n\t"
    "v_mfma_f32_16x16x32_bf16 v[120:123], v[140:143], a[16:19], v[120:123]\n\t"
    "v_cvt_pk_bf16_f32 v153, v150, v151\n\t"
    "v_mfma_f32_16x16x32_bf16 v[120:123], v[144:147], a[20:23], v[120:123]\n\t"
    "v_mfma_f32_16x16x32_bf16 v[156:159], v[92:95], a[24:27], 0\n\t"
    "buffer_store_dwordx4 v[12:15], v7, s[52:55], s61 offen offset:1152 nt\n\t"
    "v_mfma_f32_16x16x32_bf16 v[156:159], v[96:99], a[28:31], v[156:159]\n\t"
    "v_mfma_f32_16x16x32_bf16 v[156:159], v[100:103], a[32:35], v[156:159]\n\t"
    "v_mfma_f32_16x16x32_bf16 v[156:159], v[104:107], a[36:39], v[156:159]\n\t"
    "v_cvt_pk_bf16_f32 v154, v120, v121\n\t"
    "v_cvt_pk_bf16_f32 v155, v122, v123\n\t"
    "v_mfma_f32_16x16x32_bf16 v[156:159], v[108:111], a[40:43], v[156:159]\n\t"
    "v_mfma_f32_16x16x32_bf16 v[156:159], v[112:115], a[44:47], v[156:159]\n\t"
    "v_mfma_f32_16x16x32_bf16 v[12:15], v[116:119], a[24:27], 0\n\t"
    "v_mfma_f32_16x16x32_bf16 v[12:15], v[124:127], a[28:31], v[12:15]\n\t"
    "v_mfma_f32_16x16x32_bf16 v[12:15], v[128:131], a[32:35], v[12:15]\n\t"
    "v_mfma_f32_16x16x32_bf16 v[12:15], v[136:139], a[36:39], v[12:15]\n\t"
    "v_cvt_pk_bf16_f32 v120, v156, v157\n\t"
    "v_cvt_pk_bf16_f32 v121, v158, v159\n\t"
    "v_mfma_f32_16x16x32_bf16 v[12:15], v[140:143], a[40:43], v[12:15]\n\t"
    "v_mfma_f32_16x16x32_bf16 v[12:15], v[144:147], a[44:47], v[12:15]\n\t"
    "v_mfma_f32_16x16x32_bf16 v[148:151], v[92:95], a[48:51], 0\n\t"
    "buffer_store_dwordx2 v[16:17], v8, s[52:55], s61 offen offset:0 nt\n\t"
    "v_mfma_f32_16x16x32_bf16 v[148:151], v[96:99], a[52:55], v[148:151]\n\t"
    "v_mfma_f32_16x16x32_bf16 v[148:151], v[100:103], a[56:59], v[148:151]\n\t"
    "v_mfma_f32_16x16x32_bf16 v[148:151], v[104:107], a[60:63], v[148:151]\n\t"
    "v_cvt_pk_bf16_f32 v122, v12, v13\n\t"
    "v_cvt_pk_bf16_f32 v123, v14, v15\n\t"
    "v_mfma_f32_16x16x32_bf16 v[148:151], v[108:111], a[64:67], v[148:151]\n\t"
    "v_mfma_f32_16x16x32_bf16 v[148:151], v[112:115], a[68:71], v[148:151]\n\t"
    "v_mfma_f32_16x16x32_bf16 v[12:15], v[116:119], a[48:51], 0\n\t"
    "v_mfma_f32_16x16x32_bf16 v[12:15], v[124:127], a[52:55], v[12:15]\n\t"
    "v_mfma_f32_16x16x32_bf16 v[12:15], v[128:131], a[56:59], v[12:15]\n\t"
    "v_mfma_f32_16x16x32_bf16 v[12:15], v[136:139], a[60:63], v[12:15]\n\t"
    "v_cvt_pk_bf16_f32 v156, v148, v149\n\t"
    "v_cvt_pk_bf16_f32 v157, v150, v151\n\t"
    "v_mfma_f32_16x16x32_bf16 v[12:15], v[140:143], a[64:67], v[12:15]\n\t"
    "v_mfma_f32_16x16x32_bf16 v[12:15], v[144:147], a[68:71], v[12:15]\n\t"
    "v_mfma_f32_16x16x32_bf16 v[148:151], v[92:95], a[72:75], 0\n\t"
    "s_add_i32 s64, s62, %[rd]\n\t"
    "buffer_store_dwordx2 v[18:19], v8, s[52:55], s61 offen offset:384 nt\n\t"
    "v_mfma_f32_16x16x32_bf16 v[148:151], v[96:99], a[76:79], v[148:151]\n\t"
    "s_cmp_eq_u32 s64, %[rend]\n\t"
    "s_cselect_b32 s64, %[rbeg], s64\n\t"
    "v_mfma_f32_16x16x32_bf16 v[148:151], v[100:103], a[80:83], v[148:151]\n\t"
    "s_sub_i32 s65, s64, s62\n\t"
    "s_mov_b32 s62, s64\n\t"
    "v_mfma_f32_16x16x32_bf16 v[148:151], v[104:107], a[84:87], v[148:151]\n\t"
    "v_add_u32_e32 v0, s65, v0\n\t"
    "v_add_u32_e32 v1, s65, v1\n\t"
    "v_mfma_f32_16x16x32_bf16 v[148:151], v[108:111], a[88:91], v[148:151]\n\t"
    "v_add_u32_e32 v2, s65, v2\n\t"
    "v_add_u32_e32 v3, s65, v3\n\t"
    "v_mfma_f32_16x16x32_bf16 v[148:151], v[112:115], a[92:95], v[148:151]\n\t"
    "v_add_u32_e32 v4, s65, v4\n\t"
    "v_add_u32_e32 v5, s65, v5\n\t"
    "v_mfma_f32_16x16x32_bf16 v[16:19], v[116:119], a[72:75], 0\n\t"
    "v_cvt_pk_bf16_f32 v158, v12, v13\n\t"
    "v_cvt_pk_bf16_f32 v159, v14, v15\n\t"
    "v_mfma_f32_16x16x32_bf16 v[16:19], v[124:127], a[76:79], v[16:19]\n\t"
    "v_mfma_f32_16x16x32_bf16 v[16:19], v[128:131], a[80:83], v[16:19]\n\t"
    "v_mfma_f32_16x16x32_bf16 v[16:19], v[136:139], a[84:87], v[16:19]\n\t"
    "v_cvt_pk_bf16_f32 v160, v148, v149\n\t"
    "v_cvt_pk_bf16_f32 v161, v150, v151\n\t"
    "v_mfma_f32_16x16x32_bf16 v[16:19], v[140:143], a[88:91], v[16:19]\n\t"
    "v_mfma_f32_16x16x32_bf16 v[16:19], v[144:147], a[92:95], v[16:19]\n\t"
    "v_mfma_f32_16x16x32_bf16 v[12:15], v[92:95], a[96:99], 0\n\t"
    "buffer_store_dwordx2 v[20:21], v8, s[52:55], s61 offen offset:768 nt\n\t"
    "v_mfma_f32_16x16x32_bf16 v[12:15], v[96:99], a[100:103], v[12:15]\n\t"
    "v_mfma_f32_16x16x32_bf16 v[12:15], v[100:103], a[104:107], v[12:15]\n\t"
    "v_mfma_f32_16x16x32_bf16 v[12:15], v[104:107], a[108:111], v[12:15]\n\t"
    "v_cvt_pk_bf16_f32 v162, v16, v17\n\t"
    "v_cvt_pk_bf16_f32 v163, v18, v19\n\t"
    "v_mfma_f32_16x16x32_bf16 v[12:15], v[108:111], a[112:115], v[12:15]\n\t"
    "v_mfma_f32_16x16x32_bf16 v[12:15], v[112:115], a[116:119], v[12:15]\n\t"
    "v_mfma_f32_16x16x32_bf16 v[16:19], v[116:119], a[96:99], 0\n\t"
    "v_mfma_f32_16x16x32_bf16 v[16:19], v[124:127], a[100:103], v[16:19]\n\t"
    "v_mfma_f32_16x16x32_bf16 v[16:19], v[128:131], a[104:107], v[16:19]\n\t"
    "v_mfma_f32_16x16x32_bf16 v[16:19], v[136:139], a[108:111], v[16:19]\n\t"
    "v_cvt_pk_bf16_f32 v148, v12, v13\n\t"
    "v_cvt_pk_bf16_f32 v149, v14, v15\n\t"
    "v_mfma_f32_16x16x32_bf16 v[16:19], v[140:143], a[112:115], v[16:19]\n\t"
    "v_mfma_f32_16x16x32_bf16 v[16:19], v[144:147], a[116:119], v[16:19]\n\t"
    "v_mfma_f32_16x16x32_bf16 v[12:15], v[92:95], a[120:123], 0\n\t"
    "buffer_store_dwordx2 v[22:23], v8, s[52:55], s61 offen offset:1152 nt\n\t"
    "v_mfma_f32_16x16x32_bf16 v[12:15], v[96:99], a[124:127], v[12:15]\n\t"
    "s_add_u32 s61, s61, 393216\n\t"
    "v_mfma_f32_16x16x32_bf16 v[12:15], v[100:103], a[128:131], v[12:15]\n\t"
    "v_mfma_f32_16x16x32_bf16 v[12:15], v[104:107], a[132:135], v[12:15]\n\t"
    "v_cvt_pk_bf16_f32 v150, v16, v17\n\t"
    "v_cvt_pk_bf16_f32 v151, v18, v19\n\t"
    "v_mfma_f32_16x16x32_bf16 v[12:15], v[108:111], a[136:139], v[12:15]\n\t"
    "v_mfma_f32_16x16x32_bf16 v[12:15], v[112:115], a[140:143], v[12:15]\n\t"
    "v_mfma_f32_16x16x32_bf16 v[16:19], v[116:119], a[120:123], 0\n\t"
    "v_mfma_f32_16x16x32_bf16 v[16:19], v[124:127], a[124:127], v[16:19]\n\t"
    "v_mfma_f32_16x16x32_bf16 v[16:19], v[128:131], a[128:131], v[16:19]\n\t"
    "v_mfma_f32_16x16x32_bf16 v[16:19], v[136:139], a[132:135], v[16:19]\n\t"
    "v_cvt_pk_bf16_f32 v164, v12, v13\n\t"
    "v_cvt_pk_bf16_f32 v165, v14, v15\n\t"
    "v_mfma_f32_16x16x32_bf16 v[16:19], v[140:143], a[136:139], v[16:19]\n\t"
    "v_mfma_f32_16x16x32_bf16 v[16:19], v[144:147], a[140:143], v[16:19]\n\t"
    "v_mfma_f32_16x16x32_bf16 v[12:15], v[92:95], a[144:147], 0\n\t"
    "buffer_store_dwordx4 v[28:31], v7, s[52:55], s61 offen offset:0 nt\n\t"
    "v_mfma_f32_16x16x32_bf16 v[12:15], v[96:99], a[148:151], v[12:15]\n\t"
    "v_mfma_f32_16x16x32_bf16 v[12:15], v[100:103], a[152:155], v[12:15]\n\t"
    "v_mfma_f32_16x16x32_bf16 v[12:15], v[104:107], a[156:159], v[12:15]\n\t"
    "v_cvt_pk_bf16_f32 v166, v16, v17\n\t"
    "v_cvt_pk_bf16_f32 v167, v18, v19\n\t"
    "v_mfma_f32_16x16x32_bf16 v[12:15], v[108:111], a[160:163], v[12:15]\n\t"
    "v_mfma_f32_16x16x32_bf16 v[12:15], v[112:115], a[164:167], v[12:15]\n\t"
    "v_mfma_f32_16x16x32_bf16 v[16:19], v[116:119], a[144:147], 0\n\t"
    "v_mfma_f32_16x16x32_bf16 v[16:19], v[124:127], a[148:151], v[16:19]\n\t"
    "v_mfma_f32_16x16x32_bf16 v[16:19], v[128:131], a[152:155], v[16:19]\n\t"
    "v_mfma_f32_16x16x32_bf16 v[16:19], v[136:139], a[156:159], v[16:19]\n\t"
    "v_cvt_pk_bf16_f32 v168, v12, v13\n\t"
    "v_cvt_pk_bf16_f32 v169, v14, v15\n\t"
    "v_mfma_f32_16x16x32_bf16 v[16:19], v[140:143], a[160:163], v[16:19]\n\t"
    "v_mfma_f32_16x16x32_bf16 v[16:19], v[144:147], a[164:167], v[16:19]\n\t"
    "v_mfma_f32_16x16x32_bf16 v[12:15], v[92:95], a[168:171], 0\n\t"
    "buffer_store_dwordx4 v[24:27], v7, s[52:55], s61 offen offset:384 nt\n\t"
    "v_mfma_f32_16x16x32_bf16 v[12:15], v[96:99], a[172:175], v[12:15]\n\t"
    "v_mfma_f32_16x16x32_bf16 v[12:15], v[100:103], a[176:179], v[12:15]\n\t"
    "v_mfma_f32_16x16x32_bf16 v[12:15], v[104:107], a[180:183], v[12:15]\n\t"
    "v_cvt_pk_bf16_f32 v170, v16, v17\n\t"
    "v_cvt_pk_bf16_f32 v171, v18, v19\n\t"
    "v_mfma_f32_16x16x32_bf16 v[12:15], v[108:111], a[184:187], v[12:15]\n\t"
    "v_mfma_f32_16x16x32_bf16 v[12:15], v[112:115], a[188:191], v[12:15]\n\t"
    "v_mfma_f32_16x16x32_bf16 v[16:19], v[116:119], a[168:171], 0\n\t"
    "v_mfma_f32_16x16x32_bf16 v[16:19], v[124:127], a[172:175], v[16:19]\n\t"
    "v_mfma_f32_16x16x32_bf16 v[16:19], v[128:131], a[176:179], v[16:19]\n\t"
    "v_mfma_f32_16x16x32_bf16 v[16:19], v[136:139], a[180:183], v[16:19]\n\t"
    "v_cvt_pk_bf16_f32 v172, v12, v13\n\t"
    "v_cvt_pk_bf16_f32 v173, v14, v15\n\t"
    "v_mfma_f32_16x16x32_bf16 v[16:19], v[140:143], a[184:187], v[16:19]\n\t"
    "v_mfma_f32_16x16x32_bf16 v[16:19], v[144:147], a[188:191], v[16:19]\n\t"
    "v_mfma_f32_16x16x32_bf16 v[12:15], v[92:95], a[192:195], 0\n\t"
    "buffer_store_dwordx4 v[36:39], v7, s[52:55], s61 offen offset:768 nt\n\t"
    "v_mfma_f32_16x16x32_bf16 v[12:15], v[96:99], a[196:199], v[12:15]\n\t"
    "v_mfma_f32_16x16x32_bf16 v[12:15], v[100:103], a[200:203], v[12:15]\n\t"
    "v_mfma_f32_16x16x32_bf16 v[12:15], v[104:107], a[204:207], v[12:15]\n\t"
    "v_cvt_pk_bf16_f32 v174, v16, v17\n\t"
    "v_cvt_pk_bf16_f32 v175, v18, v19\n\t"
    "v_mfma_f32_16x16x32_bf16 v[12:15], v[108:111], a[208:211], v[12:15]\n\t"
    "v_mfma_f32_16x16x32_bf16 v[12:15], v[112:115], a[212:215], v[12:15]\n\t"
    "v_mfma_f32_16x16x32_bf16 v[16:19], v[116:119], a[192:195], 0\n\t"
    "v_mfma_f32_16x16x32_bf16 v[16:19], v[124:127], a[196:199], v[16:19]\n\t"
    "v_mfma_f32_16x16x32_bf16 v[16:19], v[128:131], a[200:203], v[16:19]\n\t"
    "v_mfma_f32_16x16x32_bf16 v[16:19], v[136:139], a[204:207], v[16:19]\n\t"
    "v_cvt_pk_bf16_f32 v176, v12, v13\n\t"
    "v_cvt_pk_bf16_f32 v177, v14, v15\n\t"
    "v_mfma_f32_16x16x32_bf16 v[16:19], v[140:143], a[208:211], v[16:19]\n\t"
    "v_mfma_f32_16x16x32_bf16 v[16:19], v[144:147], a[212:215], v[16:19]\n\t"
    "v_mfma_f32_16x16x32_bf16 v[12:15], v[92:95], a[216:219], 0\n\t"
    "s_barrier\n\t"
    "v_mfma_f32_16x16x32_bf16 v[12:15], v[96:99], a[220:223], v[12:15]\n\t"
    "buffer_store_dwordx4 v[32:35], v7, s[52:55], s61 offen offset:1152 nt\n\t"
    "v_mfma_f32_16x16x32_bf16 v[12:15], v[100:103], a[224:227], v[12:15]\n\t"
    "v_mfma_f32_16x16x32_bf16 v[12:15], v[104:107], a[228:231], v[12:15]\n\t"
    "v_cvt_pk_bf16_f32 v178, v16, v17\n\t"
    "v_cvt_pk_bf16_f32 v179, v18, v19\n\t"
    "v_mfma_f32_16x16x32_bf16 v[12:15], v[108:111], a[232:235], v[12:15]\n\t"
    "v_mfma_f32_16x16x32_bf16 v[12:15], v[112:115], a[236:239], v[12:15]\n\t"
    "v_mfma_f32_16x16x32_bf16 v[16:19], v[116:119], a[216:219], 0\n\t"
    "v_mfma_f32_16x16x32_bf16 v[16:19], v[124:127], a[220:223], v[16:19]\n\t"
    "v_mfma_f32_16x16x32_bf16 v[16:19], v[128:131], a[224:227], v[16:19]\n\t"
    "v_mfma_f32_16x16x32_bf16 v[16:19], v[136:139], a[228:231], v[16:19]\n\t"
    "v_cvt_pk_bf16_f32 v180, v12, v13\n\t"
    "v_cvt_pk_bf16_f32 v181, v14, v15\n\t"
    "v_mfma_f32_16x16x32_bf16 v[16:19], v[140:143], a[232:235], v[16:19]\n\t"
    "v_mfma_f32_16x16x32_bf16 v[16:19], v[144:147], a[236:239], v[16:19]\n\t"
    "v_mfma_f32_16x16x32_bf16 v[12:15], v[92:95], a[240:243], 0\n\t"
    "v_mfma_f32_16x16x32_bf16 v[12:15], v[96:99], a[244:247], v[12:15]\n\t"
    "buffer_store_dwordx2 v[40:41], v8, s[52:55], s61 offen offset:0 nt\n\t"
    "v_mfma_f32_16x16x32_bf16 v[12:15], v[100:103], a[248:251], v[12:15]\n\t"
    "v_mfma_f32_16x16x32_bf16 v[12:15], v[104:107], a[252:255], v[12:15]\n\t"
    "v_cvt_pk_bf16_f32 v182, v16, v17\n\t"
    "v_cvt_pk_bf16_f32 v183, v18, v19\n\t"
    "v_mfma_f32_16x16x32_bf16 v[12:15], v[108:111], v[224:227], v[12:15]\n\t"
    "v_mfma_f32_16x16x32_bf16 v[12:15], v[112:115], v[228:231], v[12:15]\n\t"
    "v_mfma_f32_16x16x32_bf16 v[16:19], v[116:119], a[240:243], 0\n\t"
    "v_mfma_f32_16x16x32_bf16 v[16:19], v[124:127], a[244:247], v[16:19]\n\t"
    "v_mfma_f32_16x16x32_bf16 v[16:19], v[128:131], a[248:251], v[16:19]\n\t"
    "v_mfma_f32_16x16x32_bf16 v[16:19], v[136:139], a[252:255], v[16:19]\n\t"
    "v_cvt_pk_bf16_f32 v184, v12, v13\n\t"
    "v_cvt_pk_bf16_f32 v185, v14, v15\n\t"
    "v_mfma_f32_16x16x32_bf16 v[16:19], v[140:143], v[224:227], v[16:19]\n\t"
    "v_mfma_f32_16x16x32_bf16 v[16:19], v[144:147], v[228:231], v[16:19]\n\t"
    "v_mfma_f32_16x16x32_bf16 v[12:15], v[92:95], v[232:235], 0\n\t"
    "v_mfma_f32_16x16x32_bf16 v[12:15], v[96:99], v[236:239], v[12:15]\n\t"
    "ds_read_b128 v[20:23], v6 offset:0\n\t"
    "v_mfma_f32_16x16x32_bf16 v[12:15], v[100:103], v[240:243], v[12:15]\n\t"
    "ds_read_b128 v[24:27], v6 offset:1024\n\t"
    "v_mfma_f32_16x16x32_bf16 v[12:15], v[104:107], v[244:247], v[12:15]\n\t"
    "buffer_store_dwordx2 v[42:43], v8, s[52:55], s61 offen offset:384 nt\n\t"
    "v_mfma_f32_16x16x32_bf16 v[12:15], v[108:111], v[248:251], v[12:15]\n\t"
    "v_cvt_pk_bf16_f32 v186, v16, v17\n\t"
    "v_cvt_pk_bf16_f32 v187, v18, v19\n\t"
    "v_mfma_f32_16x16x32_bf16 v[12:15], v[112:115], v[252:255], v[12:15]\n\t"
    "v_mfma_f32_16x16x32_bf16 v[28:31], v[116:119], v[232:235], 0\n\t"
    "v_mfma_f32_16x16x32_bf16 v[28:31], v[124:127], v[236:239], v[28:31]\n\t"
    "v_mfma_f32_16x16x32_bf16 v[28:31], v[128:131], v[240:243], v[28:31]\n\t"
    "v_mfma_f32_16x16x32_bf16 v[28:31], v[136:139], v[244:247], v[28:31]\n\t"
    "v_cvt_pk_bf16_f32 v92, v12, v13\n\t"
    "v_cvt_pk_bf16_f32 v93, v14, v15\n\t"
    "v_mfma_f32_16x16x32_bf16 v[28:31], v[140:143], v[248:251], v[28:31]\n\t"
    "v_mfma_f32_16x16x32_bf16 v[28:31], v[144:147], v[252:255], v[28:31]\n\t"
    "s_waitcnt lgkmcnt(0)\n\t"
    "v_mfma_f32_16x16x32_bf16 v[12:15], v[20:23], v[48:51], 0\n\t"
    "v_mfma_f32_16x16x32_bf16 v[12:15], v[24:27], v[152:155], v[12:15]\n\t"
    "v_mfma_f32_16x16x32_bf16 v[16:19], v[20:23], v[52:55], 0\n\t"
    "v_mfma_f32_16x16x32_bf16 v[16:19], v[24:27], v[120:123], v[16:19]\n\t"
    "buffer_store_dwordx2 v[44:45], v8, s[52:55], s61 offen offset:768 nt\n\t"
    "v_mfma_f32_16x16x32_bf16 v[32:35], v[20:23], v[56:59], 0\n\t"
    "v_cvt_pk_bf16_f32 v94, v28, v29\n\t"
    "v_cvt_pk_bf16_f32 v95, v30, v31\n\t"
    "v_mfma_f32_16x16x32_bf16 v[32:35], v[24:27], v[156:159], v[32:35]\n\t"
    "v_mfma_f32_16x16x32_bf16 v[36:39], v[20:23], v[60:63], 0\n\t"
    "v_mfma_f32_16x16x32_bf16 v[36:39], v[24:27], v[160:163], v[36:39]\n\t"
    "v_cvt_pk_bf16_f32 v40, v12, v16\n\t"
    "v_cvt_pk_bf16_f32 v28, v13, v17\n\t"
    "v_mfma_f32_16x16x32_bf16 v[104:107], v[20:23], v[64:67], 0\n\t"
    "v_cvt_pk_bf16_f32 v100, v14, v18\n\t"
    "v_cvt_pk_bf16_f32 v96, v15, v19\n\t"
    "v_mfma_f32_16x16x32_bf16 v[104:107], v[24:27], v[148:151], v[104:107]\n\t"
    "v_mfma_f32_16x16x32_bf16 v[108:111], v[20:23], v[68:71], 0\n\t"
    "v_mfma_f32_16x16x32_bf16 v[108:111], v[24:27], v[164:167], v[108:111]\n\t"
    "v_cvt_pk_bf16_f32 v41, v32, v36\n\t"
    "v_cvt_pk_bf16_f32 v29, v33, v37\n\t"
    "v_mfma_f32_16x16x32_bf16 v[12:15], v[20:23], v[72:75], 0\n\t"
    "v_cvt_pk_bf16_f32 v101, v34, v38\n\t"
    "v_cvt_pk_bf16_f32 v97, v35, v39\n\t"
    "v_mfma_f32_16x16x32_bf16 v[12:15], v[24:27], v[168:171], v[12:15]\n\t"
    "ds_read_b128 v[16:19], v6 offset:2048\n\t"
    "v_mfma_f32_16x16x32_bf16 v[116:119], v[20:23], v[76:79], 0\n\t"
    "ds_read_b128 v[112:115], v6 offset:3072\n\t"
    "v_mfma_f32_16x16x32_bf16 v[116:119], v[24:27], v[172:175], v[116:119]\n\t"
    "buffer_store_dwordx2 v[46:47], v8, s[52:55], s61 offen offset:1152 nt\n\t"
    "v_mfma_f32_16x16x32_bf16 v[32:35], v[20:23], v[80:83], 0\n\t"
    "s_add_u32 s61, s61, 11403264\n\t"
    "v_cvt_pk_bf16_f32 v42, v104, v108\n\t"
    "v_mfma_f32_16x16x32_bf16 v[32:35], v[24:27], v[176:179], v[32:35]\n\t"
    "v_cvt_pk_bf16_f32 v30, v105, v109\n\t"
    "v_cvt_pk_bf16_f32 v102, v106, v110\n\t"
    "v_mfma_f32_16x16x32_bf16 v[36:39], v[20:23], v[84:87], 0\n\t"
    "v_cvt_pk_bf16_f32 v98, v107, v111\n\t"
    "v_mfma_f32_16x16x32_bf16 v[36:39], v[24:27], v[180:183], v[36:39]\n\t"
    "v_cvt_pk_bf16_f32 v43, v12, v116\n\t"
    "v_cvt_pk_bf16_f32 v31, v13, v117\n\t"
    "v_mfma_f32_16x16x32_bf16 v[44:47], v[20:23], v[88:91], 0\n\t"
    "v_cvt_pk_bf16_f32 v103, v14, v118\n\t"
    "v_cvt_pk_bf16_f32 v99, v15, v119\n\t"
    "v_mfma_f32_16x16x32_bf16 v[44:47], v[24:27], v[184:187], v[44:47]\n\t"
    "v_mfma_f32_16x16x32_bf16 v[104:107], v[20:23], v[132:135], 0\n\t"
    "v_mfma_f32_16x16x32_bf16 v[104:107], v[24:27], v[92:95], v[104:107]\n\t"
    "buffer_store_dwordx4 v[40:43], v7, s[52:55], s61 offen offset:0 nt\n\t"
    "s_waitcnt lgkmcnt(0)\n\t"
    "v_mfma_f32_16x16x32_bf16 v[12:15], v[16:19], v[48:51], 0\n\t"
    "v_cvt_pk_bf16_f32 v108, v32, v36\n\t"
    "v_cvt_pk_bf16_f32 v110, v33, v37\n\t"
    "v_mfma_f32_16x16x32_bf16 v[12:15], v[112:115], v[152:155], v[12:15]\n\t"
    "v_cvt_pk_bf16_f32 v116, v34, v38\n\t"
    "v_cvt_pk_bf16_f32 v118, v35, v39\n\t"
    "v_mfma_f32_16x16x32_bf16 v[20:23], v[16:19], v[52:55], 0\n\t"
    "v_mfma_f32_16x16x32_bf16 v[20:23], v[112:115], v[120:123], v[20:23]\n\t"
    "v_cvt_pk_bf16_f32 v109, v44, v104\n\t"
    "v_cvt_pk_bf16_f32 v111, v45, v105\n\t"
    "v_mfma_f32_16x16x32_bf16 v[24:27], v[16:19], v[56:59], 0\n\t"
    "v_cvt_pk_bf16_f32 v117, v46, v106\n\t"
    "v_cvt_pk_bf16_f32 v119, v47, v107\n\t"
    "v_mfma_f32_16x16x32_bf16 v[24:27], v[112:115], v[156:159], v[24:27]\n\t"
    "v_mfma_f32_16x16x32_bf16 v[32:35], v[16:19], v[60:63], 0\n\t"
    "v_mfma_f32_16x16x32_bf16 v[32:35], v[112:115], v[160:163], v[32:35]\n\t"
    "v_cvt_pk_bf16_f32 v124, v12, v20\n\t"
    "v_cvt_pk_bf16_f32 v104, v13, v21\n\t"
    "v_mfma_f32_16x16x32_bf16 v[36:39], v[16:19], v[64:67], 0\n\t"
    "v_cvt_pk_bf16_f32 v136, v14, v22\n\t"
    "v_cvt_pk_bf16_f32 v128, v15, v23\n\t"
    "v_mfma_f32_16x16x32_bf16 v[36:39], v[112:115], v[148:151], v[36:39]\n\t"
    "v_mfma_f32_16x16x32_bf16 v[40:43], v[16:19], v[68:71], 0\n\t"
    "v_mfma_f32_16x16x32_bf16 v[40:43], v[112:115], v[164:167], v[40:43]\n\t"
    "buffer_store_dwordx4 v[28:31], v7, s[52:55], s61 offen offset:384 nt\n\t"
    "v_mfma_f32_16x16x32_bf16 v[12:15], v[16:19], v[72:75], 0\n\t"
    "v_cvt_pk_bf16_f32 v125, v24, v32\n\t"
    "v_cvt_pk_bf16_f32 v105, v25, v33\n\t"
    "v_mfma_f32_16x16x32_bf16 v[12:15], v[112:115], v[168:171], v[12:15]\n\t"
    "ds_read_b128 v[20:23], v6 offset:4096\n\t"
    "v_cvt_pk_bf16_f32 v137, v26, v34\n\t"
    "v_mfma_f32_16x16x32_bf16 v[44:47], v[16:19], v[76:79], 0\n\t"
    "ds_read_b128 v[28:31], v6 offset:5120\n\t"
    "v_cvt_pk_bf16_f32 v129, v27, v35\n\t"
    "v_mfma_f32_16x16x32_bf16 v[44:47], v[112:115], v[172:175], v[44:47]\n\t"
    "v_cvt_pk_bf16_f32 v126, v36, v40\n\t"
    "v_cvt_pk_bf16_f32 v106, v37, v41\n\t"
    "v_mfma_f32_16x16x32_bf16 v[140:143], v[16:19], v[80:83], 0\n\t"
    "v_cvt_pk_bf16_f32 v138, v38, v42\n\t"
    "v_cvt_pk_bf16_f32 v130, v39, v43\n\t"
    "v_mfma_f32_16x16x32_bf16 v[140:143], v[112:115], v[176:179], v[140:143]\n\t"
    "v_mfma_f32_16x16x32_bf16 v[24:27], v[16:19], v[84:87], 0\n\t"
    "v_mfma_f32_16x16x32_bf16 v[24:27], v[112:115], v[180:183], v[24:27]\n\t"
    "v_cvt_pk_bf16_f32 v127, v12, v44\n\t"
    "v_cvt_pk_bf16_f32 v107, v13, v45\n\t"
    "v_mfma_f32_16x16x32_bf16 v[32:35], v[16:19], v[88:91], 0\n\t"
    "v_cvt_pk_bf16_f32 v139, v14, v46\n\t"
    "v_cvt_pk_bf16_f32 v131, v15, v47\n\t"
    "v_mfma_f32_16x16x32_bf16 v[32:35], v[112:115], v[184:187], v[32:35]\n\t"
    "v_mfma_f32_16x16x32_bf16 v[36:39], v[16:19], v[132:135], 0\n\t"
    "v_mfma_f32_16x16x32_bf16 v[36:39], v[112:115], v[92:95], v[36:39]\n\t"
    "buffer_store_dwordx4 v[100:103], v7, s[52:55], s61 offen offset:768 nt\n\t"
    "s_waitcnt lgkmcnt(0)\n\t"
    "v_mfma_f32_16x16x32_bf16 v[40:43], v[20:23], v[48:51], 0\n\t"
    "v_cvt_pk_bf16_f32 v144, v140, v24\n\t"
    "v_cvt_pk_bf16_f32 v146, v141, v25\n\t"
    "v_mfma_f32_16x16x32_bf16 v[40:43], v[28:31], v[152:155], v[40:43]\n\t"
    "v_cvt_pk_bf16_f32 v188, v142, v26\n\t"
    "v_cvt_pk_bf16_f32 v190, v143, v27\n\t"
    "v_mfma_f32_16x16x32_bf16 v[16:19], v[20:23], v[52:55], 0\n\t"
    "v_mfma_f32_16x16x32_bf16 v[16:19], v[28:31], v[120:123], v[16:19]\n\t"
    "v_cvt_pk_bf16_f32 v145, v32, v36\n\t"
    "v_cvt_pk_bf16_f32 v147, v33, v37\n\t"
    "v_mfma_f32_16x16x32_bf16 v[24:27], v[20:23], v[56:59], 0\n\t"
    "v_cvt_pk_bf16_f32 v189, v34, v38\n\t"
    "v_cvt_pk_bf16_f32 v191, v35, v39\n\t"
    "v_mfma_f32_16x16x32_bf16 v[24:27], v[28:31], v[156:159], v[24:27]\n\t"
    "v_mfma_f32_16x16x32_bf16 v[44:47], v[20:23], v[60:63], 0\n\t"
    "v_mfma_f32_16x16x32_bf16 v[44:47], v[28:31], v[160:163], v[44:47]\n\t"
    "v_cvt_pk_bf16_f32 v100, v40, v16\n\t"
    "v_cvt_pk_bf16_f32 v112, v41, v17\n\t"
    "v_mfma_f32_16x16x32_bf16 v[32:35], v[20:23], v[64:67], 0\n\t"
    "v_cvt_pk_bf16_f32 v192, v42, v18\n\t"
    "v_cvt_pk_bf16_f32 v12, v43, v19\n\t"
    "v_mfma_f32_16x16x32_bf16 v[32:35], v[28:31], v[148:151], v[32:35]\n\t"
    "v_mfma_f32_16x16x32_bf16 v[36:39], v[20:23], v[68:71], 0\n\t"
    "v_mfma_f32_16x16x32_bf16 v[36:39], v[28:31], v[164:167], v[36:39]\n\t"
    "buffer_store_dwordx4 v[96:99], v7, s[52:55], s61 offen offset:1152 nt\n\t"
    "v_mfma_f32_16x16x32_bf16 v[16:19], v[20:23], v[72:75], 0\n\t"
    "s_barrier\n\t"
    "v_cvt_pk_bf16_f32 v101, v24, v44\n\t"
    "v_mfma_f32_16x16x32_bf16 v[16:19], v[28:31], v[168:171], v[16:19]\n\t"
    "ds_read_b128 v[40:43], v6 offset:6144\n\t"
    "v_cvt_pk_bf16_f32 v113, v25, v45\n\t"
    "v_mfma_f32_16x16x32_bf16 v[140:143], v[20:23], v[76:79], 0\n\t"
    "ds_read_b128 v[96:99], v6 offset:7168\n\t"
    "v_cvt_pk_bf16_f32 v193, v26, v46\n\t"
    "v_mfma_f32_16x16x32_bf16 v[140:143], v[28:31], v[172:175], v[140:143]\n\t"
    "v_cvt_pk_bf16_f32 v13, v27, v47\n\t"
    "v_cvt_pk_bf16_f32 v102, v32, v36\n\t"
    "v_mfma_f32_16x16x32_bf16 v[196:199], v[20:23], v[80:83], 0\n\t"
    "v_cvt_pk_bf16_f32 v114, v33, v37\n\t"
    "v_cvt_pk_bf16_f32 v194, v34, v38\n\t"
    "v_mfma_f32_16x16x32_bf16 v[196:199], v[28:31], v[176:179], v[196:199]\n\t"
    "v_cvt_pk_bf16_f32 v14, v35, v39\n\t"
    "v_mfma_f32_16x16x32_bf16 v[24:27], v[20:23], v[84:87], 0\n\t"
    "v_mfma_f32_16x16x32_bf16 v[24:27], v[28:31], v[180:183], v[24:27]\n\t"
    "v_cvt_pk_bf16_f32 v103, v16, v140\n\t"
    "v_cvt_pk_bf16_f32 v115, v17, v141\n\t"
    "v_mfma_f32_16x16x32_bf16 v[32:35], v[20:23], v[88:91], 0\n\t"
    "v_cvt_pk_bf16_f32 v195, v18, v142\n\t"
    "v_cvt_pk_bf16_f32 v15, v19, v143\n\t"
    "v_mfma_f32_16x16x32_bf16 v[32:35], v[28:31], v[184:187], v[32:35]\n\t"
    "v_mfma_f32_16x16x32_bf16 v[36:39], v[20:23], v[132:135], 0\n\t"
    "v_mfma_f32_16x16x32_bf16 v[36:39], v[28:31], v[92:95], v[36:39]\n\t"
    "buffer_store_dwordx2 v[108:109], v8, s[52:55], s61 offen offset:0 nt\n\t"
    "s_waitcnt lgkmcnt(0)\n\t"
    "v_mfma_f32_16x16x32_bf16 v[44:47], v[40:43], v[48:51], 0\n\t"
    "v_cvt_pk_bf16_f32 v16, v196, v24\n\t"
    "v_cvt_pk_bf16_f32 v18, v197, v25\n\t"
    "v_mfma_f32_16x16x32_bf16 v[44:47], v[96:99], v[152:155], v[44:47]\n\t"
    "v_cvt_pk_bf16_f32 v20, v198, v26\n\t"
    "v_cvt_pk_bf16_f32 v22, v199, v27\n\t"
    "v_mfma_f32_16x16x32_bf16 v[140:143], v[40:43], v[52:55], 0\n\t"
    "v_mfma_f32_16x16x32_bf16 v[140:143], v[96:99], v[120:123], v[140:143]\n\t"
    "v_cvt_pk_bf16_f32 v17, v32, v36\n\t"
    "v_cvt_pk_bf16_f32 v19, v33, v37\n\t"
    "v_mfma_f32_16x16x32_bf16 v[48:51], v[40:43], v[56:59], 0\n\t"
    "v_cvt_pk_bf16_f32 v21, v34, v38\n\t"
    "v_cvt_pk_bf16_f32 v23, v35, v39\n\t"
    "v_mfma_f32_16x16x32_bf16 v[48:51], v[96:99], v[156:159], v[48:51]\n\t"
    "v_mfma_f32_16x16x32_bf16 v[52:55], v[40:43], v[60:63], 0\n\t"
    "v_mfma_f32_16x16x32_bf16 v[52:55], v[96:99], v[160:163], v[52:55]\n\t"
    "v_cvt_pk_bf16_f32 v28, v44, v140\n\t"
    "v_cvt_pk_bf16_f32 v24, v45, v141\n\t"
    "v_mfma_f32_16x16x32_bf16 v[56:59], v[40:43], v[64:67], 0\n\t"
    "v_cvt_pk_bf16_f32 v36, v46, v142\n\t"
    "v_cvt_pk_bf16_f32 v32, v47, v143\n\t"
    "v_mfma_f32_16x16x32_bf16 v[56:59], v[96:99], v[148:151], v[56:59]\n\t"
    "v_mfma_f32_16x16x32_bf16 v[60:63], v[40:43], v[68:71], 0\n\t"
    "v_mfma_f32_16x16x32_bf16 v[60:63], v[96:99], v[164:167], v[60:63]\n\t"
    "buffer_store_dwordx2 v[110:111], v8, s[52:55], s61 offen offset:384 nt\n\t"
    "v_mfma_f32_16x16x32_bf16 v[64:67], v[40:43], v[72:75], 0\n\t"
    "ds_read_b128 v[120:123], v0 offset:0\n\t"
    "v_cvt_pk_bf16_f32 v29, v48, v52\n\t"
    "v_mfma_f32_16x16x32_bf16 v[64:67], v[96:99], v[168:171], v[64:67]\n\t"
    "ds_read_b128 v[140:143], v1 offset:0\n\t"
    "v_cvt_pk_bf16_f32 v25, v49, v53\n\t"
    "v_mfma_f32_16x16x32_bf16 v[68:71], v[40:43], v[76:79], 0\n\t"
    "ds_read_b128 v[148:151], v2 offset:0\n\t"
    "v_cvt_pk_bf16_f32 v37, v50, v54\n\t"
    "v_mfma_f32_16x16x32_bf16 v[68:71], v[96:99], v[172:175], v[68:71]\n\t"
    "ds_read_b128 v[152:155], v3 offset:0\n\t"
    "v_cvt_pk_bf16_f32 v33, v51, v55\n\t"
    "v_mfma_f32_16x16x32_bf16 v[72:75], v[40:43], v[80:83], 0\n\t"
    "ds_read_b128 v[156:159], v4 offset:0\n\t"
    "v_cvt_pk_bf16_f32 v30, v56, v60\n\t"
    "v_mfma_f32_16x16x32_bf16 v[72:75], v[96:99], v[176:179], v[72:75]\n\t"
    "ds_read_b128 v[160:163], v5 offset:0\n\t"
    "v_cvt_pk_bf16_f32 v26, v57, v61\n\t"
    "v_mfma_f32_16x16x32_bf16 v[48:51], v[40:43], v[84:87], 0\n\t"
    "ds_read_b128 v[164:167], v0 offset:6144\n\t"
    "v_cvt_pk_bf16_f32 v38, v58, v62\n\t"
    "v_mfma_f32_16x16x32_bf16 v[48:51], v[96:99], v[180:183], v[48:51]\n\t"
    "ds_read_b128 v[168:171], v1 offset:6144\n\t"
    "v_cvt_pk_bf16_f32 v34, v59, v63\n\t"
    "v_mfma_f32_16x16x32_bf16 v[52:55], v[40:43], v[88:91], 0\n\t"
    "ds_read_b128 v[172:175], v2 offset:6144\n\t"
    "v_cvt_pk_bf16_f32 v31, v64, v68\n\t"
    "v_mfma_f32_16x16x32_bf16 v[52:55], v[96:99], v[184:187], v[52:55]\n\t"
    "ds_read_b128 v[176:179], v3 offset:6144\n\t"
    "v_cvt_pk_bf16_f32 v27, v65, v69\n\t"
    "v_mfma_f32_16x16x32_bf16 v[56:59], v[40:43], v[132:135], 0\n\t"
    "ds_read_b128 v[180:183], v4 offset:6144\n\t"
    "v_cvt_pk_bf16_f32 v39, v66, v70\n\t"
    "v_mfma_f32_16x16x32_bf16 v[56:59], v[96:99], v[92:95], v[56:59]\n\t"
    "ds_read_b128 v[196:199], v5 offset:6144\n\t"
    "s_sub_u32 s60, s60, 1\n\t"
    "s_cmp_lg_u32 s60, 0\n\t"
    "s_cbranch_scc1 .Lf10_%=\n\t"
    "s_nop 7\n\t"
    "s_nop 1\n\t"
    "buffer_store_dwordx2 v[116:117], v8, s[52:55], s61 offen offset:768 nt\n\t"
    "v_cvt_pk_bf16_f32 v35, v67, v71\n\t"
    "v_cvt_pk_bf16_f32 v40, v72, v48\n\t"
    "v_cvt_pk_bf16_f32 v42, v73, v49\n\t"
    "v_cvt_pk_bf16_f32 v44, v74, v50\n\t"
    "v_cvt_pk_bf16_f32 v46, v75, v51\n\t"
    "v_cvt_pk_bf16_f32 v41, v52, v56\n\t"
    "v_cvt_pk_bf16_f32 v43, v53, v57\n\t"
    "v_cvt_pk_bf16_f32 v45, v54, v58\n\t"
    "v_cvt_pk_bf16_f32 v47, v55, v59\n\t"
    "buffer_store_dwordx2 v[118:119], v8, s[52:55], s61 offen offset:1152 nt\n\t"
    "s_add_u32 s61, s61, 393216\n\t"
    "buffer_store_dwordx4 v[124:127], v7, s[52:55], s61 offen offset:0 nt\n\t"
    "buffer_store_dwordx4 v[104:107], v7, s[52:55], s61 offen offset:384 nt\n\t"
    "buffer_store_dwordx4 v[136:139], v7, s[52:55], s61 offen offset:768 nt\n\t"
    "buffer_store_dwordx4 v[128:131], v7, s[52:55], s61 offen offset:1152 nt\n\t"
    "buffer_store_dwordx2 v[144:145], v8, s[52:55], s61 offen offset:0 nt\n\t"
    "buffer_store_dwordx2 v[146:147], v8, s[52:55], s61 offen offset:384 nt\n\t"
    "buffer_store_dwordx2 v[188:189], v8, s[52:55], s61 offen offset:768 nt\n\t"
    "buffer_store_dwordx2 v[190:191], v8, s[52:55], s61 offen offset:1152 nt\n\t"
    "s_add_u32 s61, s61, 393216\n\t"
    "buffer_store_dwordx4 v[100:103], v7, s[52:55], s61 offen offset:0 nt\n\t"
    "buffer_store_dwordx4 v[112:115], v7, s[52:55], s61 offen offset:384 nt\n\t"
    "buffer_store_dwordx4 v[192:195], v7, s[52:55], s61 offen offset:768 nt\n\t"
    "buffer_store_dwordx4 v[12:15], v7, s[52:55], s61 offen offset:1152 nt\n\t"
    "buffer_store_dwordx2 v[16:17], v8, s[52:55], s61 offen offset:0 nt\n\t"
    "buffer_store_dwordx2 v[18:19], v8, s[52:55], s61 offen offset:384 nt\n\t"
    "buffer_store_dwordx2 v[20:21], v8, s[52:55], s61 offen offset:768 nt\n\t"
    "buffer_store_dwordx2 v[22:23], v8, s[52:55], s61 offen offset:1152 nt\n\t"
    "s_add_u32 s61, s61, 393216\n\t"
    "buffer_store_dwordx4 v[28:31], v7, s[52:55], s61 offen offset:0 nt\n\t"
    "buffer_store_dwordx4 v[24:27], v7, s[52:55], s61 offen offset:384 nt\n\t"
    "buffer_store_dwordx4 v[36:39], v7, s[52:55], s61 offen offset:768 nt\n\t"
    "buffer_store_dwordx4 v[32:35], v7, s[52:55], s61 offen offset:1152 nt\n\t"
    "buffer_store_dwordx2 v[40:41], v8, s[52:55], s61 offen offset:0 nt\n\t"
    "buffer_store_dwordx2 v[42:43], v8, s[52:55], s61 offen offset:384 nt\n\t"
    "buffer_store_dwordx2 v[44:45], v8, s[52:55], s61 offen offset:768 nt\n\t"
    "buffer_store_dwordx2 v[46:47], v8, s[52:55], s61 offen offset:1152 nt\n\t"
    "s_add_u32 s61, s61, 11403264\n\t"
    "s_endpgm\n\t"
    ".p2align 6\n\t"
    ".Lpl_%=:\n\t"
    "s_mov_b32 s52, s40\n\t"
    "s_and_b32 s53, s41, 0xffff\n\t"
    "s_mul_i32 s65, %[bid], 0x1556\n\t"
    "s_lshr_b32 s65, s65, 16\n\t"
    "s_mul_i32 s69, s65, 12\n\t"
    "s_sub_u32 s69, %[bid], s69\n\t"
    "s_lshr_b32 s69, s69, 1\n\t"
    "v_lshrrev_b32_e32 v27, 2, v31\n\t"
    "v_mul_u32_u24_e32 v27, 0x180, v27\n\t"
    "v_and_b32_e32 v28, 3, v31\n\t"
    "v_lshl_or_b32 v27, v28, 4, v27\n\t"
    "s_lshl_b32 s60, s65, 6\n\t"
    "s_mov_b32 m0, 86016\n\t"
    "s_mul_i32 s61, s69, 36864\n\t"
    "buffer_load_dwordx4 v27, s[52:55], s60 offen lds\n\t"
    "s_mov_b32 m0, 88064\n\t"
    "s_add_u32 s60, s60, 6144\n\t"
    "buffer_load_dwordx4 v27, s[52:55], s60 offen lds\n\t"
    "s_mov_b32 m0, 90112\n\t"
    "s_add_u32 s60, s60, 6144\n\t"
    "buffer_load_dwordx4 v27, s[52:55], s60 offen lds\n\t"
    "s_mov_b32 m0, 92160\n\t"
    "s_add_u32 s60, s60, 6144\n\t"
    "buffer_load_dwordx4 v27, s[52:55], s60 offen lds\n\t"
    "s_mov_b32 m0, 94208\n\t"
    "s_add_u32 s60, s60, 6144\n\t"
    "buffer_load_dwordx4 v27, s[52:55], s60 offen lds\n\t"
    "s_mov_b32 m0, 96256\n\t"
    "s_add_u32 s60, s60, 6144\n\t"
    "buffer_load_dwordx4 v27, s[52:55], s60 offen lds\n\t"
    "s_mov_b32 m0, 98304\n\t"
    "s_add_u32 s60, s60, 6144\n\t"
    "buffer_load_dwordx4 v27, s[52:55], s60 offen lds\n\t"
    "s_mov_b32 m0, 100352\n\t"
    "s_add_u32 s60, s60, 6144\n\t"
    "buffer_load_dwordx4 v27, s[52:55], s60 offen lds\n\t"
    "s_mov_b32 m0, 102400\n\t"
    "s_add_u32 s60, s60, 6144\n\t"
    "buffer_load_dwordx4 v27, s[52:55], s60 offen lds\n\t"
    "s_mov_b32 m0, 104448\n\t"
    "s_add_u32 s60, s60, 6144\n\t"
    "buffer_load_dwordx4 v27, s[52:55], s60 offen lds\n\t"
    "s_mov_b32 m0, 106496\n\t"
    "s_add_u32 s60, s60, 6144\n\t"
    "buffer_load_dwordx4 v27, s[52:55], s60 offen lds\n\t"
    "s_mov_b32 m0, 108544\n\t"
    "s_add_u32 s60, s60, 6144\n\t"
    "buffer_load_dwordx4 v27, s[52:55], s60 offen lds\n\t"
    "s_add_u32 s60, s61, 768\n\t"
    "s_add_u32 s61, s61, 5376\n\t"
    "s_mov_b32 m0, 147456\n\t"
    "s_nop 0\n\t"
    "buffer_load_dwordx4 v24, s[48:51], s60 offen offset:0 lds\n\t"
    "s_mov_b32 m0, 151424\n\t"
    "s_nop 0\n\t"
    "buffer_load_dwordx4 v24, s[48:51], s60 offen offset:128 lds\n\t"
    "s_mov_b32 m0, 155392\n\t"
    "s_nop 0\n\t"
    "buffer_load_dwordx4 v24, s[48:51], s60 offen offset:256 lds\n\t"
    "s_mov_b32 m0, 149504\n\t"
    "s_nop 0\n\t"
    "buffer_load_dwordx4 v25, s[48:51], s60 offen offset:0 lds\n\t"
    "s_mov_b32 m0, 153472\n\t"
    "s_nop 0\n\t"
    "buffer_load_dwordx4 v25, s[48:51], s60 offen offset:128 lds\n\t"
    "s_mov_b32 m0, 157440\n\t"
    "s_nop 0\n\t"
    "buffer_load_dwordx4 v25, s[48:51], s60 offen offset:256 lds\n\t"
    "s_mov_b32 m0, 148480\n\t"
    "s_nop 0\n\t"
    "buffer_load_dwordx4 v24, s[48:51], s61 offen offset:0 lds\n\t"
    "s_mov_b32 m0, 152448\n\t"
    "s_nop 0\n\t"
    "buffer_load_dwordx4 v24, s[48:51], s61 offen offset:128 lds\n\t"
    "s_mov_b32 m0, 156416\n\t"
    "s_nop 0\n\t"
    "buffer_load_dwordx4 v24, s[48:51], s61 offen offset:256 lds\n\t"
    "s_mov_b32 m0, 150528\n\t"
    "s_nop 0\n\t"
    "buffer_load_dwordx4 v25, s[48:51], s61 offen offset:0 lds\n\t"
    "s_mov_b32 m0, 154496\n\t"
    "s_nop 0\n\t"
    "buffer_load_dwordx4 v25, s[48:51], s61 offen offset:128 lds\n\t"
    "s_mov_b32 m0, 158464\n\t"
    "s_nop 0\n\t"
    "buffer_load_dwordx4 v25, s[48:51], s61 offen offset:256 lds\n\t"
    "s_branch .Lpb_%=\n\t"
    ".Lrolm_%=:\n\t"
    "v_lshlrev_b32_e32 v30, 4, v31\n\t"
    "v_and_b32_e32 v28, 7, v31\n\t"
    "v_bfe_u32 v27, v31, 3, 1\n\t"
    "v_lshrrev_b32_e32 v29, 4, v31\n\t"
    "v_bfe_u32 v20, v31, 1, 3\n\t"
    "v_xor_b32_e32 v20, v20, v29\n\t"
    "v_lshlrev_b32_e32 v21, 7, v28\n\t"
    "v_lshl_or_b32 v21, v27, 11, v21\n\t"
    "v_lshl_or_b32 v21, v20, 4, v21\n\t"
    "v_add_u32_e32 v11, %[tb], v30\n\t"
    "v_lshlrev_b32_e32 v9, 10, v27\n\t"
    "v_sub_u32_e32 v9, v21, v9\n\t"
    "v_add_u32_e32 v9, %[kl], v9\n\t"
    "v_xor_b32_e32 v10, 64, v9\n\t"
    "v_add_u32_e32 v12, 0x9000, v21\n\t"
    "v_xor_b32_e32 v13, 64, v12\n\t"
    "v_add_u32_e32 v14, %[wn], v30\n\t"
    "v_add_u32_e32 v6, %[pl], v30\n\t"
    "v_mul_u32_u24_e32 v27, 0xc00, v27\n\t"
    "v_add_u32_e32 v27, %[rbeg], v27\n\t"
    "v_mad_u32_u24 v20, v28, 3, 0\n\t"
    "v_and_b32_e32 v21, 6, v20\n\t"
    "v_xor_b32_e32 v21, v21, v29\n\t"
    "v_lshl_add_u32 v20, v20, 7, v27\n\t"
    "v_lshl_add_u32 v0, v21, 4, v20\n\t"
    "v_xor_b32_e32 v1, 64, v0\n\t"
    "v_mad_u32_u24 v20, v28, 3, 1\n\t"
    "v_and_b32_e32 v21, 6, v20\n\t"
    "v_xor_b32_e32 v21, v21, v29\n\t"
    "v_lshl_add_u32 v20, v20, 7, v27\n\t"
    "v_lshl_add_u32 v2, v21, 4, v20\n\t"
    "v_xor_b32_e32 v3, 64, v2\n\t"
    "v_mad_u32_u24 v20, v28, 3, 2\n\t"
    "v_and_b32_e32 v21, 6, v20\n\t"
    "v_xor_b32_e32 v21, v21, v29\n\t"
    "v_lshl_add_u32 v20, v20, 7, v27\n\t"
    "v_lshl_add_u32 v4, v21, 4, v20\n\t"
    "v_xor_b32_e32 v5, 64, v4\n\t"
    "s_add_u32 m0, %[kl], 9984\n\t"
    "s_nop 0\n\t"
    "buffer_load_dwordx4 v24, s[48:51], %[k1] offen offset:256 lds\n\t"
    "s_add_u32 m0, %[kl], 11008\n\t"
    "s_nop 0\n\t"
    "buffer_load_dwordx4 v25, s[48:51], %[k1] offen offset:256 lds\n\t"
    "s_add_u32 m0, %[kl], 12288\n\t"
    "s_nop 0\n\t"
    "buffer_load_dwordx4 v24, s[48:51], %[k2] offen offset:0 lds\n\t"
    "s_add_u32 m0, %[kl], 13312\n\t"
    "s_nop 0\n\t"
    "buffer_load_dwordx4 v25, s[48:51], %[k2] offen offset:0 lds\n\t"
    "s_add_u32 m0, %[kl], 14208\n\t"
    "s_nop 0\n\t"
    "buffer_load_dwordx4 v24, s[48:51], %[k2] offen offset:128 lds\n\t"
    "s_add_u32 m0, %[kl], 15232\n\t"
    "s_nop 0\n\t"
    "buffer_load_dwordx4 v25, s[48:51], %[k2] offen offset:128 lds\n\t"
    "s_add_u32 m0, %[kl], 16128\n\t"
    "s_nop 0\n\t"
    "buffer_load_dwordx4 v24, s[48:51], %[k2] offen offset:256 lds\n\t"
    "s_add_u32 m0, %[kl], 17152\n\t"
    "s_nop 0\n\t"
    "buffer_load_dwordx4 v25, s[48:51], %[k2] offen offset:256 lds\n\t"
    "v_lshrrev_b32_e32 v20, 3, v31\n\t"
    "v_and_b32_e32 v21, 6, v20\n\t"
    "v_xor_b32_e32 v21, v21, v28\n\t"
    "v_lshlrev_b32_e32 v20, 7, v20\n\t"
    "v_lshl_or_b32 v7, v21, 4, v20\n\t"
    "v_add_u32_e32 v15, 0x9000, v30\n\t"
    "s_mov_b32 s68, 0x3dd53b95\n\t"
    "s_mov_b32 s52, s36\n\t"
    "s_and_b32 s53, s37, 0xffff\n\t"
    "s_mov_b32 s54, 0x6000000\n\t"
    "s_mov_b32 s55, 0x20000\n\t"
    "s_mov_b32 s67, 6\n\t"
    "s_cmp_eq_u32 %[prod], 0\n\t"
    "s_cbranch_scc1 .Ls1_%=\n\t"
    "buffer_load_dwordx2 v[22:23], off, s[56:59], %[eoff] sc1\n\t"
    "s_and_b32 s62, %[bid], 1\n\t"
    "s_and_b32 s63, s65, 1\n\t"
    "s_lshl_b32 s63, s63, 1\n\t"
    "s_add_u32 s63, s63, s62\n\t"
    "s_lshl_b32 s64, s62, 2\n\t"
    "s_lshl_b32 s62, s62, 4\n\t"
    "s_cmp_lt_u32 s65, 4\n\t"
    "s_cselect_b32 s60, s64, 8\n\t"
    "s_cselect_b32 s63, s65, s63\n\t"
    "s_cselect_b32 s62, s64, s62\n\t"
    "s_cselect_b32 s61, 4, 3\n\t"
    "s_lshl_b32 s62, s62, 1\n\t"
    "s_mul_i32 s60, s60, 6\n\t"
    "s_add_u32 s60, s60, s69\n\t"
    "s_lshl_b32 s60, s60, 2\n\t"
    "s_add_u32 s60, s60, s63\n\t"
    "s_lshl_b32 s60, s60, 8\n\t"
    "v_and_b32_e32 v16, 3, v31\n\t"
    "v_bfe_u32 v17, v31, 2, 2\n\t"
    "v_lshlrev_b32_e32 v18, s61, v16\n\t"
    "v_lshl_add_u32 v18, v17, 6, v18\n\t"
    "v_and_b32_e32 v19, 1, v29\n\t"
    "v_lshl_add_u32 v18, v19, 9, v18\n\t"
    "v_lshrrev_b32_e32 v19, 1, v29\n\t"
    "v_lshl_add_u32 v18, v19, 11, v18\n\t"
    "s_add_u32 s62, s62, 86016\n\t"
    "v_add_u32_e32 v18, s62, v18\n\t"
    "v_lshl_add_u32 v19, v16, 2, v17\n\t"
    "v_lshl_add_u32 v19, v29, 4, v19\n\t"
    "v_lshlrev_b32_e32 v19, 4, v19\n\t"
    "v_add_u32_e32 v19, %[tb], v19\n\t"
    "v_mul_u32_u24_e32 v17, 0x1800, v17\n\t"
    "v_lshl_add_u32 v20, v16, 4, v17\n\t"
    "v_lshl_add_u32 v20, v29, 6, v20\n\t"
    "v_add_u32_e32 v20, s60, v20\n\t"
    "v_add_u32_e32 v16, 0x1b000, v12\n\t"
    "v_add_u32_e32 v17, 0x1b000, v13\n\t"
    "s_waitcnt vmcnt(31)\n\t"
    "ds_read_b64_tr_b16 v[80:81], v18 offset:0\n\t"
    "ds_read_b64_tr_b16 v[82:83], v18 offset:256\n\t"
    "ds_read_b64_tr_b16 v[84:85], v18 offset:4096\n\t"
    "ds_read_b64_tr_b16 v[86:87], v18 offset:4352\n\t"
    "ds_read_b64_tr_b16 v[88:89], v18 offset:8192\n\t"
    "ds_read_b64_tr_b16 v[90:91], v18 offset:8448\n\t"
    "ds_read_b64_tr_b16 v[92:93], v18 offset:12288\n\t"
    "ds_read_b64_tr_b16 v[94:95], v18 offset:12544\n\t"
    "ds_read_b64_tr_b16 v[96:97], v18 offset:16384\n\t"
    "ds_read_b64_tr_b16 v[98:99], v18 offset:16640\n\t"
    "ds_read_b64_tr_b16 v[100:101], v18 offset:20480\n\t"
    "ds_read_b64_tr_b16 v[102:103], v18 offset:20736\n\t"
    "s_waitcnt vmcnt(19)\n\t"
    "ds_read_b128 v[32:35], v16 offset:0\n\t"
    "ds_read_b128 v[36:39], v17 offset:0\n\t"
    "ds_read_b128 v[40:43], v16 offset:4096\n\t"
    "ds_read_b128 v[44:47], v17 offset:4096\n\t"
    "ds_read_b128 v[48:51], v16 offset:8192\n\t"
    "ds_read_b128 v[52:55], v17 offset:8192\n\t"
    "ds_read_b128 v[56:59], v16 offset:1024\n\t"
    "ds_read_b128 v[60:63], v17 offset:1024\n\t"
    "ds_read_b128 v[64:67], v16 offset:5120\n\t"
    "ds_read_b128 v[68:71], v17 offset:5120\n\t"
    "ds_read_b128 v[72:75], v16 offset:9216\n\t"
    "ds_read_b128 v[76:79], v17 offset:9216\n\t"
    "s_waitcnt lgkmcnt(11)\n\t"
    "v_mfma_f32_16x16x32_bf16 v[108:111], v[32:35], v[80:83], 0\n\t"
    "s_waitcnt lgkmcnt(10)\n\t"
    "v_mfma_f32_16x16x32_bf16 v[108:111], v[36:39], v[84:87], v[108:111]\n\t"
    "s_waitcnt lgkmcnt(9)\n\t"
    "v_mfma_f32_16x16x32_bf16 v[108:111], v[40:43], v[88:91], v[108:111]\n\t"
    "s_waitcnt lgkmcnt(8)\n\t"
    "v_mfma_f32_16x16x32_bf16 v[108:111], v[44:47], v[92:95], v[108:111]\n\t"
    "s_waitcnt lgkmcnt(7)\n\t"
    "v_mfma_f32_16x16x32_bf16 v[108:111], v[48:51], v[96:99], v[108:111]\n\t"
    "s_waitcnt lgkmcnt(6)\n\t"
    "v_mfma_f32_16x16x32_bf16 v[108:111], v[52:55], v[100:103], v[108:111]\n\t"
    "s_waitcnt lgkmcnt(5)\n\t"
    "v_mfma_f32_16x16x32_bf16 v[112:115], v[56:59], v[80:83], 0\n\t"
    "s_waitcnt lgkmcnt(4)\n\t"
    "v_mfma_f32_16x16x32_bf16 v[112:115], v[60:63], v[84:87], v[112:115]\n\t"
    "s_waitcnt lgkmcnt(3)\n\t"
    "v_mfma_f32_16x16x32_bf16 v[112:115], v[64:67], v[88:91], v[112:115]\n\t"
    "s_waitcnt lgkmcnt(2)\n\t"
    "v_mfma_f32_16x16x32_bf16 v[112:115], v[68:71], v[92:95], v[112:115]\n\t"
    "s_waitcnt lgkmcnt(1)\n\t"
    "v_mfma_f32_16x16x32_bf16 v[112:115], v[72:75], v[96:99], v[112:115]\n\t"
    "s_waitcnt lgkmcnt(0)\n\t"
    "v_mfma_f32_16x16x32_bf16 v[112:115], v[76:79], v[100:103], v[112:115]\n\t"
    "v_cvt_pk_bf16_f32 v104, v108, v109\n\t"
    "v_cvt_pk_bf16_f32 v105, v110, v111\n\t"
    "s_nop 6\n\t"
    "v_cvt_pk_bf16_f32 v106, v112, v113\n\t"
    "v_cvt_pk_bf16_f32 v107, v114, v115\n\t"
    "ds_write_b128 v19, v[104:107]\n\t"
    "ds_read_b128 v[104:107], v11\n\t"
    "s_waitcnt lgkmcnt(0)\n\t"
    "buffer_store_dwordx4 v[104:107], v20, s[56:59], 0 offen sc1\n\t"
    ".Ls1_%=:\n\t"
    "s_add_u32 m0, %[wql], 49152\n\t"
    "s_add_u32 s61, %[wqg], 147456\n\t"
    "buffer_load_dwordx4 v24, s[48:51], s61 offen offset:0 lds\n\t"
    "s_add_u32 m0, %[wql], 53120\n\t"
    "s_nop 0\n\t"
    "buffer_load_dwordx4 v24, s[48:51], s61 offen offset:128 lds\n\t"
    "s_add_u32 m0, %[wql], 57088\n\t"
    "s_nop 0\n\t"
    "buffer_load_dwordx4 v24, s[48:51], s61 offen offset:256 lds\n\t"
    "s_add_u32 m0, %[wql], 51200\n\t"
    "s_nop 0\n\t"
    "buffer_load_dwordx4 v25, s[48:51], s61 offen offset:0 lds\n\t"
    "s_add_u32 m0, %[wql], 55168\n\t"
    "s_nop 0\n\t"
    "buffer_load_dwordx4 v25, s[48:51], s61 offen offset:128 lds\n\t"
    "s_add_u32 m0, %[wql], 59136\n\t"
    "s_nop 0\n\t"
    "buffer_load_dwordx4 v25, s[48:51], s61 offen offset:256 lds\n\t"
    "s_add_u32 m0, %[wql], 61440\n\t"
    "s_add_u32 s61, %[wqg], 184320\n\t"
    "buffer_load_dwordx4 v24, s[48:51], s61 offen offset:0 lds\n\t"
    "s_add_u32 m0, %[wql], 65408\n\t"
    "s_nop 0\n\t"
    "buffer_load_dwordx4 v24, s[48:51], s61 offen offset:128 lds\n\t"
    "s_add_u32 m0, %[wql], 69376\n\t"
    "s_nop 0\n\t"
    "buffer_load_dwordx4 v24, s[48:51], s61 offen offset:256 lds\n\t"
    "s_add_u32 m0, %[wql], 63488\n\t"
    "s_nop 0\n\t"
    "buffer_load_dwordx4 v25, s[48:51], s61 offen offset:0 lds\n\t"
    "s_add_u32 m0, %[wql], 67456\n\t"
    "s_nop 0\n\t"
    "buffer_load_dwordx4 v25, s[48:51], s61 offen offset:128 lds\n\t"
    "s_add_u32 m0, %[wql], 71424\n\t"
    "s_nop 0\n\t"
    "buffer_load_dwordx4 v25, s[48:51], s61 offen offset:256 lds\n\t"
    "s_waitcnt vmcnt(24)\n\t"
    "ds_read_b128 v[116:119], v9 offset:0\n\t"
    "ds_read_b128 v[120:123], v10 offset:0\n\t"
    "ds_read_b128 v[124:127], v9 offset:2048\n\t"
    "ds_read_b128 v[128:131], v10 offset:2048\n\t"
    "ds_read_b128 v[132:135], v9 offset:4096\n\t"
    "ds_read_b128 v[136:139], v10 offset:4096\n\t"
    "s_waitcnt vmcnt(18)\n\t"
    "ds_read_b128 v[140:143], v9 offset:6144\n\t"
    "ds_read_b128 v[144:147], v10 offset:6144\n\t"
    "ds_read_b128 v[148:151], v9 offset:8192\n\t"
    "ds_read_b128 v[152:155], v10 offset:8192\n\t"
    "ds_read_b128 v[156:159], v9 offset:10240\n\t"
    "ds_read_b128 v[160:163], v10 offset:10240\n\t"
    "s_cmp_eq_u32 %[prod], 0\n\t"
    "s_waitcnt vmcnt(14)\n\t"
    "s_cbranch_scc0 .Lt_%=\n\t"
    "s_waitcnt vmcnt(12)\n\t"
    ".Lt_%=:\n\t"
    "ds_read_b128 v[164:167], v9 offset:12288\n\t"
    "ds_read_b128 v[168:171], v10 offset:12288\n\t"
    "ds_read_b128 v[172:175], v9 offset:14336\n\t"
    "ds_read_b128 v[176:179], v10 offset:14336\n\t"
    "ds_read_b128 v[180:183], v9 offset:16384\n\t"
    "ds_read_b128 v[184:187], v10 offset:16384\n\t"
    "s_mov_b32 s66, %[rbeg]\n\t"
    "s_mov_b32 s61, %[wb]\n\t"
    "s_waitcnt lgkmcnt(0)\n\t"
    "s_barrier\n\t"
    "ds_read_b128 v[32:35], v12 offset:0\n\t"
    "ds_read_b128 v[36:39], v13 offset:0\n\t"
    "ds_read_b128 v[40:43], v12 offset:4096\n\t"
    "ds_read_b128 v[44:47], v13 offset:4096\n\t"
    "ds_read_b128 v[48:51], v12 offset:8192\n\t"
    "ds_read_b128 v[52:55], v13 offset:8192\n\t"
    "s_mov_b32 s60, 6\n\t"
    "s_branch .Lf2_%=\n\t"
    ".Lfs0_%=:\n\t"
    "s_waitcnt vmcnt(0)\n\t"
    "s_cmp_eq_u32 %[prod], 0\n\t"
    "s_cbranch_scc1 .Lfb0_%=\n\t"
    "v_add_co_u32_e32 v22, vcc, 0x7f4a7c15, v22\n\t"
    "v_mov_b32_e32 v16, 0x9e3779b9\n\t"
    "s_nop 0\n\t"
    "v_addc_co_u32_e32 v23, vcc, v16, v23, vcc\n\t"
    "s_mov_b64 exec, 1\n\t"
    "buffer_store_dwordx2 v[22:23], off, s[56:59], %[foff] sc1\n\t"
    "s_mov_b64 exec, -1\n\t"
    "s_branch .Lfb0_%=\n\t"
    ".Lf2_%=:\n\t"
    "s_waitcnt lgkmcnt(3)\n\t"
    "v_mfma_f32_16x16x32_bf16 v[80:83], v[32:35], v[116:119], 0\n\t"
    "ds_write_b128 v11, v[108:111] offset:1024\n\t"
    "v_mfma_f32_16x16x32_bf16 v[80:83], v[36:39], v[120:123], v[80:83]\n\t"
    "ds_read_b128 v[56:59], v12 offset:1024\n\t"
    "v_mfma_f32_16x16x32_bf16 v[80:83], v[40:43], v[124:127], v[80:83]\n\t"
    "s_waitcnt lgkmcnt(2)\n\t"
    "v_mfma_f32_16x16x32_bf16 v[80:83], v[44:47], v[128:131], v[80:83]\n\t"
    "ds_read_b128 v[60:63], v13 offset:1024\n\t"
    "v_cvt_pk_bf16_f32 v114, v100, v101\n\t"
    "v_mfma_f32_16x16x32_bf16 v[80:83], v[48:51], v[132:135], v[80:83]\n\t"
    "v_cvt_pk_bf16_f32 v115, v102, v103\n\t"
    "v_mfma_f32_16x16x32_bf16 v[80:83], v[52:55], v[136:139], v[80:83]\n\t"
    "ds_read_b128 v[64:67], v12 offset:5120\n\t"
    "v_mfma_f32_16x16x32_bf16 v[88:91], v[32:35], v[140:143], 0\n\t"
    "ds_write_b128 v11, v[112:115] offset:2048\n\t"
    "v_mfma_f32_16x16x32_bf16 v[88:91], v[36:39], v[144:147], v[88:91]\n\t"
    "ds_read_b128 v[68:71], v13 offset:5120\n\t"
    "v_mfma_f32_16x16x32_bf16 v[88:91], v[40:43], v[148:151], v[88:91]\n\t"
    "v_mov_b32_e32 v11, v14\n\t"
    "v_add_u32_e32 v14, 0x3000, v14\n\t"
    "v_mfma_f32_16x16x32_bf16 v[88:91], v[44:47], v[152:155], v[88:91]\n\t"
    "ds_read_b128 v[72:75], v12 offset:9216\n\t"
    "v_cvt_pk_bf16_f32 v104, v80, v81\n\t"
    "v_mfma_f32_16x16x32_bf16 v[88:91], v[48:51], v[156:159], v[88:91]\n\t"
    "v_cvt_pk_bf16_f32 v105, v82, v83\n\t"
    "v_mfma_f32_16x16x32_bf16 v[88:91], v[52:55], v[160:163], v[88:91]\n\t"
    "ds_read_b128 v[76:79], v13 offset:9216\n\t"
    "v_mfma_f32_16x16x32_bf16 v[96:99], v[32:35], v[164:167], 0\n\t"
    "s_cmp_eq_u32 s60, s67\n\t"
    "s_cbranch_scc1 .Lfs0_%=\n\t"
    ".Lfb0_%=:\n\t"
    "v_mfma_f32_16x16x32_bf16 v[96:99], v[36:39], v[168:171], v[96:99]\n\t"
    "v_mfma_f32_16x16x32_bf16 v[96:99], v[40:43], v[172:175], v[96:99]\n\t"
    "v_add_u32_e32 v12, 0x3000, v12\n\t"
    "v_add_u32_e32 v13, 0x3000, v13\n\t"
    "v_mfma_f32_16x16x32_bf16 v[96:99], v[44:47], v[176:179], v[96:99]\n\t"
    "v_cvt_pk_bf16_f32 v108, v88, v89\n\t"
    "v_cvt_pk_bf16_f32 v109, v90, v91\n\t"
    "v_mfma_f32_16x16x32_bf16 v[96:99], v[48:51], v[180:183], v[96:99]\n\t"
    "s_cmp_eq_u32 s60, 2\n\t"
    "s_cselect_b32 s66, %[h1l], s66\n\t"
    "v_mfma_f32_16x16x32_bf16 v[96:99], v[52:55], v[184:187], v[96:99]\n\t"
    "s_waitcnt lgkmcnt(4)\n\t"
    "v_mfma_f32_16x16x32_bf16 v[84:87], v[56:59], v[116:119], 0\n\t"
    "s_cselect_b32 s61, %[h1g], s61\n\t"
    "s_mov_b32 m0, s66\n\t"
    "v_mfma_f32_16x16x32_bf16 v[84:87], v[60:63], v[120:123], v[84:87]\n\t"
    "buffer_load_dwordx4 v7, s[52:55], s61 offen offset:0 nt lds\n\t"
    "v_mfma_f32_16x16x32_bf16 v[84:87], v[64:67], v[124:127], v[84:87]\n\t"
    "buffer_load_dwordx4 v7, s[52:55], s61 offen offset:1024 nt lds\n\t"
    "s_waitcnt lgkmcnt(0)\n\t"
    "v_mfma_f32_16x16x32_bf16 v[84:87], v[68:71], v[128:131], v[84:87]\n\t"
    "s_barrier\n\t"
    "v_mfma_f32_16x16x32_bf16 v[84:87], v[72:75], v[132:135], v[84:87]\n\t"
    "ds_read_b128 v[32:35], v12 offset:0\n\t"
    "v_cvt_pk_bf16_f32 v112, v96, v97\n\t"
    "v_mfma_f32_16x16x32_bf16 v[84:87], v[76:79], v[136:139], v[84:87]\n\t"
    "ds_read_b128 v[36:39], v13 offset:0\n\t"
    "v_cvt_pk_bf16_f32 v113, v98, v99\n\t"
    "v_mfma_f32_16x16x32_bf16 v[92:95], v[56:59], v[140:143], 0\n\t"
    "ds_read_b128 v[40:43], v12 offset:4096\n\t"
    "buffer_load_dwordx4 v7, s[52:55], s61 offen offset:2048 nt lds\n\t"
    "v_mfma_f32_16x16x32_bf16 v[92:95], v[60:63], v[144:147], v[92:95]\n\t"
    "ds_read_b128 v[44:47], v13 offset:4096\n\t"
    "s_add_u32 s61, s61, 196608\n\t"
    "v_mfma_f32_16x16x32_bf16 v[92:95], v[64:67], v[148:151], v[92:95]\n\t"
    "ds_read_b128 v[48:51], v12 offset:8192\n\t"
    "s_add_u32 m0, s66, 3072\n\t"
    "v_mfma_f32_16x16x32_bf16 v[92:95], v[68:71], v[152:155], v[92:95]\n\t"
    "ds_read_b128 v[52:55], v13 offset:8192\n\t"
    "v_cvt_pk_bf16_f32 v106, v84, v85\n\t"
    "v_mfma_f32_16x16x32_bf16 v[92:95], v[72:75], v[156:159], v[92:95]\n\t"
    "v_cvt_pk_bf16_f32 v107, v86, v87\n\t"
    "buffer_load_dwordx4 v7, s[52:55], s61 offen offset:0 nt lds\n\t"
    "v_mfma_f32_16x16x32_bf16 v[92:95], v[76:79], v[160:163], v[92:95]\n\t"
    "ds_write_b128 v11, v[104:107] offset:0\n\t"
    "v_mfma_f32_16x16x32_bf16 v[100:103], v[56:59], v[164:167], 0\n\t"
    "buffer_load_dwordx4 v7, s[52:55], s61 offen offset:1024 nt lds\n\t"
    "v_mfma_f32_16x16x32_bf16 v[100:103], v[60:63], v[168:171], v[100:103]\n\t"
    "buffer_load_dwordx4 v7, s[52:55], s61 offen offset:2048 nt lds\n\t"
    "s_add_u32 s61, s61, 196608\n\t"
    "v_mfma_f32_16x16x32_bf16 v[100:103], v[64:67], v[172:175], v[100:103]\n\t"
    "s_add_u32 s66, s66, 6144\n\t"
    "v_mfma_f32_16x16x32_bf16 v[100:103], v[68:71], v[176:179], v[100:103]\n\t"
    "v_cvt_pk_bf16_f32 v110, v92, v93\n\t"
    "v_cvt_pk_bf16_f32 v111, v94, v95\n\t"
    "v_mfma_f32_16x16x32_bf16 v[100:103], v[72:75], v[180:183], v[100:103]\n\t"
    "s_sub_u32 s60, s60, 1\n\t"
    "s_cmp_lg_u32 s60, 0\n\t"
    "v_mfma_f32_16x16x32_bf16 v[100:103], v[76:79], v[184:187], v[100:103]\n\t"
    "s_cbranch_scc1 .Lf2_%=\n\t"
    "ds_write_b128 v11, v[108:111] offset:1024\n\t"
    "s_nop 6\n\t"
    "v_cvt_pk_bf16_f32 v114, v100, v101\n\t"
    "v_cvt_pk_bf16_f32 v115, v102, v103\n\t"
    "ds_write_b128 v11, v[112:115] offset:2048\n\t"
    "v_add_u32_e32 v9, 0x9000, v15\n\t"
    "v_mov_b32_e32 v8, v15\n\t"
    "s_waitcnt lgkmcnt(0)\n\t"
    "s_barrier\n\t"
    "s_waitcnt vmcnt(30)\n\t"
    "ds_read_b128 v[220:223], v0 offset:0\n\t"
    "ds_read_b128 a[0:3], v8 offset:0\n\t"
    "ds_read_b128 v[216:219], v1 offset:0\n\t"
    "ds_read_b128 a[8:11], v8 offset:12288\n\t"
    "ds_read_b128 v[212:215], v2 offset:0\n\t"
    "ds_read_b128 a[16:19], v8 offset:24576\n\t"
    "ds_read_b128 v[36:39], v3 offset:0\n\t"
    "ds_read_b128 a[24:27], v9 offset:0\n\t"
    "ds_read_b128 v[40:43], v4 offset:0\n\t"
    "ds_read_b128 a[32:35], v9 offset:12288\n\t"
    "ds_read_b128 v[44:47], v5 offset:0\n\t"
    "ds_read_b128 a[40:43], v9 offset:24576\n\t"
    "ds_read_b128 a[4:7], v8 offset:1024\n\t"
    "ds_read_b128 a[12:15], v8 offset:13312\n\t"
    "ds_read_b128 a[20:23], v8 offset:25600\n\t"
    "ds_read_b128 a[28:31], v9 offset:1024\n\t"
    "ds_read_b128 a[36:39], v9 offset:13312\n\t"
    "s_waitcnt lgkmcnt(15)\n\t"
    "v_mfma_f32_16x16x32_bf16 v[72:75], a[0:3], v[220:223], 0\n\t"
    "ds_read_b128 a[44:47], v9 offset:25600\n\t"
    "s_waitcnt lgkmcnt(14)\n\t"
    "v_mfma_f32_16x16x32_bf16 v[72:75], a[8:11], v[216:219], v[72:75]\n\t"
    "ds_read_b128 a[48:51], v8 offset:2048\n\t"
    "s_waitcnt lgkmcnt(13)\n\t"
    "v_mfma_f32_16x16x32_bf16 v[72:75], a[16:19], v[212:215], v[72:75]\n\t"
    "ds_read_b128 a[56:59], v8 offset:14336\n\t"
    "s_waitcnt lgkmcnt(3)\n\t"
    "v_mfma_f32_16x16x32_bf16 v[72:75], a[24:27], v[36:39], v[72:75]\n\t"
    "ds_read_b128 a[64:67], v8 offset:26624\n\t"
    "v_mfma_f32_16x16x32_bf16 v[72:75], a[32:35], v[40:43], v[72:75]\n\t"
    "ds_read_b128 a[72:75], v9 offset:2048\n\t"
    "v_mfma_f32_16x16x32_bf16 v[72:75], a[40:43], v[44:47], v[72:75]\n\t"
    "ds_read_b128 a[80:83], v9 offset:14336\n\t"
    "v_mfma_f32_16x16x32_bf16 v[48:51], a[4:7], v[220:223], 0\n\t"
    "ds_read_b128 a[88:91], v9 offset:26624\n\t"
    "v_mfma_f32_16x16x32_bf16 v[48:51], a[12:15], v[216:219], v[48:51]\n\t"
    "ds_read_b128 a[52:55], v8 offset:3072\n\t"
    "v_mfma_f32_16x16x32_bf16 v[48:51], a[20:23], v[212:215], v[48:51]\n\t"
    "ds_read_b128 a[60:63], v8 offset:15360\n\t"
    "v_mfma_f32_16x16x32_bf16 v[48:51], a[28:31], v[36:39], v[48:51]\n\t"
    "ds_read_b128 a[68:71], v8 offset:27648\n\t"
    "v_mfma_f32_16x16x32_bf16 v[48:51], a[36:39], v[40:43], v[48:51]\n\t"
    "v_cvt_pk_bf16_f32 v12, v72, v73\n\t"
    "v_cvt_pk_bf16_f32 v13, v74, v75\n\t"
    "ds_read_b128 a[76:79], v9 offset:3072\n\t"
    "s_waitcnt lgkmcnt(3)\n\t"
    "v_mfma_f32_16x16x32_bf16 v[48:51], a[44:47], v[44:47], v[48:51]\n\t"
    "ds_read_b128 a[84:87], v9 offset:15360\n\t"
    "v_mfma_f32_16x16x32_bf16 v[52:55], a[48:51], v[220:223], 0\n\t"
    "ds_read_b128 a[92:95], v9 offset:27648\n\t"
    "v_mfma_f32_16x16x32_bf16 v[52:55], a[56:59], v[216:219], v[52:55]\n\t"
    "ds_read_b128 a[96:99], v8 offset:4096\n\t"
    "v_mfma_f32_16x16x32_bf16 v[52:55], a[64:67], v[212:215], v[52:55]\n\t"
    "ds_read_b128 a[104:107], v8 offset:16384\n\t"
    "v_mfma_f32_16x16x32_bf16 v[52:55], a[72:75], v[36:39], v[52:55]\n\t"
    "v_cvt_pk_bf16_f32 v14, v48, v49\n\t"
    "ds_read_b128 a[112:115], v8 offset:28672\n\t"
    "v_mfma_f32_16x16x32_bf16 v[52:55], a[80:83], v[40:43], v[52:55]\n\t"
    "v_cvt_pk_bf16_f32 v15, v50, v51\n\t"
    "ds_read_b128 a[120:123], v9 offset:4096\n\t"
    "v_mfma_f32_16x16x32_bf16 v[52:55], a[88:91], v[44:47], v[52:55]\n\t"
    "ds_read_b128 a[128:131], v9 offset:16384\n\t"
    "v_mfma_f32_16x16x32_bf16 v[56:59], a[52:55], v[220:223], 0\n\t"
    "ds_read_b128 a[136:139], v9 offset:28672\n\t"
    "s_waitcnt lgkmcnt(3)\n\t"
    "v_mfma_f32_16x16x32_bf16 v[56:59], a[60:63], v[216:219], v[56:59]\n\t"
    "ds_read_b128 a[100:103], v8 offset:5120\n\t"
    "v_mfma_f32_16x16x32_bf16 v[56:59], a[68:71], v[212:215], v[56:59]\n\t"
    "ds_read_b128 a[108:111], v8 offset:17408\n\t"
    "v_mfma_f32_16x16x32_bf16 v[56:59], a[76:79], v[36:39], v[56:59]\n\t"
    "v_cvt_pk_bf16_f32 v48, v52, v53\n\t"
    "ds_read_b128 a[116:119], v8 offset:29696\n\t"
    "v_mfma_f32_16x16x32_bf16 v[56:59], a[84:87], v[40:43], v[56:59]\n\t"
    "v_cvt_pk_bf16_f32 v49, v54, v55\n\t"
    "ds_read_b128 a[124:127], v9 offset:5120\n\t"
    "v_mfma_f32_16x16x32_bf16 v[56:59], a[92:95], v[44:47], v[56:59]\n\t"
    "ds_read_b128 a[132:135], v9 offset:17408\n\t"
    "v_mfma_f32_16x16x32_bf16 v[60:63], a[96:99], v[220:223], 0\n\t"
    "ds_read_b128 a[140:143], v9 offset:29696\n\t"
    "v_mfma_f32_16x16x32_bf16 v[60:63], a[104:107], v[216:219], v[60:63]\n\t"
    "ds_read_b128 a[144:147], v8 offset:6144\n\t"
    "v_mfma_f32_16x16x32_bf16 v[60:63], a[112:115], v[212:215], v[60:63]\n\t"
    "ds_read_b128 a[152:155], v8 offset:18432\n\t"
    "s_waitcnt lgkmcnt(3)\n\t"
    "v_mfma_f32_16x16x32_bf16 v[60:63], a[120:123], v[36:39], v[60:63]\n\t"
    "v_cvt_pk_bf16_f32 v50, v56, v57\n\t"
    "v_cvt_pk_bf16_f32 v51, v58, v59\n\t"
    "ds_read_b128 a[160:163], v8 offset:30720\n\t"
    "v_mfma_f32_16x16x32_bf16 v[60:63], a[128:131], v[40:43], v[60:63]\n\t"
    "ds_read_b128 a[168:171], v9 offset:6144\n\t"
    "v_mfma_f32_16x16x32_bf16 v[60:63], a[136:139], v[44:47], v[60:63]\n\t"
    "ds_read_b128 a[176:179], v9 offset:18432\n\t"
    "v_mfma_f32_16x16x32_bf16 v[52:55], a[100:103], v[220:223], 0\n\t"
    "ds_read_b128 a[184:187], v9 offset:30720\n\t"
    "v_mfma_f32_16x16x32_bf16 v[52:55], a[108:111], v[216:219], v[52:55]\n\t"
    "v_mfma_f32_16x16x32_bf16 v[52:55], a[116:119], v[212:215], v[52:55]\n\t"
    "v_mfma_f32_16x16x32_bf16 v[52:55], a[124:127], v[36:39], v[52:55]\n\t"
    "v_cvt_pk_bf16_f32 v56, v60, v61\n\t"
    "v_cvt_pk_bf16_f32 v57, v62, v63\n\t"
    "v_mfma_f32_16x16x32_bf16 v[52:55], a[132:135], v[40:43], v[52:55]\n\t"
    "s_waitcnt vmcnt(12)\n\t"
    "ds_read_b128 v[196:199], v0 offset:6144\n\t"
    "s_waitcnt lgkmcnt(1)\n\t"
    "v_mfma_f32_16x16x32_bf16 v[52:55], a[140:143], v[44:47], v[52:55]\n\t"
    "ds_read_b128 v[192:195], v1 offset:6144\n\t"
    "v_mfma_f32_16x16x32_bf16 v[60:63], a[144:147], v[220:223], 0\n\t"
    "ds_read_b128 v[188:191], v2 offset:6144\n\t"
    "v_mfma_f32_16x16x32_bf16 v[60:63], a[152:155], v[216:219], v[60:63]\n\t"
    "ds_read_b128 v[172:175], v0 offset:12288\n\t"
    "v_mfma_f32_16x16x32_bf16 v[60:63], a[160:163], v[212:215], v[60:63]\n\t"
    "ds_read_b128 v[168:171], v1 offset:12288\n\t"
    "v_mfma_f32_16x16x32_bf16 v[60:63], a[168:171], v[36:39], v[60:63]\n\t"
    "v_cvt_pk_bf16_f32 v58, v52, v53\n\t"
    "v_cvt_pk_bf16_f32 v59, v54, v55\n\t"
    "ds_read_b128 v[164:167], v2 offset:12288\n\t"
    "v_mfma_f32_16x16x32_bf16 v[60:63], a[176:179], v[40:43], v[60:63]\n\t"
    "ds_read_b128 v[148:151], v0 offset:18432\n\t"
    "v_mfma_f32_16x16x32_bf16 v[60:63], a[184:187], v[44:47], v[60:63]\n\t"
    "ds_read_b128 v[144:147], v1 offset:18432\n\t"
    "v_mfma_f32_16x16x32_bf16 v[52:55], v[220:223], v[12:15], 0\n\t"
    "ds_read_b128 v[140:143], v2 offset:18432\n\t"
    "v_mfma_f32_16x16x32_bf16 v[52:55], v[216:219], v[48:51], v[52:55]\n\t"
    "ds_read_b128 a[148:151], v8 offset:7168\n\t"
    "v_mfma_f32_16x16x32_bf16 v[52:55], v[212:215], v[56:59], v[52:55]\n\t"
    "ds_read_b128 a[156:159], v8 offset:19456\n\t"
    "s_waitcnt lgkmcnt(3)\n\t"
    "v_mfma_f32_16x16x32_bf16 v[64:67], v[196:199], v[12:15], 0\n\t"
    "ds_read_b128 a[164:167], v8 offset:31744\n\t"
    "v_mfma_f32_16x16x32_bf16 v[64:67], v[192:195], v[48:51], v[64:67]\n\t"
    "ds_read_b128 a[172:175], v9 offset:7168\n\t"
    "v_mfma_f32_16x16x32_bf16 v[64:67], v[188:191], v[56:59], v[64:67]\n\t"
    "ds_read_b128 a[180:183], v9 offset:19456\n\t"
    "v_mfma_f32_16x16x32_bf16 v[68:71], v[172:175], v[12:15], 0\n\t"
    "ds_read_b128 a[188:191], v9 offset:31744\n\t"
    "v_mfma_f32_16x16x32_bf16 v[68:71], v[168:171], v[48:51], v[68:71]\n\t"
    "ds_read_b128 a[192:195], v8 offset:8192\n\t"
    "v_mfma_f32_16x16x32_bf16 v[68:71], v[164:167], v[56:59], v[68:71]\n\t"
    "ds_read_b128 a[200:203], v8 offset:20480\n\t"
    "v_mfma_f32_16x16x32_bf16 v[72:75], v[148:151], v[12:15], 0\n\t"
    "ds_read_b128 a[208:211], v8 offset:32768\n\t"
    "v_mfma_f32_16x16x32_bf16 v[72:75], v[144:147], v[48:51], v[72:75]\n\t"
    "ds_read_b128 a[216:219], v9 offset:8192\n\t"
    "s_waitcnt lgkmcnt(3)\n\t"
    "v_mfma_f32_16x16x32_bf16 v[72:75], v[140:143], v[56:59], v[72:75]\n\t"
    "ds_read_b128 a[224:227], v9 offset:20480\n\t"
    "v_mfma_f32_16x16x32_bf16 v[12:15], a[148:151], v[220:223], 0\n\t"
    "ds_read_b128 a[232:235], v9 offset:32768\n\t"
    "v_mfma_f32_16x16x32_bf16 v[12:15], a[156:159], v[216:219], v[12:15]\n\t"
    "ds_read_b128 a[196:199], v8 offset:9216\n\t"
    "v_mfma_f32_16x16x32_bf16 v[12:15], a[164:167], v[212:215], v[12:15]\n\t"
    "ds_read_b128 a[204:207], v8 offset:21504\n\t"
    "v_mfma_f32_16x16x32_bf16 v[12:15], a[172:175], v[36:39], v[12:15]\n\t"
    "ds_read_b128 a[212:215], v8 offset:33792\n\t"
    "v_mfma_f32_16x16x32_bf16 v[12:15], a[180:183], v[40:43], v[12:15]\n\t"
    "ds_read_b128 a[220:223], v9 offset:9216\n\t"
    "v_mfma_f32_16x16x32_bf16 v[12:15], a[188:191], v[44:47], v[12:15]\n\t"
    "v_cvt_pk_bf16_f32 v48, v60, v61\n\t"
    "ds_read_b128 a[228:231], v9 offset:21504\n\t"
    "v_mfma_f32_16x16x32_bf16 v[56:59], a[192:195], v[220:223], 0\n\t"
    "ds_read_b128 a[236:239], v9 offset:33792\n\t"
    "s_waitcnt lgkmcnt(3)\n\t"
    "v_mfma_f32_16x16x32_bf16 v[56:59], a[200:203], v[216:219], v[56:59]\n\t"
    "ds_read_b128 a[240:243], v8 offset:10240\n\t"
    "v_mfma_f32_16x16x32_bf16 v[56:59], a[208:211], v[212:215], v[56:59]\n\t"
    "v_cvt_pk_bf16_f32 v49, v62, v63\n\t"
    "ds_read_b128 a[248:251], v8 offset:22528\n\t"
    "v_mfma_f32_16x16x32_bf16 v[56:59], a[216:219], v[36:39], v[56:59]\n\t"
    "v_cvt_pk_bf16_f32 v50, v12, v13\n\t"
    "ds_read_b128 v[224:227], v8 offset:34816\n\t"
    "v_mfma_f32_16x16x32_bf16 v[56:59], a[224:227], v[40:43], v[56:59]\n\t"
    "v_cvt_pk_bf16_f32 v51, v14, v15\n\t"
    "ds_read_b128 v[232:235], v9 offset:10240\n\t"
    "v_mfma_f32_16x16x32_bf16 v[56:59], a[232:235], v[44:47], v[56:59]\n\t"
    "ds_read_b128 v[240:243], v9 offset:22528\n\t"
    "v_mfma_f32_16x16x32_bf16 v[60:63], a[196:199], v[220:223], 0\n\t"
    "ds_read_b128 v[248:251], v9 offset:34816\n\t"
    "v_mfma_f32_16x16x32_bf16 v[60:63], a[204:207], v[216:219], v[60:63]\n\t"
    "ds_read_b128 a[244:247], v8 offset:11264\n\t"
    "v_mfma_f32_16x16x32_bf16 v[60:63], a[212:215], v[212:215], v[60:63]\n\t"
    "ds_read_b128 a[252:255], v8 offset:23552\n\t"
    "s_waitcnt lgkmcnt(3)\n\t"
    "v_mfma_f32_16x16x32_bf16 v[60:63], a[220:223], v[36:39], v[60:63]\n\t"
    "v_cvt_pk_bf16_f32 v76, v56, v57\n\t"
    "ds_read_b128 v[228:231], v8 offset:35840\n\t"
    "v_mfma_f32_16x16x32_bf16 v[60:63], a[228:231], v[40:43], v[60:63]\n\t"
    "v_cvt_pk_bf16_f32 v77, v58, v59\n\t"
    "ds_read_b128 v[236:239], v9 offset:11264\n\t"
    "v_mfma_f32_16x16x32_bf16 v[60:63], a[236:239], v[44:47], v[60:63]\n\t"
    "ds_read_b128 v[244:247], v9 offset:23552\n\t"
    "v_mfma_f32_16x16x32_bf16 v[80:83], a[240:243], v[220:223], 0\n\t"
    "ds_read_b128 v[252:255], v9 offset:35840\n\t"
    "v_mfma_f32_16x16x32_bf16 v[80:83], a[248:251], v[216:219], v[80:83]\n\t"
    "v_mfma_f32_16x16x32_bf16 v[80:83], v[224:227], v[212:215], v[80:83]\n\t"
    "v_mfma_f32_16x16x32_bf16 v[80:83], v[232:235], v[36:39], v[80:83]\n\t"
    "v_cvt_pk_bf16_f32 v78, v60, v61\n\t"
    "v_cvt_pk_bf16_f32 v79, v62, v63\n\t"
    "v_mfma_f32_16x16x32_bf16 v[80:83], v[240:243], v[40:43], v[80:83]\n\t"
    "ds_read_b128 v[184:187], v3 offset:6144\n\t"
    "s_waitcnt lgkmcnt(1)\n\t"
    "v_mfma_f32_16x16x32_bf16 v[80:83], v[248:251], v[44:47], v[80:83]\n\t"
    "ds_read_b128 v[180:183], v4 offset:6144\n\t"
    "v_mfma_f32_16x16x32_bf16 v[24:27], a[244:247], v[220:223], 0\n\t"
    "ds_read_b128 v[176:179], v5 offset:6144\n\t"
    "v_mfma_f32_16x16x32_bf16 v[24:27], a[252:255], v[216:219], v[24:27]\n\t"
    "ds_read_b128 v[208:211], v3 offset:0\n\t"
    "v_mfma_f32_16x16x32_bf16 v[24:27], v[228:231], v[212:215], v[24:27]\n\t"
    "ds_read_b128 v[204:207], v4 offset:0\n\t"
    "v_mfma_f32_16x16x32_bf16 v[24:27], v[236:239], v[36:39], v[24:27]\n\t"
    "v_cvt_pk_bf16_f32 v28, v80, v81\n\t"
    "v_cvt_pk_bf16_f32 v29, v82, v83\n\t"
    "ds_read_b128 v[200:203], v5 offset:0\n\t"
    "v_mfma_f32_16x16x32_bf16 v[24:27], v[244:247], v[40:43], v[24:27]\n\t"
    "v_mfma_f32_16x16x32_bf16 v[24:27], v[252:255], v[44:47], v[24:27]\n\t"
    "v_mfma_f32_16x16x32_bf16 v[36:39], a[0:3], v[196:199], 0\n\t"
    "ds_read_b128 v[160:163], v3 offset:12288\n\t"
    "ds_read_b128 v[156:159], v4 offset:12288\n\t"
    "v_mfma_f32_16x16x32_bf16 v[36:39], a[8:11], v[192:195], v[36:39]\n\t"
    "ds_read_b128 v[152:155], v5 offset:12288\n\t"
    "ds_read_b128 v[136:139], v3 offset:18432\n\t"
    "v_mfma_f32_16x16x32_bf16 v[36:39], a[16:19], v[188:191], v[36:39]\n\t"
    "ds_read_b128 v[132:135], v4 offset:18432\n\t"
    "ds_read_b128 v[128:131], v5 offset:18432\n\t"
    "s_waitcnt lgkmcnt(6)\n\t"
    "v_mfma_f32_16x16x32_bf16 v[36:39], a[24:27], v[184:187], v[36:39]\n\t"
    "v_cvt_pk_bf16_f32 v30, v24, v25\n\t"
    "v_cvt_pk_bf16_f32 v31, v26, v27\n\t"
    "v_mfma_f32_16x16x32_bf16 v[36:39], a[32:35], v[180:183], v[36:39]\n\t"
    "v_mfma_f32_16x16x32_bf16 v[36:39], a[40:43], v[176:179], v[36:39]\n\t"
    "v_mfma_f32_16x16x32_bf16 v[52:55], v[208:211], v[48:51], v[52:55]\n\t"
    "v_mfma_f32_16x16x32_bf16 v[52:55], v[204:207], v[76:79], v[52:55]\n\t"
    "v_mfma_f32_16x16x32_bf16 v[52:55], v[200:203], v[28:31], v[52:55]\n\t"
    "v_mfma_f32_16x16x32_bf16 v[64:67], v[184:187], v[48:51], v[64:67]\n\t"
    "v_cvt_pk_bf16_f32 v12, v36, v37\n\t"
    "v_cvt_pk_bf16_f32 v13, v38, v39\n\t"
    "v_mfma_f32_16x16x32_bf16 v[64:67], v[180:183], v[76:79], v[64:67]\n\t"
    "s_add_i32 s63, %[rbeg], %[rd]\n\t"
    "s_add_i32 s62, s63, %[rd]\n\t"
    "s_mov_b32 s60, 8\n\t"
    "s_barrier\n\t"
    "s_mov_b32 s66, %[h2l]\n\t"
    "s_mov_b32 s67, %[h2g]\n\t"
    "s_add_u32 m0, s66, 0\n\t"
    "s_add_u32 s61, s67, 0\n\t"
    "buffer_load_dwordx4 v7, s[52:55], s61 offen offset:0 nt lds\n\t"
    "buffer_load_dwordx4 v7, s[52:55], s61 offen offset:1024 nt lds\n\t"
    "buffer_load_dwordx4 v7, s[52:55], s61 offen offset:2048 nt lds\n\t"
    "s_add_u32 m0, s66, 3072\n\t"
    "s_add_u32 s61, s67, 196608\n\t"
    "buffer_load_dwordx4 v7, s[52:55], s61 offen offset:0 nt lds\n\t"
    "buffer_load_dwordx4 v7, s[52:55], s61 offen offset:1024 nt lds\n\t"
    "buffer_load_dwordx4 v7, s[52:55], s61 offen offset:2048 nt lds\n\t"
    "s_add_u32 m0, s66, 6144\n\t"
    "s_add_u32 s61, s67, 393216\n\t"
    "buffer_load_dwordx4 v7, s[52:55], s61 offen offset:0 nt lds\n\t"
    "buffer_load_dwordx4 v7, s[52:55], s61 offen offset:1024 nt lds\n\t"
    "buffer_load_dwordx4 v7, s[52:55], s61 offen offset:2048 nt lds\n\t"
    "s_add_u32 m0, s66, 9216\n\t"
    "s_add_u32 s61, s67, 589824\n\t"
    "buffer_load_dwordx4 v7, s[52:55], s61 offen offset:0 nt lds\n\t"
    "buffer_load_dwordx4 v7, s[52:55], s61 offen offset:1024 nt lds\n\t"
    "buffer_load_dwordx4 v7, s[52:55], s61 offen offset:2048 nt lds\n\t"
    "s_add_u32 s61, %[wb], 25165824\n\t"
    "s_branch .Lmid_%=\n\t"
    ".Lf3_%=:\n\t"
    "s_waitcnt lgkmcnt(0)\n\t"
    "v_mfma_f32_16x16x32_bf16 v[72:75], a[0:3], v[220:223], 0\n\t"
    "ds_write_b128 v6, v[56:59] offset:4096\n\t"
    "v_mfma_f32_16x16x32_bf16 v[72:75], a[8:11], v[216:219], v[72:75]\n\t"
    "v_cvt_pk_bf16_f32 v62, v12, v13\n\t"
    "v_mul_f32_e32 v14, v14, v18\n\t"
    "v_mfma_f32_16x16x32_bf16 v[72:75], a[16:19], v[212:215], v[72:75]\n\t"
    "v_mul_f32_e32 v15, v15, v18\n\t"
    "v_cvt_pk_bf16_f32 v63, v14, v15\n\t"
    "v_mfma_f32_16x16x32_bf16 v[72:75], a[24:27], v[36:39], v[72:75]\n\t"
    "v_cvt_pk_bf16_f32 v70, v64, v65\n\t"
    "v_cvt_pk_bf16_f32 v71, v66, v67\n\t"
    "v_mfma_f32_16x16x32_bf16 v[72:75], a[32:35], v[40:43], v[72:75]\n\t"
    "ds_write_b128 v6, v[60:63] offset:5120\n\t"
    "v_mfma_f32_16x16x32_bf16 v[72:75], a[40:43], v[44:47], v[72:75]\n\t"
    "v_mfma_f32_16x16x32_bf16 v[20:23], v[208:211], v[48:51], v[20:23]\n\t"
    "v_mfma_f32_16x16x32_bf16 v[20:23], v[204:207], v[52:55], v[20:23]\n\t"
    "v_mfma_f32_16x16x32_bf16 v[20:23], v[200:203], v[68:71], v[20:23]\n\t"
    "v_mfma_f32_16x16x32_bf16 v[24:27], v[184:187], v[48:51], v[24:27]\n\t"
    "v_cvt_pk_bf16_f32 v12, v72, v73\n\t"
    "v_cvt_pk_bf16_f32 v13, v74, v75\n\t"
    "v_mfma_f32_16x16x32_bf16 v[24:27], v[180:183], v[52:55], v[24:27]\n\t"
    "s_waitcnt lgkmcnt(0)\n\t"
    "s_barrier\n\t"
    "v_mfma_f32_16x16x32_bf16 v[24:27], v[176:179], v[68:71], v[24:27]\n\t"
    "v_mfma_f32_16x16x32_bf16 v[28:31], v[160:163], v[48:51], v[28:31]\n\t"
    "v_max3_f32 v16, v20, v21, v22\n\t"
    "v_mfma_f32_16x16x32_bf16 v[28:31], v[156:159], v[52:55], v[28:31]\n\t"
    "v_mfma_f32_16x16x32_bf16 v[28:31], v[152:155], v[68:71], v[28:31]\n\t"
    "v_mfma_f32_16x16x32_bf16 v[32:35], v[136:139], v[48:51], v[32:35]\n\t"
    "v_max3_f32 v17, v23, v24, v25\n\t"
    "v_max3_f32 v16, v16, v26, v27\n\t"
    "v_mfma_f32_16x16x32_bf16 v[32:35], v[132:135], v[52:55], v[32:35]\n\t"
    "v_mfma_f32_16x16x32_bf16 v[32:35], v[128:131], v[68:71], v[32:35]\n\t"
    "ds_read_b128 v[196:199], v0 offset:6144\n\t"
    "v_mfma_f32_16x16x32_bf16 v[48:51], a[4:7], v[220:223], 0\n\t"
    "v_max3_f32 v17, v17, v28, v29\n\t"
    "v_max3_f32 v16, v16, v30, v31\n\t"
    "v_mfma_f32_16x16x32_bf16 v[48:51], a[12:15], v[216:219], v[48:51]\n\t"
    "ds_read_b128 v[192:195], v1 offset:6144\n\t"
    "v_mfma_f32_16x16x32_bf16 v[48:51], a[20:23], v[212:215], v[48:51]\n\t"
    "v_mfma_f32_16x16x32_bf16 v[48:51], a[28:31], v[36:39], v[48:51]\n\t"
    "ds_read_b128 v[188:191], v2 offset:6144\n\t"
    "v_max3_f32 v17, v17, v32, v33\n\t"
    "v_mfma_f32_16x16x32_bf16 v[48:51], a[36:39], v[40:43], v[48:51]\n\t"
    "v_max3_f32 v16, v16, v34, v35\n\t"
    "v_max_f32_e32 v16, v16, v17\n\t"
    "v_mfma_f32_16x16x32_bf16 v[48:51], a[44:47], v[44:47], v[48:51]\n\t"
    "ds_read_b128 v[172:175], v0 offset:12288\n\t"
    "v_mov_b32_e32 v17, v16\n\t"
    "v_mfma_f32_16x16x32_bf16 v[52:55], a[48:51], v[220:223], 0\n\t"
    "v_mfma_f32_16x16x32_bf16 v[52:55], a[56:59], v[216:219], v[52:55]\n\t"
    "ds_read_b128 v[168:171], v1 offset:12288\n\t"
    "v_permlane32_swap_b32 v16, v17\n\t"
    "v_mfma_f32_16x16x32_bf16 v[52:55], a[64:67], v[212:215], v[52:55]\n\t"
    "v_max_f32_e32 v16, v16, v17\n\t"
    "v_mov_b32_e32 v17, v16\n\t"
    "v_mfma_f32_16x16x32_bf16 v[52:55], a[72:75], v[36:39], v[52:55]\n\t"
    "ds_read_b128 v[164:167], v2 offset:12288\n\t"
    "v_cvt_pk_bf16_f32 v14, v48, v49\n\t"
    "v_mfma_f32_16x16x32_bf16 v[52:55], a[80:83], v[40:43], v[52:55]\n\t"
    "v_cvt_pk_bf16_f32 v15, v50, v51\n\t"
    "v_permlane16_swap_b32 v16, v17\n\t"
    "v_mfma_f32_16x16x32_bf16 v[52:55], a[88:91], v[44:47], v[52:55]\n\t"
    "ds_read_b128 v[148:151], v0 offset:18432\n\t"
    "v_max_f32_e32 v16, v16, v17\n\t"
    "v_mfma_f32_16x16x32_bf16 v[56:59], a[52:55], v[220:223], 0\n\t"
    "v_mul_f32_e32 v16, s68, v16\n\t"
    "v_fma_f32 v20, v20, s68, -v16\n\t"
    "v_mfma_f32_16x16x32_bf16 v[56:59], a[60:63], v[216:219], v[56:59]\n\t"
    "ds_read_b128 v[144:147], v1 offset:18432\n\t"
    "v_fma_f32 v21, v21, s68, -v16\n\t"
    "v_mfma_f32_16x16x32_bf16 v[56:59], a[68:71], v[212:215], v[56:59]\n\t"
    "v_exp_f32_e32 v20, v20\n\t"
    "v_mfma_f32_16x16x32_bf16 v[56:59], a[76:79], v[36:39], v[56:59]\n\t"
    "ds_read_b128 v[140:143], v2 offset:18432\n\t"
    "v_cvt_pk_bf16_f32 v48, v52, v53\n\t"
    "v_mfma_f32_16x16x32_bf16 v[56:59], a[84:87], v[40:43], v[56:59]\n\t"
    "v_cvt_pk_bf16_f32 v49, v54, v55\n\t"
    "v_fma_f32 v22, v22, s68, -v16\n\t"
    "v_mfma_f32_16x16x32_bf16 v[56:59], a[92:95], v[44:47], v[56:59]\n\t"
    "v_exp_f32_e32 v21, v21\n\t"
    "v_mfma_f32_16x16x32_bf16 v[60:63], a[96:99], v[220:223], 0\n\t"
    "v_add_f32_e32 v18, v20, v21\n\t"
    "v_fma_f32 v23, v23, s68, -v16\n\t"
    "v_mfma_f32_16x16x32_bf16 v[60:63], a[104:107], v[216:219], v[60:63]\n\t"
    "v_exp_f32_e32 v22, v22\n\t"
    "v_mfma_f32_16x16x32_bf16 v[60:63], a[112:115], v[212:215], v[60:63]\n\t"
    "v_exp_f32_e32 v23, v23\n\t"
    "v_mfma_f32_16x16x32_bf16 v[60:63], a[120:123], v[36:39], v[60:63]\n\t"
    "v_cvt_pk_bf16_f32 v50, v56, v57\n\t"
    "v_cvt_pk_bf16_f32 v51, v58, v59\n\t"
    "v_mfma_f32_16x16x32_bf16 v[60:63], a[128:131], v[40:43], v[60:63]\n\t"
    "v_add_f32_e32 v19, v22, v23\n\t"
    "v_fma_f32 v24, v24, s68, -v16\n\t"
    "v_mfma_f32_16x16x32_bf16 v[60:63], a[136:139], v[44:47], v[60:63]\n\t"
    "v_exp_f32_e32 v24, v24\n\t"
    "v_mfma_f32_16x16x32_bf16 v[52:55], a[100:103], v[220:223], 0\n\t"
    "v_add_f32_e32 v18, v18, v24\n\t"
    "v_fma_f32 v25, v25, s68, -v16\n\t"
    "v_mfma_f32_16x16x32_bf16 v[52:55], a[108:111], v[216:219], v[52:55]\n\t"
    "v_exp_f32_e32 v25, v25\n\t"
    "v_mfma_f32_16x16x32_bf16 v[52:55], a[116:119], v[212:215], v[52:55]\n\t"
    "v_add_f32_e32 v18, v18, v25\n\t"
    "v_fma_f32 v26, v26, s68, -v16\n\t"
    "v_mfma_f32_16x16x32_bf16 v[52:55], a[124:127], v[36:39], v[52:55]\n\t"
    "v_cvt_pk_bf16_f32 v56, v60, v61\n\t"
    "v_cvt_pk_bf16_f32 v57, v62, v63\n\t"
    "v_mfma_f32_16x16x32_bf16 v[52:55], a[132:135], v[40:43], v[52:55]\n\t"
    "v_exp_f32_e32 v26, v26\n\t"
    "v_mfma_f32_16x16x32_bf16 v[52:55], a[140:143], v[44:47], v[52:55]\n\t"
    "v_add_f32_e32 v19, v19, v26\n\t"
    "v_fma_f32 v27, v27, s68, -v16\n\t"
    "v_mfma_f32_16x16x32_bf16 v[60:63], a[144:147], v[220:223], 0\n\t"
    "v_exp_f32_e32 v27, v27\n\t"
    "v_mfma_f32_16x16x32_bf16 v[60:63], a[152:155], v[216:219], v[60:63]\n\t"
    "v_add_f32_e32 v19, v19, v27\n\t"
    "v_fma_f32 v28, v28, s68, -v16\n\t"
    "v_mfma_f32_16x16x32_bf16 v[60:63], a[160:163], v[212:215], v[60:63]\n\t"
    "v_exp_f32_e32 v28, v28\n\t"
    "v_mfma_f32_16x16x32_bf16 v[60:63], a[168:171], v[36:39], v[60:63]\n\t"
    "v_cvt_pk_bf16_f32 v58, v52, v53\n\t"
    "v_cvt_pk_bf16_f32 v59, v54, v55\n\t"
    "v_mfma_f32_16x16x32_bf16 v[60:63], a[176:179], v[40:43], v[60:63]\n\t"
    "v_add_f32_e32 v18, v18, v28\n\t"
    "v_fma_f32 v29, v29, s68, -v16\n\t"
    "v_mfma_f32_16x16x32_bf16 v[60:63], a[184:187], v[44:47], v[60:63]\n\t"
    "v_exp_f32_e32 v29, v29\n\t"
    "v_mfma_f32_16x16x32_bf16 v[52:55], v[220:223], v[12:15], 0\n\t"
    "v_add_f32_e32 v18, v18, v29\n\t"
    "v_fma_f32 v30, v30, s68, -v16\n\t"
    "v_mfma_f32_16x16x32_bf16 v[52:55], v[216:219], v[48:51], v[52:55]\n\t"
    "v_exp_f32_e32 v30, v30\n\t"
    "v_mfma_f32_16x16x32_bf16 v[52:55], v[212:215], v[56:59], v[52:55]\n\t"
    "v_add_f32_e32 v19, v19, v30\n\t"
    "s_waitcnt lgkmcnt(0)\n\t"
    "v_mfma_f32_16x16x32_bf16 v[64:67], v[196:199], v[12:15], 0\n\t"
    "v_fma_f32 v31, v31, s68, -v16\n\t"
    "v_fma_f32 v32, v32, s68, -v16\n\t"
    "v_mfma_f32_16x16x32_bf16 v[64:67], v[192:195], v[48:51], v[64:67]\n\t"
    "v_exp_f32_e32 v31, v31\n\t"
    "v_mfma_f32_16x16x32_bf16 v[64:67], v[188:191], v[56:59], v[64:67]\n\t"
    "ds_read_b128 v[184:187], v3 offset:6144\n\t"
    "v_add_f32_e32 v19, v19, v31\n\t"
    "v_mfma_f32_16x16x32_bf16 v[68:71], v[172:175], v[12:15], 0\n\t"
    "v_exp_f32_e32 v32, v32\n\t"
    "v_mfma_f32_16x16x32_bf16 v[68:71], v[168:171], v[48:51], v[68:71]\n\t"
    "ds_read_b128 v[180:183], v4 offset:6144\n\t"
    "v_add_f32_e32 v18, v18, v32\n\t"
    "v_mfma_f32_16x16x32_bf16 v[68:71], v[164:167], v[56:59], v[68:71]\n\t"
    "v_fma_f32 v33, v33, s68, -v16\n\t"
    "v_fma_f32 v34, v34, s68, -v16\n\t"
    "v_mfma_f32_16x16x32_bf16 v[72:75], v[148:151], v[12:15], 0\n\t"
    "ds_read_b128 v[176:179], v5 offset:6144\n\t"
    "v_fma_f32 v35, v35, s68, -v16\n\t"
    "v_mfma_f32_16x16x32_bf16 v[72:75], v[144:147], v[48:51], v[72:75]\n\t"
    "v_exp_f32_e32 v33, v33\n\t"
    "v_mfma_f32_16x16x32_bf16 v[72:75], v[140:143], v[56:59], v[72:75]\n\t"
    "ds_read_b128 v[208:211], v3 offset:0\n\t"
    "v_add_f32_e32 v18, v18, v33\n\t"
    "v_mfma_f32_16x16x32_bf16 v[12:15], a[148:151], v[220:223], 0\n\t"
    "v_exp_f32_e32 v34, v34\n\t"
    "v_mfma_f32_16x16x32_bf16 v[12:15], a[156:159], v[216:219], v[12:15]\n\t"
    "ds_read_b128 v[204:207], v4 offset:0\n\t"
    "v_add_f32_e32 v19, v19, v34\n\t"
    "v_mfma_f32_16x16x32_bf16 v[12:15], a[164:167], v[212:215], v[12:15]\n\t"
    "v_exp_f32_e32 v35, v35\n\t"
    "v_mfma_f32_16x16x32_bf16 v[12:15], a[172:175], v[36:39], v[12:15]\n\t"
    "ds_read_b128 v[200:203], v5 offset:0\n\t"
    "v_add_f32_e32 v19, v19, v35\n\t"
    "v_mfma_f32_16x16x32_bf16 v[12:15], a[180:183], v[40:43], v[12:15]\n\t"
    "v_add_f32_e32 v18, v18, v19\n\t"
    "v_mov_b32_e32 v19, v18\n\t"
    "v_mfma_f32_16x16x32_bf16 v[12:15], a[188:191], v[44:47], v[12:15]\n\t"
    "ds_read_b128 v[160:163], v3 offset:12288\n\t"
    "v_cvt_pk_bf16_f32 v48, v60, v61\n\t"
    "v_mfma_f32_16x16x32_bf16 v[56:59], a[192:195], v[220:223], 0\n\t"
    "v_permlane32_swap_b32 v18, v19\n\t"
    "v_add_f32_e32 v18, v18, v19\n\t"
    "v_mfma_f32_16x16x32_bf16 v[56:59], a[200:203], v[216:219], v[56:59]\n\t"
    "ds_read_b128 v[156:159], v4 offset:12288\n\t"
    "v_mov_b32_e32 v19, v18\n\t"
    "v_mfma_f32_16x16x32_bf16 v[56:59], a[208:211], v[212:215], v[56:59]\n\t"
    "v_cvt_pk_bf16_f32 v49, v62, v63\n\t"
    "v_mfma_f32_16x16x32_bf16 v[56:59], a[216:219], v[36:39], v[56:59]\n\t"
    "ds_read_b128 v[152:155], v5 offset:12288\n\t"
    "v_cvt_pk_bf16_f32 v50, v12, v13\n\t"
    "v_mfma_f32_16x16x32_bf16 v[56:59], a[224:227], v[40:43], v[56:59]\n\t"
    "v_cvt_pk_bf16_f32 v51, v14, v15\n\t"
    "v_permlane16_swap_b32 v18, v19\n\t"
    "v_mfma_f32_16x16x32_bf16 v[56:59], a[232:235], v[44:47], v[56:59]\n\t"
    "ds_read_b128 v[136:139], v3 offset:18432\n\t"
    "v_add_f32_e32 v18, v18, v19\n\t"
    "v_mfma_f32_16x16x32_bf16 v[60:63], a[196:199], v[220:223], 0\n\t"
    "v_rcp_f32_e32 v18, v18\n\t"
    "v_mfma_f32_16x16x32_bf16 v[60:63], a[204:207], v[216:219], v[60:63]\n\t"
    "ds_read_b128 v[132:135], v4 offset:18432\n\t"
    "v_mul_f32_e32 v20, v20, v18\n\t"
    "v_mfma_f32_16x16x32_bf16 v[60:63], a[212:215], v[212:215], v[60:63]\n\t"
    "v_mul_f32_e32 v21, v21, v18\n\t"
    "v_cvt_pk_bf16_f32 v12, v20, v21\n\t"
    "v_mfma_f32_16x16x32_bf16 v[60:63], a[220:223], v[36:39], v[60:63]\n\t"
    "ds_read_b128 v[128:131], v5 offset:18432\n\t"
    "v_cvt_pk_bf16_f32 v76, v56, v57\n\t"
    "v_mfma_f32_16x16x32_bf16 v[60:63], a[228:231], v[40:43], v[60:63]\n\t"
    "v_cvt_pk_bf16_f32 v77, v58, v59\n\t"
    "v_mul_f32_e32 v22, v22, v18\n\t"
    "v_mfma_f32_16x16x32_bf16 v[60:63], a[236:239], v[44:47], v[60:63]\n\t"
    "v_mul_f32_e32 v23, v23, v18\n\t"
    "v_cvt_pk_bf16_f32 v13, v22, v23\n\t"
    "v_mfma_f32_16x16x32_bf16 v[80:83], a[240:243], v[220:223], 0\n\t"
    "v_mul_f32_e32 v24, v24, v18\n\t"
    "v_mul_f32_e32 v25, v25, v18\n\t"
    "v_mfma_f32_16x16x32_bf16 v[80:83], a[248:251], v[216:219], v[80:83]\n\t"
    "v_cvt_pk_bf16_f32 v14, v24, v25\n\t"
    "v_mul_f32_e32 v26, v26, v18\n\t"
    "v_mfma_f32_16x16x32_bf16 v[80:83], v[224:227], v[212:215], v[80:83]\n\t"
    "v_mul_f32_e32 v27, v27, v18\n\t"
    "v_cvt_pk_bf16_f32 v15, v26, v27\n\t"
    "v_mfma_f32_16x16x32_bf16 v[80:83], v[232:235], v[36:39], v[80:83]\n\t"
    "v_cvt_pk_bf16_f32 v78, v60, v61\n\t"
    "v_cvt_pk_bf16_f32 v79, v62, v63\n\t"
    "v_mfma_f32_16x16x32_bf16 v[80:83], v[240:243], v[40:43], v[80:83]\n\t"
    "v_mul_f32_e32 v28, v28, v18\n\t"
    "v_mul_f32_e32 v29, v29, v18\n\t"
    "v_mfma_f32_16x16x32_bf16 v[80:83], v[248:251], v[44:47], v[80:83]\n\t"
    "v_cvt_pk_bf16_f32 v20, v28, v29\n\t"
    "v_mul_f32_e32 v30, v30, v18\n\t"
    "v_mfma_f32_16x16x32_bf16 v[24:27], a[244:247], v[220:223], 0\n\t"
    "v_mul_f32_e32 v31, v31, v18\n\t"
    "v_cvt_pk_bf16_f32 v21, v30, v31\n\t"
    "v_mfma_f32_16x16x32_bf16 v[24:27], a[252:255], v[216:219], v[24:27]\n\t"
    "v_mul_f32_e32 v32, v32, v18\n\t"
    "v_mul_f32_e32 v33, v33, v18\n\t"
    "v_mfma_f32_16x16x32_bf16 v[24:27], v[228:231], v[212:215], v[24:27]\n\t"
    "v_cvt_pk_bf16_f32 v22, v32, v33\n\t"
    "v_mul_f32_e32 v34, v34, v18\n\t"
    "v_mfma_f32_16x16x32_bf16 v[24:27], v[236:239], v[36:39], v[24:27]\n\t"
    "v_cvt_pk_bf16_f32 v28, v80, v81\n\t"
    "v_cvt_pk_bf16_f32 v29, v82, v83\n\t"
    "v_mfma_f32_16x16x32_bf16 v[24:27], v[244:247], v[40:43], v[24:27]\n\t"
    "v_mul_f32_e32 v35, v35, v18\n\t"
    "v_cvt_pk_bf16_f32 v23, v34, v35\n\t"
    "v_mfma_f32_16x16x32_bf16 v[24:27], v[252:255], v[44:47], v[24:27]\n\t"
    "v_mfma_f32_16x16x32_bf16 v[36:39], a[0:3], v[196:199], 0\n\t"
    "ds_write_b128 v6, v[12:15] offset:6144\n\t"
    "v_mfma_f32_16x16x32_bf16 v[36:39], a[8:11], v[192:195], v[36:39]\n\t"
    "v_mfma_f32_16x16x32_bf16 v[36:39], a[16:19], v[188:191], v[36:39]\n\t"
    "s_waitcnt lgkmcnt(1)\n\t"
    "v_mfma_f32_16x16x32_bf16 v[36:39], a[24:27], v[184:187], v[36:39]\n\t"
    "v_cvt_pk_bf16_f32 v30, v24, v25\n\t"
    "v_cvt_pk_bf16_f32 v31, v26, v27\n\t"
    "v_mfma_f32_16x16x32_bf16 v[36:39], a[32:35], v[180:183], v[36:39]\n\t"
    "ds_write_b128 v6, v[20:23] offset:7168\n\t"
    "v_mfma_f32_16x16x32_bf16 v[36:39], a[40:43], v[176:179], v[36:39]\n\t"
    "v_mfma_f32_16x16x32_bf16 v[52:55], v[208:211], v[48:51], v[52:55]\n\t"
    "v_mfma_f32_16x16x32_bf16 v[52:55], v[204:207], v[76:79], v[52:55]\n\t"
    "v_mfma_f32_16x16x32_bf16 v[52:55], v[200:203], v[28:31], v[52:55]\n\t"
    "v_mfma_f32_16x16x32_bf16 v[64:67], v[184:187], v[48:51], v[64:67]\n\t"
    "v_cvt_pk_bf16_f32 v12, v36, v37\n\t"
    "v_cvt_pk_bf16_f32 v13, v38, v39\n\t"
    "v_mfma_f32_16x16x32_bf16 v[64:67], v[180:183], v[76:79], v[64:67]\n\t"
    "s_waitcnt lgkmcnt(0)\n\t"
    "s_barrier\n\t"
    ".Lmid_%=:\n\t"
    "v_mfma_f32_16x16x32_bf16 v[64:67], v[176:179], v[28:31], v[64:67]\n\t"
    "s_waitcnt lgkmcnt(0)\n\t"
    "v_mfma_f32_16x16x32_bf16 v[68:71], v[160:163], v[48:51], v[68:71]\n\t"
    "v_max3_f32 v16, v52, v53, v54\n\t"
    "v_mfma_f32_16x16x32_bf16 v[68:71], v[156:159], v[76:79], v[68:71]\n\t"
    "v_mfma_f32_16x16x32_bf16 v[68:71], v[152:155], v[28:31], v[68:71]\n\t"
    "v_mfma_f32_16x16x32_bf16 v[72:75], v[136:139], v[48:51], v[72:75]\n\t"
    "v_max3_f32 v17, v55, v64, v65\n\t"
    "v_max3_f32 v16, v16, v66, v67\n\t"
    "v_mfma_f32_16x16x32_bf16 v[72:75], v[132:135], v[76:79], v[72:75]\n\t"
    "v_mfma_f32_16x16x32_bf16 v[72:75], v[128:131], v[28:31], v[72:75]\n\t"
    "v_mfma_f32_16x16x32_bf16 v[20:23], a[4:7], v[196:199], 0\n\t"
    "v_max3_f32 v17, v17, v68, v69\n\t"
    "v_max3_f32 v16, v16, v70, v71\n\t"
    "v_mfma_f32_16x16x32_bf16 v[20:23], a[12:15], v[192:195], v[20:23]\n\t"
    "v_mfma_f32_16x16x32_bf16 v[20:23], a[20:23], v[188:191], v[20:23]\n\t"
    "v_mfma_f32_16x16x32_bf16 v[20:23], a[28:31], v[184:187], v[20:23]\n\t"
    "s_add_u32 m0, s62, 0\n\t"
    "v_max3_f32 v17, v17, v72, v73\n\t"
    "v_mfma_f32_16x16x32_bf16 v[20:23], a[36:39], v[180:183], v[20:23]\n\t"
    "buffer_load_dwordx4 v7, s[52:55], s61 offen offset:0 nt lds\n\t"
    "v_max3_f32 v16, v16, v74, v75\n\t"
    "v_mfma_f32_16x16x32_bf16 v[20:23], a[44:47], v[176:179], v[20:23]\n\t"
    "v_max_f32_e32 v16, v16, v17\n\t"
    "v_mov_b32_e32 v17, v16\n\t"
    "v_mfma_f32_16x16x32_bf16 v[24:27], a[48:51], v[196:199], 0\n\t"
    "v_mfma_f32_16x16x32_bf16 v[24:27], a[56:59], v[192:195], v[24:27]\n\t"
    "v_permlane32_swap_b32 v16, v17\n\t"
    "v_max_f32_e32 v16, v16, v17\n\t"
    "v_mfma_f32_16x16x32_bf16 v[24:27], a[64:67], v[188:191], v[24:27]\n\t"
    "v_mov_b32_e32 v17, v16\n\t"
    "v_mfma_f32_16x16x32_bf16 v[24:27], a[72:75], v[184:187], v[24:27]\n\t"
    "v_cvt_pk_bf16_f32 v14, v20, v21\n\t"
    "v_cvt_pk_bf16_f32 v15, v22, v23\n\t"
    "v_mfma_f32_16x16x32_bf16 v[24:27], a[80:83], v[180:183], v[24:27]\n\t"
    "v_permlane16_swap_b32 v16, v17\n\t"
    "v_max_f32_e32 v16, v16, v17\n\t"
    "v_mfma_f32_16x16x32_bf16 v[24:27], a[88:91], v[176:179], v[24:27]\n\t"
    "v_mul_f32_e32 v16, s68, v16\n\t"
    "v_fma_f32 v52, v52, s68, -v16\n\t"
    "v_mfma_f32_16x16x32_bf16 v[20:23], a[52:55], v[196:199], 0\n\t"
    "v_exp_f32_e32 v52, v52\n\t"
    "v_mfma_f32_16x16x32_bf16 v[20:23], a[60:63], v[192:195], v[20:23]\n\t"
    "v_fma_f32 v53, v53, s68, -v16\n\t"
    "v_fma_f32 v54, v54, s68, -v16\n\t"
    "v_mfma_f32_16x16x32_bf16 v[20:23], a[68:71], v[188:191], v[20:23]\n\t"
    "v_exp_f32_e32 v53, v53\n\t"
    "v_mfma_f32_16x16x32_bf16 v[20:23], a[76:79], v[184:187], v[20:23]\n\t"
    "buffer_load_dwordx4 v7, s[52:55], s61 offen offset:1024 nt lds\n\t"
    "v_cvt_pk_bf16_f32 v28, v24, v25\n\t"
    "v_mfma_f32_16x16x32_bf16 v[20:23], a[84:87], v[180:183], v[20:23]\n\t"
    "v_cvt_pk_bf16_f32 v29, v26, v27\n\t"
    "v_add_f32_e32 v18, v52, v53\n\t"
    "v_mfma_f32_16x16x32_bf16 v[20:23], a[92:95], v[176:179], v[20:23]\n\t"
    "v_exp_f32_e32 v54, v54\n\t"
    "v_mfma_f32_16x16x32_bf16 v[32:35], a[96:99], v[196:199], 0\n\t"
    "v_fma_f32 v55, v55, s68, -v16\n\t"
    "v_fma_f32 v64, v64, s68, -v16\n\t"
    "v_mfma_f32_16x16x32_bf16 v[32:35], a[104:107], v[192:195], v[32:35]\n\t"
    "v_exp_f32_e32 v55, v55\n\t"
    "v_mfma_f32_16x16x32_bf16 v[32:35], a[112:115], v[188:191], v[32:35]\n\t"
    "v_add_f32_e32 v19, v54, v55\n\t"
    "v_fma_f32 v65, v65, s68, -v16\n\t"
    "v_mfma_f32_16x16x32_bf16 v[32:35], a[120:123], v[184:187], v[32:35]\n\t"
    "v_cvt_pk_bf16_f32 v30, v20, v21\n\t"
    "v_cvt_pk_bf16_f32 v31, v22, v23\n\t"
    "v_mfma_f32_16x16x32_bf16 v[32:35], a[128:131], v[180:183], v[32:35]\n\t"
    "v_exp_f32_e32 v64, v64\n\t"
    "v_mfma_f32_16x16x32_bf16 v[32:35], a[136:139], v[176:179], v[32:35]\n\t"
    "v_add_f32_e32 v18, v18, v64\n\t"
    "v_fma_f32 v66, v66, s68, -v16\n\t"
    "v_mfma_f32_16x16x32_bf16 v[20:23], a[100:103], v[196:199], 0\n\t"
    "v_exp_f32_e32 v65, v65\n\t"
    "v_mfma_f32_16x16x32_bf16 v[20:23], a[108:111], v[192:195], v[20:23]\n\t"
    "v_add_f32_e32 v18, v18, v65\n\t"
    "v_fma_f32 v67, v67, s68, -v16\n\t"
    "v_mfma_f32_16x16x32_bf16 v[20:23], a[116:119], v[188:191], v[20:23]\n\t"
    "buffer_load_dwordx4 v7, s[52:55], s61 offen offset:2048 nt lds\n\t"
    "v_fma_f32 v68, v68, s68, -v16\n\t"
    "v_mfma_f32_16x16x32_bf16 v[20:23], a[124:127], v[184:187], v[20:23]\n\t"
    "s_add_u32 s61, s61, 196608\n\t"
    "v_cvt_pk_bf16_f32 v24, v32, v33\n\t"
    "v_mfma_f32_16x16x32_bf16 v[20:23], a[132:135], v[180:183], v[20:23]\n\t"
    "v_cvt_pk_bf16_f32 v25, v34, v35\n\t"
    "v_fma_f32 v69, v69, s68, -v16\n\t"
    "v_mfma_f32_16x16x32_bf16 v[20:23], a[140:143], v[176:179], v[20:23]\n\t"
    "v_exp_f32_e32 v66, v66\n\t"
    "v_mfma_f32_16x16x32_bf16 v[36:39], a[144:147], v[196:199], 0\n\t"
    "v_add_f32_e32 v19, v19, v66\n\t"
    "v_fma_f32 v70, v70, s68, -v16\n\t"
    "v_mfma_f32_16x16x32_bf16 v[36:39], a[152:155], v[192:195], v[36:39]\n\t"
    "v_exp_f32_e32 v67, v67\n\t"
    "v_mfma_f32_16x16x32_bf16 v[36:39], a[160:163], v[188:191], v[36:39]\n\t"
    "v_add_f32_e32 v19, v19, v67\n\t"
    "v_fma_f32 v71, v71, s68, -v16\n\t"
    "v_mfma_f32_16x16x32_bf16 v[36:39], a[168:171], v[184:187], v[36:39]\n\t"
    "v_cvt_pk_bf16_f32 v26, v20, v21\n\t"
    "v_cvt_pk_bf16_f32 v27, v22, v23\n\t"
    "v_mfma_f32_16x16x32_bf16 v[36:39], a[176:179], v[180:183], v[36:39]\n\t"
    "v_exp_f32_e32 v68, v68\n\t"
    "v_mfma_f32_16x16x32_bf16 v[36:39], a[184:187], v[176:179], v[36:39]\n\t"
    "v_add_f32_e32 v18, v18, v68\n\t"
    "v_fma_f32 v72, v72, s68, -v16\n\t"
    "v_mfma_f32_16x16x32_bf16 v[20:23], v[220:223], v[12:15], 0\n\t"
    "s_add_u32 m0, s62, 3072\n\t"
    "v_fma_f32 v73, v73, s68, -v16\n\t"
    "v_mfma_f32_16x16x32_bf16 v[20:23], v[216:219], v[28:31], v[20:23]\n\t"
    "buffer_load_dwordx4 v7, s[52:55], s61 offen offset:0 nt lds\n\t"
    "v_fma_f32 v74, v74, s68, -v16\n\t"
    "v_mfma_f32_16x16x32_bf16 v[20:23], v[212:215], v[24:27], v[20:23]\n\t"
    "v_exp_f32_e32 v69, v69\n\t"
    "v_mfma_f32_16x16x32_bf16 v[32:35], v[196:199], v[12:15], 0\n\t"
    "v_add_f32_e32 v18, v18, v69\n\t"
    "v_fma_f32 v75, v75, s68, -v16\n\t"
    "v_mfma_f32_16x16x32_bf16 v[32:35], v[192:195], v[28:31], v[32:35]\n\t"
    "v_exp_f32_e32 v70, v70\n\t"
    "v_mfma_f32_16x16x32_bf16 v[32:35], v[188:191], v[24:27], v[32:35]\n\t"
    "v_add_f32_e32 v19, v19, v70\n\t"
    "v_cvt_pk_bf16_f32 v40, v36, v37\n\t"
    "v_mfma_f32_16x16x32_bf16 v[44:47], v[172:175], v[12:15], 0\n\t"
    "v_exp_f32_e32 v71, v71\n\t"
    "v_mfma_f32_16x16x32_bf16 v[44:47], v[168:171], v[28:31], v[44:47]\n\t"
    "v_add_f32_e32 v19, v19, v71\n\t"
    "v_cvt_pk_bf16_f32 v41, v38, v39\n\t"
    "v_mfma_f32_16x16x32_bf16 v[44:47], v[164:167], v[24:27], v[44:47]\n\t"
    "v_exp_f32_e32 v72, v72\n\t"
    "v_mfma_f32_16x16x32_bf16 v[48:51], v[148:151], v[12:15], 0\n\t"
    "v_add_f32_e32 v18, v18, v72\n\t"
    "v_mfma_f32_16x16x32_bf16 v[48:51], v[144:147], v[28:31], v[48:51]\n\t"
    "v_exp_f32_e32 v73, v73\n\t"
    "v_mfma_f32_16x16x32_bf16 v[48:51], v[140:143], v[24:27], v[48:51]\n\t"
    "v_add_f32_e32 v18, v18, v73\n\t"
    "v_mfma_f32_16x16x32_bf16 v[12:15], a[148:151], v[196:199], 0\n\t"
    "buffer_load_dwordx4 v7, s[52:55], s61 offen offset:1024 nt lds\n\t"
    "v_mfma_f32_16x16x32_bf16 v[12:15], a[156:159], v[192:195], v[12:15]\n\t"
    "v_exp_f32_e32 v74, v74\n\t"
    "v_mfma_f32_16x16x32_bf16 v[12:15], a[164:167], v[188:191], v[12:15]\n\t"
    "v_add_f32_e32 v19, v19, v74\n\t"
    "v_mfma_f32_16x16x32_bf16 v[12:15], a[172:175], v[184:187], v[12:15]\n\t"
    "v_exp_f32_e32 v75, v75\n\t"
    "v_mfma_f32_16x16x32_bf16 v[12:15], a[180:183], v[180:183], v[12:15]\n\t"
    "v_add_f32_e32 v19, v19, v75\n\t"
    "v_add_f32_e32 v18, v18, v19\n\t"
    "v_mfma_f32_16x16x32_bf16 v[12:15], a[188:191], v[176:179], v[12:15]\n\t"
    "v_mov_b32_e32 v19, v18\n\t"
    "v_mfma_f32_16x16x32_bf16 v[24:27], a[192:195], v[196:199], 0\n\t"
    "v_mfma_f32_16x16x32_bf16 v[24:27], a[200:203], v[192:195], v[24:27]\n\t"
    "v_permlane32_swap_b32 v18, v19\n\t"
    "v_add_f32_e32 v18, v18, v19\n\t"
    "v_mfma_f32_16x16x32_bf16 v[24:27], a[208:211], v[188:191], v[24:27]\n\t"
    "v_mov_b32_e32 v19, v18\n\t"
    "v_mfma_f32_16x16x32_bf16 v[24:27], a[216:219], v[184:187], v[24:27]\n\t"
    "v_cvt_pk_bf16_f32 v42, v12, v13\n\t"
    "v_cvt_pk_bf16_f32 v43, v14, v15\n\t"
    "v_mfma_f32_16x16x32_bf16 v[24:27], a[224:227], v[180:183], v[24:27]\n\t"
    "v_permlane16_swap_b32 v18, v19\n\t"
    "v_add_f32_e32 v18, v18, v19\n\t"
    "v_mfma_f32_16x16x32_bf16 v[24:27], a[232:235], v[176:179], v[24:27]\n\t"
    "buffer_load_dwordx4 v7, s[52:55], s61 offen offset:2048 nt lds\n\t"
    "v_mfma_f32_16x16x32_bf16 v[12:15], a[196:199], v[196:199], 0\n\t"
    "s_add_u32 s61, s61, 196608\n\t"
    "v_mfma_f32_16x16x32_bf16 v[12:15], a[204:207], v[192:195], v[12:15]\n\t"
    "v_rcp_f32_e32 v18, v18\n\t"
    "v_mfma_f32_16x16x32_bf16 v[12:15], a[212:215], v[188:191], v[12:15]\n\t"
    "v_mul_f32_e32 v52, v52, v18\n\t"
    "v_mul_f32_e32 v53, v53, v18\n\t"
    "v_mfma_f32_16x16x32_bf16 v[12:15], a[220:223], v[184:187], v[12:15]\n\t"
    "v_cvt_pk_bf16_f32 v28, v24, v25\n\t"
    "v_cvt_pk_bf16_f32 v29, v26, v27\n\t"
    "v_mfma_f32_16x16x32_bf16 v[12:15], a[228:231], v[180:183], v[12:15]\n\t"
    "v_cvt_pk_bf16_f32 v36, v52, v53\n\t"
    "v_mul_f32_e32 v54, v54, v18\n\t"
    "v_mfma_f32_16x16x32_bf16 v[12:15], a[236:239], v[176:179], v[12:15]\n\t"
    "v_mul_f32_e32 v55, v55, v18\n\t"
    "v_cvt_pk_bf16_f32 v37, v54, v55\n\t"
    "v_mfma_f32_16x16x32_bf16 v[24:27], a[240:243], v[196:199], 0\n\t"
    "v_mul_f32_e32 v64, v64, v18\n\t"
    "v_mul_f32_e32 v65, v65, v18\n\t"
    "v_mfma_f32_16x16x32_bf16 v[24:27], a[248:251], v[192:195], v[24:27]\n\t"
    "v_cvt_pk_bf16_f32 v38, v64, v65\n\t"
    "v_mul_f32_e32 v66, v66, v18\n\t"
    "v_mfma_f32_16x16x32_bf16 v[24:27], v[224:227], v[188:191], v[24:27]\n\t"
    "v_mul_f32_e32 v67, v67, v18\n\t"
    "v_cvt_pk_bf16_f32 v39, v66, v67\n\t"
    "v_mfma_f32_16x16x32_bf16 v[24:27], v[232:235], v[184:187], v[24:27]\n\t"
    "s_add_u32 m0, s62, 6144\n\t"
    "v_cvt_pk_bf16_f32 v30, v12, v13\n\t"
    "v_mfma_f32_16x16x32_bf16 v[24:27], v[240:243], v[180:183], v[24:27]\n\t"
    "buffer_load_dwordx4 v7, s[52:55], s61 offen offset:0 nt lds\n\t"
    "v_cvt_pk_bf16_f32 v31, v14, v15\n\t"
    "v_mfma_f32_16x16x32_bf16 v[24:27], v[248:251], v[176:179], v[24:27]\n\t"
    "v_mul_f32_e32 v68, v68, v18\n\t"
    "v_mul_f32_e32 v69, v69, v18\n\t"
    "v_mfma_f32_16x16x32_bf16 v[52:55], a[244:247], v[196:199], 0\n\t"
    "v_cvt_pk_bf16_f32 v56, v68, v69\n\t"
    "v_mul_f32_e32 v70, v70, v18\n\t"
    "v_mfma_f32_16x16x32_bf16 v[52:55], a[252:255], v[192:195], v[52:55]\n\t"
    "v_mul_f32_e32 v71, v71, v18\n\t"
    "v_cvt_pk_bf16_f32 v57, v70, v71\n\t"
    "v_mfma_f32_16x16x32_bf16 v[52:55], v[228:231], v[188:191], v[52:55]\n\t"
    "v_mul_f32_e32 v72, v72, v18\n\t"
    "v_mul_f32_e32 v73, v73, v18\n\t"
    "v_mfma_f32_16x16x32_bf16 v[52:55], v[236:239], v[184:187], v[52:55]\n\t"
    "v_cvt_pk_bf16_f32 v12, v24, v25\n\t"
    "v_cvt_pk_bf16_f32 v13, v26, v27\n\t"
    "v_mfma_f32_16x16x32_bf16 v[52:55], v[244:247], v[180:183], v[52:55]\n\t"
    "v_cvt_pk_bf16_f32 v58, v72, v73\n\t"
    "v_mul_f32_e32 v74, v74, v18\n\t"
    "v_mfma_f32_16x16x32_bf16 v[52:55], v[252:255], v[176:179], v[52:55]\n\t"
    "v_mul_f32_e32 v75, v75, v18\n\t"
    "v_cvt_pk_bf16_f32 v59, v74, v75\n\t"
    "v_mfma_f32_16x16x32_bf16 v[24:27], a[0:3], v[172:175], 0\n\t"
    "ds_write_b128 v6, v[36:39] offset:0\n\t"
    "v_mfma_f32_16x16x32_bf16 v[24:27], a[8:11], v[168:171], v[24:27]\n\t"
    "v_mfma_f32_16x16x32_bf16 v[24:27], a[16:19], v[164:167], v[24:27]\n\t"
    "v_mfma_f32_16x16x32_bf16 v[24:27], a[24:27], v[160:163], v[24:27]\n\t"
    "buffer_load_dwordx4 v7, s[52:55], s61 offen offset:1024 nt lds\n\t"
    "v_cvt_pk_bf16_f32 v14, v52, v53\n\t"
    "v_mfma_f32_16x16x32_bf16 v[24:27], a[32:35], v[156:159], v[24:27]\n\t"
    "ds_write_b128 v6, v[56:59] offset:1024\n\t"
    "v_mfma_f32_16x16x32_bf16 v[24:27], a[40:43], v[152:155], v[24:27]\n\t"
    "v_cvt_pk_bf16_f32 v15, v54, v55\n\t"
    "v_mfma_f32_16x16x32_bf16 v[20:23], v[208:211], v[40:43], v[20:23]\n\t"
    "v_mfma_f32_16x16x32_bf16 v[20:23], v[204:207], v[28:31], v[20:23]\n\t"
    "v_mfma_f32_16x16x32_bf16 v[20:23], v[200:203], v[12:15], v[20:23]\n\t"
    "s_add_i32 s64, s62, %[rd]\n\t"
    "v_mfma_f32_16x16x32_bf16 v[32:35], v[184:187], v[40:43], v[32:35]\n\t"
    "s_cmp_eq_u32 s64, %[rend]\n\t"
    "s_cselect_b32 s64, %[rbeg], s64\n\t"
    "v_mfma_f32_16x16x32_bf16 v[32:35], v[180:183], v[28:31], v[32:35]\n\t"
    "s_sub_i32 s65, s63, s64\n\t"
    "v_cvt_pk_bf16_f32 v16, v24, v25\n\t"
    "v_mfma_f32_16x16x32_bf16 v[32:35], v[176:179], v[12:15], v[32:35]\n\t"
    "v_add_u32_e32 v0, s65, v0\n\t"
    "v_add_u32_e32 v1, s65, v1\n\t"
    "v_mfma_f32_16x16x32_bf16 v[44:47], v[160:163], v[40:43], v[44:47]\n\t"
    "v_add_u32_e32 v2, s65, v2\n\t"
    "v_add_u32_e32 v3, s65, v3\n\t"
    "v_mfma_f32_16x16x32_bf16 v[44:47], v[156:159], v[28:31], v[44:47]\n\t"
    "v_add_u32_e32 v4, s65, v4\n\t"
    "v_add_u32_e32 v5, s65, v5\n\t"
    "v_mfma_f32_16x16x32_bf16 v[44:47], v[152:155], v[12:15], v[44:47]\n\t"
    "buffer_load_dwordx4 v7, s[52:55], s61 offen offset:2048 nt lds\n\t"
    "v_max3_f32 v36, v20, v21, v22\n\t"
    "v_mfma_f32_16x16x32_bf16 v[48:51], v[136:139], v[40:43], v[48:51]\n\t"
    "s_add_u32 s61, s61, 196608\n\t"
    "v_max3_f32 v37, v23, v32, v33\n\t"
    "v_mfma_f32_16x16x32_bf16 v[48:51], v[132:135], v[28:31], v[48:51]\n\t"
    "v_max3_f32 v36, v36, v34, v35\n\t"
    "v_cvt_pk_bf16_f32 v17, v26, v27\n\t"
    "v_mfma_f32_16x16x32_bf16 v[48:51], v[128:131], v[12:15], v[48:51]\n\t"
    "v_mfma_f32_16x16x32_bf16 v[40:43], a[4:7], v[172:175], 0\n\t"
    "v_max3_f32 v37, v37, v44, v45\n\t"
    "v_max3_f32 v36, v36, v46, v47\n\t"
    "v_mfma_f32_16x16x32_bf16 v[40:43], a[12:15], v[168:171], v[40:43]\n\t"
    "v_mfma_f32_16x16x32_bf16 v[40:43], a[20:23], v[164:167], v[40:43]\n\t"
    "v_mfma_f32_16x16x32_bf16 v[40:43], a[28:31], v[160:163], v[40:43]\n\t"
    "v_max3_f32 v37, v37, v48, v49\n\t"
    "v_max3_f32 v36, v36, v50, v51\n\t"
    "v_mfma_f32_16x16x32_bf16 v[40:43], a[36:39], v[156:159], v[40:43]\n\t"
    "v_max_f32_e32 v36, v36, v37\n\t"
    "v_mov_b32_e32 v37, v36\n\t"
    "v_mfma_f32_16x16x32_bf16 v[40:43], a[44:47], v[152:155], v[40:43]\n\t"
    "v_mfma_f32_16x16x32_bf16 v[12:15], a[48:51], v[172:175], 0\n\t"
    "s_add_u32 m0, s62, 9216\n\t"
    "v_permlane32_swap_b32 v36, v37\n\t"
    "v_mfma_f32_16x16x32_bf16 v[12:15], a[56:59], v[168:171], v[12:15]\n\t"
    "buffer_load_dwordx4 v7, s[52:55], s61 offen offset:0 nt lds\n\t"
    "v_max_f32_e32 v36, v36, v37\n\t"
    "v_mfma_f32_16x16x32_bf16 v[12:15], a[64:67], v[164:167], v[12:15]\n\t"
    "v_mov_b32_e32 v37, v36\n\t"
    "v_mfma_f32_16x16x32_bf16 v[12:15], a[72:75], v[160:163], v[12:15]\n\t"
    "v_cvt_pk_bf16_f32 v18, v40, v41\n\t"
    "v_cvt_pk_bf16_f32 v19, v42, v43\n\t"
    "v_mfma_f32_16x16x32_bf16 v[12:15], a[80:83], v[156:159], v[12:15]\n\t"
    "v_permlane16_swap_b32 v36, v37\n\t"
    "v_max_f32_e32 v36, v36, v37\n\t"
    "v_mfma_f32_16x16x32_bf16 v[12:15], a[88:91], v[152:155], v[12:15]\n\t"
    "v_mul_f32_e32 v36, s68, v36\n\t"
    "v_fma_f32 v20, v20, s68, -v36\n\t"
    "v_mfma_f32_16x16x32_bf16 v[24:27], a[52:55], v[172:175], 0\n\t"
    "v_exp_f32_e32 v20, v20\n\t"
    "v_mfma_f32_16x16x32_bf16 v[24:27], a[60:63], v[168:171], v[24:27]\n\t"
    "v_fma_f32 v21, v21, s68, -v36\n\t"
    "v_fma_f32 v22, v22, s68, -v36\n\t"
    "v_mfma_f32_16x16x32_bf16 v[24:27], a[68:71], v[164:167], v[24:27]\n\t"
    "v_exp_f32_e32 v21, v21\n\t"
    "v_mfma_f32_16x16x32_bf16 v[24:27], a[76:79], v[160:163], v[24:27]\n\t"
    "v_cvt_pk_bf16_f32 v28, v12, v13\n\t"
    "v_cvt_pk_bf16_f32 v29, v14, v15\n\t"
    "v_mfma_f32_16x16x32_bf16 v[24:27], a[84:87], v[156:159], v[24:27]\n\t"
    "v_add_f32_e32 v38, v20, v21\n\t"
    "v_fma_f32 v23, v23, s68, -v36\n\t"
    "v_mfma_f32_16x16x32_bf16 v[24:27], a[92:95], v[152:155], v[24:27]\n\t"
    "v_exp_f32_e32 v22, v22\n\t"
    "v_mfma_f32_16x16x32_bf16 v[12:15], a[96:99], v[172:175], 0\n\t"
    "buffer_load_dwordx4 v7, s[52:55], s61 offen offset:1024 nt lds\n\t"
    "v_fma_f32 v32, v32, s68, -v36\n\t"
    "v_mfma_f32_16x16x32_bf16 v[12:15], a[104:107], v[168:171], v[12:15]\n\t"
    "v_exp_f32_e32 v23, v23\n\t"
    "v_mfma_f32_16x16x32_bf16 v[12:15], a[112:115], v[164:167], v[12:15]\n\t"
    "v_add_f32_e32 v39, v22, v23\n\t"
    "v_fma_f32 v33, v33, s68, -v36\n\t"
    "v_mfma_f32_16x16x32_bf16 v[12:15], a[120:123], v[160:163], v[12:15]\n\t"
    "v_cvt_pk_bf16_f32 v30, v24, v25\n\t"
    "v_cvt_pk_bf16_f32 v31, v26, v27\n\t"
    "v_mfma_f32_16x16x32_bf16 v[12:15], a[128:131], v[156:159], v[12:15]\n\t"
    "v_exp_f32_e32 v32, v32\n\t"
    "v_mfma_f32_16x16x32_bf16 v[12:15], a[136:139], v[152:155], v[12:15]\n\t"
    "v_add_f32_e32 v38, v38, v32\n\t"
    "v_fma_f32 v34, v34, s68, -v36\n\t"
    "v_mfma_f32_16x16x32_bf16 v[24:27], a[100:103], v[172:175], 0\n\t"
    "v_exp_f32_e32 v33, v33\n\t"
    "v_mfma_f32_16x16x32_bf16 v[24:27], a[108:111], v[168:171], v[24:27]\n\t"
    "v_add_f32_e32 v38, v38, v33\n\t"
    "v_fma_f32 v35, v35, s68, -v36\n\t"
    "v_mfma_f32_16x16x32_bf16 v[24:27], a[116:119], v[164:167], v[24:27]\n\t"
    "v_exp_f32_e32 v34, v34\n\t"
    "v_mfma_f32_16x16x32_bf16 v[24:27], a[124:127], v[160:163], v[24:27]\n\t"
    "v_cvt_pk_bf16_f32 v40, v12, v13\n\t"
    "v_cvt_pk_bf16_f32 v41, v14, v15\n\t"
    "v_mfma_f32_16x16x32_bf16 v[24:27], a[132:135], v[156:159], v[24:27]\n\t"
    "v_add_f32_e32 v39, v39, v34\n\t"
    "v_fma_f32 v44, v44, s68, -v36\n\t"
    "v_mfma_f32_16x16x32_bf16 v[24:27], a[140:143], v[152:155], v[24:27]\n\t"
    "buffer_load_dwordx4 v7, s[52:55], s61 offen offset:2048 nt lds\n\t"
    "v_fma_f32 v45, v45, s68, -v36\n\t"
    "v_mfma_f32_16x16x32_bf16 v[52:55], a[144:147], v[172:175], 0\n\t"
    "s_add_u32 s61, s61, 196608\n\t"
    "v_fma_f32 v46, v46, s68, -v36\n\t"
    "v_mfma_f32_16x16x32_bf16 v[52:55], a[152:155], v[168:171], v[52:55]\n\t"
    "v_exp_f32_e32 v35, v35\n\t"
    "v_mfma_f32_16x16x32_bf16 v[52:55], a[160:163], v[164:167], v[52:55]\n\t"
    "v_add_f32_e32 v39, v39, v35\n\t"
    "v_fma_f32 v47, v47, s68, -v36\n\t"
    "v_mfma_f32_16x16x32_bf16 v[52:55], a[168:171], v[160:163], v[52:55]\n\t"
    "v_cvt_pk_bf16_f32 v42, v24, v25\n\t"
    "v_cvt_pk_bf16_f32 v43, v26, v27\n\t"
    "v_mfma_f32_16x16x32_bf16 v[52:55], a[176:179], v[156:159], v[52:55]\n\t"
    "v_exp_f32_e32 v44, v44\n\t"
    "v_mfma_f32_16x16x32_bf16 v[52:55], a[184:187], v[152:155], v[52:55]\n\t"
    "v_add_f32_e32 v38, v38, v44\n\t"
    "v_fma_f32 v48, v48, s68, -v36\n\t"
    "v_mfma_f32_16x16x32_bf16 v[60:63], v[220:223], v[16:19], 0\n\t"
    "v_exp_f32_e32 v45, v45\n\t"
    "v_mfma_f32_16x16x32_bf16 v[60:63], v[216:219], v[28:31], v[60:63]\n\t"
    "v_add_f32_e32 v38, v38, v45\n\t"
    "v_fma_f32 v49, v49, s68, -v36\n\t"
    "v_mfma_f32_16x16x32_bf16 v[60:63], v[212:215], v[40:43], v[60:63]\n\t"
    "v_exp_f32_e32 v46, v46\n\t"
    "v_mfma_f32_16x16x32_bf16 v[68:71], v[196:199], v[16:19], 0\n\t"
    "s_add_u32 m0, s62, 12288\n\t"
    "v_add_f32_e32 v39, v39, v46\n\t"
    "v_mfma_f32_16x16x32_bf16 v[68:71], v[192:195], v[28:31], v[68:71]\n\t"
    "buffer_load_dwordx4 v7, s[52:55], s61 offen offset:0 nt lds\n\t"
    "v_fma_f32 v50, v50, s68, -v36\n\t"
    "v_mfma_f32_16x16x32_bf16 v[68:71], v[188:191], v[40:43], v[68:71]\n\t"
    "v_exp_f32_e32 v47, v47\n\t"
    "v_mfma_f32_16x16x32_bf16 v[72:75], v[172:175], v[16:19], 0\n\t"
    "v_add_f32_e32 v39, v39, v47\n\t"
    "v_fma_f32 v51, v51, s68, -v36\n\t"
    "v_mfma_f32_16x16x32_bf16 v[72:75], v[168:171], v[28:31], v[72:75]\n\t"
    "v_exp_f32_e32 v48, v48\n\t"
    "v_mfma_f32_16x16x32_bf16 v[72:75], v[164:167], v[40:43], v[72:75]\n\t"
    "v_add_f32_e32 v38, v38, v48\n\t"
    "v_cvt_pk_bf16_f32 v24, v52, v53\n\t"
    "v_mfma_f32_16x16x32_bf16 v[12:15], v[148:151], v[16:19], 0\n\t"
    "v_exp_f32_e32 v49, v49\n\t"
    "v_mfma_f32_16x16x32_bf16 v[12:15], v[144:147], v[28:31], v[12:15]\n\t"
    "v_add_f32_e32 v38, v38, v49\n\t"
    "v_cvt_pk_bf16_f32 v25, v54, v55\n\t"
    "v_mfma_f32_16x16x32_bf16 v[12:15], v[140:143], v[40:43], v[12:15]\n\t"
    "v_exp_f32_e32 v50, v50\n\t"
    "v_mfma_f32_16x16x32_bf16 v[16:19], a[148:151], v[172:175], 0\n\t"
    "v_add_f32_e32 v39, v39, v50\n\t"
    "v_mfma_f32_16x16x32_bf16 v[16:19], a[156:159], v[168:171], v[16:19]\n\t"
    "v_exp_f32_e32 v51, v51\n\t"
    "v_mfma_f32_16x16x32_bf16 v[16:19], a[164:167], v[164:167], v[16:19]\n\t"
    "v_add_f32_e32 v39, v39, v51\n\t"
    "v_add_f32_e32 v38, v38, v39\n\t"
    "v_mfma_f32_16x16x32_bf16 v[16:19], a[172:175], v[160:163], v[16:19]\n\t"
    "buffer_load_dwordx4 v7, s[52:55], s61 offen offset:1024 nt lds\n\t"
    "v_mov_b32_e32 v39, v38\n\t"
    "v_mfma_f32_16x16x32_bf16 v[16:19], a[180:183], v[156:159], v[16:19]\n\t"
    "v_mfma_f32_16x16x32_bf16 v[16:19], a[188:191], v[152:155], v[16:19]\n\t"
    "v_permlane32_swap_b32 v38, v39\n\t"
    "v_add_f32_e32 v38, v38, v39\n\t"
    "v_mfma_f32_16x16x32_bf16 v[28:31], a[192:195], v[172:175], 0\n\t"
    "v_mov_b32_e32 v39, v38\n\t"
    "v_mfma_f32_16x16x32_bf16 v[28:31], a[200:203], v[168:171], v[28:31]\n\t"
    "v_mfma_f32_16x16x32_bf16 v[28:31], a[208:211], v[164:167], v[28:31]\n\t"
    "v_permlane16_swap_b32 v38, v39\n\t"
    "v_add_f32_e32 v38, v38, v39\n\t"
    "v_mfma_f32_16x16x32_bf16 v[28:31], a[216:219], v[160:163], v[28:31]\n\t"
    "v_cvt_pk_bf16_f32 v26, v16, v17\n\t"
    "v_cvt_pk_bf16_f32 v27, v18, v19\n\t"
    "v_mfma_f32_16x16x32_bf16 v[28:31], a[224:227], v[156:159], v[28:31]\n\t"
    "v_rcp_f32_e32 v38, v38\n\t"
    "v_mfma_f32_16x16x32_bf16 v[28:31], a[232:235], v[152:155], v[28:31]\n\t"
    "v_mul_f32_e32 v20, v20, v38\n\t"
    "v_mul_f32_e32 v21, v21, v38\n\t"
    "v_mfma_f32_16x16x32_bf16 v[40:43], a[196:199], v[172:175], 0\n\t"
    "v_cvt_pk_bf16_f32 v16, v20, v21\n\t"
    "v_mul_f32_e32 v22, v22, v38\n\t"
    "v_mfma_f32_16x16x32_bf16 v[40:43], a[204:207], v[168:171], v[40:43]\n\t"
    "v_mul_f32_e32 v23, v23, v38\n\t"
    "v_cvt_pk_bf16_f32 v17, v22, v23\n\t"
    "v_mfma_f32_16x16x32_bf16 v[40:43], a[212:215], v[164:167], v[40:43]\n\t"
    "buffer_load_dwordx4 v7, s[52:55], s61 offen offset:2048 nt lds\n\t"
    "v_mul_f32_e32 v32, v32, v38\n\t"
    "v_mfma_f32_16x16x32_bf16 v[40:43], a[220:223], v[160:163], v[40:43]\n\t"
    "s_add_u32 s61, s61, 196608\n\t"
    "v_cvt_pk_bf16_f32 v52, v28, v29\n\t"
    "v_mfma_f32_16x16x32_bf16 v[40:43], a[228:231], v[156:159], v[40:43]\n\t"
    "v_cvt_pk_bf16_f32 v53, v30, v31\n\t"
    "v_mul_f32_e32 v33, v33, v38\n\t"
    "v_mfma_f32_16x16x32_bf16 v[40:43], a[236:239], v[152:155], v[40:43]\n\t"
    "v_cvt_pk_bf16_f32 v18, v32, v33\n\t"
    "v_mul_f32_e32 v34, v34, v38\n\t"
    "v_mfma_f32_16x16x32_bf16 v[20:23], a[240:243], v[172:175], 0\n\t"
    "v_mul_f32_e32 v35, v35, v38\n\t"
    "v_cvt_pk_bf16_f32 v19, v34, v35\n\t"
    "v_mfma_f32_16x16x32_bf16 v[20:23], a[248:251], v[168:171], v[20:23]\n\t"
    "v_mul_f32_e32 v44, v44, v38\n\t"
    "v_mul_f32_e32 v45, v45, v38\n\t"
    "v_mfma_f32_16x16x32_bf16 v[20:23], v[224:227], v[164:167], v[20:23]\n\t"
    "v_cvt_pk_bf16_f32 v28, v44, v45\n\t"
    "v_mul_f32_e32 v46, v46, v38\n\t"
    "v_mfma_f32_16x16x32_bf16 v[20:23], v[232:235], v[160:163], v[20:23]\n\t"
    "v_cvt_pk_bf16_f32 v54, v40, v41\n\t"
    "v_cvt_pk_bf16_f32 v55, v42, v43\n\t"
    "v_mfma_f32_16x16x32_bf16 v[20:23], v[240:243], v[156:159], v[20:23]\n\t"
    "v_mul_f32_e32 v47, v47, v38\n\t"
    "v_cvt_pk_bf16_f32 v29, v46, v47\n\t"
    "v_mfma_f32_16x16x32_bf16 v[20:23], v[248:251], v[152:155], v[20:23]\n\t"
    "v_mul_f32_e32 v48, v48, v38\n\t"
    "v_mul_f32_e32 v49, v49, v38\n\t"
    "v_mfma_f32_16x16x32_bf16 v[32:35], a[244:247], v[172:175], 0\n\t"
    "s_add_u32 m0, s62, 15360\n\t"
    "v_cvt_pk_bf16_f32 v30, v48, v49\n\t"
    "v_mfma_f32_16x16x32_bf16 v[32:35], a[252:255], v[168:171], v[32:35]\n\t"
    "buffer_load_dwordx4 v7, s[52:55], s61 offen offset:0 nt lds\n\t"
    "v_mul_f32_e32 v50, v50, v38\n\t"
    "v_mfma_f32_16x16x32_bf16 v[32:35], v[228:231], v[164:167], v[32:35]\n\t"
    "v_mul_f32_e32 v51, v51, v38\n\t"
    "v_cvt_pk_bf16_f32 v31, v50, v51\n\t"
    "v_mfma_f32_16x16x32_bf16 v[32:35], v[236:239], v[160:163], v[32:35]\n\t"
    "v_cvt_pk_bf16_f32 v40, v20, v21\n\t"
    "v_cvt_pk_bf16_f32 v41, v22, v23\n\t"
    "v_mfma_f32_16x16x32_bf16 v[32:35], v[244:247], v[156:159], v[32:35]\n\t"
    "v_mfma_f32_16x16x32_bf16 v[32:35], v[252:255], v[152:155], v[32:35]\n\t"
    "v_mfma_f32_16x16x32_bf16 v[20:23], a[0:3], v[148:151], 0\n\t"
    "ds_write_b128 v6, v[16:19] offset:2048\n\t"
    "v_mfma_f32_16x16x32_bf16 v[20:23], a[8:11], v[144:147], v[20:23]\n\t"
    "v_mfma_f32_16x16x32_bf16 v[20:23], a[16:19], v[140:143], v[20:23]\n\t"
    "v_mfma_f32_16x16x32_bf16 v[20:23], a[24:27], v[136:139], v[20:23]\n\t"
    "v_cvt_pk_bf16_f32 v42, v32, v33\n\t"
    "v_cvt_pk_bf16_f32 v43, v34, v35\n\t"
    "v_mfma_f32_16x16x32_bf16 v[20:23], a[32:35], v[132:135], v[20:23]\n\t"
    "ds_write_b128 v6, v[28:31] offset:3072\n\t"
    "v_mfma_f32_16x16x32_bf16 v[20:23], a[40:43], v[128:131], v[20:23]\n\t"
    "v_mfma_f32_16x16x32_bf16 v[60:63], v[208:211], v[24:27], v[60:63]\n\t"
    "buffer_load_dwordx4 v7, s[52:55], s61 offen offset:1024 nt lds\n\t"
    "v_mfma_f32_16x16x32_bf16 v[60:63], v[204:207], v[52:55], v[60:63]\n\t"
    "v_mfma_f32_16x16x32_bf16 v[60:63], v[200:203], v[40:43], v[60:63]\n\t"
    "v_mfma_f32_16x16x32_bf16 v[68:71], v[184:187], v[24:27], v[68:71]\n\t"
    "v_cvt_pk_bf16_f32 v44, v20, v21\n\t"
    "v_cvt_pk_bf16_f32 v45, v22, v23\n\t"
    "v_mfma_f32_16x16x32_bf16 v[68:71], v[180:183], v[52:55], v[68:71]\n\t"
    "v_mfma_f32_16x16x32_bf16 v[68:71], v[176:179], v[40:43], v[68:71]\n\t"
    "v_mfma_f32_16x16x32_bf16 v[72:75], v[160:163], v[24:27], v[72:75]\n\t"
    "v_max3_f32 v16, v60, v61, v62\n\t"
    "v_mfma_f32_16x16x32_bf16 v[72:75], v[156:159], v[52:55], v[72:75]\n\t"
    "v_mfma_f32_16x16x32_bf16 v[72:75], v[152:155], v[40:43], v[72:75]\n\t"
    "v_mfma_f32_16x16x32_bf16 v[12:15], v[136:139], v[24:27], v[12:15]\n\t"
    "v_max3_f32 v17, v63, v68, v69\n\t"
    "v_max3_f32 v16, v16, v70, v71\n\t"
    "v_mfma_f32_16x16x32_bf16 v[12:15], v[132:135], v[52:55], v[12:15]\n\t"
    "v_mfma_f32_16x16x32_bf16 v[12:15], v[128:131], v[40:43], v[12:15]\n\t"
    "buffer_load_dwordx4 v7, s[52:55], s61 offen offset:2048 nt lds\n\t"
    "v_mfma_f32_16x16x32_bf16 v[20:23], a[4:7], v[148:151], 0\n\t"
    "s_add_u32 s61, s61, 196608\n\t"
    "v_max3_f32 v17, v17, v72, v73\n\t"
    "v_mfma_f32_16x16x32_bf16 v[20:23], a[12:15], v[144:147], v[20:23]\n\t"
    "v_max3_f32 v16, v16, v74, v75\n\t"
    "v_mfma_f32_16x16x32_bf16 v[20:23], a[20:23], v[140:143], v[20:23]\n\t"
    "v_mfma_f32_16x16x32_bf16 v[20:23], a[28:31], v[136:139], v[20:23]\n\t"
    "v_max3_f32 v17, v17, v12, v13\n\t"
    "v_max3_f32 v16, v16, v14, v15\n\t"
    "v_mfma_f32_16x16x32_bf16 v[20:23], a[36:39], v[132:135], v[20:23]\n\t"
    "v_max_f32_e32 v16, v16, v17\n\t"
    "v_mov_b32_e32 v17, v16\n\t"
    "v_mfma_f32_16x16x32_bf16 v[20:23], a[44:47], v[128:131], v[20:23]\n\t"
    "v_mfma_f32_16x16x32_bf16 v[24:27], a[48:51], v[148:151], 0\n\t"
    "v_permlane32_swap_b32 v16, v17\n\t"
    "v_max_f32_e32 v16, v16, v17\n\t"
    "v_mfma_f32_16x16x32_bf16 v[24:27], a[56:59], v[144:147], v[24:27]\n\t"
    "v_mov_b32_e32 v17, v16\n\t"
    "v_mfma_f32_16x16x32_bf16 v[24:27], a[64:67], v[140:143], v[24:27]\n\t"
    "v_mfma_f32_16x16x32_bf16 v[24:27], a[72:75], v[136:139], v[24:27]\n\t"
    "s_add_u32 m0, s62, 18432\n\t"
    "v_cvt_pk_bf16_f32 v46, v20, v21\n\t"
    "v_mfma_f32_16x16x32_bf16 v[24:27], a[80:83], v[132:135], v[24:27]\n\t"
    "buffer_load_dwordx4 v7, s[52:55], s61 offen offset:0 nt lds\n\t"
    "v_cvt_pk_bf16_f32 v47, v22, v23\n\t"
    "v_mfma_f32_16x16x32_bf16 v[24:27], a[88:91], v[128:131], v[24:27]\n\t"
    "v_permlane16_swap_b32 v16, v17\n\t"
    "v_max_f32_e32 v16, v16, v17\n\t"
    "v_mfma_f32_16x16x32_bf16 v[28:31], a[52:55], v[148:151], 0\n\t"
    "v_mul_f32_e32 v16, s68, v16\n\t"
    "v_fma_f32 v60, v60, s68, -v16\n\t"
    "v_mfma_f32_16x16x32_bf16 v[28:31], a[60:63], v[144:147], v[28:31]\n\t"
    "v_exp_f32_e32 v60, v60\n\t"
    "v_mfma_f32_16x16x32_bf16 v[28:31], a[68:71], v[140:143], v[28:31]\n\t"
    "v_fma_f32 v61, v61, s68, -v16\n\t"
    "v_fma_f32 v62, v62, s68, -v16\n\t"
    "v_mfma_f32_16x16x32_bf16 v[28:31], a[76:79], v[136:139], v[28:31]\n\t"
    "v_cvt_pk_bf16_f32 v48, v24, v25\n\t"
    "v_cvt_pk_bf16_f32 v49, v26, v27\n\t"
    "v_mfma_f32_16x16x32_bf16 v[28:31], a[84:87], v[132:135], v[28:31]\n\t"
    "v_exp_f32_e32 v61, v61\n\t"
    "v_mfma_f32_16x16x32_bf16 v[28:31], a[92:95], v[128:131], v[28:31]\n\t"
    "v_add_f32_e32 v18, v60, v61\n\t"
    "v_fma_f32 v63, v63, s68, -v16\n\t"
    "v_mfma_f32_16x16x32_bf16 v[20:23], a[96:99], v[148:151], 0\n\t"
    "v_exp_f32_e32 v62, v62\n\t"
    "v_mfma_f32_16x16x32_bf16 v[20:23], a[104:107], v[144:147], v[20:23]\n\t"
    "v_exp_f32_e32 v63, v63\n\t"
    "v_mfma_f32_16x16x32_bf16 v[20:23], a[112:115], v[140:143], v[20:23]\n\t"
    "v_add_f32_e32 v19, v62, v63\n\t"
    "v_fma_f32 v68, v68, s68, -v16\n\t"
    "v_mfma_f32_16x16x32_bf16 v[20:23], a[120:123], v[136:139], v[20:23]\n\t"
    "buffer_load_dwordx4 v7, s[52:55], s61 offen offset:1024 nt lds\n\t"
    "v_cvt_pk_bf16_f32 v50, v28, v29\n\t"
    "v_mfma_f32_16x16x32_bf16 v[20:23], a[128:131], v[132:135], v[20:23]\n\t"
    "v_cvt_pk_bf16_f32 v51, v30, v31\n\t"
    "v_fma_f32 v69, v69, s68, -v16\n\t"
    "v_mfma_f32_16x16x32_bf16 v[20:23], a[136:139], v[128:131], v[20:23]\n\t"
    "v_exp_f32_e32 v68, v68\n\t"
    "v_mfma_f32_16x16x32_bf16 v[24:27], a[100:103], v[148:151], 0\n\t"
    "v_add_f32_e32 v18, v18, v68\n\t"
    "v_fma_f32 v70, v70, s68, -v16\n\t"
    "v_mfma_f32_16x16x32_bf16 v[24:27], a[108:111], v[144:147], v[24:27]\n\t"
    "v_exp_f32_e32 v69, v69\n\t"
    "v_mfma_f32_16x16x32_bf16 v[24:27], a[116:119], v[140:143], v[24:27]\n\t"
    "v_add_f32_e32 v18, v18, v69\n\t"
    "v_fma_f32 v71, v71, s68, -v16\n\t"
    "v_mfma_f32_16x16x32_bf16 v[24:27], a[124:127], v[136:139], v[24:27]\n\t"
    "v_cvt_pk_bf16_f32 v52, v20, v21\n\t"
    "v_cvt_pk_bf16_f32 v53, v22, v23\n\t"
    "v_mfma_f32_16x16x32_bf16 v[24:27], a[132:135], v[132:135], v[24:27]\n\t"
    "v_exp_f32_e32 v70, v70\n\t"
    "v_mfma_f32_16x16x32_bf16 v[24:27], a[140:143], v[128:131], v[24:27]\n\t"
    "v_add_f32_e32 v19, v19, v70\n\t"
    "v_fma_f32 v72, v72, s68, -v16\n\t"
    "v_mfma_f32_16x16x32_bf16 v[56:59], a[144:147], v[148:151], 0\n\t"
    "v_exp_f32_e32 v71, v71\n\t"
    "v_mfma_f32_16x16x32_bf16 v[56:59], a[152:155], v[144:147], v[56:59]\n\t"
    "v_add_f32_e32 v19, v19, v71\n\t"
    "v_fma_f32 v73, v73, s68, -v16\n\t"
    "v_mfma_f32_16x16x32_bf16 v[56:59], a[160:163], v[140:143], v[56:59]\n\t"
    "buffer_load_dwordx4 v7, s[52:55], s61 offen offset:2048 nt lds\n\t"
    "v_fma_f32 v74, v74, s68, -v16\n\t"
    "v_mfma_f32_16x16x32_bf16 v[56:59], a[168:171], v[136:139], v[56:59]\n\t"
    "s_add_u32 s61, s61, 196608\n\t"
    "v_cvt_pk_bf16_f32 v54, v24, v25\n\t"
    "v_mfma_f32_16x16x32_bf16 v[56:59], a[176:179], v[132:135], v[56:59]\n\t"
    "v_cvt_pk_bf16_f32 v55, v26, v27\n\t"
    "v_fma_f32 v75, v75, s68, -v16\n\t"
    "v_mfma_f32_16x16x32_bf16 v[56:59], a[184:187], v[128:131], v[56:59]\n\t"
    "v_exp_f32_e32 v72, v72\n\t"
    "v_mfma_f32_16x16x32_bf16 v[20:23], v[220:223], v[44:47], 0\n\t"
    "v_add_f32_e32 v18, v18, v72\n\t"
    "v_fma_f32 v12, v12, s68, -v16\n\t"
    "v_mfma_f32_16x16x32_bf16 v[20:23], v[216:219], v[48:51], v[20:23]\n\t"
    "v_exp_f32_e32 v73, v73\n\t"
    "v_mfma_f32_16x16x32_bf16 v[20:23], v[212:215], v[52:55], v[20:23]\n\t"
    "s_waitcnt vmcnt(21)\n\t"
    "v_add_f32_e32 v18, v18, v73\n\t"
    "v_mfma_f32_16x16x32_bf16 v[24:27], v[196:199], v[44:47], 0\n\t"
    "v_exp_f32_e32 v74, v74\n\t"
    "v_mfma_f32_16x16x32_bf16 v[24:27], v[192:195], v[48:51], v[24:27]\n\t"
    "ds_read_b128 v[220:223], v0 offset:0\n\t"
    "v_add_f32_e32 v19, v19, v74\n\t"
    "v_mfma_f32_16x16x32_bf16 v[24:27], v[188:191], v[52:55], v[24:27]\n\t"
    "v_exp_f32_e32 v75, v75\n\t"
    "v_mfma_f32_16x16x32_bf16 v[28:31], v[172:175], v[44:47], 0\n\t"
    "s_add_u32 m0, s62, 21504\n\t"
    "ds_read_b128 v[216:219], v1 offset:0\n\t"
    "v_mfma_f32_16x16x32_bf16 v[28:31], v[168:171], v[48:51], v[28:31]\n\t"
    "buffer_load_dwordx4 v7, s[52:55], s61 offen offset:0 nt lds\n\t"
    "v_add_f32_e32 v19, v19, v75\n\t"
    "v_fma_f32 v13, v13, s68, -v16\n\t"
    "v_mfma_f32_16x16x32_bf16 v[28:31], v[164:167], v[52:55], v[28:31]\n\t"
    "ds_read_b128 v[212:215], v2 offset:0\n\t"
    "v_fma_f32 v14, v14, s68, -v16\n\t"
    "v_mfma_f32_16x16x32_bf16 v[32:35], v[148:151], v[44:47], 0\n\t"
    "v_exp_f32_e32 v12, v12\n\t"
    "v_mfma_f32_16x16x32_bf16 v[32:35], v[144:147], v[48:51], v[32:35]\n\t"
    "ds_read_b128 v[36:39], v3 offset:0\n\t"
    "v_add_f32_e32 v18, v18, v12\n\t"
    "v_mfma_f32_16x16x32_bf16 v[32:35], v[140:143], v[52:55], v[32:35]\n\t"
    "v_exp_f32_e32 v13, v13\n\t"
    "v_mfma_f32_16x16x32_bf16 v[64:67], a[148:151], v[148:151], 0\n\t"
    "ds_read_b128 v[40:43], v4 offset:0\n\t"
    "v_add_f32_e32 v18, v18, v13\n\t"
    "v_mfma_f32_16x16x32_bf16 v[64:67], a[156:159], v[144:147], v[64:67]\n\t"
    "v_exp_f32_e32 v14, v14\n\t"
    "v_mfma_f32_16x16x32_bf16 v[64:67], a[164:167], v[140:143], v[64:67]\n\t"
    "ds_read_b128 v[44:47], v5 offset:0\n\t"
    "v_add_f32_e32 v19, v19, v14\n\t"
    "v_mfma_f32_16x16x32_bf16 v[64:67], a[172:175], v[136:139], v[64:67]\n\t"
    "v_fma_f32 v15, v15, s68, -v16\n\t"
    "v_cvt_pk_bf16_f32 v48, v56, v57\n\t"
    "v_mfma_f32_16x16x32_bf16 v[64:67], a[180:183], v[132:135], v[64:67]\n\t"
    "v_exp_f32_e32 v15, v15\n\t"
    "v_mfma_f32_16x16x32_bf16 v[64:67], a[188:191], v[128:131], v[64:67]\n\t"
    "v_add_f32_e32 v19, v19, v15\n\t"
    "v_add_f32_e32 v18, v18, v19\n\t"
    "v_mfma_f32_16x16x32_bf16 v[76:79], a[192:195], v[148:151], 0\n\t"
    "buffer_load_dwordx4 v7, s[52:55], s61 offen offset:1024 nt lds\n\t"
    "v_mov_b32_e32 v19, v18\n\t"
    "v_mfma_f32_16x16x32_bf16 v[76:79], a[200:203], v[144:147], v[76:79]\n\t"
    "v_cvt_pk_bf16_f32 v49, v58, v59\n\t"
    "v_mfma_f32_16x16x32_bf16 v[76:79], a[208:211], v[140:143], v[76:79]\n\t"
    "v_permlane32_swap_b32 v18, v19\n\t"
    "v_add_f32_e32 v18, v18, v19\n\t"
    "v_mfma_f32_16x16x32_bf16 v[76:79], a[216:219], v[136:139], v[76:79]\n\t"
    "v_cvt_pk_bf16_f32 v50, v64, v65\n\t"
    "v_cvt_pk_bf16_f32 v51, v66, v67\n\t"
    "v_mfma_f32_16x16x32_bf16 v[76:79], a[224:227], v[132:135], v[76:79]\n\t"
    "v_mov_b32_e32 v19, v18\n\t"
    "v_mfma_f32_16x16x32_bf16 v[76:79], a[232:235], v[128:131], v[76:79]\n\t"
    "v_mfma_f32_16x16x32_bf16 v[64:67], a[196:199], v[148:151], 0\n\t"
    "v_permlane16_swap_b32 v18, v19\n\t"
    "v_add_f32_e32 v18, v18, v19\n\t"
    "v_mfma_f32_16x16x32_bf16 v[64:67], a[204:207], v[144:147], v[64:67]\n\t"
    "v_rcp_f32_e32 v18, v18\n\t"
    "v_mfma_f32_16x16x32_bf16 v[64:67], a[212:215], v[140:143], v[64:67]\n\t"
    "v_mul_f32_e32 v60, v60, v18\n\t"
    "v_mul_f32_e32 v61, v61, v18\n\t"
    "v_mfma_f32_16x16x32_bf16 v[64:67], a[220:223], v[136:139], v[64:67]\n\t"
    "v_cvt_pk_bf16_f32 v52, v76, v77\n\t"
    "v_cvt_pk_bf16_f32 v53, v78, v79\n\t"
    "v_mfma_f32_16x16x32_bf16 v[64:67], a[228:231], v[132:135], v[64:67]\n\t"
    "v_cvt_pk_bf16_f32 v56, v60, v61\n\t"
    "v_mul_f32_e32 v62, v62, v18\n\t"
    "v_mfma_f32_16x16x32_bf16 v[64:67], a[236:239], v[128:131], v[64:67]\n\t"
    "buffer_load_dwordx4 v7, s[52:55], s61 offen offset:2048 nt lds\n\t"
    "v_mul_f32_e32 v63, v63, v18\n\t"
    "v_mfma_f32_16x16x32_bf16 v[76:79], a[240:243], v[148:151], 0\n\t"
    "s_add_u32 s61, s61, 11206656\n\t"
    "v_cvt_pk_bf16_f32 v57, v62, v63\n\t"
    "v_mfma_f32_16x16x32_bf16 v[76:79], a[248:251], v[144:147], v[76:79]\n\t"
    "v_mul_f32_e32 v68, v68, v18\n\t"
    "v_mul_f32_e32 v69, v69, v18\n\t"
    "v_mfma_f32_16x16x32_bf16 v[76:79], v[224:227], v[140:143], v[76:79]\n\t"
    "v_cvt_pk_bf16_f32 v58, v68, v69\n\t"
    "v_mul_f32_e32 v70, v70, v18\n\t"
    "v_mfma_f32_16x16x32_bf16 v[76:79], v[232:235], v[136:139], v[76:79]\n\t"
    "v_cvt_pk_bf16_f32 v54, v64, v65\n\t"
    "v_cvt_pk_bf16_f32 v55, v66, v67\n\t"
    "v_mfma_f32_16x16x32_bf16 v[76:79], v[240:243], v[132:135], v[76:79]\n\t"
    "v_mul_f32_e32 v71, v71, v18\n\t"
    "v_cvt_pk_bf16_f32 v59, v70, v71\n\t"
    "v_mfma_f32_16x16x32_bf16 v[76:79], v[248:251], v[128:131], v[76:79]\n\t"
    "v_mul_f32_e32 v72, v72, v18\n\t"
    "v_mul_f32_e32 v73, v73, v18\n\t"
    "v_mfma_f32_16x16x32_bf16 v[64:67], a[244:247], v[148:151], 0\n\t"
    "v_cvt_pk_bf16_f32 v60, v72, v73\n\t"
    "v_mul_f32_e32 v74, v74, v18\n\t"
    "v_mfma_f32_16x16x32_bf16 v[64:67], a[252:255], v[144:147], v[64:67]\n\t"
    "v_mul_f32_e32 v75, v75, v18\n\t"
    "v_cvt_pk_bf16_f32 v61, v74, v75\n\t"
    "v_mfma_f32_16x16x32_bf16 v[64:67], v[228:231], v[140:143], v[64:67]\n\t"
    "v_mul_f32_e32 v12, v12, v18\n\t"
    "v_mul_f32_e32 v13, v13, v18\n\t"
    "v_mfma_f32_16x16x32_bf16 v[64:67], v[236:239], v[136:139], v[64:67]\n\t"
    "v_cvt_pk_bf16_f32 v68, v76, v77\n\t"
    "v_cvt_pk_bf16_f32 v69, v78, v79\n\t"
    "v_mfma_f32_16x16x32_bf16 v[64:67], v[244:247], v[132:135], v[64:67]\n\t"
    "s_mov_b32 s63, s62\n\t"
    "s_mov_b32 s62, s64\n\t"
    "v_mfma_f32_16x16x32_bf16 v[64:67], v[252:255], v[128:131], v[64:67]\n\t"
    "s_sub_u32 s60, s60, 1\n\t"
    "s_cmp_lg_u32 s60, 0\n\t"
    "s_cbranch_scc1 .Lf3_%=\n\t"
    "v_mbcnt_lo_u32_b32 v8, -1, 0\n\t"
    "v_mbcnt_hi_u32_b32 v8, -1, v8\n\t"
    "v_lshlrev_b32_e32 v8, 5, v8\n\t"
    "s_nop 7\n\t"
    "s_nop 1\n\t"
    "ds_write_b128 v6, v[56:59] offset:4096\n\t"
    "v_cvt_pk_bf16_f32 v62, v12, v13\n\t"
    "v_mul_f32_e32 v14, v14, v18\n\t"
    "v_mul_f32_e32 v15, v15, v18\n\t"
    "v_cvt_pk_bf16_f32 v63, v14, v15\n\t"
    "v_cvt_pk_bf16_f32 v70, v64, v65\n\t"
    "v_cvt_pk_bf16_f32 v71, v66, v67\n\t"
    "ds_write_b128 v6, v[60:63] offset:5120\n\t"
    "s_nop 1\n\t"
    "v_mfma_f32_16x16x32_bf16 v[20:23], v[208:211], v[48:51], v[20:23]\n\t"
    "s_nop 1\n\t"
    "v_mfma_f32_16x16x32_bf16 v[20:23], v[204:207], v[52:55], v[20:23]\n\t"
    "s_nop 1\n\t"
    "v_mfma_f32_16x16x32_bf16 v[20:23], v[200:203], v[68:71], v[20:23]\n\t"
    "s_nop 1\n\t"
    "v_mfma_f32_16x16x32_bf16 v[24:27], v[184:187], v[48:51], v[24:27]\n\t"
    "s_nop 1\n\t"
    "v_mfma_f32_16x16x32_bf16 v[24:27], v[180:183], v[52:55], v[24:27]\n\t"
    "s_waitcnt lgkmcnt(0)\n\t"
    "s_barrier\n\t"
    "s_nop 1\n\t"
    "v_mfma_f32_16x16x32_bf16 v[24:27], v[176:179], v[68:71], v[24:27]\n\t"
    "s_nop 1\n\t"
    "v_mfma_f32_16x16x32_bf16 v[28:31], v[160:163], v[48:51], v[28:31]\n\t"
    "v_max3_f32 v16, v20, v21, v22\n\t"
    "s_nop 1\n\t"
    "v_mfma_f32_16x16x32_bf16 v[28:31], v[156:159], v[52:55], v[28:31]\n\t"
    "s_nop 1\n\t"
    "v_mfma_f32_16x16x32_bf16 v[28:31], v[152:155], v[68:71], v[28:31]\n\t"
    "s_nop 1\n\t"
    "v_mfma_f32_16x16x32_bf16 v[32:35], v[136:139], v[48:51], v[32:35]\n\t"
    "v_max3_f32 v17, v23, v24, v25\n\t"
    "v_max3_f32 v16, v16, v26, v27\n\t"
    "s_nop 1\n\t"
    "v_mfma_f32_16x16x32_bf16 v[32:35], v[132:135], v[52:55], v[32:35]\n\t"
    "s_nop 1\n\t"
    "v_mfma_f32_16x16x32_bf16 v[32:35], v[128:131], v[68:71], v[32:35]\n\t"
    "s_nop 7\n\t"
    "s_nop 1\n\t"
    "v_max3_f32 v17, v17, v28, v29\n\t"
    "v_max3_f32 v16, v16, v30, v31\n\t"
    "v_max3_f32 v17, v17, v32, v33\n\t"
    "v_max3_f32 v16, v16, v34, v35\n\t"
    "v_max_f32_e32 v16, v16, v17\n\t"
    "v_mov_b32_e32 v17, v16\n\t"
    "s_nop 1\n\t"
    "v_permlane32_swap_b32 v16, v17\n\t"
    "v_max_f32_e32 v16, v16, v17\n\t"
    "v_mov_b32_e32 v17, v16\n\t"
    "s_nop 1\n\t"
    "v_permlane16_swap_b32 v16, v17\n\t"
    "v_max_f32_e32 v16, v16, v17\n\t"
    "v_mul_f32_e32 v16, s68, v16\n\t"
    "v_fma_f32 v20, v20, s68, -v16\n\t"
    "v_fma_f32 v21, v21, s68, -v16\n\t"
    "v_exp_f32_e32 v20, v20\n\t"
    "s_nop 0\n\t"
    "v_fma_f32 v22, v22, s68, -v16\n\t"
    "v_exp_f32_e32 v21, v21\n\t"
    "s_nop 0\n\t"
    "v_add_f32_e32 v18, v20, v21\n\t"
    "v_fma_f32 v23, v23, s68, -v16\n\t"
    "v_exp_f32_e32 v22, v22\n\t"
    "s_nop 0\n\t"
    "v_exp_f32_e32 v23, v23\n\t"
    "s_nop 0\n\t"
    "v_add_f32_e32 v19, v22, v23\n\t"
    "v_fma_f32 v24, v24, s68, -v16\n\t"
    "v_exp_f32_e32 v24, v24\n\t"
    "s_nop 0\n\t"
    "v_add_f32_e32 v18, v18, v24\n\t"
    "v_fma_f32 v25, v25, s68, -v16\n\t"
    "v_exp_f32_e32 v25, v25\n\t"
    "s_nop 0\n\t"
    "v_add_f32_e32 v18, v18, v25\n\t"
    "v_fma_f32 v26, v26, s68, -v16\n\t"
    "v_exp_f32_e32 v26, v26\n\t"
    "s_nop 0\n\t"
    "v_add_f32_e32 v19, v19, v26\n\t"
    "v_fma_f32 v27, v27, s68, -v16\n\t"
    "v_exp_f32_e32 v27, v27\n\t"
    "s_nop 0\n\t"
    "v_add_f32_e32 v19, v19, v27\n\t"
    "v_fma_f32 v28, v28, s68, -v16\n\t"
    "v_exp_f32_e32 v28, v28\n\t"
    "s_nop 0\n\t"
    "v_add_f32_e32 v18, v18, v28\n\t"
    "v_fma_f32 v29, v29, s68, -v16\n\t"
    "v_exp_f32_e32 v29, v29\n\t"
    "s_nop 0\n\t"
    "v_add_f32_e32 v18, v18, v29\n\t"
    "v_fma_f32 v30, v30, s68, -v16\n\t"
    "v_exp_f32_e32 v30, v30\n\t"
    "s_nop 0\n\t"
    "v_add_f32_e32 v19, v19, v30\n\t"
    "v_fma_f32 v31, v31, s68, -v16\n\t"
    "v_fma_f32 v32, v32, s68, -v16\n\t"
    "v_exp_f32_e32 v31, v31\n\t"
    "s_nop 0\n\t"
    "v_add_f32_e32 v19, v19, v31\n\t"
    "v_exp_f32_e32 v32, v32\n\t"
    "s_nop 0\n\t"
    "v_add_f32_e32 v18, v18, v32\n\t"
    "v_fma_f32 v33, v33, s68, -v16\n\t"
    "v_fma_f32 v34, v34, s68, -v16\n\t"
    "v_fma_f32 v35, v35, s68, -v16\n\t"
    "v_exp_f32_e32 v33, v33\n\t"
    "s_nop 0\n\t"
    "v_add_f32_e32 v18, v18, v33\n\t"
    "v_exp_f32_e32 v34, v34\n\t"
    "s_nop 0\n\t"
    "v_add_f32_e32 v19, v19, v34\n\t"
    "v_exp_f32_e32 v35, v35\n\t"
    "s_nop 0\n\t"
    "v_add_f32_e32 v19, v19, v35\n\t"
    "v_add_f32_e32 v18, v18, v19\n\t"
    "v_mov_b32_e32 v19, v18\n\t"
    "s_nop 1\n\t"
    "v_permlane32_swap_b32 v18, v19\n\t"
    "v_add_f32_e32 v18, v18, v19\n\t"
    "v_mov_b32_e32 v19, v18\n\t"
    "s_nop 1\n\t"
    "v_permlane16_swap_b32 v18, v19\n\t"
    "v_add_f32_e32 v18, v18, v19\n\t"
    "v_rcp_f32_e32 v18, v18\n\t"
    "s_nop 0\n\t"
    "v_mul_f32_e32 v20, v20, v18\n\t"
    "v_mul_f32_e32 v21, v21, v18\n\t"
    "v_cvt_pk_bf16_f32 v12, v20, v21\n\t"
    "v_mul_f32_e32 v22, v22, v18\n\t"
    "v_mul_f32_e32 v23, v23, v18\n\t"
    "v_cvt_pk_bf16_f32 v13, v22, v23\n\t"
    "v_mul_f32_e32 v24, v24, v18\n\t"
    "v_mul_f32_e32 v25, v25, v18\n\t"
    "v_cvt_pk_bf16_f32 v14, v24, v25\n\t"
    "v_mul_f32_e32 v26, v26, v18\n\t"
    "v_mul_f32_e32 v27, v27, v18\n\t"
    "v_cvt_pk_bf16_f32 v15, v26, v27\n\t"
    "v_mul_f32_e32 v28, v28, v18\n\t"
    "v_mul_f32_e32 v29, v29, v18\n\t"
    "v_cvt_pk_bf16_f32 v20, v28, v29\n\t"
    "v_mul_f32_e32 v30, v30, v18\n\t"
    "v_mul_f32_e32 v31, v31, v18\n\t"
    "v_cvt_pk_bf16_f32 v21, v30, v31\n\t"
    "v_mul_f32_e32 v32, v32, v18\n\t"
    "v_mul_f32_e32 v33, v33, v18\n\t"
    "v_cvt_pk_bf16_f32 v22, v32, v33\n\t"
    "v_mul_f32_e32 v34, v34, v18\n\t"
    "v_mul_f32_e32 v35, v35, v18\n\t"
    "v_cvt_pk_bf16_f32 v23, v34, v35\n\t"
    "ds_write_b128 v6, v[12:15] offset:6144\n\t"
    "ds_write_b128 v6, v[20:23] offset:7168\n\t"
    "s_waitcnt lgkmcnt(0)\n\t"
    "s_barrier\n\t"
    "s_cmp_lg_u32 %[closer], 0\n\t"
    "s_cbranch_scc1 .Lx4_%=\n\t"
    "s_mov_b32 s60, 0x10000\n\t"
    "buffer_load_dwordx2 v[10:11], off, s[56:59], %[eoff] sc1\n\t"
    ".Lp5_%=:\n\t"
    "buffer_load_dwordx4 v[12:15], v8, s[56:59], %[aoff] offen sc1\n\t"
    "buffer_load_dwordx4 v[16:19], v8, s[56:59], %[aoff] offen offset:16 sc1\n\t"
    "s_waitcnt vmcnt(0)\n\t"
    "v_add_co_u32_e32 v20, vcc, 0x7f4a7c15, v10\n\t"
    "s_nop 1\n\t"
    "v_mov_b32_e32 v9, 0x9e3779b9\n\t"
    "v_addc_co_u32_e32 v21, vcc, v9, v11, vcc\n\t"
    "v_cmp_eq_u64_e32 vcc, v[12:13], v[20:21]\n\t"
    "v_cmp_eq_u64_e64 s[66:67], v[14:15], v[20:21]\n\t"
    "s_and_b64 vcc, vcc, s[66:67]\n\t"
    "v_cmp_eq_u64_e64 s[66:67], v[16:17], v[20:21]\n\t"
    "s_and_b64 vcc, vcc, s[66:67]\n\t"
    "v_cmp_eq_u64_e64 s[66:67], v[18:19], v[20:21]\n\t"
    "s_and_b64 vcc, vcc, s[66:67]\n\t"
    "s_cmp_eq_u64 vcc, exec\n\t"
    "s_cbranch_scc1 .Lk6_%=\n\t"
    "s_sleep 8\n\t"
    "s_sub_u32 s60, s60, 1\n\t"
    "s_cmp_lg_u32 s60, 0\n\t"
    "s_cbranch_scc1 .Lp5_%=\n\t"
    ".Lk6_%=:\n\t"
    "s_mov_b64 exec, 1\n\t"
    "buffer_store_dwordx2 v[20:21], off, s[56:59], %[eoff] sc1\n\t"
    "s_mov_b64 exec, -1\n\t"
    ".Lx4_%=:\n\t"
    "s_endpgm\n\t"
    :: "{v24}"(vk0), "{v25}"(vk1), "{v26}"(lane * 128), "{v31}"(lane), "{s[36:43]}"(k8), "{s[44:47]}"(k4),
       [role] "s"(role), [prod] "s"((wave * 256 + bid - 72) >> 31), [closer] "s"(bid | wave), [bid] "s"(bid), [wb] "s"(wb),
       [k0] "s"(k0), [k1] "s"(k1), [k2] "s"(k0 + 32 * 1152), [tch] "s"(wid), [kl] "s"(ldsb + role * 110592 + pair * 18432),
       [rbeg] "s"(rbeg), [rd] "s"(rd), [rend] "s"(rbeg + 3 * rd),
       [tb] "s"(ldsb + PB + wave * 4096), [pl] "s"(ldsb + PB + pair * 8192), [wn] "s"(ldsb + WQ + wave * 3072),
       [h1l] "s"(rbeg + rd + pair * 4 * 3072), [h1g] "s"(wb + STEPB + pair * 4 * ROWB),
       [h2l] "s"(rbeg + rd + (1 - pair) * 4 * 3072), [h2g] "s"(wb + STEPB + (1 - pair) * 4 * ROWB),
       [wql] "s"(ldsb + WQ + pair * 1024), [wqg] "s"(pair * 4 * 1152), [amask] "s"((unsigned long long)(1 - pair)),
       [noff] "s"(N_OFF), [ntch] "s"(N_OFF + ((bid >> 3) * 2 + pair) * 1152), [foff] "s"(F_OFF + 8 * bid), [aoff] "s"(A_OFF), [aown] "s"(A_OFF + 8 * bid), [eoff] "s"(E_OFF)
    : "memory", "scc", ACLOB, VCLOB, SCLOB);
}

extern "C" void kernel_launch(void* const* d_in, const int* in_sizes, int n_in,
                              void* d_out, int out_size, void* d_ws, size_t ws_size,
                              hipStream_t stream) {
  attn_kernel<<<256, 256, 0, stream>>>((const unsigned char*)d_in[0], (const unsigned char*)d_in[1], (const unsigned char*)d_in[2],
                                       (unsigned char*)d_ws, (unsigned char*)d_out
      );
}
```

```cpp
#define XMAP
#include <hip/hip_runtime.h>
#include <hip/hip_bf16.h>
#include <stdint.h>

using u32x4 = __attribute__((ext_vector_type(4))) unsigned;
using u32x8 = __attribute__((ext_vector_type(8))) unsigned;
#define DI __device__ __forceinline__

static constexpr int ROWB = 512 * 384;
static constexpr int STEPB = 64 * ROWB;
static constexpr unsigned XBYTES = 512u * ROWB;
static constexpr int SLOT = 24576;
static constexpr int WQ = 36864;
static constexpr int PB = 6 * SLOT;
static constexpr int N_OFF = 0, F_OFF = 80 * 1024, A_OFF = 84 * 1024, E_OFF = 88 * 1024;
static constexpr unsigned WS_BYTES = 96 * 1024;

DI unsigned chan(unsigned b, unsigned p) { return 8 * (p >> 2) + 4 * b + (p & 3); }

#define R10(p, n) p #n "0", p #n "1", p #n "2", p #n "3", p #n "4", p #n "5", p #n "6", p #n "7", p #n "8", p #n "9"
#define R240(p) R10(p, 4), R10(p, 5), R10(p, 6), R10(p, 7), R10(p, 8), R10(p, 9), R10(p, 10), R10(p, 11), R10(p, 12), R10(p, 13), \
  R10(p, 14), R10(p, 15), R10(p, 16), R10(p, 17), R10(p, 18), R10(p, 19), R10(p, 20), R10(p, 21), R10(p, 22), R10(p, 23), R10(p, 24), \
  p "250", p "251", p "252", p "253", p "254", p "255"
#define ACLOB R10("a", ), R10("a", 1), R10("a", 2), R10("a", 3), R240("a")
#define VCLOB R10("v", ), R10("v", 1), "v20", "v21", "v22", "v23", "v27", "v28", "v29", "v30", "v32", "v33", "v34", "v35", "v36", "v37", "v38", "v39", R240("v")
#define SCLOB "s48", "s49", R10("s", 5), R10("s", 6), R10("s", 7), R10("s", 8), "s90", "s91", "s92", "s93", "s94", "s95", "vcc"

__global__ __launch_bounds__(256, 1) void attn_kernel(const unsigned char* __restrict__ x,
                                                      const unsigned char* __restrict__ wqkv,
                                                      const unsigned char* __restrict__ wo,
                                                      unsigned char* ws,
                                                      unsigned char* __restrict__ out
                                                      ) {
  __shared__ u32x4 lds[10240];
  u32x8 k8; u32x4 k4;
  asm volatile(
    "s_load_dwordx8 s[36:43], %2, 0\n\t"
    "s_load_dwordx2 s[44:45], %2, 32\n\t"
    : "={s[36:43]}"(k8), "={s[44:47]}"(k4) : "s"(__builtin_amdgcn_kernarg_segment_ptr()) : "memory", ACLOB);
  __builtin_amdgcn_sched_barrier(0);
  const unsigned lane = threadIdx.x & 63, wave = __builtin_amdgcn_readfirstlane(threadIdx.x >> 6);
  const unsigned role = wave >> 1, pair = wave & 1;
  const unsigned r3 = lane >> 3, ch = lane & 7;
  const unsigned ldsb = (unsigned)(size_t)lds;
  const unsigned bid = blockIdx.x;
  const unsigned vk0 = chan(0, r3) * 1152 + (ch ^ (r3 >> 1)) * 16, vk1 = (vk0 ^ 64) + 16 * 1152;
  const unsigned c0 = 3 * wave, c1 = c0 + 1;
  const unsigned k0 = (32 * (c0 >> 1) + 4 * (c0 & 1)) * 1152 + 384, k1 = (32 * (c1 >> 1) + 4 * (c1 & 1)) * 1152 + 384;
  const unsigned wid = (bid >> 3) * 4 + wave;
  const unsigned w0 = (bid & 7) * 64 + (bid >> 3) * 2 + pair;
  const unsigned wb = (w0 >> 6) * 8 * ROWB + (w0 & 63) * 8 * 384;
  const unsigned rbeg = ldsb + pair * 5 * SLOT, rd = SLOT - pair * 2 * SLOT;

  asm volatile(
    ".Lbeg_%=:\n\t"
    "s_cmp_lg_u32 %[role], 0\n\t"
    "s_cbranch_scc1 .Lroln_%=\n\t"
    "s_cmp_eq_u32 %[prod], 0\n\t"
    "s_cbranch_scc1 .Lmo_%=\n\t"
    "s_mul_i32 s65, %[bid], 0x1556\n\t"
    "s_lshr_b32 s65, s65, 16\n\t"
    "s_mul_i32 s69, s65, 12\n\t"
    "s_sub_u32 s69, %[bid], s69\n\t"
    "s_lshr_b32 s69, s69, 1\n\t"
    "v_lshrrev_b32_e32 v27, 2, v31\n\t"
    "v_mul_u32_u24_e32 v27, 0x180, v27\n\t"
    "v_and_b32_e32 v28, 3, v31\n\t"
    "v_lshl_or_b32 v27, v28, 4, v27\n\t"
    "s_mov_b32 m0, 86016\n\t"
    "s_mov_b32 s50, 0x36000\n\t"
    "s_mov_b32 s51, 0x20000\n\t"
    "s_mov_b32 s54, 0x12000\n\t"
    "s_mov_b32 s55, 0x20000\n\t"
    "s_mov_b32 s58, 0x18000\n\t"
    "s_mov_b32 s59, 0x20000\n\t"
    "s_mov_b32 s63, 0x20000\n\t"
    "s_cmp_lt_u32 %[tch], 96\n\t"
    "s_cselect_b32 s62, s50, s54\n\t"
    "s_cselect_b32 s64, 0, 96\n\t"
    "s_sub_u32 s64, %[tch], s64\n\t"
    "s_mul_i32 s64, s64, 2304\n\t"
    "s_cmp_lt_u32 %[tch], 96\n\t"
    "s_waitcnt lgkmcnt(0)\n\t"
    "s_cselect_b64 s[60:61], s[38:39], s[40:41]\n\t"
    "s_and_b32 s61, s61, 0xffff\n\t"
    "s_mov_b32 s48, s38\n\t"
    "s_and_b32 s49, s39, 0xffff\n\t"
    "s_mov_b64 exec, 0x3ffff\n\t"
    "buffer_load_dword a255, v26, s[60:63], s64 offen\n\t"
    "s_mov_b64 exec, -1\n\t"
    "s_mov_b32 s52, s40\n\t"
    "s_and_b32 s53, s41, 0xffff\n\t"
    "s_lshl_b32 s60, s65, 6\n\t"
    "s_mul_i32 s61, s69, 36864\n\t"
    "buffer_load_dwordx4 v27, s[52:55], s60 offen lds\n\t"
    "s_mov_b32 m0, 88064\n\t"
    "s_add_u32 s60, s60, 6144\n\t"
    "buffer_load_dwordx4 v27, s[52:55], s60 offen lds\n\t"
    "s_mov_b32 m0, 90112\n\t"
    "s_add_u32 s60, s60, 6144\n\t"
    "buffer_load_dwordx4 v27, s[52:55], s60 offen lds\n\t"
    "s_mov_b32 m0, 92160\n\t"
    "s_add_u32 s60, s60, 6144\n\t"
    "buffer_load_dwordx4 v27, s[52:55], s60 offen lds\n\t"
    "s_mov_b32 m0, 94208\n\t"
    "s_add_u32 s60, s60, 6144\n\t"
    "buffer_load_dwordx4 v27, s[52:55], s60 offen lds\n\t"
    "s_mov_b32 m0, 96256\n\t"
    "s_add_u32 s60, s60, 6144\n\t"
    "buffer_load_dwordx4 v27, s[52:55], s60 offen lds\n\t"
    "s_mov_b32 m0, 98304\n\t"
    "s_add_u32 s60, s60, 6144\n\t"
    "buffer_load_dwordx4 v27, s[52:55], s60 offen lds\n\t"
    "s_mov_b32 m0, 100352\n\t"
    "s_add_u32 s60, s60, 6144\n\t"
    "buffer_load_dwordx4 v27, s[52:55], s60 offen lds\n\t"
    "s_mov_b32 m0, 102400\n\t"
    "s_add_u32 s60, s60, 6144\n\t"
    "buffer_load_dwordx4 v27, s[52:55], s60 offen lds\n\t"
    "s_mov_b32 m0, 104448\n\t"
    "s_add_u32 s60, s60, 6144\n\t"
    "buffer_load_dwordx4 v27, s[52:55], s60 offen lds\n\t"
    "s_mov_b32 m0, 106496\n\t"
    "s_add_u32 s60, s60, 6144\n\t"
    "buffer_load_dwordx4 v27, s[52:55], s60 offen lds\n\t"
    "s_mov_b32 m0, 108544\n\t"
    "s_add_u32 s60, s60, 6144\n\t"
    "buffer_load_dwordx4 v27, s[52:55], s60 offen lds\n\t"
    "s_add_u32 s60, s61, 768\n\t"
    "s_add_u32 s61, s61, 5376\n\t"
    "s_mov_b32 m0, 147456\n\t"
    "s_nop 0\n\t"
    "buffer_load_dwordx4 v24, s[48:51], s60 offen offset:0 lds\n\t"
    "s_mov_b32 m0, 151424\n\t"
    "s_nop 0\n\t"
    "buffer_load_dwordx4 v24, s[48:51], s60 offen offset:128 lds\n\t"
    "s_mov_b32 m0, 155392\n\t"
    "s_nop 0\n\t"
    "buffer_load_dwordx4 v24, s[48:51], s60 offen offset:256 lds\n\t"
    "s_mov_b32 m0, 149504\n\t"
    "s_nop 0\n\t"
    "buffer_load_dwordx4 v25, s[48:51], s60 offen offset:0 lds\n\t"
    "s_mov_b32 m0, 153472\n\t"
    "s_nop 0\n\t"
    "buffer_load_dwordx4 v25, s[48:51], s60 offen offset:128 lds\n\t"
    "s_mov_b32 m0, 157440\n\t"
    "s_nop 0\n\t"
    "buffer_load_dwordx4 v25, s[48:51], s60 offen offset:256 lds\n\t"
    "s_mov_b32 m0, 148480\n\t"
    "s_nop 0\n\t"
    "buffer_load_dwordx4 v24, s[48:51], s61 offen offset:0 lds\n\t"
    "s_mov_b32 m0, 152448\n\t"
    "s_nop 0\n\t"
    "buffer_load_dwordx4 v24, s[48:51], s61 offen offset:128 lds\n\t"
    "s_mov_b32 m0, 156416\n\t"
    "s_nop 0\n\t"
    "buffer_load_dwordx4 v24, s[48:51], s61 offen offset:256 lds\n\t"
    "s_mov_b32 m0, 150528\n\t"
    "s_nop 0\n\t"
    "buffer_load_dwordx4 v25, s[48:51], s61 offen offset:0 lds\n\t"
    "s_mov_b32 m0, 154496\n\t"
    "s_nop 0\n\t"
    "buffer_load_dwordx4 v25, s[48:51], s61 offen offset:128 lds\n\t"
    "s_mov_b32 m0, 158464\n\t"
    "s_nop 0\n\t"
    "buffer_load_dwordx4 v25, s[48:51], s61 offen offset:256 lds\n\t"
    "s_mov_b32 s56, s42\n\t"
    "s_and_b32 s57, s43, 0xffff\n\t"
    "buffer_load_dwordx2 v[22:23], off, s[56:59], %[eoff] sc1\n\t"
    "s_barrier\n\t"
    "s_add_u32 m0, %[kl], 0\n\t"
    "s_nop 0\n\t"
    "buffer_load_dwordx4 v24, s[48:51], %[k0] offen offset:0 lds\n\t"
    "s_add_u32 m0, %[kl], 1024\n\t"
    "s_nop 0\n\t"
    "buffer_load_dwordx4 v25, s[48:51], %[k0] offen offset:0 lds\n\t"
    "s_add_u32 m0, %[kl], 1920\n\t"
    "s_nop 0\n\t"
    "buffer_load_dwordx4 v24, s[48:51], %[k0] offen offset:128 lds\n\t"
    "s_add_u32 m0, %[kl], 2944\n\t"
    "s_nop 0\n\t"
    "buffer_load_dwordx4 v25, s[48:51], %[k0] offen offset:128 lds\n\t"
    "s_add_u32 m0, %[kl], 3840\n\t"
    "s_nop 0\n\t"
    "buffer_load_dwordx4 v24, s[48:51], %[k0] offen offset:256 lds\n\t"
    "s_add_u32 m0, %[kl], 4864\n\t"
    "s_nop 0\n\t"
    "buffer_load_dwordx4 v25, s[48:51], %[k0] offen offset:256 lds\n\t"
    "s_add_u32 m0, %[kl], 6144\n\t"
    "s_nop 0\n\t"
    "buffer_load_dwordx4 v24, s[48:51], %[k1] offen offset:0 lds\n\t"
    "s_add_u32 m0, %[kl], 7168\n\t"
    "s_nop 0\n\t"
    "buffer_load_dwordx4 v25, s[48:51], %[k1] offen offset:0 lds\n\t"
    "s_add_u32 m0, %[kl], 8064\n\t"
    "s_nop 0\n\t"
    "buffer_load_dwordx4 v24, s[48:51], %[k1] offen offset:128 lds\n\t"
    "s_branch .Lmc_%=\n\t"
    ".Lmo_%=:\n\t"
    "s_mov_b32 s50, 0x36000\n\t"
    "s_mov_b32 s51, 0x20000\n\t"
    "s_mov_b32 s54, 0x12000\n\t"
    "s_mov_b32 s55, 0x20000\n\t"
    "s_mov_b32 s58, 0x18000\n\t"
    "s_mov_b32 s59, 0x20000\n\t"
    "s_mov_b32 s63, 0x20000\n\t"
    "s_cmp_lt_u32 %[tch], 96\n\t"
    "s_cselect_b32 s62, s50, s54\n\t"
    "s_cselect_b32 s64, 0, 96\n\t"
    "s_sub_u32 s64, %[tch], s64\n\t"
    "s_mul_i32 s64, s64, 2304\n\t"
    "s_cmp_lt_u32 %[tch], 96\n\t"
    "s_waitcnt lgkmcnt(0)\n\t"
    "s_cselect_b64 s[60:61], s[38:39], s[40:41]\n\t"
    "s_and_b32 s61, s61, 0xffff\n\t"
    "s_mov_b32 s48, s38\n\t"
    "s_and_b32 s49, s39, 0xffff\n\t"
    "s_mov_b64 exec, 0x3ffff\n\t"
    "buffer_load_dword a255, v26, s[60:63], s64 offen\n\t"
    "s_mov_b64 exec, -1\n\t"
    "s_add_u32 m0, %[kl], 0\n\t"
    "s_nop 0\n\t"
    "buffer_load_dwordx4 v24, s[48:51], %[k0] offen offset:0 lds\n\t"
    "s_add_u32 m0, %[kl], 1024\n\t"
    "s_nop 0\n\t"
    "buffer_load_dwordx4 v25, s[48:51], %[k0] offen offset:0 lds\n\t"
    "s_add_u32 m0, %[kl], 1920\n\t"
    "s_nop 0\n\t"
    "buffer_load_dwordx4 v24, s[48:51], %[k0] offen offset:128 lds\n\t"
    "s_add_u32 m0, %[kl], 2944\n\t"
    "s_nop 0\n\t"
    "buffer_load_dwordx4 v25, s[48:51], %[k0] offen offset:128 lds\n\t"
    "s_add_u32 m0, %[kl], 3840\n\t"
    "s_nop 0\n\t"
    "buffer_load_dwordx4 v24, s[48:51], %[k0] offen offset:256 lds\n\t"
    "s_add_u32 m0, %[kl], 4864\n\t"
    "s_nop 0\n\t"
    "buffer_load_dwordx4 v25, s[48:51], %[k0] offen offset:256 lds\n\t"
    "s_add_u32 m0, %[kl], 6144\n\t"
    "s_nop 0\n\t"
    "buffer_load_dwordx4 v24, s[48:51], %[k1] offen offset:0 lds\n\t"
    "s_add_u32 m0, %[kl], 7168\n\t"
    "s_nop 0\n\t"
    "buffer_load_dwordx4 v25, s[48:51], %[k1] offen offset:0 lds\n\t"
    "s_add_u32 m0, %[kl], 8064\n\t"
    "s_nop 0\n\t"
    "buffer_load_dwordx4 v24, s[48:51], %[k1] offen offset:128 lds\n\t"
    "s_mov_b32 s56, s42\n\t"
    "s_and_b32 s57, s43, 0xffff\n\t"
    "s_barrier \n\t"
    ".Lmc_%=:\n\t"
    "s_mov_b64 exec, 0\n\t"
    "s_cmp_eq_u32 %[wqg], 0\n\t"
    "s_cbranch_scc1 .Lh1_%=\n\t"
    "s_branch .Lh2_%=\n\t"
    ".LrM_%=:\n\t"
    "s_mov_b64 exec, -1\n\t"
    "v_lshlrev_b32_e32 v30, 4, v31\n\t"
    "v_and_b32_e32 v28, 7, v31\n\t"
    "v_bfe_u32 v27, v31, 3, 1\n\t"
    "v_lshrrev_b32_e32 v29, 4, v31\n\t"
    "v_bfe_u32 v20, v31, 1, 3\n\t"
    "v_xor_b32_e32 v20, v20, v29\n\t"
    "v_lshlrev_b32_e32 v21, 7, v28\n\t"
    "v_lshl_or_b32 v21, v27, 11, v21\n\t"
    "v_lshl_or_b32 v21, v20, 4, v21\n\t"
    "v_add_u32_e32 v11, %[tb], v30\n\t"
    "v_lshlrev_b32_e32 v9, 10, v27\n\t"
    "v_sub_u32_e32 v9, v21, v9\n\t"
    "v_add_u32_e32 v9, %[kl], v9\n\t"
    "v_xor_b32_e32 v10, 64, v9\n\t"
    "v_add_u32_e32 v12, 0x9000, v21\n\t"
    "v_xor_b32_e32 v13, 64, v12\n\t"
    "v_add_u32_e32 v14, %[wn], v30\n\t"
    "v_add_u32_e32 v6, %[pl], v30\n\t"
    "v_mul_u32_u24_e32 v27, 0xc00, v27\n\t"
    "v_add_u32_e32 v27, %[rbeg], v27\n\t"
    "v_mad_u32_u24 v20, v28, 3, 0\n\t"
    "v_and_b32_e32 v21, 6, v20\n\t"
    "v_xor_b32_e32 v21, v21, v29\n\t"
    "v_lshl_add_u32 v20, v20, 7, v27\n\t"
    "v_lshl_add_u32 v0, v21, 4, v20\n\t"
    "v_xor_b32_e32 v1, 64, v0\n\t"
    "v_mad_u32_u24 v20, v28, 3, 1\n\t"
    "v_and_b32_e32 v21, 6, v20\n\t"
    "v_xor_b32_e32 v21, v21, v29\n\t"
    "v_lshl_add_u32 v20, v20, 7, v27\n\t"
    "v_lshl_add_u32 v2, v21, 4, v20\n\t"
    "v_xor_b32_e32 v3, 64, v2\n\t"
    "v_mad_u32_u24 v20, v28, 3, 2\n\t"
    "v_and_b32_e32 v21, 6, v20\n\t"
    "v_xor_b32_e32 v21, v21, v29\n\t"
    "v_lshl_add_u32 v20, v20, 7, v27\n\t"
    "v_lshl_add_u32 v4, v21, 4, v20\n\t"
    "v_xor_b32_e32 v5, 64, v4\n\t"
    "s_add_u32 m0, %[kl], 9088\n\t"
    "s_nop 0\n\t"
    "buffer_load_dwordx4 v25, s[48:51], %[k1] offen offset:128 lds\n\t"
    "s_add_u32 m0, %[kl], 9984\n\t"
    "s_nop 0\n\t"
    "buffer_load_dwordx4 v24, s[48:51], %[k1] offen offset:256 lds\n\t"
    "s_add_u32 m0, %[kl], 11008\n\t"
    "s_nop 0\n\t"
    "buffer_load_dwordx4 v25, s[48:51], %[k1] offen offset:256 lds\n\t"
    "s_add_u32 m0, %[kl], 12288\n\t"
    "s_nop 0\n\t"
    "buffer_load_dwordx4 v24, s[48:51], %[k2] offen offset:0 lds\n\t"
    "s_add_u32 m0, %[kl], 13312\n\t"
    "s_nop 0\n\t"
    "buffer_load_dwordx4 v25, s[48:51], %[k2] offen offset:0 lds\n\t"
    "s_add_u32 m0, %[kl], 14208\n\t"
    "s_nop 0\n\t"
    "buffer_load_dwordx4 v24, s[48:51], %[k2] offen offset:128 lds\n\t"
    "s_add_u32 m0, %[kl], 15232\n\t"
    "s_nop 0\n\t"
    "buffer_load_dwordx4 v25, s[48:51], %[k2] offen offset:128 lds\n\t"
    "s_add_u32 m0, %[kl], 16128\n\t"
    "s_nop 0\n\t"
    "buffer_load_dwordx4 v24, s[48:51], %[k2] offen offset:256 lds\n\t"
    "s_add_u32 m0, %[kl], 17152\n\t"
    "s_nop 0\n\t"
    "buffer_load_dwordx4 v25, s[48:51], %[k2] offen offset:256 lds\n\t"
    "s_cmp_eq_u32 %[prod], 0\n\t"
    "s_cbranch_scc1 .Lmd_%=\n\t"
    "s_and_b32 s62, %[bid], 1\n\t"
    "s_and_b32 s63, s65, 1\n\t"
    "s_lshl_b32 s63, s63, 1\n\t"
    "s_add_u32 s63, s63, s62\n\t"
    "s_lshl_b32 s64, s62, 2\n\t"
    "s_lshl_b32 s62, s62, 4\n\t"
    "s_cmp_lt_u32 s65, 4\n\t"
    "s_cselect_b32 s60, s64, 8\n\t"
    "s_cselect_b32 s63, s65, s63\n\t"
    "s_cselect_b32 s62, s64, s62\n\t"
    "s_cselect_b32 s61, 4, 3\n\t"
    "s_lshl_b32 s62, s62, 1\n\t"
    "s_mul_i32 s60, s60, 6\n\t"
    "s_add_u32 s60, s60, s69\n\t"
    "s_lshl_b32 s60, s60, 2\n\t"
    "s_add_u32 s60, s60, s63\n\t"
    "s_lshl_b32 s60, s60, 8\n\t"
    "v_and_b32_e32 v16, 3, v31\n\t"
    "v_bfe_u32 v17, v31, 2, 2\n\t"
    "v_lshlrev_b32_e32 v18, s61, v16\n\t"
    "v_lshl_add_u32 v18, v17, 6, v18\n\t"
    "v_and_b32_e32 v19, 1, v29\n\t"
    "v_lshl_add_u32 v18, v19, 9, v18\n\t"
    "v_lshrrev_b32_e32 v19, 1, v29\n\t"
    "v_lshl_add_u32 v18, v19, 11, v18\n\t"
    "s_add_u32 s62, s62, 86016\n\t"
    "v_add_u32_e32 v18, s62, v18\n\t"
    "v_lshl_add_u32 v19, v16, 2, v17\n\t"
    "v_lshl_add_u32 v19, v29, 4, v19\n\t"
    "v_lshlrev_b32_e32 v19, 4, v19\n\t"
    "v_add_u32_e32 v19, %[tb], v19\n\t"
    "v_mul_u32_u24_e32 v17, 0x1800, v17\n\t"
    "v_lshl_add_u32 v20, v16, 4, v17\n\t"
    "v_lshl_add_u32 v20, v29, 6, v20\n\t"
    "v_add_u32_e32 v20, s60, v20\n\t"
    "v_add_u32_e32 v16, 0x1b000, v12\n\t"
    "v_add_u32_e32 v17, 0x1b000, v13\n\t"
    "s_waitcnt vmcnt(31)\n\t"
    "ds_read_b64_tr_b16 v[80:81], v18 offset:0\n\t"
    "ds_read_b64_tr_b16 v[82:83], v18 offset:256\n\t"
    "ds_read_b64_tr_b16 v[84:85], v18 offset:4096\n\t"
    "ds_read_b64_tr_b16 v[86:87], v18 offset:4352\n\t"
    "ds_read_b64_tr_b16 v[88:89], v18 offset:8192\n\t"
    "ds_read_b64_tr_b16 v[90:91], v18 offset:8448\n\t"
    "ds_read_b64_tr_b16 v[92:93], v18 offset:12288\n\t"
    "ds_read_b64_tr_b16 v[94:95], v18 offset:12544\n\t"
    "ds_read_b64_tr_b16 v[96:97], v18 offset:16384\n\t"
    "ds_read_b64_tr_b16 v[98:99], v18 offset:16640\n\t"
    "ds_read_b64_tr_b16 v[100:101], v18 offset:20480\n\t"
    "ds_read_b64_tr_b16 v[102:103], v18 offset:20736\n\t"
    "s_waitcnt vmcnt(19)\n\t"
    "ds_read_b128 v[32:35], v16 offset:0\n\t"
    "ds_read_b128 v[36:39], v17 offset:0\n\t"
    "ds_read_b128 v[40:43], v16 offset:4096\n\t"
    "ds_read_b128 v[44:47], v17 offset:4096\n\t"
    "ds_read_b128 v[48:51], v16 offset:8192\n\t"
    "ds_read_b128 v[52:55], v17 offset:8192\n\t"
    "ds_read_b128 v[56:59], v16 offset:1024\n\t"
    "ds_read_b128 v[60:63], v17 offset:1024\n\t"
    "ds_read_b128 v[64:67], v16 offset:5120\n\t"
    "ds_read_b128 v[68:71], v17 offset:5120\n\t"
    "ds_read_b128 v[72:75], v16 offset:9216\n\t"
    "ds_read_b128 v[76:79], v17 offset:9216\n\t"
    "s_waitcnt lgkmcnt(11)\n\t"
    "v_mfma_f32_16x16x32_bf16 v[108:111], v[32:35], v[80:83], 0\n\t"
    "s_waitcnt lgkmcnt(10)\n\t"
    "v_mfma_f32_16x16x32_bf16 v[108:111], v[36:39], v[84:87], v[108:111]\n\t"
    "s_waitcnt lgkmcnt(9)\n\t"
    "v_mfma_f32_16x16x32_bf16 v[108:111], v[40:43], v[88:91], v[108:111]\n\t"
    "s_waitcnt lgkmcnt(8)\n\t"
    "v_mfma_f32_16x16x32_bf16 v[108:111], v[44:47], v[92:95], v[108:111]\n\t"
    "s_waitcnt lgkmcnt(7)\n\t"
    "v_mfma_f32_16x16x32_bf16 v[108:111], v[48:51], v[96:99], v[108:111]\n\t"
    "s_waitcnt lgkmcnt(6)\n\t"
    "v_mfma_f32_16x16x32_bf16 v[108:111], v[52:55], v[100:103], v[108:111]\n\t"
    "s_waitcnt lgkmcnt(5)\n\t"
    "v_mfma_f32_16x16x32_bf16 v[112:115], v[56:59], v[80:83], 0\n\t"
    "s_waitcnt lgkmcnt(4)\n\t"
    "v_mfma_f32_16x16x32_bf16 v[112:115], v[60:63], v[84:87], v[112:115]\n\t"
    "s_waitcnt lgkmcnt(3)\n\t"
    "v_mfma_f32_16x16x32_bf16 v[112:115], v[64:67], v[88:91], v[112:115]\n\t"
    "s_waitcnt lgkmcnt(2)\n\t"
    "v_mfma_f32_16x16x32_bf16 v[112:115], v[68:71], v[92:95], v[112:115]\n\t"
    "s_waitcnt lgkmcnt(1)\n\t"
    "v_mfma_f32_16x16x32_bf16 v[112:115], v[72:75], v[96:99], v[112:115]\n\t"
    "s_waitcnt lgkmcnt(0)\n\t"
    "v_mfma_f32_16x16x32_bf16 v[112:115], v[76:79], v[100:103], v[112:115]\n\t"
    "v_cvt_pk_bf16_f32 v104, v108, v109\n\t"
    "v_cvt_pk_bf16_f32 v105, v110, v111\n\t"
    "s_nop 6\n\t"
    "v_cvt_pk_bf16_f32 v106, v112, v113\n\t"
    "v_cvt_pk_bf16_f32 v107, v114, v115\n\t"
    "ds_write_b128 v19, v[104:107]\n\t"
    "ds_read_b128 v[104:107], v11\n\t"
    "s_waitcnt lgkmcnt(0)\n\t"
    "buffer_store_dwordx4 v[104:107], v20, s[56:59], 0 offen sc1\n\t"
    ".Lmd_%=:\n\t"
    "s_add_u32 m0, %[wql], 49152\n\t"
    "s_add_u32 s61, %[wqg], 147456\n\t"
    "buffer_load_dwordx4 v24, s[48:51], s61 offen offset:0 lds\n\t"
    "s_add_u32 m0, %[wql], 53120\n\t"
    "v_lshrrev_b32_e32 v20, 3, v31\n\t"
    "buffer_load_dwordx4 v24, s[48:51], s61 offen offset:128 lds\n\t"
    "s_add_u32 m0, %[wql], 57088\n\t"
    "v_and_b32_e32 v21, 6, v20\n\t"
    "buffer_load_dwordx4 v24, s[48:51], s61 offen offset:256 lds\n\t"
    "s_add_u32 m0, %[wql], 51200\n\t"
    "v_xor_b32_e32 v21, v21, v28\n\t"
    "buffer_load_dwordx4 v25, s[48:51], s61 offen offset:0 lds\n\t"
    "s_add_u32 m0, %[wql], 55168\n\t"
    "v_lshlrev_b32_e32 v20, 7, v20\n\t"
    "buffer_load_dwordx4 v25, s[48:51], s61 offen offset:128 lds\n\t"
    "s_add_u32 m0, %[wql], 59136\n\t"
    "v_lshl_or_b32 v7, v21, 4, v20\n\t"
    "buffer_load_dwordx4 v25, s[48:51], s61 offen offset:256 lds\n\t"
    "s_add_u32 m0, %[wql], 61440\n\t"
    "s_add_u32 s61, %[wqg], 184320\n\t"
    "buffer_load_dwordx4 v24, s[48:51], s61 offen offset:0 lds\n\t"
    "s_add_u32 m0, %[wql], 65408\n\t"
    "v_add_u32_e32 v15, 0x9000, v30\n\t"
    "buffer_load_dwordx4 v24, s[48:51], s61 offen offset:128 lds\n\t"
    "s_add_u32 m0, %[wql], 69376\n\t"
    "s_mov_b32 s68, 0x3dd53b95\n\t"
    "buffer_load_dwordx4 v24, s[48:51], s61 offen offset:256 lds\n\t"
    "s_add_u32 m0, %[wql], 63488\n\t"
    "s_mov_b32 s52, s36\n\t"
    "buffer_load_dwordx4 v25, s[48:51], s61 offen offset:0 lds\n\t"
    "s_add_u32 m0, %[wql], 67456\n\t"
    "s_and_b32 s53, s37, 0xffff\n\t"
    "buffer_load_dwordx4 v25, s[48:51], s61 offen offset:128 lds\n\t"
    "s_add_u32 m0, %[wql], 71424\n\t"
    "s_mov_b32 s54, 0x6000000\n\t"
    "buffer_load_dwordx4 v25, s[48:51], s61 offen offset:256 lds\n\t"
    "s_mov_b32 s55, 0x20000\n\t"
    "s_mov_b32 s67, 6\n\t"
    "s_waitcnt vmcnt(24)\n\t"
    "ds_read_b128 v[116:119], v9 offset:0\n\t"
    "ds_read_b128 v[120:123], v10 offset:0\n\t"
    "ds_read_b128 v[124:127], v9 offset:2048\n\t"
    "ds_read_b128 v[128:131], v10 offset:2048\n\t"
    "ds_read_b128 v[132:135], v9 offset:4096\n\t"
    "ds_read_b128 v[136:139], v10 offset:4096\n\t"
    "s_waitcnt vmcnt(18)\n\t"
    "ds_read_b128 v[140:143], v9 offset:6144\n\t"
    "ds_read_b128 v[144:147], v10 offset:6144\n\t"
    "ds_read_b128 v[148:151], v9 offset:8192\n\t"
    "ds_read_b128 v[152:155], v10 offset:8192\n\t"
    "ds_read_b128 v[156:159], v9 offset:10240\n\t"
    "ds_read_b128 v[160:163], v10 offset:10240\n\t"
    "s_cmp_eq_u32 %[prod], 0\n\t"
    "s_waitcnt vmcnt(13)\n\t"
    "s_cbranch_scc0 .Lt_%=\n\t"
    "s_waitcnt vmcnt(12)\n\t"
    ".Lt_%=:\n\t"
    "ds_read_b128 v[164:167], v9 offset:12288\n\t"
    "ds_read_b128 v[168:171], v10 offset:12288\n\t"
    "ds_read_b128 v[172:175], v9 offset:14336\n\t"
    "ds_read_b128 v[176:179], v10 offset:14336\n\t"
    "ds_read_b128 v[180:183], v9 offset:16384\n\t"
    "ds_read_b128 v[184:187], v10 offset:16384\n\t"
    "s_mov_b32 s66, %[rbeg]\n\t"
    "s_mov_b32 s61, %[wb]\n\t"
    "s_waitcnt lgkmcnt(0)\n\t"
    "s_barrier\n\t"
    "ds_read_b128 v[32:35], v12 offset:0\n\t"
    "ds_read_b128 v[36:39], v13 offset:0\n\t"
    "ds_read_b128 v[40:43], v12 offset:4096\n\t"
    "ds_read_b128 v[44:47], v13 offset:4096\n\t"
    "ds_read_b128 v[48:51], v12 offset:8192\n\t"
    "ds_read_b128 v[52:55], v13 offset:8192\n\t"
    "s_mov_b32 s60, 6\n\t"
    "s_branch .Lf1_%=\n\t"
    ".Lfs0_%=:\n\t"
    "s_waitcnt vmcnt(0)\n\t"
    "s_cmp_eq_u32 %[prod], 0\n\t"
    "s_cbranch_scc1 .Lfx_%=\n\t"
    "v_add_co_u32_e32 v22, vcc, 0x7f4a7c15, v22\n\t"
    "v_mov_b32_e32 v16, 0x9e3779b9\n\t"
    "s_nop 0\n\t"
    "v_addc_co_u32_e32 v23, vcc, v16, v23, vcc\n\t"
    "s_mov_b64 exec, 1\n\t"
    "buffer_store_dwordx2 v[22:23], off, s[56:59], %[foff] sc1\n\t"
    "s_mov_b64 exec, -1\n\t"
    ".Lfx_%=:\n\t"
    "s_mov_b32 m0, s66\n\t"
    "s_add_u32 s66, s66, 3072\n\t"
    "buffer_load_dwordx4 v7, s[52:55], s61 offen offset:0 nt lds\n\t"
    "buffer_load_dwordx4 v7, s[52:55], s61 offen offset:1024 nt lds\n\t"
    "buffer_load_dwordx4 v7, s[52:55], s61 offen offset:2048 nt lds\n\t"
    "s_add_u32 s61, s61, 196608\n\t"
    "s_mov_b32 m0, s66\n\t"
    "s_add_u32 s66, s66, 3072\n\t"
    "buffer_load_dwordx4 v7, s[52:55], s61 offen offset:0 nt lds\n\t"
    "buffer_load_dwordx4 v7, s[52:55], s61 offen offset:1024 nt lds\n\t"
    "buffer_load_dwordx4 v7, s[52:55], s61 offen offset:2048 nt lds\n\t"
    "s_add_u32 s61, s61, 196608\n\t"
    "s_branch .Lfb0_%=\n\t"
    ".Lf1_%=:\n\t"
    "s_waitcnt lgkmcnt(3)\n\t"
    "v_mfma_f32_16x16x32_bf16 v[80:83], v[32:35], v[116:119], 0\n\t"
    "ds_write_b128 v11, v[108:111] offset:1024\n\t"
    "v_mfma_f32_16x16x32_bf16 v[80:83], v[36:39], v[120:123], v[80:83]\n\t"
    "ds_read_b128 v[56:59], v12 offset:1024\n\t"
    "v_mfma_f32_16x16x32_bf16 v[80:83], v[40:43], v[124:127], v[80:83]\n\t"
    "s_waitcnt lgkmcnt(2)\n\t"
    "v_mfma_f32_16x16x32_bf16 v[80:83], v[44:47], v[128:131], v[80:83]\n\t"
    "ds_read_b128 v[60:63], v13 offset:1024\n\t"
    "v_cvt_pk_bf16_f32 v114, v100, v101\n\t"
    "v_mfma_f32_16x16x32_bf16 v[80:83], v[48:51], v[132:135], v[80:83]\n\t"
    "v_cvt_pk_bf16_f32 v115, v102, v103\n\t"
    "v_mfma_f32_16x16x32_bf16 v[80:83], v[52:55], v[136:139], v[80:83]\n\t"
    "ds_read_b128 v[64:67], v12 offset:5120\n\t"
    "v_mfma_f32_16x16x32_bf16 v[88:91], v[32:35], v[140:143], 0\n\t"
    "ds_write_b128 v11, v[112:115] offset:2048\n\t"
    "v_mfma_f32_16x16x32_bf16 v[88:91], v[36:39], v[144:147], v[88:91]\n\t"
    "ds_read_b128 v[68:71], v13 offset:5120\n\t"
    "v_mfma_f32_16x16x32_bf16 v[88:91], v[40:43], v[148:151], v[88:91]\n\t"
    "v_mov_b32_e32 v11, v14\n\t"
    "v_add_u32_e32 v14, 0x3000, v14\n\t"
    "v_mfma_f32_16x16x32_bf16 v[88:91], v[44:47], v[152:155], v[88:91]\n\t"
    "ds_read_b128 v[72:75], v12 offset:9216\n\t"
    "v_cvt_pk_bf16_f32 v104, v80, v81\n\t"
    "v_mfma_f32_16x16x32_bf16 v[88:91], v[48:51], v[156:159], v[88:91]\n\t"
    "v_cvt_pk_bf16_f32 v105, v82, v83\n\t"
    "v_mfma_f32_16x16x32_bf16 v[88:91], v[52:55], v[160:163], v[88:91]\n\t"
    "ds_read_b128 v[76:79], v13 offset:9216\n\t"
    "v_mfma_f32_16x16x32_bf16 v[96:99], v[32:35], v[164:167], 0\n\t"
    "s_cmp_eq_u32 s60, s67\n\t"
    "s_cbranch_scc1 .Lfs0_%=\n\t"
    ".Lfb0_%=:\n\t"
    "v_mfma_f32_16x16x32_bf16 v[96:99], v[36:39], v[168:171], v[96:99]\n\t"
    "v_mfma_f32_16x16x32_bf16 v[96:99], v[40:43], v[172:175], v[96:99]\n\t"
    "v_add_u32_e32 v12, 0x3000, v12\n\t"
    "v_add_u32_e32 v13, 0x3000, v13\n\t"
    "v_mfma_f32_16x16x32_bf16 v[96:99], v[44:47], v[176:179], v[96:99]\n\t"
    "v_cvt_pk_bf16_f32 v108, v88, v89\n\t"
    "v_cvt_pk_bf16_f32 v109, v90, v91\n\t"
    "v_mfma_f32_16x16x32_bf16 v[96:99], v[48:51], v[180:183], v[96:99]\n\t"
    "s_mov_b32 m0, s66\n\t"
    "v_mfma_f32_16x16x32_bf16 v[96:99], v[52:55], v[184:187], v[96:99]\n\t"
    "s_waitcnt lgkmcnt(4)\n\t"
    "v_mfma_f32_16x16x32_bf16 v[84:87], v[56:59], v[116:119], 0\n\t"
    "v_mfma_f32_16x16x32_bf16 v[84:87], v[60:63], v[120:123], v[84:87]\n\t"
    "v_mfma_f32_16x16x32_bf16 v[84:87], v[64:67], v[124:127], v[84:87]\n\t"
    "buffer_load_dwordx4 v7, s[52:55], s61 offen offset:0 nt lds\n\t"
    "s_waitcnt lgkmcnt(0)\n\t"
    "v_mfma_f32_16x16x32_bf16 v[84:87], v[68:71], v[128:131], v[84:87]\n\t"
    "s_barrier\n\t"
    "v_mfma_f32_16x16x32_bf16 v[84:87], v[72:75], v[132:135], v[84:87]\n\t"
    "ds_read_b128 v[32:35], v12 offset:0\n\t"
    "v_cvt_pk_bf16_f32 v112, v96, v97\n\t"
    "v_mfma_f32_16x16x32_bf16 v[84:87], v[76:79], v[136:139], v[84:87]\n\t"
    "ds_read_b128 v[36:39], v13 offset:0\n\t"
    "v_cvt_pk_bf16_f32 v113, v98, v99\n\t"
    "v_mfma_f32_16x16x32_bf16 v[92:95], v[56:59], v[140:143], 0\n\t"
    "ds_read_b128 v[40:43], v12 offset:4096\n\t"
    "buffer_load_dwordx4 v7, s[52:55], s61 offen offset:1024 nt lds\n\t"
    "v_mfma_f32_16x16x32_bf16 v[92:95], v[60:63], v[144:147], v[92:95]\n\t"
    "ds_read_b128 v[44:47], v13 offset:4096\n\t"
    "v_mfma_f32_16x16x32_bf16 v[92:95], v[64:67], v[148:151], v[92:95]\n\t"
    "ds_read_b128 v[48:51], v12 offset:8192\n\t"
    "v_mfma_f32_16x16x32_bf16 v[92:95], v[68:71], v[152:155], v[92:95]\n\t"
    "ds_read_b128 v[52:55], v13 offset:8192\n\t"
    "v_cvt_pk_bf16_f32 v106, v84, v85\n\t"
    "v_mfma_f32_16x16x32_bf16 v[92:95], v[72:75], v[156:159], v[92:95]\n\t"
    "v_cvt_pk_bf16_f32 v107, v86, v87\n\t"
    "buffer_load_dwordx4 v7, s[52:55], s61 offen offset:2048 nt lds\n\t"
    "v_mfma_f32_16x16x32_bf16 v[92:95], v[76:79], v[160:163], v[92:95]\n\t"
    "ds_write_b128 v11, v[104:107] offset:0\n\t"
    "v_mfma_f32_16x16x32_bf16 v[100:103], v[56:59], v[164:167], 0\n\t"
    "s_add_u32 s61, s61, 196608\n\t"
    "s_add_u32 s66, s66, 3072\n\t"
    "v_mfma_f32_16x16x32_bf16 v[100:103], v[60:63], v[168:171], v[100:103]\n\t"
    "v_mfma_f32_16x16x32_bf16 v[100:103], v[64:67], v[172:175], v[100:103]\n\t"
    "v_mfma_f32_16x16x32_bf16 v[100:103], v[68:71], v[176:179], v[100:103]\n\t"
    "v_cvt_pk_bf16_f32 v110, v92, v93\n\t"
    "v_cvt_pk_bf16_f32 v111, v94, v95\n\t"
    "v_mfma_f32_16x16x32_bf16 v[100:103], v[72:75], v[180:183], v[100:103]\n\t"
    ".Lh1_%=:\n\t"
    "s_cbranch_execz .LrM_%=\n\t"
    "s_sub_u32 s60, s60, 1\n\t"
    "s_cmp_lg_u32 s60, 0\n\t"
    "v_mfma_f32_16x16x32_bf16 v[100:103], v[76:79], v[184:187], v[100:103]\n\t"
    "s_cbranch_scc1 .Lf1_%=\n\t"
    "ds_write_b128 v11, v[108:111] offset:1024\n\t"
    "s_nop 6\n\t"
    "v_cvt_pk_bf16_f32 v114, v100, v101\n\t"
    "v_cvt_pk_bf16_f32 v115, v102, v103\n\t"
    "ds_write_b128 v11, v[112:115] offset:2048\n\t"
    "v_add_u32_e32 v9, 0x9000, v15\n\t"
    "v_mov_b32_e32 v8, v15\n\t"
    "s_waitcnt lgkmcnt(0)\n\t"
    "s_barrier\n\t"
    "s_add_i32 s63, %[rbeg], %[rd]\n\t"
    "s_add_i32 s62, s63, %[rd]\n\t"
    "s_waitcnt vmcnt(18)\n\t"
    "ds_read_b128 v[220:223], v0 offset:0\n\t"
    "ds_read_b128 a[0:3], v8 offset:0\n\t"
    "ds_read_b128 v[216:219], v1 offset:0\n\t"
    "ds_read_b128 a[8:11], v8 offset:12288\n\t"
    "ds_read_b128 v[212:215], v2 offset:0\n\t"
    "ds_read_b128 a[16:19], v8 offset:24576\n\t"
    "ds_read_b128 v[36:39], v3 offset:0\n\t"
    "ds_read_b128 a[24:27], v9 offset:0\n\t"
    "ds_read_b128 v[40:43], v4 offset:0\n\t"
    "ds_read_b128 a[32:35], v9 offset:12288\n\t"
    "ds_read_b128 v[44:47], v5 offset:0\n\t"
    "ds_read_b128 a[40:43], v9 offset:24576\n\t"
    "ds_read_b128 a[4:7], v8 offset:1024\n\t"
    "ds_read_b128 a[12:15], v8 offset:13312\n\t"
    "ds_read_b128 a[20:23], v8 offset:25600\n\t"
    "ds_read_b128 a[28:31], v9 offset:1024\n\t"
    "ds_read_b128 a[36:39], v9 offset:13312\n\t"
    "s_waitcnt lgkmcnt(15)\n\t"
    "v_mfma_f32_16x16x32_bf16 v[72:75], a[0:3], v[220:223], 0\n\t"
    "ds_read_b128 a[44:47], v9 offset:25600\n\t"
    "s_waitcnt lgkmcnt(14)\n\t"
    "v_mfma_f32_16x16x32_bf16 v[72:75], a[8:11], v[216:219], v[72:75]\n\t"
    "ds_read_b128 a[48:51], v8 offset:2048\n\t"
    "s_waitcnt lgkmcnt(13)\n\t"
    "v_mfma_f32_16x16x32_bf16 v[72:75], a[16:19], v[212:215], v[72:75]\n\t"
    "ds_read_b128 a[56:59], v8 offset:14336\n\t"
    "s_waitcnt lgkmcnt(3)\n\t"
    "v_mfma_f32_16x16x32_bf16 v[72:75], a[24:27], v[36:39], v[72:75]\n\t"
    "ds_read_b128 a[64:67], v8 offset:26624\n\t"
    "v_mfma_f32_16x16x32_bf16 v[72:75], a[32:35], v[40:43], v[72:75]\n\t"
    "ds_read_b128 a[72:75], v9 offset:2048\n\t"
    "v_mfma_f32_16x16x32_bf16 v[72:75], a[40:43], v[44:47], v[72:75]\n\t"
    "ds_read_b128 a[80:83], v9 offset:14336\n\t"
    "v_mfma_f32_16x16x32_bf16 v[48:51], a[4:7], v[220:223], 0\n\t"
    "ds_read_b128 a[88:91], v9 offset:26624\n\t"
    "v_mfma_f32_16x16x32_bf16 v[48:51], a[12:15], v[216:219], v[48:51]\n\t"
    "ds_read_b128 a[52:55], v8 offset:3072\n\t"
    "v_mfma_f32_16x16x32_bf16 v[48:51], a[20:23], v[212:215], v[48:51]\n\t"
    "ds_read_b128 a[60:63], v8 offset:15360\n\t"
    "v_mfma_f32_16x16x32_bf16 v[48:51], a[28:31], v[36:39], v[48:51]\n\t"
    "ds_read_b128 a[68:71], v8 offset:27648\n\t"
    "v_mfma_f32_16x16x32_bf16 v[48:51], a[36:39], v[40:43], v[48:51]\n\t"
    "v_cvt_pk_bf16_f32 v12, v72, v73\n\t"
    "v_cvt_pk_bf16_f32 v13, v74, v75\n\t"
    "ds_read_b128 a[76:79], v9 offset:3072\n\t"
    "s_waitcnt lgkmcnt(3)\n\t"
    "v_mfma_f32_16x16x32_bf16 v[48:51], a[44:47], v[44:47], v[48:51]\n\t"
    "ds_read_b128 a[84:87], v9 offset:15360\n\t"
    "v_mfma_f32_16x16x32_bf16 v[52:55], a[48:51], v[220:223], 0\n\t"
    "ds_read_b128 a[92:95], v9 offset:27648\n\t"
    "v_mfma_f32_16x16x32_bf16 v[52:55], a[56:59], v[216:219], v[52:55]\n\t"
    "ds_read_b128 a[96:99], v8 offset:4096\n\t"
    "v_mfma_f32_16x16x32_bf16 v[52:55], a[64:67], v[212:215], v[52:55]\n\t"
    "ds_read_b128 a[104:107], v8 offset:16384\n\t"
    "v_mfma_f32_16x16x32_bf16 v[52:55], a[72:75], v[36:39], v[52:55]\n\t"
    "v_cvt_pk_bf16_f32 v14, v48, v49\n\t"
    "ds_read_b128 a[112:115], v8 offset:28672\n\t"
    "v_mfma_f32_16x16x32_bf16 v[52:55], a[80:83], v[40:43], v[52:55]\n\t"
    "v_cvt_pk_bf16_f32 v15, v50, v51\n\t"
    "ds_read_b128 a[120:123], v9 offset:4096\n\t"
    "v_mfma_f32_16x16x32_bf16 v[52:55], a[88:91], v[44:47], v[52:55]\n\t"
    "ds_read_b128 a[128:131], v9 offset:16384\n\t"
    "v_mfma_f32_16x16x32_bf16 v[56:59], a[52:55], v[220:223], 0\n\t"
    "ds_read_b128 a[136:139], v9 offset:28672\n\t"
    "s_waitcnt lgkmcnt(3)\n\t"
    "v_mfma_f32_16x16x32_bf16 v[56:59], a[60:63], v[216:219], v[56:59]\n\t"
    "ds_read_b128 a[100:103], v8 offset:5120\n\t"
    "v_mfma_f32_16x16x32_bf16 v[56:59], a[68:71], v[212:215], v[56:59]\n\t"
    "ds_read_b128 a[108:111], v8 offset:17408\n\t"
    "v_mfma_f32_16x16x32_bf16 v[56:59], a[76:79], v[36:39], v[56:59]\n\t"
    "v_cvt_pk_bf16_f32 v48, v52, v53\n\t"
    "ds_read_b128 a[116:119], v8 offset:29696\n\t"
    "v_mfma_f32_16x16x32_bf16 v[56:59], a[84:87], v[40:43], v[56:59]\n\t"
    "v_cvt_pk_bf16_f32 v49, v54, v55\n\t"
    "ds_read_b128 a[124:127], v9 offset:5120\n\t"
    "v_mfma_f32_16x16x32_bf16 v[56:59], a[92:95], v[44:47], v[56:59]\n\t"
    "ds_read_b128 a[132:135], v9 offset:17408\n\t"
    "v_mfma_f32_16x16x32_bf16 v[60:63], a[96:99], v[220:223], 0\n\t"
    "ds_read_b128 a[140:143], v9 offset:29696\n\t"
    "v_mfma_f32_16x16x32_bf16 v[60:63], a[104:107], v[216:219], v[60:63]\n\t"
    "ds_read_b128 a[144:147], v8 offset:6144\n\t"
    "v_mfma_f32_16x16x32_bf16 v[60:63], a[112:115], v[212:215], v[60:63]\n\t"
    "ds_read_b128 a[152:155], v8 offset:18432\n\t"
    "s_waitcnt lgkmcnt(3)\n\t"
    "v_mfma_f32_16x16x32_bf16 v[60:63], a[120:123], v[36:39], v[60:63]\n\t"
    "v_cvt_pk_bf16_f32 v50, v56, v57\n\t"
    "v_cvt_pk_bf16_f32 v51, v58, v59\n\t"
    "ds_read_b128 a[160:163], v8 offset:30720\n\t"
    "v_mfma_f32_16x16x32_bf16 v[60:63], a[128:131], v[40:43], v[60:63]\n\t"
    "ds_read_b128 a[168:171], v9 offset:6144\n\t"
    "v_mfma_f32_16x16x32_bf16 v[60:63], a[136:139], v[44:47], v[60:63]\n\t"
    "ds_read_b128 a[176:179], v9 offset:18432\n\t"
    "v_mfma_f32_16x16x32_bf16 v[52:55], a[100:103], v[220:223], 0\n\t"
    "ds_read_b128 a[184:187], v9 offset:30720\n\t"
    "v_mfma_f32_16x16x32_bf16 v[52:55], a[108:111], v[216:219], v[52:55]\n\t"
    "v_mfma_f32_16x16x32_bf16 v[52:55], a[116:119], v[212:215], v[52:55]\n\t"
    "v_mfma_f32_16x16x32_bf16 v[52:55], a[124:127], v[36:39], v[52:55]\n\t"
    "v_cvt_pk_bf16_f32 v56, v60, v61\n\t"
    "v_cvt_pk_bf16_f32 v57, v62, v63\n\t"
    "v_mfma_f32_16x16x32_bf16 v[52:55], a[132:135], v[40:43], v[52:55]\n\t"
    "s_waitcnt vmcnt(0)\n\t"
    "ds_read_b128 v[196:199], v0 offset:6144\n\t"
    "s_waitcnt lgkmcnt(1)\n\t"
    "v_mfma_f32_16x16x32_bf16 v[52:55], a[140:143], v[44:47], v[52:55]\n\t"
    "ds_read_b128 v[192:195], v1 offset:6144\n\t"
    "v_mfma_f32_16x16x32_bf16 v[60:63], a[144:147], v[220:223], 0\n\t"
    "ds_read_b128 v[188:191], v2 offset:6144\n\t"
    "v_mfma_f32_16x16x32_bf16 v[60:63], a[152:155], v[216:219], v[60:63]\n\t"
    "ds_read_b128 v[172:175], v0 offset:12288\n\t"
    "v_mfma_f32_16x16x32_bf16 v[60:63], a[160:163], v[212:215], v[60:63]\n\t"
    "ds_read_b128 v[168:171], v1 offset:12288\n\t"
    "v_mfma_f32_16x16x32_bf16 v[60:63], a[168:171], v[36:39], v[60:63]\n\t"
    "v_cvt_pk_bf16_f32 v58, v52, v53\n\t"
    "v_cvt_pk_bf16_f32 v59, v54, v55\n\t"
    "ds_read_b128 v[164:167], v2 offset:12288\n\t"
    "v_mfma_f32_16x16x32_bf16 v[60:63], a[176:179], v[40:43], v[60:63]\n\t"
    "ds_read_b128 v[148:151], v0 offset:18432\n\t"
    "v_mfma_f32_16x16x32_bf16 v[60:63], a[184:187], v[44:47], v[60:63]\n\t"
    "ds_read_b128 v[144:147], v1 offset:18432\n\t"
    "v_mfma_f32_16x16x32_bf16 v[52:55], v[220:223], v[12:15], 0\n\t"
    "ds_read_b128 v[140:143], v2 offset:18432\n\t"
    "v_mfma_f32_16x16x32_bf16 v[52:55], v[216:219], v[48:51], v[52:55]\n\t"
    "ds_read_b128 a[148:151], v8 offset:7168\n\t"
    "v_mfma_f32_16x16x32_bf16 v[52:55], v[212:215], v[56:59], v[52:55]\n\t"
    "ds_read_b128 a[156:159], v8 offset:19456\n\t"
    "s_waitcnt lgkmcnt(3)\n\t"
    "v_mfma_f32_16x16x32_bf16 v[64:67], v[196:199], v[12:15], 0\n\t"
    "ds_read_b128 a[164:167], v8 offset:31744\n\t"
    "v_mfma_f32_16x16x32_bf16 v[64:67], v[192:195], v[48:51], v[64:67]\n\t"
    "ds_read_b128 a[172:175], v9 offset:7168\n\t"
    "v_mfma_f32_16x16x32_bf16 v[64:67], v[188:191], v[56:59], v[64:67]\n\t"
    "ds_read_b128 a[180:183], v9 offset:19456\n\t"
    "v_mfma_f32_16x16x32_bf16 v[68:71], v[172:175], v[12:15], 0\n\t"
    "ds_read_b128 a[188:191], v9 offset:31744\n\t"
    "v_mfma_f32_16x16x32_bf16 v[68:71], v[168:171], v[48:51], v[68:71]\n\t"
    "ds_read_b128 a[192:195], v8 offset:8192\n\t"
    "v_mfma_f32_16x16x32_bf16 v[68:71], v[164:167], v[56:59], v[68:71]\n\t"
    "ds_read_b128 a[200:203], v8 offset:20480\n\t"
    "v_mfma_f32_16x16x32_bf16 v[72:75], v[148:151], v[12:15], 0\n\t"
    "ds_read_b128 a[208:211], v8 offset:32768\n\t"
    "v_mfma_f32_16x16x32_bf16 v[72:75], v[144:147], v[48:51], v[72:75]\n\t"
    "ds_read_b128 a[216:219], v9 offset:8192\n\t"
    "s_waitcnt lgkmcnt(3)\n\t"
    "v_mfma_f32_16x16x32_bf16 v[72:75], v[140:143], v[56:59], v[72:75]\n\t"
    "ds_read_b128 a[224:227], v9 offset:20480\n\t"
    "v_mfma_f32_16x16x32_bf16 v[12:15], a[148:151], v[220:223], 0\n\t"
    "ds_read_b128 a[232:235], v9 offset:32768\n\t"
    "v_mfma_f32_16x16x32_bf16 v[12:15], a[156:159], v[216:219], v[12:15]\n\t"
    "ds_read_b128 a[196:199], v8 offset:9216\n\t"
    "v_mfma_f32_16x16x32_bf16 v[12:15], a[164:167], v[212:215], v[12:15]\n\t"
    "ds_read_b128 a[204:207], v8 offset:21504\n\t"
    "v_mfma_f32_16x16x32_bf16 v[12:15], a[172:175], v[36:39], v[12:15]\n\t"
    "ds_read_b128 a[212:215], v8 offset:33792\n\t"
    "v_mfma_f32_16x16x32_bf16 v[12:15], a[180:183], v[40:43], v[12:15]\n\t"
    "ds_read_b128 a[220:223], v9 offset:9216\n\t"
    "v_mfma_f32_16x16x32_bf16 v[12:15], a[188:191], v[44:47], v[12:15]\n\t"
    "v_cvt_pk_bf16_f32 v48, v60, v61\n\t"
    "ds_read_b128 a[228:231], v9 offset:21504\n\t"
    "v_mfma_f32_16x16x32_bf16 v[56:59], a[192:195], v[220:223], 0\n\t"
    "ds_read_b128 a[236:239], v9 offset:33792\n\t"
    "s_waitcnt lgkmcnt(3)\n\t"
    "v_mfma_f32_16x16x32_bf16 v[56:59], a[200:203], v[216:219], v[56:59]\n\t"
    "ds_read_b128 a[240:243], v8 offset:10240\n\t"
    "v_mfma_f32_16x16x32_bf16 v[56:59], a[208:211], v[212:215], v[56:59]\n\t"
    "v_cvt_pk_bf16_f32 v49, v62, v63\n\t"
    "ds_read_b128 a[248:251], v8 offset:22528\n\t"
    "v_mfma_f32_16x16x32_bf16 v[56:59], a[216:219], v[36:39], v[56:59]\n\t"
    "v_cvt_pk_bf16_f32 v50, v12, v13\n\t"
    "ds_read_b128 v[224:227], v8 offset:34816\n\t"
    "v_mfma_f32_16x16x32_bf16 v[56:59], a[224:227], v[40:43], v[56:59]\n\t"
    "v_cvt_pk_bf16_f32 v51, v14, v15\n\t"
    "ds_read_b128 v[232:235], v9 offset:10240\n\t"
    "v_mfma_f32_16x16x32_bf16 v[56:59], a[232:235], v[44:47], v[56:59]\n\t"
    "ds_read_b128 v[240:243], v9 offset:22528\n\t"
    "v_mfma_f32_16x16x32_bf16 v[60:63], a[196:199], v[220:223], 0\n\t"
    "ds_read_b128 v[248:251], v9 offset:34816\n\t"
    "v_mfma_f32_16x16x32_bf16 v[60:63], a[204:207], v[216:219], v[60:63]\n\t"
    "ds_read_b128 a[244:247], v8 offset:11264\n\t"
    "v_mfma_f32_16x16x32_bf16 v[60:63], a[212:215], v[212:215], v[60:63]\n\t"
    "ds_read_b128 a[252:255], v8 offset:23552\n\t"
    "s_waitcnt lgkmcnt(3)\n\t"
    "v_mfma_f32_16x16x32_bf16 v[60:63], a[220:223], v[36:39], v[60:63]\n\t"
    "v_cvt_pk_bf16_f32 v76, v56, v57\n\t"
    "ds_read_b128 v[228:231], v8 offset:35840\n\t"
    "v_mfma_f32_16x16x32_bf16 v[60:63], a[228:231], v[40:43], v[60:63]\n\t"
    "v_cvt_pk_bf16_f32 v77, v58, v59\n\t"
    "ds_read_b128 v[236:239], v9 offset:11264\n\t"
    "v_mfma_f32_16x16x32_bf16 v[60:63], a[236:239], v[44:47], v[60:63]\n\t"
    "ds_read_b128 v[244:247], v9 offset:23552\n\t"
    "v_mfma_f32_16x16x32_bf16 v[80:83], a[240:243], v[220:223], 0\n\t"
    "ds_read_b128 v[252:255], v9 offset:35840\n\t"
    "v_mfma_f32_16x16x32_bf16 v[80:83], a[248:251], v[216:219], v[80:83]\n\t"
    "v_mfma_f32_16x16x32_bf16 v[80:83], v[224:227], v[212:215], v[80:83]\n\t"
    "v_mfma_f32_16x16x32_bf16 v[80:83], v[232:235], v[36:39], v[80:83]\n\t"
    "v_cvt_pk_bf16_f32 v78, v60, v61\n\t"
    "v_cvt_pk_bf16_f32 v79, v62, v63\n\t"
    "v_mfma_f32_16x16x32_bf16 v[80:83], v[240:243], v[40:43], v[80:83]\n\t"
    "ds_read_b128 v[184:187], v3 offset:6144\n\t"
    "s_waitcnt lgkmcnt(1)\n\t"
    "v_mfma_f32_16x16x32_bf16 v[80:83], v[248:251], v[44:47], v[80:83]\n\t"
    "ds_read_b128 v[180:183], v4 offset:6144\n\t"
    "v_mfma_f32_16x16x32_bf16 v[24:27], a[244:247], v[220:223], 0\n\t"
    "ds_read_b128 v[176:179], v5 offset:6144\n\t"
    "v_mfma_f32_16x16x32_bf16 v[24:27], a[252:255], v[216:219], v[24:27]\n\t"
    "ds_read_b128 v[208:211], v3 offset:0\n\t"
    "v_mfma_f32_16x16x32_bf16 v[24:27], v[228:231], v[212:215], v[24:27]\n\t"
    "ds_read_b128 v[204:207], v4 offset:0\n\t"
    "v_mfma_f32_16x16x32_bf16 v[24:27], v[236:239], v[36:39], v[24:27]\n\t"
    "v_cvt_pk_bf16_f32 v28, v80, v81\n\t"
    "v_cvt_pk_bf16_f32 v29, v82, v83\n\t"
    "ds_read_b128 v[200:203], v5 offset:0\n\t"
    "v_mfma_f32_16x16x32_bf16 v[24:27], v[244:247], v[40:43], v[24:27]\n\t"
    "v_mfma_f32_16x16x32_bf16 v[24:27], v[252:255], v[44:47], v[24:27]\n\t"
    "v_mfma_f32_16x16x32_bf16 v[36:39], a[0:3], v[196:199], 0\n\t"
    "v_mfma_f32_16x16x32_bf16 v[36:39], a[8:11], v[192:195], v[36:39]\n\t"
    "ds_read_b128 v[160:163], v3 offset:12288\n\t"
    "v_mfma_f32_16x16x32_bf16 v[36:39], a[16:19], v[188:191], v[36:39]\n\t"
    "ds_read_b128 v[156:159], v4 offset:12288\n\t"
    "s_waitcnt lgkmcnt(2)\n\t"
    "v_mfma_f32_16x16x32_bf16 v[36:39], a[24:27], v[184:187], v[36:39]\n\t"
    "v_cvt_pk_bf16_f32 v30, v24, v25\n\t"
    "v_cvt_pk_bf16_f32 v31, v26, v27\n\t"
    "ds_read_b128 v[152:155], v5 offset:12288\n\t"
    "v_mfma_f32_16x16x32_bf16 v[36:39], a[32:35], v[180:183], v[36:39]\n\t"
    "ds_read_b128 v[136:139], v3 offset:18432\n\t"
    "v_mfma_f32_16x16x32_bf16 v[36:39], a[40:43], v[176:179], v[36:39]\n\t"
    "ds_read_b128 v[132:135], v4 offset:18432\n\t"
    "v_mfma_f32_16x16x32_bf16 v[52:55], v[208:211], v[48:51], v[52:55]\n\t"
    "ds_read_b128 v[128:131], v5 offset:18432\n\t"
    "v_mfma_f32_16x16x32_bf16 v[52:55], v[204:207], v[76:79], v[52:55]\n\t"
    "v_mfma_f32_16x16x32_bf16 v[52:55], v[200:203], v[28:31], v[52:55]\n\t"
    "v_mfma_f32_16x16x32_bf16 v[64:67], v[184:187], v[48:51], v[64:67]\n\t"
    "v_cvt_pk_bf16_f32 v12, v36, v37\n\t"
    "v_cvt_pk_bf16_f32 v13, v38, v39\n\t"
    "v_mfma_f32_16x16x32_bf16 v[64:67], v[180:183], v[76:79], v[64:67]\n\t"
    "s_barrier\n\t"
    "v_mfma_f32_16x16x32_bf16 v[64:67], v[176:179], v[28:31], v[64:67]\n\t"
    "s_waitcnt lgkmcnt(0)\n\t"
    "v_mfma_f32_16x16x32_bf16 v[68:71], v[160:163], v[48:51], v[68:71]\n\t"
    "v_max3_f32 v16, v52, v53, v54\n\t"
    "v_mfma_f32_16x16x32_bf16 v[68:71], v[156:159], v[76:79], v[68:71]\n\t"
    "v_mfma_f32_16x16x32_bf16 v[68:71], v[152:155], v[28:31], v[68:71]\n\t"
    "v_mfma_f32_16x16x32_bf16 v[72:75], v[136:139], v[48:51], v[72:75]\n\t"
    "v_max3_f32 v17, v55, v64, v65\n\t"
    "v_max3_f32 v16, v16, v66, v67\n\t"
    "v_mfma_f32_16x16x32_bf16 v[72:75], v[132:135], v[76:79], v[72:75]\n\t"
    "v_mfma_f32_16x16x32_bf16 v[72:75], v[128:131], v[28:31], v[72:75]\n\t"
    "v_mfma_f32_16x16x32_bf16 v[20:23], a[4:7], v[196:199], 0\n\t"
    "v_max3_f32 v17, v17, v68, v69\n\t"
    "v_max3_f32 v16, v16, v70, v71\n\t"
    "v_mfma_f32_16x16x32_bf16 v[20:23], a[12:15], v[192:195], v[20:23]\n\t"
    "v_mfma_f32_16x16x32_bf16 v[20:23], a[20:23], v[188:191], v[20:23]\n\t"
    "v_mfma_f32_16x16x32_bf16 v[20:23], a[28:31], v[184:187], v[20:23]\n\t"
    "v_max3_f32 v17, v17, v72, v73\n\t"
    "v_mfma_f32_16x16x32_bf16 v[20:23], a[36:39], v[180:183], v[20:23]\n\t"
    "v_max3_f32 v16, v16, v74, v75\n\t"
    "v_mfma_f32_16x16x32_bf16 v[20:23], a[44:47], v[176:179], v[20:23]\n\t"
    "v_max_f32_e32 v16, v16, v17\n\t"
    "v_mov_b32_e32 v17, v16\n\t"
    "v_mfma_f32_16x16x32_bf16 v[24:27], a[48:51], v[196:199], 0\n\t"
    "s_add_u32 m0, s63, 0\n\t"
    "s_add_u32 s61, %[wb], 12582912\n\t"
    "v_mfma_f32_16x16x32_bf16 v[24:27], a[56:59], v[192:195], v[24:27]\n\t"
    "v_permlane32_swap_b32 v16, v17\n\t"
    "v_max_f32_e32 v16, v16, v17\n\t"
    "buffer_load_dwordx4 v7, s[52:55], s61 offen offset:0 nt lds\n\t"
    "v_mfma_f32_16x16x32_bf16 v[24:27], a[64:67], v[188:191], v[24:27]\n\t"
    "v_mov_b32_e32 v17, v16\n\t"
    "buffer_load_dwordx4 v7, s[52:55], s61 offen offset:1024 nt lds\n\t"
    "v_mfma_f32_16x16x32_bf16 v[24:27], a[72:75], v[184:187], v[24:27]\n\t"
    "v_cvt_pk_bf16_f32 v14, v20, v21\n\t"
    "v_cvt_pk_bf16_f32 v15, v22, v23\n\t"
    "v_mfma_f32_16x16x32_bf16 v[24:27], a[80:83], v[180:183], v[24:27]\n\t"
    "v_permlane16_swap_b32 v16, v17\n\t"
    "v_max_f32_e32 v16, v16, v17\n\t"
    "buffer_load_dwordx4 v7, s[52:55], s61 offen offset:2048 nt lds\n\t"
    "v_mfma_f32_16x16x32_bf16 v[24:27], a[88:91], v[176:179], v[24:27]\n\t"
    "v_mul_f32_e32 v16, s68, v16\n\t"
    "v_fma_f32 v52, v52, s68, -v16\n\t"
    "s_add_u32 m0, s63, 3072\n\t"
    "s_add_u32 s61, %[wb], 12779520\n\t"
    "v_mfma_f32_16x16x32_bf16 v[20:23], a[52:55], v[196:199], 0\n\t"
    "v_exp_f32_e32 v52, v52\n\t"
    "v_mfma_f32_16x16x32_bf16 v[20:23], a[60:63], v[192:195], v[20:23]\n\t"
    "v_fma_f32 v53, v53, s68, -v16\n\t"
    "v_fma_f32 v54, v54, s68, -v16\n\t"
    "buffer_load_dwordx4 v7, s[52:55], s61 offen offset:0 nt lds\n\t"
    "v_mfma_f32_16x16x32_bf16 v[20:23], a[68:71], v[188:191], v[20:23]\n\t"
    "v_exp_f32_e32 v53, v53\n\t"
    "buffer_load_dwordx4 v7, s[52:55], s61 offen offset:1024 nt lds\n\t"
    "v_mfma_f32_16x16x32_bf16 v[20:23], a[76:79], v[184:187], v[20:23]\n\t"
    "v_cvt_pk_bf16_f32 v28, v24, v25\n\t"
    "v_mfma_f32_16x16x32_bf16 v[20:23], a[84:87], v[180:183], v[20:23]\n\t"
    "v_cvt_pk_bf16_f32 v29, v26, v27\n\t"
    "v_add_f32_e32 v18, v52, v53\n\t"
    "buffer_load_dwordx4 v7, s[52:55], s61 offen offset:2048 nt lds\n\t"
    "v_mfma_f32_16x16x32_bf16 v[20:23], a[92:95], v[176:179], v[20:23]\n\t"
    "v_exp_f32_e32 v54, v54\n\t"
    "s_add_u32 m0, s63, 6144\n\t"
    "s_add_u32 s61, %[wb], 12976128\n\t"
    "v_mfma_f32_16x16x32_bf16 v[32:35], a[96:99], v[196:199], 0\n\t"
    "v_fma_f32 v55, v55, s68, -v16\n\t"
    "v_fma_f32 v64, v64, s68, -v16\n\t"
    "v_mfma_f32_16x16x32_bf16 v[32:35], a[104:107], v[192:195], v[32:35]\n\t"
    "v_exp_f32_e32 v55, v55\n\t"
    "buffer_load_dwordx4 v7, s[52:55], s61 offen offset:0 nt lds\n\t"
    "v_mfma_f32_16x16x32_bf16 v[32:35], a[112:115], v[188:191], v[32:35]\n\t"
    "v_add_f32_e32 v19, v54, v55\n\t"
    "v_fma_f32 v65, v65, s68, -v16\n\t"
    "buffer_load_dwordx4 v7, s[52:55], s61 offen offset:1024 nt lds\n\t"
    "v_mfma_f32_16x16x32_bf16 v[32:35], a[120:123], v[184:187], v[32:35]\n\t"
    "v_cvt_pk_bf16_f32 v30, v20, v21\n\t"
    "v_cvt_pk_bf16_f32 v31, v22, v23\n\t"
    "buffer_load_dwordx4 v7, s[52:55], s61 offen offset:2048 nt lds\n\t"
    "v_mfma_f32_16x16x32_bf16 v[32:35], a[128:131], v[180:183], v[32:35]\n\t"
    "v_exp_f32_e32 v64, v64\n\t"
    "v_mfma_f32_16x16x32_bf16 v[32:35], a[136:139], v[176:179], v[32:35]\n\t"
    "v_add_f32_e32 v18, v18, v64\n\t"
    "v_fma_f32 v66, v66, s68, -v16\n\t"
    "s_add_u32 m0, s63, 9216\n\t"
    "s_add_u32 s61, %[wb], 13172736\n\t"
    "v_mfma_f32_16x16x32_bf16 v[20:23], a[100:103], v[196:199], 0\n\t"
    "v_exp_f32_e32 v65, v65\n\t"
    "buffer_load_dwordx4 v7, s[52:55], s61 offen offset:0 nt lds\n\t"
    "v_mfma_f32_16x16x32_bf16 v[20:23], a[108:111], v[192:195], v[20:23]\n\t"
    "v_add_f32_e32 v18, v18, v65\n\t"
    "v_fma_f32 v67, v67, s68, -v16\n\t"
    "v_mfma_f32_16x16x32_bf16 v[20:23], a[116:119], v[188:191], v[20:23]\n\t"
    "v_fma_f32 v68, v68, s68, -v16\n\t"
    "buffer_load_dwordx4 v7, s[52:55], s61 offen offset:1024 nt lds\n\t"
    "v_mfma_f32_16x16x32_bf16 v[20:23], a[124:127], v[184:187], v[20:23]\n\t"
    "v_cvt_pk_bf16_f32 v24, v32, v33\n\t"
    "buffer_load_dwordx4 v7, s[52:55], s61 offen offset:2048 nt lds\n\t"
    "v_mfma_f32_16x16x32_bf16 v[20:23], a[132:135], v[180:183], v[20:23]\n\t"
    "v_cvt_pk_bf16_f32 v25, v34, v35\n\t"
    "v_fma_f32 v69, v69, s68, -v16\n\t"
    "v_mfma_f32_16x16x32_bf16 v[20:23], a[140:143], v[176:179], v[20:23]\n\t"
    "v_exp_f32_e32 v66, v66\n\t"
    "s_add_u32 m0, s63, 12288\n\t"
    "s_add_u32 s61, %[wb], 13369344\n\t"
    "v_mfma_f32_16x16x32_bf16 v[36:39], a[144:147], v[196:199], 0\n\t"
    "v_add_f32_e32 v19, v19, v66\n\t"
    "v_fma_f32 v70, v70, s68, -v16\n\t"
    "buffer_load_dwordx4 v7, s[52:55], s61 offen offset:0 nt lds\n\t"
    "v_mfma_f32_16x16x32_bf16 v[36:39], a[152:155], v[192:195], v[36:39]\n\t"
    "v_exp_f32_e32 v67, v67\n\t"
    "v_mfma_f32_16x16x32_bf16 v[36:39], a[160:163], v[188:191], v[36:39]\n\t"
    "v_add_f32_e32 v19, v19, v67\n\t"
    "v_fma_f32 v71, v71, s68, -v16\n\t"
    "buffer_load_dwordx4 v7, s[52:55], s61 offen offset:1024 nt lds\n\t"
    "v_mfma_f32_16x16x32_bf16 v[36:39], a[168:171], v[184:187], v[36:39]\n\t"
    "v_cvt_pk_bf16_f32 v26, v20, v21\n\t"
    "v_cvt_pk_bf16_f32 v27, v22, v23\n\t"
    "buffer_load_dwordx4 v7, s[52:55], s61 offen offset:2048 nt lds\n\t"
    "v_mfma_f32_16x16x32_bf16 v[36:39], a[176:179], v[180:183], v[36:39]\n\t"
    "v_exp_f32_e32 v68, v68\n\t"
    "v_mfma_f32_16x16x32_bf16 v[36:39], a[184:187], v[176:179], v[36:39]\n\t"
    "v_add_f32_e32 v18, v18, v68\n\t"
    "v_fma_f32 v72, v72, s68, -v16\n\t"
    "s_mov_b32 s60, 8\n\t"
    "s_add_u32 s61, %[wb], 13565952\n\t"
    "s_branch .Lmid_%=\n\t"
    ".Lf2_%=:\n\t"
    "s_waitcnt lgkmcnt(0)\n\t"
    "v_mfma_f32_16x16x32_bf16 v[72:75], a[0:3], v[220:223], 0\n\t"
    "ds_write_b128 v6, v[56:59] offset:4096\n\t"
    "v_mfma_f32_16x16x32_bf16 v[72:75], a[8:11], v[216:219], v[72:75]\n\t"
    "v_cvt_pk_bf16_f32 v62, v12, v13\n\t"
    "v_mul_f32_e32 v14, v14, v18\n\t"
    "v_mfma_f32_16x16x32_bf16 v[72:75], a[16:19], v[212:215], v[72:75]\n\t"
    "v_mul_f32_e32 v15, v15, v18\n\t"
    "v_cvt_pk_bf16_f32 v63, v14, v15\n\t"
    "v_mfma_f32_16x16x32_bf16 v[72:75], a[24:27], v[36:39], v[72:75]\n\t"
    "v_cvt_pk_bf16_f32 v70, v64, v65\n\t"
    "v_cvt_pk_bf16_f32 v71, v66, v67\n\t"
    "v_mfma_f32_16x16x32_bf16 v[72:75], a[32:35], v[40:43], v[72:75]\n\t"
    "ds_write_b128 v6, v[60:63] offset:5120\n\t"
    "v_mfma_f32_16x16x32_bf16 v[72:75], a[40:43], v[44:47], v[72:75]\n\t"
    "v_mfma_f32_16x16x32_bf16 v[20:23], v[208:211], v[48:51], v[20:23]\n\t"
    "v_mfma_f32_16x16x32_bf16 v[20:23], v[204:207], v[52:55], v[20:23]\n\t"
    "v_mfma_f32_16x16x32_bf16 v[20:23], v[200:203], v[68:71], v[20:23]\n\t"
    "v_mfma_f32_16x16x32_bf16 v[24:27], v[184:187], v[48:51], v[24:27]\n\t"
    "v_cvt_pk_bf16_f32 v12, v72, v73\n\t"
    "v_cvt_pk_bf16_f32 v13, v74, v75\n\t"
    "v_mfma_f32_16x16x32_bf16 v[24:27], v[180:183], v[52:55], v[24:27]\n\t"
    "s_waitcnt lgkmcnt(0)\n\t"
    "s_barrier\n\t"
    "v_mfma_f32_16x16x32_bf16 v[24:27], v[176:179], v[68:71], v[24:27]\n\t"
    "v_mfma_f32_16x16x32_bf16 v[28:31], v[160:163], v[48:51], v[28:31]\n\t"
    "v_max3_f32 v16, v20, v21, v22\n\t"
    "v_mfma_f32_16x16x32_bf16 v[28:31], v[156:159], v[52:55], v[28:31]\n\t"
    "v_mfma_f32_16x16x32_bf16 v[28:31], v[152:155], v[68:71], v[28:31]\n\t"
    "v_mfma_f32_16x16x32_bf16 v[32:35], v[136:139], v[48:51], v[32:35]\n\t"
    "v_max3_f32 v17, v23, v24, v25\n\t"
    "v_max3_f32 v16, v16, v26, v27\n\t"
    "v_mfma_f32_16x16x32_bf16 v[32:35], v[132:135], v[52:55], v[32:35]\n\t"
    "s_waitcnt vmcnt(12)\n\t"
    "v_mfma_f32_16x16x32_bf16 v[32:35], v[128:131], v[68:71], v[32:35]\n\t"
    "ds_read_b128 v[196:199], v0 offset:6144\n\t"
    "v_mfma_f32_16x16x32_bf16 v[48:51], a[4:7], v[220:223], 0\n\t"
    "v_max3_f32 v17, v17, v28, v29\n\t"
    "v_max3_f32 v16, v16, v30, v31\n\t"
    "v_mfma_f32_16x16x32_bf16 v[48:51], a[12:15], v[216:219], v[48:51]\n\t"
    "ds_read_b128 v[192:195], v1 offset:6144\n\t"
    "v_mfma_f32_16x16x32_bf16 v[48:51], a[20:23], v[212:215], v[48:51]\n\t"
    "v_mfma_f32_16x16x32_bf16 v[48:51], a[28:31], v[36:39], v[48:51]\n\t"
    "ds_read_b128 v[188:191], v2 offset:6144\n\t"
    "v_max3_f32 v17, v17, v32, v33\n\t"
    "v_mfma_f32_16x16x32_bf16 v[48:51], a[36:39], v[40:43], v[48:51]\n\t"
    "v_max3_f32 v16, v16, v34, v35\n\t"
    "v_max_f32_e32 v16, v16, v17\n\t"
    "v_mfma_f32_16x16x32_bf16 v[48:51], a[44:47], v[44:47], v[48:51]\n\t"
    "ds_read_b128 v[172:175], v0 offset:12288\n\t"
    "v_mov_b32_e32 v17, v16\n\t"
    "v_mfma_f32_16x16x32_bf16 v[52:55], a[48:51], v[220:223], 0\n\t"
    "v_mfma_f32_16x16x32_bf16 v[52:55], a[56:59], v[216:219], v[52:55]\n\t"
    "ds_read_b128 v[168:171], v1 offset:12288\n\t"
    "v_permlane32_swap_b32 v16, v17\n\t"
    "v_mfma_f32_16x16x32_bf16 v[52:55], a[64:67], v[212:215], v[52:55]\n\t"
    "v_max_f32_e32 v16, v16, v17\n\t"
    "v_mov_b32_e32 v17, v16\n\t"
    "v_mfma_f32_16x16x32_bf16 v[52:55], a[72:75], v[36:39], v[52:55]\n\t"
    "ds_read_b128 v[164:167], v2 offset:12288\n\t"
    "v_cvt_pk_bf16_f32 v14, v48, v49\n\t"
    "v_mfma_f32_16x16x32_bf16 v[52:55], a[80:83], v[40:43], v[52:55]\n\t"
    "v_cvt_pk_bf16_f32 v15, v50, v51\n\t"
    "v_permlane16_swap_b32 v16, v17\n\t"
    "v_mfma_f32_16x16x32_bf16 v[52:55], a[88:91], v[44:47], v[52:55]\n\t"
    "ds_read_b128 v[148:151], v0 offset:18432\n\t"
    "v_max_f32_e32 v16, v16, v17\n\t"
    "v_mfma_f32_16x16x32_bf16 v[56:59], a[52:55], v[220:223], 0\n\t"
    "v_mul_f32_e32 v16, s68, v16\n\t"
    "v_fma_f32 v20, v20, s68, -v16\n\t"
    "v_mfma_f32_16x16x32_bf16 v[56:59], a[60:63], v[216:219], v[56:59]\n\t"
    "ds_read_b128 v[144:147], v1 offset:18432\n\t"
    "v_fma_f32 v21, v21, s68, -v16\n\t"
    "v_mfma_f32_16x16x32_bf16 v[56:59], a[68:71], v[212:215], v[56:59]\n\t"
    "v_exp_f32_e32 v20, v20\n\t"
    "v_mfma_f32_16x16x32_bf16 v[56:59], a[76:79], v[36:39], v[56:59]\n\t"
    "ds_read_b128 v[140:143], v2 offset:18432\n\t"
    "v_cvt_pk_bf16_f32 v48, v52, v53\n\t"
    "v_mfma_f32_16x16x32_bf16 v[56:59], a[84:87], v[40:43], v[56:59]\n\t"
    "v_cvt_pk_bf16_f32 v49, v54, v55\n\t"
    "v_fma_f32 v22, v22, s68, -v16\n\t"
    "v_mfma_f32_16x16x32_bf16 v[56:59], a[92:95], v[44:47], v[56:59]\n\t"
    "v_exp_f32_e32 v21, v21\n\t"
    "v_mfma_f32_16x16x32_bf16 v[60:63], a[96:99], v[220:223], 0\n\t"
    "v_add_f32_e32 v18, v20, v21\n\t"
    "v_fma_f32 v23, v23, s68, -v16\n\t"
    "v_mfma_f32_16x16x32_bf16 v[60:63], a[104:107], v[216:219], v[60:63]\n\t"
    "v_exp_f32_e32 v22, v22\n\t"
    "v_mfma_f32_16x16x32_bf16 v[60:63], a[112:115], v[212:215], v[60:63]\n\t"
    "v_exp_f32_e32 v23, v23\n\t"
    "v_mfma_f32_16x16x32_bf16 v[60:63], a[120:123], v[36:39], v[60:63]\n\t"
    "v_cvt_pk_bf16_f32 v50, v56, v57\n\t"
    "v_cvt_pk_bf16_f32 v51, v58, v59\n\t"
    "v_mfma_f32_16x16x32_bf16 v[60:63], a[128:131], v[40:43], v[60:63]\n\t"
    "v_add_f32_e32 v19, v22, v23\n\t"
    "v_fma_f32 v24, v24, s68, -v16\n\t"
    "v_mfma_f32_16x16x32_bf16 v[60:63], a[136:139], v[44:47], v[60:63]\n\t"
    "v_exp_f32_e32 v24, v24\n\t"
    "v_mfma_f32_16x16x32_bf16 v[52:55], a[100:103], v[220:223], 0\n\t"
    "v_add_f32_e32 v18, v18, v24\n\t"
    "v_fma_f32 v25, v25, s68, -v16\n\t"
    "v_mfma_f32_16x16x32_bf16 v[52:55], a[108:111], v[216:219], v[52:55]\n\t"
    "v_exp_f32_e32 v25, v25\n\t"
    "v_mfma_f32_16x16x32_bf16 v[52:55], a[116:119], v[212:215], v[52:55]\n\t"
    "v_add_f32_e32 v18, v18, v25\n\t"
    "v_fma_f32 v26, v26, s68, -v16\n\t"
    "v_mfma_f32_16x16x32_bf16 v[52:55], a[124:127], v[36:39], v[52:55]\n\t"
    "v_cvt_pk_bf16_f32 v56, v60, v61\n\t"
    "v_cvt_pk_bf16_f32 v57, v62, v63\n\t"
    "v_mfma_f32_16x16x32_bf16 v[52:55], a[132:135], v[40:43], v[52:55]\n\t"
    "v_exp_f32_e32 v26, v26\n\t"
    "v_mfma_f32_16x16x32_bf16 v[52:55], a[140:143], v[44:47], v[52:55]\n\t"
    "v_add_f32_e32 v19, v19, v26\n\t"
    "v_fma_f32 v27, v27, s68, -v16\n\t"
    "v_mfma_f32_16x16x32_bf16 v[60:63], a[144:147], v[220:223], 0\n\t"
    "v_exp_f32_e32 v27, v27\n\t"
    "v_mfma_f32_16x16x32_bf16 v[60:63], a[152:155], v[216:219], v[60:63]\n\t"
    "v_add_f32_e32 v19, v19, v27\n\t"
    "v_fma_f32 v28, v28, s68, -v16\n\t"
    "v_mfma_f32_16x16x32_bf16 v[60:63], a[160:163], v[212:215], v[60:63]\n\t"
    "v_exp_f32_e32 v28, v28\n\t"
    "v_mfma_f32_16x16x32_bf16 v[60:63], a[168:171], v[36:39], v[60:63]\n\t"
    "v_cvt_pk_bf16_f32 v58, v52, v53\n\t"
    "v_cvt_pk_bf16_f32 v59, v54, v55\n\t"
    "v_mfma_f32_16x16x32_bf16 v[60:63], a[176:179], v[40:43], v[60:63]\n\t"
    "v_add_f32_e32 v18, v18, v28\n\t"
    "v_fma_f32 v29, v29, s68, -v16\n\t"
    "v_mfma_f32_16x16x32_bf16 v[60:63], a[184:187], v[44:47], v[60:63]\n\t"
    "v_exp_f32_e32 v29, v29\n\t"
    "v_mfma_f32_16x16x32_bf16 v[52:55], v[220:223], v[12:15], 0\n\t"
    "v_add_f32_e32 v18, v18, v29\n\t"
    "v_fma_f32 v30, v30, s68, -v16\n\t"
    "v_mfma_f32_16x16x32_bf16 v[52:55], v[216:219], v[48:51], v[52:55]\n\t"
    "v_exp_f32_e32 v30, v30\n\t"
    "v_mfma_f32_16x16x32_bf16 v[52:55], v[212:215], v[56:59], v[52:55]\n\t"
    "v_add_f32_e32 v19, v19, v30\n\t"
    "s_waitcnt lgkmcnt(0)\n\t"
    "v_mfma_f32_16x16x32_bf16 v[64:67], v[196:199], v[12:15], 0\n\t"
    "v_fma_f32 v31, v31, s68, -v16\n\t"
    "v_fma_f32 v32, v32, s68, -v16\n\t"
    "v_mfma_f32_16x16x32_bf16 v[64:67], v[192:195], v[48:51], v[64:67]\n\t"
    "v_exp_f32_e32 v31, v31\n\t"
    "v_mfma_f32_16x16x32_bf16 v[64:67], v[188:191], v[56:59], v[64:67]\n\t"
    "ds_read_b128 v[184:187], v3 offset:6144\n\t"
    "v_add_f32_e32 v19, v19, v31\n\t"
    "v_mfma_f32_16x16x32_bf16 v[68:71], v[172:175], v[12:15], 0\n\t"
    "v_exp_f32_e32 v32, v32\n\t"
    "v_mfma_f32_16x16x32_bf16 v[68:71], v[168:171], v[48:51], v[68:71]\n\t"
    "ds_read_b128 v[180:183], v4 offset:6144\n\t"
    "v_add_f32_e32 v18, v18, v32\n\t"
    "v_mfma_f32_16x16x32_bf16 v[68:71], v[164:167], v[56:59], v[68:71]\n\t"
    "v_fma_f32 v33, v33, s68, -v16\n\t"
    "v_fma_f32 v34, v34, s68, -v16\n\t"
    "v_mfma_f32_16x16x32_bf16 v[72:75], v[148:151], v[12:15], 0\n\t"
    "ds_read_b128 v[176:179], v5 offset:6144\n\t"
    "v_fma_f32 v35, v35, s68, -v16\n\t"
    "v_mfma_f32_16x16x32_bf16 v[72:75], v[144:147], v[48:51], v[72:75]\n\t"
    "v_exp_f32_e32 v33, v33\n\t"
    "v_mfma_f32_16x16x32_bf16 v[72:75], v[140:143], v[56:59], v[72:75]\n\t"
    "ds_read_b128 v[208:211], v3 offset:0\n\t"
    "v_add_f32_e32 v18, v18, v33\n\t"
    "v_mfma_f32_16x16x32_bf16 v[12:15], a[148:151], v[220:223], 0\n\t"
    "v_exp_f32_e32 v34, v34\n\t"
    "v_mfma_f32_16x16x32_bf16 v[12:15], a[156:159], v[216:219], v[12:15]\n\t"
    "ds_read_b128 v[204:207], v4 offset:0\n\t"
    "v_add_f32_e32 v19, v19, v34\n\t"
    "v_mfma_f32_16x16x32_bf16 v[12:15], a[164:167], v[212:215], v[12:15]\n\t"
    "v_exp_f32_e32 v35, v35\n\t"
    "v_mfma_f32_16x16x32_bf16 v[12:15], a[172:175], v[36:39], v[12:15]\n\t"
    "ds_read_b128 v[200:203], v5 offset:0\n\t"
    "v_add_f32_e32 v19, v19, v35\n\t"
    "v_mfma_f32_16x16x32_bf16 v[12:15], a[180:183], v[40:43], v[12:15]\n\t"
    "v_add_f32_e32 v18, v18, v19\n\t"
    "v_mov_b32_e32 v19, v18\n\t"
    "v_mfma_f32_16x16x32_bf16 v[12:15], a[188:191], v[44:47], v[12:15]\n\t"
    "ds_read_b128 v[160:163], v3 offset:12288\n\t"
    "v_cvt_pk_bf16_f32 v48, v60, v61\n\t"
    "v_mfma_f32_16x16x32_bf16 v[56:59], a[192:195], v[220:223], 0\n\t"
    "v_permlane32_swap_b32 v18, v19\n\t"
    "v_add_f32_e32 v18, v18, v19\n\t"
    "v_mfma_f32_16x16x32_bf16 v[56:59], a[200:203], v[216:219], v[56:59]\n\t"
    "ds_read_b128 v[156:159], v4 offset:12288\n\t"
    "v_mov_b32_e32 v19, v18\n\t"
    "v_mfma_f32_16x16x32_bf16 v[56:59], a[208:211], v[212:215], v[56:59]\n\t"
    "v_cvt_pk_bf16_f32 v49, v62, v63\n\t"
    ".Lh2_%=:\n\t"
    "s_cbranch_execz .Lh3_%=\n\t"
    "v_mfma_f32_16x16x32_bf16 v[56:59], a[216:219], v[36:39], v[56:59]\n\t"
    "ds_read_b128 v[152:155], v5 offset:12288\n\t"
    "v_cvt_pk_bf16_f32 v50, v12, v13\n\t"
    "v_mfma_f32_16x16x32_bf16 v[56:59], a[224:227], v[40:43], v[56:59]\n\t"
    "v_cvt_pk_bf16_f32 v51, v14, v15\n\t"
    "v_permlane16_swap_b32 v18, v19\n\t"
    "v_mfma_f32_16x16x32_bf16 v[56:59], a[232:235], v[44:47], v[56:59]\n\t"
    "ds_read_b128 v[136:139], v3 offset:18432\n\t"
    "v_add_f32_e32 v18, v18, v19\n\t"
    "v_mfma_f32_16x16x32_bf16 v[60:63], a[196:199], v[220:223], 0\n\t"
    "v_rcp_f32_e32 v18, v18\n\t"
    "v_mfma_f32_16x16x32_bf16 v[60:63], a[204:207], v[216:219], v[60:63]\n\t"
    "ds_read_b128 v[132:135], v4 offset:18432\n\t"
    "v_mul_f32_e32 v20, v20, v18\n\t"
    "v_mfma_f32_16x16x32_bf16 v[60:63], a[212:215], v[212:215], v[60:63]\n\t"
    "v_mul_f32_e32 v21, v21, v18\n\t"
    "v_cvt_pk_bf16_f32 v12, v20, v21\n\t"
    "v_mfma_f32_16x16x32_bf16 v[60:63], a[220:223], v[36:39], v[60:63]\n\t"
    "ds_read_b128 v[128:131], v5 offset:18432\n\t"
    "v_cvt_pk_bf16_f32 v76, v56, v57\n\t"
    "v_mfma_f32_16x16x32_bf16 v[60:63], a[228:231], v[40:43], v[60:63]\n\t"
    "v_cvt_pk_bf16_f32 v77, v58, v59\n\t"
    "v_mul_f32_e32 v22, v22, v18\n\t"
    "v_mfma_f32_16x16x32_bf16 v[60:63], a[236:239], v[44:47], v[60:63]\n\t"
    "v_mul_f32_e32 v23, v23, v18\n\t"
    "v_cvt_pk_bf16_f32 v13, v22, v23\n\t"
    "v_mfma_f32_16x16x32_bf16 v[80:83], a[240:243], v[220:223], 0\n\t"
    "v_mul_f32_e32 v24, v24, v18\n\t"
    "v_mul_f32_e32 v25, v25, v18\n\t"
    "v_mfma_f32_16x16x32_bf16 v[80:83], a[248:251], v[216:219], v[80:83]\n\t"
    "v_cvt_pk_bf16_f32 v14, v24, v25\n\t"
    "v_mul_f32_e32 v26, v26, v18\n\t"
    "v_mfma_f32_16x16x32_bf16 v[80:83], v[224:227], v[212:215], v[80:83]\n\t"
    "v_mul_f32_e32 v27, v27, v18\n\t"
    "v_cvt_pk_bf16_f32 v15, v26, v27\n\t"
    "v_mfma_f32_16x16x32_bf16 v[80:83], v[232:235], v[36:39], v[80:83]\n\t"
    "v_cvt_pk_bf16_f32 v78, v60, v61\n\t"
    "v_cvt_pk_bf16_f32 v79, v62, v63\n\t"
    "v_mfma_f32_16x16x32_bf16 v[80:83], v[240:243], v[40:43], v[80:83]\n\t"
    "v_mul_f32_e32 v28, v28, v18\n\t"
    "v_mul_f32_e32 v29, v29, v18\n\t"
    "v_mfma_f32_16x16x32_bf16 v[80:83], v[248:251], v[44:47], v[80:83]\n\t"
    "v_cvt_pk_bf16_f32 v20, v28, v29\n\t"
    "v_mul_f32_e32 v30, v30, v18\n\t"
    "v_mfma_f32_16x16x32_bf16 v[24:27], a[244:247], v[220:223], 0\n\t"
    "v_mul_f32_e32 v31, v31, v18\n\t"
    "v_cvt_pk_bf16_f32 v21, v30, v31\n\t"
    "v_mfma_f32_16x16x32_bf16 v[24:27], a[252:255], v[216:219], v[24:27]\n\t"
    "v_mul_f32_e32 v32, v32, v18\n\t"
    "v_mul_f32_e32 v33, v33, v18\n\t"
    "v_mfma_f32_16x16x32_bf16 v[24:27], v[228:231], v[212:215], v[24:27]\n\t"
    "v_cvt_pk_bf16_f32 v22, v32, v33\n\t"
    "v_mul_f32_e32 v34, v34, v18\n\t"
    "v_mfma_f32_16x16x32_bf16 v[24:27], v[236:239], v[36:39], v[24:27]\n\t"
    "v_cvt_pk_bf16_f32 v28, v80, v81\n\t"
    "v_cvt_pk_bf16_f32 v29, v82, v83\n\t"
    "v_mfma_f32_16x16x32_bf16 v[24:27], v[244:247], v[40:43], v[24:27]\n\t"
    "v_mul_f32_e32 v35, v35, v18\n\t"
    "v_cvt_pk_bf16_f32 v23, v34, v35\n\t"
    "v_mfma_f32_16x16x32_bf16 v[24:27], v[252:255], v[44:47], v[24:27]\n\t"
    "v_mfma_f32_16x16x32_bf16 v[36:39], a[0:3], v[196:199], 0\n\t"
    "ds_write_b128 v6, v[12:15] offset:6144\n\t"
    "v_mfma_f32_16x16x32_bf16 v[36:39], a[8:11], v[192:195], v[36:39]\n\t"
    "v_mfma_f32_16x16x32_bf16 v[36:39], a[16:19], v[188:191], v[36:39]\n\t"
    "s_waitcnt lgkmcnt(1)\n\t"
    "v_mfma_f32_16x16x32_bf16 v[36:39], a[24:27], v[184:187], v[36:39]\n\t"
    "v_cvt_pk_bf16_f32 v30, v24, v25\n\t"
    "v_cvt_pk_bf16_f32 v31, v26, v27\n\t"
    "v_mfma_f32_16x16x32_bf16 v[36:39], a[32:35], v[180:183], v[36:39]\n\t"
    "ds_write_b128 v6, v[20:23] offset:7168\n\t"
    "v_mfma_f32_16x16x32_bf16 v[36:39], a[40:43], v[176:179], v[36:39]\n\t"
    "v_mfma_f32_16x16x32_bf16 v[52:55], v[208:211], v[48:51], v[52:55]\n\t"
    "v_mfma_f32_16x16x32_bf16 v[52:55], v[204:207], v[76:79], v[52:55]\n\t"
    "v_mfma_f32_16x16x32_bf16 v[52:55], v[200:203], v[28:31], v[52:55]\n\t"
    "v_mfma_f32_16x16x32_bf16 v[64:67], v[184:187], v[48:51], v[64:67]\n\t"
    "v_cvt_pk_bf16_f32 v12, v36, v37\n\t"
    "v_cvt_pk_bf16_f32 v13, v38, v39\n\t"
    "v_mfma_f32_16x16x32_bf16 v[64:67], v[180:183], v[76:79], v[64:67]\n\t"
    "s_waitcnt lgkmcnt(0)\n\t"
    "s_barrier\n\t"
    "v_mfma_f32_16x16x32_bf16 v[64:67], v[176:179], v[28:31], v[64:67]\n\t"
    "v_mfma_f32_16x16x32_bf16 v[68:71], v[160:163], v[48:51], v[68:71]\n\t"
    "v_max3_f32 v16, v52, v53, v54\n\t"
    "v_mfma_f32_16x16x32_bf16 v[68:71], v[156:159], v[76:79], v[68:71]\n\t"
    "v_mfma_f32_16x16x32_bf16 v[68:71], v[152:155], v[28:31], v[68:71]\n\t"
    "v_mfma_f32_16x16x32_bf16 v[72:75], v[136:139], v[48:51], v[72:75]\n\t"
    "v_max3_f32 v17, v55, v64, v65\n\t"
    "v_max3_f32 v16, v16, v66, v67\n\t"
    "v_mfma_f32_16x16x32_bf16 v[72:75], v[132:135], v[76:79], v[72:75]\n\t"
    "v_mfma_f32_16x16x32_bf16 v[72:75], v[128:131], v[28:31], v[72:75]\n\t"
    "v_mfma_f32_16x16x32_bf16 v[20:23], a[4:7], v[196:199], 0\n\t"
    "v_max3_f32 v17, v17, v68, v69\n\t"
    "v_max3_f32 v16, v16, v70, v71\n\t"
    "v_mfma_f32_16x16x32_bf16 v[20:23], a[12:15], v[192:195], v[20:23]\n\t"
    "v_mfma_f32_16x16x32_bf16 v[20:23], a[20:23], v[188:191], v[20:23]\n\t"
    "v_mfma_f32_16x16x32_bf16 v[20:23], a[28:31], v[184:187], v[20:23]\n\t"
    "s_add_u32 m0, s63, 12288\n\t"
    "v_max3_f32 v17, v17, v72, v73\n\t"
    "v_mfma_f32_16x16x32_bf16 v[20:23], a[36:39], v[180:183], v[20:23]\n\t"
    "buffer_load_dwordx4 v7, s[52:55], s61 offen offset:0 nt lds\n\t"
    "v_max3_f32 v16, v16, v74, v75\n\t"
    "v_mfma_f32_16x16x32_bf16 v[20:23], a[44:47], v[176:179], v[20:23]\n\t"
    "v_max_f32_e32 v16, v16, v17\n\t"
    "v_mov_b32_e32 v17, v16\n\t"
    "v_mfma_f32_16x16x32_bf16 v[24:27], a[48:51], v[196:199], 0\n\t"
    "v_mfma_f32_16x16x32_bf16 v[24:27], a[56:59], v[192:195], v[24:27]\n\t"
    "v_permlane32_swap_b32 v16, v17\n\t"
    "v_max_f32_e32 v16, v16, v17\n\t"
    "v_mfma_f32_16x16x32_bf16 v[24:27], a[64:67], v[188:191], v[24:27]\n\t"
    "v_mov_b32_e32 v17, v16\n\t"
    "v_mfma_f32_16x16x32_bf16 v[24:27], a[72:75], v[184:187], v[24:27]\n\t"
    "v_cvt_pk_bf16_f32 v14, v20, v21\n\t"
    "v_cvt_pk_bf16_f32 v15, v22, v23\n\t"
    "v_mfma_f32_16x16x32_bf16 v[24:27], a[80:83], v[180:183], v[24:27]\n\t"
    "v_permlane16_swap_b32 v16, v17\n\t"
    "v_max_f32_e32 v16, v16, v17\n\t"
    "v_mfma_f32_16x16x32_bf16 v[24:27], a[88:91], v[176:179], v[24:27]\n\t"
    "v_mul_f32_e32 v16, s68, v16\n\t"
    "v_fma_f32 v52, v52, s68, -v16\n\t"
    "v_mfma_f32_16x16x32_bf16 v[20:23], a[52:55], v[196:199], 0\n\t"
    "v_exp_f32_e32 v52, v52\n\t"
    "v_mfma_f32_16x16x32_bf16 v[20:23], a[60:63], v[192:195], v[20:23]\n\t"
    "v_fma_f32 v53, v53, s68, -v16\n\t"
    "v_fma_f32 v54, v54, s68, -v16\n\t"
    "v_mfma_f32_16x16x32_bf16 v[20:23], a[68:71], v[188:191], v[20:23]\n\t"
    "v_exp_f32_e32 v53, v53\n\t"
    "v_mfma_f32_16x16x32_bf16 v[20:23], a[76:79], v[184:187], v[20:23]\n\t"
    "buffer_load_dwordx4 v7, s[52:55], s61 offen offset:1024 nt lds\n\t"
    "v_cvt_pk_bf16_f32 v28, v24, v25\n\t"
    "v_mfma_f32_16x16x32_bf16 v[20:23], a[84:87], v[180:183], v[20:23]\n\t"
    "v_cvt_pk_bf16_f32 v29, v26, v27\n\t"
    "v_add_f32_e32 v18, v52, v53\n\t"
    "v_mfma_f32_16x16x32_bf16 v[20:23], a[92:95], v[176:179], v[20:23]\n\t"
    "v_exp_f32_e32 v54, v54\n\t"
    "v_mfma_f32_16x16x32_bf16 v[32:35], a[96:99], v[196:199], 0\n\t"
    "v_fma_f32 v55, v55, s68, -v16\n\t"
    "v_fma_f32 v64, v64, s68, -v16\n\t"
    "v_mfma_f32_16x16x32_bf16 v[32:35], a[104:107], v[192:195], v[32:35]\n\t"
    "v_exp_f32_e32 v55, v55\n\t"
    "v_mfma_f32_16x16x32_bf16 v[32:35], a[112:115], v[188:191], v[32:35]\n\t"
    "v_add_f32_e32 v19, v54, v55\n\t"
    "v_fma_f32 v65, v65, s68, -v16\n\t"
    "v_mfma_f32_16x16x32_bf16 v[32:35], a[120:123], v[184:187], v[32:35]\n\t"
    "v_cvt_pk_bf16_f32 v30, v20, v21\n\t"
    "v_cvt_pk_bf16_f32 v31, v22, v23\n\t"
    "v_mfma_f32_16x16x32_bf16 v[32:35], a[128:131], v[180:183], v[32:35]\n\t"
    "v_exp_f32_e32 v64, v64\n\t"
    "v_mfma_f32_16x16x32_bf16 v[32:35], a[136:139], v[176:179], v[32:35]\n\t"
    "v_add_f32_e32 v18, v18, v64\n\t"
    "v_fma_f32 v66, v66, s68, -v16\n\t"
    "v_mfma_f32_16x16x32_bf16 v[20:23], a[100:103], v[196:199], 0\n\t"
    "v_exp_f32_e32 v65, v65\n\t"
    "v_mfma_f32_16x16x32_bf16 v[20:23], a[108:111], v[192:195], v[20:23]\n\t"
    "v_add_f32_e32 v18, v18, v65\n\t"
    "v_fma_f32 v67, v67, s68, -v16\n\t"
    "v_mfma_f32_16x16x32_bf16 v[20:23], a[116:119], v[188:191], v[20:23]\n\t"
    "buffer_load_dwordx4 v7, s[52:55], s61 offen offset:2048 nt lds\n\t"
    "v_fma_f32 v68, v68, s68, -v16\n\t"
    "v_mfma_f32_16x16x32_bf16 v[20:23], a[124:127], v[184:187], v[20:23]\n\t"
    "s_add_u32 s61, s61, 196608\n\t"
    "v_cvt_pk_bf16_f32 v24, v32, v33\n\t"
    "v_mfma_f32_16x16x32_bf16 v[20:23], a[132:135], v[180:183], v[20:23]\n\t"
    "v_cvt_pk_bf16_f32 v25, v34, v35\n\t"
    "v_fma_f32 v69, v69, s68, -v16\n\t"
    "v_mfma_f32_16x16x32_bf16 v[20:23], a[140:143], v[176:179], v[20:23]\n\t"
    "v_exp_f32_e32 v66, v66\n\t"
    "v_mfma_f32_16x16x32_bf16 v[36:39], a[144:147], v[196:199], 0\n\t"
    "v_add_f32_e32 v19, v19, v66\n\t"
    "v_fma_f32 v70, v70, s68, -v16\n\t"
    "v_mfma_f32_16x16x32_bf16 v[36:39], a[152:155], v[192:195], v[36:39]\n\t"
    "v_exp_f32_e32 v67, v67\n\t"
    "v_mfma_f32_16x16x32_bf16 v[36:39], a[160:163], v[188:191], v[36:39]\n\t"
    "v_add_f32_e32 v19, v19, v67\n\t"
    "v_fma_f32 v71, v71, s68, -v16\n\t"
    "v_mfma_f32_16x16x32_bf16 v[36:39], a[168:171], v[184:187], v[36:39]\n\t"
    "v_cvt_pk_bf16_f32 v26, v20, v21\n\t"
    "v_cvt_pk_bf16_f32 v27, v22, v23\n\t"
    "v_mfma_f32_16x16x32_bf16 v[36:39], a[176:179], v[180:183], v[36:39]\n\t"
    "v_exp_f32_e32 v68, v68\n\t"
    "v_mfma_f32_16x16x32_bf16 v[36:39], a[184:187], v[176:179], v[36:39]\n\t"
    "v_add_f32_e32 v18, v18, v68\n\t"
    "v_fma_f32 v72, v72, s68, -v16\n\t"
    ".Lmid_%=:\n\t"
    "v_mfma_f32_16x16x32_bf16 v[20:23], v[220:223], v[12:15], 0\n\t"
    "s_add_u32 m0, s63, 15360\n\t"
    "v_fma_f32 v73, v73, s68, -v16\n\t"
    "v_mfma_f32_16x16x32_bf16 v[20:23], v[216:219], v[28:31], v[20:23]\n\t"
    "buffer_load_dwordx4 v7, s[52:55], s61 offen offset:0 nt lds\n\t"
    "v_fma_f32 v74, v74, s68, -v16\n\t"
    "v_mfma_f32_16x16x32_bf16 v[20:23], v[212:215], v[24:27], v[20:23]\n\t"
    "v_exp_f32_e32 v69, v69\n\t"
    "v_mfma_f32_16x16x32_bf16 v[32:35], v[196:199], v[12:15], 0\n\t"
    "v_add_f32_e32 v18, v18, v69\n\t"
    "v_fma_f32 v75, v75, s68, -v16\n\t"
    "v_mfma_f32_16x16x32_bf16 v[32:35], v[192:195], v[28:31], v[32:35]\n\t"
    "v_exp_f32_e32 v70, v70\n\t"
    "v_mfma_f32_16x16x32_bf16 v[32:35], v[188:191], v[24:27], v[32:35]\n\t"
    "v_add_f32_e32 v19, v19, v70\n\t"
    "v_cvt_pk_bf16_f32 v40, v36, v37\n\t"
    "v_mfma_f32_16x16x32_bf16 v[44:47], v[172:175], v[12:15], 0\n\t"
    "v_exp_f32_e32 v71, v71\n\t"
    "v_mfma_f32_16x16x32_bf16 v[44:47], v[168:171], v[28:31], v[44:47]\n\t"
    "v_add_f32_e32 v19, v19, v71\n\t"
    "v_cvt_pk_bf16_f32 v41, v38, v39\n\t"
    "v_mfma_f32_16x16x32_bf16 v[44:47], v[164:167], v[24:27], v[44:47]\n\t"
    "v_exp_f32_e32 v72, v72\n\t"
    "v_mfma_f32_16x16x32_bf16 v[48:51], v[148:151], v[12:15], 0\n\t"
    "v_add_f32_e32 v18, v18, v72\n\t"
    "v_mfma_f32_16x16x32_bf16 v[48:51], v[144:147], v[28:31], v[48:51]\n\t"
    "v_exp_f32_e32 v73, v73\n\t"
    "v_mfma_f32_16x16x32_bf16 v[48:51], v[140:143], v[24:27], v[48:51]\n\t"
    "v_add_f32_e32 v18, v18, v73\n\t"
    "v_mfma_f32_16x16x32_bf16 v[12:15], a[148:151], v[196:199], 0\n\t"
    "buffer_load_dwordx4 v7, s[52:55], s61 offen offset:1024 nt lds\n\t"
    "v_mfma_f32_16x16x32_bf16 v[12:15], a[156:159], v[192:195], v[12:15]\n\t"
    "v_exp_f32_e32 v74, v74\n\t"
    "v_mfma_f32_16x16x32_bf16 v[12:15], a[164:167], v[188:191], v[12:15]\n\t"
    "v_add_f32_e32 v19, v19, v74\n\t"
    "v_mfma_f32_16x16x32_bf16 v[12:15], a[172:175], v[184:187], v[12:15]\n\t"
    "v_exp_f32_e32 v75, v75\n\t"
    "v_mfma_f32_16x16x32_bf16 v[12:15], a[180:183], v[180:183], v[12:15]\n\t"
    "v_add_f32_e32 v19, v19, v75\n\t"
    "v_add_f32_e32 v18, v18, v19\n\t"
    "v_mfma_f32_16x16x32_bf16 v[12:15], a[188:191], v[176:179], v[12:15]\n\t"
    "v_mov_b32_e32 v19, v18\n\t"
    "v_mfma_f32_16x16x32_bf16 v[24:27], a[192:195], v[196:199], 0\n\t"
    "v_mfma_f32_16x16x32_bf16 v[24:27], a[200:203], v[192:195], v[24:27]\n\t"
    "v_permlane32_swap_b32 v18, v19\n\t"
    "v_add_f32_e32 v18, v18, v19\n\t"
    "v_mfma_f32_16x16x32_bf16 v[24:27], a[208:211], v[188:191], v[24:27]\n\t"
    "v_mov_b32_e32 v19, v18\n\t"
    "v_mfma_f32_16x16x32_bf16 v[24:27], a[216:219], v[184:187], v[24:27]\n\t"
    "v_cvt_pk_bf16_f32 v42, v12, v13\n\t"
    "v_cvt_pk_bf16_f32 v43, v14, v15\n\t"
    "v_mfma_f32_16x16x32_bf16 v[24:27], a[224:227], v[180:183], v[24:27]\n\t"
    "v_permlane16_swap_b32 v18, v19\n\t"
    "v_add_f32_e32 v18, v18, v19\n\t"
    "v_mfma_f32_16x16x32_bf16 v[24:27], a[232:235], v[176:179], v[24:27]\n\t"
    "buffer_load_dwordx4 v7, s[52:55], s61 offen offset:2048 nt lds\n\t"
    "v_mfma_f32_16x16x32_bf16 v[12:15], a[196:199], v[196:199], 0\n\t"
    "s_add_u32 s61, s61, 196608\n\t"
    "v_mfma_f32_16x16x32_bf16 v[12:15], a[204:207], v[192:195], v[12:15]\n\t"
    "v_rcp_f32_e32 v18, v18\n\t"
    "v_mfma_f32_16x16x32_bf16 v[12:15], a[212:215], v[188:191], v[12:15]\n\t"
    "v_mul_f32_e32 v52, v52, v18\n\t"
    "v_mul_f32_e32 v53, v53, v18\n\t"
    "v_mfma_f32_16x16x32_bf16 v[12:15], a[220:223], v[184:187], v[12:15]\n\t"
    "v_cvt_pk_bf16_f32 v28, v24, v25\n\t"
    "v_cvt_pk_bf16_f32 v29, v26, v27\n\t"
    "v_mfma_f32_16x16x32_bf16 v[12:15], a[228:231], v[180:183], v[12:15]\n\t"
    "v_cvt_pk_bf16_f32 v36, v52, v53\n\t"
    "v_mul_f32_e32 v54, v54, v18\n\t"
    "v_mfma_f32_16x16x32_bf16 v[12:15], a[236:239], v[176:179], v[12:15]\n\t"
    "v_mul_f32_e32 v55, v55, v18\n\t"
    "v_cvt_pk_bf16_f32 v37, v54, v55\n\t"
    "v_mfma_f32_16x16x32_bf16 v[24:27], a[240:243], v[196:199], 0\n\t"
    "v_mul_f32_e32 v64, v64, v18\n\t"
    "v_mul_f32_e32 v65, v65, v18\n\t"
    "v_mfma_f32_16x16x32_bf16 v[24:27], a[248:251], v[192:195], v[24:27]\n\t"
    "v_cvt_pk_bf16_f32 v38, v64, v65\n\t"
    "v_mul_f32_e32 v66, v66, v18\n\t"
    "v_mfma_f32_16x16x32_bf16 v[24:27], v[224:227], v[188:191], v[24:27]\n\t"
    "v_mul_f32_e32 v67, v67, v18\n\t"
    "v_cvt_pk_bf16_f32 v39, v66, v67\n\t"
    "v_mfma_f32_16x16x32_bf16 v[24:27], v[232:235], v[184:187], v[24:27]\n\t"
    "s_add_u32 m0, s63, 18432\n\t"
    "v_cvt_pk_bf16_f32 v30, v12, v13\n\t"
    "v_mfma_f32_16x16x32_bf16 v[24:27], v[240:243], v[180:183], v[24:27]\n\t"
    "buffer_load_dwordx4 v7, s[52:55], s61 offen offset:0 nt lds\n\t"
    "v_cvt_pk_bf16_f32 v31, v14, v15\n\t"
    "v_mfma_f32_16x16x32_bf16 v[24:27], v[248:251], v[176:179], v[24:27]\n\t"
    "v_mul_f32_e32 v68, v68, v18\n\t"
    "v_mul_f32_e32 v69, v69, v18\n\t"
    "v_mfma_f32_16x16x32_bf16 v[52:55], a[244:247], v[196:199], 0\n\t"
    "v_cvt_pk_bf16_f32 v56, v68, v69\n\t"
    "v_mul_f32_e32 v70, v70, v18\n\t"
    "v_mfma_f32_16x16x32_bf16 v[52:55], a[252:255], v[192:195], v[52:55]\n\t"
    "v_mul_f32_e32 v71, v71, v18\n\t"
    "v_cvt_pk_bf16_f32 v57, v70, v71\n\t"
    "v_mfma_f32_16x16x32_bf16 v[52:55], v[228:231], v[188:191], v[52:55]\n\t"
    "v_mul_f32_e32 v72, v72, v18\n\t"
    "v_mul_f32_e32 v73, v73, v18\n\t"
    "v_mfma_f32_16x16x32_bf16 v[52:55], v[236:239], v[184:187], v[52:55]\n\t"
    "v_cvt_pk_bf16_f32 v12, v24, v25\n\t"
    "v_cvt_pk_bf16_f32 v13, v26, v27\n\t"
    "v_mfma_f32_16x16x32_bf16 v[52:55], v[244:247], v[180:183], v[52:55]\n\t"
    "v_cvt_pk_bf16_f32 v58, v72, v73\n\t"
    "v_mul_f32_e32 v74, v74, v18\n\t"
    "v_mfma_f32_16x16x32_bf16 v[52:55], v[252:255], v[176:179], v[52:55]\n\t"
    "v_mul_f32_e32 v75, v75, v18\n\t"
    "v_cvt_pk_bf16_f32 v59, v74, v75\n\t"
    "v_mfma_f32_16x16x32_bf16 v[24:27], a[0:3], v[172:175], 0\n\t"
    "ds_write_b128 v6, v[36:39] offset:0\n\t"
    "v_mfma_f32_16x16x32_bf16 v[24:27], a[8:11], v[168:171], v[24:27]\n\t"
    "v_mfma_f32_16x16x32_bf16 v[24:27], a[16:19], v[164:167], v[24:27]\n\t"
    "v_mfma_f32_16x16x32_bf16 v[24:27], a[24:27], v[160:163], v[24:27]\n\t"
    "buffer_load_dwordx4 v7, s[52:55], s61 offen offset:1024 nt lds\n\t"
    "v_cvt_pk_bf16_f32 v14, v52, v53\n\t"
    "v_mfma_f32_16x16x32_bf16 v[24:27], a[32:35], v[156:159], v[24:27]\n\t"
    "ds_write_b128 v6, v[56:59] offset:1024\n\t"
    "v_mfma_f32_16x16x32_bf16 v[24:27], a[40:43], v[152:155], v[24:27]\n\t"
    "v_cvt_pk_bf16_f32 v15, v54, v55\n\t"
    "v_mfma_f32_16x16x32_bf16 v[20:23], v[208:211], v[40:43], v[20:23]\n\t"
    "v_mfma_f32_16x16x32_bf16 v[20:23], v[204:207], v[28:31], v[20:23]\n\t"
    "v_mfma_f32_16x16x32_bf16 v[20:23], v[200:203], v[12:15], v[20:23]\n\t"
    "s_add_i32 s64, s62, %[rd]\n\t"
    "v_mfma_f32_16x16x32_bf16 v[32:35], v[184:187], v[40:43], v[32:35]\n\t"
    "s_cmp_eq_u32 s64, %[rend]\n\t"
    "s_cselect_b32 s64, %[rbeg], s64\n\t"
    "v_mfma_f32_16x16x32_bf16 v[32:35], v[180:183], v[28:31], v[32:35]\n\t"
    "s_sub_i32 s65, s63, s64\n\t"
    "v_cvt_pk_bf16_f32 v16, v24, v25\n\t"
    "v_mfma_f32_16x16x32_bf16 v[32:35], v[176:179], v[12:15], v[32:35]\n\t"
    "v_add_u32_e32 v0, s65, v0\n\t"
    "v_add_u32_e32 v1, s65, v1\n\t"
    "v_mfma_f32_16x16x32_bf16 v[44:47], v[160:163], v[40:43], v[44:47]\n\t"
    "v_add_u32_e32 v2, s65, v2\n\t"
    "v_add_u32_e32 v3, s65, v3\n\t"
    "v_mfma_f32_16x16x32_bf16 v[44:47], v[156:159], v[28:31], v[44:47]\n\t"
    "v_add_u32_e32 v4, s65, v4\n\t"
    "v_add_u32_e32 v5, s65, v5\n\t"
    "v_mfma_f32_16x16x32_bf16 v[44:47], v[152:155], v[12:15], v[44:47]\n\t"
    "buffer_load_dwordx4 v7, s[52:55], s61 offen offset:2048 nt lds\n\t"
    "v_max3_f32 v36, v20, v21, v22\n\t"
    "v_mfma_f32_16x16x32_bf16 v[48:51], v[136:139], v[40:43], v[48:51]\n\t"
    "s_add_u32 s61, s61, 196608\n\t"
    "v_max3_f32 v37, v23, v32, v33\n\t"
    "v_mfma_f32_16x16x32_bf16 v[48:51], v[132:135], v[28:31], v[48:51]\n\t"
    "v_max3_f32 v36, v36, v34, v35\n\t"
    "v_cvt_pk_bf16_f32 v17, v26, v27\n\t"
    "v_mfma_f32_16x16x32_bf16 v[48:51], v[128:131], v[12:15], v[48:51]\n\t"
    "v_mfma_f32_16x16x32_bf16 v[40:43], a[4:7], v[172:175], 0\n\t"
    "v_max3_f32 v37, v37, v44, v45\n\t"
    "v_max3_f32 v36, v36, v46, v47\n\t"
    "v_mfma_f32_16x16x32_bf16 v[40:43], a[12:15], v[168:171], v[40:43]\n\t"
    "v_mfma_f32_16x16x32_bf16 v[40:43], a[20:23], v[164:167], v[40:43]\n\t"
    "v_mfma_f32_16x16x32_bf16 v[40:43], a[28:31], v[160:163], v[40:43]\n\t"
    "v_max3_f32 v37, v37, v48, v49\n\t"
    "v_max3_f32 v36, v36, v50, v51\n\t"
    "v_mfma_f32_16x16x32_bf16 v[40:43], a[36:39], v[156:159], v[40:43]\n\t"
    "v_max_f32_e32 v36, v36, v37\n\t"
    "v_mov_b32_e32 v37, v36\n\t"
    "v_mfma_f32_16x16x32_bf16 v[40:43], a[44:47], v[152:155], v[40:43]\n\t"
    "v_mfma_f32_16x16x32_bf16 v[12:15], a[48:51], v[172:175], 0\n\t"
    "s_add_u32 m0, s63, 21504\n\t"
    "v_permlane32_swap_b32 v36, v37\n\t"
    "v_mfma_f32_16x16x32_bf16 v[12:15], a[56:59], v[168:171], v[12:15]\n\t"
    "buffer_load_dwordx4 v7, s[52:55], s61 offen offset:0 nt lds\n\t"
    "v_max_f32_e32 v36, v36, v37\n\t"
    "v_mfma_f32_16x16x32_bf16 v[12:15], a[64:67], v[164:167], v[12:15]\n\t"
    "v_mov_b32_e32 v37, v36\n\t"
    "v_mfma_f32_16x16x32_bf16 v[12:15], a[72:75], v[160:163], v[12:15]\n\t"
    "v_cvt_pk_bf16_f32 v18, v40, v41\n\t"
    "v_cvt_pk_bf16_f32 v19, v42, v43\n\t"
    "v_mfma_f32_16x16x32_bf16 v[12:15], a[80:83], v[156:159], v[12:15]\n\t"
    "v_permlane16_swap_b32 v36, v37\n\t"
    "v_max_f32_e32 v36, v36, v37\n\t"
    "v_mfma_f32_16x16x32_bf16 v[12:15], a[88:91], v[152:155], v[12:15]\n\t"
    "v_mul_f32_e32 v36, s68, v36\n\t"
    "v_fma_f32 v20, v20, s68, -v36\n\t"
    "v_mfma_f32_16x16x32_bf16 v[24:27], a[52:55], v[172:175], 0\n\t"
    "v_exp_f32_e32 v20, v20\n\t"
    "v_mfma_f32_16x16x32_bf16 v[24:27], a[60:63], v[168:171], v[24:27]\n\t"
    "v_fma_f32 v21, v21, s68, -v36\n\t"
    "v_fma_f32 v22, v22, s68, -v36\n\t"
    "v_mfma_f32_16x16x32_bf16 v[24:27], a[68:71], v[164:167], v[24:27]\n\t"
    "v_exp_f32_e32 v21, v21\n\t"
    "v_mfma_f32_16x16x32_bf16 v[24:27], a[76:79], v[160:163], v[24:27]\n\t"
    "v_cvt_pk_bf16_f32 v28, v12, v13\n\t"
    "v_cvt_pk_bf16_f32 v29, v14, v15\n\t"
    "v_mfma_f32_16x16x32_bf16 v[24:27], a[84:87], v[156:159], v[24:27]\n\t"
    "v_add_f32_e32 v38, v20, v21\n\t"
    "v_fma_f32 v23, v23, s68, -v36\n\t"
    "v_mfma_f32_16x16x32_bf16 v[24:27], a[92:95], v[152:155], v[24:27]\n\t"
    "v_exp_f32_e32 v22, v22\n\t"
    "v_mfma_f32_16x16x32_bf16 v[12:15], a[96:99], v[172:175], 0\n\t"
    "buffer_load_dwordx4 v7, s[52:55], s61 offen offset:1024 nt lds\n\t"
    "v_fma_f32 v32, v32, s68, -v36\n\t"
    "v_mfma_f32_16x16x32_bf16 v[12:15], a[104:107], v[168:171], v[12:15]\n\t"
    "v_exp_f32_e32 v23, v23\n\t"
    "v_mfma_f32_16x16x32_bf16 v[12:15], a[112:115], v[164:167], v[12:15]\n\t"
    "v_add_f32_e32 v39, v22, v23\n\t"
    "v_fma_f32 v33, v33, s68, -v36\n\t"
    "v_mfma_f32_16x16x32_bf16 v[12:15], a[120:123], v[160:163], v[12:15]\n\t"
    "v_cvt_pk_bf16_f32 v30, v24, v25\n\t"
    "v_cvt_pk_bf16_f32 v31, v26, v27\n\t"
    "v_mfma_f32_16x16x32_bf16 v[12:15], a[128:131], v[156:159], v[12:15]\n\t"
    "v_exp_f32_e32 v32, v32\n\t"
    "v_mfma_f32_16x16x32_bf16 v[12:15], a[136:139], v[152:155], v[12:15]\n\t"
    "v_add_f32_e32 v38, v38, v32\n\t"
    "v_fma_f32 v34, v34, s68, -v36\n\t"
    "v_mfma_f32_16x16x32_bf16 v[24:27], a[100:103], v[172:175], 0\n\t"
    "v_exp_f32_e32 v33, v33\n\t"
    "v_mfma_f32_16x16x32_bf16 v[24:27], a[108:111], v[168:171], v[24:27]\n\t"
    "v_add_f32_e32 v38, v38, v33\n\t"
    "v_fma_f32 v35, v35, s68, -v36\n\t"
    "v_mfma_f32_16x16x32_bf16 v[24:27], a[116:119], v[164:167], v[24:27]\n\t"
    "v_exp_f32_e32 v34, v34\n\t"
    "v_mfma_f32_16x16x32_bf16 v[24:27], a[124:127], v[160:163], v[24:27]\n\t"
    "v_cvt_pk_bf16_f32 v40, v12, v13\n\t"
    "v_cvt_pk_bf16_f32 v41, v14, v15\n\t"
    "v_mfma_f32_16x16x32_bf16 v[24:27], a[132:135], v[156:159], v[24:27]\n\t"
    "v_add_f32_e32 v39, v39, v34\n\t"
    "v_fma_f32 v44, v44, s68, -v36\n\t"
    "v_mfma_f32_16x16x32_bf16 v[24:27], a[140:143], v[152:155], v[24:27]\n\t"
    "buffer_load_dwordx4 v7, s[52:55], s61 offen offset:2048 nt lds\n\t"
    "v_fma_f32 v45, v45, s68, -v36\n\t"
    "v_mfma_f32_16x16x32_bf16 v[52:55], a[144:147], v[172:175], 0\n\t"
    "s_add_u32 s61, s61, 11206656\n\t"
    "v_fma_f32 v46, v46, s68, -v36\n\t"
    "v_mfma_f32_16x16x32_bf16 v[52:55], a[152:155], v[168:171], v[52:55]\n\t"
    "v_exp_f32_e32 v35, v35\n\t"
    "v_mfma_f32_16x16x32_bf16 v[52:55], a[160:163], v[164:167], v[52:55]\n\t"
    "v_add_f32_e32 v39, v39, v35\n\t"
    "v_fma_f32 v47, v47, s68, -v36\n\t"
    "v_mfma_f32_16x16x32_bf16 v[52:55], a[168:171], v[160:163], v[52:55]\n\t"
    "v_cvt_pk_bf16_f32 v42, v24, v25\n\t"
    "v_cvt_pk_bf16_f32 v43, v26, v27\n\t"
    "v_mfma_f32_16x16x32_bf16 v[52:55], a[176:179], v[156:159], v[52:55]\n\t"
    "v_exp_f32_e32 v44, v44\n\t"
    "v_mfma_f32_16x16x32_bf16 v[52:55], a[184:187], v[152:155], v[52:55]\n\t"
    "v_add_f32_e32 v38, v38, v44\n\t"
    "v_fma_f32 v48, v48, s68, -v36\n\t"
    "v_mfma_f32_16x16x32_bf16 v[60:63], v[220:223], v[16:19], 0\n\t"
    "v_exp_f32_e32 v45, v45\n\t"
    "v_mfma_f32_16x16x32_bf16 v[60:63], v[216:219], v[28:31], v[60:63]\n\t"
    "v_add_f32_e32 v38, v38, v45\n\t"
    "v_fma_f32 v49, v49, s68, -v36\n\t"
    "v_mfma_f32_16x16x32_bf16 v[60:63], v[212:215], v[40:43], v[60:63]\n\t"
    "v_exp_f32_e32 v46, v46\n\t"
    "v_mfma_f32_16x16x32_bf16 v[68:71], v[196:199], v[16:19], 0\n\t"
    "s_add_u32 m0, s62, 0\n\t"
    "v_add_f32_e32 v39, v39, v46\n\t"
    "v_mfma_f32_16x16x32_bf16 v[68:71], v[192:195], v[28:31], v[68:71]\n\t"
    "buffer_load_dwordx4 v7, s[52:55], s61 offen offset:0 nt lds\n\t"
    "v_fma_f32 v50, v50, s68, -v36\n\t"
    "v_mfma_f32_16x16x32_bf16 v[68:71], v[188:191], v[40:43], v[68:71]\n\t"
    "v_exp_f32_e32 v47, v47\n\t"
    "v_mfma_f32_16x16x32_bf16 v[72:75], v[172:175], v[16:19], 0\n\t"
    "v_add_f32_e32 v39, v39, v47\n\t"
    "v_fma_f32 v51, v51, s68, -v36\n\t"
    "v_mfma_f32_16x16x32_bf16 v[72:75], v[168:171], v[28:31], v[72:75]\n\t"
    "v_exp_f32_e32 v48, v48\n\t"
    "v_mfma_f32_16x16x32_bf16 v[72:75], v[164:167], v[40:43], v[72:75]\n\t"
    "v_add_f32_e32 v38, v38, v48\n\t"
    "v_cvt_pk_bf16_f32 v24, v52, v53\n\t"
    "v_mfma_f32_16x16x32_bf16 v[12:15], v[148:151], v[16:19], 0\n\t"
    "v_exp_f32_e32 v49, v49\n\t"
    "v_mfma_f32_16x16x32_bf16 v[12:15], v[144:147], v[28:31], v[12:15]\n\t"
    "v_add_f32_e32 v38, v38, v49\n\t"
    "v_cvt_pk_bf16_f32 v25, v54, v55\n\t"
    "v_mfma_f32_16x16x32_bf16 v[12:15], v[140:143], v[40:43], v[12:15]\n\t"
    "v_exp_f32_e32 v50, v50\n\t"
    "v_mfma_f32_16x16x32_bf16 v[16:19], a[148:151], v[172:175], 0\n\t"
    "v_add_f32_e32 v39, v39, v50\n\t"
    "v_mfma_f32_16x16x32_bf16 v[16:19], a[156:159], v[168:171], v[16:19]\n\t"
    "v_exp_f32_e32 v51, v51\n\t"
    "v_mfma_f32_16x16x32_bf16 v[16:19], a[164:167], v[164:167], v[16:19]\n\t"
    "v_add_f32_e32 v39, v39, v51\n\t"
    "v_add_f32_e32 v38, v38, v39\n\t"
    "v_mfma_f32_16x16x32_bf16 v[16:19], a[172:175], v[160:163], v[16:19]\n\t"
    "buffer_load_dwordx4 v7, s[52:55], s61 offen offset:1024 nt lds\n\t"
    "v_mov_b32_e32 v39, v38\n\t"
    "v_mfma_f32_16x16x32_bf16 v[16:19], a[180:183], v[156:159], v[16:19]\n\t"
    "v_mfma_f32_16x16x32_bf16 v[16:19], a[188:191], v[152:155], v[16:19]\n\t"
    "v_permlane32_swap_b32 v38, v39\n\t"
    "v_add_f32_e32 v38, v38, v39\n\t"
    "v_mfma_f32_16x16x32_bf16 v[28:31], a[192:195], v[172:175], 0\n\t"
    "v_mov_b32_e32 v39, v38\n\t"
    "v_mfma_f32_16x16x32_bf16 v[28:31], a[200:203], v[168:171], v[28:31]\n\t"
    "v_mfma_f32_16x16x32_bf16 v[28:31], a[208:211], v[164:167], v[28:31]\n\t"
    "v_permlane16_swap_b32 v38, v39\n\t"
    "v_add_f32_e32 v38, v38, v39\n\t"
    "v_mfma_f32_16x16x32_bf16 v[28:31], a[216:219], v[160:163], v[28:31]\n\t"
    "v_cvt_pk_bf16_f32 v26, v16, v17\n\t"
    "v_cvt_pk_bf16_f32 v27, v18, v19\n\t"
    "v_mfma_f32_16x16x32_bf16 v[28:31], a[224:227], v[156:159], v[28:31]\n\t"
    "v_rcp_f32_e32 v38, v38\n\t"
    "v_mfma_f32_16x16x32_bf16 v[28:31], a[232:235], v[152:155], v[28:31]\n\t"
    "v_mul_f32_e32 v20, v20, v38\n\t"
    "v_mul_f32_e32 v21, v21, v38\n\t"
    "v_mfma_f32_16x16x32_bf16 v[40:43], a[196:199], v[172:175], 0\n\t"
    "v_cvt_pk_bf16_f32 v16, v20, v21\n\t"
    "v_mul_f32_e32 v22, v22, v38\n\t"
    "v_mfma_f32_16x16x32_bf16 v[40:43], a[204:207], v[168:171], v[40:43]\n\t"
    "v_mul_f32_e32 v23, v23, v38\n\t"
    "v_cvt_pk_bf16_f32 v17, v22, v23\n\t"
    "v_mfma_f32_16x16x32_bf16 v[40:43], a[212:215], v[164:167], v[40:43]\n\t"
    "buffer_load_dwordx4 v7, s[52:55], s61 offen offset:2048 nt lds\n\t"
    "v_mul_f32_e32 v32, v32, v38\n\t"
    "v_mfma_f32_16x16x32_bf16 v[40:43], a[220:223], v[160:163], v[40:43]\n\t"
    "s_add_u32 s61, s61, 196608\n\t"
    "v_cvt_pk_bf16_f32 v52, v28, v29\n\t"
    "v_mfma_f32_16x16x32_bf16 v[40:43], a[228:231], v[156:159], v[40:43]\n\t"
    "v_cvt_pk_bf16_f32 v53, v30, v31\n\t"
    "v_mul_f32_e32 v33, v33, v38\n\t"
    "v_mfma_f32_16x16x32_bf16 v[40:43], a[236:239], v[152:155], v[40:43]\n\t"
    "v_cvt_pk_bf16_f32 v18, v32, v33\n\t"
    "v_mul_f32_e32 v34, v34, v38\n\t"
    "v_mfma_f32_16x16x32_bf16 v[20:23], a[240:243], v[172:175], 0\n\t"
    "v_mul_f32_e32 v35, v35, v38\n\t"
    "v_cvt_pk_bf16_f32 v19, v34, v35\n\t"
    "v_mfma_f32_16x16x32_bf16 v[20:23], a[248:251], v[168:171], v[20:23]\n\t"
    "v_mul_f32_e32 v44, v44, v38\n\t"
    "v_mul_f32_e32 v45, v45, v38\n\t"
    "v_mfma_f32_16x16x32_bf16 v[20:23], v[224:227], v[164:167], v[20:23]\n\t"
    "v_cvt_pk_bf16_f32 v28, v44, v45\n\t"
    "v_mul_f32_e32 v46, v46, v38\n\t"
    "v_mfma_f32_16x16x32_bf16 v[20:23], v[232:235], v[160:163], v[20:23]\n\t"
    "v_cvt_pk_bf16_f32 v54, v40, v41\n\t"
    "v_cvt_pk_bf16_f32 v55, v42, v43\n\t"
    "v_mfma_f32_16x16x32_bf16 v[20:23], v[240:243], v[156:159], v[20:23]\n\t"
    "v_mul_f32_e32 v47, v47, v38\n\t"
    "v_cvt_pk_bf16_f32 v29, v46, v47\n\t"
    "v_mfma_f32_16x16x32_bf16 v[20:23], v[248:251], v[152:155], v[20:23]\n\t"
    "v_mul_f32_e32 v48, v48, v38\n\t"
    "v_mul_f32_e32 v49, v49, v38\n\t"
    "v_mfma_f32_16x16x32_bf16 v[32:35], a[244:247], v[172:175], 0\n\t"
    "s_add_u32 m0, s62, 3072\n\t"
    "v_cvt_pk_bf16_f32 v30, v48, v49\n\t"
    "v_mfma_f32_16x16x32_bf16 v[32:35], a[252:255], v[168:171], v[32:35]\n\t"
    "buffer_load_dwordx4 v7, s[52:55], s61 offen offset:0 nt lds\n\t"
    "v_mul_f32_e32 v50, v50, v38\n\t"
    "v_mfma_f32_16x16x32_bf16 v[32:35], v[228:231], v[164:167], v[32:35]\n\t"
    "v_mul_f32_e32 v51, v51, v38\n\t"
    "v_cvt_pk_bf16_f32 v31, v50, v51\n\t"
    "v_mfma_f32_16x16x32_bf16 v[32:35], v[236:239], v[160:163], v[32:35]\n\t"
    "v_cvt_pk_bf16_f32 v40, v20, v21\n\t"
    "v_cvt_pk_bf16_f32 v41, v22, v23\n\t"
    "v_mfma_f32_16x16x32_bf16 v[32:35], v[244:247], v[156:159], v[32:35]\n\t"
    "v_mfma_f32_16x16x32_bf16 v[32:35], v[252:255], v[152:155], v[32:35]\n\t"
    "v_mfma_f32_16x16x32_bf16 v[20:23], a[0:3], v[148:151], 0\n\t"
    "ds_write_b128 v6, v[16:19] offset:2048\n\t"
    "v_mfma_f32_16x16x32_bf16 v[20:23], a[8:11], v[144:147], v[20:23]\n\t"
    "v_mfma_f32_16x16x32_bf16 v[20:23], a[16:19], v[140:143], v[20:23]\n\t"
    "v_mfma_f32_16x16x32_bf16 v[20:23], a[24:27], v[136:139], v[20:23]\n\t"
    "v_cvt_pk_bf16_f32 v42, v32, v33\n\t"
    "v_cvt_pk_bf16_f32 v43, v34, v35\n\t"
    "v_mfma_f32_16x16x32_bf16 v[20:23], a[32:35], v[132:135], v[20:23]\n\t"
    "ds_write_b128 v6, v[28:31] offset:3072\n\t"
    "v_mfma_f32_16x16x32_bf16 v[20:23], a[40:43], v[128:131], v[20:23]\n\t"
    "v_mfma_f32_16x16x32_bf16 v[60:63], v[208:211], v[24:27], v[60:63]\n\t"
    "buffer_load_dwordx4 v7, s[52:55], s61 offen offset:1024 nt lds\n\t"
    "v_mfma_f32_16x16x32_bf16 v[60:63], v[204:207], v[52:55], v[60:63]\n\t"
    "v_mfma_f32_16x16x32_bf16 v[60:63], v[200:203], v[40:43], v[60:63]\n\t"
    "v_mfma_f32_16x16x32_bf16 v[68:71], v[184:187], v[24:27], v[68:71]\n\t"
    "v_cvt_pk_bf16_f32 v44, v20, v21\n\t"
    "v_cvt_pk_bf16_f32 v45, v22, v23\n\t"
    "v_mfma_f32_16x16x32_bf16 v[68:71], v[180:183], v[52:55], v[68:71]\n\t"
    "v_mfma_f32_16x16x32_bf16 v[68:71], v[176:179], v[40:43], v[68:71]\n\t"
    "v_mfma_f32_16x16x32_bf16 v[72:75], v[160:163], v[24:27], v[72:75]\n\t"
    "v_max3_f32 v16, v60, v61, v62\n\t"
    "v_mfma_f32_16x16x32_bf16 v[72:75], v[156:159], v[52:55], v[72:75]\n\t"
    "v_mfma_f32_16x16x32_bf16 v[72:75], v[152:155], v[40:43], v[72:75]\n\t"
    "v_mfma_f32_16x16x32_bf16 v[12:15], v[136:139], v[24:27], v[12:15]\n\t"
    "v_max3_f32 v17, v63, v68, v69\n\t"
    "v_max3_f32 v16, v16, v70, v71\n\t"
    "v_mfma_f32_16x16x32_bf16 v[12:15], v[132:135], v[52:55], v[12:15]\n\t"
    "v_mfma_f32_16x16x32_bf16 v[12:15], v[128:131], v[40:43], v[12:15]\n\t"
    "buffer_load_dwordx4 v7, s[52:55], s61 offen offset:2048 nt lds\n\t"
    "v_mfma_f32_16x16x32_bf16 v[20:23], a[4:7], v[148:151], 0\n\t"
    "s_add_u32 s61, s61, 196608\n\t"
    "v_max3_f32 v17, v17, v72, v73\n\t"
    "v_mfma_f32_16x16x32_bf16 v[20:23], a[12:15], v[144:147], v[20:23]\n\t"
    "v_max3_f32 v16, v16, v74, v75\n\t"
    "v_mfma_f32_16x16x32_bf16 v[20:23], a[20:23], v[140:143], v[20:23]\n\t"
    "v_mfma_f32_16x16x32_bf16 v[20:23], a[28:31], v[136:139], v[20:23]\n\t"
    "v_max3_f32 v17, v17, v12, v13\n\t"
    "v_max3_f32 v16, v16, v14, v15\n\t"
    "v_mfma_f32_16x16x32_bf16 v[20:23], a[36:39], v[132:135], v[20:23]\n\t"
    "v_max_f32_e32 v16, v16, v17\n\t"
    "v_mov_b32_e32 v17, v16\n\t"
    "v_mfma_f32_16x16x32_bf16 v[20:23], a[44:47], v[128:131], v[20:23]\n\t"
    "v_mfma_f32_16x16x32_bf16 v[24:27], a[48:51], v[148:151], 0\n\t"
    "v_permlane32_swap_b32 v16, v17\n\t"
    "v_max_f32_e32 v16, v16, v17\n\t"
    "v_mfma_f32_16x16x32_bf16 v[24:27], a[56:59], v[144:147], v[24:27]\n\t"
    "v_mov_b32_e32 v17, v16\n\t"
    "v_mfma_f32_16x16x32_bf16 v[24:27], a[64:67], v[140:143], v[24:27]\n\t"
    "v_mfma_f32_16x16x32_bf16 v[24:27], a[72:75], v[136:139], v[24:27]\n\t"
    "s_add_u32 m0, s62, 6144\n\t"
    "v_cvt_pk_bf16_f32 v46, v20, v21\n\t"
    "v_mfma_f32_16x16x32_bf16 v[24:27], a[80:83], v[132:135], v[24:27]\n\t"
    "buffer_load_dwordx4 v7, s[52:55], s61 offen offset:0 nt lds\n\t"
    "v_cvt_pk_bf16_f32 v47, v22, v23\n\t"
    "v_mfma_f32_16x16x32_bf16 v[24:27], a[88:91], v[128:131], v[24:27]\n\t"
    "v_permlane16_swap_b32 v16, v17\n\t"
    "v_max_f32_e32 v16, v16, v17\n\t"
    "v_mfma_f32_16x16x32_bf16 v[28:31], a[52:55], v[148:151], 0\n\t"
    "v_mul_f32_e32 v16, s68, v16\n\t"
    "v_fma_f32 v60, v60, s68, -v16\n\t"
    "v_mfma_f32_16x16x32_bf16 v[28:31], a[60:63], v[144:147], v[28:31]\n\t"
    "v_exp_f32_e32 v60, v60\n\t"
    "v_mfma_f32_16x16x32_bf16 v[28:31], a[68:71], v[140:143], v[28:31]\n\t"
    "v_fma_f32 v61, v61, s68, -v16\n\t"
    "v_fma_f32 v62, v62, s68, -v16\n\t"
    ".Lh3_%=:\n\t"
    "s_cbranch_execz .LrM_%=\n\t"
    "v_mfma_f32_16x16x32_bf16 v[28:31], a[76:79], v[136:139], v[28:31]\n\t"
    "v_cvt_pk_bf16_f32 v48, v24, v25\n\t"
    "v_cvt_pk_bf16_f32 v49, v26, v27\n\t"
    "v_mfma_f32_16x16x32_bf16 v[28:31], a[84:87], v[132:135], v[28:31]\n\t"
    "v_exp_f32_e32 v61, v61\n\t"
    "v_mfma_f32_16x16x32_bf16 v[28:31], a[92:95], v[128:131], v[28:31]\n\t"
    "v_add_f32_e32 v18, v60, v61\n\t"
    "v_fma_f32 v63, v63, s68, -v16\n\t"
    "v_mfma_f32_16x16x32_bf16 v[20:23], a[96:99], v[148:151], 0\n\t"
    "v_exp_f32_e32 v62, v62\n\t"
    "v_mfma_f32_16x16x32_bf16 v[20:23], a[104:107], v[144:147], v[20:23]\n\t"
    "v_exp_f32_e32 v63, v63\n\t"
    "v_mfma_f32_16x16x32_bf16 v[20:23], a[112:115], v[140:143], v[20:23]\n\t"
    "v_add_f32_e32 v19, v62, v63\n\t"
    "v_fma_f32 v68, v68, s68, -v16\n\t"
    "v_mfma_f32_16x16x32_bf16 v[20:23], a[120:123], v[136:139], v[20:23]\n\t"
    "buffer_load_dwordx4 v7, s[52:55], s61 offen offset:1024 nt lds\n\t"
    "v_cvt_pk_bf16_f32 v50, v28, v29\n\t"
    "v_mfma_f32_16x16x32_bf16 v[20:23], a[128:131], v[132:135], v[20:23]\n\t"
    "v_cvt_pk_bf16_f32 v51, v30, v31\n\t"
    "v_fma_f32 v69, v69, s68, -v16\n\t"
    "v_mfma_f32_16x16x32_bf16 v[20:23], a[136:139], v[128:131], v[20:23]\n\t"
    "v_exp_f32_e32 v68, v68\n\t"
    "v_mfma_f32_16x16x32_bf16 v[24:27], a[100:103], v[148:151], 0\n\t"
    "v_add_f32_e32 v18, v18, v68\n\t"
    "v_fma_f32 v70, v70, s68, -v16\n\t"
    "v_mfma_f32_16x16x32_bf16 v[24:27], a[108:111], v[144:147], v[24:27]\n\t"
    "v_exp_f32_e32 v69, v69\n\t"
    "v_mfma_f32_16x16x32_bf16 v[24:27], a[116:119], v[140:143], v[24:27]\n\t"
    "v_add_f32_e32 v18, v18, v69\n\t"
    "v_fma_f32 v71, v71, s68, -v16\n\t"
    "v_mfma_f32_16x16x32_bf16 v[24:27], a[124:127], v[136:139], v[24:27]\n\t"
    "v_cvt_pk_bf16_f32 v52, v20, v21\n\t"
    "v_cvt_pk_bf16_f32 v53, v22, v23\n\t"
    "v_mfma_f32_16x16x32_bf16 v[24:27], a[132:135], v[132:135], v[24:27]\n\t"
    "v_exp_f32_e32 v70, v70\n\t"
    "v_mfma_f32_16x16x32_bf16 v[24:27], a[140:143], v[128:131], v[24:27]\n\t"
    "v_add_f32_e32 v19, v19, v70\n\t"
    "v_fma_f32 v72, v72, s68, -v16\n\t"
    "v_mfma_f32_16x16x32_bf16 v[56:59], a[144:147], v[148:151], 0\n\t"
    "v_exp_f32_e32 v71, v71\n\t"
    "v_mfma_f32_16x16x32_bf16 v[56:59], a[152:155], v[144:147], v[56:59]\n\t"
    "v_add_f32_e32 v19, v19, v71\n\t"
    "v_fma_f32 v73, v73, s68, -v16\n\t"
    "v_mfma_f32_16x16x32_bf16 v[56:59], a[160:163], v[140:143], v[56:59]\n\t"
    "buffer_load_dwordx4 v7, s[52:55], s61 offen offset:2048 nt lds\n\t"
    "v_fma_f32 v74, v74, s68, -v16\n\t"
    "v_mfma_f32_16x16x32_bf16 v[56:59], a[168:171], v[136:139], v[56:59]\n\t"
    "s_add_u32 s61, s61, 196608\n\t"
    "v_cvt_pk_bf16_f32 v54, v24, v25\n\t"
    "v_mfma_f32_16x16x32_bf16 v[56:59], a[176:179], v[132:135], v[56:59]\n\t"
    "v_cvt_pk_bf16_f32 v55, v26, v27\n\t"
    "v_fma_f32 v75, v75, s68, -v16\n\t"
    "v_mfma_f32_16x16x32_bf16 v[56:59], a[184:187], v[128:131], v[56:59]\n\t"
    "v_exp_f32_e32 v72, v72\n\t"
    "v_mfma_f32_16x16x32_bf16 v[20:23], v[220:223], v[44:47], 0\n\t"
    "v_add_f32_e32 v18, v18, v72\n\t"
    "v_fma_f32 v12, v12, s68, -v16\n\t"
    "v_mfma_f32_16x16x32_bf16 v[20:23], v[216:219], v[48:51], v[20:23]\n\t"
    "v_exp_f32_e32 v73, v73\n\t"
    "v_mfma_f32_16x16x32_bf16 v[20:23], v[212:215], v[52:55], v[20:23]\n\t"
    "s_waitcnt vmcnt(27)\n\t"
    "v_add_f32_e32 v18, v18, v73\n\t"
    "v_mfma_f32_16x16x32_bf16 v[24:27], v[196:199], v[44:47], 0\n\t"
    "v_exp_f32_e32 v74, v74\n\t"
    "v_mfma_f32_16x16x32_bf16 v[24:27], v[192:195], v[48:51], v[24:27]\n\t"
    "ds_read_b128 v[220:223], v0 offset:0\n\t"
    "v_add_f32_e32 v19, v19, v74\n\t"
    "v_mfma_f32_16x16x32_bf16 v[24:27], v[188:191], v[52:55], v[24:27]\n\t"
    "v_exp_f32_e32 v75, v75\n\t"
    "v_mfma_f32_16x16x32_bf16 v[28:31], v[172:175], v[44:47], 0\n\t"
    "s_add_u32 m0, s62, 9216\n\t"
    "ds_read_b128 v[216:219], v1 offset:0\n\t"
    "v_mfma_f32_16x16x32_bf16 v[28:31], v[168:171], v[48:51], v[28:31]\n\t"
    "buffer_load_dwordx4 v7, s[52:55], s61 offen offset:0 nt lds\n\t"
    "v_add_f32_e32 v19, v19, v75\n\t"
    "v_fma_f32 v13, v13, s68, -v16\n\t"
    "v_mfma_f32_16x16x32_bf16 v[28:31], v[164:167], v[52:55], v[28:31]\n\t"
    "ds_read_b128 v[212:215], v2 offset:0\n\t"
    "v_fma_f32 v14, v14, s68, -v16\n\t"
    "v_mfma_f32_16x16x32_bf16 v[32:35], v[148:151], v[44:47], 0\n\t"
    "v_exp_f32_e32 v12, v12\n\t"
    "v_mfma_f32_16x16x32_bf16 v[32:35], v[144:147], v[48:51], v[32:35]\n\t"
    "ds_read_b128 v[36:39], v3 offset:0\n\t"
    "v_add_f32_e32 v18, v18, v12\n\t"
    "v_mfma_f32_16x16x32_bf16 v[32:35], v[140:143], v[52:55], v[32:35]\n\t"
    "v_exp_f32_e32 v13, v13\n\t"
    "v_mfma_f32_16x16x32_bf16 v[64:67], a[148:151], v[148:151], 0\n\t"
    "ds_read_b128 v[40:43], v4 offset:0\n\t"
    "v_add_f32_e32 v18, v18, v13\n\t"
    "v_mfma_f32_16x16x32_bf16 v[64:67], a[156:159], v[144:147], v[64:67]\n\t"
    "v_exp_f32_e32 v14, v14\n\t"
    "v_mfma_f32_16x16x32_bf16 v[64:67], a[164:167], v[140:143], v[64:67]\n\t"
    "ds_read_b128 v[44:47], v5 offset:0\n\t"
    "v_add_f32_e32 v19, v19, v14\n\t"
    "v_mfma_f32_16x16x32_bf16 v[64:67], a[172:175], v[136:139], v[64:67]\n\t"
    "v_fma_f32 v15, v15, s68, -v16\n\t"
    "v_cvt_pk_bf16_f32 v48, v56, v57\n\t"
    "v_mfma_f32_16x16x32_bf16 v[64:67], a[180:183], v[132:135], v[64:67]\n\t"
    "v_exp_f32_e32 v15, v15\n\t"
    "v_mfma_f32_16x16x32_bf16 v[64:67], a[188:191], v[128:131], v[64:67]\n\t"
    "v_add_f32_e32 v19, v19, v15\n\t"
    "v_add_f32_e32 v18, v18, v19\n\t"
    "v_mfma_f32_16x16x32_bf16 v[76:79], a[192:195], v[148:151], 0\n\t"
    "buffer_load_dwordx4 v7, s[52:55], s61 offen offset:1024 nt lds\n\t"
    "v_mov_b32_e32 v19, v18\n\t"
    "v_mfma_f32_16x16x32_bf16 v[76:79], a[200:203], v[144:147], v[76:79]\n\t"
    "v_cvt_pk_bf16_f32 v49, v58, v59\n\t"
    "v_mfma_f32_16x16x32_bf16 v[76:79], a[208:211], v[140:143], v[76:79]\n\t"
    "v_permlane32_swap_b32 v18, v19\n\t"
    "v_add_f32_e32 v18, v18, v19\n\t"
    "v_mfma_f32_16x16x32_bf16 v[76:79], a[216:219], v[136:139], v[76:79]\n\t"
    "v_cvt_pk_bf16_f32 v50, v64, v65\n\t"
    "v_cvt_pk_bf16_f32 v51, v66, v67\n\t"
    "v_mfma_f32_16x16x32_bf16 v[76:79], a[224:227], v[132:135], v[76:79]\n\t"
    "v_mov_b32_e32 v19, v18\n\t"
    "v_mfma_f32_16x16x32_bf16 v[76:79], a[232:235], v[128:131], v[76:79]\n\t"
    "v_mfma_f32_16x16x32_bf16 v[64:67], a[196:199], v[148:151], 0\n\t"
    "v_permlane16_swap_b32 v18, v19\n\t"
    "v_add_f32_e32 v18, v18, v19\n\t"
    "v_mfma_f32_16x16x32_bf16 v[64:67], a[204:207], v[144:147], v[64:67]\n\t"
    "v_rcp_f32_e32 v18, v18\n\t"
    "v_mfma_f32_16x16x32_bf16 v[64:67], a[212:215], v[140:143], v[64:67]\n\t"
    "v_mul_f32_e32 v60, v60, v18\n\t"
    "v_mul_f32_e32 v61, v61, v18\n\t"
    "v_mfma_f32_16x16x32_bf16 v[64:67], a[220:223], v[136:139], v[64:67]\n\t"
    "v_cvt_pk_bf16_f32 v52, v76, v77\n\t"
    "v_cvt_pk_bf16_f32 v53, v78, v79\n\t"
    "v_mfma_f32_16x16x32_bf16 v[64:67], a[228:231], v[132:135], v[64:67]\n\t"
    "v_cvt_pk_bf16_f32 v56, v60, v61\n\t"
    "v_mul_f32_e32 v62, v62, v18\n\t"
    "v_mfma_f32_16x16x32_bf16 v[64:67], a[236:239], v[128:131], v[64:67]\n\t"
    "buffer_load_dwordx4 v7, s[52:55], s61 offen offset:2048 nt lds\n\t"
    "v_mul_f32_e32 v63, v63, v18\n\t"
    "v_mfma_f32_16x16x32_bf16 v[76:79], a[240:243], v[148:151], 0\n\t"
    "s_add_u32 s61, s61, 196608\n\t"
    "v_cvt_pk_bf16_f32 v57, v62, v63\n\t"
    "v_mfma_f32_16x16x32_bf16 v[76:79], a[248:251], v[144:147], v[76:79]\n\t"
    "v_mul_f32_e32 v68, v68, v18\n\t"
    "v_mul_f32_e32 v69, v69, v18\n\t"
    "v_mfma_f32_16x16x32_bf16 v[76:79], v[224:227], v[140:143], v[76:79]\n\t"
    "v_cvt_pk_bf16_f32 v58, v68, v69\n\t"
    "v_mul_f32_e32 v70, v70, v18\n\t"
    "v_mfma_f32_16x16x32_bf16 v[76:79], v[232:235], v[136:139], v[76:79]\n\t"
    "v_cvt_pk_bf16_f32 v54, v64, v65\n\t"
    "v_cvt_pk_bf16_f32 v55, v66, v67\n\t"
    "v_mfma_f32_16x16x32_bf16 v[76:79], v[240:243], v[132:135], v[76:79]\n\t"
    "v_mul_f32_e32 v71, v71, v18\n\t"
    "v_cvt_pk_bf16_f32 v59, v70, v71\n\t"
    "v_mfma_f32_16x16x32_bf16 v[76:79], v[248:251], v[128:131], v[76:79]\n\t"
    "v_mul_f32_e32 v72, v72, v18\n\t"
    "v_mul_f32_e32 v73, v73, v18\n\t"
    "v_mfma_f32_16x16x32_bf16 v[64:67], a[244:247], v[148:151], 0\n\t"
    "v_cvt_pk_bf16_f32 v60, v72, v73\n\t"
    "v_mul_f32_e32 v74, v74, v18\n\t"
    "v_mfma_f32_16x16x32_bf16 v[64:67], a[252:255], v[144:147], v[64:67]\n\t"
    "v_mul_f32_e32 v75, v75, v18\n\t"
    "v_cvt_pk_bf16_f32 v61, v74, v75\n\t"
    "v_mfma_f32_16x16x32_bf16 v[64:67], v[228:231], v[140:143], v[64:67]\n\t"
    "v_mul_f32_e32 v12, v12, v18\n\t"
    "v_mul_f32_e32 v13, v13, v18\n\t"
    "v_mfma_f32_16x16x32_bf16 v[64:67], v[236:239], v[136:139], v[64:67]\n\t"
    "v_cvt_pk_bf16_f32 v68, v76, v77\n\t"
    "v_cvt_pk_bf16_f32 v69, v78, v79\n\t"
    "v_mfma_f32_16x16x32_bf16 v[64:67], v[244:247], v[132:135], v[64:67]\n\t"
    "s_mov_b32 s63, s62\n\t"
    "s_mov_b32 s62, s64\n\t"
    "v_mfma_f32_16x16x32_bf16 v[64:67], v[252:255], v[128:131], v[64:67]\n\t"
    "s_sub_u32 s60, s60, 1\n\t"
    "s_cmp_lg_u32 s60, 0\n\t"
    "s_cbranch_scc1 .Lf2_%=\n\t"
    "v_mbcnt_lo_u32_b32 v8, -1, 0\n\t"
    "v_mbcnt_hi_u32_b32 v8, -1, v8\n\t"
    "v_lshlrev_b32_e32 v8, 5, v8\n\t"
    "s_nop 7\n\t"
    "s_nop 1\n\t"
    "ds_write_b128 v6, v[56:59] offset:4096\n\t"
    "v_cvt_pk_bf16_f32 v62, v12, v13\n\t"
    "v_mul_f32_e32 v14, v14, v18\n\t"
    "v_mul_f32_e32 v15, v15, v18\n\t"
    "v_cvt_pk_bf16_f32 v63, v14, v15\n\t"
    "v_cvt_pk_bf16_f32 v70, v64, v65\n\t"
    "v_cvt_pk_bf16_f32 v71, v66, v67\n\t"
    "ds_write_b128 v6, v[60:63] offset:5120\n\t"
    "s_nop 1\n\t"
    "v_mfma_f32_16x16x32_bf16 v[20:23], v[208:211], v[48:51], v[20:23]\n\t"
    "s_nop 1\n\t"
    "v_mfma_f32_16x16x32_bf16 v[20:23], v[204:207], v[52:55], v[20:23]\n\t"
    "s_nop 1\n\t"
    "v_mfma_f32_16x16x32_bf16 v[20:23], v[200:203], v[68:71], v[20:23]\n\t"
    "s_nop 1\n\t"
    "v_mfma_f32_16x16x32_bf16 v[24:27], v[184:187], v[48:51], v[24:27]\n\t"
    "s_nop 1\n\t"
    "v_mfma_f32_16x16x32_bf16 v[24:27], v[180:183], v[52:55], v[24:27]\n\t"
    "s_waitcnt lgkmcnt(0)\n\t"
    "s_barrier\n\t"
    "s_nop 1\n\t"
    "v_mfma_f32_16x16x32_bf16 v[24:27], v[176:179], v[68:71], v[24:27]\n\t"
    "s_nop 1\n\t"
    "v_mfma_f32_16x16x32_bf16 v[28:31], v[160:163], v[48:51], v[28:31]\n\t"
    "v_max3_f32 v16, v20, v21, v22\n\t"
    "s_nop 1\n\t"
    "v_mfma_f32_16x16x32_bf16 v[28:31], v[156:159], v[52:55], v[28:31]\n\t"
    "s_nop 1\n\t"
    "v_mfma_f32_16x16x32_bf16 v[28:31], v[152:155], v[68:71], v[28:31]\n\t"
    "s_nop 1\n\t"
    "v_mfma_f32_16x16x32_bf16 v[32:35], v[136:139], v[48:51], v[32:35]\n\t"
    "v_max3_f32 v17, v23, v24, v25\n\t"
    "v_max3_f32 v16, v16, v26, v27\n\t"
    "s_nop 1\n\t"
    "v_mfma_f32_16x16x32_bf16 v[32:35], v[132:135], v[52:55], v[32:35]\n\t"
    "s_nop 1\n\t"
    "v_mfma_f32_16x16x32_bf16 v[32:35], v[128:131], v[68:71], v[32:35]\n\t"
    "s_nop 7\n\t"
    "s_nop 1\n\t"
    "v_max3_f32 v17, v17, v28, v29\n\t"
    "v_max3_f32 v16, v16, v30, v31\n\t"
    "v_max3_f32 v17, v17, v32, v33\n\t"
    "v_max3_f32 v16, v16, v34, v35\n\t"
    "v_max_f32_e32 v16, v16, v17\n\t"
    "v_mov_b32_e32 v17, v16\n\t"
    "s_nop 1\n\t"
    "v_permlane32_swap_b32 v16, v17\n\t"
    "v_max_f32_e32 v16, v16, v17\n\t"
    "v_mov_b32_e32 v17, v16\n\t"
    "s_nop 1\n\t"
    "v_permlane16_swap_b32 v16, v17\n\t"
    "v_max_f32_e32 v16, v16, v17\n\t"
    "v_mul_f32_e32 v16, s68, v16\n\t"
    "v_fma_f32 v20, v20, s68, -v16\n\t"
    "v_fma_f32 v21, v21, s68, -v16\n\t"
    "v_exp_f32_e32 v20, v20\n\t"
    "s_nop 0\n\t"
    "v_fma_f32 v22, v22, s68, -v16\n\t"
    "v_exp_f32_e32 v21, v21\n\t"
    "s_nop 0\n\t"
    "v_add_f32_e32 v18, v20, v21\n\t"
    "v_fma_f32 v23, v23, s68, -v16\n\t"
    "v_exp_f32_e32 v22, v22\n\t"
    "s_nop 0\n\t"
    "v_exp_f32_e32 v23, v23\n\t"
    "s_nop 0\n\t"
    "v_add_f32_e32 v19, v22, v23\n\t"
    "v_fma_f32 v24, v24, s68, -v16\n\t"
    "v_exp_f32_e32 v24, v24\n\t"
    "s_nop 0\n\t"
    "v_add_f32_e32 v18, v18, v24\n\t"
    "v_fma_f32 v25, v25, s68, -v16\n\t"
    "v_exp_f32_e32 v25, v25\n\t"
    "s_nop 0\n\t"
    "v_add_f32_e32 v18, v18, v25\n\t"
    "v_fma_f32 v26, v26, s68, -v16\n\t"
    "v_exp_f32_e32 v26, v26\n\t"
    "s_nop 0\n\t"
    "v_add_f32_e32 v19, v19, v26\n\t"
    "v_fma_f32 v27, v27, s68, -v16\n\t"
    "v_exp_f32_e32 v27, v27\n\t"
    "s_nop 0\n\t"
    "v_add_f32_e32 v19, v19, v27\n\t"
    "v_fma_f32 v28, v28, s68, -v16\n\t"
    "v_exp_f32_e32 v28, v28\n\t"
    "s_nop 0\n\t"
    "v_add_f32_e32 v18, v18, v28\n\t"
    "v_fma_f32 v29, v29, s68, -v16\n\t"
    "v_exp_f32_e32 v29, v29\n\t"
    "s_nop 0\n\t"
    "v_add_f32_e32 v18, v18, v29\n\t"
    "v_fma_f32 v30, v30, s68, -v16\n\t"
    "v_exp_f32_e32 v30, v30\n\t"
    "s_nop 0\n\t"
    "v_add_f32_e32 v19, v19, v30\n\t"
    "v_fma_f32 v31, v31, s68, -v16\n\t"
    "v_fma_f32 v32, v32, s68, -v16\n\t"
    "v_exp_f32_e32 v31, v31\n\t"
    "s_nop 0\n\t"
    "v_add_f32_e32 v19, v19, v31\n\t"
    "v_exp_f32_e32 v32, v32\n\t"
    "s_nop 0\n\t"
    "v_add_f32_e32 v18, v18, v32\n\t"
    "v_fma_f32 v33, v33, s68, -v16\n\t"
    "v_fma_f32 v34, v34, s68, -v16\n\t"
    "v_fma_f32 v35, v35, s68, -v16\n\t"
    "v_exp_f32_e32 v33, v33\n\t"
    "s_nop 0\n\t"
    "v_add_f32_e32 v18, v18, v33\n\t"
    "v_exp_f32_e32 v34, v34\n\t"
    "s_nop 0\n\t"
    "v_add_f32_e32 v19, v19, v34\n\t"
    "v_exp_f32_e32 v35, v35\n\t"
    "s_nop 0\n\t"
    "v_add_f32_e32 v19, v19, v35\n\t"
    "v_add_f32_e32 v18, v18, v19\n\t"
    "v_mov_b32_e32 v19, v18\n\t"
    "s_nop 1\n\t"
    "v_permlane32_swap_b32 v18, v19\n\t"
    "v_add_f32_e32 v18, v18, v19\n\t"
    "v_mov_b32_e32 v19, v18\n\t"
    "s_nop 1\n\t"
    "v_permlane16_swap_b32 v18, v19\n\t"
    "v_add_f32_e32 v18, v18, v19\n\t"
    "v_rcp_f32_e32 v18, v18\n\t"
    "s_nop 0\n\t"
    "v_mul_f32_e32 v20, v20, v18\n\t"
    "v_mul_f32_e32 v21, v21, v18\n\t"
    "v_cvt_pk_bf16_f32 v12, v20, v21\n\t"
    "v_mul_f32_e32 v22, v22, v18\n\t"
    "v_mul_f32_e32 v23, v23, v18\n\t"
    "v_cvt_pk_bf16_f32 v13, v22, v23\n\t"
    "v_mul_f32_e32 v24, v24, v18\n\t"
    "v_mul_f32_e32 v25, v25, v18\n\t"
    "v_cvt_pk_bf16_f32 v14, v24, v25\n\t"
    "v_mul_f32_e32 v26, v26, v18\n\t"
    "v_mul_f32_e32 v27, v27, v18\n\t"
    "v_cvt_pk_bf16_f32 v15, v26, v27\n\t"
    "v_mul_f32_e32 v28, v28, v18\n\t"
    "v_mul_f32_e32 v29, v29, v18\n\t"
    "v_cvt_pk_bf16_f32 v20, v28, v29\n\t"
    "v_mul_f32_e32 v30, v30, v18\n\t"
    "v_mul_f32_e32 v31, v31, v18\n\t"
    "v_cvt_pk_bf16_f32 v21, v30, v31\n\t"
    "v_mul_f32_e32 v32, v32, v18\n\t"
    "v_mul_f32_e32 v33, v33, v18\n\t"
    "v_cvt_pk_bf16_f32 v22, v32, v33\n\t"
    "v_mul_f32_e32 v34, v34, v18\n\t"
    "v_mul_f32_e32 v35, v35, v18\n\t"
    "v_cvt_pk_bf16_f32 v23, v34, v35\n\t"
    "ds_write_b128 v6, v[12:15] offset:6144\n\t"
    "ds_write_b128 v6, v[20:23] offset:7168\n\t"
    "s_waitcnt lgkmcnt(0)\n\t"
    "s_barrier\n\t"
    "s_cmp_lg_u32 %[closer], 0\n\t"
    "s_cbranch_scc1 .Lx3_%=\n\t"
    "s_mov_b32 s60, 0x10000\n\t"
    "buffer_load_dwordx2 v[10:11], off, s[56:59], %[eoff] sc1\n\t"
    ".Lp4_%=:\n\t"
    "buffer_load_dwordx4 v[12:15], v8, s[56:59], %[aoff] offen sc1\n\t"
    "buffer_load_dwordx4 v[16:19], v8, s[56:59], %[aoff] offen offset:16 sc1\n\t"
    "s_waitcnt vmcnt(0)\n\t"
    "v_add_co_u32_e32 v20, vcc, 0x7f4a7c15, v10\n\t"
    "s_nop 1\n\t"
    "v_mov_b32_e32 v9, 0x9e3779b9\n\t"
    "v_addc_co_u32_e32 v21, vcc, v9, v11, vcc\n\t"
    "v_cmp_eq_u64_e32 vcc, v[12:13], v[20:21]\n\t"
    "v_cmp_eq_u64_e64 s[66:67], v[14:15], v[20:21]\n\t"
    "s_and_b64 vcc, vcc, s[66:67]\n\t"
    "v_cmp_eq_u64_e64 s[66:67], v[16:17], v[20:21]\n\t"
    "s_and_b64 vcc, vcc, s[66:67]\n\t"
    "v_cmp_eq_u64_e64 s[66:67], v[18:19], v[20:21]\n\t"
    "s_and_b64 vcc, vcc, s[66:67]\n\t"
    "s_cmp_eq_u64 vcc, exec\n\t"
    "s_cbranch_scc1 .Lk5_%=\n\t"
    "s_sleep 8\n\t"
    "s_sub_u32 s60, s60, 1\n\t"
    "s_cmp_lg_u32 s60, 0\n\t"
    "s_cbranch_scc1 .Lp4_%=\n\t"
    ".Lk5_%=:\n\t"
    "s_mov_b64 exec, 1\n\t"
    "buffer_store_dwordx2 v[20:21], off, s[56:59], %[eoff] sc1\n\t"
    "s_mov_b64 exec, -1\n\t"
    ".Lx3_%=:\n\t"
    "s_endpgm\n\t"
    ".p2align 12\n\t"
    ".Lroln_%=:\n\t"
    "s_mov_b32 s50, 0x36000\n\t"
    "s_mov_b32 s51, 0x20000\n\t"
    "s_mov_b32 s54, 0x12000\n\t"
    "s_mov_b32 s55, 0x20000\n\t"
    "s_mov_b32 s58, 0x18000\n\t"
    "s_mov_b32 s59, 0x20000\n\t"
    "s_mov_b32 s63, 0x20000\n\t"
    "s_cmp_lt_u32 %[tch], 96\n\t"
    "s_cselect_b32 s62, s50, s54\n\t"
    "s_cselect_b32 s64, 0, 96\n\t"
    "s_sub_u32 s64, %[tch], s64\n\t"
    "s_mul_i32 s64, s64, 2304\n\t"
    "s_cmp_lt_u32 %[tch], 96\n\t"
    "s_waitcnt lgkmcnt(0)\n\t"
    "s_cselect_b64 s[60:61], s[38:39], s[40:41]\n\t"
    "s_and_b32 s61, s61, 0xffff\n\t"
    "s_mov_b32 s48, s38\n\t"
    "s_and_b32 s49, s39, 0xffff\n\t"
    "s_mov_b64 exec, 0x3ffff\n\t"
    "buffer_load_dword a255, v26, s[60:63], s64 offen\n\t"
    "s_mov_b64 exec, -1\n\t"
    "s_add_u32 m0, %[kl], 0\n\t"
    "s_nop 0\n\t"
    "buffer_load_dwordx4 v24, s[48:51], %[k0] offen offset:0 lds\n\t"
    "s_add_u32 m0, %[kl], 1024\n\t"
    "s_nop 0\n\t"
    "buffer_load_dwordx4 v25, s[48:51], %[k0] offen offset:0 lds\n\t"
    "s_add_u32 m0, %[kl], 1920\n\t"
    "s_nop 0\n\t"
    "buffer_load_dwordx4 v24, s[48:51], %[k0] offen offset:128 lds\n\t"
    "s_add_u32 m0, %[kl], 2944\n\t"
    "s_nop 0\n\t"
    "buffer_load_dwordx4 v25, s[48:51], %[k0] offen offset:128 lds\n\t"
    "s_add_u32 m0, %[kl], 3840\n\t"
    "s_nop 0\n\t"
    "buffer_load_dwordx4 v24, s[48:51], %[k0] offen offset:256 lds\n\t"
    "s_add_u32 m0, %[kl], 4864\n\t"
    "s_nop 0\n\t"
    "buffer_load_dwordx4 v25, s[48:51], %[k0] offen offset:256 lds\n\t"
    "s_add_u32 m0, %[kl], 6144\n\t"
    "s_nop 0\n\t"
    "buffer_load_dwordx4 v24, s[48:51], %[k1] offen offset:0 lds\n\t"
    "s_add_u32 m0, %[kl], 7168\n\t"
    "s_nop 0\n\t"
    "buffer_load_dwordx4 v25, s[48:51], %[k1] offen offset:0 lds\n\t"
    "s_add_u32 m0, %[kl], 8064\n\t"
    "s_nop 0\n\t"
    "buffer_load_dwordx4 v24, s[48:51], %[k1] offen offset:128 lds\n\t"
    "s_mov_b32 s56, s42\n\t"
    "s_and_b32 s57, s43, 0xffff\n\t"
    "s_barrier\n\t"
    "s_mov_b64 exec, 0\n\t"
    "s_cmp_eq_u32 %[wqg], 0\n\t"
    "s_cbranch_scc1 .Lh5_%=\n\t"
    "s_branch .Lh6_%=\n\t"
    ".LrN_%=:\n\t"
    "s_mov_b64 exec, -1\n\t"
    "v_lshlrev_b32_e32 v30, 4, v31\n\t"
    "v_and_b32_e32 v28, 7, v31\n\t"
    "v_bfe_u32 v27, v31, 3, 1\n\t"
    "v_lshrrev_b32_e32 v29, 4, v31\n\t"
    "v_bfe_u32 v20, v31, 1, 3\n\t"
    "v_xor_b32_e32 v20, v20, v29\n\t"
    "v_lshlrev_b32_e32 v21, 7, v28\n\t"
    "v_lshl_or_b32 v21, v27, 11, v21\n\t"
    "v_lshl_or_b32 v21, v20, 4, v21\n\t"
    "v_add_u32_e32 v11, %[tb], v30\n\t"
    "v_lshlrev_b32_e32 v9, 10, v27\n\t"
    "v_sub_u32_e32 v9, v21, v9\n\t"
    "v_add_u32_e32 v9, %[kl], v9\n\t"
    "v_xor_b32_e32 v10, 64, v9\n\t"
    "v_add_u32_e32 v12, 0x9000, v21\n\t"
    "v_xor_b32_e32 v13, 64, v12\n\t"
    "v_add_u32_e32 v14, %[wn], v30\n\t"
    "v_add_u32_e32 v6, %[pl], v30\n\t"
    "v_mul_u32_u24_e32 v27, 0xc00, v27\n\t"
    "v_add_u32_e32 v27, %[rbeg], v27\n\t"
    "v_mad_u32_u24 v20, v28, 3, 0\n\t"
    "v_and_b32_e32 v21, 6, v20\n\t"
    "v_xor_b32_e32 v21, v21, v29\n\t"
    "v_lshl_add_u32 v20, v20, 7, v27\n\t"
    "v_lshl_add_u32 v0, v21, 4, v20\n\t"
    "v_xor_b32_e32 v1, 64, v0\n\t"
    "v_mad_u32_u24 v20, v28, 3, 1\n\t"
    "v_and_b32_e32 v21, 6, v20\n\t"
    "v_xor_b32_e32 v21, v21, v29\n\t"
    "v_lshl_add_u32 v20, v20, 7, v27\n\t"
    "v_lshl_add_u32 v2, v21, 4, v20\n\t"
    "v_xor_b32_e32 v3, 64, v2\n\t"
    "v_mad_u32_u24 v20, v28, 3, 2\n\t"
    "v_and_b32_e32 v21, 6, v20\n\t"
    "v_xor_b32_e32 v21, v21, v29\n\t"
    "v_lshl_add_u32 v20, v20, 7, v27\n\t"
    "v_lshl_add_u32 v4, v21, 4, v20\n\t"
    "v_xor_b32_e32 v5, 64, v4\n\t"
    "s_add_u32 m0, %[kl], 9088\n\t"
    "s_nop 0\n\t"
    "buffer_load_dwordx4 v25, s[48:51], %[k1] offen offset:128 lds\n\t"
    "s_add_u32 m0, %[kl], 9984\n\t"
    "s_nop 0\n\t"
    "buffer_load_dwordx4 v24, s[48:51], %[k1] offen offset:256 lds\n\t"
    "s_add_u32 m0, %[kl], 11008\n\t"
    "s_nop 0\n\t"
    "buffer_load_dwordx4 v25, s[48:51], %[k1] offen offset:256 lds\n\t"
    "s_add_u32 m0, %[kl], 12288\n\t"
    "s_nop 0\n\t"
    "buffer_load_dwordx4 v24, s[48:51], %[k2] offen offset:0 lds\n\t"
    "s_add_u32 m0, %[kl], 13312\n\t"
    "s_nop 0\n\t"
    "buffer_load_dwordx4 v25, s[48:51], %[k2] offen offset:0 lds\n\t"
    "s_add_u32 m0, %[kl], 14208\n\t"
    "s_nop 0\n\t"
    "buffer_load_dwordx4 v24, s[48:51], %[k2] offen offset:128 lds\n\t"
    "s_add_u32 m0, %[kl], 15232\n\t"
    "s_nop 0\n\t"
    "buffer_load_dwordx4 v25, s[48:51], %[k2] offen offset:128 lds\n\t"
    "s_add_u32 m0, %[kl], 16128\n\t"
    "s_nop 0\n\t"
    "buffer_load_dwordx4 v24, s[48:51], %[k2] offen offset:256 lds\n\t"
    "s_add_u32 m0, %[kl], 17152\n\t"
    "s_nop 0\n\t"
    "buffer_load_dwordx4 v25, s[48:51], %[k2] offen offset:256 lds\n\t"
    "s_add_u32 m0, %[wql], 0\n\t"
    "s_add_u32 s61, %[wqg], 0\n\t"
    "buffer_load_dwordx4 v24, s[48:51], s61 offen offset:0 lds\n\t"
    "s_add_u32 m0, %[wql], 3968\n\t"
    "v_lshrrev_b32_e32 v20, 5, v31\n\t"
    "buffer_load_dwordx4 v24, s[48:51], s61 offen offset:128 lds\n\t"
    "s_add_u32 m0, %[wql], 7936\n\t"
    "v_mul_u32_u24_e32 v20, 0x30000, v20\n\t"
    "buffer_load_dwordx4 v24, s[48:51], s61 offen offset:256 lds\n\t"
    "s_add_u32 m0, %[wql], 2048\n\t"
    "v_and_b32_e32 v21, 1, v29\n\t"
    "buffer_load_dwordx4 v25, s[48:51], s61 offen offset:0 lds\n\t"
    "s_add_u32 m0, %[wql], 6016\n\t"
    "v_mul_u32_u24_e32 v21, 0x600, v21\n\t"
    "buffer_load_dwordx4 v25, s[48:51], s61 offen offset:128 lds\n\t"
    "s_add_u32 m0, %[wql], 9984\n\t"
    "v_add_u32_e32 v20, v20, v21\n\t"
    "buffer_load_dwordx4 v25, s[48:51], s61 offen offset:256 lds\n\t"
    "s_add_u32 m0, %[wql], 12288\n\t"
    "s_add_u32 s61, %[wqg], 36864\n\t"
    "buffer_load_dwordx4 v24, s[48:51], s61 offen offset:0 lds\n\t"
    "s_add_u32 m0, %[wql], 16256\n\t"
    "v_and_b32_e32 v21, 15, v31\n\t"
    "buffer_load_dwordx4 v24, s[48:51], s61 offen offset:128 lds\n\t"
    "s_add_u32 m0, %[wql], 20224\n\t"
    "v_lshl_add_u32 v7, v21, 4, v20\n\t"
    "buffer_load_dwordx4 v24, s[48:51], s61 offen offset:256 lds\n\t"
    "s_add_u32 m0, %[wql], 14336\n\t"
    "v_lshl_add_u32 v8, v21, 3, v20\n\t"
    "buffer_load_dwordx4 v25, s[48:51], s61 offen offset:0 lds\n\t"
    "s_add_u32 m0, %[wql], 18304\n\t"
    "v_add_u32_e32 v8, 0x100, v8\n\t"
    "buffer_load_dwordx4 v25, s[48:51], s61 offen offset:128 lds\n\t"
    "s_add_u32 m0, %[wql], 22272\n\t"
    "v_and_b32_e32 v20, 48, v30\n\t"
    "buffer_load_dwordx4 v25, s[48:51], s61 offen offset:256 lds\n\t"
    "s_add_u32 m0, %[wql], 24576\n\t"
    "s_add_u32 s61, %[wqg], 73728\n\t"
    "buffer_load_dwordx4 v24, s[48:51], s61 offen offset:0 lds\n\t"
    "s_add_u32 m0, %[wql], 28544\n\t"
    "v_lshl_or_b32 v20, v29, 6, v20\n\t"
    "buffer_load_dwordx4 v24, s[48:51], s61 offen offset:128 lds\n\t"
    "s_add_u32 m0, %[wql], 32512\n\t"
    "v_lshrrev_b32_e32 v21, 2, v21\n\t"
    "buffer_load_dwordx4 v24, s[48:51], s61 offen offset:256 lds\n\t"
    "s_add_u32 m0, %[wql], 26624\n\t"
    "v_lshl_or_b32 v15, v21, 8, v20\n\t"
    "buffer_load_dwordx4 v25, s[48:51], s61 offen offset:0 lds\n\t"
    "s_add_u32 m0, %[wql], 30592\n\t"
    "v_min_u32_e32 v21, 35, v31\n\t"
    "buffer_load_dwordx4 v25, s[48:51], s61 offen offset:128 lds\n\t"
    "s_add_u32 m0, %[wql], 34560\n\t"
    "v_lshlrev_b32_e32 v21, 4, v21\n\t"
    "buffer_load_dwordx4 v25, s[48:51], s61 offen offset:256 lds\n\t"
    "s_add_u32 m0, %[wql], 36864\n\t"
    "s_add_u32 s61, %[wqg], 110592\n\t"
    "buffer_load_dwordx4 v24, s[48:51], s61 offen offset:0 lds\n\t"
    "s_add_u32 m0, %[wql], 40832\n\t"
    "v_add_u32_e32 v21, 0x14000, v21\n\t"
    "buffer_load_dwordx4 v24, s[48:51], s61 offen offset:128 lds\n\t"
    "s_add_u32 m0, %[wql], 44800\n\t"
    "s_mov_b32 s52, s44\n\t"
    "buffer_load_dwordx4 v24, s[48:51], s61 offen offset:256 lds\n\t"
    "s_add_u32 m0, %[wql], 38912\n\t"
    "s_and_b32 s53, s45, 0xffff\n\t"
    "buffer_load_dwordx4 v25, s[48:51], s61 offen offset:0 lds\n\t"
    "s_add_u32 m0, %[wql], 42880\n\t"
    "s_mov_b32 s54, 0x6000000\n\t"
    "buffer_load_dwordx4 v25, s[48:51], s61 offen offset:128 lds\n\t"
    "s_add_u32 m0, %[wql], 46848\n\t"
    "s_mov_b32 s55, 0x20000\n\t"
    "buffer_load_dwordx4 v25, s[48:51], s61 offen offset:256 lds\n\t"
    "buffer_load_dwordx2 v[22:23], off, s[56:59], %[eoff] sc1\n\t"
    "s_waitcnt vmcnt(37)\n\t"
    "ds_read_b128 v[116:119], v9 offset:0\n\t"
    "ds_read_b128 v[120:123], v10 offset:0\n\t"
    "ds_read_b128 v[124:127], v9 offset:2048\n\t"
    "ds_read_b128 v[128:131], v10 offset:2048\n\t"
    "ds_read_b128 v[132:135], v9 offset:4096\n\t"
    "ds_read_b128 v[136:139], v10 offset:4096\n\t"
    "s_waitcnt vmcnt(31)\n\t"
    "ds_read_b128 v[140:143], v9 offset:6144\n\t"
    "ds_read_b128 v[144:147], v10 offset:6144\n\t"
    "ds_read_b128 v[148:151], v9 offset:8192\n\t"
    "ds_read_b128 v[152:155], v10 offset:8192\n\t"
    "ds_read_b128 v[156:159], v9 offset:10240\n\t"
    "ds_read_b128 v[160:163], v10 offset:10240\n\t"
    "s_waitcnt vmcnt(25)\n\t"
    "ds_read_b128 v[164:167], v9 offset:12288\n\t"
    "ds_read_b128 v[168:171], v10 offset:12288\n\t"
    "ds_read_b128 v[172:175], v9 offset:14336\n\t"
    "ds_read_b128 v[176:179], v10 offset:14336\n\t"
    "ds_read_b128 v[180:183], v9 offset:16384\n\t"
    "ds_read_b128 v[184:187], v10 offset:16384\n\t"
    "s_mov_b32 s67, 2\n\t"
    "s_waitcnt vmcnt(19) lgkmcnt(0)\n\t"
    "s_barrier\n\t"
    "ds_read_b128 v[32:35], v12 offset:0\n\t"
    "ds_read_b128 v[36:39], v13 offset:0\n\t"
    "ds_read_b128 v[40:43], v12 offset:4096\n\t"
    "ds_read_b128 v[44:47], v13 offset:4096\n\t"
    "ds_read_b128 v[48:51], v12 offset:8192\n\t"
    "ds_read_b128 v[52:55], v13 offset:8192\n\t"
    "s_mov_b32 s60, 6\n\t"
    "s_branch .Lf6_%=\n\t"
    ".Lfs1_%=:\n\t"
    "s_waitcnt vmcnt(0)\n\t"
    "v_add_co_u32_e32 v22, vcc, 0x7f4a7c15, v22\n\t"
    "v_mov_b32_e32 v16, 0x9e3779b9\n\t"
    "s_nop 0\n\t"
    "v_addc_co_u32_e32 v23, vcc, v16, v23, vcc\n\t"
    "buffer_load_dwordx4 v[16:19], v21, s[56:59], 0 offen sc1\n\t"
    "s_branch .Lfb1_%=\n\t"
    ".Lf6_%=:\n\t"
    "s_waitcnt lgkmcnt(3)\n\t"
    "v_mfma_f32_16x16x32_bf16 v[80:83], v[32:35], v[116:119], 0\n\t"
    "v_mfma_f32_16x16x32_bf16 v[80:83], v[36:39], v[120:123], v[80:83]\n\t"
    "ds_read_b128 v[56:59], v12 offset:1024\n\t"
    "v_mfma_f32_16x16x32_bf16 v[80:83], v[40:43], v[124:127], v[80:83]\n\t"
    "s_waitcnt lgkmcnt(1)\n\t"
    "v_mfma_f32_16x16x32_bf16 v[80:83], v[44:47], v[128:131], v[80:83]\n\t"
    "ds_read_b128 v[60:63], v13 offset:1024\n\t"
    "v_cvt_pk_bf16_f32 v114, v100, v101\n\t"
    "v_mfma_f32_16x16x32_bf16 v[80:83], v[48:51], v[132:135], v[80:83]\n\t"
    "v_cvt_pk_bf16_f32 v115, v102, v103\n\t"
    "v_mfma_f32_16x16x32_bf16 v[80:83], v[52:55], v[136:139], v[80:83]\n\t"
    "ds_read_b128 v[64:67], v12 offset:5120\n\t"
    "v_mfma_f32_16x16x32_bf16 v[88:91], v[32:35], v[140:143], 0\n\t"
    "ds_write_b128 v11, v[108:111] offset:1024\n\t"
    "v_mfma_f32_16x16x32_bf16 v[88:91], v[36:39], v[144:147], v[88:91]\n\t"
    "ds_read_b128 v[68:71], v13 offset:5120\n\t"
    "v_mfma_f32_16x16x32_bf16 v[88:91], v[40:43], v[148:151], v[88:91]\n\t"
    "v_mfma_f32_16x16x32_bf16 v[88:91], v[44:47], v[152:155], v[88:91]\n\t"
    "ds_read_b128 v[72:75], v12 offset:9216\n\t"
    "v_cvt_pk_bf16_f32 v104, v80, v81\n\t"
    "v_mfma_f32_16x16x32_bf16 v[88:91], v[48:51], v[156:159], v[88:91]\n\t"
    "ds_write_b128 v11, v[112:115] offset:2048\n\t"
    "v_mfma_f32_16x16x32_bf16 v[88:91], v[52:55], v[160:163], v[88:91]\n\t"
    "ds_read_b128 v[76:79], v13 offset:9216\n\t"
    "v_cvt_pk_bf16_f32 v105, v82, v83\n\t"
    "v_mfma_f32_16x16x32_bf16 v[96:99], v[32:35], v[164:167], 0\n\t"
    "v_mov_b32_e32 v11, v14\n\t"
    "v_add_u32_e32 v14, 0x3000, v14\n\t"
    "v_mfma_f32_16x16x32_bf16 v[96:99], v[36:39], v[168:171], v[96:99]\n\t"
    "s_cmp_eq_u32 s60, s67\n\t"
    "s_cbranch_scc1 .Lfs1_%=\n\t"
    ".Lfb1_%=:\n\t"
    "v_mfma_f32_16x16x32_bf16 v[96:99], v[40:43], v[172:175], v[96:99]\n\t"
    "v_add_u32_e32 v12, 0x3000, v12\n\t"
    "v_add_u32_e32 v13, 0x3000, v13\n\t"
    "v_mfma_f32_16x16x32_bf16 v[96:99], v[44:47], v[176:179], v[96:99]\n\t"
    "v_cvt_pk_bf16_f32 v108, v88, v89\n\t"
    "v_cvt_pk_bf16_f32 v109, v90, v91\n\t"
    "v_mfma_f32_16x16x32_bf16 v[96:99], v[48:51], v[180:183], v[96:99]\n\t"
    "v_mfma_f32_16x16x32_bf16 v[96:99], v[52:55], v[184:187], v[96:99]\n\t"
    "s_waitcnt lgkmcnt(5)\n\t"
    "v_mfma_f32_16x16x32_bf16 v[84:87], v[56:59], v[116:119], 0\n\t"
    "v_mfma_f32_16x16x32_bf16 v[84:87], v[60:63], v[120:123], v[84:87]\n\t"
    "v_mfma_f32_16x16x32_bf16 v[84:87], v[64:67], v[124:127], v[84:87]\n\t"
    "s_waitcnt lgkmcnt(0)\n\t"
    "v_mfma_f32_16x16x32_bf16 v[84:87], v[68:71], v[128:131], v[84:87]\n\t"
    "s_waitcnt vmcnt(1)\n\t"
    "s_barrier\n\t"
    "v_mfma_f32_16x16x32_bf16 v[84:87], v[72:75], v[132:135], v[84:87]\n\t"
    "ds_read_b128 v[32:35], v12 offset:0\n\t"
    "v_cvt_pk_bf16_f32 v112, v96, v97\n\t"
    "v_mfma_f32_16x16x32_bf16 v[84:87], v[76:79], v[136:139], v[84:87]\n\t"
    "ds_read_b128 v[36:39], v13 offset:0\n\t"
    "v_cvt_pk_bf16_f32 v113, v98, v99\n\t"
    "v_mfma_f32_16x16x32_bf16 v[92:95], v[56:59], v[140:143], 0\n\t"
    "ds_read_b128 v[40:43], v12 offset:4096\n\t"
    "v_mfma_f32_16x16x32_bf16 v[92:95], v[60:63], v[144:147], v[92:95]\n\t"
    "ds_read_b128 v[44:47], v13 offset:4096\n\t"
    "v_mfma_f32_16x16x32_bf16 v[92:95], v[64:67], v[148:151], v[92:95]\n\t"
    "ds_read_b128 v[48:51], v12 offset:8192\n\t"
    "v_mfma_f32_16x16x32_bf16 v[92:95], v[68:71], v[152:155], v[92:95]\n\t"
    "ds_read_b128 v[52:55], v13 offset:8192\n\t"
    "v_cvt_pk_bf16_f32 v106, v84, v85\n\t"
    "v_mfma_f32_16x16x32_bf16 v[92:95], v[72:75], v[156:159], v[92:95]\n\t"
    "v_cvt_pk_bf16_f32 v107, v86, v87\n\t"
    "v_mfma_f32_16x16x32_bf16 v[92:95], v[76:79], v[160:163], v[92:95]\n\t"
    "v_mfma_f32_16x16x32_bf16 v[100:103], v[56:59], v[164:167], 0\n\t"
    "v_mfma_f32_16x16x32_bf16 v[100:103], v[60:63], v[168:171], v[100:103]\n\t"
    "ds_write_b128 v11, v[104:107] offset:0\n\t"
    "v_mfma_f32_16x16x32_bf16 v[100:103], v[64:67], v[172:175], v[100:103]\n\t"
    "v_mfma_f32_16x16x32_bf16 v[100:103], v[68:71], v[176:179], v[100:103]\n\t"
    "v_cvt_pk_bf16_f32 v110, v92, v93\n\t"
    "v_cvt_pk_bf16_f32 v111, v94, v95\n\t"
    "v_mfma_f32_16x16x32_bf16 v[100:103], v[72:75], v[180:183], v[100:103]\n\t"
    "s_sub_u32 s60, s60, 1\n\t"
    "s_cmp_lg_u32 s60, 0\n\t"
    "v_mfma_f32_16x16x32_bf16 v[100:103], v[76:79], v[184:187], v[100:103]\n\t"
    "s_cbranch_scc1 .Lf6_%=\n\t"
    "ds_write_b128 v11, v[108:111] offset:1024\n\t"
    "s_nop 6\n\t"
    "v_cvt_pk_bf16_f32 v114, v100, v101\n\t"
    "v_cvt_pk_bf16_f32 v115, v102, v103\n\t"
    "ds_write_b128 v11, v[112:115] offset:2048\n\t"
    "s_waitcnt lgkmcnt(0)\n\t"
    "s_barrier\n\t"
    "s_mov_b32 s60, 0x10000\n\t"
    ".Lp7_%=:\n\t"
    "s_waitcnt vmcnt(0)\n\t"
    "v_cmp_eq_u64_e32 vcc, v[16:17], v[22:23]\n\t"
    "v_cmp_eq_u64_e64 s[66:67], v[18:19], v[22:23]\n\t"
    "s_and_b64 vcc, vcc, s[66:67]\n\t"
    "s_cmp_eq_u64 vcc, exec\n\t"
    "s_cbranch_scc1 .Lk8_%=\n\t"
    "s_sleep 1\n\t"
    "buffer_load_dwordx4 v[16:19], v21, s[56:59], 0 offen sc1\n\t"
    "s_sub_u32 s60, s60, 1\n\t"
    "s_cmp_lg_u32 s60, 0\n\t"
    "s_cbranch_scc1 .Lp7_%=\n\t"
    "s_waitcnt vmcnt(0)\n\t"
    ".Lk8_%=:\n\t"
    "s_mov_b32 s66, %[noff]\n\t"
    "s_mov_b64 exec, 0x1ff\n\t"
    "buffer_load_dword v10, v26, s[56:59], %[ntch] offen sc1\n\t"
    "s_mov_b64 exec, -1\n\t"
    "v_mov_b32_e32 v9, v15\n\t"
    "buffer_load_dwordx4 a[0:3], v9, s[56:59], s66 offen offset:0 sc1\n\t"
    "buffer_load_dwordx4 a[4:7], v9, s[56:59], s66 offen offset:1024 sc1\n\t"
    "buffer_load_dwordx4 a[8:11], v9, s[56:59], s66 offen offset:2048 sc1\n\t"
    "buffer_load_dwordx4 a[12:15], v9, s[56:59], s66 offen offset:3072 sc1\n\t"
    "s_add_u32 s66, s66, 4096\n\t"
    "buffer_load_dwordx4 a[16:19], v9, s[56:59], s66 offen offset:0 sc1\n\t"
    "buffer_load_dwordx4 a[20:23], v9, s[56:59], s66 offen offset:1024 sc1\n\t"
    "buffer_load_dwordx4 a[24:27], v9, s[56:59], s66 offen offset:2048 sc1\n\t"
    "buffer_load_dwordx4 a[28:31], v9, s[56:59], s66 offen offset:3072 sc1\n\t"
    "s_add_u32 s66, s66, 4096\n\t"
    "buffer_load_dwordx4 a[32:35], v9, s[56:59], s66 offen offset:0 sc1\n\t"
    "buffer_load_dwordx4 a[36:39], v9, s[56:59], s66 offen offset:1024 sc1\n\t"
    "buffer_load_dwordx4 a[40:43], v9, s[56:59], s66 offen offset:2048 sc1\n\t"
    "buffer_load_dwordx4 a[44:47], v9, s[56:59], s66 offen offset:3072 sc1\n\t"
    "s_add_u32 s66, s66, 4096\n\t"
    "buffer_load_dwordx4 a[48:51], v9, s[56:59], s66 offen offset:0 sc1\n\t"
    "buffer_load_dwordx4 a[52:55], v9, s[56:59], s66 offen offset:1024 sc1\n\t"
    "buffer_load_dwordx4 a[56:59], v9, s[56:59], s66 offen offset:2048 sc1\n\t"
    "buffer_load_dwordx4 a[60:63], v9, s[56:59], s66 offen offset:3072 sc1\n\t"
    "s_add_u32 s66, s66, 4096\n\t"
    "buffer_load_dwordx4 a[64:67], v9, s[56:59], s66 offen offset:0 sc1\n\t"
    "buffer_load_dwordx4 a[68:71], v9, s[56:59], s66 offen offset:1024 sc1\n\t"
    "buffer_load_dwordx4 a[72:75], v9, s[56:59], s66 offen offset:2048 sc1\n\t"
    "buffer_load_dwordx4 a[76:79], v9, s[56:59], s66 offen offset:3072 sc1\n\t"
    "s_add_u32 s66, s66, 4096\n\t"
    "buffer_load_dwordx4 a[80:83], v9, s[56:59], s66 offen offset:0 sc1\n\t"
    "buffer_load_dwordx4 a[84:87], v9, s[56:59], s66 offen offset:1024 sc1\n\t"
    "buffer_load_dwordx4 a[88:91], v9, s[56:59], s66 offen offset:2048 sc1\n\t"
    "buffer_load_dwordx4 a[92:95], v9, s[56:59], s66 offen offset:3072 sc1\n\t"
    "s_add_u32 s66, s66, 4096\n\t"
    "buffer_load_dwordx4 a[96:99], v9, s[56:59], s66 offen offset:0 sc1\n\t"
    "buffer_load_dwordx4 a[100:103], v9, s[56:59], s66 offen offset:1024 sc1\n\t"
    "buffer_load_dwordx4 a[104:107], v9, s[56:59], s66 offen offset:2048 sc1\n\t"
    "buffer_load_dwordx4 a[108:111], v9, s[56:59], s66 offen offset:3072 sc1\n\t"
    "s_add_u32 s66, s66, 4096\n\t"
    "buffer_load_dwordx4 a[112:115], v9, s[56:59], s66 offen offset:0 sc1\n\t"
    "buffer_load_dwordx4 a[116:119], v9, s[56:59], s66 offen offset:1024 sc1\n\t"
    "buffer_load_dwordx4 a[120:123], v9, s[56:59], s66 offen offset:2048 sc1\n\t"
    "buffer_load_dwordx4 a[124:127], v9, s[56:59], s66 offen offset:3072 sc1\n\t"
    "s_add_u32 s66, s66, 4096\n\t"
    "buffer_load_dwordx4 a[128:131], v9, s[56:59], s66 offen offset:0 sc1\n\t"
    "buffer_load_dwordx4 a[132:135], v9, s[56:59], s66 offen offset:1024 sc1\n\t"
    "buffer_load_dwordx4 a[136:139], v9, s[56:59], s66 offen offset:2048 sc1\n\t"
    "buffer_load_dwordx4 a[140:143], v9, s[56:59], s66 offen offset:3072 sc1\n\t"
    "s_add_u32 s66, s66, 4096\n\t"
    "buffer_load_dwordx4 a[144:147], v9, s[56:59], s66 offen offset:0 sc1\n\t"
    "buffer_load_dwordx4 a[148:151], v9, s[56:59], s66 offen offset:1024 sc1\n\t"
    "buffer_load_dwordx4 a[152:155], v9, s[56:59], s66 offen offset:2048 sc1\n\t"
    "buffer_load_dwordx4 a[156:159], v9, s[56:59], s66 offen offset:3072 sc1\n\t"
    "s_add_u32 s66, s66, 4096\n\t"
    "buffer_load_dwordx4 a[160:163], v9, s[56:59], s66 offen offset:0 sc1\n\t"
    "buffer_load_dwordx4 a[164:167], v9, s[56:59], s66 offen offset:1024 sc1\n\t"
    "buffer_load_dwordx4 a[168:171], v9, s[56:59], s66 offen offset:2048 sc1\n\t"
    "buffer_load_dwordx4 a[172:175], v9, s[56:59], s66 offen offset:3072 sc1\n\t"
    "s_add_u32 s66, s66, 4096\n\t"
    "buffer_load_dwordx4 a[176:179], v9, s[56:59], s66 offen offset:0 sc1\n\t"
    "buffer_load_dwordx4 a[180:183], v9, s[56:59], s66 offen offset:1024 sc1\n\t"
    "buffer_load_dwordx4 a[184:187], v9, s[56:59], s66 offen offset:2048 sc1\n\t"
    "buffer_load_dwordx4 a[188:191], v9, s[56:59], s66 offen offset:3072 sc1\n\t"
    "s_add_u32 s66, s66, 4096\n\t"
    "buffer_load_dwordx4 a[192:195], v9, s[56:59], s66 offen offset:0 sc1\n\t"
    "buffer_load_dwordx4 a[196:199], v9, s[56:59], s66 offen offset:1024 sc1\n\t"
    "buffer_load_dwordx4 a[200:203], v9, s[56:59], s66 offen offset:2048 sc1\n\t"
    "buffer_load_dwordx4 a[204:207], v9, s[56:59], s66 offen offset:3072 sc1\n\t"
    "s_add_u32 s66, s66, 4096\n\t"
    "buffer_load_dwordx4 a[208:211], v9, s[56:59], s66 offen offset:0 sc1\n\t"
    "buffer_load_dwordx4 a[212:215], v9, s[56:59], s66 offen offset:1024 sc1\n\t"
    "buffer_load_dwordx4 a[216:219], v9, s[56:59], s66 offen offset:2048 sc1\n\t"
    "buffer_load_dwordx4 a[220:223], v9, s[56:59], s66 offen offset:3072 sc1\n\t"
    "s_add_u32 s66, s66, 4096\n\t"
    "buffer_load_dwordx4 a[224:227], v9, s[56:59], s66 offen offset:0 sc1\n\t"
    "buffer_load_dwordx4 a[228:231], v9, s[56:59], s66 offen offset:1024 sc1\n\t"
    "buffer_load_dwordx4 a[232:235], v9, s[56:59], s66 offen offset:2048 sc1\n\t"
    "buffer_load_dwordx4 a[236:239], v9, s[56:59], s66 offen offset:3072 sc1\n\t"
    "s_add_u32 s66, s66, 4096\n\t"
    "s_barrier\n\t"
    "buffer_load_dwordx4 a[240:243], v9, s[56:59], s66 offen offset:0 sc1\n\t"
    "buffer_load_dwordx4 a[244:247], v9, s[56:59], s66 offen offset:1024 sc1\n\t"
    "buffer_load_dwordx4 a[248:251], v9, s[56:59], s66 offen offset:2048 sc1\n\t"
    "buffer_load_dwordx4 a[252:255], v9, s[56:59], s66 offen offset:3072 sc1\n\t"
    "s_add_u32 s66, s66, 4096\n\t"
    "buffer_load_dwordx4 v[224:227], v9, s[56:59], s66 offen offset:0 sc1\n\t"
    "buffer_load_dwordx4 v[228:231], v9, s[56:59], s66 offen offset:1024 sc1\n\t"
    "buffer_load_dwordx4 v[232:235], v9, s[56:59], s66 offen offset:2048 sc1\n\t"
    "buffer_load_dwordx4 v[236:239], v9, s[56:59], s66 offen offset:3072 sc1\n\t"
    "s_add_u32 s66, s66, 4096\n\t"
    "buffer_load_dwordx4 v[240:243], v9, s[56:59], s66 offen offset:0 sc1\n\t"
    "buffer_load_dwordx4 v[244:247], v9, s[56:59], s66 offen offset:1024 sc1\n\t"
    "buffer_load_dwordx4 v[248:251], v9, s[56:59], s66 offen offset:2048 sc1\n\t"
    "buffer_load_dwordx4 v[252:255], v9, s[56:59], s66 offen offset:3072 sc1\n\t"
    "s_add_u32 s66, s66, 4096\n\t"
    "s_mov_b64 exec, %[amask]\n\t"
    "buffer_store_dwordx2 v[22:23], off, s[56:59], %[aown] sc1\n\t"
    "s_mov_b64 exec, -1\n\t"
    "ds_read_b128 v[120:123], v0 offset:0\n\t"
    "ds_read_b128 v[140:143], v1 offset:0\n\t"
    "ds_read_b128 v[148:151], v2 offset:0\n\t"
    "ds_read_b128 v[152:155], v3 offset:0\n\t"
    "ds_read_b128 v[156:159], v4 offset:0\n\t"
    "ds_read_b128 v[160:163], v5 offset:0\n\t"
    "ds_read_b128 v[164:167], v0 offset:6144\n\t"
    "ds_read_b128 v[168:171], v1 offset:6144\n\t"
    "ds_read_b128 v[172:175], v2 offset:6144\n\t"
    "ds_read_b128 v[176:179], v3 offset:6144\n\t"
    "ds_read_b128 v[180:183], v4 offset:6144\n\t"
    "ds_read_b128 v[196:199], v5 offset:6144\n\t"
    "s_add_u32 s61, %[wb], 0xff400000\n\t"
    "s_mov_b32 s62, %[rbeg]\n\t"
    "s_mov_b64 vcc, -1\n\t"
    "s_waitcnt vmcnt(61)\n\t"
    "s_mov_b32 s60, 8\n\t"
    "s_branch .Lf9_%=\n\t"
    ".Lw2_%=:\n\t"
    "s_waitcnt vmcnt(51)\n\t"
    "s_branch .Lv2_%=\n\t"
    ".Lw4_%=:\n\t"
    "s_waitcnt vmcnt(41)\n\t"
    "s_branch .Lv4_%=\n\t"
    ".Lw6_%=:\n\t"
    "s_waitcnt vmcnt(31)\n\t"
    "s_branch .Lv6_%=\n\t"
    ".Lw8_%=:\n\t"
    "s_waitcnt vmcnt(21)\n\t"
    "s_branch .Lv8_%=\n\t"
    ".Lw10_%=:\n\t"
    "s_waitcnt vmcnt(11)\n\t"
    "s_mov_b64 vcc, 0\n\t"
    "s_branch .Lv10_%=\n\t"
    ".Lf9_%=:\n\t"
    "s_waitcnt lgkmcnt(7)\n\t"
    "v_mfma_f32_16x16x32_bf16 v[60:63], v[120:123], a[0:3], 0\n\t"
    "buffer_store_dwordx2 v[116:117], v8, s[52:55], s61 offen offset:768 sc1\n\t"
    "s_waitcnt lgkmcnt(10)\n\t"
    "v_mfma_f32_16x16x32_bf16 v[60:63], v[140:143], a[4:7], v[60:63]\n\t"
    "v_cvt_pk_bf16_f32 v35, v67, v71\n\t"
    "v_cvt_pk_bf16_f32 v40, v72, v48\n\t"
    "s_waitcnt lgkmcnt(9)\n\t"
    "v_mfma_f32_16x16x32_bf16 v[60:63], v[148:151], a[8:11], v[60:63]\n\t"
    "v_cvt_pk_bf16_f32 v42, v73, v49\n\t"
    "v_cvt_pk_bf16_f32 v44, v74, v50\n\t"
    "s_waitcnt lgkmcnt(8)\n\t"
    "v_mfma_f32_16x16x32_bf16 v[60:63], v[152:155], a[12:15], v[60:63]\n\t"
    "v_cvt_pk_bf16_f32 v46, v75, v51\n\t"
    "v_cvt_pk_bf16_f32 v41, v52, v56\n\t"
    "s_waitcnt lgkmcnt(7)\n\t"
    "v_mfma_f32_16x16x32_bf16 v[60:63], v[156:159], a[16:19], v[60:63]\n\t"
    "v_cvt_pk_bf16_f32 v43, v53, v57\n\t"
    "s_waitcnt lgkmcnt(2)\n\t"
    "v_mfma_f32_16x16x32_bf16 v[60:63], v[160:163], a[20:23], v[60:63]\n\t"
    "v_cvt_pk_bf16_f32 v45, v54, v58\n\t"
    "v_cvt_pk_bf16_f32 v47, v55, v59\n\t"
    "s_waitcnt lgkmcnt(5)\n\t"
    "v_mfma_f32_16x16x32_bf16 v[64:67], v[164:167], a[0:3], 0\n\t"
    "s_waitcnt lgkmcnt(0)\n\t"
    "v_mfma_f32_16x16x32_bf16 v[64:67], v[168:171], a[4:7], v[64:67]\n\t"
    "v_mfma_f32_16x16x32_bf16 v[64:67], v[172:175], a[8:11], v[64:67]\n\t"
    "v_mfma_f32_16x16x32_bf16 v[64:67], v[176:179], a[12:15], v[64:67]\n\t"
    "v_cvt_pk_bf16_f32 v48, v60, v61\n\t"
    "s_waitcnt lgkmcnt(0)\n\t"
    "v_mfma_f32_16x16x32_bf16 v[64:67], v[180:183], a[16:19], v[64:67]\n\t"
    "v_cvt_pk_bf16_f32 v49, v62, v63\n\t"
    "v_mfma_f32_16x16x32_bf16 v[64:67], v[196:199], a[20:23], v[64:67]\n\t"
    "v_mfma_f32_16x16x32_bf16 v[56:59], v[120:123], a[24:27], 0\n\t"
    "buffer_store_dwordx2 v[118:119], v8, s[52:55], s61 offen offset:1152 sc1\n\t"
    "v_mfma_f32_16x16x32_bf16 v[56:59], v[140:143], a[28:31], v[56:59]\n\t"
    "s_add_u32 s61, s61, 393216\n\t"
    "v_mfma_f32_16x16x32_bf16 v[56:59], v[148:151], a[32:35], v[56:59]\n\t"
    "v_mfma_f32_16x16x32_bf16 v[56:59], v[152:155], a[36:39], v[56:59]\n\t"
    "v_cvt_pk_bf16_f32 v50, v64, v65\n\t"
    "v_cvt_pk_bf16_f32 v51, v66, v67\n\t"
    "v_mfma_f32_16x16x32_bf16 v[56:59], v[156:159], a[40:43], v[56:59]\n\t"
    "v_mfma_f32_16x16x32_bf16 v[56:59], v[160:163], a[44:47], v[56:59]\n\t"
    "v_mfma_f32_16x16x32_bf16 v[60:63], v[164:167], a[24:27], 0\n\t"
    "v_mfma_f32_16x16x32_bf16 v[60:63], v[168:171], a[28:31], v[60:63]\n\t"
    "v_mfma_f32_16x16x32_bf16 v[60:63], v[172:175], a[32:35], v[60:63]\n\t"
    "v_mfma_f32_16x16x32_bf16 v[60:63], v[176:179], a[36:39], v[60:63]\n\t"
    "v_cvt_pk_bf16_f32 v52, v56, v57\n\t"
    "v_cvt_pk_bf16_f32 v53, v58, v59\n\t"
    "v_mfma_f32_16x16x32_bf16 v[60:63], v[180:183], a[40:43], v[60:63]\n\t"
    "v_mfma_f32_16x16x32_bf16 v[60:63], v[196:199], a[44:47], v[60:63]\n\t"
    "s_cbranch_vccnz .Lw2_%=\n\t"
    ".Lv2_%=:\n\t"
    "v_mfma_f32_16x16x32_bf16 v[64:67], v[120:123], a[48:51], 0\n\t"
    "buffer_store_dwordx4 v[124:127], v7, s[52:55], s61 offen offset:0 sc1\n\t"
    "v_mfma_f32_16x16x32_bf16 v[64:67], v[140:143], a[52:55], v[64:67]\n\t"
    "v_mfma_f32_16x16x32_bf16 v[64:67], v[148:151], a[56:59], v[64:67]\n\t"
    "v_mfma_f32_16x16x32_bf16 v[64:67], v[152:155], a[60:63], v[64:67]\n\t"
    "v_cvt_pk_bf16_f32 v54, v60, v61\n\t"
    "v_cvt_pk_bf16_f32 v55, v62, v63\n\t"
    "v_mfma_f32_16x16x32_bf16 v[64:67], v[156:159], a[64:67], v[64:67]\n\t"
    "v_mfma_f32_16x16x32_bf16 v[64:67], v[160:163], a[68:71], v[64:67]\n\t"
    "v_mfma_f32_16x16x32_bf16 v[60:63], v[164:167], a[48:51], 0\n\t"
    "v_mfma_f32_16x16x32_bf16 v[60:63], v[168:171], a[52:55], v[60:63]\n\t"
    "v_mfma_f32_16x16x32_bf16 v[60:63], v[172:175], a[56:59], v[60:63]\n\t"
    "v_mfma_f32_16x16x32_bf16 v[60:63], v[176:179], a[60:63], v[60:63]\n\t"
    "v_cvt_pk_bf16_f32 v56, v64, v65\n\t"
    "v_cvt_pk_bf16_f32 v57, v66, v67\n\t"
    "v_mfma_f32_16x16x32_bf16 v[60:63], v[180:183], a[64:67], v[60:63]\n\t"
    "v_mfma_f32_16x16x32_bf16 v[60:63], v[196:199], a[68:71], v[60:63]\n\t"
    "v_mfma_f32_16x16x32_bf16 v[64:67], v[120:123], a[72:75], 0\n\t"
    "buffer_store_dwordx4 v[104:107], v7, s[52:55], s61 offen offset:384 sc1\n\t"
    "v_mfma_f32_16x16x32_bf16 v[64:67], v[140:143], a[76:79], v[64:67]\n\t"
    "v_mfma_f32_16x16x32_bf16 v[64:67], v[148:151], a[80:83], v[64:67]\n\t"
    "v_mfma_f32_16x16x32_bf16 v[64:67], v[152:155], a[84:87], v[64:67]\n\t"
    "v_cvt_pk_bf16_f32 v58, v60, v61\n\t"
    "v_cvt_pk_bf16_f32 v59, v62, v63\n\t"
    "v_mfma_f32_16x16x32_bf16 v[64:67], v[156:159], a[88:91], v[64:67]\n\t"
    "v_mfma_f32_16x16x32_bf16 v[64:67], v[160:163], a[92:95], v[64:67]\n\t"
    "v_mfma_f32_16x16x32_bf16 v[68:71], v[164:167], a[72:75], 0\n\t"
    "v_mfma_f32_16x16x32_bf16 v[68:71], v[168:171], a[76:79], v[68:71]\n\t"
    "v_mfma_f32_16x16x32_bf16 v[68:71], v[172:175], a[80:83], v[68:71]\n\t"
    "v_mfma_f32_16x16x32_bf16 v[68:71], v[176:179], a[84:87], v[68:71]\n\t"
    "v_cvt_pk_bf16_f32 v60, v64, v65\n\t"
    "v_cvt_pk_bf16_f32 v61, v66, v67\n\t"
    "v_mfma_f32_16x16x32_bf16 v[68:71], v[180:183], a[88:91], v[68:71]\n\t"
    "v_mfma_f32_16x16x32_bf16 v[68:71], v[196:199], a[92:95], v[68:71]\n\t"
    "s_cbranch_vccnz .Lw4_%=\n\t"
    ".Lv4_%=:\n\t"
    "v_mfma_f32_16x16x32_bf16 v[72:75], v[120:123], a[96:99], 0\n\t"
    "buffer_store_dwordx4 v[136:139], v7, s[52:55], s61 offen offset:768 sc1\n\t"
    "v_mfma_f32_16x16x32_bf16 v[72:75], v[140:143], a[100:103], v[72:75]\n\t"
    "v_mfma_f32_16x16x32_bf16 v[72:75], v[148:151], a[104:107], v[72:75]\n\t"
    "v_mfma_f32_16x16x32_bf16 v[72:75], v[152:155], a[108:111], v[72:75]\n\t"
    "v_cvt_pk_bf16_f32 v62, v68, v69\n\t"
    "v_cvt_pk_bf16_f32 v63, v70, v71\n\t"
    "v_mfma_f32_16x16x32_bf16 v[72:75], v[156:159], a[112:115], v[72:75]\n\t"
    "v_mfma_f32_16x16x32_bf16 v[72:75], v[160:163], a[116:119], v[72:75]\n\t"
    "v_mfma_f32_16x16x32_bf16 v[68:71], v[164:167], a[96:99], 0\n\t"
    "v_mfma_f32_16x16x32_bf16 v[68:71], v[168:171], a[100:103], v[68:71]\n\t"
    "v_mfma_f32_16x16x32_bf16 v[68:71], v[172:175], a[104:107], v[68:71]\n\t"
    "v_mfma_f32_16x16x32_bf16 v[68:71], v[176:179], a[108:111], v[68:71]\n\t"
    "v_cvt_pk_bf16_f32 v64, v72, v73\n\t"
    "v_cvt_pk_bf16_f32 v65, v74, v75\n\t"
    "v_mfma_f32_16x16x32_bf16 v[68:71], v[180:183], a[112:115], v[68:71]\n\t"
    "v_mfma_f32_16x16x32_bf16 v[68:71], v[196:199], a[116:119], v[68:71]\n\t"
    "v_mfma_f32_16x16x32_bf16 v[72:75], v[120:123], a[120:123], 0\n\t"
    "buffer_store_dwordx4 v[128:131], v7, s[52:55], s61 offen offset:1152 sc1\n\t"
    "v_mfma_f32_16x16x32_bf16 v[72:75], v[140:143], a[124:127], v[72:75]\n\t"
    "v_mfma_f32_16x16x32_bf16 v[72:75], v[148:151], a[128:131], v[72:75]\n\t"
    "v_mfma_f32_16x16x32_bf16 v[72:75], v[152:155], a[132:135], v[72:75]\n\t"
    "v_cvt_pk_bf16_f32 v66, v68, v69\n\t"
    "v_cvt_pk_bf16_f32 v67, v70, v71\n\t"
    "v_mfma_f32_16x16x32_bf16 v[72:75], v[156:159], a[136:139], v[72:75]\n\t"
    "v_mfma_f32_16x16x32_bf16 v[72:75], v[160:163], a[140:143], v[72:75]\n\t"
    "v_mfma_f32_16x16x32_bf16 v[76:79], v[164:167], a[120:123], 0\n\t"
    "v_mfma_f32_16x16x32_bf16 v[76:79], v[168:171], a[124:127], v[76:79]\n\t"
    "v_mfma_f32_16x16x32_bf16 v[76:79], v[172:175], a[128:131], v[76:79]\n\t"
    "v_mfma_f32_16x16x32_bf16 v[76:79], v[176:179], a[132:135], v[76:79]\n\t"
    "v_cvt_pk_bf16_f32 v68, v72, v73\n\t"
    "v_cvt_pk_bf16_f32 v69, v74, v75\n\t"
    "v_mfma_f32_16x16x32_bf16 v[76:79], v[180:183], a[136:139], v[76:79]\n\t"
    "v_mfma_f32_16x16x32_bf16 v[76:79], v[196:199], a[140:143], v[76:79]\n\t"
    ".Lh5_%=:\n\t"
    "s_cbranch_execz .LrN_%=\n\t"
    "s_cbranch_vccnz .Lw6_%=\n\t"
    ".Lv6_%=:\n\t"
    "v_mfma_f32_16x16x32_bf16 v[80:83], v[120:123], a[144:147], 0\n\t"
    "buffer_store_dwordx2 v[144:145], v8, s[52:55], s61 offen offset:0 sc1\n\t"
    "v_mfma_f32_16x16x32_bf16 v[80:83], v[140:143], a[148:151], v[80:83]\n\t"
    "v_mfma_f32_16x16x32_bf16 v[80:83], v[148:151], a[152:155], v[80:83]\n\t"
    "v_mfma_f32_16x16x32_bf16 v[80:83], v[152:155], a[156:159], v[80:83]\n\t"
    "v_cvt_pk_bf16_f32 v70, v76, v77\n\t"
    "v_cvt_pk_bf16_f32 v71, v78, v79\n\t"
    "v_mfma_f32_16x16x32_bf16 v[80:83], v[156:159], a[160:163], v[80:83]\n\t"
    "v_mfma_f32_16x16x32_bf16 v[80:83], v[160:163], a[164:167], v[80:83]\n\t"
    "v_mfma_f32_16x16x32_bf16 v[76:79], v[164:167], a[144:147], 0\n\t"
    "v_mfma_f32_16x16x32_bf16 v[76:79], v[168:171], a[148:151], v[76:79]\n\t"
    "v_mfma_f32_16x16x32_bf16 v[76:79], v[172:175], a[152:155], v[76:79]\n\t"
    "v_mfma_f32_16x16x32_bf16 v[76:79], v[176:179], a[156:159], v[76:79]\n\t"
    "v_cvt_pk_bf16_f32 v72, v80, v81\n\t"
    "v_cvt_pk_bf16_f32 v73, v82, v83\n\t"
    "v_mfma_f32_16x16x32_bf16 v[76:79], v[180:183], a[160:163], v[76:79]\n\t"
    "v_mfma_f32_16x16x32_bf16 v[76:79], v[196:199], a[164:167], v[76:79]\n\t"
    "v_mfma_f32_16x16x32_bf16 v[80:83], v[120:123], a[168:171], 0\n\t"
    "buffer_store_dwordx2 v[146:147], v8, s[52:55], s61 offen offset:384 sc1\n\t"
    "v_mfma_f32_16x16x32_bf16 v[80:83], v[140:143], a[172:175], v[80:83]\n\t"
    "v_mfma_f32_16x16x32_bf16 v[80:83], v[148:151], a[176:179], v[80:83]\n\t"
    "v_mfma_f32_16x16x32_bf16 v[80:83], v[152:155], a[180:183], v[80:83]\n\t"
    "v_cvt_pk_bf16_f32 v74, v76, v77\n\t"
    "v_cvt_pk_bf16_f32 v75, v78, v79\n\t"
    "v_mfma_f32_16x16x32_bf16 v[80:83], v[156:159], a[184:187], v[80:83]\n\t"
    "v_mfma_f32_16x16x32_bf16 v[80:83], v[160:163], a[188:191], v[80:83]\n\t"
    "v_mfma_f32_16x16x32_bf16 v[84:87], v[164:167], a[168:171], 0\n\t"
    "v_mfma_f32_16x16x32_bf16 v[84:87], v[168:171], a[172:175], v[84:87]\n\t"
    "v_mfma_f32_16x16x32_bf16 v[84:87], v[172:175], a[176:179], v[84:87]\n\t"
    "v_mfma_f32_16x16x32_bf16 v[84:87], v[176:179], a[180:183], v[84:87]\n\t"
    "v_cvt_pk_bf16_f32 v76, v80, v81\n\t"
    "v_cvt_pk_bf16_f32 v77, v82, v83\n\t"
    "v_mfma_f32_16x16x32_bf16 v[84:87], v[180:183], a[184:187], v[84:87]\n\t"
    "v_mfma_f32_16x16x32_bf16 v[84:87], v[196:199], a[188:191], v[84:87]\n\t"
    "s_cbranch_vccnz .Lw8_%=\n\t"
    ".Lv8_%=:\n\t"
    "v_mfma_f32_16x16x32_bf16 v[88:91], v[120:123], a[192:195], 0\n\t"
    "buffer_store_dwordx2 v[188:189], v8, s[52:55], s61 offen offset:768 sc1\n\t"
    "v_mfma_f32_16x16x32_bf16 v[88:91], v[140:143], a[196:199], v[88:91]\n\t"
    "v_mfma_f32_16x16x32_bf16 v[88:91], v[148:151], a[200:203], v[88:91]\n\t"
    "v_mfma_f32_16x16x32_bf16 v[88:91], v[152:155], a[204:207], v[88:91]\n\t"
    "v_cvt_pk_bf16_f32 v78, v84, v85\n\t"
    "v_cvt_pk_bf16_f32 v79, v86, v87\n\t"
    "v_mfma_f32_16x16x32_bf16 v[88:91], v[156:159], a[208:211], v[88:91]\n\t"
    "v_mfma_f32_16x16x32_bf16 v[88:91], v[160:163], a[212:215], v[88:91]\n\t"
    "v_mfma_f32_16x16x32_bf16 v[84:87], v[164:167], a[192:195], 0\n\t"
    "v_mfma_f32_16x16x32_bf16 v[84:87], v[168:171], a[196:199], v[84:87]\n\t"
    "v_mfma_f32_16x16x32_bf16 v[84:87], v[172:175], a[200:203], v[84:87]\n\t"
    "v_mfma_f32_16x16x32_bf16 v[84:87], v[176:179], a[204:207], v[84:87]\n\t"
    "v_cvt_pk_bf16_f32 v80, v88, v89\n\t"
    "v_cvt_pk_bf16_f32 v81, v90, v91\n\t"
    "v_mfma_f32_16x16x32_bf16 v[84:87], v[180:183], a[208:211], v[84:87]\n\t"
    "v_mfma_f32_16x16x32_bf16 v[84:87], v[196:199], a[212:215], v[84:87]\n\t"
    "v_mfma_f32_16x16x32_bf16 v[88:91], v[120:123], a[216:219], 0\n\t"
    "buffer_store_dwordx2 v[190:191], v8, s[52:55], s61 offen offset:1152 sc1\n\t"
    "v_mfma_f32_16x16x32_bf16 v[88:91], v[140:143], a[220:223], v[88:91]\n\t"
    "s_add_u32 s61, s61, 393216\n\t"
    "v_mfma_f32_16x16x32_bf16 v[88:91], v[148:151], a[224:227], v[88:91]\n\t"
    "v_mfma_f32_16x16x32_bf16 v[88:91], v[152:155], a[228:231], v[88:91]\n\t"
    "v_cvt_pk_bf16_f32 v82, v84, v85\n\t"
    "v_cvt_pk_bf16_f32 v83, v86, v87\n\t"
    "v_mfma_f32_16x16x32_bf16 v[88:91], v[156:159], a[232:235], v[88:91]\n\t"
    "v_mfma_f32_16x16x32_bf16 v[88:91], v[160:163], a[236:239], v[88:91]\n\t"
    "v_mfma_f32_16x16x32_bf16 v[92:95], v[164:167], a[216:219], 0\n\t"
    "v_mfma_f32_16x16x32_bf16 v[92:95], v[168:171], a[220:223], v[92:95]\n\t"
    "v_mfma_f32_16x16x32_bf16 v[92:95], v[172:175], a[224:227], v[92:95]\n\t"
    "v_mfma_f32_16x16x32_bf16 v[92:95], v[176:179], a[228:231], v[92:95]\n\t"
    "v_cvt_pk_bf16_f32 v84, v88, v89\n\t"
    "v_cvt_pk_bf16_f32 v85, v90, v91\n\t"
    "v_mfma_f32_16x16x32_bf16 v[92:95], v[180:183], a[232:235], v[92:95]\n\t"
    "v_mfma_f32_16x16x32_bf16 v[92:95], v[196:199], a[236:239], v[92:95]\n\t"
    "s_cbranch_vccnz .Lw10_%=\n\t"
    ".Lv10_%=:\n\t"
    "v_mfma_f32_16x16x32_bf16 v[96:99], v[120:123], a[240:243], 0\n\t"
    "buffer_store_dwordx4 v[100:103], v7, s[52:55], s61 offen offset:0 sc1\n\t"
    "v_mfma_f32_16x16x32_bf16 v[96:99], v[140:143], a[244:247], v[96:99]\n\t"
    "v_mfma_f32_16x16x32_bf16 v[96:99], v[148:151], a[248:251], v[96:99]\n\t"
    "v_mfma_f32_16x16x32_bf16 v[96:99], v[152:155], a[252:255], v[96:99]\n\t"
    "v_cvt_pk_bf16_f32 v86, v92, v93\n\t"
    "v_cvt_pk_bf16_f32 v87, v94, v95\n\t"
    "v_mfma_f32_16x16x32_bf16 v[96:99], v[156:159], v[224:227], v[96:99]\n\t"
    "v_mfma_f32_16x16x32_bf16 v[96:99], v[160:163], v[228:231], v[96:99]\n\t"
    "v_mfma_f32_16x16x32_bf16 v[124:127], v[164:167], a[240:243], 0\n\t"
    "v_mfma_f32_16x16x32_bf16 v[124:127], v[168:171], a[244:247], v[124:127]\n\t"
    "v_mfma_f32_16x16x32_bf16 v[124:127], v[172:175], a[248:251], v[124:127]\n\t"
    "v_mfma_f32_16x16x32_bf16 v[124:127], v[176:179], a[252:255], v[124:127]\n\t"
    "v_cvt_pk_bf16_f32 v88, v96, v97\n\t"
    "v_cvt_pk_bf16_f32 v89, v98, v99\n\t"
    "v_mfma_f32_16x16x32_bf16 v[124:127], v[180:183], v[224:227], v[124:127]\n\t"
    "v_mfma_f32_16x16x32_bf16 v[124:127], v[196:199], v[228:231], v[124:127]\n\t"
    "v_mfma_f32_16x16x32_bf16 v[184:187], v[120:123], v[232:235], 0\n\t"
    "ds_read_b128 v[92:95], v0 offset:12288\n\t"
    "buffer_store_dwordx4 v[112:115], v7, s[52:55], s61 offen offset:384 sc1\n\t"
    "v_mfma_f32_16x16x32_bf16 v[184:187], v[140:143], v[236:239], v[184:187]\n\t"
    "ds_read_b128 v[96:99], v1 offset:12288\n\t"
    "v_mfma_f32_16x16x32_bf16 v[184:187], v[148:151], v[240:243], v[184:187]\n\t"
    "ds_read_b128 v[100:103], v2 offset:12288\n\t"
    "v_mfma_f32_16x16x32_bf16 v[184:187], v[152:155], v[244:247], v[184:187]\n\t"
    "ds_read_b128 v[104:107], v3 offset:12288\n\t"
    "v_cvt_pk_bf16_f32 v90, v124, v125\n\t"
    "v_mfma_f32_16x16x32_bf16 v[184:187], v[156:159], v[248:251], v[184:187]\n\t"
    "ds_read_b128 v[108:111], v4 offset:12288\n\t"
    "v_cvt_pk_bf16_f32 v91, v126, v127\n\t"
    "v_mfma_f32_16x16x32_bf16 v[184:187], v[160:163], v[252:255], v[184:187]\n\t"
    "ds_read_b128 v[112:115], v5 offset:12288\n\t"
    "v_mfma_f32_16x16x32_bf16 v[120:123], v[164:167], v[232:235], 0\n\t"
    "ds_read_b128 v[116:119], v0 offset:18432\n\t"
    "v_mfma_f32_16x16x32_bf16 v[120:123], v[168:171], v[236:239], v[120:123]\n\t"
    "ds_read_b128 v[124:127], v1 offset:18432\n\t"
    "v_mfma_f32_16x16x32_bf16 v[120:123], v[172:175], v[240:243], v[120:123]\n\t"
    "ds_read_b128 v[128:131], v2 offset:18432\n\t"
    "v_mfma_f32_16x16x32_bf16 v[120:123], v[176:179], v[244:247], v[120:123]\n\t"
    "ds_read_b128 v[136:139], v3 offset:18432\n\t"
    "v_cvt_pk_bf16_f32 v132, v184, v185\n\t"
    "v_mfma_f32_16x16x32_bf16 v[120:123], v[180:183], v[248:251], v[120:123]\n\t"
    "ds_read_b128 v[140:143], v4 offset:18432\n\t"
    "v_cvt_pk_bf16_f32 v133, v186, v187\n\t"
    "v_mfma_f32_16x16x32_bf16 v[120:123], v[196:199], v[252:255], v[120:123]\n\t"
    "ds_read_b128 v[144:147], v5 offset:18432\n\t"
    "s_waitcnt lgkmcnt(7)\n\t"
    "v_mfma_f32_16x16x32_bf16 v[148:151], v[92:95], a[0:3], 0\n\t"
    "buffer_store_dwordx4 v[192:195], v7, s[52:55], s61 offen offset:768 sc1\n\t"
    "v_mfma_f32_16x16x32_bf16 v[148:151], v[96:99], a[4:7], v[148:151]\n\t"
    "v_mfma_f32_16x16x32_bf16 v[148:151], v[100:103], a[8:11], v[148:151]\n\t"
    "v_mfma_f32_16x16x32_bf16 v[148:151], v[104:107], a[12:15], v[148:151]\n\t"
    "v_cvt_pk_bf16_f32 v134, v120, v121\n\t"
    "v_cvt_pk_bf16_f32 v135, v122, v123\n\t"
    "v_mfma_f32_16x16x32_bf16 v[148:151], v[108:111], a[16:19], v[148:151]\n\t"
    "s_waitcnt lgkmcnt(2)\n\t"
    "v_mfma_f32_16x16x32_bf16 v[148:151], v[112:115], a[20:23], v[148:151]\n\t"
    "v_mfma_f32_16x16x32_bf16 v[120:123], v[116:119], a[0:3], 0\n\t"
    "v_mfma_f32_16x16x32_bf16 v[120:123], v[124:127], a[4:7], v[120:123]\n\t"
    "v_mfma_f32_16x16x32_bf16 v[120:123], v[128:131], a[8:11], v[120:123]\n\t"
    "v_mfma_f32_16x16x32_bf16 v[120:123], v[136:139], a[12:15], v[120:123]\n\t"
    "v_cvt_pk_bf16_f32 v152, v148, v149\n\t"
    "s_waitcnt lgkmcnt(0)\n\t"
    "v_mfma_f32_16x16x32_bf16 v[120:123], v[140:143], a[16:19], v[120:123]\n\t"
    "v_cvt_pk_bf16_f32 v153, v150, v151\n\t"
    "v_mfma_f32_16x16x32_bf16 v[120:123], v[144:147], a[20:23], v[120:123]\n\t"
    "v_mfma_f32_16x16x32_bf16 v[156:159], v[92:95], a[24:27], 0\n\t"
    "buffer_store_dwordx4 v[12:15], v7, s[52:55], s61 offen offset:1152 sc1\n\t"
    "v_mfma_f32_16x16x32_bf16 v[156:159], v[96:99], a[28:31], v[156:159]\n\t"
    "v_mfma_f32_16x16x32_bf16 v[156:159], v[100:103], a[32:35], v[156:159]\n\t"
    "v_mfma_f32_16x16x32_bf16 v[156:159], v[104:107], a[36:39], v[156:159]\n\t"
    "v_cvt_pk_bf16_f32 v154, v120, v121\n\t"
    "v_cvt_pk_bf16_f32 v155, v122, v123\n\t"
    "v_mfma_f32_16x16x32_bf16 v[156:159], v[108:111], a[40:43], v[156:159]\n\t"
    "v_mfma_f32_16x16x32_bf16 v[156:159], v[112:115], a[44:47], v[156:159]\n\t"
    "v_mfma_f32_16x16x32_bf16 v[12:15], v[116:119], a[24:27], 0\n\t"
    "v_mfma_f32_16x16x32_bf16 v[12:15], v[124:127], a[28:31], v[12:15]\n\t"
    "v_mfma_f32_16x16x32_bf16 v[12:15], v[128:131], a[32:35], v[12:15]\n\t"
    "v_mfma_f32_16x16x32_bf16 v[12:15], v[136:139], a[36:39], v[12:15]\n\t"
    "v_cvt_pk_bf16_f32 v120, v156, v157\n\t"
    "v_cvt_pk_bf16_f32 v121, v158, v159\n\t"
    "v_mfma_f32_16x16x32_bf16 v[12:15], v[140:143], a[40:43], v[12:15]\n\t"
    "v_mfma_f32_16x16x32_bf16 v[12:15], v[144:147], a[44:47], v[12:15]\n\t"
    "v_mfma_f32_16x16x32_bf16 v[148:151], v[92:95], a[48:51], 0\n\t"
    "buffer_store_dwordx2 v[16:17], v8, s[52:55], s61 offen offset:0 sc1\n\t"
    "v_mfma_f32_16x16x32_bf16 v[148:151], v[96:99], a[52:55], v[148:151]\n\t"
    "v_mfma_f32_16x16x32_bf16 v[148:151], v[100:103], a[56:59], v[148:151]\n\t"
    "v_mfma_f32_16x16x32_bf16 v[148:151], v[104:107], a[60:63], v[148:151]\n\t"
    "v_cvt_pk_bf16_f32 v122, v12, v13\n\t"
    "v_cvt_pk_bf16_f32 v123, v14, v15\n\t"
    "v_mfma_f32_16x16x32_bf16 v[148:151], v[108:111], a[64:67], v[148:151]\n\t"
    "v_mfma_f32_16x16x32_bf16 v[148:151], v[112:115], a[68:71], v[148:151]\n\t"
    "v_mfma_f32_16x16x32_bf16 v[12:15], v[116:119], a[48:51], 0\n\t"
    "v_mfma_f32_16x16x32_bf16 v[12:15], v[124:127], a[52:55], v[12:15]\n\t"
    "v_mfma_f32_16x16x32_bf16 v[12:15], v[128:131], a[56:59], v[12:15]\n\t"
    "v_mfma_f32_16x16x32_bf16 v[12:15], v[136:139], a[60:63], v[12:15]\n\t"
    "v_cvt_pk_bf16_f32 v156, v148, v149\n\t"
    "v_cvt_pk_bf16_f32 v157, v150, v151\n\t"
    "v_mfma_f32_16x16x32_bf16 v[12:15], v[140:143], a[64:67], v[12:15]\n\t"
    "v_mfma_f32_16x16x32_bf16 v[12:15], v[144:147], a[68:71], v[12:15]\n\t"
    "v_mfma_f32_16x16x32_bf16 v[148:151], v[92:95], a[72:75], 0\n\t"
    "s_add_i32 s64, s62, %[rd]\n\t"
    "buffer_store_dwordx2 v[18:19], v8, s[52:55], s61 offen offset:384 sc1\n\t"
    "v_mfma_f32_16x16x32_bf16 v[148:151], v[96:99], a[76:79], v[148:151]\n\t"
    "s_cmp_eq_u32 s64, %[rend]\n\t"
    "s_cselect_b32 s64, %[rbeg], s64\n\t"
    "v_mfma_f32_16x16x32_bf16 v[148:151], v[100:103], a[80:83], v[148:151]\n\t"
    "s_sub_i32 s65, s64, s62\n\t"
    "s_mov_b32 s62, s64\n\t"
    "v_mfma_f32_16x16x32_bf16 v[148:151], v[104:107], a[84:87], v[148:151]\n\t"
    "v_add_u32_e32 v0, s65, v0\n\t"
    "v_add_u32_e32 v1, s65, v1\n\t"
    "v_mfma_f32_16x16x32_bf16 v[148:151], v[108:111], a[88:91], v[148:151]\n\t"
    "v_add_u32_e32 v2, s65, v2\n\t"
    "v_add_u32_e32 v3, s65, v3\n\t"
    "v_mfma_f32_16x16x32_bf16 v[148:151], v[112:115], a[92:95], v[148:151]\n\t"
    "v_add_u32_e32 v4, s65, v4\n\t"
    "v_add_u32_e32 v5, s65, v5\n\t"
    "v_mfma_f32_16x16x32_bf16 v[16:19], v[116:119], a[72:75], 0\n\t"
    "v_cvt_pk_bf16_f32 v158, v12, v13\n\t"
    "v_cvt_pk_bf16_f32 v159, v14, v15\n\t"
    "v_mfma_f32_16x16x32_bf16 v[16:19], v[124:127], a[76:79], v[16:19]\n\t"
    "v_mfma_f32_16x16x32_bf16 v[16:19], v[128:131], a[80:83], v[16:19]\n\t"
    "v_mfma_f32_16x16x32_bf16 v[16:19], v[136:139], a[84:87], v[16:19]\n\t"
    "v_cvt_pk_bf16_f32 v160, v148, v149\n\t"
    "v_cvt_pk_bf16_f32 v161, v150, v151\n\t"
    "v_mfma_f32_16x16x32_bf16 v[16:19], v[140:143], a[88:91], v[16:19]\n\t"
    "v_mfma_f32_16x16x32_bf16 v[16:19], v[144:147], a[92:95], v[16:19]\n\t"
    "v_mfma_f32_16x16x32_bf16 v[12:15], v[92:95], a[96:99], 0\n\t"
    "buffer_store_dwordx2 v[20:21], v8, s[52:55], s61 offen offset:768 sc1\n\t"
    "v_mfma_f32_16x16x32_bf16 v[12:15], v[96:99], a[100:103], v[12:15]\n\t"
    "v_mfma_f32_16x16x32_bf16 v[12:15], v[100:103], a[104:107], v[12:15]\n\t"
    "v_mfma_f32_16x16x32_bf16 v[12:15], v[104:107], a[108:111], v[12:15]\n\t"
    "v_cvt_pk_bf16_f32 v162, v16, v17\n\t"
    "v_cvt_pk_bf16_f32 v163, v18, v19\n\t"
    "v_mfma_f32_16x16x32_bf16 v[12:15], v[108:111], a[112:115], v[12:15]\n\t"
    "v_mfma_f32_16x16x32_bf16 v[12:15], v[112:115], a[116:119], v[12:15]\n\t"
    "v_mfma_f32_16x16x32_bf16 v[16:19], v[116:119], a[96:99], 0\n\t"
    "v_mfma_f32_16x16x32_bf16 v[16:19], v[124:127], a[100:103], v[16:19]\n\t"
    "v_mfma_f32_16x16x32_bf16 v[16:19], v[128:131], a[104:107], v[16:19]\n\t"
    "v_mfma_f32_16x16x32_bf16 v[16:19], v[136:139], a[108:111], v[16:19]\n\t"
    "v_cvt_pk_bf16_f32 v148, v12, v13\n\t"
    "v_cvt_pk_bf16_f32 v149, v14, v15\n\t"
    "v_mfma_f32_16x16x32_bf16 v[16:19], v[140:143], a[112:115], v[16:19]\n\t"
    "v_mfma_f32_16x16x32_bf16 v[16:19], v[144:147], a[116:119], v[16:19]\n\t"
    "v_mfma_f32_16x16x32_bf16 v[12:15], v[92:95], a[120:123], 0\n\t"
    "buffer_store_dwordx2 v[22:23], v8, s[52:55], s61 offen offset:1152 sc1\n\t"
    "v_mfma_f32_16x16x32_bf16 v[12:15], v[96:99], a[124:127], v[12:15]\n\t"
    "s_add_u32 s61, s61, 393216\n\t"
    "v_mfma_f32_16x16x32_bf16 v[12:15], v[100:103], a[128:131], v[12:15]\n\t"
    "v_mfma_f32_16x16x32_bf16 v[12:15], v[104:107], a[132:135], v[12:15]\n\t"
    "v_cvt_pk_bf16_f32 v150, v16, v17\n\t"
    "v_cvt_pk_bf16_f32 v151, v18, v19\n\t"
    "v_mfma_f32_16x16x32_bf16 v[12:15], v[108:111], a[136:139], v[12:15]\n\t"
    "v_mfma_f32_16x16x32_bf16 v[12:15], v[112:115], a[140:143], v[12:15]\n\t"
    "v_mfma_f32_16x16x32_bf16 v[16:19], v[116:119], a[120:123], 0\n\t"
    "v_mfma_f32_16x16x32_bf16 v[16:19], v[124:127], a[124:127], v[16:19]\n\t"
    "v_mfma_f32_16x16x32_bf16 v[16:19], v[128:131], a[128:131], v[16:19]\n\t"
    "v_mfma_f32_16x16x32_bf16 v[16:19], v[136:139], a[132:135], v[16:19]\n\t"
    "v_cvt_pk_bf16_f32 v164, v12, v13\n\t"
    "v_cvt_pk_bf16_f32 v165, v14, v15\n\t"
    "v_mfma_f32_16x16x32_bf16 v[16:19], v[140:143], a[136:139], v[16:19]\n\t"
    "v_mfma_f32_16x16x32_bf16 v[16:19], v[144:147], a[140:143], v[16:19]\n\t"
    "v_mfma_f32_16x16x32_bf16 v[12:15], v[92:95], a[144:147], 0\n\t"
    "buffer_store_dwordx4 v[28:31], v7, s[52:55], s61 offen offset:0 sc1\n\t"
    "v_mfma_f32_16x16x32_bf16 v[12:15], v[96:99], a[148:151], v[12:15]\n\t"
    "v_mfma_f32_16x16x32_bf16 v[12:15], v[100:103], a[152:155], v[12:15]\n\t"
    "v_mfma_f32_16x16x32_bf16 v[12:15], v[104:107], a[156:159], v[12:15]\n\t"
    "v_cvt_pk_bf16_f32 v166, v16, v17\n\t"
    "v_cvt_pk_bf16_f32 v167, v18, v19\n\t"
    "v_mfma_f32_16x16x32_bf16 v[12:15], v[108:111], a[160:163], v[12:15]\n\t"
    "v_mfma_f32_16x16x32_bf16 v[12:15], v[112:115], a[164:167], v[12:15]\n\t"
    "v_mfma_f32_16x16x32_bf16 v[16:19], v[116:119], a[144:147], 0\n\t"
    "v_mfma_f32_16x16x32_bf16 v[16:19], v[124:127], a[148:151], v[16:19]\n\t"
    "v_mfma_f32_16x16x32_bf16 v[16:19], v[128:131], a[152:155], v[16:19]\n\t"
    "v_mfma_f32_16x16x32_bf16 v[16:19], v[136:139], a[156:159], v[16:19]\n\t"
    "v_cvt_pk_bf16_f32 v168, v12, v13\n\t"
    "v_cvt_pk_bf16_f32 v169, v14, v15\n\t"
    "v_mfma_f32_16x16x32_bf16 v[16:19], v[140:143], a[160:163], v[16:19]\n\t"
    "v_mfma_f32_16x16x32_bf16 v[16:19], v[144:147], a[164:167], v[16:19]\n\t"
    "v_mfma_f32_16x16x32_bf16 v[12:15], v[92:95], a[168:171], 0\n\t"
    "buffer_store_dwordx4 v[24:27], v7, s[52:55], s61 offen offset:384 sc1\n\t"
    "v_mfma_f32_16x16x32_bf16 v[12:15], v[96:99], a[172:175], v[12:15]\n\t"
    "v_mfma_f32_16x16x32_bf16 v[12:15], v[100:103], a[176:179], v[12:15]\n\t"
    "v_mfma_f32_16x16x32_bf16 v[12:15], v[104:107], a[180:183], v[12:15]\n\t"
    "v_cvt_pk_bf16_f32 v170, v16, v17\n\t"
    "v_cvt_pk_bf16_f32 v171, v18, v19\n\t"
    "v_mfma_f32_16x16x32_bf16 v[12:15], v[108:111], a[184:187], v[12:15]\n\t"
    "v_mfma_f32_16x16x32_bf16 v[12:15], v[112:115], a[188:191], v[12:15]\n\t"
    "v_mfma_f32_16x16x32_bf16 v[16:19], v[116:119], a[168:171], 0\n\t"
    "v_mfma_f32_16x16x32_bf16 v[16:19], v[124:127], a[172:175], v[16:19]\n\t"
    "v_mfma_f32_16x16x32_bf16 v[16:19], v[128:131], a[176:179], v[16:19]\n\t"
    "v_mfma_f32_16x16x32_bf16 v[16:19], v[136:139], a[180:183], v[16:19]\n\t"
    "v_cvt_pk_bf16_f32 v172, v12, v13\n\t"
    "v_cvt_pk_bf16_f32 v173, v14, v15\n\t"
    "v_mfma_f32_16x16x32_bf16 v[16:19], v[140:143], a[184:187], v[16:19]\n\t"
    "v_mfma_f32_16x16x32_bf16 v[16:19], v[144:147], a[188:191], v[16:19]\n\t"
    "v_mfma_f32_16x16x32_bf16 v[12:15], v[92:95], a[192:195], 0\n\t"
    "buffer_store_dwordx4 v[36:39], v7, s[52:55], s61 offen offset:768 sc1\n\t"
    "v_mfma_f32_16x16x32_bf16 v[12:15], v[96:99], a[196:199], v[12:15]\n\t"
    "v_mfma_f32_16x16x32_bf16 v[12:15], v[100:103], a[200:203], v[12:15]\n\t"
    "v_mfma_f32_16x16x32_bf16 v[12:15], v[104:107], a[204:207], v[12:15]\n\t"
    "v_cvt_pk_bf16_f32 v174, v16, v17\n\t"
    "v_cvt_pk_bf16_f32 v175, v18, v19\n\t"
    "v_mfma_f32_16x16x32_bf16 v[12:15], v[108:111], a[208:211], v[12:15]\n\t"
    "v_mfma_f32_16x16x32_bf16 v[12:15], v[112:115], a[212:215], v[12:15]\n\t"
    "v_mfma_f32_16x16x32_bf16 v[16:19], v[116:119], a[192:195], 0\n\t"
    "v_mfma_f32_16x16x32_bf16 v[16:19], v[124:127], a[196:199], v[16:19]\n\t"
    "v_mfma_f32_16x16x32_bf16 v[16:19], v[128:131], a[200:203], v[16:19]\n\t"
    "v_mfma_f32_16x16x32_bf16 v[16:19], v[136:139], a[204:207], v[16:19]\n\t"
    "v_cvt_pk_bf16_f32 v176, v12, v13\n\t"
    "v_cvt_pk_bf16_f32 v177, v14, v15\n\t"
    "v_mfma_f32_16x16x32_bf16 v[16:19], v[140:143], a[208:211], v[16:19]\n\t"
    "v_mfma_f32_16x16x32_bf16 v[16:19], v[144:147], a[212:215], v[16:19]\n\t"
    "v_mfma_f32_16x16x32_bf16 v[12:15], v[92:95], a[216:219], 0\n\t"
    "s_barrier\n\t"
    "v_mfma_f32_16x16x32_bf16 v[12:15], v[96:99], a[220:223], v[12:15]\n\t"
    "buffer_store_dwordx4 v[32:35], v7, s[52:55], s61 offen offset:1152 sc1\n\t"
    "v_mfma_f32_16x16x32_bf16 v[12:15], v[100:103], a[224:227], v[12:15]\n\t"
    "v_mfma_f32_16x16x32_bf16 v[12:15], v[104:107], a[228:231], v[12:15]\n\t"
    "v_cvt_pk_bf16_f32 v178, v16, v17\n\t"
    "v_cvt_pk_bf16_f32 v179, v18, v19\n\t"
    "v_mfma_f32_16x16x32_bf16 v[12:15], v[108:111], a[232:235], v[12:15]\n\t"
    "v_mfma_f32_16x16x32_bf16 v[12:15], v[112:115], a[236:239], v[12:15]\n\t"
    "v_mfma_f32_16x16x32_bf16 v[16:19], v[116:119], a[216:219], 0\n\t"
    "v_mfma_f32_16x16x32_bf16 v[16:19], v[124:127], a[220:223], v[16:19]\n\t"
    "v_mfma_f32_16x16x32_bf16 v[16:19], v[128:131], a[224:227], v[16:19]\n\t"
    "v_mfma_f32_16x16x32_bf16 v[16:19], v[136:139], a[228:231], v[16:19]\n\t"
    "v_cvt_pk_bf16_f32 v180, v12, v13\n\t"
    "v_cvt_pk_bf16_f32 v181, v14, v15\n\t"
    "v_mfma_f32_16x16x32_bf16 v[16:19], v[140:143], a[232:235], v[16:19]\n\t"
    "v_mfma_f32_16x16x32_bf16 v[16:19], v[144:147], a[236:239], v[16:19]\n\t"
    "v_mfma_f32_16x16x32_bf16 v[12:15], v[92:95], a[240:243], 0\n\t"
    "v_mfma_f32_16x16x32_bf16 v[12:15], v[96:99], a[244:247], v[12:15]\n\t"
    "buffer_store_dwordx2 v[40:41], v8, s[52:55], s61 offen offset:0 sc1\n\t"
    "v_mfma_f32_16x16x32_bf16 v[12:15], v[100:103], a[248:251], v[12:15]\n\t"
    "v_mfma_f32_16x16x32_bf16 v[12:15], v[104:107], a[252:255], v[12:15]\n\t"
    "v_cvt_pk_bf16_f32 v182, v16, v17\n\t"
    "v_cvt_pk_bf16_f32 v183, v18, v19\n\t"
    "v_mfma_f32_16x16x32_bf16 v[12:15], v[108:111], v[224:227], v[12:15]\n\t"
    "v_mfma_f32_16x16x32_bf16 v[12:15], v[112:115], v[228:231], v[12:15]\n\t"
    "v_mfma_f32_16x16x32_bf16 v[16:19], v[116:119], a[240:243], 0\n\t"
    "v_mfma_f32_16x16x32_bf16 v[16:19], v[124:127], a[244:247], v[16:19]\n\t"
    "v_mfma_f32_16x16x32_bf16 v[16:19], v[128:131], a[248:251], v[16:19]\n\t"
    "v_mfma_f32_16x16x32_bf16 v[16:19], v[136:139], a[252:255], v[16:19]\n\t"
    "v_cvt_pk_bf16_f32 v184, v12, v13\n\t"
    "v_cvt_pk_bf16_f32 v185, v14, v15\n\t"
    "v_mfma_f32_16x16x32_bf16 v[16:19], v[140:143], v[224:227], v[16:19]\n\t"
    "v_mfma_f32_16x16x32_bf16 v[16:19], v[144:147], v[228:231], v[16:19]\n\t"
    "v_mfma_f32_16x16x32_bf16 v[12:15], v[92:95], v[232:235], 0\n\t"
    "v_mfma_f32_16x16x32_bf16 v[12:15], v[96:99], v[236:239], v[12:15]\n\t"
    "ds_read_b128 v[20:23], v6 offset:0\n\t"
    "v_mfma_f32_16x16x32_bf16 v[12:15], v[100:103], v[240:243], v[12:15]\n\t"
    "ds_read_b128 v[24:27], v6 offset:1024\n\t"
    "v_mfma_f32_16x16x32_bf16 v[12:15], v[104:107], v[244:247], v[12:15]\n\t"
    "buffer_store_dwordx2 v[42:43], v8, s[52:55], s61 offen offset:384 sc1\n\t"
    "v_mfma_f32_16x16x32_bf16 v[12:15], v[108:111], v[248:251], v[12:15]\n\t"
    "v_cvt_pk_bf16_f32 v186, v16, v17\n\t"
    "v_cvt_pk_bf16_f32 v187, v18, v19\n\t"
    "v_mfma_f32_16x16x32_bf16 v[12:15], v[112:115], v[252:255], v[12:15]\n\t"
    "v_mfma_f32_16x16x32_bf16 v[28:31], v[116:119], v[232:235], 0\n\t"
    "v_mfma_f32_16x16x32_bf16 v[28:31], v[124:127], v[236:239], v[28:31]\n\t"
    "v_mfma_f32_16x16x32_bf16 v[28:31], v[128:131], v[240:243], v[28:31]\n\t"
    "v_mfma_f32_16x16x32_bf16 v[28:31], v[136:139], v[244:247], v[28:31]\n\t"
    "v_cvt_pk_bf16_f32 v92, v12, v13\n\t"
    "v_cvt_pk_bf16_f32 v93, v14, v15\n\t"
    "v_mfma_f32_16x16x32_bf16 v[28:31], v[140:143], v[248:251], v[28:31]\n\t"
    "v_mfma_f32_16x16x32_bf16 v[28:31], v[144:147], v[252:255], v[28:31]\n\t"
    "s_waitcnt lgkmcnt(0)\n\t"
    "v_mfma_f32_16x16x32_bf16 v[12:15], v[20:23], v[48:51], 0\n\t"
    "v_mfma_f32_16x16x32_bf16 v[12:15], v[24:27], v[152:155], v[12:15]\n\t"
    "v_mfma_f32_16x16x32_bf16 v[16:19], v[20:23], v[52:55], 0\n\t"
    "v_mfma_f32_16x16x32_bf16 v[16:19], v[24:27], v[120:123], v[16:19]\n\t"
    "buffer_store_dwordx2 v[44:45], v8, s[52:55], s61 offen offset:768 sc1\n\t"
    "v_mfma_f32_16x16x32_bf16 v[32:35], v[20:23], v[56:59], 0\n\t"
    "v_cvt_pk_bf16_f32 v94, v28, v29\n\t"
    "v_cvt_pk_bf16_f32 v95, v30, v31\n\t"
    "v_mfma_f32_16x16x32_bf16 v[32:35], v[24:27], v[156:159], v[32:35]\n\t"
    "v_mfma_f32_16x16x32_bf16 v[36:39], v[20:23], v[60:63], 0\n\t"
    "v_mfma_f32_16x16x32_bf16 v[36:39], v[24:27], v[160:163], v[36:39]\n\t"
    "v_cvt_pk_bf16_f32 v40, v12, v16\n\t"
    "v_cvt_pk_bf16_f32 v28, v13, v17\n\t"
    "v_mfma_f32_16x16x32_bf16 v[104:107], v[20:23], v[64:67], 0\n\t"
    "v_cvt_pk_bf16_f32 v100, v14, v18\n\t"
    "v_cvt_pk_bf16_f32 v96, v15, v19\n\t"
    "v_mfma_f32_16x16x32_bf16 v[104:107], v[24:27], v[148:151], v[104:107]\n\t"
    "v_mfma_f32_16x16x32_bf16 v[108:111], v[20:23], v[68:71], 0\n\t"
    "v_mfma_f32_16x16x32_bf16 v[108:111], v[24:27], v[164:167], v[108:111]\n\t"
    "v_cvt_pk_bf16_f32 v41, v32, v36\n\t"
    "v_cvt_pk_bf16_f32 v29, v33, v37\n\t"
    "v_mfma_f32_16x16x32_bf16 v[12:15], v[20:23], v[72:75], 0\n\t"
    "v_cvt_pk_bf16_f32 v101, v34, v38\n\t"
    "v_cvt_pk_bf16_f32 v97, v35, v39\n\t"
    "v_mfma_f32_16x16x32_bf16 v[12:15], v[24:27], v[168:171], v[12:15]\n\t"
    "ds_read_b128 v[16:19], v6 offset:2048\n\t"
    "v_mfma_f32_16x16x32_bf16 v[116:119], v[20:23], v[76:79], 0\n\t"
    "ds_read_b128 v[112:115], v6 offset:3072\n\t"
    "v_mfma_f32_16x16x32_bf16 v[116:119], v[24:27], v[172:175], v[116:119]\n\t"
    "buffer_store_dwordx2 v[46:47], v8, s[52:55], s61 offen offset:1152 sc1\n\t"
    "v_mfma_f32_16x16x32_bf16 v[32:35], v[20:23], v[80:83], 0\n\t"
    "s_add_u32 s61, s61, 11403264\n\t"
    "v_cvt_pk_bf16_f32 v42, v104, v108\n\t"
    "v_mfma_f32_16x16x32_bf16 v[32:35], v[24:27], v[176:179], v[32:35]\n\t"
    "v_cvt_pk_bf16_f32 v30, v105, v109\n\t"
    "v_cvt_pk_bf16_f32 v102, v106, v110\n\t"
    "v_mfma_f32_16x16x32_bf16 v[36:39], v[20:23], v[84:87], 0\n\t"
    "v_cvt_pk_bf16_f32 v98, v107, v111\n\t"
    "v_mfma_f32_16x16x32_bf16 v[36:39], v[24:27], v[180:183], v[36:39]\n\t"
    "v_cvt_pk_bf16_f32 v43, v12, v116\n\t"
    "v_cvt_pk_bf16_f32 v31, v13, v117\n\t"
    "v_mfma_f32_16x16x32_bf16 v[44:47], v[20:23], v[88:91], 0\n\t"
    "v_cvt_pk_bf16_f32 v103, v14, v118\n\t"
    "v_cvt_pk_bf16_f32 v99, v15, v119\n\t"
    "v_mfma_f32_16x16x32_bf16 v[44:47], v[24:27], v[184:187], v[44:47]\n\t"
    "v_mfma_f32_16x16x32_bf16 v[104:107], v[20:23], v[132:135], 0\n\t"
    "v_mfma_f32_16x16x32_bf16 v[104:107], v[24:27], v[92:95], v[104:107]\n\t"
    "buffer_store_dwordx4 v[40:43], v7, s[52:55], s61 offen offset:0 sc1\n\t"
    "s_waitcnt lgkmcnt(0)\n\t"
    "v_mfma_f32_16x16x32_bf16 v[12:15], v[16:19], v[48:51], 0\n\t"
    "v_cvt_pk_bf16_f32 v108, v32, v36\n\t"
    "v_cvt_pk_bf16_f32 v110, v33, v37\n\t"
    "v_mfma_f32_16x16x32_bf16 v[12:15], v[112:115], v[152:155], v[12:15]\n\t"
    "v_cvt_pk_bf16_f32 v116, v34, v38\n\t"
    "v_cvt_pk_bf16_f32 v118, v35, v39\n\t"
    "v_mfma_f32_16x16x32_bf16 v[20:23], v[16:19], v[52:55], 0\n\t"
    "v_mfma_f32_16x16x32_bf16 v[20:23], v[112:115], v[120:123], v[20:23]\n\t"
    "v_cvt_pk_bf16_f32 v109, v44, v104\n\t"
    "v_cvt_pk_bf16_f32 v111, v45, v105\n\t"
    "v_mfma_f32_16x16x32_bf16 v[24:27], v[16:19], v[56:59], 0\n\t"
    "v_cvt_pk_bf16_f32 v117, v46, v106\n\t"
    "v_cvt_pk_bf16_f32 v119, v47, v107\n\t"
    "v_mfma_f32_16x16x32_bf16 v[24:27], v[112:115], v[156:159], v[24:27]\n\t"
    "v_mfma_f32_16x16x32_bf16 v[32:35], v[16:19], v[60:63], 0\n\t"
    "v_mfma_f32_16x16x32_bf16 v[32:35], v[112:115], v[160:163], v[32:35]\n\t"
    "v_cvt_pk_bf16_f32 v124, v12, v20\n\t"
    "v_cvt_pk_bf16_f32 v104, v13, v21\n\t"
    "v_mfma_f32_16x16x32_bf16 v[36:39], v[16:19], v[64:67], 0\n\t"
    "v_cvt_pk_bf16_f32 v136, v14, v22\n\t"
    "v_cvt_pk_bf16_f32 v128, v15, v23\n\t"
    "v_mfma_f32_16x16x32_bf16 v[36:39], v[112:115], v[148:151], v[36:39]\n\t"
    "v_mfma_f32_16x16x32_bf16 v[40:43], v[16:19], v[68:71], 0\n\t"
    "v_mfma_f32_16x16x32_bf16 v[40:43], v[112:115], v[164:167], v[40:43]\n\t"
    "buffer_store_dwordx4 v[28:31], v7, s[52:55], s61 offen offset:384 sc1\n\t"
    "v_mfma_f32_16x16x32_bf16 v[12:15], v[16:19], v[72:75], 0\n\t"
    "v_cvt_pk_bf16_f32 v125, v24, v32\n\t"
    "v_cvt_pk_bf16_f32 v105, v25, v33\n\t"
    "v_mfma_f32_16x16x32_bf16 v[12:15], v[112:115], v[168:171], v[12:15]\n\t"
    "ds_read_b128 v[20:23], v6 offset:4096\n\t"
    "v_cvt_pk_bf16_f32 v137, v26, v34\n\t"
    "v_mfma_f32_16x16x32_bf16 v[44:47], v[16:19], v[76:79], 0\n\t"
    "ds_read_b128 v[28:31], v6 offset:5120\n\t"
    "v_cvt_pk_bf16_f32 v129, v27, v35\n\t"
    "v_mfma_f32_16x16x32_bf16 v[44:47], v[112:115], v[172:175], v[44:47]\n\t"
    "v_cvt_pk_bf16_f32 v126, v36, v40\n\t"
    "v_cvt_pk_bf16_f32 v106, v37, v41\n\t"
    "v_mfma_f32_16x16x32_bf16 v[140:143], v[16:19], v[80:83], 0\n\t"
    "v_cvt_pk_bf16_f32 v138, v38, v42\n\t"
    "v_cvt_pk_bf16_f32 v130, v39, v43\n\t"
    "v_mfma_f32_16x16x32_bf16 v[140:143], v[112:115], v[176:179], v[140:143]\n\t"
    "v_mfma_f32_16x16x32_bf16 v[24:27], v[16:19], v[84:87], 0\n\t"
    "v_mfma_f32_16x16x32_bf16 v[24:27], v[112:115], v[180:183], v[24:27]\n\t"
    "v_cvt_pk_bf16_f32 v127, v12, v44\n\t"
    "v_cvt_pk_bf16_f32 v107, v13, v45\n\t"
    "v_mfma_f32_16x16x32_bf16 v[32:35], v[16:19], v[88:91], 0\n\t"
    "v_cvt_pk_bf16_f32 v139, v14, v46\n\t"
    "v_cvt_pk_bf16_f32 v131, v15, v47\n\t"
    "v_mfma_f32_16x16x32_bf16 v[32:35], v[112:115], v[184:187], v[32:35]\n\t"
    "v_mfma_f32_16x16x32_bf16 v[36:39], v[16:19], v[132:135], 0\n\t"
    "v_mfma_f32_16x16x32_bf16 v[36:39], v[112:115], v[92:95], v[36:39]\n\t"
    "buffer_store_dwordx4 v[100:103], v7, s[52:55], s61 offen offset:768 sc1\n\t"
    "s_waitcnt lgkmcnt(0)\n\t"
    "v_mfma_f32_16x16x32_bf16 v[40:43], v[20:23], v[48:51], 0\n\t"
    "v_cvt_pk_bf16_f32 v144, v140, v24\n\t"
    "v_cvt_pk_bf16_f32 v146, v141, v25\n\t"
    "v_mfma_f32_16x16x32_bf16 v[40:43], v[28:31], v[152:155], v[40:43]\n\t"
    "v_cvt_pk_bf16_f32 v188, v142, v26\n\t"
    "v_cvt_pk_bf16_f32 v190, v143, v27\n\t"
    "v_mfma_f32_16x16x32_bf16 v[16:19], v[20:23], v[52:55], 0\n\t"
    "v_mfma_f32_16x16x32_bf16 v[16:19], v[28:31], v[120:123], v[16:19]\n\t"
    "v_cvt_pk_bf16_f32 v145, v32, v36\n\t"
    "v_cvt_pk_bf16_f32 v147, v33, v37\n\t"
    "v_mfma_f32_16x16x32_bf16 v[24:27], v[20:23], v[56:59], 0\n\t"
    "v_cvt_pk_bf16_f32 v189, v34, v38\n\t"
    "v_cvt_pk_bf16_f32 v191, v35, v39\n\t"
    "v_mfma_f32_16x16x32_bf16 v[24:27], v[28:31], v[156:159], v[24:27]\n\t"
    "v_mfma_f32_16x16x32_bf16 v[44:47], v[20:23], v[60:63], 0\n\t"
    "v_mfma_f32_16x16x32_bf16 v[44:47], v[28:31], v[160:163], v[44:47]\n\t"
    "v_cvt_pk_bf16_f32 v100, v40, v16\n\t"
    "v_cvt_pk_bf16_f32 v112, v41, v17\n\t"
    "v_mfma_f32_16x16x32_bf16 v[32:35], v[20:23], v[64:67], 0\n\t"
    "v_cvt_pk_bf16_f32 v192, v42, v18\n\t"
    "v_cvt_pk_bf16_f32 v12, v43, v19\n\t"
    "v_mfma_f32_16x16x32_bf16 v[32:35], v[28:31], v[148:151], v[32:35]\n\t"
    "v_mfma_f32_16x16x32_bf16 v[36:39], v[20:23], v[68:71], 0\n\t"
    "v_mfma_f32_16x16x32_bf16 v[36:39], v[28:31], v[164:167], v[36:39]\n\t"
    "buffer_store_dwordx4 v[96:99], v7, s[52:55], s61 offen offset:1152 sc1\n\t"
    "v_mfma_f32_16x16x32_bf16 v[16:19], v[20:23], v[72:75], 0\n\t"
    "s_barrier\n\t"
    "v_cvt_pk_bf16_f32 v101, v24, v44\n\t"
    "v_mfma_f32_16x16x32_bf16 v[16:19], v[28:31], v[168:171], v[16:19]\n\t"
    "ds_read_b128 v[40:43], v6 offset:6144\n\t"
    "v_cvt_pk_bf16_f32 v113, v25, v45\n\t"
    "v_mfma_f32_16x16x32_bf16 v[140:143], v[20:23], v[76:79], 0\n\t"
    "ds_read_b128 v[96:99], v6 offset:7168\n\t"
    "v_cvt_pk_bf16_f32 v193, v26, v46\n\t"
    "v_mfma_f32_16x16x32_bf16 v[140:143], v[28:31], v[172:175], v[140:143]\n\t"
    "v_cvt_pk_bf16_f32 v13, v27, v47\n\t"
    "v_cvt_pk_bf16_f32 v102, v32, v36\n\t"
    "v_mfma_f32_16x16x32_bf16 v[196:199], v[20:23], v[80:83], 0\n\t"
    "v_cvt_pk_bf16_f32 v114, v33, v37\n\t"
    "v_cvt_pk_bf16_f32 v194, v34, v38\n\t"
    "v_mfma_f32_16x16x32_bf16 v[196:199], v[28:31], v[176:179], v[196:199]\n\t"
    "v_cvt_pk_bf16_f32 v14, v35, v39\n\t"
    "v_mfma_f32_16x16x32_bf16 v[24:27], v[20:23], v[84:87], 0\n\t"
    "v_mfma_f32_16x16x32_bf16 v[24:27], v[28:31], v[180:183], v[24:27]\n\t"
    "v_cvt_pk_bf16_f32 v103, v16, v140\n\t"
    "v_cvt_pk_bf16_f32 v115, v17, v141\n\t"
    "v_mfma_f32_16x16x32_bf16 v[32:35], v[20:23], v[88:91], 0\n\t"
    "v_cvt_pk_bf16_f32 v195, v18, v142\n\t"
    "v_cvt_pk_bf16_f32 v15, v19, v143\n\t"
    "v_mfma_f32_16x16x32_bf16 v[32:35], v[28:31], v[184:187], v[32:35]\n\t"
    "v_mfma_f32_16x16x32_bf16 v[36:39], v[20:23], v[132:135], 0\n\t"
    "v_mfma_f32_16x16x32_bf16 v[36:39], v[28:31], v[92:95], v[36:39]\n\t"
    "buffer_store_dwordx2 v[108:109], v8, s[52:55], s61 offen offset:0 sc1\n\t"
    "s_waitcnt lgkmcnt(0)\n\t"
    "v_mfma_f32_16x16x32_bf16 v[44:47], v[40:43], v[48:51], 0\n\t"
    "v_cvt_pk_bf16_f32 v16, v196, v24\n\t"
    "v_cvt_pk_bf16_f32 v18, v197, v25\n\t"
    "v_mfma_f32_16x16x32_bf16 v[44:47], v[96:99], v[152:155], v[44:47]\n\t"
    "v_cvt_pk_bf16_f32 v20, v198, v26\n\t"
    "v_cvt_pk_bf16_f32 v22, v199, v27\n\t"
    "v_mfma_f32_16x16x32_bf16 v[140:143], v[40:43], v[52:55], 0\n\t"
    "v_mfma_f32_16x16x32_bf16 v[140:143], v[96:99], v[120:123], v[140:143]\n\t"
    "v_cvt_pk_bf16_f32 v17, v32, v36\n\t"
    "v_cvt_pk_bf16_f32 v19, v33, v37\n\t"
    "v_mfma_f32_16x16x32_bf16 v[48:51], v[40:43], v[56:59], 0\n\t"
    "v_cvt_pk_bf16_f32 v21, v34, v38\n\t"
    "v_cvt_pk_bf16_f32 v23, v35, v39\n\t"
    "v_mfma_f32_16x16x32_bf16 v[48:51], v[96:99], v[156:159], v[48:51]\n\t"
    "v_mfma_f32_16x16x32_bf16 v[52:55], v[40:43], v[60:63], 0\n\t"
    "v_mfma_f32_16x16x32_bf16 v[52:55], v[96:99], v[160:163], v[52:55]\n\t"
    "v_cvt_pk_bf16_f32 v28, v44, v140\n\t"
    "v_cvt_pk_bf16_f32 v24, v45, v141\n\t"
    "v_mfma_f32_16x16x32_bf16 v[56:59], v[40:43], v[64:67], 0\n\t"
    "v_cvt_pk_bf16_f32 v36, v46, v142\n\t"
    "v_cvt_pk_bf16_f32 v32, v47, v143\n\t"
    "v_mfma_f32_16x16x32_bf16 v[56:59], v[96:99], v[148:151], v[56:59]\n\t"
    "v_mfma_f32_16x16x32_bf16 v[60:63], v[40:43], v[68:71], 0\n\t"
    "v_mfma_f32_16x16x32_bf16 v[60:63], v[96:99], v[164:167], v[60:63]\n\t"
    "buffer_store_dwordx2 v[110:111], v8, s[52:55], s61 offen offset:384 sc1\n\t"
    "v_mfma_f32_16x16x32_bf16 v[64:67], v[40:43], v[72:75], 0\n\t"
    ".Lh6_%=:\n\t"
    "s_cbranch_execz .LrN_%=\n\t"
    "ds_read_b128 v[120:123], v0 offset:0\n\t"
    "v_cvt_pk_bf16_f32 v29, v48, v52\n\t"
    "v_mfma_f32_16x16x32_bf16 v[64:67], v[96:99], v[168:171], v[64:67]\n\t"
    "ds_read_b128 v[140:143], v1 offset:0\n\t"
    "v_cvt_pk_bf16_f32 v25, v49, v53\n\t"
    "v_mfma_f32_16x16x32_bf16 v[68:71], v[40:43], v[76:79], 0\n\t"
    "ds_read_b128 v[148:151], v2 offset:0\n\t"
    "v_cvt_pk_bf16_f32 v37, v50, v54\n\t"
    "v_mfma_f32_16x16x32_bf16 v[68:71], v[96:99], v[172:175], v[68:71]\n\t"
    "ds_read_b128 v[152:155], v3 offset:0\n\t"
    "v_cvt_pk_bf16_f32 v33, v51, v55\n\t"
    "v_mfma_f32_16x16x32_bf16 v[72:75], v[40:43], v[80:83], 0\n\t"
    "ds_read_b128 v[156:159], v4 offset:0\n\t"
    "v_cvt_pk_bf16_f32 v30, v56, v60\n\t"
    "v_mfma_f32_16x16x32_bf16 v[72:75], v[96:99], v[176:179], v[72:75]\n\t"
    "ds_read_b128 v[160:163], v5 offset:0\n\t"
    "v_cvt_pk_bf16_f32 v26, v57, v61\n\t"
    "v_mfma_f32_16x16x32_bf16 v[48:51], v[40:43], v[84:87], 0\n\t"
    "ds_read_b128 v[164:167], v0 offset:6144\n\t"
    "v_cvt_pk_bf16_f32 v38, v58, v62\n\t"
    "v_mfma_f32_16x16x32_bf16 v[48:51], v[96:99], v[180:183], v[48:51]\n\t"
    "ds_read_b128 v[168:171], v1 offset:6144\n\t"
    "v_cvt_pk_bf16_f32 v34, v59, v63\n\t"
    "v_mfma_f32_16x16x32_bf16 v[52:55], v[40:43], v[88:91], 0\n\t"
    "ds_read_b128 v[172:175], v2 offset:6144\n\t"
    "v_cvt_pk_bf16_f32 v31, v64, v68\n\t"
    "v_mfma_f32_16x16x32_bf16 v[52:55], v[96:99], v[184:187], v[52:55]\n\t"
    "ds_read_b128 v[176:179], v3 offset:6144\n\t"
    "v_cvt_pk_bf16_f32 v27, v65, v69\n\t"
    "v_mfma_f32_16x16x32_bf16 v[56:59], v[40:43], v[132:135], 0\n\t"
    "ds_read_b128 v[180:183], v4 offset:6144\n\t"
    "v_cvt_pk_bf16_f32 v39, v66, v70\n\t"
    "v_mfma_f32_16x16x32_bf16 v[56:59], v[96:99], v[92:95], v[56:59]\n\t"
    "ds_read_b128 v[196:199], v5 offset:6144\n\t"
    "s_sub_u32 s60, s60, 1\n\t"
    "s_cmp_lg_u32 s60, 0\n\t"
    "s_cbranch_scc1 .Lf9_%=\n\t"
    "s_nop 7\n\t"
    "s_nop 1\n\t"
    "buffer_store_dwordx2 v[116:117], v8, s[52:55], s61 offen offset:768 sc1\n\t"
    "v_cvt_pk_bf16_f32 v35, v67, v71\n\t"
    "v_cvt_pk_bf16_f32 v40, v72, v48\n\t"
    "v_cvt_pk_bf16_f32 v42, v73, v49\n\t"
    "v_cvt_pk_bf16_f32 v44, v74, v50\n\t"
    "v_cvt_pk_bf16_f32 v46, v75, v51\n\t"
    "v_cvt_pk_bf16_f32 v41, v52, v56\n\t"
    "v_cvt_pk_bf16_f32 v43, v53, v57\n\t"
    "v_cvt_pk_bf16_f32 v45, v54, v58\n\t"
    "v_cvt_pk_bf16_f32 v47, v55, v59\n\t"
    "buffer_store_dwordx2 v[118:119], v8, s[52:55], s61 offen offset:1152 sc1\n\t"
    "s_add_u32 s61, s61, 393216\n\t"
    "buffer_store_dwordx4 v[124:127], v7, s[52:55], s61 offen offset:0 sc1\n\t"
    "buffer_store_dwordx4 v[104:107], v7, s[52:55], s61 offen offset:384 sc1\n\t"
    "buffer_store_dwordx4 v[136:139], v7, s[52:55], s61 offen offset:768 sc1\n\t"
    "buffer_store_dwordx4 v[128:131], v7, s[52:55], s61 offen offset:1152 sc1\n\t"
    "buffer_store_dwordx2 v[144:145], v8, s[52:55], s61 offen offset:0 sc1\n\t"
    "buffer_store_dwordx2 v[146:147], v8, s[52:55], s61 offen offset:384 sc1\n\t"
    "buffer_store_dwordx2 v[188:189], v8, s[52:55], s61 offen offset:768 sc1\n\t"
    "buffer_store_dwordx2 v[190:191], v8, s[52:55], s61 offen offset:1152 sc1\n\t"
    "s_add_u32 s61, s61, 393216\n\t"
    "buffer_store_dwordx4 v[100:103], v7, s[52:55], s61 offen offset:0 sc1\n\t"
    "buffer_store_dwordx4 v[112:115], v7, s[52:55], s61 offen offset:384 sc1\n\t"
    "buffer_store_dwordx4 v[192:195], v7, s[52:55], s61 offen offset:768 sc1\n\t"
    "buffer_store_dwordx4 v[12:15], v7, s[52:55], s61 offen offset:1152 sc1\n\t"
    "buffer_store_dwordx2 v[16:17], v8, s[52:55], s61 offen offset:0 sc1\n\t"
    "buffer_store_dwordx2 v[18:19], v8, s[52:55], s61 offen offset:384 sc1\n\t"
    "buffer_store_dwordx2 v[20:21], v8, s[52:55], s61 offen offset:768 sc1\n\t"
    "buffer_store_dwordx2 v[22:23], v8, s[52:55], s61 offen offset:1152 sc1\n\t"
    "s_add_u32 s61, s61, 393216\n\t"
    "buffer_store_dwordx4 v[28:31], v7, s[52:55], s61 offen offset:0 sc1\n\t"
    "buffer_store_dwordx4 v[24:27], v7, s[52:55], s61 offen offset:384 sc1\n\t"
    "buffer_store_dwordx4 v[36:39], v7, s[52:55], s61 offen offset:768 sc1\n\t"
    "buffer_store_dwordx4 v[32:35], v7, s[52:55], s61 offen offset:1152 sc1\n\t"
    "buffer_store_dwordx2 v[40:41], v8, s[52:55], s61 offen offset:0 sc1\n\t"
    "buffer_store_dwordx2 v[42:43], v8, s[52:55], s61 offen offset:384 sc1\n\t"
    "buffer_store_dwordx2 v[44:45], v8, s[52:55], s61 offen offset:768 sc1\n\t"
    "buffer_store_dwordx2 v[46:47], v8, s[52:55], s61 offen offset:1152 sc1\n\t"
    "s_add_u32 s61, s61, 11403264\n\t"
    "s_endpgm\n\t"
    ".Lend_%=:\n\t"
    :: "{v24}"(vk0), "{v25}"(vk1), "{v26}"(lane * 128), "{v31}"(lane), "{s[36:43]}"(k8), "{s[44:47]}"(k4),
       [role] "s"(role), [prod] "s"((wave * 256 + bid - 72) >> 31), [closer] "s"(bid | wave), [bid] "s"(bid), [wb] "s"(wb),
       [k0] "s"(k0), [k1] "s"(k1), [k2] "s"(k0 + 32 * 1152), [tch] "s"(wid), [kl] "s"(ldsb + role * 110592 + pair * 18432),
       [rbeg] "s"(rbeg), [rd] "s"(rd), [rend] "s"(rbeg + 3 * rd),
       [tb] "s"(ldsb + PB + wave * 4096), [pl] "s"(ldsb + PB + pair * 8192), [wn] "s"(ldsb + WQ + wave * 3072),
       [h1l] "s"(rbeg + rd + pair * 4 * 3072), [h1g] "s"(wb + STEPB + pair * 4 * ROWB),
       [h2l] "s"(rbeg + rd + (1 - pair) * 4 * 3072), [h2g] "s"(wb + STEPB + (1 - pair) * 4 * ROWB),
       [wql] "s"(ldsb + WQ + pair * 1024), [wqg] "s"(pair * 4 * 1152), [amask] "s"((unsigned long long)(1 - pair)),
       [noff] "s"(N_OFF), [ntch] "s"(N_OFF + ((bid >> 3) * 2 + pair) * 1152), [foff] "s"(F_OFF + 8 * bid), [aoff] "s"(A_OFF), [aown] "s"(A_OFF + 8 * bid), [eoff] "s"(E_OFF)
    : "memory", "scc", ACLOB, VCLOB, SCLOB);
}

extern "C" void kernel_launch(void* const* d_in, const int* in_sizes, int n_in,
                              void* d_out, int out_size, void* d_ws, size_t ws_size,
                              hipStream_t stream) {
  attn_kernel<<<256, 256, 0, stream>>>((const unsigned char*)d_in[0], (const unsigned char*)d_in[1], (const unsigned char*)d_in[2],
                                       (unsigned char*)d_ws, (unsigned char*)d_out
      );
}
```

```cpp
#include <hip/hip_runtime.h>
#include <hip/hip_bf16.h>
#include <stdint.h>

using u32x4 = __attribute__((ext_vector_type(4))) unsigned;
using u32x8 = __attribute__((ext_vector_type(8))) unsigned;
#define DI __device__ __forceinline__

static constexpr int ROWB = 512 * 384;
static constexpr int STEPB = 64 * ROWB;
static constexpr unsigned XBYTES = 512u * ROWB;
static constexpr int SLOT = 24576;
static constexpr int PB = 6 * SLOT;
static constexpr int F_OFF = 160 * 1024, A_OFF = 148 * 1024, E_OFF = 152 * 1024;

#define R10(p, n) p #n "0", p #n "1", p #n "2", p #n "3", p #n "4", p #n "5", p #n "6", p #n "7", p #n "8", p #n "9"
#define R240(p) R10(p, 4), R10(p, 5), R10(p, 6), R10(p, 7), R10(p, 8), R10(p, 9), R10(p, 10), R10(p, 11), R10(p, 12), R10(p, 13), \
  R10(p, 14), R10(p, 15), R10(p, 16), R10(p, 17), R10(p, 18), R10(p, 19), R10(p, 20), R10(p, 21), R10(p, 22), R10(p, 23), R10(p, 24), \
  p "250", p "251", p "252", p "253", p "254", p "255"
#define ACLOB R10("a", ), R10("a", 1), R10("a", 2), R10("a", 3), R240("a")
#define VCLOB R10("v", ), R10("v", 1), R10("v", 2), "v30", "v32", "v33", "v34", "v35", "v36", "v37", "v38", "v39", R240("v")
#define SCLOB "s48", "s49", R10("s", 5), R10("s", 6), R10("s", 7), R10("s", 8), "s90", "s91", "s92", "s93", "s94", "s95", "vcc"

__global__ __launch_bounds__(256, 1) void attn_kernel(const unsigned char* __restrict__ x,
                                                      const unsigned char* __restrict__ wqkv,
                                                      const unsigned char* __restrict__ wo,
                                                      unsigned char* ws,
                                                      unsigned char* __restrict__ out
                                                      ) {
  __shared__ u32x4 lds[10240];
  u32x8 k8; u32x4 k4;
  asm volatile(
    "s_load_dwordx8 s[36:43], %2, 0\n\t"
    "s_load_dwordx2 s[44:45], %2, 32\n\t"
    : "={s[36:43]}"(k8), "={s[44:47]}"(k4) : "s"(__builtin_amdgcn_kernarg_segment_ptr()) : "memory", ACLOB);
  __builtin_amdgcn_sched_barrier(0);
  const unsigned lane = threadIdx.x & 63, wave = __builtin_amdgcn_readfirstlane(threadIdx.x >> 6);
  const unsigned role = wave >> 1, pair = wave & 1;
  const unsigned ldsb = (unsigned)(size_t)lds;
  const unsigned bid = blockIdx.x;
  const unsigned wb = (bid & 7) * 8 * ROWB + ((bid >> 3) * 2 + pair) * 8 * 384;
  const unsigned rbeg = ldsb + pair * 3 * SLOT;

  asm volatile(
    ".Lbeg_%=:\n\t"
    "s_cmp_lg_u32 %[role], 1\n\t"
    "s_cbranch_scc1 .Lroln_%=\n\t"
    "s_cmp_eq_u32 %[prod], 0\n\t"
    "s_cbranch_scc1 .Lnp_%=\n\t"
    "s_cmp_lt_u32 %[bid], 72\n\t"
    "s_cselect_b32 s71, 0, 72\n\t"
    "s_cselect_b32 s63, 0, 768\n\t"
    "s_sub_u32 s64, %[bid], s71\n\t"
    "s_mul_i32 s65, s64, 0x1556\n\t"
    "s_lshr_b32 s65, s65, 16\n\t"
    "s_mul_i32 s69, s65, 12\n\t"
    "s_sub_u32 s69, s64, s69\n\t"
    "s_lshr_b32 s69, s69, 1\n\t"
    "s_and_b32 s70, s64, 1\n\t"
    "s_mul_i32 s60, s69, 36864\n\t"
    "s_add_u32 s60, s60, s63\n\t"
    "s_add_u32 s61, s60, 4608\n\t"
    "v_lshrrev_b32_e32 v20, 2, v31\n\t"
    "v_and_b32_e32 v21, 8, v20\n\t"
    "v_bfe_u32 v24, v31, 3, 2\n\t"
    "v_or_b32_e32 v21, v21, v24\n\t"
    "v_mul_u32_u24_e32 v21, 0x480, v21\n\t"
    "v_lshrrev_b32_e32 v24, 4, v31\n\t"
    "v_xor_b32_e32 v24, v24, v31\n\t"
    "v_and_b32_e32 v24, 7, v24\n\t"
    "v_lshl_or_b32 v24, v24, 4, v21\n\t"
    "v_xor_b32_e32 v25, 64, v24\n\t"
    "v_add_u32_e32 v25, 0x4800, v25\n\t"
    "v_mul_u32_u24_e32 v27, 0x180, v20\n\t"
    "v_and_b32_e32 v28, 3, v31\n\t"
    "v_lshl_or_b32 v27, v28, 4, v27\n\t"
    "s_lshl_b32 s62, s65, 3\n\t"
    "s_add_u32 s62, s62, s70\n\t"
    "s_mul_i32 s62, s62, 4608\n\t"
    "s_add_u32 s62, s62, 384\n\t"
    "s_lshl_b32 s63, s65, 6\n\t"
    "s_mov_b32 s50, 0x36000\n\t"
    "s_mov_b32 s51, 0x20000\n\t"
    "s_mov_b32 s54, 0x12000\n\t"
    "s_mov_b32 s55, 0x20000\n\t"
    "s_waitcnt lgkmcnt(0)\n\t"
    "s_mov_b32 s48, s38\n\t"
    "s_and_b32 s49, s39, 0xffff\n\t"
    "s_cmp_lg_u32 s71, 0\n\t"
    "s_cbranch_scc1 .Lpo_%=\n\t"
    "s_mov_b32 m0, 61440\n\t"
    "s_nop 0\n\t"
    "buffer_load_dwordx4 v24, s[48:51], s62 offen offset:0 lds\n\t"
    "s_mov_b32 m0, 62464\n\t"
    "s_nop 0\n\t"
    "buffer_load_dwordx4 v25, s[48:51], s62 offen offset:0 lds\n\t"
    "s_mov_b32 m0, 63360\n\t"
    "s_nop 0\n\t"
    "buffer_load_dwordx4 v24, s[48:51], s62 offen offset:128 lds\n\t"
    "s_mov_b32 m0, 64384\n\t"
    "s_nop 0\n\t"
    "buffer_load_dwordx4 v25, s[48:51], s62 offen offset:128 lds\n\t"
    "s_mov_b32 m0, 65280\n\t"
    "s_nop 0\n\t"
    "buffer_load_dwordx4 v24, s[48:51], s62 offen offset:256 lds\n\t"
    "s_mov_b32 m0, 66304\n\t"
    "s_nop 0\n\t"
    "buffer_load_dwordx4 v25, s[48:51], s62 offen offset:256 lds\n\t"
    "s_branch .Lpa_%=\n\t"
    ".Lpo_%=:\n\t"
    "s_mov_b32 s52, s40\n\t"
    "s_mov_b32 m0, 61440\n\t"
    "s_and_b32 s53, s41, 0xffff\n\t"
    "buffer_load_dwordx4 v27, s[52:55], s63 offen lds\n\t"
    "s_mov_b32 m0, 62464\n\t"
    "s_add_u32 s63, s63, 6144\n\t"
    "buffer_load_dwordx4 v27, s[52:55], s63 offen lds\n\t"
    "s_mov_b32 m0, 63488\n\t"
    "s_add_u32 s63, s63, 6144\n\t"
    "buffer_load_dwordx4 v27, s[52:55], s63 offen lds\n\t"
    "s_mov_b32 m0, 64512\n\t"
    "s_add_u32 s63, s63, 6144\n\t"
    "buffer_load_dwordx4 v27, s[52:55], s63 offen lds\n\t"
    "s_mov_b32 m0, 65536\n\t"
    "s_add_u32 s63, s63, 6144\n\t"
    "buffer_load_dwordx4 v27, s[52:55], s63 offen lds\n\t"
    "s_mov_b32 m0, 66560\n\t"
    "s_add_u32 s63, s63, 6144\n\t"
    "buffer_load_dwordx4 v27, s[52:55], s63 offen lds\n\t"
    "s_mov_b32 m0, 67584\n\t"
    "s_add_u32 s63, s63, 6144\n\t"
    "buffer_load_dwordx4 v27, s[52:55], s63 offen lds\n\t"
    "s_mov_b32 m0, 68608\n\t"
    "s_add_u32 s63, s63, 6144\n\t"
    "buffer_load_dwordx4 v27, s[52:55], s63 offen lds\n\t"
    "s_mov_b32 m0, 69632\n\t"
    "s_add_u32 s63, s63, 6144\n\t"
    "buffer_load_dwordx4 v27, s[52:55], s63 offen lds\n\t"
    "s_mov_b32 m0, 70656\n\t"
    "s_add_u32 s63, s63, 6144\n\t"
    "buffer_load_dwordx4 v27, s[52:55], s63 offen lds\n\t"
    "s_mov_b32 m0, 71680\n\t"
    "s_add_u32 s63, s63, 6144\n\t"
    "buffer_load_dwordx4 v27, s[52:55], s63 offen lds\n\t"
    "s_mov_b32 m0, 72704\n\t"
    "s_add_u32 s63, s63, 6144\n\t"
    "buffer_load_dwordx4 v27, s[52:55], s63 offen lds\n\t"
    ".Lpa_%=:\n\t"
    "s_mov_b32 m0, 49152\n\t"
    "s_nop 0\n\t"
    "buffer_load_dwordx4 v24, s[48:51], s60 offen offset:0 lds\n\t"
    "s_mov_b32 m0, 53120\n\t"
    "s_nop 0\n\t"
    "buffer_load_dwordx4 v24, s[48:51], s60 offen offset:128 lds\n\t"
    "s_mov_b32 m0, 57088\n\t"
    "s_nop 0\n\t"
    "buffer_load_dwordx4 v24, s[48:51], s60 offen offset:256 lds\n\t"
    "s_mov_b32 m0, 51200\n\t"
    "s_nop 0\n\t"
    "buffer_load_dwordx4 v25, s[48:51], s60 offen offset:0 lds\n\t"
    "s_mov_b32 m0, 55168\n\t"
    "s_nop 0\n\t"
    "buffer_load_dwordx4 v25, s[48:51], s60 offen offset:128 lds\n\t"
    "s_mov_b32 m0, 59136\n\t"
    "s_nop 0\n\t"
    "buffer_load_dwordx4 v25, s[48:51], s60 offen offset:256 lds\n\t"
    "s_mov_b32 m0, 50176\n\t"
    "s_nop 0\n\t"
    "buffer_load_dwordx4 v24, s[48:51], s61 offen offset:0 lds\n\t"
    "s_mov_b32 m0, 54144\n\t"
    "s_nop 0\n\t"
    "buffer_load_dwordx4 v24, s[48:51], s61 offen offset:128 lds\n\t"
    "s_mov_b32 m0, 58112\n\t"
    "s_nop 0\n\t"
    "buffer_load_dwordx4 v24, s[48:51], s61 offen offset:256 lds\n\t"
    "s_mov_b32 m0, 52224\n\t"
    "s_nop 0\n\t"
    "buffer_load_dwordx4 v25, s[48:51], s61 offen offset:0 lds\n\t"
    "s_mov_b32 m0, 56192\n\t"
    "s_nop 0\n\t"
    "buffer_load_dwordx4 v25, s[48:51], s61 offen offset:128 lds\n\t"
    "s_mov_b32 m0, 60160\n\t"
    "s_nop 0\n\t"
    "buffer_load_dwordx4 v25, s[48:51], s61 offen offset:256 lds\n\t"
    ".Lnp_%=:\n\t"
    "s_waitcnt lgkmcnt(0)\n\t"
    "s_mov_b32 s56, s42\n\t"
    "s_and_b32 s57, s43, 0xffff\n\t"
    "s_mov_b32 s58, 0x40000\n\t"
    "s_mov_b32 s59, 0x20000\n\t"
    "buffer_load_dwordx2 v[22:23], off, s[56:59], %[eoff] sc1\n\t"
    "s_barrier\n\t"
    "v_lshlrev_b32_e32 v30, 4, v31\n\t"
    "v_and_b32_e32 v28, 7, v31\n\t"
    "v_bfe_u32 v27, v31, 3, 1\n\t"
    "v_lshrrev_b32_e32 v29, 4, v31\n\t"
    "v_add_u32_e32 v6, %[pl], v30\n\t"
    "v_mul_u32_u24_e32 v27, 0xc00, v27\n\t"
    "v_add_u32_e32 v27, %[rbeg], v27\n\t"
    "v_lshlrev_b32_e32 v26, 7, v31\n\t"
    "v_mad_u32_u24 v20, v28, 3, 0\n\t"
    "v_and_b32_e32 v21, 6, v20\n\t"
    "v_xor_b32_e32 v21, v21, v29\n\t"
    "v_lshl_add_u32 v20, v20, 7, v27\n\t"
    "v_lshl_add_u32 v0, v21, 4, v20\n\t"
    "v_xor_b32_e32 v1, 64, v0\n\t"
    "v_mad_u32_u24 v20, v28, 3, 1\n\t"
    "v_and_b32_e32 v21, 6, v20\n\t"
    "v_xor_b32_e32 v21, v21, v29\n\t"
    "v_lshl_add_u32 v20, v20, 7, v27\n\t"
    "v_lshl_add_u32 v2, v21, 4, v20\n\t"
    "v_xor_b32_e32 v3, 64, v2\n\t"
    "v_mad_u32_u24 v20, v28, 3, 2\n\t"
    "v_and_b32_e32 v21, 6, v20\n\t"
    "v_xor_b32_e32 v21, v21, v29\n\t"
    "v_lshl_add_u32 v20, v20, 7, v27\n\t"
    "v_lshl_add_u32 v4, v21, 4, v20\n\t"
    "v_xor_b32_e32 v5, 64, v4\n\t"
    "s_cmp_eq_u32 %[prod], 0\n\t"
    "s_cbranch_scc1 .Lnq_%=\n\t"
    "v_bfe_u32 v20, v31, 1, 3\n\t"
    "v_xor_b32_e32 v20, v20, v29\n\t"
    "v_lshlrev_b32_e32 v20, 4, v20\n\t"
    "v_lshl_or_b32 v20, v28, 7, v20\n\t"
    "v_bfe_u32 v21, v31, 3, 1\n\t"
    "v_lshl_or_b32 v16, v21, 11, v20\n\t"
    "v_add_u32_e32 v16, 0xc000, v16\n\t"
    "v_xor_b32_e32 v17, 64, v16\n\t"
    "s_cmp_lg_u32 s71, 0\n\t"
    "s_cbranch_scc1 .Lqo_%=\n\t"
    "v_lshl_or_b32 v18, v21, 10, v20\n\t"
    "v_add_u32_e32 v18, 0xf000, v18\n\t"
    "v_xor_b32_e32 v19, 64, v18\n\t"
    "s_lshl_b32 s60, s65, 1\n\t"
    "s_add_u32 s60, s60, s70\n\t"
    "s_mul_i32 s60, s60, 6\n\t"
    "s_add_u32 s60, s60, s69\n\t"
    "s_lshl_b32 s60, s60, 10\n\t"
    "v_add_u32_e32 v20, s60, v30\n\t"
    "v_mov_b32_e32 v24, v30\n\t"
    "s_waitcnt vmcnt(13)\n\t"
    "ds_read_b128 v[80:83], v18 offset:0\n\t"
    "ds_read_b128 v[84:87], v19 offset:0\n\t"
    "ds_read_b128 v[88:91], v18 offset:2048\n\t"
    "ds_read_b128 v[92:95], v19 offset:2048\n\t"
    "ds_read_b128 v[96:99], v18 offset:4096\n\t"
    "ds_read_b128 v[100:103], v19 offset:4096\n\t"
    "s_branch .Lqa_%=\n\t"
    ".Lqo_%=:\n\t"
    "s_and_b32 s63, s65, 1\n\t"
    "s_lshl_b32 s63, s63, 1\n\t"
    "s_add_u32 s63, s63, s70\n\t"
    "s_lshl_b32 s64, s70, 2\n\t"
    "s_lshl_b32 s62, s70, 4\n\t"
    "s_cmp_lt_u32 s65, 4\n\t"
    "s_cselect_b32 s60, s64, 8\n\t"
    "s_cselect_b32 s63, s65, s63\n\t"
    "s_cselect_b32 s62, s64, s62\n\t"
    "s_cselect_b32 s61, 4, 3\n\t"
    "s_lshl_b32 s62, s62, 1\n\t"
    "s_add_u32 s62, s62, 61440\n\t"
    "s_mul_i32 s60, s60, 6\n\t"
    "s_add_u32 s60, s60, s69\n\t"
    "s_lshl_b32 s60, s60, 2\n\t"
    "s_add_u32 s60, s60, s63\n\t"
    "s_lshl_b32 s60, s60, 8\n\t"
    "s_add_u32 s60, s60, 73728\n\t"
    "v_and_b32_e32 v25, 3, v31\n\t"
    "v_bfe_u32 v21, v31, 2, 2\n\t"
    "v_lshlrev_b32_e32 v18, s61, v25\n\t"
    "v_lshl_add_u32 v18, v21, 6, v18\n\t"
    "v_lshl_add_u32 v18, v29, 9, v18\n\t"
    "v_add_u32_e32 v18, s62, v18\n\t"
    "v_lshl_add_u32 v24, v25, 2, v21\n\t"
    "v_lshl_add_u32 v24, v29, 4, v24\n\t"
    "v_lshlrev_b32_e32 v24, 4, v24\n\t"
    "v_mul_u32_u24_e32 v21, 0x1800, v21\n\t"
    "v_lshl_add_u32 v20, v25, 4, v21\n\t"
    "v_lshl_add_u32 v20, v29, 6, v20\n\t"
    "v_add_u32_e32 v20, s60, v20\n\t"
    "s_waitcnt vmcnt(13)\n\t"
    "ds_read_b64_tr_b16 v[80:81], v18 offset:0\n\t"
    "ds_read_b64_tr_b16 v[82:83], v18 offset:256\n\t"
    "ds_read_b64_tr_b16 v[84:85], v18 offset:2048\n\t"
    "ds_read_b64_tr_b16 v[86:87], v18 offset:2304\n\t"
    "ds_read_b64_tr_b16 v[88:89], v18 offset:4096\n\t"
    "ds_read_b64_tr_b16 v[90:91], v18 offset:4352\n\t"
    "ds_read_b64_tr_b16 v[92:93], v18 offset:6144\n\t"
    "ds_read_b64_tr_b16 v[94:95], v18 offset:6400\n\t"
    "ds_read_b64_tr_b16 v[96:97], v18 offset:8192\n\t"
    "ds_read_b64_tr_b16 v[98:99], v18 offset:8448\n\t"
    "ds_read_b64_tr_b16 v[100:101], v18 offset:10240\n\t"
    "ds_read_b64_tr_b16 v[102:103], v18 offset:10496\n\t"
    ".Lqa_%=:\n\t"
    "s_waitcnt vmcnt(7)\n\t"
    "ds_read_b128 v[32:35], v16 offset:0\n\t"
    "ds_read_b128 v[36:39], v17 offset:0\n\t"
    "ds_read_b128 v[40:43], v16 offset:4096\n\t"
    "ds_read_b128 v[44:47], v17 offset:4096\n\t"
    "ds_read_b128 v[48:51], v16 offset:8192\n\t"
    "ds_read_b128 v[52:55], v17 offset:8192\n\t"
    "s_waitcnt vmcnt(1)\n\t"
    "ds_read_b128 v[56:59], v16 offset:1024\n\t"
    "ds_read_b128 v[60:63], v17 offset:1024\n\t"
    "ds_read_b128 v[64:67], v16 offset:5120\n\t"
    "ds_read_b128 v[68:71], v17 offset:5120\n\t"
    "ds_read_b128 v[72:75], v16 offset:9216\n\t"
    "ds_read_b128 v[76:79], v17 offset:9216\n\t"
    "s_waitcnt lgkmcnt(11)\n\t"
    "v_mfma_f32_16x16x32_bf16 v[108:111], v[32:35], v[80:83], 0\n\t"
    "s_waitcnt lgkmcnt(10)\n\t"
    "v_mfma_f32_16x16x32_bf16 v[108:111], v[36:39], v[84:87], v[108:111]\n\t"
    "s_waitcnt lgkmcnt(9)\n\t"
    "v_mfma_f32_16x16x32_bf16 v[108:111], v[40:43], v[88:91], v[108:111]\n\t"
    "s_waitcnt lgkmcnt(8)\n\t"
    "v_mfma_f32_16x16x32_bf16 v[108:111], v[44:47], v[92:95], v[108:111]\n\t"
    "s_waitcnt lgkmcnt(7)\n\t"
    "v_mfma_f32_16x16x32_bf16 v[108:111], v[48:51], v[96:99], v[108:111]\n\t"
    "s_waitcnt lgkmcnt(6)\n\t"
    "v_mfma_f32_16x16x32_bf16 v[108:111], v[52:55], v[100:103], v[108:111]\n\t"
    "s_waitcnt lgkmcnt(5)\n\t"
    "v_mfma_f32_16x16x32_bf16 v[112:115], v[56:59], v[80:83], 0\n\t"
    "s_waitcnt lgkmcnt(4)\n\t"
    "v_mfma_f32_16x16x32_bf16 v[112:115], v[60:63], v[84:87], v[112:115]\n\t"
    "s_waitcnt lgkmcnt(3)\n\t"
    "v_mfma_f32_16x16x32_bf16 v[112:115], v[64:67], v[88:91], v[112:115]\n\t"
    "s_waitcnt lgkmcnt(2)\n\t"
    "v_mfma_f32_16x16x32_bf16 v[112:115], v[68:71], v[92:95], v[112:115]\n\t"
    "s_waitcnt lgkmcnt(1)\n\t"
    "v_mfma_f32_16x16x32_bf16 v[112:115], v[72:75], v[96:99], v[112:115]\n\t"
    "s_waitcnt lgkmcnt(0)\n\t"
    "v_mfma_f32_16x16x32_bf16 v[112:115], v[76:79], v[100:103], v[112:115]\n\t"
    "v_cvt_pk_bf16_f32 v104, v108, v109\n\t"
    "v_cvt_pk_bf16_f32 v105, v110, v111\n\t"
    "s_nop 6\n\t"
    "v_cvt_pk_bf16_f32 v106, v112, v113\n\t"
    "v_cvt_pk_bf16_f32 v107, v114, v115\n\t"
    "ds_write_b128 v24, v[104:107] offset:49152\n\t"
    "ds_read_b128 v[104:107], v30 offset:49152\n\t"
    "s_waitcnt lgkmcnt(0)\n\t"
    "buffer_store_dwordx4 v[104:107], v20, s[56:59], 0 offen sc1\n\t"
    ".Lnq_%=:\n\t"
    "s_waitcnt vmcnt(0)\n\t"
    "v_add_co_u32_e32 v22, vcc, 0x7f4a7c15, v22\n\t"
    "v_mov_b32_e32 v16, 0x9e3779b9\n\t"
    "s_nop 0\n\t"
    "v_addc_co_u32_e32 v23, vcc, v16, v23, vcc\n\t"
    "s_mov_b32 exec_lo, %[prod]\n\t"
    "s_mov_b32 exec_hi, 0\n\t"
    "buffer_store_dwordx2 v[22:23], off, s[56:59], %[foff] sc1\n\t"
    "s_mov_b64 exec, -1\n\t"
    "v_lshrrev_b32_e32 v20, 5, v31\n\t"
    "v_mul_u32_u24_e32 v20, 0x30000, v20\n\t"
    "v_and_b32_e32 v21, 1, v29\n\t"
    "v_mul_u32_u24_e32 v21, 0x600, v21\n\t"
    "v_add_u32_e32 v20, v20, v21\n\t"
    "v_and_b32_e32 v21, 15, v31\n\t"
    "v_lshl_add_u32 v7, v21, 4, v20\n\t"
    "v_lshl_add_u32 v8, v21, 3, v20\n\t"
    "v_add_u32_e32 v8, 0x100, v8\n\t"
    "v_and_b32_e32 v20, 48, v30\n\t"
    "v_lshl_or_b32 v20, v29, 6, v20\n\t"
    "v_lshrrev_b32_e32 v21, 2, v21\n\t"
    "v_lshl_or_b32 v9, v21, 8, v20\n\t"
    "v_min_u32_e32 v21, 35, v31\n\t"
    "v_mul_u32_u24_e32 v21, 128, v21\n\t"
    "v_add_u32_e32 v21, 0x28000, v21\n\t"
    "s_mov_b32 s52, s44\n\t"
    "s_and_b32 s53, s45, 0xffff\n\t"
    "s_mov_b32 s54, 0x6000000\n\t"
    "s_mov_b32 s55, 0x20000\n\t"
    "s_cmp_eq_u32 %[pair], 0\n\t"
    "s_cbranch_scc1 .Lng_%=\n\t"
    "s_sleep 48\n\t"
    "s_mov_b32 s64, 0\n\t"
    "s_mov_b32 s65, 4608\n\t"
    "buffer_load_dwordx2 v[12:13], v21, s[56:59], s64 offen sc1\n\t"
    "buffer_load_dwordx2 v[14:15], v21, s[56:59], s65 offen sc1\n\t"
    "s_waitcnt vmcnt(0)\n\t"
    "s_mov_b32 s60, 0x10000\n\t"
    ".Lp5_%=:\n\t"
    "v_cmp_eq_u64_e32 vcc, v[12:13], v[22:23]\n\t"
    "v_cmp_eq_u64_e64 s[70:71], v[14:15], v[22:23]\n\t"
    "s_and_b64 vcc, vcc, s[70:71]\n\t"
    "s_cmp_eq_u64 vcc, exec\n\t"
    "s_cbranch_scc1 .Lk6_%=\n\t"
    "s_sleep 4\n\t"
    "buffer_load_dwordx2 v[12:13], v21, s[56:59], s64 offen sc1\n\t"
    "buffer_load_dwordx2 v[14:15], v21, s[56:59], s65 offen sc1\n\t"
    "s_sub_u32 s60, s60, 1\n\t"
    "s_cmp_lg_u32 s60, 0\n\t"
    "s_waitcnt vmcnt(0)\n\t"
    "s_cbranch_scc1 .Lp5_%=\n\t"
    ".Lk6_%=:\n\t"
    ".Lng_%=:\n\t"
    "s_mul_i32 s66, %[pair], 12288\n\t"
    "s_mul_i32 s62, %[pair], 73728\n\t"
    "s_add_u32 s62, s62, 36864\n\t"
    "v_add_u32_e32 v11, s62, v30\n\t"
    "s_mov_b32 s63, 73728\n\t"
    "s_barrier\n\t"
    "s_add_u32 s67, %[tch], 0\n\t"
    "s_mov_b64 exec, 0x1ff\n\t"
    "buffer_load_dword v10, v26, s[56:59], s67 offen sc1\n\t"
    "s_mov_b64 exec, -1\n\t"
    "buffer_load_dwordx4 v[32:35], v30, s[56:59], s66 offen offset:0 sc1\n\t"
    "buffer_load_dwordx4 v[36:39], v30, s[56:59], s66 offen offset:1024 sc1\n\t"
    "buffer_load_dwordx4 v[40:43], v30, s[56:59], s66 offen offset:2048 sc1\n\t"
    "buffer_load_dwordx4 v[44:47], v30, s[56:59], s66 offen offset:3072 sc1\n\t"
    "s_add_u32 s66, s66, 4096\n\t"
    "buffer_load_dwordx4 v[48:51], v30, s[56:59], s66 offen offset:0 sc1\n\t"
    "buffer_load_dwordx4 v[52:55], v30, s[56:59], s66 offen offset:1024 sc1\n\t"
    "buffer_load_dwordx4 v[56:59], v30, s[56:59], s66 offen offset:2048 sc1\n\t"
    "buffer_load_dwordx4 v[60:63], v30, s[56:59], s66 offen offset:3072 sc1\n\t"
    "s_add_u32 s66, s66, 4096\n\t"
    "buffer_load_dwordx4 v[64:67], v30, s[56:59], s66 offen offset:0 sc1\n\t"
    "buffer_load_dwordx4 v[68:71], v30, s[56:59], s66 offen offset:1024 sc1\n\t"
    "buffer_load_dwordx4 v[72:75], v30, s[56:59], s66 offen offset:2048 sc1\n\t"
    "buffer_load_dwordx4 v[76:79], v30, s[56:59], s66 offen offset:3072 sc1\n\t"
    "s_add_u32 s66, s66, 16384\n\t"
    "buffer_load_dwordx4 v[80:83], v30, s[56:59], s66 offen offset:0 sc1\n\t"
    "buffer_load_dwordx4 v[84:87], v30, s[56:59], s66 offen offset:1024 sc1\n\t"
    "buffer_load_dwordx4 v[88:91], v30, s[56:59], s66 offen offset:2048 sc1\n\t"
    "buffer_load_dwordx4 v[92:95], v30, s[56:59], s66 offen offset:3072 sc1\n\t"
    "s_add_u32 s66, s66, 4096\n\t"
    "buffer_load_dwordx4 v[96:99], v30, s[56:59], s66 offen offset:0 sc1\n\t"
    "buffer_load_dwordx4 v[100:103], v30, s[56:59], s66 offen offset:1024 sc1\n\t"
    "buffer_load_dwordx4 v[104:107], v30, s[56:59], s66 offen offset:2048 sc1\n\t"
    "buffer_load_dwordx4 v[108:111], v30, s[56:59], s66 offen offset:3072 sc1\n\t"
    "s_add_u32 s66, s66, 4096\n\t"
    "buffer_load_dwordx4 v[112:115], v30, s[56:59], s66 offen offset:0 sc1\n\t"
    "buffer_load_dwordx4 v[116:119], v30, s[56:59], s66 offen offset:1024 sc1\n\t"
    "buffer_load_dwordx4 v[120:123], v30, s[56:59], s66 offen offset:2048 sc1\n\t"
    "buffer_load_dwordx4 v[124:127], v30, s[56:59], s66 offen offset:3072 sc1\n\t"
    "s_add_u32 s66, s66, 16384\n\t"
    "s_mov_b32 s64, 9216\n\t"
    "s_mov_b32 s65, 13824\n\t"
    "buffer_load_dwordx2 v[12:13], v21, s[56:59], s64 offen sc1\n\t"
    "buffer_load_dwordx2 v[14:15], v21, s[56:59], s65 offen sc1\n\t"
    "s_waitcnt vmcnt(25)\n\t"
    "ds_write_b128 v11, v[32:35] offset:0\n\t"
    "buffer_load_dwordx4 v[128:131], v30, s[56:59], s66 offen offset:0 sc1\n\t"
    "s_waitcnt vmcnt(25)\n\t"
    "ds_write_b128 v11, v[36:39] offset:1024\n\t"
    "buffer_load_dwordx4 v[132:135], v30, s[56:59], s66 offen offset:1024 sc1\n\t"
    "s_waitcnt vmcnt(25)\n\t"
    "ds_write_b128 v11, v[40:43] offset:2048\n\t"
    "buffer_load_dwordx4 v[136:139], v30, s[56:59], s66 offen offset:2048 sc1\n\t"
    "s_waitcnt vmcnt(25)\n\t"
    "ds_write_b128 v11, v[44:47] offset:3072\n\t"
    "buffer_load_dwordx4 v[140:143], v30, s[56:59], s66 offen offset:3072 sc1\n\t"
    "s_add_u32 s66, s66, 4096\n\t"
    "s_waitcnt vmcnt(25)\n\t"
    "ds_write_b128 v11, v[48:51] offset:4096\n\t"
    "buffer_load_dwordx4 v[144:147], v30, s[56:59], s66 offen offset:0 sc1\n\t"
    "s_waitcnt vmcnt(25)\n\t"
    "ds_write_b128 v11, v[52:55] offset:5120\n\t"
    "buffer_load_dwordx4 v[148:151], v30, s[56:59], s66 offen offset:1024 sc1\n\t"
    "s_waitcnt vmcnt(25)\n\t"
    "ds_write_b128 v11, v[56:59] offset:6144\n\t"
    "buffer_load_dwordx4 v[152:155], v30, s[56:59], s66 offen offset:2048 sc1\n\t"
    "s_waitcnt vmcnt(25)\n\t"
    "ds_write_b128 v11, v[60:63] offset:7168\n\t"
    "buffer_load_dwordx4 v[156:159], v30, s[56:59], s66 offen offset:3072 sc1\n\t"
    "s_add_u32 s66, s66, 4096\n\t"
    "s_waitcnt vmcnt(25)\n\t"
    "ds_write_b128 v11, v[64:67] offset:8192\n\t"
    "buffer_load_dwordx4 v[160:163], v30, s[56:59], s66 offen offset:0 sc1\n\t"
    "s_waitcnt vmcnt(25)\n\t"
    "ds_write_b128 v11, v[68:71] offset:9216\n\t"
    "buffer_load_dwordx4 v[164:167], v30, s[56:59], s66 offen offset:1024 sc1\n\t"
    "s_waitcnt vmcnt(25)\n\t"
    "ds_write_b128 v11, v[72:75] offset:10240\n\t"
    "buffer_load_dwordx4 v[168:171], v30, s[56:59], s66 offen offset:2048 sc1\n\t"
    "s_waitcnt vmcnt(25)\n\t"
    "ds_write_b128 v11, v[76:79] offset:11264\n\t"
    "s_waitcnt lgkmcnt(0)\n\t"
    "s_barrier\n\t"
    "buffer_load_dwordx4 v[172:175], v30, s[56:59], s66 offen offset:3072 sc1\n\t"
    "s_add_u32 s66, s66, 16384\n\t"
    "s_waitcnt vmcnt(25)\n\t"
    "ds_write_b128 v11, v[80:83] offset:12288\n\t"
    "s_waitcnt vmcnt(24)\n\t"
    "ds_write_b128 v11, v[84:87] offset:13312\n\t"
    "s_waitcnt vmcnt(23)\n\t"
    "ds_write_b128 v11, v[88:91] offset:14336\n\t"
    "s_waitcnt vmcnt(22)\n\t"
    "ds_write_b128 v11, v[92:95] offset:15360\n\t"
    "s_waitcnt vmcnt(21)\n\t"
    "ds_write_b128 v11, v[96:99] offset:16384\n\t"
    "s_waitcnt vmcnt(20)\n\t"
    "ds_write_b128 v11, v[100:103] offset:17408\n\t"
    "s_waitcnt vmcnt(19)\n\t"
    "ds_write_b128 v11, v[104:107] offset:18432\n\t"
    "s_waitcnt vmcnt(18)\n\t"
    "ds_write_b128 v11, v[108:111] offset:19456\n\t"
    "s_waitcnt vmcnt(17)\n\t"
    "ds_write_b128 v11, v[112:115] offset:20480\n\t"
    "s_waitcnt vmcnt(16)\n\t"
    "ds_write_b128 v11, v[116:119] offset:21504\n\t"
    "s_waitcnt vmcnt(15)\n\t"
    "ds_write_b128 v11, v[120:123] offset:22528\n\t"
    "s_waitcnt vmcnt(14)\n\t"
    "ds_write_b128 v11, v[124:127] offset:23552\n\t"
    "s_waitcnt lgkmcnt(0)\n\t"
    "s_barrier\n\t"
    "s_waitcnt vmcnt(12)\n\t"
    "s_mov_b32 s60, 0x10000\n\t"
    ".Lp7_%=:\n\t"
    "v_cmp_eq_u64_e32 vcc, v[12:13], v[22:23]\n\t"
    "v_cmp_eq_u64_e64 s[70:71], v[14:15], v[22:23]\n\t"
    "s_and_b64 vcc, vcc, s[70:71]\n\t"
    "s_cmp_eq_u64 vcc, exec\n\t"
    "s_cbranch_scc1 .Lk8_%=\n\t"
    "s_sleep 4\n\t"
    "buffer_load_dwordx2 v[12:13], v21, s[56:59], s64 offen sc1\n\t"
    "buffer_load_dwordx2 v[14:15], v21, s[56:59], s65 offen sc1\n\t"
    "s_sub_u32 s60, s60, 1\n\t"
    "s_cmp_lg_u32 s60, 0\n\t"
    "s_waitcnt vmcnt(0)\n\t"
    "s_cbranch_scc1 .Lp7_%=\n\t"
    ".Lk8_%=:\n\t"
    "s_add_u32 s67, %[tch], 73728\n\t"
    "s_mov_b64 exec, 0x1ff\n\t"
    "buffer_load_dword v10, v26, s[56:59], s67 offen sc1\n\t"
    "s_mov_b64 exec, -1\n\t"
    "buffer_load_dwordx4 a[0:3], v9, s[56:59], s63 offen offset:0 sc1\n\t"
    "buffer_load_dwordx4 a[4:7], v9, s[56:59], s63 offen offset:1024 sc1\n\t"
    "buffer_load_dwordx4 a[8:11], v9, s[56:59], s63 offen offset:2048 sc1\n\t"
    "buffer_load_dwordx4 a[12:15], v9, s[56:59], s63 offen offset:3072 sc1\n\t"
    "s_add_u32 s63, s63, 4096\n\t"
    "s_waitcnt vmcnt(16)\n\t"
    "ds_write_b128 v11, v[128:131] offset:24576\n\t"
    "buffer_load_dwordx4 a[16:19], v9, s[56:59], s63 offen offset:0 sc1\n\t"
    "s_waitcnt vmcnt(16)\n\t"
    "ds_write_b128 v11, v[132:135] offset:25600\n\t"
    "buffer_load_dwordx4 a[20:23], v9, s[56:59], s63 offen offset:1024 sc1\n\t"
    "s_waitcnt vmcnt(16)\n\t"
    "ds_write_b128 v11, v[136:139] offset:26624\n\t"
    "buffer_load_dwordx4 a[24:27], v9, s[56:59], s63 offen offset:2048 sc1\n\t"
    "s_waitcnt vmcnt(16)\n\t"
    "ds_write_b128 v11, v[140:143] offset:27648\n\t"
    "buffer_load_dwordx4 a[28:31], v9, s[56:59], s63 offen offset:3072 sc1\n\t"
    "s_add_u32 s63, s63, 4096\n\t"
    "s_waitcnt vmcnt(16)\n\t"
    "ds_write_b128 v11, v[144:147] offset:28672\n\t"
    "buffer_load_dwordx4 a[32:35], v9, s[56:59], s63 offen offset:0 sc1\n\t"
    "s_waitcnt vmcnt(16)\n\t"
    "ds_write_b128 v11, v[148:151] offset:29696\n\t"
    "buffer_load_dwordx4 a[36:39], v9, s[56:59], s63 offen offset:1024 sc1\n\t"
    "s_waitcnt vmcnt(16)\n\t"
    "ds_write_b128 v11, v[152:155] offset:30720\n\t"
    "buffer_load_dwordx4 a[40:43], v9, s[56:59], s63 offen offset:2048 sc1\n\t"
    "s_waitcnt vmcnt(16)\n\t"
    "ds_write_b128 v11, v[156:159] offset:31744\n\t"
    "buffer_load_dwordx4 a[44:47], v9, s[56:59], s63 offen offset:3072 sc1\n\t"
    "s_add_u32 s63, s63, 4096\n\t"
    "s_waitcnt vmcnt(16)\n\t"
    "ds_write_b128 v11, v[160:163] offset:32768\n\t"
    "buffer_load_dwordx4 a[48:51], v9, s[56:59], s63 offen offset:0 sc1\n\t"
    "s_waitcnt vmcnt(16)\n\t"
    "ds_write_b128 v11, v[164:167] offset:33792\n\t"
    "buffer_load_dwordx4 a[52:55], v9, s[56:59], s63 offen offset:1024 sc1\n\t"
    "s_waitcnt vmcnt(16)\n\t"
    "ds_write_b128 v11, v[168:171] offset:34816\n\t"
    "buffer_load_dwordx4 a[56:59], v9, s[56:59], s63 offen offset:2048 sc1\n\t"
    "s_waitcnt vmcnt(16)\n\t"
    "ds_write_b128 v11, v[172:175] offset:35840\n\t"
    "s_waitcnt lgkmcnt(0)\n\t"
    "s_barrier\n\t"
    "buffer_load_dwordx4 a[60:63], v9, s[56:59], s63 offen offset:3072 sc1\n\t"
    "s_add_u32 s63, s63, 4096\n\t"
    "buffer_load_dwordx4 a[64:67], v9, s[56:59], s63 offen offset:0 sc1\n\t"
    "buffer_load_dwordx4 a[68:71], v9, s[56:59], s63 offen offset:1024 sc1\n\t"
    "buffer_load_dwordx4 a[72:75], v9, s[56:59], s63 offen offset:2048 sc1\n\t"
    "buffer_load_dwordx4 a[76:79], v9, s[56:59], s63 offen offset:3072 sc1\n\t"
    "s_add_u32 s63, s63, 4096\n\t"
    "buffer_load_dwordx4 a[80:83], v9, s[56:59], s63 offen offset:0 sc1\n\t"
    "buffer_load_dwordx4 a[84:87], v9, s[56:59], s63 offen offset:1024 sc1\n\t"
    "buffer_load_dwordx4 a[88:91], v9, s[56:59], s63 offen offset:2048 sc1\n\t"
    "buffer_load_dwordx4 a[92:95], v9, s[56:59], s63 offen offset:3072 sc1\n\t"
    "s_add_u32 s63, s63, 4096\n\t"
    "buffer_load_dwordx4 a[96:99], v9, s[56:59], s63 offen offset:0 sc1\n\t"
    "buffer_load_dwordx4 a[100:103], v9, s[56:59], s63 offen offset:1024 sc1\n\t"
    "buffer_load_dwordx4 a[104:107], v9, s[56:59], s63 offen offset:2048 sc1\n\t"
    "buffer_load_dwordx4 a[108:111], v9, s[56:59], s63 offen offset:3072 sc1\n\t"
    "s_add_u32 s63, s63, 4096\n\t"
    "buffer_load_dwordx4 a[112:115], v9, s[56:59], s63 offen offset:0 sc1\n\t"
    "buffer_load_dwordx4 a[116:119], v9, s[56:59], s63 offen offset:1024 sc1\n\t"
    "buffer_load_dwordx4 a[120:123], v9, s[56:59], s63 offen offset:2048 sc1\n\t"
    "buffer_load_dwordx4 a[124:127], v9, s[56:59], s63 offen offset:3072 sc1\n\t"
    "s_add_u32 s63, s63, 4096\n\t"
    "buffer_load_dwordx4 a[128:131], v9, s[56:59], s63 offen offset:0 sc1\n\t"
    "buffer_load_dwordx4 a[132:135], v9, s[56:59], s63 offen offset:1024 sc1\n\t"
    "buffer_load_dwordx4 a[136:139], v9, s[56:59], s63 offen offset:2048 sc1\n\t"
    "buffer_load_dwordx4 a[140:143], v9, s[56:59], s63 offen offset:3072 sc1\n\t"
    "s_add_u32 s63, s63, 4096\n\t"
    "buffer_load_dwordx4 a[144:147], v9, s[56:59], s63 offen offset:0 sc1\n\t"
    "buffer_load_dwordx4 a[148:151], v9, s[56:59], s63 offen offset:1024 sc1\n\t"
    "buffer_load_dwordx4 a[152:155], v9, s[56:59], s63 offen offset:2048 sc1\n\t"
    "buffer_load_dwordx4 a[156:159], v9, s[56:59], s63 offen offset:3072 sc1\n\t"
    "s_add_u32 s63, s63, 4096\n\t"
    "buffer_load_dwordx4 a[160:163], v9, s[56:59], s63 offen offset:0 sc1\n\t"
    "buffer_load_dwordx4 a[164:167], v9, s[56:59], s63 offen offset:1024 sc1\n\t"
    "buffer_load_dwordx4 a[168:171], v9, s[56:59], s63 offen offset:2048 sc1\n\t"
    "buffer_load_dwordx4 a[172:175], v9, s[56:59], s63 offen offset:3072 sc1\n\t"
    "s_add_u32 s63, s63, 4096\n\t"
    "buffer_load_dwordx4 a[176:179], v9, s[56:59], s63 offen offset:0 sc1\n\t"
    "buffer_load_dwordx4 a[180:183], v9, s[56:59], s63 offen offset:1024 sc1\n\t"
    "buffer_load_dwordx4 a[184:187], v9, s[56:59], s63 offen offset:2048 sc1\n\t"
    "buffer_load_dwordx4 a[188:191], v9, s[56:59], s63 offen offset:3072 sc1\n\t"
    "s_add_u32 s63, s63, 4096\n\t"
    "buffer_load_dwordx4 a[192:195], v9, s[56:59], s63 offen offset:0 sc1\n\t"
    "buffer_load_dwordx4 a[196:199], v9, s[56:59], s63 offen offset:1024 sc1\n\t"
    "buffer_load_dwordx4 a[200:203], v9, s[56:59], s63 offen offset:2048 sc1\n\t"
    "buffer_load_dwordx4 a[204:207], v9, s[56:59], s63 offen offset:3072 sc1\n\t"
    "s_add_u32 s63, s63, 4096\n\t"
    "buffer_load_dwordx4 a[208:211], v9, s[56:59], s63 offen offset:0 sc1\n\t"
    "buffer_load_dwordx4 a[212:215], v9, s[56:59], s63 offen offset:1024 sc1\n\t"
    "buffer_load_dwordx4 a[216:219], v9, s[56:59], s63 offen offset:2048 sc1\n\t"
    "buffer_load_dwordx4 a[220:223], v9, s[56:59], s63 offen offset:3072 sc1\n\t"
    "s_add_u32 s63, s63, 4096\n\t"
    "buffer_load_dwordx4 a[224:227], v9, s[56:59], s63 offen offset:0 sc1\n\t"
    "buffer_load_dwordx4 a[228:231], v9, s[56:59], s63 offen offset:1024 sc1\n\t"
    "buffer_load_dwordx4 a[232:235], v9, s[56:59], s63 offen offset:2048 sc1\n\t"
    "buffer_load_dwordx4 a[236:239], v9, s[56:59], s63 offen offset:3072 sc1\n\t"
    "s_add_u32 s63, s63, 4096\n\t"
    "buffer_load_dwordx4 a[240:243], v9, s[56:59], s63 offen offset:0 sc1\n\t"
    "buffer_load_dwordx4 a[244:247], v9, s[56:59], s63 offen offset:1024 sc1\n\t"
    "buffer_load_dwordx4 a[248:251], v9, s[56:59], s63 offen offset:2048 sc1\n\t"
    "buffer_load_dwordx4 a[252:255], v9, s[56:59], s63 offen offset:3072 sc1\n\t"
    "s_add_u32 s63, s63, 4096\n\t"
    "buffer_load_dwordx4 v[224:227], v9, s[56:59], s63 offen offset:0 sc1\n\t"
    "buffer_load_dwordx4 v[228:231], v9, s[56:59], s63 offen offset:1024 sc1\n\t"
    "buffer_load_dwordx4 v[232:235], v9, s[56:59], s63 offen offset:2048 sc1\n\t"
    "buffer_load_dwordx4 v[236:239], v9, s[56:59], s63 offen offset:3072 sc1\n\t"
    "s_add_u32 s63, s63, 4096\n\t"
    "buffer_load_dwordx4 v[240:243], v9, s[56:59], s63 offen offset:0 sc1\n\t"
    "buffer_load_dwordx4 v[244:247], v9, s[56:59], s63 offen offset:1024 sc1\n\t"
    "buffer_load_dwordx4 v[248:251], v9, s[56:59], s63 offen offset:2048 sc1\n\t"
    "buffer_load_dwordx4 v[252:255], v9, s[56:59], s63 offen offset:3072 sc1\n\t"
    "s_add_u32 s63, s63, 4096\n\t"
    "s_barrier\n\t"
    "s_mov_b64 exec, %[amask]\n\t"
    "buffer_store_dwordx2 v[22:23], off, s[56:59], %[aown] sc1\n\t"
    "s_mov_b64 exec, -1\n\t"
    "ds_read_b128 v[60:63], v0 offset:0\n\t"
    "ds_read_b128 v[64:67], v1 offset:0\n\t"
    "ds_read_b128 v[68:71], v2 offset:0\n\t"
    "ds_read_b128 v[72:75], v3 offset:0\n\t"
    ".Lh1_%=:\n\t"
    "s_cbranch_execz .Lh2_%=\n\t"
    "ds_read_b128 v[76:79], v4 offset:0\n\t"
    "ds_read_b128 v[80:83], v5 offset:0\n\t"
    "ds_read_b128 v[84:87], v0 offset:6144\n\t"
    "ds_read_b128 v[88:91], v1 offset:6144\n\t"
    "ds_read_b128 v[92:95], v2 offset:6144\n\t"
    "ds_read_b128 v[108:111], v3 offset:6144\n\t"
    "ds_read_b128 v[96:99], v4 offset:6144\n\t"
    "ds_read_b128 v[136:139], v5 offset:6144\n\t"
    "s_add_u32 s61, %[wb], 0xff400000\n\t"
    "s_mov_b32 s62, %[rbeg]\n\t"
    "s_mov_b64 vcc, -1\n\t"
    "s_waitcnt vmcnt(63)\n\t"
    "s_mov_b32 s60, 8\n\t"
    ".Lf9_%=:\n\t"
    "s_waitcnt lgkmcnt(7)\n\t"
    "v_mfma_f32_16x16x32_bf16 v[24:27], v[60:63], a[0:3], 0\n\t"
    "buffer_store_dwordx2 v[144:145], v8, s[52:55], s61 offen offset:768 sc1\n\t"
    "s_waitcnt lgkmcnt(10)\n\t"
    "v_mfma_f32_16x16x32_bf16 v[24:27], v[64:67], a[4:7], v[24:27]\n\t"
    "v_cvt_pk_bf16_f32 v187, v31, v35\n\t"
    "v_cvt_pk_bf16_f32 v172, v36, v12\n\t"
    "s_waitcnt lgkmcnt(9)\n\t"
    "v_mfma_f32_16x16x32_bf16 v[24:27], v[68:71], a[8:11], v[24:27]\n\t"
    "v_cvt_pk_bf16_f32 v174, v37, v13\n\t"
    "v_cvt_pk_bf16_f32 v188, v38, v14\n\t"
    "s_waitcnt lgkmcnt(8)\n\t"
    "v_mfma_f32_16x16x32_bf16 v[24:27], v[72:75], a[12:15], v[24:27]\n\t"
    "v_cvt_pk_bf16_f32 v190, v39, v15\n\t"
    "v_cvt_pk_bf16_f32 v173, v16, v20\n\t"
    "s_waitcnt lgkmcnt(7)\n\t"
    "v_mfma_f32_16x16x32_bf16 v[24:27], v[76:79], a[16:19], v[24:27]\n\t"
    "v_cvt_pk_bf16_f32 v175, v17, v21\n\t"
    "s_waitcnt lgkmcnt(2)\n\t"
    "v_mfma_f32_16x16x32_bf16 v[24:27], v[80:83], a[20:23], v[24:27]\n\t"
    "v_cvt_pk_bf16_f32 v189, v18, v22\n\t"
    "v_cvt_pk_bf16_f32 v191, v19, v23\n\t"
    "s_waitcnt lgkmcnt(5)\n\t"
    "v_mfma_f32_16x16x32_bf16 v[28:31], v[84:87], a[0:3], 0\n\t"
    "s_waitcnt lgkmcnt(0)\n\t"
    "v_mfma_f32_16x16x32_bf16 v[28:31], v[88:91], a[4:7], v[28:31]\n\t"
    "v_mfma_f32_16x16x32_bf16 v[28:31], v[92:95], a[8:11], v[28:31]\n\t"
    "v_mfma_f32_16x16x32_bf16 v[28:31], v[108:111], a[12:15], v[28:31]\n\t"
    "v_cvt_pk_bf16_f32 v12, v24, v25\n\t"
    "s_waitcnt lgkmcnt(0)\n\t"
    "v_mfma_f32_16x16x32_bf16 v[28:31], v[96:99], a[16:19], v[28:31]\n\t"
    "v_cvt_pk_bf16_f32 v13, v26, v27\n\t"
    "v_mfma_f32_16x16x32_bf16 v[28:31], v[136:139], a[20:23], v[28:31]\n\t"
    "s_waitcnt vmcnt(62)\n\t"
    "v_mfma_f32_16x16x32_bf16 v[20:23], v[60:63], a[24:27], 0\n\t"
    "buffer_store_dwordx2 v[146:147], v8, s[52:55], s61 offen offset:1152 sc1\n\t"
    "v_mfma_f32_16x16x32_bf16 v[20:23], v[64:67], a[28:31], v[20:23]\n\t"
    "s_add_u32 s61, s61, 393216\n\t"
    "v_mfma_f32_16x16x32_bf16 v[20:23], v[68:71], a[32:35], v[20:23]\n\t"
    "v_mfma_f32_16x16x32_bf16 v[20:23], v[72:75], a[36:39], v[20:23]\n\t"
    "v_cvt_pk_bf16_f32 v14, v28, v29\n\t"
    "v_cvt_pk_bf16_f32 v15, v30, v31\n\t"
    "v_mfma_f32_16x16x32_bf16 v[20:23], v[76:79], a[40:43], v[20:23]\n\t"
    "v_mfma_f32_16x16x32_bf16 v[20:23], v[80:83], a[44:47], v[20:23]\n\t"
    "v_mfma_f32_16x16x32_bf16 v[24:27], v[84:87], a[24:27], 0\n\t"
    "v_mfma_f32_16x16x32_bf16 v[24:27], v[88:91], a[28:31], v[24:27]\n\t"
    "v_mfma_f32_16x16x32_bf16 v[24:27], v[92:95], a[32:35], v[24:27]\n\t"
    "v_mfma_f32_16x16x32_bf16 v[24:27], v[108:111], a[36:39], v[24:27]\n\t"
    "v_cvt_pk_bf16_f32 v16, v20, v21\n\t"
    "v_cvt_pk_bf16_f32 v17, v22, v23\n\t"
    "v_mfma_f32_16x16x32_bf16 v[24:27], v[96:99], a[40:43], v[24:27]\n\t"
    "v_mfma_f32_16x16x32_bf16 v[24:27], v[136:139], a[44:47], v[24:27]\n\t"
    "s_waitcnt vmcnt(57)\n\t"
    "v_mfma_f32_16x16x32_bf16 v[28:31], v[60:63], a[48:51], 0\n\t"
    "buffer_store_dwordx4 v[132:135], v7, s[52:55], s61 offen offset:0 sc1\n\t"
    "v_mfma_f32_16x16x32_bf16 v[28:31], v[64:67], a[52:55], v[28:31]\n\t"
    "v_mfma_f32_16x16x32_bf16 v[28:31], v[68:71], a[56:59], v[28:31]\n\t"
    "v_mfma_f32_16x16x32_bf16 v[28:31], v[72:75], a[60:63], v[28:31]\n\t"
    "v_cvt_pk_bf16_f32 v18, v24, v25\n\t"
    "v_cvt_pk_bf16_f32 v19, v26, v27\n\t"
    "v_mfma_f32_16x16x32_bf16 v[28:31], v[76:79], a[64:67], v[28:31]\n\t"
    "v_mfma_f32_16x16x32_bf16 v[28:31], v[80:83], a[68:71], v[28:31]\n\t"
    "v_mfma_f32_16x16x32_bf16 v[24:27], v[84:87], a[48:51], 0\n\t"
    "v_mfma_f32_16x16x32_bf16 v[24:27], v[88:91], a[52:55], v[24:27]\n\t"
    "v_mfma_f32_16x16x32_bf16 v[24:27], v[92:95], a[56:59], v[24:27]\n\t"
    "v_mfma_f32_16x16x32_bf16 v[24:27], v[108:111], a[60:63], v[24:27]\n\t"
    "v_cvt_pk_bf16_f32 v20, v28, v29\n\t"
    "v_cvt_pk_bf16_f32 v21, v30, v31\n\t"
    "v_mfma_f32_16x16x32_bf16 v[24:27], v[96:99], a[64:67], v[24:27]\n\t"
    "v_mfma_f32_16x16x32_bf16 v[24:27], v[136:139], a[68:71], v[24:27]\n\t"
    "s_waitcnt vmcnt(52)\n\t"
    "v_mfma_f32_16x16x32_bf16 v[28:31], v[60:63], a[72:75], 0\n\t"
    "buffer_store_dwordx4 v[128:131], v7, s[52:55], s61 offen offset:384 sc1\n\t"
    "v_mfma_f32_16x16x32_bf16 v[28:31], v[64:67], a[76:79], v[28:31]\n\t"
    "v_mfma_f32_16x16x32_bf16 v[28:31], v[68:71], a[80:83], v[28:31]\n\t"
    "v_mfma_f32_16x16x32_bf16 v[28:31], v[72:75], a[84:87], v[28:31]\n\t"
    "v_cvt_pk_bf16_f32 v22, v24, v25\n\t"
    "v_cvt_pk_bf16_f32 v23, v26, v27\n\t"
    "v_mfma_f32_16x16x32_bf16 v[28:31], v[76:79], a[88:91], v[28:31]\n\t"
    "v_mfma_f32_16x16x32_bf16 v[28:31], v[80:83], a[92:95], v[28:31]\n\t"
    "v_mfma_f32_16x16x32_bf16 v[32:35], v[84:87], a[72:75], 0\n\t"
    "v_mfma_f32_16x16x32_bf16 v[32:35], v[88:91], a[76:79], v[32:35]\n\t"
    "v_mfma_f32_16x16x32_bf16 v[32:35], v[92:95], a[80:83], v[32:35]\n\t"
    "v_mfma_f32_16x16x32_bf16 v[32:35], v[108:111], a[84:87], v[32:35]\n\t"
    "v_cvt_pk_bf16_f32 v24, v28, v29\n\t"
    "v_cvt_pk_bf16_f32 v25, v30, v31\n\t"
    "v_mfma_f32_16x16x32_bf16 v[32:35], v[96:99], a[88:91], v[32:35]\n\t"
    "v_mfma_f32_16x16x32_bf16 v[32:35], v[136:139], a[92:95], v[32:35]\n\t"
    "s_waitcnt vmcnt(47)\n\t"
    "v_mfma_f32_16x16x32_bf16 v[36:39], v[60:63], a[96:99], 0\n\t"
    "buffer_store_dwordx4 v[164:167], v7, s[52:55], s61 offen offset:768 sc1\n\t"
    "v_mfma_f32_16x16x32_bf16 v[36:39], v[64:67], a[100:103], v[36:39]\n\t"
    "v_mfma_f32_16x16x32_bf16 v[36:39], v[68:71], a[104:107], v[36:39]\n\t"
    "v_mfma_f32_16x16x32_bf16 v[36:39], v[72:75], a[108:111], v[36:39]\n\t"
    "v_cvt_pk_bf16_f32 v26, v32, v33\n\t"
    "v_cvt_pk_bf16_f32 v27, v34, v35\n\t"
    "v_mfma_f32_16x16x32_bf16 v[36:39], v[76:79], a[112:115], v[36:39]\n\t"
    "v_mfma_f32_16x16x32_bf16 v[36:39], v[80:83], a[116:119], v[36:39]\n\t"
    "v_mfma_f32_16x16x32_bf16 v[32:35], v[84:87], a[96:99], 0\n\t"
    "v_mfma_f32_16x16x32_bf16 v[32:35], v[88:91], a[100:103], v[32:35]\n\t"
    "v_mfma_f32_16x16x32_bf16 v[32:35], v[92:95], a[104:107], v[32:35]\n\t"
    "v_mfma_f32_16x16x32_bf16 v[32:35], v[108:111], a[108:111], v[32:35]\n\t"
    "v_cvt_pk_bf16_f32 v28, v36, v37\n\t"
    "v_cvt_pk_bf16_f32 v29, v38, v39\n\t"
    "v_mfma_f32_16x16x32_bf16 v[32:35], v[96:99], a[112:115], v[32:35]\n\t"
    "v_mfma_f32_16x16x32_bf16 v[32:35], v[136:139], a[116:119], v[32:35]\n\t"
    "s_waitcnt vmcnt(42)\n\t"
    "v_mfma_f32_16x16x32_bf16 v[36:39], v[60:63], a[120:123], 0\n\t"
    "buffer_store_dwordx4 v[160:163], v7, s[52:55], s61 offen offset:1152 sc1\n\t"
    "v_mfma_f32_16x16x32_bf16 v[36:39], v[64:67], a[124:127], v[36:39]\n\t"
    "v_mfma_f32_16x16x32_bf16 v[36:39], v[68:71], a[128:131], v[36:39]\n\t"
    "v_mfma_f32_16x16x32_bf16 v[36:39], v[72:75], a[132:135], v[36:39]\n\t"
    "v_cvt_pk_bf16_f32 v30, v32, v33\n\t"
    "v_cvt_pk_bf16_f32 v31, v34, v35\n\t"
    "v_mfma_f32_16x16x32_bf16 v[36:39], v[76:79], a[136:139], v[36:39]\n\t"
    "v_mfma_f32_16x16x32_bf16 v[36:39], v[80:83], a[140:143], v[36:39]\n\t"
    "v_mfma_f32_16x16x32_bf16 v[40:43], v[84:87], a[120:123], 0\n\t"
    "v_mfma_f32_16x16x32_bf16 v[40:43], v[88:91], a[124:127], v[40:43]\n\t"
    "v_mfma_f32_16x16x32_bf16 v[40:43], v[92:95], a[128:131], v[40:43]\n\t"
    "v_mfma_f32_16x16x32_bf16 v[40:43], v[108:111], a[132:135], v[40:43]\n\t"
    "v_cvt_pk_bf16_f32 v32, v36, v37\n\t"
    "v_cvt_pk_bf16_f32 v33, v38, v39\n\t"
    "v_mfma_f32_16x16x32_bf16 v[40:43], v[96:99], a[136:139], v[40:43]\n\t"
    "v_mfma_f32_16x16x32_bf16 v[40:43], v[136:139], a[140:143], v[40:43]\n\t"
    "s_waitcnt vmcnt(37)\n\t"
    "v_mfma_f32_16x16x32_bf16 v[44:47], v[60:63], a[144:147], 0\n\t"
    "buffer_store_dwordx2 v[140:141], v8, s[52:55], s61 offen offset:0 sc1\n\t"
    "v_mfma_f32_16x16x32_bf16 v[44:47], v[64:67], a[148:151], v[44:47]\n\t"
    "v_mfma_f32_16x16x32_bf16 v[44:47], v[68:71], a[152:155], v[44:47]\n\t"
    "v_mfma_f32_16x16x32_bf16 v[44:47], v[72:75], a[156:159], v[44:47]\n\t"
    "v_cvt_pk_bf16_f32 v34, v40, v41\n\t"
    "v_cvt_pk_bf16_f32 v35, v42, v43\n\t"
    "v_mfma_f32_16x16x32_bf16 v[44:47], v[76:79], a[160:163], v[44:47]\n\t"
    "v_mfma_f32_16x16x32_bf16 v[44:47], v[80:83], a[164:167], v[44:47]\n\t"
    "v_mfma_f32_16x16x32_bf16 v[40:43], v[84:87], a[144:147], 0\n\t"
    "v_mfma_f32_16x16x32_bf16 v[40:43], v[88:91], a[148:151], v[40:43]\n\t"
    "v_mfma_f32_16x16x32_bf16 v[40:43], v[92:95], a[152:155], v[40:43]\n\t"
    "v_mfma_f32_16x16x32_bf16 v[40:43], v[108:111], a[156:159], v[40:43]\n\t"
    "v_cvt_pk_bf16_f32 v36, v44, v45\n\t"
    "v_cvt_pk_bf16_f32 v37, v46, v47\n\t"
    "v_mfma_f32_16x16x32_bf16 v[40:43], v[96:99], a[160:163], v[40:43]\n\t"
    "v_mfma_f32_16x16x32_bf16 v[40:43], v[136:139], a[164:167], v[40:43]\n\t"
    "s_waitcnt vmcnt(32)\n\t"
    "v_mfma_f32_16x16x32_bf16 v[44:47], v[60:63], a[168:171], 0\n\t"
    "buffer_store_dwordx2 v[142:143], v8, s[52:55], s61 offen offset:384 sc1\n\t"
    "v_mfma_f32_16x16x32_bf16 v[44:47], v[64:67], a[172:175], v[44:47]\n\t"
    "v_mfma_f32_16x16x32_bf16 v[44:47], v[68:71], a[176:179], v[44:47]\n\t"
    "v_mfma_f32_16x16x32_bf16 v[44:47], v[72:75], a[180:183], v[44:47]\n\t"
    "v_cvt_pk_bf16_f32 v38, v40, v41\n\t"
    "v_cvt_pk_bf16_f32 v39, v42, v43\n\t"
    "v_mfma_f32_16x16x32_bf16 v[44:47], v[76:79], a[184:187], v[44:47]\n\t"
    "v_mfma_f32_16x16x32_bf16 v[44:47], v[80:83], a[188:191], v[44:47]\n\t"
    "v_mfma_f32_16x16x32_bf16 v[48:51], v[84:87], a[168:171], 0\n\t"
    "v_mfma_f32_16x16x32_bf16 v[48:51], v[88:91], a[172:175], v[48:51]\n\t"
    "v_mfma_f32_16x16x32_bf16 v[48:51], v[92:95], a[176:179], v[48:51]\n\t"
    "v_mfma_f32_16x16x32_bf16 v[48:51], v[108:111], a[180:183], v[48:51]\n\t"
    "v_cvt_pk_bf16_f32 v40, v44, v45\n\t"
    "v_cvt_pk_bf16_f32 v41, v46, v47\n\t"
    "v_mfma_f32_16x16x32_bf16 v[48:51], v[96:99], a[184:187], v[48:51]\n\t"
    "v_mfma_f32_16x16x32_bf16 v[48:51], v[136:139], a[188:191], v[48:51]\n\t"
    "s_waitcnt vmcnt(27)\n\t"
    "v_mfma_f32_16x16x32_bf16 v[52:55], v[60:63], a[192:195], 0\n\t"
    "buffer_store_dwordx2 v[120:121], v8, s[52:55], s61 offen offset:768 sc1\n\t"
    "v_mfma_f32_16x16x32_bf16 v[52:55], v[64:67], a[196:199], v[52:55]\n\t"
    "v_mfma_f32_16x16x32_bf16 v[52:55], v[68:71], a[200:203], v[52:55]\n\t"
    "v_mfma_f32_16x16x32_bf16 v[52:55], v[72:75], a[204:207], v[52:55]\n\t"
    "v_cvt_pk_bf16_f32 v42, v48, v49\n\t"
    "v_cvt_pk_bf16_f32 v43, v50, v51\n\t"
    "v_mfma_f32_16x16x32_bf16 v[52:55], v[76:79], a[208:211], v[52:55]\n\t"
    "v_mfma_f32_16x16x32_bf16 v[52:55], v[80:83], a[212:215], v[52:55]\n\t"
    "v_mfma_f32_16x16x32_bf16 v[48:51], v[84:87], a[192:195], 0\n\t"
    "v_mfma_f32_16x16x32_bf16 v[48:51], v[88:91], a[196:199], v[48:51]\n\t"
    "v_mfma_f32_16x16x32_bf16 v[48:51], v[92:95], a[200:203], v[48:51]\n\t"
    "v_mfma_f32_16x16x32_bf16 v[48:51], v[108:111], a[204:207], v[48:51]\n\t"
    "v_cvt_pk_bf16_f32 v44, v52, v53\n\t"
    "v_cvt_pk_bf16_f32 v45, v54, v55\n\t"
    "v_mfma_f32_16x16x32_bf16 v[48:51], v[96:99], a[208:211], v[48:51]\n\t"
    "v_mfma_f32_16x16x32_bf16 v[48:51], v[136:139], a[212:215], v[48:51]\n\t"
    "s_waitcnt vmcnt(22)\n\t"
    "v_mfma_f32_16x16x32_bf16 v[52:55], v[60:63], a[216:219], 0\n\t"
    "buffer_store_dwordx2 v[122:123], v8, s[52:55], s61 offen offset:1152 sc1\n\t"
    "v_mfma_f32_16x16x32_bf16 v[52:55], v[64:67], a[220:223], v[52:55]\n\t"
    "s_add_u32 s61, s61, 393216\n\t"
    "v_mfma_f32_16x16x32_bf16 v[52:55], v[68:71], a[224:227], v[52:55]\n\t"
    "v_mfma_f32_16x16x32_bf16 v[52:55], v[72:75], a[228:231], v[52:55]\n\t"
    "v_cvt_pk_bf16_f32 v46, v48, v49\n\t"
    "v_cvt_pk_bf16_f32 v47, v50, v51\n\t"
    "v_mfma_f32_16x16x32_bf16 v[52:55], v[76:79], a[232:235], v[52:55]\n\t"
    "v_mfma_f32_16x16x32_bf16 v[52:55], v[80:83], a[236:239], v[52:55]\n\t"
    "v_mfma_f32_16x16x32_bf16 v[56:59], v[84:87], a[216:219], 0\n\t"
    "v_mfma_f32_16x16x32_bf16 v[56:59], v[88:91], a[220:223], v[56:59]\n\t"
    "v_mfma_f32_16x16x32_bf16 v[56:59], v[92:95], a[224:227], v[56:59]\n\t"
    "v_mfma_f32_16x16x32_bf16 v[56:59], v[108:111], a[228:231], v[56:59]\n\t"
    "v_cvt_pk_bf16_f32 v48, v52, v53\n\t"
    "v_cvt_pk_bf16_f32 v49, v54, v55\n\t"
    "v_mfma_f32_16x16x32_bf16 v[56:59], v[96:99], a[232:235], v[56:59]\n\t"
    "v_mfma_f32_16x16x32_bf16 v[56:59], v[136:139], a[236:239], v[56:59]\n\t"
    "s_waitcnt vmcnt(17)\n\t"
    "v_mfma_f32_16x16x32_bf16 v[100:103], v[60:63], a[240:243], 0\n\t"
    "buffer_store_dwordx4 v[168:171], v7, s[52:55], s61 offen offset:0 sc1\n\t"
    "v_mfma_f32_16x16x32_bf16 v[100:103], v[64:67], a[244:247], v[100:103]\n\t"
    "v_mfma_f32_16x16x32_bf16 v[100:103], v[68:71], a[248:251], v[100:103]\n\t"
    "v_mfma_f32_16x16x32_bf16 v[100:103], v[72:75], a[252:255], v[100:103]\n\t"
    "v_cvt_pk_bf16_f32 v50, v56, v57\n\t"
    "v_cvt_pk_bf16_f32 v51, v58, v59\n\t"
    "v_mfma_f32_16x16x32_bf16 v[100:103], v[76:79], v[224:227], v[100:103]\n\t"
    "v_mfma_f32_16x16x32_bf16 v[100:103], v[80:83], v[228:231], v[100:103]\n\t"
    "v_mfma_f32_16x16x32_bf16 v[56:59], v[84:87], a[240:243], 0\n\t"
    "v_mfma_f32_16x16x32_bf16 v[56:59], v[88:91], a[244:247], v[56:59]\n\t"
    "v_mfma_f32_16x16x32_bf16 v[56:59], v[92:95], a[248:251], v[56:59]\n\t"
    "v_mfma_f32_16x16x32_bf16 v[56:59], v[108:111], a[252:255], v[56:59]\n\t"
    "v_cvt_pk_bf16_f32 v52, v100, v101\n\t"
    "v_cvt_pk_bf16_f32 v53, v102, v103\n\t"
    "v_mfma_f32_16x16x32_bf16 v[56:59], v[96:99], v[224:227], v[56:59]\n\t"
    "v_mfma_f32_16x16x32_bf16 v[56:59], v[136:139], v[228:231], v[56:59]\n\t"
    "s_waitcnt vmcnt(12)\n\t"
    "v_mfma_f32_16x16x32_bf16 v[100:103], v[60:63], v[232:235], 0\n\t"
    "ds_read_b128 v[104:107], v0 offset:12288\n\t"
    "buffer_store_dwordx4 v[156:159], v7, s[52:55], s61 offen offset:384 sc1\n\t"
    "v_mfma_f32_16x16x32_bf16 v[100:103], v[64:67], v[236:239], v[100:103]\n\t"
    "ds_read_b128 v[112:115], v1 offset:12288\n\t"
    "v_mfma_f32_16x16x32_bf16 v[100:103], v[68:71], v[240:243], v[100:103]\n\t"
    "ds_read_b128 v[120:123], v2 offset:12288\n\t"
    "v_mfma_f32_16x16x32_bf16 v[100:103], v[72:75], v[244:247], v[100:103]\n\t"
    "ds_read_b128 v[128:131], v3 offset:12288\n\t"
    "v_cvt_pk_bf16_f32 v54, v56, v57\n\t"
    "v_mfma_f32_16x16x32_bf16 v[100:103], v[76:79], v[248:251], v[100:103]\n\t"
    "ds_read_b128 v[132:135], v4 offset:12288\n\t"
    "v_cvt_pk_bf16_f32 v55, v58, v59\n\t"
    "v_mfma_f32_16x16x32_bf16 v[100:103], v[80:83], v[252:255], v[100:103]\n\t"
    "ds_read_b128 v[140:143], v5 offset:12288\n\t"
    "v_mfma_f32_16x16x32_bf16 v[60:63], v[84:87], v[232:235], 0\n\t"
    "ds_read_b128 v[144:147], v0 offset:18432\n\t"
    "v_mfma_f32_16x16x32_bf16 v[60:63], v[88:91], v[236:239], v[60:63]\n\t"
    "ds_read_b128 v[156:159], v1 offset:18432\n\t"
    "v_mfma_f32_16x16x32_bf16 v[60:63], v[92:95], v[240:243], v[60:63]\n\t"
    "ds_read_b128 v[160:163], v2 offset:18432\n\t"
    "v_mfma_f32_16x16x32_bf16 v[60:63], v[108:111], v[244:247], v[60:63]\n\t"
    "ds_read_b128 v[164:167], v3 offset:18432\n\t"
    "v_cvt_pk_bf16_f32 v56, v100, v101\n\t"
    "v_mfma_f32_16x16x32_bf16 v[60:63], v[96:99], v[248:251], v[60:63]\n\t"
    "ds_read_b128 v[168:171], v4 offset:18432\n\t"
    "v_cvt_pk_bf16_f32 v57, v102, v103\n\t"
    "v_mfma_f32_16x16x32_bf16 v[60:63], v[136:139], v[252:255], v[60:63]\n\t"
    "ds_read_b128 v[196:199], v5 offset:18432\n\t"
    "s_waitcnt lgkmcnt(7)\n\t"
    "v_mfma_f32_16x16x32_bf16 v[64:67], v[104:107], a[0:3], 0\n\t"
    "buffer_store_dwordx4 v[180:183], v7, s[52:55], s61 offen offset:768 sc1\n\t"
    "v_mfma_f32_16x16x32_bf16 v[64:67], v[112:115], a[4:7], v[64:67]\n\t"
    "v_mfma_f32_16x16x32_bf16 v[64:67], v[120:123], a[8:11], v[64:67]\n\t"
    "v_mfma_f32_16x16x32_bf16 v[64:67], v[128:131], a[12:15], v[64:67]\n\t"
    "v_cvt_pk_bf16_f32 v58, v60, v61\n\t"
    "v_cvt_pk_bf16_f32 v59, v62, v63\n\t"
    "v_mfma_f32_16x16x32_bf16 v[64:67], v[132:135], a[16:19], v[64:67]\n\t"
    "s_waitcnt lgkmcnt(2)\n\t"
    "v_mfma_f32_16x16x32_bf16 v[64:67], v[140:143], a[20:23], v[64:67]\n\t"
    "v_mfma_f32_16x16x32_bf16 v[68:71], v[144:147], a[0:3], 0\n\t"
    "v_mfma_f32_16x16x32_bf16 v[68:71], v[156:159], a[4:7], v[68:71]\n\t"
    "v_mfma_f32_16x16x32_bf16 v[68:71], v[160:163], a[8:11], v[68:71]\n\t"
    "v_mfma_f32_16x16x32_bf16 v[68:71], v[164:167], a[12:15], v[68:71]\n\t"
    "v_cvt_pk_bf16_f32 v60, v64, v65\n\t"
    "s_waitcnt lgkmcnt(0)\n\t"
    "v_mfma_f32_16x16x32_bf16 v[68:71], v[168:171], a[16:19], v[68:71]\n\t"
    "v_cvt_pk_bf16_f32 v61, v66, v67\n\t"
    "v_mfma_f32_16x16x32_bf16 v[68:71], v[196:199], a[20:23], v[68:71]\n\t"
    "v_mfma_f32_16x16x32_bf16 v[72:75], v[104:107], a[24:27], 0\n\t"
    "buffer_store_dwordx4 v[176:179], v7, s[52:55], s61 offen offset:1152 sc1\n\t"
    "v_mfma_f32_16x16x32_bf16 v[72:75], v[112:115], a[28:31], v[72:75]\n\t"
    "v_mfma_f32_16x16x32_bf16 v[72:75], v[120:123], a[32:35], v[72:75]\n\t"
    "v_mfma_f32_16x16x32_bf16 v[72:75], v[128:131], a[36:39], v[72:75]\n\t"
    "v_cvt_pk_bf16_f32 v62, v68, v69\n\t"
    "v_cvt_pk_bf16_f32 v63, v70, v71\n\t"
    "v_mfma_f32_16x16x32_bf16 v[72:75], v[132:135], a[40:43], v[72:75]\n\t"
    "v_mfma_f32_16x16x32_bf16 v[72:75], v[140:143], a[44:47], v[72:75]\n\t"
    "v_mfma_f32_16x16x32_bf16 v[68:71], v[144:147], a[24:27], 0\n\t"
    "v_mfma_f32_16x16x32_bf16 v[68:71], v[156:159], a[28:31], v[68:71]\n\t"
    "v_mfma_f32_16x16x32_bf16 v[68:71], v[160:163], a[32:35], v[68:71]\n\t"
    "v_mfma_f32_16x16x32_bf16 v[68:71], v[164:167], a[36:39], v[68:71]\n\t"
    "v_cvt_pk_bf16_f32 v64, v72, v73\n\t"
    "v_cvt_pk_bf16_f32 v65, v74, v75\n\t"
    "v_mfma_f32_16x16x32_bf16 v[68:71], v[168:171], a[40:43], v[68:71]\n\t"
    "v_mfma_f32_16x16x32_bf16 v[68:71], v[196:199], a[44:47], v[68:71]\n\t"
    "v_mfma_f32_16x16x32_bf16 v[72:75], v[104:107], a[48:51], 0\n\t"
    "buffer_store_dwordx2 v[116:117], v8, s[52:55], s61 offen offset:0 sc1\n\t"
    "v_mfma_f32_16x16x32_bf16 v[72:75], v[112:115], a[52:55], v[72:75]\n\t"
    "v_mfma_f32_16x16x32_bf16 v[72:75], v[120:123], a[56:59], v[72:75]\n\t"
    "v_mfma_f32_16x16x32_bf16 v[72:75], v[128:131], a[60:63], v[72:75]\n\t"
    "v_cvt_pk_bf16_f32 v66, v68, v69\n\t"
    "v_cvt_pk_bf16_f32 v67, v70, v71\n\t"
    "v_mfma_f32_16x16x32_bf16 v[72:75], v[132:135], a[64:67], v[72:75]\n\t"
    "v_mfma_f32_16x16x32_bf16 v[72:75], v[140:143], a[68:71], v[72:75]\n\t"
    "v_mfma_f32_16x16x32_bf16 v[76:79], v[144:147], a[48:51], 0\n\t"
    "v_mfma_f32_16x16x32_bf16 v[76:79], v[156:159], a[52:55], v[76:79]\n\t"
    "v_mfma_f32_16x16x32_bf16 v[76:79], v[160:163], a[56:59], v[76:79]\n\t"
    "v_mfma_f32_16x16x32_bf16 v[76:79], v[164:167], a[60:63], v[76:79]\n\t"
    "v_cvt_pk_bf16_f32 v68, v72, v73\n\t"
    "v_cvt_pk_bf16_f32 v69, v74, v75\n\t"
    "v_mfma_f32_16x16x32_bf16 v[76:79], v[168:171], a[64:67], v[76:79]\n\t"
    "v_mfma_f32_16x16x32_bf16 v[76:79], v[196:199], a[68:71], v[76:79]\n\t"
    "v_mfma_f32_16x16x32_bf16 v[80:83], v[104:107], a[72:75], 0\n\t"
    "s_add_i32 s64, s62, 24576\n\t"
    "buffer_store_dwordx2 v[118:119], v8, s[52:55], s61 offen offset:384 sc1\n\t"
    "v_mfma_f32_16x16x32_bf16 v[80:83], v[112:115], a[76:79], v[80:83]\n\t"
    "s_cmp_eq_u32 s64, %[rend]\n\t"
    "s_cselect_b32 s64, %[rbeg], s64\n\t"
    "v_mfma_f32_16x16x32_bf16 v[80:83], v[120:123], a[80:83], v[80:83]\n\t"
    "s_sub_i32 s65, s64, s62\n\t"
    "s_mov_b32 s62, s64\n\t"
    "v_mfma_f32_16x16x32_bf16 v[80:83], v[128:131], a[84:87], v[80:83]\n\t"
    "v_add_u32_e32 v0, s65, v0\n\t"
    "v_add_u32_e32 v1, s65, v1\n\t"
    "v_mfma_f32_16x16x32_bf16 v[80:83], v[132:135], a[88:91], v[80:83]\n\t"
    "v_add_u32_e32 v2, s65, v2\n\t"
    "v_add_u32_e32 v3, s65, v3\n\t"
    "v_mfma_f32_16x16x32_bf16 v[80:83], v[140:143], a[92:95], v[80:83]\n\t"
    "v_add_u32_e32 v4, s65, v4\n\t"
    "v_add_u32_e32 v5, s65, v5\n\t"
    "v_mfma_f32_16x16x32_bf16 v[84:87], v[144:147], a[72:75], 0\n\t"
    "v_cvt_pk_bf16_f32 v70, v76, v77\n\t"
    "v_cvt_pk_bf16_f32 v71, v78, v79\n\t"
    "v_mfma_f32_16x16x32_bf16 v[84:87], v[156:159], a[76:79], v[84:87]\n\t"
    "v_mfma_f32_16x16x32_bf16 v[84:87], v[160:163], a[80:83], v[84:87]\n\t"
    "v_mfma_f32_16x16x32_bf16 v[84:87], v[164:167], a[84:87], v[84:87]\n\t"
    "v_cvt_pk_bf16_f32 v72, v80, v81\n\t"
    "v_cvt_pk_bf16_f32 v73, v82, v83\n\t"
    "v_mfma_f32_16x16x32_bf16 v[84:87], v[168:171], a[88:91], v[84:87]\n\t"
    "v_mfma_f32_16x16x32_bf16 v[84:87], v[196:199], a[92:95], v[84:87]\n\t"
    "v_mfma_f32_16x16x32_bf16 v[80:83], v[104:107], a[96:99], 0\n\t"
    "buffer_store_dwordx2 v[148:149], v8, s[52:55], s61 offen offset:768 sc1\n\t"
    "v_mfma_f32_16x16x32_bf16 v[80:83], v[112:115], a[100:103], v[80:83]\n\t"
    "v_mfma_f32_16x16x32_bf16 v[80:83], v[120:123], a[104:107], v[80:83]\n\t"
    "v_mfma_f32_16x16x32_bf16 v[80:83], v[128:131], a[108:111], v[80:83]\n\t"
    "v_cvt_pk_bf16_f32 v74, v84, v85\n\t"
    "v_cvt_pk_bf16_f32 v75, v86, v87\n\t"
    "v_mfma_f32_16x16x32_bf16 v[80:83], v[132:135], a[112:115], v[80:83]\n\t"
    "v_mfma_f32_16x16x32_bf16 v[80:83], v[140:143], a[116:119], v[80:83]\n\t"
    "v_mfma_f32_16x16x32_bf16 v[84:87], v[144:147], a[96:99], 0\n\t"
    "v_mfma_f32_16x16x32_bf16 v[84:87], v[156:159], a[100:103], v[84:87]\n\t"
    "v_mfma_f32_16x16x32_bf16 v[84:87], v[160:163], a[104:107], v[84:87]\n\t"
    "v_mfma_f32_16x16x32_bf16 v[84:87], v[164:167], a[108:111], v[84:87]\n\t"
    "v_cvt_pk_bf16_f32 v76, v80, v81\n\t"
    "v_cvt_pk_bf16_f32 v77, v82, v83\n\t"
    "v_mfma_f32_16x16x32_bf16 v[84:87], v[168:171], a[112:115], v[84:87]\n\t"
    "v_mfma_f32_16x16x32_bf16 v[84:87], v[196:199], a[116:119], v[84:87]\n\t"
    "v_mfma_f32_16x16x32_bf16 v[88:91], v[104:107], a[120:123], 0\n\t"
    "buffer_store_dwordx2 v[150:151], v8, s[52:55], s61 offen offset:1152 sc1\n\t"
    "v_mfma_f32_16x16x32_bf16 v[88:91], v[112:115], a[124:127], v[88:91]\n\t"
    "s_add_u32 s61, s61, 393216\n\t"
    "v_mfma_f32_16x16x32_bf16 v[88:91], v[120:123], a[128:131], v[88:91]\n\t"
    "v_mfma_f32_16x16x32_bf16 v[88:91], v[128:131], a[132:135], v[88:91]\n\t"
    "v_cvt_pk_bf16_f32 v78, v84, v85\n\t"
    "v_cvt_pk_bf16_f32 v79, v86, v87\n\t"
    "v_mfma_f32_16x16x32_bf16 v[88:91], v[132:135], a[136:139], v[88:91]\n\t"
    "v_mfma_f32_16x16x32_bf16 v[88:91], v[140:143], a[140:143], v[88:91]\n\t"
    "v_mfma_f32_16x16x32_bf16 v[84:87], v[144:147], a[120:123], 0\n\t"
    "v_mfma_f32_16x16x32_bf16 v[84:87], v[156:159], a[124:127], v[84:87]\n\t"
    "v_mfma_f32_16x16x32_bf16 v[84:87], v[160:163], a[128:131], v[84:87]\n\t"
    "v_mfma_f32_16x16x32_bf16 v[84:87], v[164:167], a[132:135], v[84:87]\n\t"
    "v_cvt_pk_bf16_f32 v80, v88, v89\n\t"
    "v_cvt_pk_bf16_f32 v81, v90, v91\n\t"
    "v_mfma_f32_16x16x32_bf16 v[84:87], v[168:171], a[136:139], v[84:87]\n\t"
    "v_mfma_f32_16x16x32_bf16 v[84:87], v[196:199], a[140:143], v[84:87]\n\t"
    "v_mfma_f32_16x16x32_bf16 v[88:91], v[104:107], a[144:147], 0\n\t"
    "buffer_store_dwordx4 v[152:155], v7, s[52:55], s61 offen offset:0 sc1\n\t"
    "v_mfma_f32_16x16x32_bf16 v[88:91], v[112:115], a[148:151], v[88:91]\n\t"
    "v_mfma_f32_16x16x32_bf16 v[88:91], v[120:123], a[152:155], v[88:91]\n\t"
    "v_mfma_f32_16x16x32_bf16 v[88:91], v[128:131], a[156:159], v[88:91]\n\t"
    "v_cvt_pk_bf16_f32 v82, v84, v85\n\t"
    "v_cvt_pk_bf16_f32 v83, v86, v87\n\t"
    "v_mfma_f32_16x16x32_bf16 v[88:91], v[132:135], a[160:163], v[88:91]\n\t"
    "v_mfma_f32_16x16x32_bf16 v[88:91], v[140:143], a[164:167], v[88:91]\n\t"
    "v_mfma_f32_16x16x32_bf16 v[92:95], v[144:147], a[144:147], 0\n\t"
    "v_mfma_f32_16x16x32_bf16 v[92:95], v[156:159], a[148:151], v[92:95]\n\t"
    "v_mfma_f32_16x16x32_bf16 v[92:95], v[160:163], a[152:155], v[92:95]\n\t"
    "v_mfma_f32_16x16x32_bf16 v[92:95], v[164:167], a[156:159], v[92:95]\n\t"
    "v_cvt_pk_bf16_f32 v84, v88, v89\n\t"
    "v_cvt_pk_bf16_f32 v85, v90, v91\n\t"
    "v_mfma_f32_16x16x32_bf16 v[92:95], v[168:171], a[160:163], v[92:95]\n\t"
    "v_mfma_f32_16x16x32_bf16 v[92:95], v[196:199], a[164:167], v[92:95]\n\t"
    "v_mfma_f32_16x16x32_bf16 v[96:99], v[104:107], a[168:171], 0\n\t"
    "buffer_store_dwordx4 v[124:127], v7, s[52:55], s61 offen offset:384 sc1\n\t"
    "v_mfma_f32_16x16x32_bf16 v[96:99], v[112:115], a[172:175], v[96:99]\n\t"
    "v_mfma_f32_16x16x32_bf16 v[96:99], v[120:123], a[176:179], v[96:99]\n\t"
    "v_mfma_f32_16x16x32_bf16 v[96:99], v[128:131], a[180:183], v[96:99]\n\t"
    "v_cvt_pk_bf16_f32 v86, v92, v93\n\t"
    "v_cvt_pk_bf16_f32 v87, v94, v95\n\t"
    "v_mfma_f32_16x16x32_bf16 v[96:99], v[132:135], a[184:187], v[96:99]\n\t"
    "v_mfma_f32_16x16x32_bf16 v[96:99], v[140:143], a[188:191], v[96:99]\n\t"
    "v_mfma_f32_16x16x32_bf16 v[92:95], v[144:147], a[168:171], 0\n\t"
    "v_mfma_f32_16x16x32_bf16 v[92:95], v[156:159], a[172:175], v[92:95]\n\t"
    "v_mfma_f32_16x16x32_bf16 v[92:95], v[160:163], a[176:179], v[92:95]\n\t"
    "v_mfma_f32_16x16x32_bf16 v[92:95], v[164:167], a[180:183], v[92:95]\n\t"
    "v_cvt_pk_bf16_f32 v88, v96, v97\n\t"
    "v_cvt_pk_bf16_f32 v89, v98, v99\n\t"
    "v_mfma_f32_16x16x32_bf16 v[92:95], v[168:171], a[184:187], v[92:95]\n\t"
    "v_mfma_f32_16x16x32_bf16 v[92:95], v[196:199], a[188:191], v[92:95]\n\t"
    "v_mfma_f32_16x16x32_bf16 v[96:99], v[104:107], a[192:195], 0\n\t"
    "buffer_store_dwordx4 v[192:195], v7, s[52:55], s61 offen offset:768 sc1\n\t"
    "v_mfma_f32_16x16x32_bf16 v[96:99], v[112:115], a[196:199], v[96:99]\n\t"
    "v_mfma_f32_16x16x32_bf16 v[96:99], v[120:123], a[200:203], v[96:99]\n\t"
    "v_mfma_f32_16x16x32_bf16 v[96:99], v[128:131], a[204:207], v[96:99]\n\t"
    "v_cvt_pk_bf16_f32 v90, v92, v93\n\t"
    "v_cvt_pk_bf16_f32 v91, v94, v95\n\t"
    "v_mfma_f32_16x16x32_bf16 v[96:99], v[132:135], a[208:211], v[96:99]\n\t"
    "v_mfma_f32_16x16x32_bf16 v[96:99], v[140:143], a[212:215], v[96:99]\n\t"
    "v_mfma_f32_16x16x32_bf16 v[100:103], v[144:147], a[192:195], 0\n\t"
    "v_mfma_f32_16x16x32_bf16 v[100:103], v[156:159], a[196:199], v[100:103]\n\t"
    "v_mfma_f32_16x16x32_bf16 v[100:103], v[160:163], a[200:203], v[100:103]\n\t"
    "v_mfma_f32_16x16x32_bf16 v[100:103], v[164:167], a[204:207], v[100:103]\n\t"
    "v_cvt_pk_bf16_f32 v92, v96, v97\n\t"
    "v_cvt_pk_bf16_f32 v93, v98, v99\n\t"
    "v_mfma_f32_16x16x32_bf16 v[100:103], v[168:171], a[208:211], v[100:103]\n\t"
    "s_barrier\n\t"
    "v_mfma_f32_16x16x32_bf16 v[100:103], v[196:199], a[212:215], v[100:103]\n\t"
    "v_mfma_f32_16x16x32_bf16 v[108:111], v[104:107], a[216:219], 0\n\t"
    "buffer_store_dwordx4 v[184:187], v7, s[52:55], s61 offen offset:1152 sc1\n\t"
    "v_mfma_f32_16x16x32_bf16 v[108:111], v[112:115], a[220:223], v[108:111]\n\t"
    "v_mfma_f32_16x16x32_bf16 v[108:111], v[120:123], a[224:227], v[108:111]\n\t"
    "v_mfma_f32_16x16x32_bf16 v[108:111], v[128:131], a[228:231], v[108:111]\n\t"
    "v_cvt_pk_bf16_f32 v94, v100, v101\n\t"
    "v_cvt_pk_bf16_f32 v95, v102, v103\n\t"
    "v_mfma_f32_16x16x32_bf16 v[108:111], v[132:135], a[232:235], v[108:111]\n\t"
    "v_mfma_f32_16x16x32_bf16 v[108:111], v[140:143], a[236:239], v[108:111]\n\t"
    "v_mfma_f32_16x16x32_bf16 v[100:103], v[144:147], a[216:219], 0\n\t"
    "v_mfma_f32_16x16x32_bf16 v[100:103], v[156:159], a[220:223], v[100:103]\n\t"
    "v_mfma_f32_16x16x32_bf16 v[100:103], v[160:163], a[224:227], v[100:103]\n\t"
    "v_mfma_f32_16x16x32_bf16 v[100:103], v[164:167], a[228:231], v[100:103]\n\t"
    "v_cvt_pk_bf16_f32 v96, v108, v109\n\t"
    "v_cvt_pk_bf16_f32 v97, v110, v111\n\t"
    "v_mfma_f32_16x16x32_bf16 v[100:103], v[168:171], a[232:235], v[100:103]\n\t"
    "v_mfma_f32_16x16x32_bf16 v[100:103], v[196:199], a[236:239], v[100:103]\n\t"
    "v_mfma_f32_16x16x32_bf16 v[108:111], v[104:107], a[240:243], 0\n\t"
    "buffer_store_dwordx2 v[172:173], v8, s[52:55], s61 offen offset:0 sc1\n\t"
    "v_mfma_f32_16x16x32_bf16 v[108:111], v[112:115], a[244:247], v[108:111]\n\t"
    "v_mfma_f32_16x16x32_bf16 v[108:111], v[120:123], a[248:251], v[108:111]\n\t"
    "v_mfma_f32_16x16x32_bf16 v[108:111], v[128:131], a[252:255], v[108:111]\n\t"
    "v_cvt_pk_bf16_f32 v98, v100, v101\n\t"
    "v_cvt_pk_bf16_f32 v99, v102, v103\n\t"
    "v_mfma_f32_16x16x32_bf16 v[108:111], v[132:135], v[224:227], v[108:111]\n\t"
    "v_mfma_f32_16x16x32_bf16 v[108:111], v[140:143], v[228:231], v[108:111]\n\t"
    "v_mfma_f32_16x16x32_bf16 v[116:119], v[144:147], a[240:243], 0\n\t"
    "v_mfma_f32_16x16x32_bf16 v[116:119], v[156:159], a[244:247], v[116:119]\n\t"
    "v_mfma_f32_16x16x32_bf16 v[116:119], v[160:163], a[248:251], v[116:119]\n\t"
    "v_mfma_f32_16x16x32_bf16 v[116:119], v[164:167], a[252:255], v[116:119]\n\t"
    "v_cvt_pk_bf16_f32 v100, v108, v109\n\t"
    "v_cvt_pk_bf16_f32 v101, v110, v111\n\t"
    "v_mfma_f32_16x16x32_bf16 v[116:119], v[168:171], v[224:227], v[116:119]\n\t"
    "v_mfma_f32_16x16x32_bf16 v[116:119], v[196:199], v[228:231], v[116:119]\n\t"
    "v_mfma_f32_16x16x32_bf16 v[108:111], v[104:107], v[232:235], 0\n\t"
    "buffer_store_dwordx2 v[174:175], v8, s[52:55], s61 offen offset:384 sc1\n\t"
    "v_mfma_f32_16x16x32_bf16 v[108:111], v[112:115], v[236:239], v[108:111]\n\t"
    "ds_read_b128 v[148:151], v6 offset:0\n\t"
    "v_mfma_f32_16x16x32_bf16 v[108:111], v[120:123], v[240:243], v[108:111]\n\t"
    "ds_read_b128 v[152:155], v6 offset:1024\n\t"
    "v_mfma_f32_16x16x32_bf16 v[108:111], v[128:131], v[244:247], v[108:111]\n\t"
    "v_cvt_pk_bf16_f32 v102, v116, v117\n\t"
    "v_cvt_pk_bf16_f32 v103, v118, v119\n\t"
    "v_mfma_f32_16x16x32_bf16 v[108:111], v[132:135], v[248:251], v[108:111]\n\t"
    "v_mfma_f32_16x16x32_bf16 v[108:111], v[140:143], v[252:255], v[108:111]\n\t"
    "v_mfma_f32_16x16x32_bf16 v[112:115], v[144:147], v[232:235], 0\n\t"
    "v_mfma_f32_16x16x32_bf16 v[112:115], v[156:159], v[236:239], v[112:115]\n\t"
    "v_mfma_f32_16x16x32_bf16 v[112:115], v[160:163], v[240:243], v[112:115]\n\t"
    "v_mfma_f32_16x16x32_bf16 v[112:115], v[164:167], v[244:247], v[112:115]\n\t"
    "v_cvt_pk_bf16_f32 v104, v108, v109\n\t"
    "v_cvt_pk_bf16_f32 v105, v110, v111\n\t"
    "v_mfma_f32_16x16x32_bf16 v[112:115], v[168:171], v[248:251], v[112:115]\n\t"
    "v_mfma_f32_16x16x32_bf16 v[112:115], v[196:199], v[252:255], v[112:115]\n\t"
    "s_waitcnt lgkmcnt(0)\n\t"
    "v_mfma_f32_16x16x32_bf16 v[120:123], v[148:151], v[12:15], 0\n\t"
    "buffer_store_dwordx2 v[188:189], v8, s[52:55], s61 offen offset:768 sc1\n\t"
    "v_mfma_f32_16x16x32_bf16 v[120:123], v[152:155], v[60:63], v[120:123]\n\t"
    "v_mfma_f32_16x16x32_bf16 v[124:127], v[148:151], v[16:19], 0\n\t"
    "v_mfma_f32_16x16x32_bf16 v[124:127], v[152:155], v[64:67], v[124:127]\n\t"
    "v_cvt_pk_bf16_f32 v106, v112, v113\n\t"
    "v_cvt_pk_bf16_f32 v107, v114, v115\n\t"
    "v_mfma_f32_16x16x32_bf16 v[128:131], v[148:151], v[20:23], 0\n\t"
    "v_mfma_f32_16x16x32_bf16 v[128:131], v[152:155], v[68:71], v[128:131]\n\t"
    "v_mfma_f32_16x16x32_bf16 v[132:135], v[148:151], v[24:27], 0\n\t"
    "v_mfma_f32_16x16x32_bf16 v[132:135], v[152:155], v[72:75], v[132:135]\n\t"
    "v_cvt_pk_bf16_f32 v156, v120, v124\n\t"
    "v_cvt_pk_bf16_f32 v108, v121, v125\n\t"
    "v_mfma_f32_16x16x32_bf16 v[136:139], v[148:151], v[28:31], 0\n\t"
    "v_cvt_pk_bf16_f32 v116, v122, v126\n\t"
    "v_cvt_pk_bf16_f32 v112, v123, v127\n\t"
    "v_mfma_f32_16x16x32_bf16 v[136:139], v[152:155], v[76:79], v[136:139]\n\t"
    "v_mfma_f32_16x16x32_bf16 v[140:143], v[148:151], v[32:35], 0\n\t"
    "v_mfma_f32_16x16x32_bf16 v[140:143], v[152:155], v[80:83], v[140:143]\n\t"
    "v_cvt_pk_bf16_f32 v157, v128, v132\n\t"
    "v_cvt_pk_bf16_f32 v109, v129, v133\n\t"
    "v_mfma_f32_16x16x32_bf16 v[144:147], v[148:151], v[36:39], 0\n\t"
    "buffer_store_dwordx2 v[190:191], v8, s[52:55], s61 offen offset:1152 sc1\n\t"
    "v_mfma_f32_16x16x32_bf16 v[144:147], v[152:155], v[84:87], v[144:147]\n\t"
    "ds_read_b128 v[120:123], v6 offset:2048\n\t"
    "s_add_u32 s61, s61, 11403264\n\t"
    "v_mfma_f32_16x16x32_bf16 v[160:163], v[148:151], v[40:43], 0\n\t"
    "ds_read_b128 v[124:127], v6 offset:3072\n\t"
    "v_cvt_pk_bf16_f32 v117, v130, v134\n\t"
    "v_mfma_f32_16x16x32_bf16 v[160:163], v[152:155], v[88:91], v[160:163]\n\t"
    "v_cvt_pk_bf16_f32 v113, v131, v135\n\t"
    "v_cvt_pk_bf16_f32 v158, v136, v140\n\t"
    "v_mfma_f32_16x16x32_bf16 v[164:167], v[148:151], v[44:47], 0\n\t"
    "v_cvt_pk_bf16_f32 v110, v137, v141\n\t"
    "v_cvt_pk_bf16_f32 v118, v138, v142\n\t"
    "v_mfma_f32_16x16x32_bf16 v[164:167], v[152:155], v[92:95], v[164:167]\n\t"
    "v_cvt_pk_bf16_f32 v114, v139, v143\n\t"
    "v_mfma_f32_16x16x32_bf16 v[168:171], v[148:151], v[48:51], 0\n\t"
    "v_mfma_f32_16x16x32_bf16 v[168:171], v[152:155], v[96:99], v[168:171]\n\t"
    "v_cvt_pk_bf16_f32 v159, v144, v160\n\t"
    "v_cvt_pk_bf16_f32 v111, v145, v161\n\t"
    "v_mfma_f32_16x16x32_bf16 v[128:131], v[148:151], v[52:55], 0\n\t"
    "v_cvt_pk_bf16_f32 v119, v146, v162\n\t"
    "v_cvt_pk_bf16_f32 v115, v147, v163\n\t"
    "v_mfma_f32_16x16x32_bf16 v[128:131], v[152:155], v[100:103], v[128:131]\n\t"
    "v_mfma_f32_16x16x32_bf16 v[132:135], v[148:151], v[56:59], 0\n\t"
    "v_mfma_f32_16x16x32_bf16 v[132:135], v[152:155], v[104:107], v[132:135]\n\t"
    "buffer_store_dwordx4 v[156:159], v7, s[52:55], s61 offen offset:0 sc1\n\t"
    "s_waitcnt lgkmcnt(0)\n\t"
    "v_mfma_f32_16x16x32_bf16 v[140:143], v[120:123], v[12:15], 0\n\t"
    ".Lh2_%=:\n\t"
    "s_cbranch_execz .LrM_%=\n\t"
    "v_cvt_pk_bf16_f32 v136, v164, v168\n\t"
    "v_cvt_pk_bf16_f32 v138, v165, v169\n\t"
    "v_mfma_f32_16x16x32_bf16 v[140:143], v[124:127], v[60:63], v[140:143]\n\t"
    "v_cvt_pk_bf16_f32 v144, v166, v170\n\t"
    "v_cvt_pk_bf16_f32 v146, v167, v171\n\t"
    "v_mfma_f32_16x16x32_bf16 v[148:151], v[120:123], v[16:19], 0\n\t"
    "v_mfma_f32_16x16x32_bf16 v[148:151], v[124:127], v[64:67], v[148:151]\n\t"
    "v_cvt_pk_bf16_f32 v137, v128, v132\n\t"
    "v_cvt_pk_bf16_f32 v139, v129, v133\n\t"
    "v_mfma_f32_16x16x32_bf16 v[152:155], v[120:123], v[20:23], 0\n\t"
    "v_cvt_pk_bf16_f32 v145, v130, v134\n\t"
    "v_cvt_pk_bf16_f32 v147, v131, v135\n\t"
    "v_mfma_f32_16x16x32_bf16 v[152:155], v[124:127], v[68:71], v[152:155]\n\t"
    "v_mfma_f32_16x16x32_bf16 v[156:159], v[120:123], v[24:27], 0\n\t"
    "v_mfma_f32_16x16x32_bf16 v[156:159], v[124:127], v[72:75], v[156:159]\n\t"
    "v_cvt_pk_bf16_f32 v132, v140, v148\n\t"
    "v_cvt_pk_bf16_f32 v128, v141, v149\n\t"
    "v_mfma_f32_16x16x32_bf16 v[168:171], v[120:123], v[28:31], 0\n\t"
    "v_cvt_pk_bf16_f32 v164, v142, v150\n\t"
    "v_cvt_pk_bf16_f32 v160, v143, v151\n\t"
    "v_mfma_f32_16x16x32_bf16 v[168:171], v[124:127], v[76:79], v[168:171]\n\t"
    "v_mfma_f32_16x16x32_bf16 v[172:175], v[120:123], v[32:35], 0\n\t"
    "v_mfma_f32_16x16x32_bf16 v[172:175], v[124:127], v[80:83], v[172:175]\n\t"
    "buffer_store_dwordx4 v[108:111], v7, s[52:55], s61 offen offset:384 sc1\n\t"
    "v_mfma_f32_16x16x32_bf16 v[140:143], v[120:123], v[36:39], 0\n\t"
    "v_cvt_pk_bf16_f32 v133, v152, v156\n\t"
    "v_cvt_pk_bf16_f32 v129, v153, v157\n\t"
    "v_mfma_f32_16x16x32_bf16 v[140:143], v[124:127], v[84:87], v[140:143]\n\t"
    "ds_read_b128 v[148:151], v6 offset:4096\n\t"
    "v_cvt_pk_bf16_f32 v165, v154, v158\n\t"
    "v_mfma_f32_16x16x32_bf16 v[176:179], v[120:123], v[40:43], 0\n\t"
    "ds_read_b128 v[108:111], v6 offset:5120\n\t"
    "v_cvt_pk_bf16_f32 v161, v155, v159\n\t"
    "v_mfma_f32_16x16x32_bf16 v[176:179], v[124:127], v[88:91], v[176:179]\n\t"
    "v_cvt_pk_bf16_f32 v134, v168, v172\n\t"
    "v_cvt_pk_bf16_f32 v130, v169, v173\n\t"
    "v_mfma_f32_16x16x32_bf16 v[180:183], v[120:123], v[44:47], 0\n\t"
    "v_cvt_pk_bf16_f32 v166, v170, v174\n\t"
    "v_cvt_pk_bf16_f32 v162, v171, v175\n\t"
    "v_mfma_f32_16x16x32_bf16 v[180:183], v[124:127], v[92:95], v[180:183]\n\t"
    "v_mfma_f32_16x16x32_bf16 v[152:155], v[120:123], v[48:51], 0\n\t"
    "v_mfma_f32_16x16x32_bf16 v[152:155], v[124:127], v[96:99], v[152:155]\n\t"
    "v_cvt_pk_bf16_f32 v135, v140, v176\n\t"
    "v_cvt_pk_bf16_f32 v131, v141, v177\n\t"
    "v_mfma_f32_16x16x32_bf16 v[156:159], v[120:123], v[52:55], 0\n\t"
    "v_cvt_pk_bf16_f32 v167, v142, v178\n\t"
    "v_cvt_pk_bf16_f32 v163, v143, v179\n\t"
    "v_mfma_f32_16x16x32_bf16 v[156:159], v[124:127], v[100:103], v[156:159]\n\t"
    "v_mfma_f32_16x16x32_bf16 v[168:171], v[120:123], v[56:59], 0\n\t"
    "v_mfma_f32_16x16x32_bf16 v[168:171], v[124:127], v[104:107], v[168:171]\n\t"
    "buffer_store_dwordx4 v[116:119], v7, s[52:55], s61 offen offset:768 sc1\n\t"
    "s_waitcnt lgkmcnt(0)\n\t"
    "v_mfma_f32_16x16x32_bf16 v[172:175], v[148:151], v[12:15], 0\n\t"
    "v_cvt_pk_bf16_f32 v140, v180, v152\n\t"
    "v_cvt_pk_bf16_f32 v142, v181, v153\n\t"
    "v_mfma_f32_16x16x32_bf16 v[172:175], v[108:111], v[60:63], v[172:175]\n\t"
    "v_cvt_pk_bf16_f32 v120, v182, v154\n\t"
    "v_cvt_pk_bf16_f32 v122, v183, v155\n\t"
    "v_mfma_f32_16x16x32_bf16 v[116:119], v[148:151], v[16:19], 0\n\t"
    "v_mfma_f32_16x16x32_bf16 v[116:119], v[108:111], v[64:67], v[116:119]\n\t"
    "v_cvt_pk_bf16_f32 v141, v156, v168\n\t"
    "v_cvt_pk_bf16_f32 v143, v157, v169\n\t"
    "v_mfma_f32_16x16x32_bf16 v[124:127], v[148:151], v[20:23], 0\n\t"
    "v_cvt_pk_bf16_f32 v121, v158, v170\n\t"
    "v_cvt_pk_bf16_f32 v123, v159, v171\n\t"
    "v_mfma_f32_16x16x32_bf16 v[124:127], v[108:111], v[68:71], v[124:127]\n\t"
    "v_mfma_f32_16x16x32_bf16 v[152:155], v[148:151], v[24:27], 0\n\t"
    "v_mfma_f32_16x16x32_bf16 v[152:155], v[108:111], v[72:75], v[152:155]\n\t"
    "v_cvt_pk_bf16_f32 v168, v172, v116\n\t"
    "v_cvt_pk_bf16_f32 v156, v173, v117\n\t"
    "v_mfma_f32_16x16x32_bf16 v[184:187], v[148:151], v[28:31], 0\n\t"
    "v_cvt_pk_bf16_f32 v180, v174, v118\n\t"
    "v_cvt_pk_bf16_f32 v176, v175, v119\n\t"
    "v_mfma_f32_16x16x32_bf16 v[184:187], v[108:111], v[76:79], v[184:187]\n\t"
    "v_mfma_f32_16x16x32_bf16 v[188:191], v[148:151], v[32:35], 0\n\t"
    "v_mfma_f32_16x16x32_bf16 v[188:191], v[108:111], v[80:83], v[188:191]\n\t"
    "buffer_store_dwordx4 v[112:115], v7, s[52:55], s61 offen offset:1152 sc1\n\t"
    "v_mfma_f32_16x16x32_bf16 v[116:119], v[148:151], v[36:39], 0\n\t"
    "s_barrier\n\t"
    "v_cvt_pk_bf16_f32 v169, v124, v152\n\t"
    "v_mfma_f32_16x16x32_bf16 v[116:119], v[108:111], v[84:87], v[116:119]\n\t"
    "ds_read_b128 v[172:175], v6 offset:6144\n\t"
    "v_cvt_pk_bf16_f32 v157, v125, v153\n\t"
    "v_mfma_f32_16x16x32_bf16 v[192:195], v[148:151], v[40:43], 0\n\t"
    "ds_read_b128 v[112:115], v6 offset:7168\n\t"
    "v_cvt_pk_bf16_f32 v181, v126, v154\n\t"
    "v_mfma_f32_16x16x32_bf16 v[192:195], v[108:111], v[88:91], v[192:195]\n\t"
    "v_cvt_pk_bf16_f32 v177, v127, v155\n\t"
    "v_cvt_pk_bf16_f32 v170, v184, v188\n\t"
    "v_mfma_f32_16x16x32_bf16 v[196:199], v[148:151], v[44:47], 0\n\t"
    "v_cvt_pk_bf16_f32 v158, v185, v189\n\t"
    "v_cvt_pk_bf16_f32 v182, v186, v190\n\t"
    "v_mfma_f32_16x16x32_bf16 v[196:199], v[108:111], v[92:95], v[196:199]\n\t"
    "v_cvt_pk_bf16_f32 v178, v187, v191\n\t"
    "v_mfma_f32_16x16x32_bf16 v[124:127], v[148:151], v[48:51], 0\n\t"
    "v_mfma_f32_16x16x32_bf16 v[124:127], v[108:111], v[96:99], v[124:127]\n\t"
    "v_cvt_pk_bf16_f32 v171, v116, v192\n\t"
    "v_cvt_pk_bf16_f32 v159, v117, v193\n\t"
    "v_mfma_f32_16x16x32_bf16 v[152:155], v[148:151], v[52:55], 0\n\t"
    "v_cvt_pk_bf16_f32 v183, v118, v194\n\t"
    "v_cvt_pk_bf16_f32 v179, v119, v195\n\t"
    "v_mfma_f32_16x16x32_bf16 v[152:155], v[108:111], v[100:103], v[152:155]\n\t"
    "v_mfma_f32_16x16x32_bf16 v[184:187], v[148:151], v[56:59], 0\n\t"
    "v_mfma_f32_16x16x32_bf16 v[184:187], v[108:111], v[104:107], v[184:187]\n\t"
    "buffer_store_dwordx2 v[136:137], v8, s[52:55], s61 offen offset:0 sc1\n\t"
    "s_waitcnt lgkmcnt(0)\n\t"
    "v_mfma_f32_16x16x32_bf16 v[188:191], v[172:175], v[12:15], 0\n\t"
    "v_cvt_pk_bf16_f32 v116, v196, v124\n\t"
    "v_cvt_pk_bf16_f32 v118, v197, v125\n\t"
    "v_mfma_f32_16x16x32_bf16 v[188:191], v[112:115], v[60:63], v[188:191]\n\t"
    "v_cvt_pk_bf16_f32 v148, v198, v126\n\t"
    "v_cvt_pk_bf16_f32 v150, v199, v127\n\t"
    "v_mfma_f32_16x16x32_bf16 v[108:111], v[172:175], v[16:19], 0\n\t"
    "v_mfma_f32_16x16x32_bf16 v[108:111], v[112:115], v[64:67], v[108:111]\n\t"
    "v_cvt_pk_bf16_f32 v117, v152, v184\n\t"
    "v_cvt_pk_bf16_f32 v119, v153, v185\n\t"
    "v_mfma_f32_16x16x32_bf16 v[12:15], v[172:175], v[20:23], 0\n\t"
    "v_cvt_pk_bf16_f32 v149, v154, v186\n\t"
    "v_cvt_pk_bf16_f32 v151, v155, v187\n\t"
    "v_mfma_f32_16x16x32_bf16 v[12:15], v[112:115], v[68:71], v[12:15]\n\t"
    "v_mfma_f32_16x16x32_bf16 v[16:19], v[172:175], v[24:27], 0\n\t"
    "v_mfma_f32_16x16x32_bf16 v[16:19], v[112:115], v[72:75], v[16:19]\n\t"
    "v_cvt_pk_bf16_f32 v152, v188, v108\n\t"
    "v_cvt_pk_bf16_f32 v124, v189, v109\n\t"
    "v_mfma_f32_16x16x32_bf16 v[20:23], v[172:175], v[28:31], 0\n\t"
    "v_cvt_pk_bf16_f32 v192, v190, v110\n\t"
    "v_cvt_pk_bf16_f32 v184, v191, v111\n\t"
    "v_mfma_f32_16x16x32_bf16 v[20:23], v[112:115], v[76:79], v[20:23]\n\t"
    "v_mfma_f32_16x16x32_bf16 v[24:27], v[172:175], v[32:35], 0\n\t"
    "v_mfma_f32_16x16x32_bf16 v[24:27], v[112:115], v[80:83], v[24:27]\n\t"
    "buffer_store_dwordx2 v[138:139], v8, s[52:55], s61 offen offset:384 sc1\n\t"
    "v_mfma_f32_16x16x32_bf16 v[28:31], v[172:175], v[36:39], 0\n\t"
    "ds_read_b128 v[60:63], v0 offset:0\n\t"
    "v_cvt_pk_bf16_f32 v153, v12, v16\n\t"
    "v_mfma_f32_16x16x32_bf16 v[28:31], v[112:115], v[84:87], v[28:31]\n\t"
    "ds_read_b128 v[64:67], v1 offset:0\n\t"
    "v_cvt_pk_bf16_f32 v125, v13, v17\n\t"
    "v_mfma_f32_16x16x32_bf16 v[32:35], v[172:175], v[40:43], 0\n\t"
    "ds_read_b128 v[68:71], v2 offset:0\n\t"
    "v_cvt_pk_bf16_f32 v193, v14, v18\n\t"
    "v_mfma_f32_16x16x32_bf16 v[32:35], v[112:115], v[88:91], v[32:35]\n\t"
    "ds_read_b128 v[72:75], v3 offset:0\n\t"
    "v_cvt_pk_bf16_f32 v185, v15, v19\n\t"
    "v_mfma_f32_16x16x32_bf16 v[36:39], v[172:175], v[44:47], 0\n\t"
    "ds_read_b128 v[76:79], v4 offset:0\n\t"
    "v_cvt_pk_bf16_f32 v154, v20, v24\n\t"
    "v_mfma_f32_16x16x32_bf16 v[36:39], v[112:115], v[92:95], v[36:39]\n\t"
    "ds_read_b128 v[80:83], v5 offset:0\n\t"
    "v_cvt_pk_bf16_f32 v126, v21, v25\n\t"
    "v_mfma_f32_16x16x32_bf16 v[12:15], v[172:175], v[48:51], 0\n\t"
    "ds_read_b128 v[84:87], v0 offset:6144\n\t"
    "v_cvt_pk_bf16_f32 v194, v22, v26\n\t"
    "v_mfma_f32_16x16x32_bf16 v[12:15], v[112:115], v[96:99], v[12:15]\n\t"
    "ds_read_b128 v[88:91], v1 offset:6144\n\t"
    "v_cvt_pk_bf16_f32 v186, v23, v27\n\t"
    "v_mfma_f32_16x16x32_bf16 v[16:19], v[172:175], v[52:55], 0\n\t"
    "ds_read_b128 v[92:95], v2 offset:6144\n\t"
    "v_cvt_pk_bf16_f32 v155, v28, v32\n\t"
    "v_mfma_f32_16x16x32_bf16 v[16:19], v[112:115], v[100:103], v[16:19]\n\t"
    "ds_read_b128 v[108:111], v3 offset:6144\n\t"
    "v_cvt_pk_bf16_f32 v127, v29, v33\n\t"
    "v_mfma_f32_16x16x32_bf16 v[20:23], v[172:175], v[56:59], 0\n\t"
    "ds_read_b128 v[96:99], v4 offset:6144\n\t"
    "v_cvt_pk_bf16_f32 v195, v30, v34\n\t"
    "v_mfma_f32_16x16x32_bf16 v[20:23], v[112:115], v[104:107], v[20:23]\n\t"
    "ds_read_b128 v[136:139], v5 offset:6144\n\t"
    "s_sub_u32 s60, s60, 1\n\t"
    "s_cmp_lg_u32 s60, 0\n\t"
    "s_cbranch_scc1 .Lf9_%=\n\t"
    "s_nop 7\n\t"
    "s_nop 1\n\t"
    "buffer_store_dwordx2 v[144:145], v8, s[52:55], s61 offen offset:768 sc1\n\t"
    "v_cvt_pk_bf16_f32 v187, v31, v35\n\t"
    "v_cvt_pk_bf16_f32 v172, v36, v12\n\t"
    "v_cvt_pk_bf16_f32 v174, v37, v13\n\t"
    "v_cvt_pk_bf16_f32 v188, v38, v14\n\t"
    "v_cvt_pk_bf16_f32 v190, v39, v15\n\t"
    "v_cvt_pk_bf16_f32 v173, v16, v20\n\t"
    "v_cvt_pk_bf16_f32 v175, v17, v21\n\t"
    "v_cvt_pk_bf16_f32 v189, v18, v22\n\t"
    "v_cvt_pk_bf16_f32 v191, v19, v23\n\t"
    "buffer_store_dwordx2 v[146:147], v8, s[52:55], s61 offen offset:1152 sc1\n\t"
    "s_add_u32 s61, s61, 393216\n\t"
    "buffer_store_dwordx4 v[132:135], v7, s[52:55], s61 offen offset:0 sc1\n\t"
    "buffer_store_dwordx4 v[128:131], v7, s[52:55], s61 offen offset:384 sc1\n\t"
    "buffer_store_dwordx4 v[164:167], v7, s[52:55], s61 offen offset:768 sc1\n\t"
    "buffer_store_dwordx4 v[160:163], v7, s[52:55], s61 offen offset:1152 sc1\n\t"
    "buffer_store_dwordx2 v[140:141], v8, s[52:55], s61 offen offset:0 sc1\n\t"
    "buffer_store_dwordx2 v[142:143], v8, s[52:55], s61 offen offset:384 sc1\n\t"
    "buffer_store_dwordx2 v[120:121], v8, s[52:55], s61 offen offset:768 sc1\n\t"
    "buffer_store_dwordx2 v[122:123], v8, s[52:55], s61 offen offset:1152 sc1\n\t"
    "s_add_u32 s61, s61, 393216\n\t"
    "buffer_store_dwordx4 v[168:171], v7, s[52:55], s61 offen offset:0 sc1\n\t"
    "buffer_store_dwordx4 v[156:159], v7, s[52:55], s61 offen offset:384 sc1\n\t"
    "buffer_store_dwordx4 v[180:183], v7, s[52:55], s61 offen offset:768 sc1\n\t"
    "buffer_store_dwordx4 v[176:179], v7, s[52:55], s61 offen offset:1152 sc1\n\t"
    "buffer_store_dwordx2 v[116:117], v8, s[52:55], s61 offen offset:0 sc1\n\t"
    "buffer_store_dwordx2 v[118:119], v8, s[52:55], s61 offen offset:384 sc1\n\t"
    "buffer_store_dwordx2 v[148:149], v8, s[52:55], s61 offen offset:768 sc1\n\t"
    "buffer_store_dwordx2 v[150:151], v8, s[52:55], s61 offen offset:1152 sc1\n\t"
    "s_add_u32 s61, s61, 393216\n\t"
    "buffer_store_dwordx4 v[152:155], v7, s[52:55], s61 offen offset:0 sc1\n\t"
    "buffer_store_dwordx4 v[124:127], v7, s[52:55], s61 offen offset:384 sc1\n\t"
    "buffer_store_dwordx4 v[192:195], v7, s[52:55], s61 offen offset:768 sc1\n\t"
    "buffer_store_dwordx4 v[184:187], v7, s[52:55], s61 offen offset:1152 sc1\n\t"
    "buffer_store_dwordx2 v[172:173], v8, s[52:55], s61 offen offset:0 sc1\n\t"
    "buffer_store_dwordx2 v[174:175], v8, s[52:55], s61 offen offset:384 sc1\n\t"
    "buffer_store_dwordx2 v[188:189], v8, s[52:55], s61 offen offset:768 sc1\n\t"
    "buffer_store_dwordx2 v[190:191], v8, s[52:55], s61 offen offset:1152 sc1\n\t"
    "s_add_u32 s61, s61, 11403264\n\t"
    "s_endpgm\n\t"
    ".p2align 12\n\t"
    ".Lroln_%=:\n\t"
    "v_lshrrev_b32_e32 v20, 3, v31\n\t"
    "v_and_b32_e32 v21, 6, v20\n\t"
    "v_and_b32_e32 v28, 7, v31\n\t"
    "v_xor_b32_e32 v21, v21, v28\n\t"
    "v_lshlrev_b32_e32 v20, 7, v20\n\t"
    "v_lshl_or_b32 v7, v21, 4, v20\n\t"
    "s_mov_b32 s54, 0x6000000\n\t"
    "s_mov_b32 s55, 0x20000\n\t"
    "s_waitcnt lgkmcnt(0)\n\t"
    "s_mov_b32 s52, s36\n\t"
    "s_and_b32 s53, s37, 0xffff\n\t"
    "s_barrier\n\t"
    "s_add_u32 m0, %[rbeg], 0\n\t"
    "s_add_u32 s61, %[wb], 0\n\t"
    "buffer_load_dwordx4 v7, s[52:55], s61 offen offset:0 nt lds\n\t"
    "buffer_load_dwordx4 v7, s[52:55], s61 offen offset:1024 nt lds\n\t"
    "buffer_load_dwordx4 v7, s[52:55], s61 offen offset:2048 nt lds\n\t"
    "s_add_u32 m0, %[rbeg], 3072\n\t"
    "s_add_u32 s61, %[wb], 196608\n\t"
    "buffer_load_dwordx4 v7, s[52:55], s61 offen offset:0 nt lds\n\t"
    "buffer_load_dwordx4 v7, s[52:55], s61 offen offset:1024 nt lds\n\t"
    "buffer_load_dwordx4 v7, s[52:55], s61 offen offset:2048 nt lds\n\t"
    "v_lshlrev_b32_e32 v30, 4, v31\n\t"
    "v_and_b32_e32 v28, 7, v31\n\t"
    "v_bfe_u32 v27, v31, 3, 1\n\t"
    "v_lshrrev_b32_e32 v29, 4, v31\n\t"
    "v_add_u32_e32 v6, %[pl], v30\n\t"
    "v_mul_u32_u24_e32 v27, 0xc00, v27\n\t"
    "v_add_u32_e32 v27, %[rbeg], v27\n\t"
    "v_lshlrev_b32_e32 v26, 7, v31\n\t"
    "v_mad_u32_u24 v20, v28, 3, 0\n\t"
    "v_and_b32_e32 v21, 6, v20\n\t"
    "v_xor_b32_e32 v21, v21, v29\n\t"
    "v_lshl_add_u32 v20, v20, 7, v27\n\t"
    "v_lshl_add_u32 v0, v21, 4, v20\n\t"
    "v_xor_b32_e32 v1, 64, v0\n\t"
    "v_mad_u32_u24 v20, v28, 3, 1\n\t"
    "v_and_b32_e32 v21, 6, v20\n\t"
    "v_xor_b32_e32 v21, v21, v29\n\t"
    "v_lshl_add_u32 v20, v20, 7, v27\n\t"
    "v_lshl_add_u32 v2, v21, 4, v20\n\t"
    "v_xor_b32_e32 v3, 64, v2\n\t"
    "v_mad_u32_u24 v20, v28, 3, 2\n\t"
    "v_and_b32_e32 v21, 6, v20\n\t"
    "v_xor_b32_e32 v21, v21, v29\n\t"
    "v_lshl_add_u32 v20, v20, 7, v27\n\t"
    "v_lshl_add_u32 v4, v21, 4, v20\n\t"
    "v_xor_b32_e32 v5, 64, v4\n\t"
    "s_mov_b32 s56, s42\n\t"
    "s_and_b32 s57, s43, 0xffff\n\t"
    "s_mov_b32 s58, 0x40000\n\t"
    "s_mov_b32 s59, 0x20000\n\t"
    "v_add_u32_e32 v8, 0x9000, v30\n\t"
    "v_add_u32_e32 v9, 0x1b000, v30\n\t"
    "s_mov_b32 s68, 0x3dd53b95\n\t"
    "s_add_u32 m0, %[rbeg], 6144\n\t"
    "s_add_u32 s61, %[wb], 393216\n\t"
    "buffer_load_dwordx4 v7, s[52:55], s61 offen offset:0 nt lds\n\t"
    "buffer_load_dwordx4 v7, s[52:55], s61 offen offset:1024 nt lds\n\t"
    "buffer_load_dwordx4 v7, s[52:55], s61 offen offset:2048 nt lds\n\t"
    "s_mov_b64 exec, 0\n\t"
    "s_cmp_eq_u32 %[pair], 0\n\t"
    "s_cbranch_scc1 .Lh4_%=\n\t"
    "s_branch .Lh1_%=\n\t"
    ".LrM_%=:\n\t"
    "s_mov_b64 exec, -1\n\t"
    "s_sleep 8\n\t"
    "s_add_u32 m0, %[rbeg], 9216\n\t"
    "s_add_u32 s61, %[wb], 589824\n\t"
    "buffer_load_dwordx4 v7, s[52:55], s61 offen offset:0 nt lds\n\t"
    "buffer_load_dwordx4 v7, s[52:55], s61 offen offset:1024 nt lds\n\t"
    "buffer_load_dwordx4 v7, s[52:55], s61 offen offset:2048 nt lds\n\t"
    "s_add_u32 m0, %[rbeg], 12288\n\t"
    "s_add_u32 s61, %[wb], 786432\n\t"
    "buffer_load_dwordx4 v7, s[52:55], s61 offen offset:0 nt lds\n\t"
    "buffer_load_dwordx4 v7, s[52:55], s61 offen offset:1024 nt lds\n\t"
    "buffer_load_dwordx4 v7, s[52:55], s61 offen offset:2048 nt lds\n\t"
    "s_add_u32 m0, %[rbeg], 15360\n\t"
    "s_add_u32 s61, %[wb], 983040\n\t"
    "buffer_load_dwordx4 v7, s[52:55], s61 offen offset:0 nt lds\n\t"
    "buffer_load_dwordx4 v7, s[52:55], s61 offen offset:1024 nt lds\n\t"
    "buffer_load_dwordx4 v7, s[52:55], s61 offen offset:2048 nt lds\n\t"
    "s_add_u32 m0, %[rbeg], 18432\n\t"
    "s_add_u32 s61, %[wb], 1179648\n\t"
    "buffer_load_dwordx4 v7, s[52:55], s61 offen offset:0 nt lds\n\t"
    "buffer_load_dwordx4 v7, s[52:55], s61 offen offset:1024 nt lds\n\t"
    "buffer_load_dwordx4 v7, s[52:55], s61 offen offset:2048 nt lds\n\t"
    "s_add_u32 m0, %[rbeg], 21504\n\t"
    "s_add_u32 s61, %[wb], 1376256\n\t"
    "buffer_load_dwordx4 v7, s[52:55], s61 offen offset:0 nt lds\n\t"
    "buffer_load_dwordx4 v7, s[52:55], s61 offen offset:1024 nt lds\n\t"
    "buffer_load_dwordx4 v7, s[52:55], s61 offen offset:2048 nt lds\n\t"
    "s_waitcnt vmcnt(0)\n\t"
    "s_barrier\n\t"
    "ds_read_b128 v[196:199], v0 offset:6144\n\t"
    "ds_read_b128 v[192:195], v1 offset:6144\n\t"
    "ds_read_b128 v[188:191], v2 offset:6144\n\t"
    "ds_read_b128 v[172:175], v0 offset:12288\n\t"
    "ds_read_b128 v[168:171], v1 offset:12288\n\t"
    "ds_read_b128 v[164:167], v2 offset:12288\n\t"
    "ds_read_b128 v[148:151], v0 offset:18432\n\t"
    "ds_read_b128 v[144:147], v1 offset:18432\n\t"
    "ds_read_b128 v[140:143], v2 offset:18432\n\t"
    "ds_read_b128 v[184:187], v3 offset:6144\n\t"
    "ds_read_b128 v[180:183], v4 offset:6144\n\t"
    "ds_read_b128 v[176:179], v5 offset:6144\n\t"
    "ds_read_b128 v[208:211], v3 offset:0\n\t"
    "ds_read_b128 v[204:207], v4 offset:0\n\t"
    "ds_read_b128 v[200:203], v5 offset:0\n\t"
    "ds_read_b128 v[160:163], v3 offset:12288\n\t"
    "ds_read_b128 v[156:159], v4 offset:12288\n\t"
    "ds_read_b128 v[152:155], v5 offset:12288\n\t"
    "ds_read_b128 v[136:139], v3 offset:18432\n\t"
    "ds_read_b128 v[132:135], v4 offset:18432\n\t"
    "ds_read_b128 v[128:131], v5 offset:18432\n\t"
    "ds_read_b128 v[220:223], v0 offset:0\n\t"
    "ds_read_b128 v[216:219], v1 offset:0\n\t"
    "ds_read_b128 v[212:215], v2 offset:0\n\t"
    "ds_read_b128 v[28:31], v3 offset:0\n\t"
    "ds_read_b128 v[32:35], v4 offset:0\n\t"
    "ds_read_b128 v[36:39], v5 offset:0\n\t"
    "s_add_i32 s63, %[rbeg], 24576\n\t"
    "s_add_i32 s62, %[rbeg], 49152\n\t"
    "s_barrier\n\t"
    "ds_read_b128 a[0:3], v8 offset:0\n\t"
    "ds_read_b128 a[8:11], v8 offset:1024\n\t"
    "ds_read_b128 a[16:19], v8 offset:2048\n\t"
    "ds_read_b128 a[24:27], v8 offset:3072\n\t"
    "ds_read_b128 a[32:35], v8 offset:4096\n\t"
    "ds_read_b128 a[40:43], v8 offset:5120\n\t"
    "ds_read_b128 a[4:7], v8 offset:6144\n\t"
    "ds_read_b128 a[12:15], v8 offset:7168\n\t"
    "ds_read_b128 a[20:23], v8 offset:8192\n\t"
    "ds_read_b128 a[28:31], v8 offset:9216\n\t"
    "ds_read_b128 a[36:39], v8 offset:10240\n\t"
    "s_waitcnt lgkmcnt(10)\n\t"
    "v_mfma_f32_16x16x32_bf16 v[64:67], a[0:3], v[220:223], 0\n\t"
    "ds_read_b128 a[44:47], v8 offset:11264\n\t"
    "s_waitcnt lgkmcnt(10)\n\t"
    "v_mfma_f32_16x16x32_bf16 v[64:67], a[8:11], v[216:219], v[64:67]\n\t"
    "ds_read_b128 a[48:51], v9 offset:0\n\t"
    "s_waitcnt lgkmcnt(10)\n\t"
    "v_mfma_f32_16x16x32_bf16 v[64:67], a[16:19], v[212:215], v[64:67]\n\t"
    "ds_read_b128 a[56:59], v9 offset:1024\n\t"
    "s_waitcnt lgkmcnt(10)\n\t"
    "v_mfma_f32_16x16x32_bf16 v[64:67], a[24:27], v[28:31], v[64:67]\n\t"
    "ds_read_b128 a[64:67], v9 offset:2048\n\t"
    "s_waitcnt lgkmcnt(4)\n\t"
    "v_mfma_f32_16x16x32_bf16 v[64:67], a[32:35], v[32:35], v[64:67]\n\t"
    "ds_read_b128 a[72:75], v9 offset:3072\n\t"
    "v_mfma_f32_16x16x32_bf16 v[64:67], a[40:43], v[36:39], v[64:67]\n\t"
    "ds_read_b128 a[80:83], v9 offset:4096\n\t"
    "v_mfma_f32_16x16x32_bf16 v[40:43], a[4:7], v[220:223], 0\n\t"
    "ds_read_b128 a[88:91], v9 offset:5120\n\t"
    "v_mfma_f32_16x16x32_bf16 v[40:43], a[12:15], v[216:219], v[40:43]\n\t"
    "ds_read_b128 a[52:55], v9 offset:6144\n\t"
    "v_mfma_f32_16x16x32_bf16 v[40:43], a[20:23], v[212:215], v[40:43]\n\t"
    "ds_read_b128 a[60:63], v9 offset:7168\n\t"
    "v_mfma_f32_16x16x32_bf16 v[40:43], a[28:31], v[28:31], v[40:43]\n\t"
    "ds_read_b128 a[68:71], v9 offset:8192\n\t"
    "v_mfma_f32_16x16x32_bf16 v[40:43], a[36:39], v[32:35], v[40:43]\n\t"
    "v_cvt_pk_bf16_f32 v44, v64, v65\n\t"
    "v_cvt_pk_bf16_f32 v45, v66, v67\n\t"
    "ds_read_b128 a[76:79], v9 offset:9216\n\t"
    "s_waitcnt lgkmcnt(4)\n\t"
    "v_mfma_f32_16x16x32_bf16 v[40:43], a[44:47], v[36:39], v[40:43]\n\t"
    "ds_read_b128 a[84:87], v9 offset:10240\n\t"
    "v_mfma_f32_16x16x32_bf16 v[48:51], a[48:51], v[220:223], 0\n\t"
    "ds_read_b128 a[92:95], v9 offset:11264\n\t"
    "v_mfma_f32_16x16x32_bf16 v[48:51], a[56:59], v[216:219], v[48:51]\n\t"
    "s_barrier\n\t"
    "ds_read_b128 a[96:99], v8 offset:12288\n\t"
    "v_mfma_f32_16x16x32_bf16 v[48:51], a[64:67], v[212:215], v[48:51]\n\t"
    "ds_read_b128 a[104:107], v8 offset:13312\n\t"
    "v_mfma_f32_16x16x32_bf16 v[48:51], a[72:75], v[28:31], v[48:51]\n\t"
    "v_cvt_pk_bf16_f32 v46, v40, v41\n\t"
    "ds_read_b128 a[112:115], v8 offset:14336\n\t"
    "v_mfma_f32_16x16x32_bf16 v[48:51], a[80:83], v[32:35], v[48:51]\n\t"
    "v_cvt_pk_bf16_f32 v47, v42, v43\n\t"
    "ds_read_b128 a[120:123], v8 offset:15360\n\t"
    "v_mfma_f32_16x16x32_bf16 v[48:51], a[88:91], v[36:39], v[48:51]\n\t"
    "ds_read_b128 a[128:131], v8 offset:16384\n\t"
    "s_waitcnt lgkmcnt(4)\n\t"
    "v_mfma_f32_16x16x32_bf16 v[56:59], a[52:55], v[220:223], 0\n\t"
    "ds_read_b128 a[136:139], v8 offset:17408\n\t"
    "v_mfma_f32_16x16x32_bf16 v[56:59], a[60:63], v[216:219], v[56:59]\n\t"
    "ds_read_b128 a[100:103], v8 offset:18432\n\t"
    "v_mfma_f32_16x16x32_bf16 v[56:59], a[68:71], v[212:215], v[56:59]\n\t"
    "ds_read_b128 a[108:111], v8 offset:19456\n\t"
    "v_mfma_f32_16x16x32_bf16 v[56:59], a[76:79], v[28:31], v[56:59]\n\t"
    "v_cvt_pk_bf16_f32 v40, v48, v49\n\t"
    "ds_read_b128 a[116:119], v8 offset:20480\n\t"
    "v_mfma_f32_16x16x32_bf16 v[56:59], a[84:87], v[32:35], v[56:59]\n\t"
    "v_cvt_pk_bf16_f32 v41, v50, v51\n\t"
    "ds_read_b128 a[124:127], v8 offset:21504\n\t"
    "v_mfma_f32_16x16x32_bf16 v[56:59], a[92:95], v[36:39], v[56:59]\n\t"
    "ds_read_b128 a[132:135], v8 offset:22528\n\t"
    "v_mfma_f32_16x16x32_bf16 v[60:63], a[96:99], v[220:223], 0\n\t"
    "ds_read_b128 a[140:143], v8 offset:23552\n\t"
    "s_waitcnt lgkmcnt(4)\n\t"
    "v_mfma_f32_16x16x32_bf16 v[60:63], a[104:107], v[216:219], v[60:63]\n\t"
    "ds_read_b128 a[144:147], v9 offset:12288\n\t"
    "v_mfma_f32_16x16x32_bf16 v[60:63], a[112:115], v[212:215], v[60:63]\n\t"
    "ds_read_b128 a[152:155], v9 offset:13312\n\t"
    "v_mfma_f32_16x16x32_bf16 v[60:63], a[120:123], v[28:31], v[60:63]\n\t"
    "v_cvt_pk_bf16_f32 v42, v56, v57\n\t"
    "v_cvt_pk_bf16_f32 v43, v58, v59\n\t"
    "ds_read_b128 a[160:163], v9 offset:14336\n\t"
    "v_mfma_f32_16x16x32_bf16 v[60:63], a[128:131], v[32:35], v[60:63]\n\t"
    "ds_read_b128 a[168:171], v9 offset:15360\n\t"
    "v_mfma_f32_16x16x32_bf16 v[60:63], a[136:139], v[36:39], v[60:63]\n\t"
    "ds_read_b128 a[176:179], v9 offset:16384\n\t"
    "v_mfma_f32_16x16x32_bf16 v[48:51], a[100:103], v[220:223], 0\n\t"
    "ds_read_b128 a[184:187], v9 offset:17408\n\t"
    "v_mfma_f32_16x16x32_bf16 v[48:51], a[108:111], v[216:219], v[48:51]\n\t"
    "s_waitcnt lgkmcnt(3)\n\t"
    "v_mfma_f32_16x16x32_bf16 v[48:51], a[116:119], v[212:215], v[48:51]\n\t"
    "v_mfma_f32_16x16x32_bf16 v[48:51], a[124:127], v[28:31], v[48:51]\n\t"
    "v_cvt_pk_bf16_f32 v56, v60, v61\n\t"
    "v_cvt_pk_bf16_f32 v57, v62, v63\n\t"
    "v_mfma_f32_16x16x32_bf16 v[48:51], a[132:135], v[32:35], v[48:51]\n\t"
    "v_mfma_f32_16x16x32_bf16 v[48:51], a[140:143], v[36:39], v[48:51]\n\t"
    "v_mfma_f32_16x16x32_bf16 v[60:63], a[144:147], v[220:223], 0\n\t"
    "v_mfma_f32_16x16x32_bf16 v[60:63], a[152:155], v[216:219], v[60:63]\n\t"
    "v_mfma_f32_16x16x32_bf16 v[60:63], a[160:163], v[212:215], v[60:63]\n\t"
    "s_waitcnt lgkmcnt(0)\n\t"
    "v_mfma_f32_16x16x32_bf16 v[60:63], a[168:171], v[28:31], v[60:63]\n\t"
    "v_cvt_pk_bf16_f32 v58, v48, v49\n\t"
    "v_mfma_f32_16x16x32_bf16 v[60:63], a[176:179], v[32:35], v[60:63]\n\t"
    "v_cvt_pk_bf16_f32 v59, v50, v51\n\t"
    "v_mfma_f32_16x16x32_bf16 v[60:63], a[184:187], v[36:39], v[60:63]\n\t"
    "v_mfma_f32_16x16x32_bf16 v[64:67], v[220:223], v[44:47], 0\n\t"
    "v_mfma_f32_16x16x32_bf16 v[64:67], v[216:219], v[40:43], v[64:67]\n\t"
    "ds_read_b128 a[148:151], v9 offset:18432\n\t"
    "v_mfma_f32_16x16x32_bf16 v[64:67], v[212:215], v[56:59], v[64:67]\n\t"
    "ds_read_b128 a[156:159], v9 offset:19456\n\t"
    "v_mfma_f32_16x16x32_bf16 v[48:51], v[196:199], v[44:47], 0\n\t"
    "ds_read_b128 a[164:167], v9 offset:20480\n\t"
    "v_mfma_f32_16x16x32_bf16 v[48:51], v[192:195], v[40:43], v[48:51]\n\t"
    "ds_read_b128 a[172:175], v9 offset:21504\n\t"
    "v_mfma_f32_16x16x32_bf16 v[48:51], v[188:191], v[56:59], v[48:51]\n\t"
    "ds_read_b128 a[180:183], v9 offset:22528\n\t"
    "v_mfma_f32_16x16x32_bf16 v[68:71], v[172:175], v[44:47], 0\n\t"
    "ds_read_b128 a[188:191], v9 offset:23552\n\t"
    "v_mfma_f32_16x16x32_bf16 v[68:71], v[168:171], v[40:43], v[68:71]\n\t"
    "s_barrier\n\t"
    "ds_read_b128 a[192:195], v8 offset:24576\n\t"
    "v_mfma_f32_16x16x32_bf16 v[68:71], v[164:167], v[56:59], v[68:71]\n\t"
    "ds_read_b128 a[200:203], v8 offset:25600\n\t"
    "v_mfma_f32_16x16x32_bf16 v[72:75], v[148:151], v[44:47], 0\n\t"
    "ds_read_b128 a[208:211], v8 offset:26624\n\t"
    "v_mfma_f32_16x16x32_bf16 v[72:75], v[144:147], v[40:43], v[72:75]\n\t"
    "ds_read_b128 a[216:219], v8 offset:27648\n\t"
    "v_mfma_f32_16x16x32_bf16 v[72:75], v[140:143], v[56:59], v[72:75]\n\t"
    "ds_read_b128 a[224:227], v8 offset:28672\n\t"
    "s_waitcnt lgkmcnt(4)\n\t"
    "v_mfma_f32_16x16x32_bf16 v[44:47], a[148:151], v[220:223], 0\n\t"
    "ds_read_b128 a[232:235], v8 offset:29696\n\t"
    "v_mfma_f32_16x16x32_bf16 v[44:47], a[156:159], v[216:219], v[44:47]\n\t"
    "ds_read_b128 a[196:199], v8 offset:30720\n\t"
    "v_mfma_f32_16x16x32_bf16 v[44:47], a[164:167], v[212:215], v[44:47]\n\t"
    "ds_read_b128 a[204:207], v8 offset:31744\n\t"
    "v_mfma_f32_16x16x32_bf16 v[44:47], a[172:175], v[28:31], v[44:47]\n\t"
    "v_cvt_pk_bf16_f32 v40, v60, v61\n\t"
    "ds_read_b128 a[212:215], v8 offset:32768\n\t"
    "v_mfma_f32_16x16x32_bf16 v[44:47], a[180:183], v[32:35], v[44:47]\n\t"
    "ds_read_b128 a[220:223], v8 offset:33792\n\t"
    "v_mfma_f32_16x16x32_bf16 v[44:47], a[188:191], v[36:39], v[44:47]\n\t"
    "ds_read_b128 a[228:231], v8 offset:34816\n\t"
    "v_mfma_f32_16x16x32_bf16 v[56:59], a[192:195], v[220:223], 0\n\t"
    "ds_read_b128 a[236:239], v8 offset:35840\n\t"
    "s_waitcnt lgkmcnt(4)\n\t"
    "v_mfma_f32_16x16x32_bf16 v[56:59], a[200:203], v[216:219], v[56:59]\n\t"
    "v_cvt_pk_bf16_f32 v41, v62, v63\n\t"
    "ds_read_b128 a[240:243], v9 offset:24576\n\t"
    "v_mfma_f32_16x16x32_bf16 v[56:59], a[208:211], v[212:215], v[56:59]\n\t"
    "ds_read_b128 a[248:251], v9 offset:25600\n\t"
    "v_mfma_f32_16x16x32_bf16 v[56:59], a[216:219], v[28:31], v[56:59]\n\t"
    "v_cvt_pk_bf16_f32 v42, v44, v45\n\t"
    "ds_read_b128 v[224:227], v9 offset:26624\n\t"
    "v_mfma_f32_16x16x32_bf16 v[56:59], a[224:227], v[32:35], v[56:59]\n\t"
    "v_cvt_pk_bf16_f32 v43, v46, v47\n\t"
    "ds_read_b128 v[232:235], v9 offset:27648\n\t"
    "v_mfma_f32_16x16x32_bf16 v[56:59], a[232:235], v[36:39], v[56:59]\n\t"
    "ds_read_b128 v[240:243], v9 offset:28672\n\t"
    "v_mfma_f32_16x16x32_bf16 v[60:63], a[196:199], v[220:223], 0\n\t"
    "ds_read_b128 v[248:251], v9 offset:29696\n\t"
    "v_mfma_f32_16x16x32_bf16 v[60:63], a[204:207], v[216:219], v[60:63]\n\t"
    "ds_read_b128 a[244:247], v9 offset:30720\n\t"
    "s_waitcnt lgkmcnt(4)\n\t"
    "v_mfma_f32_16x16x32_bf16 v[60:63], a[212:215], v[212:215], v[60:63]\n\t"
    "ds_read_b128 a[252:255], v9 offset:31744\n\t"
    "v_mfma_f32_16x16x32_bf16 v[60:63], a[220:223], v[28:31], v[60:63]\n\t"
    "v_cvt_pk_bf16_f32 v44, v56, v57\n\t"
    "ds_read_b128 v[228:231], v9 offset:32768\n\t"
    "v_mfma_f32_16x16x32_bf16 v[60:63], a[228:231], v[32:35], v[60:63]\n\t"
    "v_cvt_pk_bf16_f32 v45, v58, v59\n\t"
    "ds_read_b128 v[236:239], v9 offset:33792\n\t"
    "v_mfma_f32_16x16x32_bf16 v[60:63], a[236:239], v[36:39], v[60:63]\n\t"
    "ds_read_b128 v[244:247], v9 offset:34816\n\t"
    "v_mfma_f32_16x16x32_bf16 v[80:83], a[240:243], v[220:223], 0\n\t"
    "ds_read_b128 v[252:255], v9 offset:35840\n\t"
    "v_mfma_f32_16x16x32_bf16 v[80:83], a[248:251], v[216:219], v[80:83]\n\t"
    "v_mfma_f32_16x16x32_bf16 v[80:83], v[224:227], v[212:215], v[80:83]\n\t"
    "s_waitcnt lgkmcnt(2)\n\t"
    "v_mfma_f32_16x16x32_bf16 v[80:83], v[232:235], v[28:31], v[80:83]\n\t"
    "v_cvt_pk_bf16_f32 v46, v60, v61\n\t"
    "v_cvt_pk_bf16_f32 v47, v62, v63\n\t"
    "v_mfma_f32_16x16x32_bf16 v[80:83], v[240:243], v[32:35], v[80:83]\n\t"
    "v_mfma_f32_16x16x32_bf16 v[80:83], v[248:251], v[36:39], v[80:83]\n\t"
    "v_mfma_f32_16x16x32_bf16 v[12:15], a[244:247], v[220:223], 0\n\t"
    "v_mfma_f32_16x16x32_bf16 v[12:15], a[252:255], v[216:219], v[12:15]\n\t"
    "v_mfma_f32_16x16x32_bf16 v[12:15], v[228:231], v[212:215], v[12:15]\n\t"
    "v_mfma_f32_16x16x32_bf16 v[12:15], v[236:239], v[28:31], v[12:15]\n\t"
    "v_cvt_pk_bf16_f32 v16, v80, v81\n\t"
    "v_cvt_pk_bf16_f32 v17, v82, v83\n\t"
    "s_waitcnt lgkmcnt(0)\n\t"
    "v_mfma_f32_16x16x32_bf16 v[12:15], v[244:247], v[32:35], v[12:15]\n\t"
    "v_mfma_f32_16x16x32_bf16 v[12:15], v[252:255], v[36:39], v[12:15]\n\t"
    "v_mfma_f32_16x16x32_bf16 v[20:23], a[0:3], v[196:199], 0\n\t"
    "v_mfma_f32_16x16x32_bf16 v[20:23], a[8:11], v[192:195], v[20:23]\n\t"
    "v_mfma_f32_16x16x32_bf16 v[20:23], a[16:19], v[188:191], v[20:23]\n\t"
    "v_mfma_f32_16x16x32_bf16 v[20:23], a[24:27], v[184:187], v[20:23]\n\t"
    "v_cvt_pk_bf16_f32 v18, v12, v13\n\t"
    "v_mfma_f32_16x16x32_bf16 v[20:23], a[32:35], v[180:183], v[20:23]\n\t"
    "v_mfma_f32_16x16x32_bf16 v[20:23], a[40:43], v[176:179], v[20:23]\n\t"
    "v_cvt_pk_bf16_f32 v19, v14, v15\n\t"
    "v_mfma_f32_16x16x32_bf16 v[64:67], v[208:211], v[40:43], v[64:67]\n\t"
    "v_mfma_f32_16x16x32_bf16 v[64:67], v[204:207], v[44:47], v[64:67]\n\t"
    "v_mfma_f32_16x16x32_bf16 v[64:67], v[200:203], v[16:19], v[64:67]\n\t"
    "v_mfma_f32_16x16x32_bf16 v[48:51], v[184:187], v[40:43], v[48:51]\n\t"
    "v_cvt_pk_bf16_f32 v12, v20, v21\n\t"
    "v_cvt_pk_bf16_f32 v13, v22, v23\n\t"
    "v_mfma_f32_16x16x32_bf16 v[48:51], v[180:183], v[44:47], v[48:51]\n\t"
    "v_mfma_f32_16x16x32_bf16 v[48:51], v[176:179], v[16:19], v[48:51]\n\t"
    "v_mfma_f32_16x16x32_bf16 v[68:71], v[160:163], v[40:43], v[68:71]\n\t"
    "v_max3_f32 v20, v64, v65, v66\n\t"
    "v_mfma_f32_16x16x32_bf16 v[68:71], v[156:159], v[44:47], v[68:71]\n\t"
    "v_mfma_f32_16x16x32_bf16 v[68:71], v[152:155], v[16:19], v[68:71]\n\t"
    "v_mfma_f32_16x16x32_bf16 v[72:75], v[136:139], v[40:43], v[72:75]\n\t"
    "v_max3_f32 v21, v67, v48, v49\n\t"
    "v_max3_f32 v20, v20, v50, v51\n\t"
    "v_mfma_f32_16x16x32_bf16 v[72:75], v[132:135], v[44:47], v[72:75]\n\t"
    "v_mfma_f32_16x16x32_bf16 v[72:75], v[128:131], v[16:19], v[72:75]\n\t"
    "v_mfma_f32_16x16x32_bf16 v[24:27], a[4:7], v[196:199], 0\n\t"
    "v_max3_f32 v21, v21, v68, v69\n\t"
    "v_max3_f32 v20, v20, v70, v71\n\t"
    "v_mfma_f32_16x16x32_bf16 v[24:27], a[12:15], v[192:195], v[24:27]\n\t"
    "v_mfma_f32_16x16x32_bf16 v[24:27], a[20:23], v[188:191], v[24:27]\n\t"
    "v_mfma_f32_16x16x32_bf16 v[24:27], a[28:31], v[184:187], v[24:27]\n\t"
    "v_max3_f32 v21, v21, v72, v73\n\t"
    "v_max3_f32 v20, v20, v74, v75\n\t"
    "v_mfma_f32_16x16x32_bf16 v[24:27], a[36:39], v[180:183], v[24:27]\n\t"
    "v_max_f32_e32 v20, v20, v21\n\t"
    "v_mov_b32_e32 v21, v20\n\t"
    "v_mfma_f32_16x16x32_bf16 v[24:27], a[44:47], v[176:179], v[24:27]\n\t"
    "v_mfma_f32_16x16x32_bf16 v[16:19], a[48:51], v[196:199], 0\n\t"
    "v_permlane32_swap_b32 v20, v21\n\t"
    "v_max_f32_e32 v20, v20, v21\n\t"
    "v_mfma_f32_16x16x32_bf16 v[16:19], a[56:59], v[192:195], v[16:19]\n\t"
    "v_mov_b32_e32 v21, v20\n\t"
    "v_mfma_f32_16x16x32_bf16 v[16:19], a[64:67], v[188:191], v[16:19]\n\t"
    "v_mfma_f32_16x16x32_bf16 v[16:19], a[72:75], v[184:187], v[16:19]\n\t"
    "v_cvt_pk_bf16_f32 v14, v24, v25\n\t"
    "v_cvt_pk_bf16_f32 v15, v26, v27\n\t"
    "v_mfma_f32_16x16x32_bf16 v[16:19], a[80:83], v[180:183], v[16:19]\n\t"
    "v_permlane16_swap_b32 v20, v21\n\t"
    "v_max_f32_e32 v20, v20, v21\n\t"
    "v_mfma_f32_16x16x32_bf16 v[16:19], a[88:91], v[176:179], v[16:19]\n\t"
    "v_mul_f32_e32 v20, s68, v20\n\t"
    "v_fma_f32 v64, v64, s68, -v20\n\t"
    "v_mfma_f32_16x16x32_bf16 v[24:27], a[52:55], v[196:199], 0\n\t"
    "v_exp_f32_e32 v64, v64\n\t"
    "v_mfma_f32_16x16x32_bf16 v[24:27], a[60:63], v[192:195], v[24:27]\n\t"
    "v_fma_f32 v65, v65, s68, -v20\n\t"
    "v_fma_f32 v66, v66, s68, -v20\n\t"
    "v_mfma_f32_16x16x32_bf16 v[24:27], a[68:71], v[188:191], v[24:27]\n\t"
    "v_exp_f32_e32 v65, v65\n\t"
    "v_mfma_f32_16x16x32_bf16 v[24:27], a[76:79], v[184:187], v[24:27]\n\t"
    "v_cvt_pk_bf16_f32 v28, v16, v17\n\t"
    "v_cvt_pk_bf16_f32 v29, v18, v19\n\t"
    "v_mfma_f32_16x16x32_bf16 v[24:27], a[84:87], v[180:183], v[24:27]\n\t"
    "v_add_f32_e32 v22, v64, v65\n\t"
    "v_fma_f32 v67, v67, s68, -v20\n\t"
    "v_mfma_f32_16x16x32_bf16 v[24:27], a[92:95], v[176:179], v[24:27]\n\t"
    "v_fma_f32 v48, v48, s68, -v20\n\t"
    "v_mfma_f32_16x16x32_bf16 v[16:19], a[96:99], v[196:199], 0\n\t"
    "v_fma_f32 v49, v49, s68, -v20\n\t"
    "v_mfma_f32_16x16x32_bf16 v[16:19], a[104:107], v[192:195], v[16:19]\n\t"
    "v_exp_f32_e32 v66, v66\n\t"
    "v_mfma_f32_16x16x32_bf16 v[16:19], a[112:115], v[188:191], v[16:19]\n\t"
    "v_exp_f32_e32 v67, v67\n\t"
    "v_mfma_f32_16x16x32_bf16 v[16:19], a[120:123], v[184:187], v[16:19]\n\t"
    "v_cvt_pk_bf16_f32 v30, v24, v25\n\t"
    "v_cvt_pk_bf16_f32 v31, v26, v27\n\t"
    "v_mfma_f32_16x16x32_bf16 v[16:19], a[128:131], v[180:183], v[16:19]\n\t"
    "v_add_f32_e32 v23, v66, v67\n\t"
    "v_fma_f32 v50, v50, s68, -v20\n\t"
    "v_mfma_f32_16x16x32_bf16 v[16:19], a[136:139], v[176:179], v[16:19]\n\t"
    "v_exp_f32_e32 v48, v48\n\t"
    "v_mfma_f32_16x16x32_bf16 v[24:27], a[100:103], v[196:199], 0\n\t"
    "v_add_f32_e32 v22, v22, v48\n\t"
    "v_fma_f32 v51, v51, s68, -v20\n\t"
    "v_mfma_f32_16x16x32_bf16 v[24:27], a[108:111], v[192:195], v[24:27]\n\t"
    "v_exp_f32_e32 v49, v49\n\t"
    "v_mfma_f32_16x16x32_bf16 v[24:27], a[116:119], v[188:191], v[24:27]\n\t"
    "v_add_f32_e32 v22, v22, v49\n\t"
    "v_fma_f32 v68, v68, s68, -v20\n\t"
    "s_barrier\n\t"
    "v_mfma_f32_16x16x32_bf16 v[24:27], a[124:127], v[184:187], v[24:27]\n\t"
    "v_cvt_pk_bf16_f32 v32, v16, v17\n\t"
    "v_cvt_pk_bf16_f32 v33, v18, v19\n\t"
    "v_mfma_f32_16x16x32_bf16 v[24:27], a[132:135], v[180:183], v[24:27]\n\t"
    "v_exp_f32_e32 v50, v50\n\t"
    "v_mfma_f32_16x16x32_bf16 v[24:27], a[140:143], v[176:179], v[24:27]\n\t"
    "v_add_f32_e32 v23, v23, v50\n\t"
    "v_fma_f32 v69, v69, s68, -v20\n\t"
    "v_mfma_f32_16x16x32_bf16 v[16:19], a[144:147], v[196:199], 0\n\t"
    "v_exp_f32_e32 v51, v51\n\t"
    "v_mfma_f32_16x16x32_bf16 v[16:19], a[152:155], v[192:195], v[16:19]\n\t"
    "v_add_f32_e32 v23, v23, v51\n\t"
    "v_fma_f32 v70, v70, s68, -v20\n\t"
    "v_mfma_f32_16x16x32_bf16 v[16:19], a[160:163], v[188:191], v[16:19]\n\t"
    "v_fma_f32 v71, v71, s68, -v20\n\t"
    "v_mfma_f32_16x16x32_bf16 v[16:19], a[168:171], v[184:187], v[16:19]\n\t"
    "v_cvt_pk_bf16_f32 v34, v24, v25\n\t"
    "v_mfma_f32_16x16x32_bf16 v[16:19], a[176:179], v[180:183], v[16:19]\n\t"
    "v_cvt_pk_bf16_f32 v35, v26, v27\n\t"
    "v_fma_f32 v72, v72, s68, -v20\n\t"
    "v_mfma_f32_16x16x32_bf16 v[16:19], a[184:187], v[176:179], v[16:19]\n\t"
    "v_exp_f32_e32 v68, v68\n\t"
    "v_mfma_f32_16x16x32_bf16 v[36:39], v[220:223], v[12:15], 0\n\t"
    "v_add_f32_e32 v22, v22, v68\n\t"
    "v_fma_f32 v73, v73, s68, -v20\n\t"
    "s_add_u32 m0, %[rbeg], 24576\n\t"
    "s_add_u32 s61, %[wb], 12582912\n\t"
    "v_mfma_f32_16x16x32_bf16 v[36:39], v[216:219], v[28:31], v[36:39]\n\t"
    "v_exp_f32_e32 v69, v69\n\t"
    "buffer_load_dwordx4 v7, s[52:55], s61 offen offset:0 nt lds\n\t"
    "v_mfma_f32_16x16x32_bf16 v[36:39], v[212:215], v[32:35], v[36:39]\n\t"
    "v_add_f32_e32 v22, v22, v69\n\t"
    "v_fma_f32 v74, v74, s68, -v20\n\t"
    "v_mfma_f32_16x16x32_bf16 v[24:27], v[196:199], v[12:15], 0\n\t"
    "v_exp_f32_e32 v70, v70\n\t"
    "buffer_load_dwordx4 v7, s[52:55], s61 offen offset:1024 nt lds\n\t"
    "v_mfma_f32_16x16x32_bf16 v[24:27], v[192:195], v[28:31], v[24:27]\n\t"
    "v_add_f32_e32 v23, v23, v70\n\t"
    "v_fma_f32 v75, v75, s68, -v20\n\t"
    "v_mfma_f32_16x16x32_bf16 v[24:27], v[188:191], v[32:35], v[24:27]\n\t"
    "v_exp_f32_e32 v71, v71\n\t"
    "buffer_load_dwordx4 v7, s[52:55], s61 offen offset:2048 nt lds\n\t"
    "v_mfma_f32_16x16x32_bf16 v[40:43], v[172:175], v[12:15], 0\n\t"
    "v_add_f32_e32 v23, v23, v71\n\t"
    "v_cvt_pk_bf16_f32 v44, v16, v17\n\t"
    "v_mfma_f32_16x16x32_bf16 v[40:43], v[168:171], v[28:31], v[40:43]\n\t"
    "v_exp_f32_e32 v72, v72\n\t"
    "s_add_u32 m0, %[rbeg], 27648\n\t"
    "s_add_u32 s61, %[wb], 12779520\n\t"
    "v_mfma_f32_16x16x32_bf16 v[40:43], v[164:167], v[32:35], v[40:43]\n\t"
    "v_add_f32_e32 v22, v22, v72\n\t"
    "v_cvt_pk_bf16_f32 v45, v18, v19\n\t"
    "buffer_load_dwordx4 v7, s[52:55], s61 offen offset:0 nt lds\n\t"
    "v_mfma_f32_16x16x32_bf16 v[52:55], v[148:151], v[12:15], 0\n\t"
    "v_exp_f32_e32 v73, v73\n\t"
    "v_mfma_f32_16x16x32_bf16 v[52:55], v[144:147], v[28:31], v[52:55]\n\t"
    "v_add_f32_e32 v22, v22, v73\n\t"
    "buffer_load_dwordx4 v7, s[52:55], s61 offen offset:1024 nt lds\n\t"
    "v_mfma_f32_16x16x32_bf16 v[52:55], v[140:143], v[32:35], v[52:55]\n\t"
    "v_exp_f32_e32 v74, v74\n\t"
    "v_mfma_f32_16x16x32_bf16 v[12:15], a[148:151], v[196:199], 0\n\t"
    "v_add_f32_e32 v23, v23, v74\n\t"
    "buffer_load_dwordx4 v7, s[52:55], s61 offen offset:2048 nt lds\n\t"
    "v_mfma_f32_16x16x32_bf16 v[12:15], a[156:159], v[192:195], v[12:15]\n\t"
    "v_mfma_f32_16x16x32_bf16 v[12:15], a[164:167], v[188:191], v[12:15]\n\t"
    "v_exp_f32_e32 v75, v75\n\t"
    "s_add_u32 m0, %[rbeg], 30720\n\t"
    "s_add_u32 s61, %[wb], 12976128\n\t"
    "v_mfma_f32_16x16x32_bf16 v[12:15], a[172:175], v[184:187], v[12:15]\n\t"
    "v_add_f32_e32 v23, v23, v75\n\t"
    "v_add_f32_e32 v22, v22, v23\n\t"
    "v_mfma_f32_16x16x32_bf16 v[12:15], a[180:183], v[180:183], v[12:15]\n\t"
    "v_mov_b32_e32 v23, v22\n\t"
    "buffer_load_dwordx4 v7, s[52:55], s61 offen offset:0 nt lds\n\t"
    "v_mfma_f32_16x16x32_bf16 v[12:15], a[188:191], v[176:179], v[12:15]\n\t"
    "buffer_load_dwordx4 v7, s[52:55], s61 offen offset:1024 nt lds\n\t"
    "v_mfma_f32_16x16x32_bf16 v[16:19], a[192:195], v[196:199], 0\n\t"
    "v_permlane32_swap_b32 v22, v23\n\t"
    "v_add_f32_e32 v22, v22, v23\n\t"
    "v_mfma_f32_16x16x32_bf16 v[16:19], a[200:203], v[192:195], v[16:19]\n\t"
    "v_mov_b32_e32 v23, v22\n\t"
    "buffer_load_dwordx4 v7, s[52:55], s61 offen offset:2048 nt lds\n\t"
    "v_mfma_f32_16x16x32_bf16 v[16:19], a[208:211], v[188:191], v[16:19]\n\t"
    "v_mfma_f32_16x16x32_bf16 v[16:19], a[216:219], v[184:187], v[16:19]\n\t"
    "v_cvt_pk_bf16_f32 v46, v12, v13\n\t"
    "v_cvt_pk_bf16_f32 v47, v14, v15\n\t"
    "s_add_u32 m0, %[rbeg], 33792\n\t"
    "s_add_u32 s61, %[wb], 13172736\n\t"
    "v_mfma_f32_16x16x32_bf16 v[16:19], a[224:227], v[180:183], v[16:19]\n\t"
    "v_permlane16_swap_b32 v22, v23\n\t"
    "v_add_f32_e32 v22, v22, v23\n\t"
    "v_mfma_f32_16x16x32_bf16 v[16:19], a[232:235], v[176:179], v[16:19]\n\t"
    "v_rcp_f32_e32 v22, v22\n\t"
    "buffer_load_dwordx4 v7, s[52:55], s61 offen offset:0 nt lds\n\t"
    "v_mfma_f32_16x16x32_bf16 v[12:15], a[196:199], v[196:199], 0\n\t"
    "v_mul_f32_e32 v64, v64, v22\n\t"
    "v_mul_f32_e32 v65, v65, v22\n\t"
    "v_mfma_f32_16x16x32_bf16 v[12:15], a[204:207], v[192:195], v[12:15]\n\t"
    "v_cvt_pk_bf16_f32 v28, v64, v65\n\t"
    "v_mul_f32_e32 v66, v66, v22\n\t"
    "buffer_load_dwordx4 v7, s[52:55], s61 offen offset:1024 nt lds\n\t"
    "v_mfma_f32_16x16x32_bf16 v[12:15], a[212:215], v[188:191], v[12:15]\n\t"
    "v_mul_f32_e32 v67, v67, v22\n\t"
    "v_cvt_pk_bf16_f32 v29, v66, v67\n\t"
    "buffer_load_dwordx4 v7, s[52:55], s61 offen offset:2048 nt lds\n\t"
    "v_mfma_f32_16x16x32_bf16 v[12:15], a[220:223], v[184:187], v[12:15]\n\t"
    "v_cvt_pk_bf16_f32 v32, v16, v17\n\t"
    "v_cvt_pk_bf16_f32 v33, v18, v19\n\t"
    "v_mfma_f32_16x16x32_bf16 v[12:15], a[228:231], v[180:183], v[12:15]\n\t"
    "v_mul_f32_e32 v48, v48, v22\n\t"
    "v_mul_f32_e32 v49, v49, v22\n\t"
    "s_add_u32 m0, %[rbeg], 36864\n\t"
    "s_add_u32 s61, %[wb], 13369344\n\t"
    "v_mfma_f32_16x16x32_bf16 v[12:15], a[236:239], v[176:179], v[12:15]\n\t"
    "v_cvt_pk_bf16_f32 v30, v48, v49\n\t"
    "v_mfma_f32_16x16x32_bf16 v[16:19], a[240:243], v[196:199], 0\n\t"
    "v_mul_f32_e32 v50, v50, v22\n\t"
    "buffer_load_dwordx4 v7, s[52:55], s61 offen offset:0 nt lds\n\t"
    "v_mfma_f32_16x16x32_bf16 v[16:19], a[248:251], v[192:195], v[16:19]\n\t"
    "v_mul_f32_e32 v51, v51, v22\n\t"
    "v_cvt_pk_bf16_f32 v31, v50, v51\n\t"
    "v_mfma_f32_16x16x32_bf16 v[16:19], v[224:227], v[188:191], v[16:19]\n\t"
    "v_mul_f32_e32 v68, v68, v22\n\t"
    "v_mul_f32_e32 v69, v69, v22\n\t"
    "buffer_load_dwordx4 v7, s[52:55], s61 offen offset:1024 nt lds\n\t"
    "v_mfma_f32_16x16x32_bf16 v[16:19], v[232:235], v[184:187], v[16:19]\n\t"
    "v_cvt_pk_bf16_f32 v34, v12, v13\n\t"
    "v_cvt_pk_bf16_f32 v35, v14, v15\n\t"
    "v_mfma_f32_16x16x32_bf16 v[16:19], v[240:243], v[180:183], v[16:19]\n\t"
    "v_cvt_pk_bf16_f32 v48, v68, v69\n\t"
    "v_mul_f32_e32 v70, v70, v22\n\t"
    "buffer_load_dwordx4 v7, s[52:55], s61 offen offset:2048 nt lds\n\t"
    "v_mfma_f32_16x16x32_bf16 v[16:19], v[248:251], v[176:179], v[16:19]\n\t"
    "v_mul_f32_e32 v71, v71, v22\n\t"
    "v_cvt_pk_bf16_f32 v49, v70, v71\n\t"
    "s_add_u32 m0, %[rbeg], 39936\n\t"
    "s_add_u32 s61, %[wb], 13565952\n\t"
    "v_mfma_f32_16x16x32_bf16 v[12:15], a[244:247], v[196:199], 0\n\t"
    "v_mul_f32_e32 v72, v72, v22\n\t"
    "v_mul_f32_e32 v73, v73, v22\n\t"
    "v_mfma_f32_16x16x32_bf16 v[12:15], a[252:255], v[192:195], v[12:15]\n\t"
    "v_cvt_pk_bf16_f32 v50, v72, v73\n\t"
    "v_mul_f32_e32 v74, v74, v22\n\t"
    "buffer_load_dwordx4 v7, s[52:55], s61 offen offset:0 nt lds\n\t"
    "v_mfma_f32_16x16x32_bf16 v[12:15], v[228:231], v[188:191], v[12:15]\n\t"
    "v_mul_f32_e32 v75, v75, v22\n\t"
    "v_cvt_pk_bf16_f32 v51, v74, v75\n\t"
    "v_mfma_f32_16x16x32_bf16 v[12:15], v[236:239], v[184:187], v[12:15]\n\t"
    "v_cvt_pk_bf16_f32 v56, v16, v17\n\t"
    "v_cvt_pk_bf16_f32 v57, v18, v19\n\t"
    "buffer_load_dwordx4 v7, s[52:55], s61 offen offset:1024 nt lds\n\t"
    "v_mfma_f32_16x16x32_bf16 v[12:15], v[244:247], v[180:183], v[12:15]\n\t"
    "v_mfma_f32_16x16x32_bf16 v[12:15], v[252:255], v[176:179], v[12:15]\n\t"
    "buffer_load_dwordx4 v7, s[52:55], s61 offen offset:2048 nt lds\n\t"
    "v_mfma_f32_16x16x32_bf16 v[16:19], a[0:3], v[172:175], 0\n\t"
    "ds_write_b128 v6, v[28:31] offset:0\n\t"
    "v_mfma_f32_16x16x32_bf16 v[16:19], a[8:11], v[168:171], v[16:19]\n\t"
    "s_mov_b32 s60, 8\n\t"
    "s_add_u32 s61, %[wb], 13762560\n\t"
    ".Lmid_%=:\n\t"
    "v_mfma_f32_16x16x32_bf16 v[16:19], a[16:19], v[164:167], v[16:19]\n\t"
    "s_add_u32 m0, s63, 18432\n\t"
    "v_mfma_f32_16x16x32_bf16 v[16:19], a[24:27], v[160:163], v[16:19]\n\t"
    ".Lh4_%=:\n\t"
    "s_cbranch_execz .Lh5_%=\n\t"
    "buffer_load_dwordx4 v7, s[52:55], s61 offen offset:0 nt lds\n\t"
    "v_cvt_pk_bf16_f32 v58, v12, v13\n\t"
    "v_mfma_f32_16x16x32_bf16 v[16:19], a[32:35], v[156:159], v[16:19]\n\t"
    "ds_write_b128 v6, v[48:51] offset:1024\n\t"
    "v_mfma_f32_16x16x32_bf16 v[16:19], a[40:43], v[152:155], v[16:19]\n\t"
    "v_cvt_pk_bf16_f32 v59, v14, v15\n\t"
    "v_mfma_f32_16x16x32_bf16 v[36:39], v[208:211], v[44:47], v[36:39]\n\t"
    "v_mfma_f32_16x16x32_bf16 v[36:39], v[204:207], v[32:35], v[36:39]\n\t"
    "v_mfma_f32_16x16x32_bf16 v[36:39], v[200:203], v[56:59], v[36:39]\n\t"
    "s_add_i32 s64, s62, 24576\n\t"
    "v_mfma_f32_16x16x32_bf16 v[24:27], v[184:187], v[44:47], v[24:27]\n\t"
    "s_cmp_eq_u32 s64, %[rend]\n\t"
    "s_cselect_b32 s64, %[rbeg], s64\n\t"
    "v_mfma_f32_16x16x32_bf16 v[24:27], v[180:183], v[32:35], v[24:27]\n\t"
    "s_sub_i32 s65, s63, s64\n\t"
    "v_cvt_pk_bf16_f32 v12, v16, v17\n\t"
    "v_mfma_f32_16x16x32_bf16 v[24:27], v[176:179], v[56:59], v[24:27]\n\t"
    "v_add_u32_e32 v0, s65, v0\n\t"
    "v_add_u32_e32 v1, s65, v1\n\t"
    "v_mfma_f32_16x16x32_bf16 v[40:43], v[160:163], v[44:47], v[40:43]\n\t"
    "v_add_u32_e32 v2, s65, v2\n\t"
    "v_add_u32_e32 v3, s65, v3\n\t"
    "v_mfma_f32_16x16x32_bf16 v[40:43], v[156:159], v[32:35], v[40:43]\n\t"
    "v_add_u32_e32 v4, s65, v4\n\t"
    "v_add_u32_e32 v5, s65, v5\n\t"
    "v_mfma_f32_16x16x32_bf16 v[40:43], v[152:155], v[56:59], v[40:43]\n\t"
    "v_max3_f32 v20, v36, v37, v38\n\t"
    "v_cvt_pk_bf16_f32 v13, v18, v19\n\t"
    "v_mfma_f32_16x16x32_bf16 v[52:55], v[136:139], v[44:47], v[52:55]\n\t"
    "v_max3_f32 v21, v39, v24, v25\n\t"
    "v_max3_f32 v20, v20, v26, v27\n\t"
    "v_mfma_f32_16x16x32_bf16 v[52:55], v[132:135], v[32:35], v[52:55]\n\t"
    "v_mfma_f32_16x16x32_bf16 v[52:55], v[128:131], v[56:59], v[52:55]\n\t"
    "v_mfma_f32_16x16x32_bf16 v[16:19], a[4:7], v[172:175], 0\n\t"
    "v_max3_f32 v21, v21, v40, v41\n\t"
    "v_max3_f32 v20, v20, v42, v43\n\t"
    "v_mfma_f32_16x16x32_bf16 v[16:19], a[12:15], v[168:171], v[16:19]\n\t"
    "buffer_load_dwordx4 v7, s[52:55], s61 offen offset:1024 nt lds\n\t"
    "v_mfma_f32_16x16x32_bf16 v[16:19], a[20:23], v[164:167], v[16:19]\n\t"
    "v_mfma_f32_16x16x32_bf16 v[16:19], a[28:31], v[160:163], v[16:19]\n\t"
    "v_max3_f32 v21, v21, v52, v53\n\t"
    "v_max3_f32 v20, v20, v54, v55\n\t"
    "v_mfma_f32_16x16x32_bf16 v[16:19], a[36:39], v[156:159], v[16:19]\n\t"
    "v_max_f32_e32 v20, v20, v21\n\t"
    "v_mov_b32_e32 v21, v20\n\t"
    "v_mfma_f32_16x16x32_bf16 v[16:19], a[44:47], v[152:155], v[16:19]\n\t"
    "v_mfma_f32_16x16x32_bf16 v[28:31], a[48:51], v[172:175], 0\n\t"
    "v_permlane32_swap_b32 v20, v21\n\t"
    "v_max_f32_e32 v20, v20, v21\n\t"
    "v_mfma_f32_16x16x32_bf16 v[28:31], a[56:59], v[168:171], v[28:31]\n\t"
    "v_mov_b32_e32 v21, v20\n\t"
    "v_mfma_f32_16x16x32_bf16 v[28:31], a[64:67], v[164:167], v[28:31]\n\t"
    "v_mfma_f32_16x16x32_bf16 v[28:31], a[72:75], v[160:163], v[28:31]\n\t"
    "v_cvt_pk_bf16_f32 v14, v16, v17\n\t"
    "v_cvt_pk_bf16_f32 v15, v18, v19\n\t"
    "v_mfma_f32_16x16x32_bf16 v[28:31], a[80:83], v[156:159], v[28:31]\n\t"
    "v_permlane16_swap_b32 v20, v21\n\t"
    "v_max_f32_e32 v20, v20, v21\n\t"
    "v_mfma_f32_16x16x32_bf16 v[28:31], a[88:91], v[152:155], v[28:31]\n\t"
    "v_mul_f32_e32 v20, s68, v20\n\t"
    "v_fma_f32 v36, v36, s68, -v20\n\t"
    "v_mfma_f32_16x16x32_bf16 v[16:19], a[52:55], v[172:175], 0\n\t"
    "v_exp_f32_e32 v36, v36\n\t"
    "v_mfma_f32_16x16x32_bf16 v[16:19], a[60:63], v[168:171], v[16:19]\n\t"
    "v_fma_f32 v37, v37, s68, -v20\n\t"
    "v_fma_f32 v38, v38, s68, -v20\n\t"
    "v_mfma_f32_16x16x32_bf16 v[16:19], a[68:71], v[164:167], v[16:19]\n\t"
    "v_exp_f32_e32 v37, v37\n\t"
    "v_mfma_f32_16x16x32_bf16 v[16:19], a[76:79], v[160:163], v[16:19]\n\t"
    "v_cvt_pk_bf16_f32 v32, v28, v29\n\t"
    "v_cvt_pk_bf16_f32 v33, v30, v31\n\t"
    "v_mfma_f32_16x16x32_bf16 v[16:19], a[84:87], v[156:159], v[16:19]\n\t"
    "v_add_f32_e32 v22, v36, v37\n\t"
    "v_fma_f32 v39, v39, s68, -v20\n\t"
    "v_mfma_f32_16x16x32_bf16 v[16:19], a[92:95], v[152:155], v[16:19]\n\t"
    "buffer_load_dwordx4 v7, s[52:55], s61 offen offset:2048 nt lds\n\t"
    "v_fma_f32 v24, v24, s68, -v20\n\t"
    "v_mfma_f32_16x16x32_bf16 v[28:31], a[96:99], v[172:175], 0\n\t"
    "s_add_u32 s61, s61, 196608\n\t"
    "v_fma_f32 v25, v25, s68, -v20\n\t"
    "v_mfma_f32_16x16x32_bf16 v[28:31], a[104:107], v[168:171], v[28:31]\n\t"
    "v_exp_f32_e32 v38, v38\n\t"
    "v_mfma_f32_16x16x32_bf16 v[28:31], a[112:115], v[164:167], v[28:31]\n\t"
    "v_exp_f32_e32 v39, v39\n\t"
    "v_mfma_f32_16x16x32_bf16 v[28:31], a[120:123], v[160:163], v[28:31]\n\t"
    "v_cvt_pk_bf16_f32 v34, v16, v17\n\t"
    "v_cvt_pk_bf16_f32 v35, v18, v19\n\t"
    "v_mfma_f32_16x16x32_bf16 v[28:31], a[128:131], v[156:159], v[28:31]\n\t"
    "v_add_f32_e32 v23, v38, v39\n\t"
    "v_fma_f32 v26, v26, s68, -v20\n\t"
    "v_mfma_f32_16x16x32_bf16 v[28:31], a[136:139], v[152:155], v[28:31]\n\t"
    "v_exp_f32_e32 v24, v24\n\t"
    "v_mfma_f32_16x16x32_bf16 v[16:19], a[100:103], v[172:175], 0\n\t"
    "v_add_f32_e32 v22, v22, v24\n\t"
    "v_fma_f32 v27, v27, s68, -v20\n\t"
    "v_mfma_f32_16x16x32_bf16 v[16:19], a[108:111], v[168:171], v[16:19]\n\t"
    "v_exp_f32_e32 v25, v25\n\t"
    "v_mfma_f32_16x16x32_bf16 v[16:19], a[116:119], v[164:167], v[16:19]\n\t"
    "v_add_f32_e32 v22, v22, v25\n\t"
    "v_fma_f32 v40, v40, s68, -v20\n\t"
    "v_mfma_f32_16x16x32_bf16 v[16:19], a[124:127], v[160:163], v[16:19]\n\t"
    "v_cvt_pk_bf16_f32 v44, v28, v29\n\t"
    "v_cvt_pk_bf16_f32 v45, v30, v31\n\t"
    "v_mfma_f32_16x16x32_bf16 v[16:19], a[132:135], v[156:159], v[16:19]\n\t"
    "v_exp_f32_e32 v26, v26\n\t"
    "v_mfma_f32_16x16x32_bf16 v[16:19], a[140:143], v[152:155], v[16:19]\n\t"
    "v_add_f32_e32 v23, v23, v26\n\t"
    "v_fma_f32 v41, v41, s68, -v20\n\t"
    "v_mfma_f32_16x16x32_bf16 v[28:31], a[144:147], v[172:175], 0\n\t"
    "v_exp_f32_e32 v27, v27\n\t"
    "v_mfma_f32_16x16x32_bf16 v[28:31], a[152:155], v[168:171], v[28:31]\n\t"
    "v_add_f32_e32 v23, v23, v27\n\t"
    "v_fma_f32 v42, v42, s68, -v20\n\t"
    "v_mfma_f32_16x16x32_bf16 v[28:31], a[160:163], v[164:167], v[28:31]\n\t"
    "s_add_u32 m0, s63, 21504\n\t"
    "v_fma_f32 v43, v43, s68, -v20\n\t"
    "v_mfma_f32_16x16x32_bf16 v[28:31], a[168:171], v[160:163], v[28:31]\n\t"
    "buffer_load_dwordx4 v7, s[52:55], s61 offen offset:0 nt lds\n\t"
    "v_cvt_pk_bf16_f32 v46, v16, v17\n\t"
    "v_mfma_f32_16x16x32_bf16 v[28:31], a[176:179], v[156:159], v[28:31]\n\t"
    "v_cvt_pk_bf16_f32 v47, v18, v19\n\t"
    "v_fma_f32 v52, v52, s68, -v20\n\t"
    "v_mfma_f32_16x16x32_bf16 v[28:31], a[184:187], v[152:155], v[28:31]\n\t"
    "v_exp_f32_e32 v40, v40\n\t"
    "v_mfma_f32_16x16x32_bf16 v[56:59], v[220:223], v[12:15], 0\n\t"
    "v_add_f32_e32 v22, v22, v40\n\t"
    "v_fma_f32 v53, v53, s68, -v20\n\t"
    "v_mfma_f32_16x16x32_bf16 v[56:59], v[216:219], v[32:35], v[56:59]\n\t"
    "v_exp_f32_e32 v41, v41\n\t"
    "v_mfma_f32_16x16x32_bf16 v[56:59], v[212:215], v[44:47], v[56:59]\n\t"
    "v_add_f32_e32 v22, v22, v41\n\t"
    "v_fma_f32 v54, v54, s68, -v20\n\t"
    "v_mfma_f32_16x16x32_bf16 v[60:63], v[196:199], v[12:15], 0\n\t"
    "v_exp_f32_e32 v42, v42\n\t"
    "v_mfma_f32_16x16x32_bf16 v[60:63], v[192:195], v[32:35], v[60:63]\n\t"
    "v_add_f32_e32 v23, v23, v42\n\t"
    "v_fma_f32 v55, v55, s68, -v20\n\t"
    "v_mfma_f32_16x16x32_bf16 v[60:63], v[188:191], v[44:47], v[60:63]\n\t"
    "v_exp_f32_e32 v43, v43\n\t"
    "v_mfma_f32_16x16x32_bf16 v[64:67], v[172:175], v[12:15], 0\n\t"
    "v_add_f32_e32 v23, v23, v43\n\t"
    "v_cvt_pk_bf16_f32 v16, v28, v29\n\t"
    "v_mfma_f32_16x16x32_bf16 v[64:67], v[168:171], v[32:35], v[64:67]\n\t"
    "v_exp_f32_e32 v52, v52\n\t"
    "v_mfma_f32_16x16x32_bf16 v[64:67], v[164:167], v[44:47], v[64:67]\n\t"
    "v_add_f32_e32 v22, v22, v52\n\t"
    "v_cvt_pk_bf16_f32 v17, v30, v31\n\t"
    "v_mfma_f32_16x16x32_bf16 v[68:71], v[148:151], v[12:15], 0\n\t"
    "v_exp_f32_e32 v53, v53\n\t"
    "v_mfma_f32_16x16x32_bf16 v[68:71], v[144:147], v[32:35], v[68:71]\n\t"
    "v_add_f32_e32 v22, v22, v53\n\t"
    "v_mfma_f32_16x16x32_bf16 v[68:71], v[140:143], v[44:47], v[68:71]\n\t"
    "v_exp_f32_e32 v54, v54\n\t"
    "v_mfma_f32_16x16x32_bf16 v[12:15], a[148:151], v[172:175], 0\n\t"
    "v_add_f32_e32 v23, v23, v54\n\t"
    "v_mfma_f32_16x16x32_bf16 v[12:15], a[156:159], v[168:171], v[12:15]\n\t"
    "buffer_load_dwordx4 v7, s[52:55], s61 offen offset:1024 nt lds\n\t"
    "v_mfma_f32_16x16x32_bf16 v[12:15], a[164:167], v[164:167], v[12:15]\n\t"
    "v_exp_f32_e32 v55, v55\n\t"
    "v_mfma_f32_16x16x32_bf16 v[12:15], a[172:175], v[160:163], v[12:15]\n\t"
    "v_add_f32_e32 v23, v23, v55\n\t"
    "v_add_f32_e32 v22, v22, v23\n\t"
    "v_mfma_f32_16x16x32_bf16 v[12:15], a[180:183], v[156:159], v[12:15]\n\t"
    "v_mov_b32_e32 v23, v22\n\t"
    "v_mfma_f32_16x16x32_bf16 v[12:15], a[188:191], v[152:155], v[12:15]\n\t"
    "v_mfma_f32_16x16x32_bf16 v[28:31], a[192:195], v[172:175], 0\n\t"
    "v_permlane32_swap_b32 v22, v23\n\t"
    "v_add_f32_e32 v22, v22, v23\n\t"
    "v_mfma_f32_16x16x32_bf16 v[28:31], a[200:203], v[168:171], v[28:31]\n\t"
    "v_mov_b32_e32 v23, v22\n\t"
    "v_mfma_f32_16x16x32_bf16 v[28:31], a[208:211], v[164:167], v[28:31]\n\t"
    "v_mfma_f32_16x16x32_bf16 v[28:31], a[216:219], v[160:163], v[28:31]\n\t"
    "v_cvt_pk_bf16_f32 v18, v12, v13\n\t"
    "v_cvt_pk_bf16_f32 v19, v14, v15\n\t"
    "v_mfma_f32_16x16x32_bf16 v[28:31], a[224:227], v[156:159], v[28:31]\n\t"
    "v_permlane16_swap_b32 v22, v23\n\t"
    "v_add_f32_e32 v22, v22, v23\n\t"
    "v_mfma_f32_16x16x32_bf16 v[28:31], a[232:235], v[152:155], v[28:31]\n\t"
    "v_rcp_f32_e32 v22, v22\n\t"
    "v_mfma_f32_16x16x32_bf16 v[12:15], a[196:199], v[172:175], 0\n\t"
    "v_mul_f32_e32 v36, v36, v22\n\t"
    "v_mul_f32_e32 v37, v37, v22\n\t"
    "v_mfma_f32_16x16x32_bf16 v[12:15], a[204:207], v[168:171], v[12:15]\n\t"
    "v_cvt_pk_bf16_f32 v32, v36, v37\n\t"
    "v_mul_f32_e32 v38, v38, v22\n\t"
    "v_mfma_f32_16x16x32_bf16 v[12:15], a[212:215], v[164:167], v[12:15]\n\t"
    "v_mul_f32_e32 v39, v39, v22\n\t"
    "v_cvt_pk_bf16_f32 v33, v38, v39\n\t"
    "v_mfma_f32_16x16x32_bf16 v[12:15], a[220:223], v[160:163], v[12:15]\n\t"
    "v_cvt_pk_bf16_f32 v44, v28, v29\n\t"
    "v_cvt_pk_bf16_f32 v45, v30, v31\n\t"
    "v_mfma_f32_16x16x32_bf16 v[12:15], a[228:231], v[156:159], v[12:15]\n\t"
    "v_mul_f32_e32 v24, v24, v22\n\t"
    "v_mul_f32_e32 v25, v25, v22\n\t"
    "v_mfma_f32_16x16x32_bf16 v[12:15], a[236:239], v[152:155], v[12:15]\n\t"
    "buffer_load_dwordx4 v7, s[52:55], s61 offen offset:2048 nt lds\n\t"
    "v_cvt_pk_bf16_f32 v34, v24, v25\n\t"
    "v_mfma_f32_16x16x32_bf16 v[28:31], a[240:243], v[172:175], 0\n\t"
    "s_add_u32 s61, s61, 11206656\n\t"
    "v_mul_f32_e32 v26, v26, v22\n\t"
    "v_mfma_f32_16x16x32_bf16 v[28:31], a[248:251], v[168:171], v[28:31]\n\t"
    "v_mul_f32_e32 v27, v27, v22\n\t"
    "v_cvt_pk_bf16_f32 v35, v26, v27\n\t"
    "v_mfma_f32_16x16x32_bf16 v[28:31], v[224:227], v[164:167], v[28:31]\n\t"
    "v_mul_f32_e32 v40, v40, v22\n\t"
    "v_mul_f32_e32 v41, v41, v22\n\t"
    "v_mfma_f32_16x16x32_bf16 v[28:31], v[232:235], v[160:163], v[28:31]\n\t"
    "v_cvt_pk_bf16_f32 v46, v12, v13\n\t"
    "v_cvt_pk_bf16_f32 v47, v14, v15\n\t"
    "v_mfma_f32_16x16x32_bf16 v[28:31], v[240:243], v[156:159], v[28:31]\n\t"
    "v_cvt_pk_bf16_f32 v24, v40, v41\n\t"
    "v_mul_f32_e32 v42, v42, v22\n\t"
    "v_mfma_f32_16x16x32_bf16 v[28:31], v[248:251], v[152:155], v[28:31]\n\t"
    "v_mul_f32_e32 v43, v43, v22\n\t"
    "v_cvt_pk_bf16_f32 v25, v42, v43\n\t"
    "v_mfma_f32_16x16x32_bf16 v[12:15], a[244:247], v[172:175], 0\n\t"
    "v_mul_f32_e32 v52, v52, v22\n\t"
    "v_mul_f32_e32 v53, v53, v22\n\t"
    "v_mfma_f32_16x16x32_bf16 v[12:15], a[252:255], v[168:171], v[12:15]\n\t"
    "v_cvt_pk_bf16_f32 v26, v52, v53\n\t"
    "v_mul_f32_e32 v54, v54, v22\n\t"
    "v_mfma_f32_16x16x32_bf16 v[12:15], v[228:231], v[164:167], v[12:15]\n\t"
    "v_mul_f32_e32 v55, v55, v22\n\t"
    "v_cvt_pk_bf16_f32 v27, v54, v55\n\t"
    "v_mfma_f32_16x16x32_bf16 v[12:15], v[236:239], v[160:163], v[12:15]\n\t"
    "v_cvt_pk_bf16_f32 v36, v28, v29\n\t"
    "v_cvt_pk_bf16_f32 v37, v30, v31\n\t"
    "v_mfma_f32_16x16x32_bf16 v[12:15], v[244:247], v[156:159], v[12:15]\n\t"
    "v_mfma_f32_16x16x32_bf16 v[12:15], v[252:255], v[152:155], v[12:15]\n\t"
    "v_mfma_f32_16x16x32_bf16 v[20:23], a[0:3], v[148:151], 0\n\t"
    "ds_write_b128 v6, v[32:35] offset:2048\n\t"
    "v_mfma_f32_16x16x32_bf16 v[20:23], a[8:11], v[144:147], v[20:23]\n\t"
    "v_mfma_f32_16x16x32_bf16 v[20:23], a[16:19], v[140:143], v[20:23]\n\t"
    "s_add_u32 m0, s62, 0\n\t"
    "v_mfma_f32_16x16x32_bf16 v[20:23], a[24:27], v[136:139], v[20:23]\n\t"
    "buffer_load_dwordx4 v7, s[52:55], s61 offen offset:0 nt lds\n\t"
    "v_cvt_pk_bf16_f32 v38, v12, v13\n\t"
    "v_mfma_f32_16x16x32_bf16 v[20:23], a[32:35], v[132:135], v[20:23]\n\t"
    "ds_write_b128 v6, v[24:27] offset:3072\n\t"
    "v_mfma_f32_16x16x32_bf16 v[20:23], a[40:43], v[128:131], v[20:23]\n\t"
    "v_cvt_pk_bf16_f32 v39, v14, v15\n\t"
    "v_mfma_f32_16x16x32_bf16 v[56:59], v[208:211], v[16:19], v[56:59]\n\t"
    "v_mfma_f32_16x16x32_bf16 v[56:59], v[204:207], v[44:47], v[56:59]\n\t"
    "v_mfma_f32_16x16x32_bf16 v[56:59], v[200:203], v[36:39], v[56:59]\n\t"
    "v_mfma_f32_16x16x32_bf16 v[60:63], v[184:187], v[16:19], v[60:63]\n\t"
    "v_cvt_pk_bf16_f32 v32, v20, v21\n\t"
    "v_cvt_pk_bf16_f32 v33, v22, v23\n\t"
    "v_mfma_f32_16x16x32_bf16 v[60:63], v[180:183], v[44:47], v[60:63]\n\t"
    "v_mfma_f32_16x16x32_bf16 v[60:63], v[176:179], v[36:39], v[60:63]\n\t"
    "v_mfma_f32_16x16x32_bf16 v[64:67], v[160:163], v[16:19], v[64:67]\n\t"
    "v_max3_f32 v72, v56, v57, v58\n\t"
    "v_mfma_f32_16x16x32_bf16 v[64:67], v[156:159], v[44:47], v[64:67]\n\t"
    "v_mfma_f32_16x16x32_bf16 v[64:67], v[152:155], v[36:39], v[64:67]\n\t"
    "v_mfma_f32_16x16x32_bf16 v[68:71], v[136:139], v[16:19], v[68:71]\n\t"
    "v_max3_f32 v73, v59, v60, v61\n\t"
    "v_max3_f32 v72, v72, v62, v63\n\t"
    "v_mfma_f32_16x16x32_bf16 v[68:71], v[132:135], v[44:47], v[68:71]\n\t"
    "v_mfma_f32_16x16x32_bf16 v[68:71], v[128:131], v[36:39], v[68:71]\n\t"
    "v_mfma_f32_16x16x32_bf16 v[12:15], a[4:7], v[148:151], 0\n\t"
    "v_max3_f32 v73, v73, v64, v65\n\t"
    "v_max3_f32 v72, v72, v66, v67\n\t"
    "v_mfma_f32_16x16x32_bf16 v[12:15], a[12:15], v[144:147], v[12:15]\n\t"
    "buffer_load_dwordx4 v7, s[52:55], s61 offen offset:1024 nt lds\n\t"
    "v_mfma_f32_16x16x32_bf16 v[12:15], a[20:23], v[140:143], v[12:15]\n\t"
    "v_mfma_f32_16x16x32_bf16 v[12:15], a[28:31], v[136:139], v[12:15]\n\t"
    "v_max3_f32 v73, v73, v68, v69\n\t"
    "v_max3_f32 v72, v72, v70, v71\n\t"
    "v_mfma_f32_16x16x32_bf16 v[12:15], a[36:39], v[132:135], v[12:15]\n\t"
    "v_max_f32_e32 v72, v72, v73\n\t"
    "v_mov_b32_e32 v73, v72\n\t"
    "v_mfma_f32_16x16x32_bf16 v[12:15], a[44:47], v[128:131], v[12:15]\n\t"
    "v_mfma_f32_16x16x32_bf16 v[16:19], a[48:51], v[148:151], 0\n\t"
    "v_permlane32_swap_b32 v72, v73\n\t"
    "v_max_f32_e32 v72, v72, v73\n\t"
    "v_mfma_f32_16x16x32_bf16 v[16:19], a[56:59], v[144:147], v[16:19]\n\t"
    "v_mov_b32_e32 v73, v72\n\t"
    "v_mfma_f32_16x16x32_bf16 v[16:19], a[64:67], v[140:143], v[16:19]\n\t"
    "v_mfma_f32_16x16x32_bf16 v[16:19], a[72:75], v[136:139], v[16:19]\n\t"
    "v_cvt_pk_bf16_f32 v34, v12, v13\n\t"
    "v_cvt_pk_bf16_f32 v35, v14, v15\n\t"
    "v_mfma_f32_16x16x32_bf16 v[16:19], a[80:83], v[132:135], v[16:19]\n\t"
    "v_permlane16_swap_b32 v72, v73\n\t"
    "v_max_f32_e32 v72, v72, v73\n\t"
    "v_mfma_f32_16x16x32_bf16 v[16:19], a[88:91], v[128:131], v[16:19]\n\t"
    "v_mul_f32_e32 v72, s68, v72\n\t"
    "v_fma_f32 v56, v56, s68, -v72\n\t"
    "v_mfma_f32_16x16x32_bf16 v[12:15], a[52:55], v[148:151], 0\n\t"
    "v_exp_f32_e32 v56, v56\n\t"
    "v_mfma_f32_16x16x32_bf16 v[12:15], a[60:63], v[144:147], v[12:15]\n\t"
    "v_fma_f32 v57, v57, s68, -v72\n\t"
    "v_fma_f32 v58, v58, s68, -v72\n\t"
    "v_mfma_f32_16x16x32_bf16 v[12:15], a[68:71], v[140:143], v[12:15]\n\t"
    "v_exp_f32_e32 v57, v57\n\t"
    "v_mfma_f32_16x16x32_bf16 v[12:15], a[76:79], v[136:139], v[12:15]\n\t"
    "v_cvt_pk_bf16_f32 v36, v16, v17\n\t"
    "v_cvt_pk_bf16_f32 v37, v18, v19\n\t"
    "v_mfma_f32_16x16x32_bf16 v[12:15], a[84:87], v[132:135], v[12:15]\n\t"
    "v_add_f32_e32 v74, v56, v57\n\t"
    "v_fma_f32 v59, v59, s68, -v72\n\t"
    "v_mfma_f32_16x16x32_bf16 v[12:15], a[92:95], v[128:131], v[12:15]\n\t"
    "buffer_load_dwordx4 v7, s[52:55], s61 offen offset:2048 nt lds\n\t"
    "v_fma_f32 v60, v60, s68, -v72\n\t"
    "v_mfma_f32_16x16x32_bf16 v[16:19], a[96:99], v[148:151], 0\n\t"
    "s_add_u32 s61, s61, 196608\n\t"
    "v_fma_f32 v61, v61, s68, -v72\n\t"
    "v_mfma_f32_16x16x32_bf16 v[16:19], a[104:107], v[144:147], v[16:19]\n\t"
    "v_exp_f32_e32 v58, v58\n\t"
    "v_mfma_f32_16x16x32_bf16 v[16:19], a[112:115], v[140:143], v[16:19]\n\t"
    "v_exp_f32_e32 v59, v59\n\t"
    "v_mfma_f32_16x16x32_bf16 v[16:19], a[120:123], v[136:139], v[16:19]\n\t"
    "v_cvt_pk_bf16_f32 v38, v12, v13\n\t"
    "v_cvt_pk_bf16_f32 v39, v14, v15\n\t"
    "v_mfma_f32_16x16x32_bf16 v[16:19], a[128:131], v[132:135], v[16:19]\n\t"
    "v_add_f32_e32 v75, v58, v59\n\t"
    "v_fma_f32 v62, v62, s68, -v72\n\t"
    "v_mfma_f32_16x16x32_bf16 v[16:19], a[136:139], v[128:131], v[16:19]\n\t"
    "v_exp_f32_e32 v60, v60\n\t"
    "v_mfma_f32_16x16x32_bf16 v[20:23], a[100:103], v[148:151], 0\n\t"
    "v_add_f32_e32 v74, v74, v60\n\t"
    "v_fma_f32 v63, v63, s68, -v72\n\t"
    "v_mfma_f32_16x16x32_bf16 v[20:23], a[108:111], v[144:147], v[20:23]\n\t"
    "v_exp_f32_e32 v61, v61\n\t"
    "v_mfma_f32_16x16x32_bf16 v[20:23], a[116:119], v[140:143], v[20:23]\n\t"
    "v_add_f32_e32 v74, v74, v61\n\t"
    "v_fma_f32 v64, v64, s68, -v72\n\t"
    "v_mfma_f32_16x16x32_bf16 v[20:23], a[124:127], v[136:139], v[20:23]\n\t"
    "v_cvt_pk_bf16_f32 v40, v16, v17\n\t"
    "v_cvt_pk_bf16_f32 v41, v18, v19\n\t"
    "v_mfma_f32_16x16x32_bf16 v[20:23], a[132:135], v[132:135], v[20:23]\n\t"
    "v_exp_f32_e32 v62, v62\n\t"
    "v_mfma_f32_16x16x32_bf16 v[20:23], a[140:143], v[128:131], v[20:23]\n\t"
    "v_add_f32_e32 v75, v75, v62\n\t"
    "v_fma_f32 v65, v65, s68, -v72\n\t"
    "v_mfma_f32_16x16x32_bf16 v[44:47], a[144:147], v[148:151], 0\n\t"
    "v_exp_f32_e32 v63, v63\n\t"
    "v_mfma_f32_16x16x32_bf16 v[44:47], a[152:155], v[144:147], v[44:47]\n\t"
    "v_add_f32_e32 v75, v75, v63\n\t"
    "v_fma_f32 v66, v66, s68, -v72\n\t"
    "v_mfma_f32_16x16x32_bf16 v[44:47], a[160:163], v[140:143], v[44:47]\n\t"
    "s_add_u32 m0, s62, 3072\n\t"
    "v_fma_f32 v67, v67, s68, -v72\n\t"
    "v_mfma_f32_16x16x32_bf16 v[44:47], a[168:171], v[136:139], v[44:47]\n\t"
    "buffer_load_dwordx4 v7, s[52:55], s61 offen offset:0 nt lds\n\t"
    "v_cvt_pk_bf16_f32 v42, v20, v21\n\t"
    "v_mfma_f32_16x16x32_bf16 v[44:47], a[176:179], v[132:135], v[44:47]\n\t"
    "v_cvt_pk_bf16_f32 v43, v22, v23\n\t"
    "v_fma_f32 v68, v68, s68, -v72\n\t"
    "v_mfma_f32_16x16x32_bf16 v[44:47], a[184:187], v[128:131], v[44:47]\n\t"
    "v_exp_f32_e32 v64, v64\n\t"
    "v_mfma_f32_16x16x32_bf16 v[12:15], v[220:223], v[32:35], 0\n\t"
    "v_add_f32_e32 v74, v74, v64\n\t"
    "v_fma_f32 v69, v69, s68, -v72\n\t"
    "v_mfma_f32_16x16x32_bf16 v[12:15], v[216:219], v[36:39], v[12:15]\n\t"
    "v_exp_f32_e32 v65, v65\n\t"
    "v_mfma_f32_16x16x32_bf16 v[12:15], v[212:215], v[40:43], v[12:15]\n\t"
    "s_waitcnt vmcnt(22)\n\t"
    "v_add_f32_e32 v74, v74, v65\n\t"
    "v_mfma_f32_16x16x32_bf16 v[16:19], v[196:199], v[32:35], 0\n\t"
    "v_exp_f32_e32 v66, v66\n\t"
    "v_mfma_f32_16x16x32_bf16 v[16:19], v[192:195], v[36:39], v[16:19]\n\t"
    "ds_read_b128 v[220:223], v0 offset:0\n\t"
    "v_add_f32_e32 v75, v75, v66\n\t"
    "v_mfma_f32_16x16x32_bf16 v[16:19], v[188:191], v[40:43], v[16:19]\n\t"
    "v_exp_f32_e32 v67, v67\n\t"
    "v_mfma_f32_16x16x32_bf16 v[20:23], v[172:175], v[32:35], 0\n\t"
    "ds_read_b128 v[216:219], v1 offset:0\n\t"
    "v_add_f32_e32 v75, v75, v67\n\t"
    "v_mfma_f32_16x16x32_bf16 v[20:23], v[168:171], v[36:39], v[20:23]\n\t"
    "v_exp_f32_e32 v68, v68\n\t"
    "v_fma_f32 v70, v70, s68, -v72\n\t"
    "v_mfma_f32_16x16x32_bf16 v[20:23], v[164:167], v[40:43], v[20:23]\n\t"
    "ds_read_b128 v[212:215], v2 offset:0\n\t"
    "v_add_f32_e32 v74, v74, v68\n\t"
    "v_mfma_f32_16x16x32_bf16 v[24:27], v[148:151], v[32:35], 0\n\t"
    "v_exp_f32_e32 v69, v69\n\t"
    "v_mfma_f32_16x16x32_bf16 v[24:27], v[144:147], v[36:39], v[24:27]\n\t"
    "ds_read_b128 v[28:31], v3 offset:0\n\t"
    "v_add_f32_e32 v74, v74, v69\n\t"
    "v_mfma_f32_16x16x32_bf16 v[24:27], v[140:143], v[40:43], v[24:27]\n\t"
    "v_exp_f32_e32 v70, v70\n\t"
    "v_mfma_f32_16x16x32_bf16 v[48:51], a[148:151], v[148:151], 0\n\t"
    "ds_read_b128 v[32:35], v4 offset:0\n\t"
    "v_add_f32_e32 v75, v75, v70\n\t"
    "v_mfma_f32_16x16x32_bf16 v[48:51], a[156:159], v[144:147], v[48:51]\n\t"
    "buffer_load_dwordx4 v7, s[52:55], s61 offen offset:1024 nt lds\n\t"
    "v_fma_f32 v71, v71, s68, -v72\n\t"
    "v_mfma_f32_16x16x32_bf16 v[48:51], a[164:167], v[140:143], v[48:51]\n\t"
    "ds_read_b128 v[36:39], v5 offset:0\n\t"
    "v_cvt_pk_bf16_f32 v40, v44, v45\n\t"
    "v_mfma_f32_16x16x32_bf16 v[48:51], a[172:175], v[136:139], v[48:51]\n\t"
    "v_exp_f32_e32 v71, v71\n\t"
    "v_mfma_f32_16x16x32_bf16 v[48:51], a[180:183], v[132:135], v[48:51]\n\t"
    "v_add_f32_e32 v75, v75, v71\n\t"
    "v_add_f32_e32 v74, v74, v75\n\t"
    "v_mfma_f32_16x16x32_bf16 v[48:51], a[188:191], v[128:131], v[48:51]\n\t"
    "v_mov_b32_e32 v75, v74\n\t"
    "v_cvt_pk_bf16_f32 v41, v46, v47\n\t"
    "v_mfma_f32_16x16x32_bf16 v[52:55], a[192:195], v[148:151], 0\n\t"
    "v_mfma_f32_16x16x32_bf16 v[52:55], a[200:203], v[144:147], v[52:55]\n\t"
    "v_permlane32_swap_b32 v74, v75\n\t"
    "v_add_f32_e32 v74, v74, v75\n\t"
    "v_mfma_f32_16x16x32_bf16 v[52:55], a[208:211], v[140:143], v[52:55]\n\t"
    "v_mov_b32_e32 v75, v74\n\t"
    "v_mfma_f32_16x16x32_bf16 v[52:55], a[216:219], v[136:139], v[52:55]\n\t"
    "v_cvt_pk_bf16_f32 v42, v48, v49\n\t"
    "v_cvt_pk_bf16_f32 v43, v50, v51\n\t"
    "v_mfma_f32_16x16x32_bf16 v[52:55], a[224:227], v[132:135], v[52:55]\n\t"
    "v_permlane16_swap_b32 v74, v75\n\t"
    "v_add_f32_e32 v74, v74, v75\n\t"
    "v_mfma_f32_16x16x32_bf16 v[52:55], a[232:235], v[128:131], v[52:55]\n\t"
    "v_rcp_f32_e32 v74, v74\n\t"
    "v_mfma_f32_16x16x32_bf16 v[76:79], a[196:199], v[148:151], 0\n\t"
    "v_mul_f32_e32 v56, v56, v74\n\t"
    "v_mul_f32_e32 v57, v57, v74\n\t"
    "v_mfma_f32_16x16x32_bf16 v[76:79], a[204:207], v[144:147], v[76:79]\n\t"
    "v_cvt_pk_bf16_f32 v44, v56, v57\n\t"
    "v_mul_f32_e32 v58, v58, v74\n\t"
    "v_mfma_f32_16x16x32_bf16 v[76:79], a[212:215], v[140:143], v[76:79]\n\t"
    "v_mul_f32_e32 v59, v59, v74\n\t"
    "v_cvt_pk_bf16_f32 v45, v58, v59\n\t"
    "v_mfma_f32_16x16x32_bf16 v[76:79], a[220:223], v[136:139], v[76:79]\n\t"
    "v_cvt_pk_bf16_f32 v48, v52, v53\n\t"
    "v_cvt_pk_bf16_f32 v49, v54, v55\n\t"
    "v_mfma_f32_16x16x32_bf16 v[76:79], a[228:231], v[132:135], v[76:79]\n\t"
    "v_mul_f32_e32 v60, v60, v74\n\t"
    "v_mul_f32_e32 v61, v61, v74\n\t"
    "v_mfma_f32_16x16x32_bf16 v[76:79], a[236:239], v[128:131], v[76:79]\n\t"
    "buffer_load_dwordx4 v7, s[52:55], s61 offen offset:2048 nt lds\n\t"
    "v_cvt_pk_bf16_f32 v46, v60, v61\n\t"
    "v_mfma_f32_16x16x32_bf16 v[80:83], a[240:243], v[148:151], 0\n\t"
    "s_add_u32 s61, s61, 196608\n\t"
    "v_mul_f32_e32 v62, v62, v74\n\t"
    "v_mfma_f32_16x16x32_bf16 v[80:83], a[248:251], v[144:147], v[80:83]\n\t"
    "v_mul_f32_e32 v63, v63, v74\n\t"
    "v_cvt_pk_bf16_f32 v47, v62, v63\n\t"
    "v_mfma_f32_16x16x32_bf16 v[80:83], v[224:227], v[140:143], v[80:83]\n\t"
    "v_mul_f32_e32 v64, v64, v74\n\t"
    "v_mul_f32_e32 v65, v65, v74\n\t"
    "v_mfma_f32_16x16x32_bf16 v[80:83], v[232:235], v[136:139], v[80:83]\n\t"
    "v_cvt_pk_bf16_f32 v50, v76, v77\n\t"
    "v_cvt_pk_bf16_f32 v51, v78, v79\n\t"
    "v_mfma_f32_16x16x32_bf16 v[80:83], v[240:243], v[132:135], v[80:83]\n\t"
    "v_cvt_pk_bf16_f32 v52, v64, v65\n\t"
    "v_mul_f32_e32 v66, v66, v74\n\t"
    "v_mfma_f32_16x16x32_bf16 v[80:83], v[248:251], v[128:131], v[80:83]\n\t"
    "v_mul_f32_e32 v67, v67, v74\n\t"
    "v_cvt_pk_bf16_f32 v53, v66, v67\n\t"
    "v_mfma_f32_16x16x32_bf16 v[56:59], a[244:247], v[148:151], 0\n\t"
    "v_mul_f32_e32 v68, v68, v74\n\t"
    "v_mul_f32_e32 v69, v69, v74\n\t"
    "v_mfma_f32_16x16x32_bf16 v[56:59], a[252:255], v[144:147], v[56:59]\n\t"
    "v_cvt_pk_bf16_f32 v54, v68, v69\n\t"
    "v_mul_f32_e32 v70, v70, v74\n\t"
    "v_mfma_f32_16x16x32_bf16 v[56:59], v[228:231], v[140:143], v[56:59]\n\t"
    "v_mul_f32_e32 v71, v71, v74\n\t"
    "v_cvt_pk_bf16_f32 v55, v70, v71\n\t"
    "v_mfma_f32_16x16x32_bf16 v[56:59], v[236:239], v[136:139], v[56:59]\n\t"
    "v_cvt_pk_bf16_f32 v60, v80, v81\n\t"
    "v_cvt_pk_bf16_f32 v61, v82, v83\n\t"
    "v_mfma_f32_16x16x32_bf16 v[56:59], v[244:247], v[132:135], v[56:59]\n\t"
    "s_mov_b32 s63, s62\n\t"
    "s_mov_b32 s62, s64\n\t"
    "v_mfma_f32_16x16x32_bf16 v[56:59], v[252:255], v[128:131], v[56:59]\n\t"
    "s_sub_u32 s60, s60, 1\n\t"
    "s_cmp_lg_u32 s60, 0\n\t"
    "s_cbranch_scc0 .Ltail_%=\n\t"
    "s_waitcnt lgkmcnt(0)\n\t"
    "v_mfma_f32_16x16x32_bf16 v[64:67], a[0:3], v[220:223], 0\n\t"
    "ds_write_b128 v6, v[44:47] offset:4096\n\t"
    "v_mfma_f32_16x16x32_bf16 v[64:67], a[8:11], v[216:219], v[64:67]\n\t"
    "v_mfma_f32_16x16x32_bf16 v[64:67], a[16:19], v[212:215], v[64:67]\n\t"
    "s_add_u32 m0, s63, 6144\n\t"
    "v_mfma_f32_16x16x32_bf16 v[64:67], a[24:27], v[28:31], v[64:67]\n\t"
    "buffer_load_dwordx4 v7, s[52:55], s61 offen offset:0 nt lds\n\t"
    "v_cvt_pk_bf16_f32 v62, v56, v57\n\t"
    "v_mfma_f32_16x16x32_bf16 v[64:67], a[32:35], v[32:35], v[64:67]\n\t"
    "ds_write_b128 v6, v[52:55] offset:5120\n\t"
    "v_mfma_f32_16x16x32_bf16 v[64:67], a[40:43], v[36:39], v[64:67]\n\t"
    "v_cvt_pk_bf16_f32 v63, v58, v59\n\t"
    "v_mfma_f32_16x16x32_bf16 v[12:15], v[208:211], v[40:43], v[12:15]\n\t"
    "v_mfma_f32_16x16x32_bf16 v[12:15], v[204:207], v[48:51], v[12:15]\n\t"
    "v_mfma_f32_16x16x32_bf16 v[12:15], v[200:203], v[60:63], v[12:15]\n\t"
    "v_mfma_f32_16x16x32_bf16 v[16:19], v[184:187], v[40:43], v[16:19]\n\t"
    "v_cvt_pk_bf16_f32 v44, v64, v65\n\t"
    "v_cvt_pk_bf16_f32 v45, v66, v67\n\t"
    "v_mfma_f32_16x16x32_bf16 v[16:19], v[180:183], v[48:51], v[16:19]\n\t"
    "s_waitcnt lgkmcnt(0)\n\t"
    "s_barrier \n\t"
    "v_mfma_f32_16x16x32_bf16 v[16:19], v[176:179], v[60:63], v[16:19]\n\t"
    "v_mfma_f32_16x16x32_bf16 v[20:23], v[160:163], v[40:43], v[20:23]\n\t"
    "v_max3_f32 v52, v12, v13, v14\n\t"
    "v_mfma_f32_16x16x32_bf16 v[20:23], v[156:159], v[48:51], v[20:23]\n\t"
    "v_mfma_f32_16x16x32_bf16 v[20:23], v[152:155], v[60:63], v[20:23]\n\t"
    "v_mfma_f32_16x16x32_bf16 v[24:27], v[136:139], v[40:43], v[24:27]\n\t"
    "v_max3_f32 v53, v15, v16, v17\n\t"
    "v_max3_f32 v52, v52, v18, v19\n\t"
    "v_mfma_f32_16x16x32_bf16 v[24:27], v[132:135], v[48:51], v[24:27]\n\t"
    "s_waitcnt vmcnt(7)\n\t"
    "v_mfma_f32_16x16x32_bf16 v[24:27], v[128:131], v[60:63], v[24:27]\n\t"
    "ds_read_b128 v[196:199], v0 offset:6144\n\t"
    "v_mfma_f32_16x16x32_bf16 v[40:43], a[4:7], v[220:223], 0\n\t"
    "v_max3_f32 v53, v53, v20, v21\n\t"
    "v_max3_f32 v52, v52, v22, v23\n\t"
    "v_mfma_f32_16x16x32_bf16 v[40:43], a[12:15], v[216:219], v[40:43]\n\t"
    "buffer_load_dwordx4 v7, s[52:55], s61 offen offset:1024 nt lds\n\t"
    "ds_read_b128 v[192:195], v1 offset:6144\n\t"
    "v_mfma_f32_16x16x32_bf16 v[40:43], a[20:23], v[212:215], v[40:43]\n\t"
    "v_mfma_f32_16x16x32_bf16 v[40:43], a[28:31], v[28:31], v[40:43]\n\t"
    "ds_read_b128 v[188:191], v2 offset:6144\n\t"
    "v_max3_f32 v53, v53, v24, v25\n\t"
    "v_mfma_f32_16x16x32_bf16 v[40:43], a[36:39], v[32:35], v[40:43]\n\t"
    "v_max3_f32 v52, v52, v26, v27\n\t"
    "v_max_f32_e32 v52, v52, v53\n\t"
    "v_mfma_f32_16x16x32_bf16 v[40:43], a[44:47], v[36:39], v[40:43]\n\t"
    "ds_read_b128 v[172:175], v0 offset:12288\n\t"
    "v_mov_b32_e32 v53, v52\n\t"
    "v_mfma_f32_16x16x32_bf16 v[48:51], a[48:51], v[220:223], 0\n\t"
    "v_mfma_f32_16x16x32_bf16 v[48:51], a[56:59], v[216:219], v[48:51]\n\t"
    "ds_read_b128 v[168:171], v1 offset:12288\n\t"
    "v_permlane32_swap_b32 v52, v53\n\t"
    "v_mfma_f32_16x16x32_bf16 v[48:51], a[64:67], v[212:215], v[48:51]\n\t"
    "v_max_f32_e32 v52, v52, v53\n\t"
    "v_mov_b32_e32 v53, v52\n\t"
    "v_mfma_f32_16x16x32_bf16 v[48:51], a[72:75], v[28:31], v[48:51]\n\t"
    "ds_read_b128 v[164:167], v2 offset:12288\n\t"
    "v_cvt_pk_bf16_f32 v46, v40, v41\n\t"
    "v_mfma_f32_16x16x32_bf16 v[48:51], a[80:83], v[32:35], v[48:51]\n\t"
    "v_cvt_pk_bf16_f32 v47, v42, v43\n\t"
    "v_permlane16_swap_b32 v52, v53\n\t"
    "v_mfma_f32_16x16x32_bf16 v[48:51], a[88:91], v[36:39], v[48:51]\n\t"
    "ds_read_b128 v[148:151], v0 offset:18432\n\t"
    "v_max_f32_e32 v52, v52, v53\n\t"
    "v_mfma_f32_16x16x32_bf16 v[56:59], a[52:55], v[220:223], 0\n\t"
    "v_mul_f32_e32 v52, s68, v52\n\t"
    "v_fma_f32 v12, v12, s68, -v52\n\t"
    "v_mfma_f32_16x16x32_bf16 v[56:59], a[60:63], v[216:219], v[56:59]\n\t"
    "ds_read_b128 v[144:147], v1 offset:18432\n\t"
    "v_fma_f32 v13, v13, s68, -v52\n\t"
    "v_mfma_f32_16x16x32_bf16 v[56:59], a[68:71], v[212:215], v[56:59]\n\t"
    "v_exp_f32_e32 v12, v12\n\t"
    "v_mfma_f32_16x16x32_bf16 v[56:59], a[76:79], v[28:31], v[56:59]\n\t"
    "ds_read_b128 v[140:143], v2 offset:18432\n\t"
    "v_cvt_pk_bf16_f32 v40, v48, v49\n\t"
    "v_fma_f32 v14, v14, s68, -v52\n\t"
    "v_mfma_f32_16x16x32_bf16 v[56:59], a[84:87], v[32:35], v[56:59]\n\t"
    "v_cvt_pk_bf16_f32 v41, v50, v51\n\t"
    "v_fma_f32 v15, v15, s68, -v52\n\t"
    "v_mfma_f32_16x16x32_bf16 v[56:59], a[92:95], v[36:39], v[56:59]\n\t"
    "buffer_load_dwordx4 v7, s[52:55], s61 offen offset:2048 nt lds\n\t"
    "v_fma_f32 v16, v16, s68, -v52\n\t"
    "v_mfma_f32_16x16x32_bf16 v[60:63], a[96:99], v[220:223], 0\n\t"
    "s_add_u32 s61, s61, 196608\n\t"
    "v_fma_f32 v17, v17, s68, -v52\n\t"
    "v_mfma_f32_16x16x32_bf16 v[60:63], a[104:107], v[216:219], v[60:63]\n\t"
    "v_exp_f32_e32 v13, v13\n\t"
    "v_mfma_f32_16x16x32_bf16 v[60:63], a[112:115], v[212:215], v[60:63]\n\t"
    "v_add_f32_e32 v54, v12, v13\n\t"
    "v_fma_f32 v18, v18, s68, -v52\n\t"
    "v_mfma_f32_16x16x32_bf16 v[60:63], a[120:123], v[28:31], v[60:63]\n\t"
    "v_cvt_pk_bf16_f32 v42, v56, v57\n\t"
    "v_cvt_pk_bf16_f32 v43, v58, v59\n\t"
    "v_mfma_f32_16x16x32_bf16 v[60:63], a[128:131], v[32:35], v[60:63]\n\t"
    "v_exp_f32_e32 v14, v14\n\t"
    "v_mfma_f32_16x16x32_bf16 v[60:63], a[136:139], v[36:39], v[60:63]\n\t"
    "v_exp_f32_e32 v15, v15\n\t"
    "v_mfma_f32_16x16x32_bf16 v[48:51], a[100:103], v[220:223], 0\n\t"
    "v_add_f32_e32 v55, v14, v15\n\t"
    "v_fma_f32 v19, v19, s68, -v52\n\t"
    "v_mfma_f32_16x16x32_bf16 v[48:51], a[108:111], v[216:219], v[48:51]\n\t"
    "v_exp_f32_e32 v16, v16\n\t"
    "v_mfma_f32_16x16x32_bf16 v[48:51], a[116:119], v[212:215], v[48:51]\n\t"
    "v_add_f32_e32 v54, v54, v16\n\t"
    "v_fma_f32 v20, v20, s68, -v52\n\t"
    "v_mfma_f32_16x16x32_bf16 v[48:51], a[124:127], v[28:31], v[48:51]\n\t"
    "v_cvt_pk_bf16_f32 v56, v60, v61\n\t"
    "v_cvt_pk_bf16_f32 v57, v62, v63\n\t"
    "v_mfma_f32_16x16x32_bf16 v[48:51], a[132:135], v[32:35], v[48:51]\n\t"
    "v_exp_f32_e32 v17, v17\n\t"
    "v_mfma_f32_16x16x32_bf16 v[48:51], a[140:143], v[36:39], v[48:51]\n\t"
    "v_add_f32_e32 v54, v54, v17\n\t"
    "v_fma_f32 v21, v21, s68, -v52\n\t"
    "v_mfma_f32_16x16x32_bf16 v[60:63], a[144:147], v[220:223], 0\n\t"
    "v_exp_f32_e32 v18, v18\n\t"
    "v_mfma_f32_16x16x32_bf16 v[60:63], a[152:155], v[216:219], v[60:63]\n\t"
    "v_add_f32_e32 v55, v55, v18\n\t"
    "v_exp_f32_e32 v19, v19\n\t"
    "v_mfma_f32_16x16x32_bf16 v[60:63], a[160:163], v[212:215], v[60:63]\n\t"
    "s_add_u32 m0, s63, 9216\n\t"
    "v_add_f32_e32 v55, v55, v19\n\t"
    "v_mfma_f32_16x16x32_bf16 v[60:63], a[168:171], v[28:31], v[60:63]\n\t"
    "buffer_load_dwordx4 v7, s[52:55], s61 offen offset:0 nt lds\n\t"
    "v_cvt_pk_bf16_f32 v58, v48, v49\n\t"
    "v_mfma_f32_16x16x32_bf16 v[60:63], a[176:179], v[32:35], v[60:63]\n\t"
    "v_cvt_pk_bf16_f32 v59, v50, v51\n\t"
    "v_fma_f32 v22, v22, s68, -v52\n\t"
    ".Lh5_%=:\n\t"
    "s_cbranch_execz .LrM_%=\n\t"
    "v_mfma_f32_16x16x32_bf16 v[60:63], a[184:187], v[36:39], v[60:63]\n\t"
    "v_exp_f32_e32 v20, v20\n\t"
    "v_mfma_f32_16x16x32_bf16 v[64:67], v[220:223], v[44:47], 0\n\t"
    "v_add_f32_e32 v54, v54, v20\n\t"
    "v_fma_f32 v23, v23, s68, -v52\n\t"
    "v_mfma_f32_16x16x32_bf16 v[64:67], v[216:219], v[40:43], v[64:67]\n\t"
    "v_exp_f32_e32 v21, v21\n\t"
    "v_mfma_f32_16x16x32_bf16 v[64:67], v[212:215], v[56:59], v[64:67]\n\t"
    "v_add_f32_e32 v54, v54, v21\n\t"
    "s_waitcnt lgkmcnt(0)\n\t"
    "v_mfma_f32_16x16x32_bf16 v[48:51], v[196:199], v[44:47], 0\n\t"
    "v_exp_f32_e32 v22, v22\n\t"
    "v_mfma_f32_16x16x32_bf16 v[48:51], v[192:195], v[40:43], v[48:51]\n\t"
    "v_add_f32_e32 v55, v55, v22\n\t"
    "v_fma_f32 v24, v24, s68, -v52\n\t"
    "v_mfma_f32_16x16x32_bf16 v[48:51], v[188:191], v[56:59], v[48:51]\n\t"
    "ds_read_b128 v[184:187], v3 offset:6144\n\t"
    "v_fma_f32 v25, v25, s68, -v52\n\t"
    "v_mfma_f32_16x16x32_bf16 v[68:71], v[172:175], v[44:47], 0\n\t"
    "v_exp_f32_e32 v23, v23\n\t"
    "v_mfma_f32_16x16x32_bf16 v[68:71], v[168:171], v[40:43], v[68:71]\n\t"
    "ds_read_b128 v[180:183], v4 offset:6144\n\t"
    "v_add_f32_e32 v55, v55, v23\n\t"
    "v_mfma_f32_16x16x32_bf16 v[68:71], v[164:167], v[56:59], v[68:71]\n\t"
    "v_exp_f32_e32 v24, v24\n\t"
    "v_mfma_f32_16x16x32_bf16 v[72:75], v[148:151], v[44:47], 0\n\t"
    "ds_read_b128 v[176:179], v5 offset:6144\n\t"
    "v_add_f32_e32 v54, v54, v24\n\t"
    "v_mfma_f32_16x16x32_bf16 v[72:75], v[144:147], v[40:43], v[72:75]\n\t"
    "v_exp_f32_e32 v25, v25\n\t"
    "v_mfma_f32_16x16x32_bf16 v[72:75], v[140:143], v[56:59], v[72:75]\n\t"
    "ds_read_b128 v[208:211], v3 offset:0\n\t"
    "v_add_f32_e32 v54, v54, v25\n\t"
    "v_mfma_f32_16x16x32_bf16 v[44:47], a[148:151], v[220:223], 0\n\t"
    "v_fma_f32 v26, v26, s68, -v52\n\t"
    "v_exp_f32_e32 v26, v26\n\t"
    "v_mfma_f32_16x16x32_bf16 v[44:47], a[156:159], v[216:219], v[44:47]\n\t"
    "buffer_load_dwordx4 v7, s[52:55], s61 offen offset:1024 nt lds\n\t"
    "ds_read_b128 v[204:207], v4 offset:0\n\t"
    "v_mfma_f32_16x16x32_bf16 v[44:47], a[164:167], v[212:215], v[44:47]\n\t"
    "v_add_f32_e32 v55, v55, v26\n\t"
    "v_fma_f32 v27, v27, s68, -v52\n\t"
    "v_mfma_f32_16x16x32_bf16 v[44:47], a[172:175], v[28:31], v[44:47]\n\t"
    "ds_read_b128 v[200:203], v5 offset:0\n\t"
    "v_cvt_pk_bf16_f32 v40, v60, v61\n\t"
    "v_mfma_f32_16x16x32_bf16 v[44:47], a[180:183], v[32:35], v[44:47]\n\t"
    "v_exp_f32_e32 v27, v27\n\t"
    "v_mfma_f32_16x16x32_bf16 v[44:47], a[188:191], v[36:39], v[44:47]\n\t"
    "ds_read_b128 v[160:163], v3 offset:12288\n\t"
    "v_add_f32_e32 v55, v55, v27\n\t"
    "v_mfma_f32_16x16x32_bf16 v[56:59], a[192:195], v[220:223], 0\n\t"
    "v_add_f32_e32 v54, v54, v55\n\t"
    "v_mov_b32_e32 v55, v54\n\t"
    "v_mfma_f32_16x16x32_bf16 v[56:59], a[200:203], v[216:219], v[56:59]\n\t"
    "ds_read_b128 v[156:159], v4 offset:12288\n\t"
    "v_cvt_pk_bf16_f32 v41, v62, v63\n\t"
    "v_mfma_f32_16x16x32_bf16 v[56:59], a[208:211], v[212:215], v[56:59]\n\t"
    "v_permlane32_swap_b32 v54, v55\n\t"
    "v_add_f32_e32 v54, v54, v55\n\t"
    "v_mfma_f32_16x16x32_bf16 v[56:59], a[216:219], v[28:31], v[56:59]\n\t"
    "ds_read_b128 v[152:155], v5 offset:12288\n\t"
    "v_cvt_pk_bf16_f32 v42, v44, v45\n\t"
    "v_mfma_f32_16x16x32_bf16 v[56:59], a[224:227], v[32:35], v[56:59]\n\t"
    "v_cvt_pk_bf16_f32 v43, v46, v47\n\t"
    "v_mov_b32_e32 v55, v54\n\t"
    "v_mfma_f32_16x16x32_bf16 v[56:59], a[232:235], v[36:39], v[56:59]\n\t"
    "ds_read_b128 v[136:139], v3 offset:18432\n\t"
    "v_mfma_f32_16x16x32_bf16 v[60:63], a[196:199], v[220:223], 0\n\t"
    "v_permlane16_swap_b32 v54, v55\n\t"
    "v_add_f32_e32 v54, v54, v55\n\t"
    "v_mfma_f32_16x16x32_bf16 v[60:63], a[204:207], v[216:219], v[60:63]\n\t"
    "ds_read_b128 v[132:135], v4 offset:18432\n\t"
    "v_mfma_f32_16x16x32_bf16 v[60:63], a[212:215], v[212:215], v[60:63]\n\t"
    "v_rcp_f32_e32 v54, v54\n\t"
    "v_mfma_f32_16x16x32_bf16 v[60:63], a[220:223], v[28:31], v[60:63]\n\t"
    "ds_read_b128 v[128:131], v5 offset:18432\n\t"
    "v_cvt_pk_bf16_f32 v44, v56, v57\n\t"
    "v_mfma_f32_16x16x32_bf16 v[60:63], a[228:231], v[32:35], v[60:63]\n\t"
    "v_cvt_pk_bf16_f32 v45, v58, v59\n\t"
    "v_mul_f32_e32 v12, v12, v54\n\t"
    "v_mfma_f32_16x16x32_bf16 v[60:63], a[236:239], v[36:39], v[60:63]\n\t"
    "buffer_load_dwordx4 v7, s[52:55], s61 offen offset:2048 nt lds\n\t"
    "v_mul_f32_e32 v13, v13, v54\n\t"
    "v_cvt_pk_bf16_f32 v76, v12, v13\n\t"
    "v_mfma_f32_16x16x32_bf16 v[80:83], a[240:243], v[220:223], 0\n\t"
    "s_add_u32 s61, s61, 196608\n\t"
    "v_mul_f32_e32 v14, v14, v54\n\t"
    "v_mfma_f32_16x16x32_bf16 v[80:83], a[248:251], v[216:219], v[80:83]\n\t"
    "v_mul_f32_e32 v15, v15, v54\n\t"
    "v_cvt_pk_bf16_f32 v77, v14, v15\n\t"
    "v_mfma_f32_16x16x32_bf16 v[80:83], v[224:227], v[212:215], v[80:83]\n\t"
    "v_mul_f32_e32 v16, v16, v54\n\t"
    "v_mul_f32_e32 v17, v17, v54\n\t"
    "v_mfma_f32_16x16x32_bf16 v[80:83], v[232:235], v[28:31], v[80:83]\n\t"
    "v_cvt_pk_bf16_f32 v46, v60, v61\n\t"
    "v_cvt_pk_bf16_f32 v47, v62, v63\n\t"
    "v_mfma_f32_16x16x32_bf16 v[80:83], v[240:243], v[32:35], v[80:83]\n\t"
    "v_cvt_pk_bf16_f32 v78, v16, v17\n\t"
    "v_mul_f32_e32 v18, v18, v54\n\t"
    "v_mfma_f32_16x16x32_bf16 v[80:83], v[248:251], v[36:39], v[80:83]\n\t"
    "v_mul_f32_e32 v19, v19, v54\n\t"
    "v_cvt_pk_bf16_f32 v79, v18, v19\n\t"
    "v_mfma_f32_16x16x32_bf16 v[12:15], a[244:247], v[220:223], 0\n\t"
    "v_mul_f32_e32 v20, v20, v54\n\t"
    "v_mul_f32_e32 v21, v21, v54\n\t"
    "v_mfma_f32_16x16x32_bf16 v[12:15], a[252:255], v[216:219], v[12:15]\n\t"
    "v_cvt_pk_bf16_f32 v56, v20, v21\n\t"
    "v_mul_f32_e32 v22, v22, v54\n\t"
    "v_mfma_f32_16x16x32_bf16 v[12:15], v[228:231], v[212:215], v[12:15]\n\t"
    "v_mul_f32_e32 v23, v23, v54\n\t"
    "v_cvt_pk_bf16_f32 v57, v22, v23\n\t"
    "v_mfma_f32_16x16x32_bf16 v[12:15], v[236:239], v[28:31], v[12:15]\n\t"
    "v_cvt_pk_bf16_f32 v16, v80, v81\n\t"
    "v_cvt_pk_bf16_f32 v17, v82, v83\n\t"
    "v_mfma_f32_16x16x32_bf16 v[12:15], v[244:247], v[32:35], v[12:15]\n\t"
    "v_mul_f32_e32 v24, v24, v54\n\t"
    "v_mul_f32_e32 v25, v25, v54\n\t"
    "v_mfma_f32_16x16x32_bf16 v[12:15], v[252:255], v[36:39], v[12:15]\n\t"
    "v_cvt_pk_bf16_f32 v58, v24, v25\n\t"
    "v_mul_f32_e32 v26, v26, v54\n\t"
    "v_mfma_f32_16x16x32_bf16 v[20:23], a[0:3], v[196:199], 0\n\t"
    "ds_write_b128 v6, v[76:79] offset:6144\n\t"
    "v_mfma_f32_16x16x32_bf16 v[20:23], a[8:11], v[192:195], v[20:23]\n\t"
    "v_mul_f32_e32 v27, v27, v54\n\t"
    "v_cvt_pk_bf16_f32 v59, v26, v27\n\t"
    "v_mfma_f32_16x16x32_bf16 v[20:23], a[16:19], v[188:191], v[20:23]\n\t"
    "s_add_u32 m0, s63, 12288\n\t"
    "s_waitcnt lgkmcnt(1)\n\t"
    "v_mfma_f32_16x16x32_bf16 v[20:23], a[24:27], v[184:187], v[20:23]\n\t"
    "buffer_load_dwordx4 v7, s[52:55], s61 offen offset:0 nt lds\n\t"
    "v_cvt_pk_bf16_f32 v18, v12, v13\n\t"
    "v_mfma_f32_16x16x32_bf16 v[20:23], a[32:35], v[180:183], v[20:23]\n\t"
    "ds_write_b128 v6, v[56:59] offset:7168\n\t"
    "v_mfma_f32_16x16x32_bf16 v[20:23], a[40:43], v[176:179], v[20:23]\n\t"
    "v_cvt_pk_bf16_f32 v19, v14, v15\n\t"
    "v_mfma_f32_16x16x32_bf16 v[64:67], v[208:211], v[40:43], v[64:67]\n\t"
    "v_mfma_f32_16x16x32_bf16 v[64:67], v[204:207], v[44:47], v[64:67]\n\t"
    "v_mfma_f32_16x16x32_bf16 v[64:67], v[200:203], v[16:19], v[64:67]\n\t"
    "v_mfma_f32_16x16x32_bf16 v[48:51], v[184:187], v[40:43], v[48:51]\n\t"
    "v_cvt_pk_bf16_f32 v12, v20, v21\n\t"
    "v_cvt_pk_bf16_f32 v13, v22, v23\n\t"
    "v_mfma_f32_16x16x32_bf16 v[48:51], v[180:183], v[44:47], v[48:51]\n\t"
    "s_waitcnt lgkmcnt(0)\n\t"
    "s_barrier \n\t"
    "v_mfma_f32_16x16x32_bf16 v[48:51], v[176:179], v[16:19], v[48:51]\n\t"
    "v_mfma_f32_16x16x32_bf16 v[68:71], v[160:163], v[40:43], v[68:71]\n\t"
    "v_max3_f32 v20, v64, v65, v66\n\t"
    "v_mfma_f32_16x16x32_bf16 v[68:71], v[156:159], v[44:47], v[68:71]\n\t"
    "v_mfma_f32_16x16x32_bf16 v[68:71], v[152:155], v[16:19], v[68:71]\n\t"
    "v_mfma_f32_16x16x32_bf16 v[72:75], v[136:139], v[40:43], v[72:75]\n\t"
    "v_max3_f32 v21, v67, v48, v49\n\t"
    "v_max3_f32 v20, v20, v50, v51\n\t"
    "v_mfma_f32_16x16x32_bf16 v[72:75], v[132:135], v[44:47], v[72:75]\n\t"
    "v_mfma_f32_16x16x32_bf16 v[72:75], v[128:131], v[16:19], v[72:75]\n\t"
    "v_mfma_f32_16x16x32_bf16 v[24:27], a[4:7], v[196:199], 0\n\t"
    "v_max3_f32 v21, v21, v68, v69\n\t"
    "v_max3_f32 v20, v20, v70, v71\n\t"
    "v_mfma_f32_16x16x32_bf16 v[24:27], a[12:15], v[192:195], v[24:27]\n\t"
    "buffer_load_dwordx4 v7, s[52:55], s61 offen offset:1024 nt lds\n\t"
    "v_mfma_f32_16x16x32_bf16 v[24:27], a[20:23], v[188:191], v[24:27]\n\t"
    "v_mfma_f32_16x16x32_bf16 v[24:27], a[28:31], v[184:187], v[24:27]\n\t"
    "v_max3_f32 v21, v21, v72, v73\n\t"
    "v_max3_f32 v20, v20, v74, v75\n\t"
    "v_mfma_f32_16x16x32_bf16 v[24:27], a[36:39], v[180:183], v[24:27]\n\t"
    "v_max_f32_e32 v20, v20, v21\n\t"
    "v_mov_b32_e32 v21, v20\n\t"
    "v_mfma_f32_16x16x32_bf16 v[24:27], a[44:47], v[176:179], v[24:27]\n\t"
    "v_mfma_f32_16x16x32_bf16 v[16:19], a[48:51], v[196:199], 0\n\t"
    "v_permlane32_swap_b32 v20, v21\n\t"
    "v_max_f32_e32 v20, v20, v21\n\t"
    "v_mfma_f32_16x16x32_bf16 v[16:19], a[56:59], v[192:195], v[16:19]\n\t"
    "v_mov_b32_e32 v21, v20\n\t"
    "v_mfma_f32_16x16x32_bf16 v[16:19], a[64:67], v[188:191], v[16:19]\n\t"
    "v_mfma_f32_16x16x32_bf16 v[16:19], a[72:75], v[184:187], v[16:19]\n\t"
    "v_cvt_pk_bf16_f32 v14, v24, v25\n\t"
    "v_cvt_pk_bf16_f32 v15, v26, v27\n\t"
    "v_mfma_f32_16x16x32_bf16 v[16:19], a[80:83], v[180:183], v[16:19]\n\t"
    "v_permlane16_swap_b32 v20, v21\n\t"
    "v_max_f32_e32 v20, v20, v21\n\t"
    "v_mfma_f32_16x16x32_bf16 v[16:19], a[88:91], v[176:179], v[16:19]\n\t"
    "v_mul_f32_e32 v20, s68, v20\n\t"
    "v_fma_f32 v64, v64, s68, -v20\n\t"
    "v_mfma_f32_16x16x32_bf16 v[24:27], a[52:55], v[196:199], 0\n\t"
    "v_exp_f32_e32 v64, v64\n\t"
    "v_mfma_f32_16x16x32_bf16 v[24:27], a[60:63], v[192:195], v[24:27]\n\t"
    "v_fma_f32 v65, v65, s68, -v20\n\t"
    "v_fma_f32 v66, v66, s68, -v20\n\t"
    "v_mfma_f32_16x16x32_bf16 v[24:27], a[68:71], v[188:191], v[24:27]\n\t"
    "v_exp_f32_e32 v65, v65\n\t"
    "v_mfma_f32_16x16x32_bf16 v[24:27], a[76:79], v[184:187], v[24:27]\n\t"
    "v_cvt_pk_bf16_f32 v28, v16, v17\n\t"
    "v_cvt_pk_bf16_f32 v29, v18, v19\n\t"
    "v_mfma_f32_16x16x32_bf16 v[24:27], a[84:87], v[180:183], v[24:27]\n\t"
    "v_add_f32_e32 v22, v64, v65\n\t"
    "v_fma_f32 v67, v67, s68, -v20\n\t"
    "v_mfma_f32_16x16x32_bf16 v[24:27], a[92:95], v[176:179], v[24:27]\n\t"
    "buffer_load_dwordx4 v7, s[52:55], s61 offen offset:2048 nt lds\n\t"
    "v_fma_f32 v48, v48, s68, -v20\n\t"
    "v_mfma_f32_16x16x32_bf16 v[16:19], a[96:99], v[196:199], 0\n\t"
    "s_add_u32 s61, s61, 196608\n\t"
    "v_fma_f32 v49, v49, s68, -v20\n\t"
    "v_mfma_f32_16x16x32_bf16 v[16:19], a[104:107], v[192:195], v[16:19]\n\t"
    "v_exp_f32_e32 v66, v66\n\t"
    "v_mfma_f32_16x16x32_bf16 v[16:19], a[112:115], v[188:191], v[16:19]\n\t"
    "v_exp_f32_e32 v67, v67\n\t"
    "v_mfma_f32_16x16x32_bf16 v[16:19], a[120:123], v[184:187], v[16:19]\n\t"
    "v_cvt_pk_bf16_f32 v30, v24, v25\n\t"
    "v_cvt_pk_bf16_f32 v31, v26, v27\n\t"
    "v_mfma_f32_16x16x32_bf16 v[16:19], a[128:131], v[180:183], v[16:19]\n\t"
    "v_add_f32_e32 v23, v66, v67\n\t"
    "v_fma_f32 v50, v50, s68, -v20\n\t"
    "v_mfma_f32_16x16x32_bf16 v[16:19], a[136:139], v[176:179], v[16:19]\n\t"
    "v_exp_f32_e32 v48, v48\n\t"
    "v_mfma_f32_16x16x32_bf16 v[24:27], a[100:103], v[196:199], 0\n\t"
    "v_add_f32_e32 v22, v22, v48\n\t"
    "v_fma_f32 v51, v51, s68, -v20\n\t"
    "v_mfma_f32_16x16x32_bf16 v[24:27], a[108:111], v[192:195], v[24:27]\n\t"
    "v_exp_f32_e32 v49, v49\n\t"
    "v_mfma_f32_16x16x32_bf16 v[24:27], a[116:119], v[188:191], v[24:27]\n\t"
    "v_add_f32_e32 v22, v22, v49\n\t"
    "v_fma_f32 v68, v68, s68, -v20\n\t"
    "v_mfma_f32_16x16x32_bf16 v[24:27], a[124:127], v[184:187], v[24:27]\n\t"
    "v_cvt_pk_bf16_f32 v32, v16, v17\n\t"
    "v_cvt_pk_bf16_f32 v33, v18, v19\n\t"
    "v_mfma_f32_16x16x32_bf16 v[24:27], a[132:135], v[180:183], v[24:27]\n\t"
    "v_exp_f32_e32 v50, v50\n\t"
    "v_mfma_f32_16x16x32_bf16 v[24:27], a[140:143], v[176:179], v[24:27]\n\t"
    "v_add_f32_e32 v23, v23, v50\n\t"
    "v_fma_f32 v69, v69, s68, -v20\n\t"
    "v_mfma_f32_16x16x32_bf16 v[16:19], a[144:147], v[196:199], 0\n\t"
    "v_exp_f32_e32 v51, v51\n\t"
    "v_mfma_f32_16x16x32_bf16 v[16:19], a[152:155], v[192:195], v[16:19]\n\t"
    "v_add_f32_e32 v23, v23, v51\n\t"
    "v_fma_f32 v70, v70, s68, -v20\n\t"
    "v_mfma_f32_16x16x32_bf16 v[16:19], a[160:163], v[188:191], v[16:19]\n\t"
    "s_add_u32 m0, s63, 15360\n\t"
    "v_fma_f32 v71, v71, s68, -v20\n\t"
    "v_mfma_f32_16x16x32_bf16 v[16:19], a[168:171], v[184:187], v[16:19]\n\t"
    "buffer_load_dwordx4 v7, s[52:55], s61 offen offset:0 nt lds\n\t"
    "v_cvt_pk_bf16_f32 v34, v24, v25\n\t"
    "v_mfma_f32_16x16x32_bf16 v[16:19], a[176:179], v[180:183], v[16:19]\n\t"
    "v_cvt_pk_bf16_f32 v35, v26, v27\n\t"
    "v_fma_f32 v72, v72, s68, -v20\n\t"
    "v_mfma_f32_16x16x32_bf16 v[16:19], a[184:187], v[176:179], v[16:19]\n\t"
    "v_exp_f32_e32 v68, v68\n\t"
    "v_mfma_f32_16x16x32_bf16 v[36:39], v[220:223], v[12:15], 0\n\t"
    "v_add_f32_e32 v22, v22, v68\n\t"
    "v_fma_f32 v73, v73, s68, -v20\n\t"
    "v_mfma_f32_16x16x32_bf16 v[36:39], v[216:219], v[28:31], v[36:39]\n\t"
    "v_exp_f32_e32 v69, v69\n\t"
    "v_mfma_f32_16x16x32_bf16 v[36:39], v[212:215], v[32:35], v[36:39]\n\t"
    "v_add_f32_e32 v22, v22, v69\n\t"
    "v_fma_f32 v74, v74, s68, -v20\n\t"
    "v_mfma_f32_16x16x32_bf16 v[24:27], v[196:199], v[12:15], 0\n\t"
    "v_exp_f32_e32 v70, v70\n\t"
    "v_mfma_f32_16x16x32_bf16 v[24:27], v[192:195], v[28:31], v[24:27]\n\t"
    "v_add_f32_e32 v23, v23, v70\n\t"
    "v_fma_f32 v75, v75, s68, -v20\n\t"
    "v_mfma_f32_16x16x32_bf16 v[24:27], v[188:191], v[32:35], v[24:27]\n\t"
    "v_exp_f32_e32 v71, v71\n\t"
    "v_mfma_f32_16x16x32_bf16 v[40:43], v[172:175], v[12:15], 0\n\t"
    "v_add_f32_e32 v23, v23, v71\n\t"
    "v_cvt_pk_bf16_f32 v44, v16, v17\n\t"
    "v_mfma_f32_16x16x32_bf16 v[40:43], v[168:171], v[28:31], v[40:43]\n\t"
    "v_exp_f32_e32 v72, v72\n\t"
    "v_mfma_f32_16x16x32_bf16 v[40:43], v[164:167], v[32:35], v[40:43]\n\t"
    "v_add_f32_e32 v22, v22, v72\n\t"
    "v_cvt_pk_bf16_f32 v45, v18, v19\n\t"
    "v_mfma_f32_16x16x32_bf16 v[52:55], v[148:151], v[12:15], 0\n\t"
    "v_exp_f32_e32 v73, v73\n\t"
    "v_mfma_f32_16x16x32_bf16 v[52:55], v[144:147], v[28:31], v[52:55]\n\t"
    "v_add_f32_e32 v22, v22, v73\n\t"
    "v_mfma_f32_16x16x32_bf16 v[52:55], v[140:143], v[32:35], v[52:55]\n\t"
    "v_exp_f32_e32 v74, v74\n\t"
    "v_mfma_f32_16x16x32_bf16 v[12:15], a[148:151], v[196:199], 0\n\t"
    "v_add_f32_e32 v23, v23, v74\n\t"
    "v_mfma_f32_16x16x32_bf16 v[12:15], a[156:159], v[192:195], v[12:15]\n\t"
    "buffer_load_dwordx4 v7, s[52:55], s61 offen offset:1024 nt lds\n\t"
    "v_mfma_f32_16x16x32_bf16 v[12:15], a[164:167], v[188:191], v[12:15]\n\t"
    "v_exp_f32_e32 v75, v75\n\t"
    "v_mfma_f32_16x16x32_bf16 v[12:15], a[172:175], v[184:187], v[12:15]\n\t"
    "v_add_f32_e32 v23, v23, v75\n\t"
    "v_add_f32_e32 v22, v22, v23\n\t"
    "v_mfma_f32_16x16x32_bf16 v[12:15], a[180:183], v[180:183], v[12:15]\n\t"
    "v_mov_b32_e32 v23, v22\n\t"
    "v_mfma_f32_16x16x32_bf16 v[12:15], a[188:191], v[176:179], v[12:15]\n\t"
    "v_mfma_f32_16x16x32_bf16 v[16:19], a[192:195], v[196:199], 0\n\t"
    "v_permlane32_swap_b32 v22, v23\n\t"
    "v_add_f32_e32 v22, v22, v23\n\t"
    "v_mfma_f32_16x16x32_bf16 v[16:19], a[200:203], v[192:195], v[16:19]\n\t"
    "v_mov_b32_e32 v23, v22\n\t"
    "v_mfma_f32_16x16x32_bf16 v[16:19], a[208:211], v[188:191], v[16:19]\n\t"
    "v_mfma_f32_16x16x32_bf16 v[16:19], a[216:219], v[184:187], v[16:19]\n\t"
    "v_cvt_pk_bf16_f32 v46, v12, v13\n\t"
    "v_cvt_pk_bf16_f32 v47, v14, v15\n\t"
    "v_mfma_f32_16x16x32_bf16 v[16:19], a[224:227], v[180:183], v[16:19]\n\t"
    "v_permlane16_swap_b32 v22, v23\n\t"
    "v_add_f32_e32 v22, v22, v23\n\t"
    "v_mfma_f32_16x16x32_bf16 v[16:19], a[232:235], v[176:179], v[16:19]\n\t"
    "v_rcp_f32_e32 v22, v22\n\t"
    "v_mfma_f32_16x16x32_bf16 v[12:15], a[196:199], v[196:199], 0\n\t"
    "v_mul_f32_e32 v64, v64, v22\n\t"
    "v_mul_f32_e32 v65, v65, v22\n\t"
    "v_mfma_f32_16x16x32_bf16 v[12:15], a[204:207], v[192:195], v[12:15]\n\t"
    "v_cvt_pk_bf16_f32 v28, v64, v65\n\t"
    "v_mul_f32_e32 v66, v66, v22\n\t"
    "v_mfma_f32_16x16x32_bf16 v[12:15], a[212:215], v[188:191], v[12:15]\n\t"
    "v_mul_f32_e32 v67, v67, v22\n\t"
    "v_cvt_pk_bf16_f32 v29, v66, v67\n\t"
    "v_mfma_f32_16x16x32_bf16 v[12:15], a[220:223], v[184:187], v[12:15]\n\t"
    "v_cvt_pk_bf16_f32 v32, v16, v17\n\t"
    "v_cvt_pk_bf16_f32 v33, v18, v19\n\t"
    "v_mfma_f32_16x16x32_bf16 v[12:15], a[228:231], v[180:183], v[12:15]\n\t"
    "v_mul_f32_e32 v48, v48, v22\n\t"
    "v_mul_f32_e32 v49, v49, v22\n\t"
    "v_mfma_f32_16x16x32_bf16 v[12:15], a[236:239], v[176:179], v[12:15]\n\t"
    "buffer_load_dwordx4 v7, s[52:55], s61 offen offset:2048 nt lds\n\t"
    "v_cvt_pk_bf16_f32 v30, v48, v49\n\t"
    "v_mfma_f32_16x16x32_bf16 v[16:19], a[240:243], v[196:199], 0\n\t"
    "s_add_u32 s61, s61, 196608\n\t"
    "v_mul_f32_e32 v50, v50, v22\n\t"
    "v_mfma_f32_16x16x32_bf16 v[16:19], a[248:251], v[192:195], v[16:19]\n\t"
    "v_mul_f32_e32 v51, v51, v22\n\t"
    "v_cvt_pk_bf16_f32 v31, v50, v51\n\t"
    "v_mfma_f32_16x16x32_bf16 v[16:19], v[224:227], v[188:191], v[16:19]\n\t"
    "v_mul_f32_e32 v68, v68, v22\n\t"
    "v_mul_f32_e32 v69, v69, v22\n\t"
    "v_mfma_f32_16x16x32_bf16 v[16:19], v[232:235], v[184:187], v[16:19]\n\t"
    "v_cvt_pk_bf16_f32 v34, v12, v13\n\t"
    "v_cvt_pk_bf16_f32 v35, v14, v15\n\t"
    "v_mfma_f32_16x16x32_bf16 v[16:19], v[240:243], v[180:183], v[16:19]\n\t"
    "v_cvt_pk_bf16_f32 v48, v68, v69\n\t"
    "v_mul_f32_e32 v70, v70, v22\n\t"
    "v_mfma_f32_16x16x32_bf16 v[16:19], v[248:251], v[176:179], v[16:19]\n\t"
    "v_mul_f32_e32 v71, v71, v22\n\t"
    "v_cvt_pk_bf16_f32 v49, v70, v71\n\t"
    "v_mfma_f32_16x16x32_bf16 v[12:15], a[244:247], v[196:199], 0\n\t"
    "v_mul_f32_e32 v72, v72, v22\n\t"
    "v_mul_f32_e32 v73, v73, v22\n\t"
    "v_mfma_f32_16x16x32_bf16 v[12:15], a[252:255], v[192:195], v[12:15]\n\t"
    "v_cvt_pk_bf16_f32 v50, v72, v73\n\t"
    "v_mul_f32_e32 v74, v74, v22\n\t"
    "v_mfma_f32_16x16x32_bf16 v[12:15], v[228:231], v[188:191], v[12:15]\n\t"
    "v_mul_f32_e32 v75, v75, v22\n\t"
    "v_cvt_pk_bf16_f32 v51, v74, v75\n\t"
    "v_mfma_f32_16x16x32_bf16 v[12:15], v[236:239], v[184:187], v[12:15]\n\t"
    "v_cvt_pk_bf16_f32 v56, v16, v17\n\t"
    "v_cvt_pk_bf16_f32 v57, v18, v19\n\t"
    "v_mfma_f32_16x16x32_bf16 v[12:15], v[244:247], v[180:183], v[12:15]\n\t"
    "v_mfma_f32_16x16x32_bf16 v[12:15], v[252:255], v[176:179], v[12:15]\n\t"
    "v_mfma_f32_16x16x32_bf16 v[16:19], a[0:3], v[172:175], 0\n\t"
    "ds_write_b128 v6, v[28:31] offset:0\n\t"
    "v_mfma_f32_16x16x32_bf16 v[16:19], a[8:11], v[168:171], v[16:19]\n\t"
    "s_branch .Lmid_%=\n\t"
    ".Ltail_%=:\n\t"
    "v_mbcnt_lo_u32_b32 v8, -1, 0\n\t"
    "v_mbcnt_hi_u32_b32 v8, -1, v8\n\t"
    "v_lshlrev_b32_e32 v8, 5, v8\n\t"
    "s_nop 7\n\t"
    "s_nop 1\n\t"
    "ds_write_b128 v6, v[44:47] offset:4096\n\t"
    "v_cvt_pk_bf16_f32 v62, v56, v57\n\t"
    "ds_write_b128 v6, v[52:55] offset:5120\n\t"
    "v_cvt_pk_bf16_f32 v63, v58, v59\n\t"
    "s_nop 1\n\t"
    "v_mfma_f32_16x16x32_bf16 v[12:15], v[208:211], v[40:43], v[12:15]\n\t"
    "s_nop 1\n\t"
    "v_mfma_f32_16x16x32_bf16 v[12:15], v[204:207], v[48:51], v[12:15]\n\t"
    "s_nop 1\n\t"
    "v_mfma_f32_16x16x32_bf16 v[12:15], v[200:203], v[60:63], v[12:15]\n\t"
    "s_nop 1\n\t"
    "v_mfma_f32_16x16x32_bf16 v[16:19], v[184:187], v[40:43], v[16:19]\n\t"
    "s_nop 1\n\t"
    "v_mfma_f32_16x16x32_bf16 v[16:19], v[180:183], v[48:51], v[16:19]\n\t"
    "s_waitcnt lgkmcnt(0)\n\t"
    "s_barrier\n\t"
    "s_nop 1\n\t"
    "v_mfma_f32_16x16x32_bf16 v[16:19], v[176:179], v[60:63], v[16:19]\n\t"
    "s_nop 1\n\t"
    "v_mfma_f32_16x16x32_bf16 v[20:23], v[160:163], v[40:43], v[20:23]\n\t"
    "v_max3_f32 v52, v12, v13, v14\n\t"
    "s_nop 1\n\t"
    "v_mfma_f32_16x16x32_bf16 v[20:23], v[156:159], v[48:51], v[20:23]\n\t"
    "s_nop 1\n\t"
    "v_mfma_f32_16x16x32_bf16 v[20:23], v[152:155], v[60:63], v[20:23]\n\t"
    "s_nop 1\n\t"
    "v_mfma_f32_16x16x32_bf16 v[24:27], v[136:139], v[40:43], v[24:27]\n\t"
    "v_max3_f32 v53, v15, v16, v17\n\t"
    "v_max3_f32 v52, v52, v18, v19\n\t"
    "s_nop 1\n\t"
    "v_mfma_f32_16x16x32_bf16 v[24:27], v[132:135], v[48:51], v[24:27]\n\t"
    "s_nop 1\n\t"
    "v_mfma_f32_16x16x32_bf16 v[24:27], v[128:131], v[60:63], v[24:27]\n\t"
    "s_nop 7\n\t"
    "s_nop 1\n\t"
    "v_max3_f32 v53, v53, v20, v21\n\t"
    "v_max3_f32 v52, v52, v22, v23\n\t"
    "v_max3_f32 v53, v53, v24, v25\n\t"
    "v_max3_f32 v52, v52, v26, v27\n\t"
    "v_max_f32_e32 v52, v52, v53\n\t"
    "v_mov_b32_e32 v53, v52\n\t"
    "s_nop 1\n\t"
    "v_permlane32_swap_b32 v52, v53\n\t"
    "v_max_f32_e32 v52, v52, v53\n\t"
    "v_mov_b32_e32 v53, v52\n\t"
    "s_nop 1\n\t"
    "v_permlane16_swap_b32 v52, v53\n\t"
    "v_max_f32_e32 v52, v52, v53\n\t"
    "v_mul_f32_e32 v52, s68, v52\n\t"
    "v_fma_f32 v12, v12, s68, -v52\n\t"
    "v_fma_f32 v13, v13, s68, -v52\n\t"
    "v_exp_f32_e32 v12, v12\n\t"
    "s_nop 0\n\t"
    "v_fma_f32 v14, v14, s68, -v52\n\t"
    "v_fma_f32 v15, v15, s68, -v52\n\t"
    "v_fma_f32 v16, v16, s68, -v52\n\t"
    "v_fma_f32 v17, v17, s68, -v52\n\t"
    "v_exp_f32_e32 v13, v13\n\t"
    "s_nop 0\n\t"
    "v_add_f32_e32 v54, v12, v13\n\t"
    "v_fma_f32 v18, v18, s68, -v52\n\t"
    "v_exp_f32_e32 v14, v14\n\t"
    "s_nop 0\n\t"
    "v_exp_f32_e32 v15, v15\n\t"
    "s_nop 0\n\t"
    "v_add_f32_e32 v55, v14, v15\n\t"
    "v_fma_f32 v19, v19, s68, -v52\n\t"
    "v_exp_f32_e32 v16, v16\n\t"
    "s_nop 0\n\t"
    "v_add_f32_e32 v54, v54, v16\n\t"
    "v_fma_f32 v20, v20, s68, -v52\n\t"
    "v_exp_f32_e32 v17, v17\n\t"
    "s_nop 0\n\t"
    "v_add_f32_e32 v54, v54, v17\n\t"
    "v_fma_f32 v21, v21, s68, -v52\n\t"
    "v_exp_f32_e32 v18, v18\n\t"
    "s_nop 0\n\t"
    "v_add_f32_e32 v55, v55, v18\n\t"
    "v_exp_f32_e32 v19, v19\n\t"
    "s_nop 0\n\t"
    "v_add_f32_e32 v55, v55, v19\n\t"
    "v_fma_f32 v22, v22, s68, -v52\n\t"
    "v_exp_f32_e32 v20, v20\n\t"
    "s_nop 0\n\t"
    "v_add_f32_e32 v54, v54, v20\n\t"
    "v_fma_f32 v23, v23, s68, -v52\n\t"
    "v_exp_f32_e32 v21, v21\n\t"
    "s_nop 0\n\t"
    "v_add_f32_e32 v54, v54, v21\n\t"
    "v_exp_f32_e32 v22, v22\n\t"
    "s_nop 0\n\t"
    "v_add_f32_e32 v55, v55, v22\n\t"
    "v_fma_f32 v24, v24, s68, -v52\n\t"
    "v_fma_f32 v25, v25, s68, -v52\n\t"
    "v_exp_f32_e32 v23, v23\n\t"
    "s_nop 0\n\t"
    "v_add_f32_e32 v55, v55, v23\n\t"
    "v_exp_f32_e32 v24, v24\n\t"
    "s_nop 0\n\t"
    "v_add_f32_e32 v54, v54, v24\n\t"
    "v_exp_f32_e32 v25, v25\n\t"
    "s_nop 0\n\t"
    "v_add_f32_e32 v54, v54, v25\n\t"
    "v_fma_f32 v26, v26, s68, -v52\n\t"
    "v_exp_f32_e32 v26, v26\n\t"
    "s_nop 0\n\t"
    "v_add_f32_e32 v55, v55, v26\n\t"
    "v_fma_f32 v27, v27, s68, -v52\n\t"
    "v_exp_f32_e32 v27, v27\n\t"
    "s_nop 0\n\t"
    "v_add_f32_e32 v55, v55, v27\n\t"
    "v_add_f32_e32 v54, v54, v55\n\t"
    "v_mov_b32_e32 v55, v54\n\t"
    "s_nop 1\n\t"
    "v_permlane32_swap_b32 v54, v55\n\t"
    "v_add_f32_e32 v54, v54, v55\n\t"
    "v_mov_b32_e32 v55, v54\n\t"
    "s_nop 1\n\t"
    "v_permlane16_swap_b32 v54, v55\n\t"
    "v_add_f32_e32 v54, v54, v55\n\t"
    "v_rcp_f32_e32 v54, v54\n\t"
    "s_nop 0\n\t"
    "v_mul_f32_e32 v12, v12, v54\n\t"
    "v_mul_f32_e32 v13, v13, v54\n\t"
    "v_cvt_pk_bf16_f32 v76, v12, v13\n\t"
    "v_mul_f32_e32 v14, v14, v54\n\t"
    "v_mul_f32_e32 v15, v15, v54\n\t"
    "v_cvt_pk_bf16_f32 v77, v14, v15\n\t"
    "v_mul_f32_e32 v16, v16, v54\n\t"
    "v_mul_f32_e32 v17, v17, v54\n\t"
    "v_cvt_pk_bf16_f32 v78, v16, v17\n\t"
    "v_mul_f32_e32 v18, v18, v54\n\t"
    "v_mul_f32_e32 v19, v19, v54\n\t"
    "v_cvt_pk_bf16_f32 v79, v18, v19\n\t"
    "v_mul_f32_e32 v20, v20, v54\n\t"
    "v_mul_f32_e32 v21, v21, v54\n\t"
    "v_cvt_pk_bf16_f32 v56, v20, v21\n\t"
    "v_mul_f32_e32 v22, v22, v54\n\t"
    "v_mul_f32_e32 v23, v23, v54\n\t"
    "v_cvt_pk_bf16_f32 v57, v22, v23\n\t"
    "v_mul_f32_e32 v24, v24, v54\n\t"
    "v_mul_f32_e32 v25, v25, v54\n\t"
    "v_cvt_pk_bf16_f32 v58, v24, v25\n\t"
    "v_mul_f32_e32 v26, v26, v54\n\t"
    "ds_write_b128 v6, v[76:79] offset:6144\n\t"
    "v_mul_f32_e32 v27, v27, v54\n\t"
    "v_cvt_pk_bf16_f32 v59, v26, v27\n\t"
    "ds_write_b128 v6, v[56:59] offset:7168\n\t"
    "s_waitcnt lgkmcnt(0)\n\t"
    "s_barrier\n\t"
    "s_cmp_lg_u32 %[closer], 0\n\t"
    "s_cbranch_scc1 .Lx2_%=\n\t"
    "s_mov_b32 s60, 0x10000\n\t"
    "buffer_load_dwordx2 v[10:11], off, s[56:59], %[eoff] sc1\n\t"
    ".Lp3_%=:\n\t"
    "buffer_load_dwordx4 v[12:15], v8, s[56:59], %[aoff] offen sc1\n\t"
    "buffer_load_dwordx4 v[16:19], v8, s[56:59], %[aoff] offen offset:16 sc1\n\t"
    "s_waitcnt vmcnt(0)\n\t"
    "v_add_co_u32_e32 v20, vcc, 0x7f4a7c15, v10\n\t"
    "s_nop 1\n\t"
    "v_mov_b32_e32 v9, 0x9e3779b9\n\t"
    "v_addc_co_u32_e32 v21, vcc, v9, v11, vcc\n\t"
    "v_cmp_eq_u64_e32 vcc, v[12:13], v[20:21]\n\t"
    "v_cmp_eq_u64_e64 s[66:67], v[14:15], v[20:21]\n\t"
    "s_and_b64 vcc, vcc, s[66:67]\n\t"
    "v_cmp_eq_u64_e64 s[66:67], v[16:17], v[20:21]\n\t"
    "s_and_b64 vcc, vcc, s[66:67]\n\t"
    "v_cmp_eq_u64_e64 s[66:67], v[18:19], v[20:21]\n\t"
    "s_and_b64 vcc, vcc, s[66:67]\n\t"
    "s_cmp_eq_u64 vcc, exec\n\t"
    "s_cbranch_scc1 .Lk4_%=\n\t"
    "s_sleep 8\n\t"
    "s_sub_u32 s60, s60, 1\n\t"
    "s_cmp_lg_u32 s60, 0\n\t"
    "s_cbranch_scc1 .Lp3_%=\n\t"
    ".Lk4_%=:\n\t"
    "s_mov_b64 exec, 1\n\t"
    "buffer_store_dwordx2 v[20:21], off, s[56:59], %[eoff] sc1\n\t"
    "s_mov_b64 exec, -1\n\t"
    ".Lx2_%=:\n\t"
    "s_endpgm\n\t"
    ".Lend_%=:\n\t"
    :: "{v31}"(lane), "{s[36:43]}"(k8), "{s[44:47]}"(k4),
       [role] "s"(role), [pair] "s"(pair), [prod] "s"((pair * 256 + bid - 144) >> 31), [closer] "s"(bid | wave), [bid] "s"(bid), [wb] "s"(wb),
       [rbeg] "s"(rbeg), [rend] "s"(rbeg + 3 * SLOT), [pl] "s"(ldsb + PB + pair * 8192), [amask] "s"((unsigned long long)pair),
       [tch] "s"(((bid >> 3) * 2 + pair) * 1152), [foff] "s"(F_OFF + 128 * bid), [aoff] "s"(A_OFF), [aown] "s"(A_OFF + 8 * bid), [eoff] "s"(E_OFF)
    : "memory", "scc", ACLOB, VCLOB, SCLOB);
}

extern "C" void kernel_launch(void* const* d_in, const int* in_sizes, int n_in,
                              void* d_out, int out_size, void* d_ws, size_t ws_size,
                              hipStream_t stream) {
  attn_kernel<<<256, 256, 0, stream>>>((const unsigned char*)d_in[0], (const unsigned char*)d_in[1], (const unsigned char*)d_in[2],
                                       (unsigned char*)d_ws, (unsigned char*)d_out
      );
}
```

```cpp
#include <hip/hip_runtime.h>
#include <hip/hip_bf16.h>
#include <stdint.h>

using u32x4 = __attribute__((ext_vector_type(4))) unsigned;
using u32x8 = __attribute__((ext_vector_type(8))) unsigned;
#define DI __device__ __forceinline__

static constexpr int ROWB = 512 * 384;
static constexpr int STEPB = 64 * ROWB;
static constexpr unsigned XBYTES = 512u * ROWB;
static constexpr int SLOT = 24576;
static constexpr int PB = 6 * SLOT;
static constexpr int F_OFF = 160 * 1024, A_OFF = 148 * 1024, E_OFF = 152 * 1024;

#define R10(p, n) p #n "0", p #n "1", p #n "2", p #n "3", p #n "4", p #n "5", p #n "6", p #n "7", p #n "8", p #n "9"
#define R240(p) R10(p, 4), R10(p, 5), R10(p, 6), R10(p, 7), R10(p, 8), R10(p, 9), R10(p, 10), R10(p, 11), R10(p, 12), R10(p, 13), \
  R10(p, 14), R10(p, 15), R10(p, 16), R10(p, 17), R10(p, 18), R10(p, 19), R10(p, 20), R10(p, 21), R10(p, 22), R10(p, 23), R10(p, 24), \
  p "250", p "251", p "252", p "253", p "254", p "255"
#define ACLOB R10("a", ), R10("a", 1), R10("a", 2), R10("a", 3), R240("a")
#define VCLOB R10("v", ), R10("v", 1), R10("v", 2), "v30", "v32", "v33", "v34", "v35", "v36", "v37", "v38", "v39", R240("v")
#define SCLOB "s48", "s49", R10("s", 5), R10("s", 6), R10("s", 7), R10("s", 8), "s90", "s91", "s92", "s93", "s94", "s95", "s96", "s97", "vcc"

__global__ __launch_bounds__(256, 1) void attn_kernel(const unsigned char* __restrict__ x,
                                                      const unsigned char* __restrict__ wqkv,
                                                      const unsigned char* __restrict__ wo,
                                                      unsigned char* ws,
                                                      unsigned char* __restrict__ out
                                                      ) {
  __shared__ u32x4 lds[10240];
  u32x8 k8; u32x4 k4;
  asm volatile(
    "s_load_dwordx8 s[36:43], %2, 0\n\t"
    "s_load_dwordx2 s[44:45], %2, 32\n\t"
    : "={s[36:43]}"(k8), "={s[44:47]}"(k4) : "s"(__builtin_amdgcn_kernarg_segment_ptr()) : "memory", ACLOB);
  __builtin_amdgcn_sched_barrier(0);
  const unsigned lane = threadIdx.x & 63, wave = __builtin_amdgcn_readfirstlane(threadIdx.x >> 6);
  const unsigned role = wave >> 1, pair = wave & 1;
  const unsigned ldsb = (unsigned)(size_t)lds;
  const unsigned bid = blockIdx.x;
  const unsigned wb = (bid & 7) * 8 * ROWB + ((bid >> 3) * 2 + pair) * 8 * 384;
  const unsigned rbeg = ldsb + pair * 3 * SLOT;

  asm volatile(
    ".Lbeg_%=:\n\t"
    "s_cmp_lg_u32 %[role], 1\n\t"
    "s_cbranch_scc1 .Lroln_%=\n\t"
    "s_cmp_eq_u32 %[prod], 0\n\t"
    "s_cbranch_scc1 .Lnp_%=\n\t"
    "s_cmp_lt_u32 %[bid], 72\n\t"
    "s_cselect_b32 s71, 0, 72\n\t"
    "s_cselect_b32 s63, 0, 768\n\t"
    "s_sub_u32 s64, %[bid], s71\n\t"
    "s_mul_i32 s65, s64, 0x1556\n\t"
    "s_lshr_b32 s65, s65, 16\n\t"
    "s_mul_i32 s69, s65, 12\n\t"
    "s_sub_u32 s69, s64, s69\n\t"
    "s_lshr_b32 s69, s69, 1\n\t"
    "s_and_b32 s70, s64, 1\n\t"
    "s_mul_i32 s60, s69, 36864\n\t"
    "s_add_u32 s60, s60, s63\n\t"
    "s_add_u32 s61, s60, 4608\n\t"
    "v_lshrrev_b32_e32 v20, 2, v31\n\t"
    "v_and_b32_e32 v21, 8, v20\n\t"
    "v_bfe_u32 v24, v31, 3, 2\n\t"
    "v_or_b32_e32 v21, v21, v24\n\t"
    "v_mul_u32_u24_e32 v21, 0x480, v21\n\t"
    "v_lshrrev_b32_e32 v24, 4, v31\n\t"
    "v_xor_b32_e32 v24, v24, v31\n\t"
    "v_and_b32_e32 v24, 7, v24\n\t"
    "v_lshl_or_b32 v24, v24, 4, v21\n\t"
    "v_xor_b32_e32 v25, 64, v24\n\t"
    "v_add_u32_e32 v25, 0x4800, v25\n\t"
    "v_mul_u32_u24_e32 v27, 0x180, v20\n\t"
    "v_and_b32_e32 v28, 3, v31\n\t"
    "v_lshl_or_b32 v27, v28, 4, v27\n\t"
    "s_lshl_b32 s62, s65, 3\n\t"
    "s_add_u32 s62, s62, s70\n\t"
    "s_mul_i32 s62, s62, 4608\n\t"
    "s_add_u32 s62, s62, 384\n\t"
    "s_lshl_b32 s63, s65, 6\n\t"
    "s_mov_b32 s50, 0x36000\n\t"
    "s_mov_b32 s51, 0x20000\n\t"
    "s_mov_b32 s54, 0x12000\n\t"
    "s_mov_b32 s55, 0x20000\n\t"
    "s_waitcnt lgkmcnt(0)\n\t"
    "s_mov_b32 s48, s38\n\t"
    "s_and_b32 s49, s39, 0xffff\n\t"
    "s_cmp_lg_u32 s71, 0\n\t"
    "s_cbranch_scc1 .Lpo_%=\n\t"
    "s_mov_b32 m0, 61440\n\t"
    "s_nop 0\n\t"
    "buffer_load_dwordx4 v24, s[48:51], s62 offen offset:0 lds\n\t"
    "s_mov_b32 m0, 62464\n\t"
    "s_nop 0\n\t"
    "buffer_load_dwordx4 v25, s[48:51], s62 offen offset:0 lds\n\t"
    "s_mov_b32 m0, 63360\n\t"
    "s_nop 0\n\t"
    "buffer_load_dwordx4 v24, s[48:51], s62 offen offset:128 lds\n\t"
    "s_mov_b32 m0, 64384\n\t"
    "s_nop 0\n\t"
    "buffer_load_dwordx4 v25, s[48:51], s62 offen offset:128 lds\n\t"
    "s_mov_b32 m0, 65280\n\t"
    "s_nop 0\n\t"
    "buffer_load_dwordx4 v24, s[48:51], s62 offen offset:256 lds\n\t"
    "s_mov_b32 m0, 66304\n\t"
    "s_nop 0\n\t"
    "buffer_load_dwordx4 v25, s[48:51], s62 offen offset:256 lds\n\t"
    "s_branch .Lpa_%=\n\t"
    ".Lpo_%=:\n\t"
    "s_mov_b32 s52, s40\n\t"
    "s_mov_b32 m0, 61440\n\t"
    "s_and_b32 s53, s41, 0xffff\n\t"
    "buffer_load_dwordx4 v27, s[52:55], s63 offen lds\n\t"
    "s_mov_b32 m0, 62464\n\t"
    "s_add_u32 s63, s63, 6144\n\t"
    "buffer_load_dwordx4 v27, s[52:55], s63 offen lds\n\t"
    "s_mov_b32 m0, 63488\n\t"
    "s_add_u32 s63, s63, 6144\n\t"
    "buffer_load_dwordx4 v27, s[52:55], s63 offen lds\n\t"
    "s_mov_b32 m0, 64512\n\t"
    "s_add_u32 s63, s63, 6144\n\t"
    "buffer_load_dwordx4 v27, s[52:55], s63 offen lds\n\t"
    "s_mov_b32 m0, 65536\n\t"
    "s_add_u32 s63, s63, 6144\n\t"
    "buffer_load_dwordx4 v27, s[52:55], s63 offen lds\n\t"
    "s_mov_b32 m0, 66560\n\t"
    "s_add_u32 s63, s63, 6144\n\t"
    "buffer_load_dwordx4 v27, s[52:55], s63 offen lds\n\t"
    "s_mov_b32 m0, 67584\n\t"
    "s_add_u32 s63, s63, 6144\n\t"
    "buffer_load_dwordx4 v27, s[52:55], s63 offen lds\n\t"
    "s_mov_b32 m0, 68608\n\t"
    "s_add_u32 s63, s63, 6144\n\t"
    "buffer_load_dwordx4 v27, s[52:55], s63 offen lds\n\t"
    "s_mov_b32 m0, 69632\n\t"
    "s_add_u32 s63, s63, 6144\n\t"
    "buffer_load_dwordx4 v27, s[52:55], s63 offen lds\n\t"
    "s_mov_b32 m0, 70656\n\t"
    "s_add_u32 s63, s63, 6144\n\t"
    "buffer_load_dwordx4 v27, s[52:55], s63 offen lds\n\t"
    "s_mov_b32 m0, 71680\n\t"
    "s_add_u32 s63, s63, 6144\n\t"
    "buffer_load_dwordx4 v27, s[52:55], s63 offen lds\n\t"
    "s_mov_b32 m0, 72704\n\t"
    "s_add_u32 s63, s63, 6144\n\t"
    "buffer_load_dwordx4 v27, s[52:55], s63 offen lds\n\t"
    ".Lpa_%=:\n\t"
    "s_mov_b32 m0, 49152\n\t"
    "s_nop 0\n\t"
    "buffer_load_dwordx4 v24, s[48:51], s60 offen offset:0 lds\n\t"
    "s_mov_b32 m0, 53120\n\t"
    "s_nop 0\n\t"
    "buffer_load_dwordx4 v24, s[48:51], s60 offen offset:128 lds\n\t"
    "s_mov_b32 m0, 57088\n\t"
    "s_nop 0\n\t"
    "buffer_load_dwordx4 v24, s[48:51], s60 offen offset:256 lds\n\t"
    "s_mov_b32 m0, 51200\n\t"
    "s_nop 0\n\t"
    "buffer_load_dwordx4 v25, s[48:51], s60 offen offset:0 lds\n\t"
    "s_mov_b32 m0, 55168\n\t"
    "s_nop 0\n\t"
    "buffer_load_dwordx4 v25, s[48:51], s60 offen offset:128 lds\n\t"
    "s_mov_b32 m0, 59136\n\t"
    "s_nop 0\n\t"
    "buffer_load_dwordx4 v25, s[48:51], s60 offen offset:256 lds\n\t"
    "s_mov_b32 m0, 50176\n\t"
    "s_nop 0\n\t"
    "buffer_load_dwordx4 v24, s[48:51], s61 offen offset:0 lds\n\t"
    "s_mov_b32 m0, 54144\n\t"
    "s_nop 0\n\t"
    "buffer_load_dwordx4 v24, s[48:51], s61 offen offset:128 lds\n\t"
    "s_mov_b32 m0, 58112\n\t"
    "s_nop 0\n\t"
    "buffer_load_dwordx4 v24, s[48:51], s61 offen offset:256 lds\n\t"
    "s_mov_b32 m0, 52224\n\t"
    "s_nop 0\n\t"
    "buffer_load_dwordx4 v25, s[48:51], s61 offen offset:0 lds\n\t"
    "s_mov_b32 m0, 56192\n\t"
    "s_nop 0\n\t"
    "buffer_load_dwordx4 v25, s[48:51], s61 offen offset:128 lds\n\t"
    "s_mov_b32 m0, 60160\n\t"
    "s_nop 0\n\t"
    "buffer_load_dwordx4 v25, s[48:51], s61 offen offset:256 lds\n\t"
    ".Lnp_%=:\n\t"
    "s_waitcnt lgkmcnt(0)\n\t"
    "s_mov_b32 s56, s42\n\t"
    "s_and_b32 s57, s43, 0xffff\n\t"
    "s_mov_b32 s58, 0x40000\n\t"
    "s_mov_b32 s59, 0x20000\n\t"
    "buffer_load_dwordx2 v[22:23], off, s[56:59], %[eoff] sc1\n\t"
    "s_barrier\n\t"
    "v_lshlrev_b32_e32 v30, 4, v31\n\t"
    "v_and_b32_e32 v28, 7, v31\n\t"
    "v_bfe_u32 v27, v31, 3, 1\n\t"
    "v_lshrrev_b32_e32 v29, 4, v31\n\t"
    "v_add_u32_e32 v6, %[pl], v30\n\t"
    "v_mul_u32_u24_e32 v27, 0xc00, v27\n\t"
    "v_add_u32_e32 v27, %[rbeg], v27\n\t"
    "v_lshlrev_b32_e32 v26, 7, v31\n\t"
    "v_mad_u32_u24 v20, v28, 3, 0\n\t"
    "v_and_b32_e32 v21, 6, v20\n\t"
    "v_xor_b32_e32 v21, v21, v29\n\t"
    "v_lshl_add_u32 v20, v20, 7, v27\n\t"
    "v_lshl_add_u32 v0, v21, 4, v20\n\t"
    "v_xor_b32_e32 v1, 64, v0\n\t"
    "v_mad_u32_u24 v20, v28, 3, 1\n\t"
    "v_and_b32_e32 v21, 6, v20\n\t"
    "v_xor_b32_e32 v21, v21, v29\n\t"
    "v_lshl_add_u32 v20, v20, 7, v27\n\t"
    "v_lshl_add_u32 v2, v21, 4, v20\n\t"
    "v_xor_b32_e32 v3, 64, v2\n\t"
    "v_mad_u32_u24 v20, v28, 3, 2\n\t"
    "v_and_b32_e32 v21, 6, v20\n\t"
    "v_xor_b32_e32 v21, v21, v29\n\t"
    "v_lshl_add_u32 v20, v20, 7, v27\n\t"
    "v_lshl_add_u32 v4, v21, 4, v20\n\t"
    "v_xor_b32_e32 v5, 64, v4\n\t"
    "s_cmp_eq_u32 %[prod], 0\n\t"
    "s_cbranch_scc1 .Lnq_%=\n\t"
    "v_bfe_u32 v20, v31, 1, 3\n\t"
    "v_xor_b32_e32 v20, v20, v29\n\t"
    "v_lshlrev_b32_e32 v20, 4, v20\n\t"
    "v_lshl_or_b32 v20, v28, 7, v20\n\t"
    "v_bfe_u32 v21, v31, 3, 1\n\t"
    "v_lshl_or_b32 v16, v21, 11, v20\n\t"
    "v_add_u32_e32 v16, 0xc000, v16\n\t"
    "v_xor_b32_e32 v17, 64, v16\n\t"
    "s_cmp_lg_u32 s71, 0\n\t"
    "s_cbranch_scc1 .Lqo_%=\n\t"
    "v_lshl_or_b32 v18, v21, 10, v20\n\t"
    "v_add_u32_e32 v18, 0xf000, v18\n\t"
    "v_xor_b32_e32 v19, 64, v18\n\t"
    "s_mul_i32 s60, s70, 6\n\t"
    "s_add_u32 s60, s60, s65\n\t"
    "s_mul_i32 s60, s60, 6\n\t"
    "s_add_u32 s60, s60, s69\n\t"
    "s_lshl_b32 s60, s60, 10\n\t"
    "v_add_u32_e32 v20, s60, v30\n\t"
    "v_mov_b32_e32 v24, v30\n\t"
    "s_waitcnt vmcnt(13)\n\t"
    "ds_read_b128 v[80:83], v18 offset:0\n\t"
    "ds_read_b128 v[84:87], v19 offset:0\n\t"
    "ds_read_b128 v[88:91], v18 offset:2048\n\t"
    "ds_read_b128 v[92:95], v19 offset:2048\n\t"
    "ds_read_b128 v[96:99], v18 offset:4096\n\t"
    "ds_read_b128 v[100:103], v19 offset:4096\n\t"
    "s_branch .Lqa_%=\n\t"
    ".Lqo_%=:\n\t"
    "s_and_b32 s63, s65, 1\n\t"
    "s_lshl_b32 s63, s63, 1\n\t"
    "s_add_u32 s63, s63, s70\n\t"
    "s_lshl_b32 s64, s70, 2\n\t"
    "s_lshl_b32 s62, s70, 4\n\t"
    "s_cmp_lt_u32 s65, 4\n\t"
    "s_cselect_b32 s60, s64, 8\n\t"
    "s_cselect_b32 s63, s65, s63\n\t"
    "s_cselect_b32 s62, s64, s62\n\t"
    "s_cselect_b32 s61, 4, 3\n\t"
    "s_lshl_b32 s62, s62, 1\n\t"
    "s_add_u32 s62, s62, 61440\n\t"
    "s_mul_i32 s60, s60, 6\n\t"
    "s_add_u32 s60, s60, s69\n\t"
    "s_lshl_b32 s60, s60, 2\n\t"
    "s_add_u32 s60, s60, s63\n\t"
    "s_lshl_b32 s60, s60, 8\n\t"
    "s_add_u32 s60, s60, 73728\n\t"
    "v_and_b32_e32 v25, 3, v31\n\t"
    "v_bfe_u32 v21, v31, 2, 2\n\t"
    "v_lshlrev_b32_e32 v18, s61, v25\n\t"
    "v_lshl_add_u32 v18, v21, 6, v18\n\t"
    "v_lshl_add_u32 v18, v29, 9, v18\n\t"
    "v_add_u32_e32 v18, s62, v18\n\t"
    "v_lshl_add_u32 v24, v25, 2, v21\n\t"
    "v_lshl_add_u32 v24, v29, 4, v24\n\t"
    "v_lshlrev_b32_e32 v24, 4, v24\n\t"
    "v_mul_u32_u24_e32 v21, 0x1800, v21\n\t"
    "v_lshl_add_u32 v20, v25, 4, v21\n\t"
    "v_lshl_add_u32 v20, v29, 6, v20\n\t"
    "v_add_u32_e32 v20, s60, v20\n\t"
    "s_waitcnt vmcnt(13)\n\t"
    "ds_read_b64_tr_b16 v[80:81], v18 offset:0\n\t"
    "ds_read_b64_tr_b16 v[82:83], v18 offset:256\n\t"
    "ds_read_b64_tr_b16 v[84:85], v18 offset:2048\n\t"
    "ds_read_b64_tr_b16 v[86:87], v18 offset:2304\n\t"
    "ds_read_b64_tr_b16 v[88:89], v18 offset:4096\n\t"
    "ds_read_b64_tr_b16 v[90:91], v18 offset:4352\n\t"
    "ds_read_b64_tr_b16 v[92:93], v18 offset:6144\n\t"
    "ds_read_b64_tr_b16 v[94:95], v18 offset:6400\n\t"
    "ds_read_b64_tr_b16 v[96:97], v18 offset:8192\n\t"
    "ds_read_b64_tr_b16 v[98:99], v18 offset:8448\n\t"
    "ds_read_b64_tr_b16 v[100:101], v18 offset:10240\n\t"
    "ds_read_b64_tr_b16 v[102:103], v18 offset:10496\n\t"
    ".Lqa_%=:\n\t"
    "s_waitcnt vmcnt(7)\n\t"
    "ds_read_b128 v[32:35], v16 offset:0\n\t"
    "ds_read_b128 v[36:39], v17 offset:0\n\t"
    "ds_read_b128 v[40:43], v16 offset:4096\n\t"
    "ds_read_b128 v[44:47], v17 offset:4096\n\t"
    "ds_read_b128 v[48:51], v16 offset:8192\n\t"
    "ds_read_b128 v[52:55], v17 offset:8192\n\t"
    "s_waitcnt vmcnt(1)\n\t"
    "ds_read_b128 v[56:59], v16 offset:1024\n\t"
    "ds_read_b128 v[60:63], v17 offset:1024\n\t"
    "ds_read_b128 v[64:67], v16 offset:5120\n\t"
    "ds_read_b128 v[68:71], v17 offset:5120\n\t"
    "ds_read_b128 v[72:75], v16 offset:9216\n\t"
    "ds_read_b128 v[76:79], v17 offset:9216\n\t"
    "s_waitcnt lgkmcnt(11)\n\t"
    "v_mfma_f32_16x16x32_bf16 v[108:111], v[32:35], v[80:83], 0\n\t"
    "s_waitcnt lgkmcnt(10)\n\t"
    "v_mfma_f32_16x16x32_bf16 v[108:111], v[36:39], v[84:87], v[108:111]\n\t"
    "s_waitcnt lgkmcnt(9)\n\t"
    "v_mfma_f32_16x16x32_bf16 v[108:111], v[40:43], v[88:91], v[108:111]\n\t"
    "s_waitcnt lgkmcnt(8)\n\t"
    "v_mfma_f32_16x16x32_bf16 v[108:111], v[44:47], v[92:95], v[108:111]\n\t"
    "s_waitcnt lgkmcnt(7)\n\t"
    "v_mfma_f32_16x16x32_bf16 v[108:111], v[48:51], v[96:99], v[108:111]\n\t"
    "s_waitcnt lgkmcnt(6)\n\t"
    "v_mfma_f32_16x16x32_bf16 v[108:111], v[52:55], v[100:103], v[108:111]\n\t"
    "s_waitcnt lgkmcnt(5)\n\t"
    "v_mfma_f32_16x16x32_bf16 v[112:115], v[56:59], v[80:83], 0\n\t"
    "s_waitcnt lgkmcnt(4)\n\t"
    "v_mfma_f32_16x16x32_bf16 v[112:115], v[60:63], v[84:87], v[112:115]\n\t"
    "s_waitcnt lgkmcnt(3)\n\t"
    "v_mfma_f32_16x16x32_bf16 v[112:115], v[64:67], v[88:91], v[112:115]\n\t"
    "s_waitcnt lgkmcnt(2)\n\t"
    "v_mfma_f32_16x16x32_bf16 v[112:115], v[68:71], v[92:95], v[112:115]\n\t"
    "s_waitcnt lgkmcnt(1)\n\t"
    "v_mfma_f32_16x16x32_bf16 v[112:115], v[72:75], v[96:99], v[112:115]\n\t"
    "s_waitcnt lgkmcnt(0)\n\t"
    "v_mfma_f32_16x16x32_bf16 v[112:115], v[76:79], v[100:103], v[112:115]\n\t"
    "v_cvt_pk_bf16_f32 v104, v108, v109\n\t"
    "v_cvt_pk_bf16_f32 v105, v110, v111\n\t"
    "s_nop 6\n\t"
    "v_cvt_pk_bf16_f32 v106, v112, v113\n\t"
    "v_cvt_pk_bf16_f32 v107, v114, v115\n\t"
    "ds_write_b128 v24, v[104:107] offset:49152\n\t"
    "ds_read_b128 v[104:107], v30 offset:49152\n\t"
    "s_waitcnt lgkmcnt(0)\n\t"
    "buffer_store_dwordx4 v[104:107], v20, s[56:59], 0 offen sc1\n\t"
    ".Lnq_%=:\n\t"
    "s_waitcnt vmcnt(0)\n\t"
    "v_add_co_u32_e32 v22, vcc, 0x7f4a7c15, v22\n\t"
    "v_mov_b32_e32 v16, 0x9e3779b9\n\t"
    "s_nop 0\n\t"
    "v_addc_co_u32_e32 v23, vcc, v16, v23, vcc\n\t"
    "s_mov_b32 exec_lo, %[prod]\n\t"
    "s_mov_b32 exec_hi, 0\n\t"
    "buffer_store_dwordx2 v[22:23], off, s[56:59], %[foff] sc1\n\t"
    "s_mov_b64 exec, -1\n\t"
    "v_lshrrev_b32_e32 v20, 5, v31\n\t"
    "v_mul_u32_u24_e32 v20, 0x30000, v20\n\t"
    "v_and_b32_e32 v21, 1, v29\n\t"
    "v_mul_u32_u24_e32 v21, 0x600, v21\n\t"
    "v_add_u32_e32 v20, v20, v21\n\t"
    "v_and_b32_e32 v21, 15, v31\n\t"
    "v_lshl_add_u32 v7, v21, 4, v20\n\t"
    "v_lshl_add_u32 v8, v21, 3, v20\n\t"
    "v_add_u32_e32 v8, 0x100, v8\n\t"
    "v_and_b32_e32 v20, 48, v30\n\t"
    "v_lshl_or_b32 v20, v29, 6, v20\n\t"
    "v_lshrrev_b32_e32 v21, 2, v21\n\t"
    "v_lshl_or_b32 v9, v21, 8, v20\n\t"
    "v_min_u32_e32 v21, 35, v31\n\t"
    "v_mul_u32_u24_e32 v21, 64, v21\n\t"
    "v_add_u32_e32 v21, 0x28000, v21\n\t"
    "s_mov_b32 s52, s44\n\t"
    "s_and_b32 s53, s45, 0xffff\n\t"
    "s_mov_b32 s54, 0x6000000\n\t"
    "s_mov_b32 s55, 0x20000\n\t"
    "s_cmp_eq_u32 %[pair], 0\n\t"
    "s_cbranch_scc1 .Lng_%=\n\t"
    "s_sleep 45\n\t"
    "s_mov_b32 s64, 0\n\t"
    "s_mov_b32 s65, 2304\n\t"
    "buffer_load_dwordx2 v[12:13], v21, s[56:59], s64 offen sc1\n\t"
    "buffer_load_dwordx2 v[14:15], v21, s[56:59], s65 offen sc1\n\t"
    "s_waitcnt vmcnt(0)\n\t"
    "s_mov_b32 s60, 0x10000\n\t"
    ".Lp5_%=:\n\t"
    "v_cmp_eq_u64_e32 vcc, v[12:13], v[22:23]\n\t"
    "v_cmp_eq_u64_e64 s[70:71], v[14:15], v[22:23]\n\t"
    "s_and_b64 vcc, vcc, s[70:71]\n\t"
    "s_cmp_eq_u64 vcc, exec\n\t"
    "s_cbranch_scc1 .Lk6_%=\n\t"
    "s_sleep 4\n\t"
    "buffer_load_dwordx2 v[12:13], v21, s[56:59], s64 offen sc1\n\t"
    "buffer_load_dwordx2 v[14:15], v21, s[56:59], s65 offen sc1\n\t"
    "s_sub_u32 s60, s60, 1\n\t"
    "s_cmp_lg_u32 s60, 0\n\t"
    "s_waitcnt vmcnt(0)\n\t"
    "s_cbranch_scc1 .Lp5_%=\n\t"
    ".Lk6_%=:\n\t"
    ".Lng_%=:\n\t"
    "s_mul_i32 s66, %[pair], 36864\n\t"
    "s_mul_i32 s62, %[pair], 73728\n\t"
    "s_add_u32 s62, s62, 36864\n\t"
    "v_add_u32_e32 v11, s62, v30\n\t"
    "s_mov_b32 s63, 73728\n\t"
    "s_barrier\n\t"
    "s_add_u32 s67, %[tch], 0\n\t"
    "s_mov_b64 exec, 0x1ff\n\t"
    "buffer_load_dword v10, v26, s[56:59], s67 offen sc1\n\t"
    "s_mov_b64 exec, -1\n\t"
    "buffer_load_dwordx4 v[32:35], v30, s[56:59], s66 offen offset:0 sc1\n\t"
    "buffer_load_dwordx4 v[36:39], v30, s[56:59], s66 offen offset:1024 sc1\n\t"
    "buffer_load_dwordx4 v[40:43], v30, s[56:59], s66 offen offset:2048 sc1\n\t"
    "buffer_load_dwordx4 v[44:47], v30, s[56:59], s66 offen offset:3072 sc1\n\t"
    "s_add_u32 s66, s66, 4096\n\t"
    "buffer_load_dwordx4 v[48:51], v30, s[56:59], s66 offen offset:0 sc1\n\t"
    "buffer_load_dwordx4 v[52:55], v30, s[56:59], s66 offen offset:1024 sc1\n\t"
    "buffer_load_dwordx4 v[56:59], v30, s[56:59], s66 offen offset:2048 sc1\n\t"
    "buffer_load_dwordx4 v[60:63], v30, s[56:59], s66 offen offset:3072 sc1\n\t"
    "s_add_u32 s66, s66, 4096\n\t"
    "buffer_load_dwordx4 v[64:67], v30, s[56:59], s66 offen offset:0 sc1\n\t"
    "buffer_load_dwordx4 v[68:71], v30, s[56:59], s66 offen offset:1024 sc1\n\t"
    "buffer_load_dwordx4 v[72:75], v30, s[56:59], s66 offen offset:2048 sc1\n\t"
    "buffer_load_dwordx4 v[76:79], v30, s[56:59], s66 offen offset:3072 sc1\n\t"
    "s_add_u32 s66, s66, 4096\n\t"
    "buffer_load_dwordx4 v[80:83], v30, s[56:59], s66 offen offset:0 sc1\n\t"
    "buffer_load_dwordx4 v[84:87], v30, s[56:59], s66 offen offset:1024 sc1\n\t"
    "buffer_load_dwordx4 v[88:91], v30, s[56:59], s66 offen offset:2048 sc1\n\t"
    "buffer_load_dwordx4 v[92:95], v30, s[56:59], s66 offen offset:3072 sc1\n\t"
    "s_add_u32 s66, s66, 4096\n\t"
    "buffer_load_dwordx4 v[96:99], v30, s[56:59], s66 offen offset:0 sc1\n\t"
    "buffer_load_dwordx4 v[100:103], v30, s[56:59], s66 offen offset:1024 sc1\n\t"
    "buffer_load_dwordx4 v[104:107], v30, s[56:59], s66 offen offset:2048 sc1\n\t"
    "buffer_load_dwordx4 v[108:111], v30, s[56:59], s66 offen offset:3072 sc1\n\t"
    "s_add_u32 s66, s66, 4096\n\t"
    "buffer_load_dwordx4 v[112:115], v30, s[56:59], s66 offen offset:0 sc1\n\t"
    "buffer_load_dwordx4 v[116:119], v30, s[56:59], s66 offen offset:1024 sc1\n\t"
    "buffer_load_dwordx4 v[120:123], v30, s[56:59], s66 offen offset:2048 sc1\n\t"
    "buffer_load_dwordx4 v[124:127], v30, s[56:59], s66 offen offset:3072 sc1\n\t"
    "s_add_u32 s66, s66, 4096\n\t"
    "s_mov_b32 s64, 4608\n\t"
    "s_mov_b32 s65, 6912\n\t"
    "buffer_load_dwordx2 v[12:13], v21, s[56:59], s64 offen sc1\n\t"
    "buffer_load_dwordx2 v[14:15], v21, s[56:59], s65 offen sc1\n\t"
    "s_waitcnt vmcnt(25)\n\t"
    "ds_write_b128 v11, v[32:35] offset:0\n\t"
    "buffer_load_dwordx4 v[128:131], v30, s[56:59], s66 offen offset:0 sc1\n\t"
    "s_waitcnt vmcnt(25)\n\t"
    "ds_write_b128 v11, v[36:39] offset:1024\n\t"
    "buffer_load_dwordx4 v[132:135], v30, s[56:59], s66 offen offset:1024 sc1\n\t"
    "s_waitcnt vmcnt(25)\n\t"
    "ds_write_b128 v11, v[40:43] offset:2048\n\t"
    "buffer_load_dwordx4 v[136:139], v30, s[56:59], s66 offen offset:2048 sc1\n\t"
    "s_waitcnt vmcnt(25)\n\t"
    "ds_write_b128 v11, v[44:47] offset:3072\n\t"
    "buffer_load_dwordx4 v[140:143], v30, s[56:59], s66 offen offset:3072 sc1\n\t"
    "s_add_u32 s66, s66, 4096\n\t"
    "s_waitcnt vmcnt(25)\n\t"
    "ds_write_b128 v11, v[48:51] offset:4096\n\t"
    "buffer_load_dwordx4 v[144:147], v30, s[56:59], s66 offen offset:0 sc1\n\t"
    "s_waitcnt vmcnt(25)\n\t"
    "ds_write_b128 v11, v[52:55] offset:5120\n\t"
    "s_waitcnt lgkmcnt(0)\n\t"
    "s_barrier\n\t"
    "buffer_load_dwordx4 v[148:151], v30, s[56:59], s66 offen offset:1024 sc1\n\t"
    "s_waitcnt vmcnt(25)\n\t"
    "ds_write_b128 v11, v[56:59] offset:6144\n\t"
    "buffer_load_dwordx4 v[152:155], v30, s[56:59], s66 offen offset:2048 sc1\n\t"
    "s_waitcnt vmcnt(25)\n\t"
    "ds_write_b128 v11, v[60:63] offset:7168\n\t"
    "buffer_load_dwordx4 v[156:159], v30, s[56:59], s66 offen offset:3072 sc1\n\t"
    "s_add_u32 s66, s66, 4096\n\t"
    "s_waitcnt vmcnt(25)\n\t"
    "ds_write_b128 v11, v[64:67] offset:8192\n\t"
    "buffer_load_dwordx4 v[160:163], v30, s[56:59], s66 offen offset:0 sc1\n\t"
    "s_waitcnt vmcnt(25)\n\t"
    "ds_write_b128 v11, v[68:71] offset:9216\n\t"
    "buffer_load_dwordx4 v[164:167], v30, s[56:59], s66 offen offset:1024 sc1\n\t"
    "s_waitcnt vmcnt(25)\n\t"
    "ds_write_b128 v11, v[72:75] offset:10240\n\t"
    "buffer_load_dwordx4 v[168:171], v30, s[56:59], s66 offen offset:2048 sc1\n\t"
    "s_waitcnt vmcnt(25)\n\t"
    "ds_write_b128 v11, v[76:79] offset:11264\n\t"
    "s_waitcnt lgkmcnt(0)\n\t"
    "s_barrier\n\t"
    "buffer_load_dwordx4 v[172:175], v30, s[56:59], s66 offen offset:3072 sc1\n\t"
    "s_add_u32 s66, s66, 4096\n\t"
    "s_waitcnt vmcnt(25)\n\t"
    "ds_write_b128 v11, v[80:83] offset:12288\n\t"
    "s_waitcnt vmcnt(24)\n\t"
    "ds_write_b128 v11, v[84:87] offset:13312\n\t"
    "s_waitcnt vmcnt(23)\n\t"
    "ds_write_b128 v11, v[88:91] offset:14336\n\t"
    "s_waitcnt vmcnt(22)\n\t"
    "ds_write_b128 v11, v[92:95] offset:15360\n\t"
    "s_waitcnt vmcnt(21)\n\t"
    "ds_write_b128 v11, v[96:99] offset:16384\n\t"
    "s_waitcnt vmcnt(20)\n\t"
    "ds_write_b128 v11, v[100:103] offset:17408\n\t"
    "s_waitcnt vmcnt(19)\n\t"
    "ds_write_b128 v11, v[104:107] offset:18432\n\t"
    "s_waitcnt vmcnt(18)\n\t"
    "ds_write_b128 v11, v[108:111] offset:19456\n\t"
    "s_waitcnt vmcnt(17)\n\t"
    "ds_write_b128 v11, v[112:115] offset:20480\n\t"
    "s_waitcnt vmcnt(16)\n\t"
    "ds_write_b128 v11, v[116:119] offset:21504\n\t"
    "s_waitcnt vmcnt(15)\n\t"
    "ds_write_b128 v11, v[120:123] offset:22528\n\t"
    "s_waitcnt vmcnt(14)\n\t"
    "ds_write_b128 v11, v[124:127] offset:23552\n\t"
    "s_waitcnt lgkmcnt(0)\n\t"
    "s_barrier\n\t"
    "s_waitcnt vmcnt(12)\n\t"
    "s_mov_b32 s60, 0x10000\n\t"
    ".Lp7_%=:\n\t"
    "v_cmp_eq_u64_e32 vcc, v[12:13], v[22:23]\n\t"
    "v_cmp_eq_u64_e64 s[70:71], v[14:15], v[22:23]\n\t"
    "s_and_b64 vcc, vcc, s[70:71]\n\t"
    "s_cmp_eq_u64 vcc, exec\n\t"
    "s_cbranch_scc1 .Lk8_%=\n\t"
    "s_sleep 4\n\t"
    "buffer_load_dwordx2 v[12:13], v21, s[56:59], s64 offen sc1\n\t"
    "buffer_load_dwordx2 v[14:15], v21, s[56:59], s65 offen sc1\n\t"
    "s_sub_u32 s60, s60, 1\n\t"
    "s_cmp_lg_u32 s60, 0\n\t"
    "s_waitcnt vmcnt(0)\n\t"
    "s_cbranch_scc1 .Lp7_%=\n\t"
    ".Lk8_%=:\n\t"
    "s_add_u32 s67, %[tch], 73728\n\t"
    "s_mov_b64 exec, 0x1ff\n\t"
    "buffer_load_dword v10, v26, s[56:59], s67 offen sc1\n\t"
    "s_mov_b64 exec, -1\n\t"
    "buffer_load_dwordx4 a[0:3], v9, s[56:59], s63 offen offset:0 sc1\n\t"
    "buffer_load_dwordx4 a[4:7], v9, s[56:59], s63 offen offset:1024 sc1\n\t"
    "buffer_load_dwordx4 a[8:11], v9, s[56:59], s63 offen offset:2048 sc1\n\t"
    "buffer_load_dwordx4 a[12:15], v9, s[56:59], s63 offen offset:3072 sc1\n\t"
    "s_add_u32 s63, s63, 4096\n\t"
    "s_waitcnt vmcnt(16)\n\t"
    "ds_write_b128 v11, v[128:131] offset:24576\n\t"
    "buffer_load_dwordx4 a[16:19], v9, s[56:59], s63 offen offset:0 sc1\n\t"
    "s_waitcnt vmcnt(16)\n\t"
    "ds_write_b128 v11, v[132:135] offset:25600\n\t"
    "buffer_load_dwordx4 a[20:23], v9, s[56:59], s63 offen offset:1024 sc1\n\t"
    "s_waitcnt vmcnt(16)\n\t"
    "ds_write_b128 v11, v[136:139] offset:26624\n\t"
    "buffer_load_dwordx4 a[24:27], v9, s[56:59], s63 offen offset:2048 sc1\n\t"
    "s_waitcnt vmcnt(16)\n\t"
    "ds_write_b128 v11, v[140:143] offset:27648\n\t"
    "buffer_load_dwordx4 a[28:31], v9, s[56:59], s63 offen offset:3072 sc1\n\t"
    "s_add_u32 s63, s63, 4096\n\t"
    "s_waitcnt vmcnt(16)\n\t"
    "ds_write_b128 v11, v[144:147] offset:28672\n\t"
    "buffer_load_dwordx4 a[32:35], v9, s[56:59], s63 offen offset:0 sc1\n\t"
    "s_waitcnt vmcnt(16)\n\t"
    "ds_write_b128 v11, v[148:151] offset:29696\n\t"
    "buffer_load_dwordx4 a[36:39], v9, s[56:59], s63 offen offset:1024 sc1\n\t"
    "s_waitcnt vmcnt(16)\n\t"
    "ds_write_b128 v11, v[152:155] offset:30720\n\t"
    "buffer_load_dwordx4 a[40:43], v9, s[56:59], s63 offen offset:2048 sc1\n\t"
    "s_waitcnt vmcnt(16)\n\t"
    "ds_write_b128 v11, v[156:159] offset:31744\n\t"
    "buffer_load_dwordx4 a[44:47], v9, s[56:59], s63 offen offset:3072 sc1\n\t"
    "s_add_u32 s63, s63, 4096\n\t"
    "s_waitcnt vmcnt(16)\n\t"
    "ds_write_b128 v11, v[160:163] offset:32768\n\t"
    "buffer_load_dwordx4 a[48:51], v9, s[56:59], s63 offen offset:0 sc1\n\t"
    "s_waitcnt vmcnt(16)\n\t"
    "ds_write_b128 v11, v[164:167] offset:33792\n\t"
    "buffer_load_dwordx4 a[52:55], v9, s[56:59], s63 offen offset:1024 sc1\n\t"
    "s_waitcnt vmcnt(16)\n\t"
    "ds_write_b128 v11, v[168:171] offset:34816\n\t"
    "buffer_load_dwordx4 a[56:59], v9, s[56:59], s63 offen offset:2048 sc1\n\t"
    "s_waitcnt vmcnt(16)\n\t"
    "ds_write_b128 v11, v[172:175] offset:35840\n\t"
    "s_waitcnt lgkmcnt(0)\n\t"
    "s_barrier\n\t"
    "buffer_load_dwordx4 a[60:63], v9, s[56:59], s63 offen offset:3072 sc1\n\t"
    "s_add_u32 s63, s63, 4096\n\t"
    "buffer_load_dwordx4 a[64:67], v9, s[56:59], s63 offen offset:0 sc1\n\t"
    "buffer_load_dwordx4 a[68:71], v9, s[56:59], s63 offen offset:1024 sc1\n\t"
    "buffer_load_dwordx4 a[72:75], v9, s[56:59], s63 offen offset:2048 sc1\n\t"
    "buffer_load_dwordx4 a[76:79], v9, s[56:59], s63 offen offset:3072 sc1\n\t"
    "s_add_u32 s63, s63, 4096\n\t"
    "buffer_load_dwordx4 a[80:83], v9, s[56:59], s63 offen offset:0 sc1\n\t"
    "buffer_load_dwordx4 a[84:87], v9, s[56:59], s63 offen offset:1024 sc1\n\t"
    "buffer_load_dwordx4 a[88:91], v9, s[56:59], s63 offen offset:2048 sc1\n\t"
    "buffer_load_dwordx4 a[92:95], v9, s[56:59], s63 offen offset:3072 sc1\n\t"
    "s_add_u32 s63, s63, 4096\n\t"
    "buffer_load_dwordx4 a[96:99], v9, s[56:59], s63 offen offset:0 sc1\n\t"
    "buffer_load_dwordx4 a[100:103], v9, s[56:59], s63 offen offset:1024 sc1\n\t"
    "buffer_load_dwordx4 a[104:107], v9, s[56:59], s63 offen offset:2048 sc1\n\t"
    "buffer_load_dwordx4 a[108:111], v9, s[56:59], s63 offen offset:3072 sc1\n\t"
    "s_add_u32 s63, s63, 4096\n\t"
    "buffer_load_dwordx4 a[112:115], v9, s[56:59], s63 offen offset:0 sc1\n\t"
    "buffer_load_dwordx4 a[116:119], v9, s[56:59], s63 offen offset:1024 sc1\n\t"
    "buffer_load_dwordx4 a[120:123], v9, s[56:59], s63 offen offset:2048 sc1\n\t"
    "buffer_load_dwordx4 a[124:127], v9, s[56:59], s63 offen offset:3072 sc1\n\t"
    "s_add_u32 s63, s63, 4096\n\t"
    "buffer_load_dwordx4 a[128:131], v9, s[56:59], s63 offen offset:0 sc1\n\t"
    "buffer_load_dwordx4 a[132:135], v9, s[56:59], s63 offen offset:1024 sc1\n\t"
    "buffer_load_dwordx4 a[136:139], v9, s[56:59], s63 offen offset:2048 sc1\n\t"
    "buffer_load_dwordx4 a[140:143], v9, s[56:59], s63 offen offset:3072 sc1\n\t"
    "s_add_u32 s63, s63, 4096\n\t"
    "buffer_load_dwordx4 a[144:147], v9, s[56:59], s63 offen offset:0 sc1\n\t"
    "buffer_load_dwordx4 a[148:151], v9, s[56:59], s63 offen offset:1024 sc1\n\t"
    "buffer_load_dwordx4 a[152:155], v9, s[56:59], s63 offen offset:2048 sc1\n\t"
    "buffer_load_dwordx4 a[156:159], v9, s[56:59], s63 offen offset:3072 sc1\n\t"
    "s_add_u32 s63, s63, 4096\n\t"
    "buffer_load_dwordx4 a[160:163], v9, s[56:59], s63 offen offset:0 sc1\n\t"
    "buffer_load_dwordx4 a[164:167], v9, s[56:59], s63 offen offset:1024 sc1\n\t"
    "buffer_load_dwordx4 a[168:171], v9, s[56:59], s63 offen offset:2048 sc1\n\t"
    "buffer_load_dwordx4 a[172:175], v9, s[56:59], s63 offen offset:3072 sc1\n\t"
    "s_add_u32 s63, s63, 4096\n\t"
    "buffer_load_dwordx4 a[176:179], v9, s[56:59], s63 offen offset:0 sc1\n\t"
    "buffer_load_dwordx4 a[180:183], v9, s[56:59], s63 offen offset:1024 sc1\n\t"
    "buffer_load_dwordx4 a[184:187], v9, s[56:59], s63 offen offset:2048 sc1\n\t"
    "buffer_load_dwordx4 a[188:191], v9, s[56:59], s63 offen offset:3072 sc1\n\t"
    "s_add_u32 s63, s63, 4096\n\t"
    "buffer_load_dwordx4 a[192:195], v9, s[56:59], s63 offen offset:0 sc1\n\t"
    "buffer_load_dwordx4 a[196:199], v9, s[56:59], s63 offen offset:1024 sc1\n\t"
    "buffer_load_dwordx4 a[200:203], v9, s[56:59], s63 offen offset:2048 sc1\n\t"
    "buffer_load_dwordx4 a[204:207], v9, s[56:59], s63 offen offset:3072 sc1\n\t"
    "s_add_u32 s63, s63, 4096\n\t"
    "buffer_load_dwordx4 a[208:211], v9, s[56:59], s63 offen offset:0 sc1\n\t"
    "buffer_load_dwordx4 a[212:215], v9, s[56:59], s63 offen offset:1024 sc1\n\t"
    "buffer_load_dwordx4 a[216:219], v9, s[56:59], s63 offen offset:2048 sc1\n\t"
    "buffer_load_dwordx4 a[220:223], v9, s[56:59], s63 offen offset:3072 sc1\n\t"
    "s_add_u32 s63, s63, 4096\n\t"
    "buffer_load_dwordx4 a[224:227], v9, s[56:59], s63 offen offset:0 sc1\n\t"
    "buffer_load_dwordx4 a[228:231], v9, s[56:59], s63 offen offset:1024 sc1\n\t"
    "buffer_load_dwordx4 a[232:235], v9, s[56:59], s63 offen offset:2048 sc1\n\t"
    "buffer_load_dwordx4 a[236:239], v9, s[56:59], s63 offen offset:3072 sc1\n\t"
    "s_add_u32 s63, s63, 4096\n\t"
    "buffer_load_dwordx4 a[240:243], v9, s[56:59], s63 offen offset:0 sc1\n\t"
    "buffer_load_dwordx4 a[244:247], v9, s[56:59], s63 offen offset:1024 sc1\n\t"
    "buffer_load_dwordx4 a[248:251], v9, s[56:59], s63 offen offset:2048 sc1\n\t"
    "buffer_load_dwordx4 a[252:255], v9, s[56:59], s63 offen offset:3072 sc1\n\t"
    "s_add_u32 s63, s63, 4096\n\t"
    "buffer_load_dwordx4 v[224:227], v9, s[56:59], s63 offen offset:0 sc1\n\t"
    "buffer_load_dwordx4 v[228:231], v9, s[56:59], s63 offen offset:1024 sc1\n\t"
    "buffer_load_dwordx4 v[232:235], v9, s[56:59], s63 offen offset:2048 sc1\n\t"
    "buffer_load_dwordx4 v[236:239], v9, s[56:59], s63 offen offset:3072 sc1\n\t"
    "s_add_u32 s63, s63, 4096\n\t"
    "buffer_load_dwordx4 v[240:243], v9, s[56:59], s63 offen offset:0 sc1\n\t"
    "buffer_load_dwordx4 v[244:247], v9, s[56:59], s63 offen offset:1024 sc1\n\t"
    "buffer_load_dwordx4 v[248:251], v9, s[56:59], s63 offen offset:2048 sc1\n\t"
    "buffer_load_dwordx4 v[252:255], v9, s[56:59], s63 offen offset:3072 sc1\n\t"
    "s_add_u32 s63, s63, 4096\n\t"
    "s_barrier\n\t"
    "s_mov_b64 exec, %[amask]\n\t"
    "buffer_store_dwordx2 v[22:23], off, s[56:59], %[aown] sc1\n\t"
    "s_mov_b64 exec, -1\n\t"
    "ds_read_b128 v[60:63], v0 offset:0\n\t"
    "ds_read_b128 v[64:67], v1 offset:0\n\t"
    "ds_read_b128 v[68:71], v2 offset:0\n\t"
    ".Lh1_%=:\n\t"
    "s_cbranch_execz .Lh2_%=\n\t"
    "ds_read_b128 v[72:75], v3 offset:0\n\t"
    "ds_read_b128 v[76:79], v4 offset:0\n\t"
    "ds_read_b128 v[80:83], v5 offset:0\n\t"
    "ds_read_b128 v[84:87], v0 offset:6144\n\t"
    "ds_read_b128 v[88:91], v1 offset:6144\n\t"
    "ds_read_b128 v[92:95], v2 offset:6144\n\t"
    "ds_read_b128 v[108:111], v3 offset:6144\n\t"
    "ds_read_b128 v[96:99], v4 offset:6144\n\t"
    "ds_read_b128 v[136:139], v5 offset:6144\n\t"
    "s_add_u32 s61, %[wb], 0xff400000\n\t"
    "s_mov_b32 s62, %[rbeg]\n\t"
    "s_mov_b32 s60, 8\n\t"
    ".Lmin_%=:\n\t"
    "s_waitcnt lgkmcnt(7)\n\t"
    "s_waitcnt vmcnt(63)\n\t"
    "v_mfma_f32_16x16x32_bf16 v[24:27], v[60:63], a[0:3], 0\n\t"
    "buffer_store_dwordx2 v[144:145], v8, s[52:55], s61 offen offset:768 sc1\n\t"
    "s_waitcnt lgkmcnt(10)\n\t"
    "v_mfma_f32_16x16x32_bf16 v[24:27], v[64:67], a[4:7], v[24:27]\n\t"
    "v_cvt_pk_bf16_f32 v187, v31, v35\n\t"
    "v_cvt_pk_bf16_f32 v172, v36, v12\n\t"
    "s_waitcnt lgkmcnt(9)\n\t"
    "v_mfma_f32_16x16x32_bf16 v[24:27], v[68:71], a[8:11], v[24:27]\n\t"
    "v_cvt_pk_bf16_f32 v174, v37, v13\n\t"
    "v_cvt_pk_bf16_f32 v188, v38, v14\n\t"
    "s_waitcnt lgkmcnt(8)\n\t"
    "v_mfma_f32_16x16x32_bf16 v[24:27], v[72:75], a[12:15], v[24:27]\n\t"
    "v_cvt_pk_bf16_f32 v190, v39, v15\n\t"
    "v_cvt_pk_bf16_f32 v173, v16, v20\n\t"
    "s_waitcnt lgkmcnt(7)\n\t"
    "v_mfma_f32_16x16x32_bf16 v[24:27], v[76:79], a[16:19], v[24:27]\n\t"
    "v_cvt_pk_bf16_f32 v175, v17, v21\n\t"
    "s_waitcnt lgkmcnt(2)\n\t"
    "v_mfma_f32_16x16x32_bf16 v[24:27], v[80:83], a[20:23], v[24:27]\n\t"
    "v_cvt_pk_bf16_f32 v189, v18, v22\n\t"
    "v_cvt_pk_bf16_f32 v191, v19, v23\n\t"
    "s_waitcnt lgkmcnt(5)\n\t"
    "v_mfma_f32_16x16x32_bf16 v[28:31], v[84:87], a[0:3], 0\n\t"
    "s_waitcnt lgkmcnt(0)\n\t"
    "v_mfma_f32_16x16x32_bf16 v[28:31], v[88:91], a[4:7], v[28:31]\n\t"
    "v_mfma_f32_16x16x32_bf16 v[28:31], v[92:95], a[8:11], v[28:31]\n\t"
    "v_mfma_f32_16x16x32_bf16 v[28:31], v[108:111], a[12:15], v[28:31]\n\t"
    "v_cvt_pk_bf16_f32 v12, v24, v25\n\t"
    "s_waitcnt lgkmcnt(0)\n\t"
    "v_mfma_f32_16x16x32_bf16 v[28:31], v[96:99], a[16:19], v[28:31]\n\t"
    "v_cvt_pk_bf16_f32 v13, v26, v27\n\t"
    "v_mfma_f32_16x16x32_bf16 v[28:31], v[136:139], a[20:23], v[28:31]\n\t"
    "s_waitcnt vmcnt(62)\n\t"
    "v_mfma_f32_16x16x32_bf16 v[20:23], v[60:63], a[24:27], 0\n\t"
    "buffer_store_dwordx2 v[146:147], v8, s[52:55], s61 offen offset:1152 sc1\n\t"
    "v_mfma_f32_16x16x32_bf16 v[20:23], v[64:67], a[28:31], v[20:23]\n\t"
    "s_add_u32 s61, s61, 393216\n\t"
    "v_mfma_f32_16x16x32_bf16 v[20:23], v[68:71], a[32:35], v[20:23]\n\t"
    "v_mfma_f32_16x16x32_bf16 v[20:23], v[72:75], a[36:39], v[20:23]\n\t"
    "v_cvt_pk_bf16_f32 v14, v28, v29\n\t"
    "v_cvt_pk_bf16_f32 v15, v30, v31\n\t"
    "v_mfma_f32_16x16x32_bf16 v[20:23], v[76:79], a[40:43], v[20:23]\n\t"
    "v_mfma_f32_16x16x32_bf16 v[20:23], v[80:83], a[44:47], v[20:23]\n\t"
    "v_mfma_f32_16x16x32_bf16 v[24:27], v[84:87], a[24:27], 0\n\t"
    "v_mfma_f32_16x16x32_bf16 v[24:27], v[88:91], a[28:31], v[24:27]\n\t"
    "v_mfma_f32_16x16x32_bf16 v[24:27], v[92:95], a[32:35], v[24:27]\n\t"
    "v_mfma_f32_16x16x32_bf16 v[24:27], v[108:111], a[36:39], v[24:27]\n\t"
    "v_cvt_pk_bf16_f32 v16, v20, v21\n\t"
    "v_cvt_pk_bf16_f32 v17, v22, v23\n\t"
    "v_mfma_f32_16x16x32_bf16 v[24:27], v[96:99], a[40:43], v[24:27]\n\t"
    "v_mfma_f32_16x16x32_bf16 v[24:27], v[136:139], a[44:47], v[24:27]\n\t"
    "s_waitcnt vmcnt(57)\n\t"
    "v_mfma_f32_16x16x32_bf16 v[28:31], v[60:63], a[48:51], 0\n\t"
    "buffer_store_dwordx4 v[132:135], v7, s[52:55], s61 offen offset:0 sc1\n\t"
    "v_mfma_f32_16x16x32_bf16 v[28:31], v[64:67], a[52:55], v[28:31]\n\t"
    "v_mfma_f32_16x16x32_bf16 v[28:31], v[68:71], a[56:59], v[28:31]\n\t"
    "v_mfma_f32_16x16x32_bf16 v[28:31], v[72:75], a[60:63], v[28:31]\n\t"
    "v_cvt_pk_bf16_f32 v18, v24, v25\n\t"
    "v_cvt_pk_bf16_f32 v19, v26, v27\n\t"
    "v_mfma_f32_16x16x32_bf16 v[28:31], v[76:79], a[64:67], v[28:31]\n\t"
    "v_mfma_f32_16x16x32_bf16 v[28:31], v[80:83], a[68:71], v[28:31]\n\t"
    "v_mfma_f32_16x16x32_bf16 v[24:27], v[84:87], a[48:51], 0\n\t"
    "v_mfma_f32_16x16x32_bf16 v[24:27], v[88:91], a[52:55], v[24:27]\n\t"
    "v_mfma_f32_16x16x32_bf16 v[24:27], v[92:95], a[56:59], v[24:27]\n\t"
    "v_mfma_f32_16x16x32_bf16 v[24:27], v[108:111], a[60:63], v[24:27]\n\t"
    "v_cvt_pk_bf16_f32 v20, v28, v29\n\t"
    "v_cvt_pk_bf16_f32 v21, v30, v31\n\t"
    "v_mfma_f32_16x16x32_bf16 v[24:27], v[96:99], a[64:67], v[24:27]\n\t"
    "v_mfma_f32_16x16x32_bf16 v[24:27], v[136:139], a[68:71], v[24:27]\n\t"
    "s_waitcnt vmcnt(52)\n\t"
    "v_mfma_f32_16x16x32_bf16 v[28:31], v[60:63], a[72:75], 0\n\t"
    "buffer_store_dwordx4 v[128:131], v7, s[52:55], s61 offen offset:384 sc1\n\t"
    "v_mfma_f32_16x16x32_bf16 v[28:31], v[64:67], a[76:79], v[28:31]\n\t"
    "v_mfma_f32_16x16x32_bf16 v[28:31], v[68:71], a[80:83], v[28:31]\n\t"
    "v_mfma_f32_16x16x32_bf16 v[28:31], v[72:75], a[84:87], v[28:31]\n\t"
    "v_cvt_pk_bf16_f32 v22, v24, v25\n\t"
    "v_cvt_pk_bf16_f32 v23, v26, v27\n\t"
    "v_mfma_f32_16x16x32_bf16 v[28:31], v[76:79], a[88:91], v[28:31]\n\t"
    "v_mfma_f32_16x16x32_bf16 v[28:31], v[80:83], a[92:95], v[28:31]\n\t"
    "v_mfma_f32_16x16x32_bf16 v[32:35], v[84:87], a[72:75], 0\n\t"
    "v_mfma_f32_16x16x32_bf16 v[32:35], v[88:91], a[76:79], v[32:35]\n\t"
    "v_mfma_f32_16x16x32_bf16 v[32:35], v[92:95], a[80:83], v[32:35]\n\t"
    "v_mfma_f32_16x16x32_bf16 v[32:35], v[108:111], a[84:87], v[32:35]\n\t"
    "v_cvt_pk_bf16_f32 v24, v28, v29\n\t"
    "v_cvt_pk_bf16_f32 v25, v30, v31\n\t"
    "v_mfma_f32_16x16x32_bf16 v[32:35], v[96:99], a[88:91], v[32:35]\n\t"
    "v_mfma_f32_16x16x32_bf16 v[32:35], v[136:139], a[92:95], v[32:35]\n\t"
    "s_waitcnt vmcnt(47)\n\t"
    "v_mfma_f32_16x16x32_bf16 v[36:39], v[60:63], a[96:99], 0\n\t"
    "buffer_store_dwordx4 v[164:167], v7, s[52:55], s61 offen offset:768 sc1\n\t"
    "v_mfma_f32_16x16x32_bf16 v[36:39], v[64:67], a[100:103], v[36:39]\n\t"
    "v_mfma_f32_16x16x32_bf16 v[36:39], v[68:71], a[104:107], v[36:39]\n\t"
    "v_mfma_f32_16x16x32_bf16 v[36:39], v[72:75], a[108:111], v[36:39]\n\t"
    "v_cvt_pk_bf16_f32 v26, v32, v33\n\t"
    "v_cvt_pk_bf16_f32 v27, v34, v35\n\t"
    "v_mfma_f32_16x16x32_bf16 v[36:39], v[76:79], a[112:115], v[36:39]\n\t"
    "v_mfma_f32_16x16x32_bf16 v[36:39], v[80:83], a[116:119], v[36:39]\n\t"
    "v_mfma_f32_16x16x32_bf16 v[32:35], v[84:87], a[96:99], 0\n\t"
    "v_mfma_f32_16x16x32_bf16 v[32:35], v[88:91], a[100:103], v[32:35]\n\t"
    "v_mfma_f32_16x16x32_bf16 v[32:35], v[92:95], a[104:107], v[32:35]\n\t"
    "v_mfma_f32_16x16x32_bf16 v[32:35], v[108:111], a[108:111], v[32:35]\n\t"
    "v_cvt_pk_bf16_f32 v28, v36, v37\n\t"
    "v_cvt_pk_bf16_f32 v29, v38, v39\n\t"
    "v_mfma_f32_16x16x32_bf16 v[32:35], v[96:99], a[112:115], v[32:35]\n\t"
    "v_mfma_f32_16x16x32_bf16 v[32:35], v[136:139], a[116:119], v[32:35]\n\t"
    "s_waitcnt vmcnt(42)\n\t"
    "v_mfma_f32_16x16x32_bf16 v[36:39], v[60:63], a[120:123], 0\n\t"
    "buffer_store_dwordx4 v[160:163], v7, s[52:55], s61 offen offset:1152 sc1\n\t"
    "v_mfma_f32_16x16x32_bf16 v[36:39], v[64:67], a[124:127], v[36:39]\n\t"
    "v_mfma_f32_16x16x32_bf16 v[36:39], v[68:71], a[128:131], v[36:39]\n\t"
    "v_mfma_f32_16x16x32_bf16 v[36:39], v[72:75], a[132:135], v[36:39]\n\t"
    "v_cvt_pk_bf16_f32 v30, v32, v33\n\t"
    "v_cvt_pk_bf16_f32 v31, v34, v35\n\t"
    "v_mfma_f32_16x16x32_bf16 v[36:39], v[76:79], a[136:139], v[36:39]\n\t"
    "v_mfma_f32_16x16x32_bf16 v[36:39], v[80:83], a[140:143], v[36:39]\n\t"
    "v_mfma_f32_16x16x32_bf16 v[40:43], v[84:87], a[120:123], 0\n\t"
    "v_mfma_f32_16x16x32_bf16 v[40:43], v[88:91], a[124:127], v[40:43]\n\t"
    "v_mfma_f32_16x16x32_bf16 v[40:43], v[92:95], a[128:131], v[40:43]\n\t"
    "v_mfma_f32_16x16x32_bf16 v[40:43], v[108:111], a[132:135], v[40:43]\n\t"
    "v_cvt_pk_bf16_f32 v32, v36, v37\n\t"
    "v_cvt_pk_bf16_f32 v33, v38, v39\n\t"
    "v_mfma_f32_16x16x32_bf16 v[40:43], v[96:99], a[136:139], v[40:43]\n\t"
    "v_mfma_f32_16x16x32_bf16 v[40:43], v[136:139], a[140:143], v[40:43]\n\t"
    "s_waitcnt vmcnt(37)\n\t"
    "v_mfma_f32_16x16x32_bf16 v[44:47], v[60:63], a[144:147], 0\n\t"
    "buffer_store_dwordx2 v[140:141], v8, s[52:55], s61 offen offset:0 sc1\n\t"
    "v_mfma_f32_16x16x32_bf16 v[44:47], v[64:67], a[148:151], v[44:47]\n\t"
    "v_mfma_f32_16x16x32_bf16 v[44:47], v[68:71], a[152:155], v[44:47]\n\t"
    "v_mfma_f32_16x16x32_bf16 v[44:47], v[72:75], a[156:159], v[44:47]\n\t"
    "v_cvt_pk_bf16_f32 v34, v40, v41\n\t"
    "v_cvt_pk_bf16_f32 v35, v42, v43\n\t"
    "v_mfma_f32_16x16x32_bf16 v[44:47], v[76:79], a[160:163], v[44:47]\n\t"
    "v_mfma_f32_16x16x32_bf16 v[44:47], v[80:83], a[164:167], v[44:47]\n\t"
    "v_mfma_f32_16x16x32_bf16 v[40:43], v[84:87], a[144:147], 0\n\t"
    "v_mfma_f32_16x16x32_bf16 v[40:43], v[88:91], a[148:151], v[40:43]\n\t"
    "v_mfma_f32_16x16x32_bf16 v[40:43], v[92:95], a[152:155], v[40:43]\n\t"
    "v_mfma_f32_16x16x32_bf16 v[40:43], v[108:111], a[156:159], v[40:43]\n\t"
    "v_cvt_pk_bf16_f32 v36, v44, v45\n\t"
    "v_cvt_pk_bf16_f32 v37, v46, v47\n\t"
    "v_mfma_f32_16x16x32_bf16 v[40:43], v[96:99], a[160:163], v[40:43]\n\t"
    "v_mfma_f32_16x16x32_bf16 v[40:43], v[136:139], a[164:167], v[40:43]\n\t"
    "s_waitcnt vmcnt(32)\n\t"
    "v_mfma_f32_16x16x32_bf16 v[44:47], v[60:63], a[168:171], 0\n\t"
    "buffer_store_dwordx2 v[142:143], v8, s[52:55], s61 offen offset:384 sc1\n\t"
    "v_mfma_f32_16x16x32_bf16 v[44:47], v[64:67], a[172:175], v[44:47]\n\t"
    "v_mfma_f32_16x16x32_bf16 v[44:47], v[68:71], a[176:179], v[44:47]\n\t"
    "v_mfma_f32_16x16x32_bf16 v[44:47], v[72:75], a[180:183], v[44:47]\n\t"
    "v_cvt_pk_bf16_f32 v38, v40, v41\n\t"
    "v_cvt_pk_bf16_f32 v39, v42, v43\n\t"
    "v_mfma_f32_16x16x32_bf16 v[44:47], v[76:79], a[184:187], v[44:47]\n\t"
    "v_mfma_f32_16x16x32_bf16 v[44:47], v[80:83], a[188:191], v[44:47]\n\t"
    "v_mfma_f32_16x16x32_bf16 v[48:51], v[84:87], a[168:171], 0\n\t"
    "v_mfma_f32_16x16x32_bf16 v[48:51], v[88:91], a[172:175], v[48:51]\n\t"
    "v_mfma_f32_16x16x32_bf16 v[48:51], v[92:95], a[176:179], v[48:51]\n\t"
    "v_mfma_f32_16x16x32_bf16 v[48:51], v[108:111], a[180:183], v[48:51]\n\t"
    "v_cvt_pk_bf16_f32 v40, v44, v45\n\t"
    "v_cvt_pk_bf16_f32 v41, v46, v47\n\t"
    "v_mfma_f32_16x16x32_bf16 v[48:51], v[96:99], a[184:187], v[48:51]\n\t"
    "v_mfma_f32_16x16x32_bf16 v[48:51], v[136:139], a[188:191], v[48:51]\n\t"
    "s_waitcnt vmcnt(27)\n\t"
    "v_mfma_f32_16x16x32_bf16 v[52:55], v[60:63], a[192:195], 0\n\t"
    "buffer_store_dwordx2 v[120:121], v8, s[52:55], s61 offen offset:768 sc1\n\t"
    "v_mfma_f32_16x16x32_bf16 v[52:55], v[64:67], a[196:199], v[52:55]\n\t"
    "v_mfma_f32_16x16x32_bf16 v[52:55], v[68:71], a[200:203], v[52:55]\n\t"
    "v_mfma_f32_16x16x32_bf16 v[52:55], v[72:75], a[204:207], v[52:55]\n\t"
    "v_cvt_pk_bf16_f32 v42, v48, v49\n\t"
    "v_cvt_pk_bf16_f32 v43, v50, v51\n\t"
    "v_mfma_f32_16x16x32_bf16 v[52:55], v[76:79], a[208:211], v[52:55]\n\t"
    "v_mfma_f32_16x16x32_bf16 v[52:55], v[80:83], a[212:215], v[52:55]\n\t"
    "v_mfma_f32_16x16x32_bf16 v[48:51], v[84:87], a[192:195], 0\n\t"
    "v_mfma_f32_16x16x32_bf16 v[48:51], v[88:91], a[196:199], v[48:51]\n\t"
    "v_mfma_f32_16x16x32_bf16 v[48:51], v[92:95], a[200:203], v[48:51]\n\t"
    "v_mfma_f32_16x16x32_bf16 v[48:51], v[108:111], a[204:207], v[48:51]\n\t"
    "v_cvt_pk_bf16_f32 v44, v52, v53\n\t"
    "v_cvt_pk_bf16_f32 v45, v54, v55\n\t"
    "v_mfma_f32_16x16x32_bf16 v[48:51], v[96:99], a[208:211], v[48:51]\n\t"
    "v_mfma_f32_16x16x32_bf16 v[48:51], v[136:139], a[212:215], v[48:51]\n\t"
    "s_waitcnt vmcnt(22)\n\t"
    "v_mfma_f32_16x16x32_bf16 v[52:55], v[60:63], a[216:219], 0\n\t"
    "buffer_store_dwordx2 v[122:123], v8, s[52:55], s61 offen offset:1152 sc1\n\t"
    "v_mfma_f32_16x16x32_bf16 v[52:55], v[64:67], a[220:223], v[52:55]\n\t"
    "s_add_u32 s61, s61, 393216\n\t"
    "v_mfma_f32_16x16x32_bf16 v[52:55], v[68:71], a[224:227], v[52:55]\n\t"
    "v_mfma_f32_16x16x32_bf16 v[52:55], v[72:75], a[228:231], v[52:55]\n\t"
    "v_cvt_pk_bf16_f32 v46, v48, v49\n\t"
    "v_cvt_pk_bf16_f32 v47, v50, v51\n\t"
    "v_mfma_f32_16x16x32_bf16 v[52:55], v[76:79], a[232:235], v[52:55]\n\t"
    "v_mfma_f32_16x16x32_bf16 v[52:55], v[80:83], a[236:239], v[52:55]\n\t"
    "v_mfma_f32_16x16x32_bf16 v[56:59], v[84:87], a[216:219], 0\n\t"
    "v_mfma_f32_16x16x32_bf16 v[56:59], v[88:91], a[220:223], v[56:59]\n\t"
    "v_mfma_f32_16x16x32_bf16 v[56:59], v[92:95], a[224:227], v[56:59]\n\t"
    "v_mfma_f32_16x16x32_bf16 v[56:59], v[108:111], a[228:231], v[56:59]\n\t"
    "v_cvt_pk_bf16_f32 v48, v52, v53\n\t"
    "v_cvt_pk_bf16_f32 v49, v54, v55\n\t"
    "v_mfma_f32_16x16x32_bf16 v[56:59], v[96:99], a[232:235], v[56:59]\n\t"
    "v_mfma_f32_16x16x32_bf16 v[56:59], v[136:139], a[236:239], v[56:59]\n\t"
    "s_waitcnt vmcnt(17)\n\t"
    "v_mfma_f32_16x16x32_bf16 v[100:103], v[60:63], a[240:243], 0\n\t"
    "buffer_store_dwordx4 v[168:171], v7, s[52:55], s61 offen offset:0 sc1\n\t"
    "v_mfma_f32_16x16x32_bf16 v[100:103], v[64:67], a[244:247], v[100:103]\n\t"
    "v_mfma_f32_16x16x32_bf16 v[100:103], v[68:71], a[248:251], v[100:103]\n\t"
    "v_mfma_f32_16x16x32_bf16 v[100:103], v[72:75], a[252:255], v[100:103]\n\t"
    "v_cvt_pk_bf16_f32 v50, v56, v57\n\t"
    "v_cvt_pk_bf16_f32 v51, v58, v59\n\t"
    "v_mfma_f32_16x16x32_bf16 v[100:103], v[76:79], v[224:227], v[100:103]\n\t"
    "v_mfma_f32_16x16x32_bf16 v[100:103], v[80:83], v[228:231], v[100:103]\n\t"
    "v_mfma_f32_16x16x32_bf16 v[56:59], v[84:87], a[240:243], 0\n\t"
    "v_mfma_f32_16x16x32_bf16 v[56:59], v[88:91], a[244:247], v[56:59]\n\t"
    "v_mfma_f32_16x16x32_bf16 v[56:59], v[92:95], a[248:251], v[56:59]\n\t"
    "v_mfma_f32_16x16x32_bf16 v[56:59], v[108:111], a[252:255], v[56:59]\n\t"
    "v_cvt_pk_bf16_f32 v52, v100, v101\n\t"
    "v_cvt_pk_bf16_f32 v53, v102, v103\n\t"
    "v_mfma_f32_16x16x32_bf16 v[56:59], v[96:99], v[224:227], v[56:59]\n\t"
    "v_mfma_f32_16x16x32_bf16 v[56:59], v[136:139], v[228:231], v[56:59]\n\t"
    "s_waitcnt vmcnt(12)\n\t"
    "v_mfma_f32_16x16x32_bf16 v[100:103], v[60:63], v[232:235], 0\n\t"
    "ds_read_b128 v[104:107], v0 offset:12288\n\t"
    "buffer_store_dwordx4 v[156:159], v7, s[52:55], s61 offen offset:384 sc1\n\t"
    "v_mfma_f32_16x16x32_bf16 v[100:103], v[64:67], v[236:239], v[100:103]\n\t"
    "ds_read_b128 v[112:115], v1 offset:12288\n\t"
    "v_mfma_f32_16x16x32_bf16 v[100:103], v[68:71], v[240:243], v[100:103]\n\t"
    "ds_read_b128 v[120:123], v2 offset:12288\n\t"
    "v_mfma_f32_16x16x32_bf16 v[100:103], v[72:75], v[244:247], v[100:103]\n\t"
    "ds_read_b128 v[128:131], v3 offset:12288\n\t"
    "v_cvt_pk_bf16_f32 v54, v56, v57\n\t"
    "v_mfma_f32_16x16x32_bf16 v[100:103], v[76:79], v[248:251], v[100:103]\n\t"
    "ds_read_b128 v[132:135], v4 offset:12288\n\t"
    "v_cvt_pk_bf16_f32 v55, v58, v59\n\t"
    "v_mfma_f32_16x16x32_bf16 v[100:103], v[80:83], v[252:255], v[100:103]\n\t"
    "ds_read_b128 v[140:143], v5 offset:12288\n\t"
    "v_mfma_f32_16x16x32_bf16 v[60:63], v[84:87], v[232:235], 0\n\t"
    "ds_read_b128 v[144:147], v0 offset:18432\n\t"
    "v_mfma_f32_16x16x32_bf16 v[60:63], v[88:91], v[236:239], v[60:63]\n\t"
    "ds_read_b128 v[156:159], v1 offset:18432\n\t"
    "v_mfma_f32_16x16x32_bf16 v[60:63], v[92:95], v[240:243], v[60:63]\n\t"
    "ds_read_b128 v[160:163], v2 offset:18432\n\t"
    "v_mfma_f32_16x16x32_bf16 v[60:63], v[108:111], v[244:247], v[60:63]\n\t"
    "ds_read_b128 v[164:167], v3 offset:18432\n\t"
    "v_cvt_pk_bf16_f32 v56, v100, v101\n\t"
    "v_mfma_f32_16x16x32_bf16 v[60:63], v[96:99], v[248:251], v[60:63]\n\t"
    "ds_read_b128 v[168:171], v4 offset:18432\n\t"
    "v_cvt_pk_bf16_f32 v57, v102, v103\n\t"
    "v_mfma_f32_16x16x32_bf16 v[60:63], v[136:139], v[252:255], v[60:63]\n\t"
    "ds_read_b128 v[196:199], v5 offset:18432\n\t"
    "s_waitcnt lgkmcnt(7)\n\t"
    "v_mfma_f32_16x16x32_bf16 v[64:67], v[104:107], a[0:3], 0\n\t"
    "buffer_store_dwordx4 v[180:183], v7, s[52:55], s61 offen offset:768 sc1\n\t"
    "v_mfma_f32_16x16x32_bf16 v[64:67], v[112:115], a[4:7], v[64:67]\n\t"
    "v_mfma_f32_16x16x32_bf16 v[64:67], v[120:123], a[8:11], v[64:67]\n\t"
    "v_mfma_f32_16x16x32_bf16 v[64:67], v[128:131], a[12:15], v[64:67]\n\t"
    "v_cvt_pk_bf16_f32 v58, v60, v61\n\t"
    "v_cvt_pk_bf16_f32 v59, v62, v63\n\t"
    "v_mfma_f32_16x16x32_bf16 v[64:67], v[132:135], a[16:19], v[64:67]\n\t"
    "s_waitcnt lgkmcnt(2)\n\t"
    "v_mfma_f32_16x16x32_bf16 v[64:67], v[140:143], a[20:23], v[64:67]\n\t"
    "v_mfma_f32_16x16x32_bf16 v[68:71], v[144:147], a[0:3], 0\n\t"
    "v_mfma_f32_16x16x32_bf16 v[68:71], v[156:159], a[4:7], v[68:71]\n\t"
    "v_mfma_f32_16x16x32_bf16 v[68:71], v[160:163], a[8:11], v[68:71]\n\t"
    "v_mfma_f32_16x16x32_bf16 v[68:71], v[164:167], a[12:15], v[68:71]\n\t"
    "v_cvt_pk_bf16_f32 v60, v64, v65\n\t"
    "s_waitcnt lgkmcnt(0)\n\t"
    "v_mfma_f32_16x16x32_bf16 v[68:71], v[168:171], a[16:19], v[68:71]\n\t"
    "v_cvt_pk_bf16_f32 v61, v66, v67\n\t"
    "v_mfma_f32_16x16x32_bf16 v[68:71], v[196:199], a[20:23], v[68:71]\n\t"
    "v_mfma_f32_16x16x32_bf16 v[72:75], v[104:107], a[24:27], 0\n\t"
    "buffer_store_dwordx4 v[176:179], v7, s[52:55], s61 offen offset:1152 sc1\n\t"
    "v_mfma_f32_16x16x32_bf16 v[72:75], v[112:115], a[28:31], v[72:75]\n\t"
    "v_mfma_f32_16x16x32_bf16 v[72:75], v[120:123], a[32:35], v[72:75]\n\t"
    "v_mfma_f32_16x16x32_bf16 v[72:75], v[128:131], a[36:39], v[72:75]\n\t"
    "v_cvt_pk_bf16_f32 v62, v68, v69\n\t"
    "v_cvt_pk_bf16_f32 v63, v70, v71\n\t"
    "v_mfma_f32_16x16x32_bf16 v[72:75], v[132:135], a[40:43], v[72:75]\n\t"
    "v_mfma_f32_16x16x32_bf16 v[72:75], v[140:143], a[44:47], v[72:75]\n\t"
    "v_mfma_f32_16x16x32_bf16 v[68:71], v[144:147], a[24:27], 0\n\t"
    "v_mfma_f32_16x16x32_bf16 v[68:71], v[156:159], a[28:31], v[68:71]\n\t"
    "v_mfma_f32_16x16x32_bf16 v[68:71], v[160:163], a[32:35], v[68:71]\n\t"
    "v_mfma_f32_16x16x32_bf16 v[68:71], v[164:167], a[36:39], v[68:71]\n\t"
    "v_cvt_pk_bf16_f32 v64, v72, v73\n\t"
    "v_cvt_pk_bf16_f32 v65, v74, v75\n\t"
    "v_mfma_f32_16x16x32_bf16 v[68:71], v[168:171], a[40:43], v[68:71]\n\t"
    "v_mfma_f32_16x16x32_bf16 v[68:71], v[196:199], a[44:47], v[68:71]\n\t"
    "v_mfma_f32_16x16x32_bf16 v[72:75], v[104:107], a[48:51], 0\n\t"
    "buffer_store_dwordx2 v[116:117], v8, s[52:55], s61 offen offset:0 sc1\n\t"
    "v_mfma_f32_16x16x32_bf16 v[72:75], v[112:115], a[52:55], v[72:75]\n\t"
    "v_mfma_f32_16x16x32_bf16 v[72:75], v[120:123], a[56:59], v[72:75]\n\t"
    "v_mfma_f32_16x16x32_bf16 v[72:75], v[128:131], a[60:63], v[72:75]\n\t"
    "v_cvt_pk_bf16_f32 v66, v68, v69\n\t"
    "v_cvt_pk_bf16_f32 v67, v70, v71\n\t"
    "v_mfma_f32_16x16x32_bf16 v[72:75], v[132:135], a[64:67], v[72:75]\n\t"
    "v_mfma_f32_16x16x32_bf16 v[72:75], v[140:143], a[68:71], v[72:75]\n\t"
    "v_mfma_f32_16x16x32_bf16 v[76:79], v[144:147], a[48:51], 0\n\t"
    "v_mfma_f32_16x16x32_bf16 v[76:79], v[156:159], a[52:55], v[76:79]\n\t"
    "v_mfma_f32_16x16x32_bf16 v[76:79], v[160:163], a[56:59], v[76:79]\n\t"
    "v_mfma_f32_16x16x32_bf16 v[76:79], v[164:167], a[60:63], v[76:79]\n\t"
    "v_cvt_pk_bf16_f32 v68, v72, v73\n\t"
    "v_cvt_pk_bf16_f32 v69, v74, v75\n\t"
    "v_mfma_f32_16x16x32_bf16 v[76:79], v[168:171], a[64:67], v[76:79]\n\t"
    "v_mfma_f32_16x16x32_bf16 v[76:79], v[196:199], a[68:71], v[76:79]\n\t"
    "v_mfma_f32_16x16x32_bf16 v[80:83], v[104:107], a[72:75], 0\n\t"
    "s_add_i32 s64, s62, 24576\n\t"
    "buffer_store_dwordx2 v[118:119], v8, s[52:55], s61 offen offset:384 sc1\n\t"
    "v_mfma_f32_16x16x32_bf16 v[80:83], v[112:115], a[76:79], v[80:83]\n\t"
    "s_cmp_eq_u32 s64, %[rend]\n\t"
    "s_cselect_b32 s64, %[rbeg], s64\n\t"
    "v_mfma_f32_16x16x32_bf16 v[80:83], v[120:123], a[80:83], v[80:83]\n\t"
    "s_sub_i32 s65, s64, s62\n\t"
    "s_mov_b32 s62, s64\n\t"
    "v_mfma_f32_16x16x32_bf16 v[80:83], v[128:131], a[84:87], v[80:83]\n\t"
    "v_add_u32_e32 v0, s65, v0\n\t"
    "v_add_u32_e32 v1, s65, v1\n\t"
    "v_mfma_f32_16x16x32_bf16 v[80:83], v[132:135], a[88:91], v[80:83]\n\t"
    "v_add_u32_e32 v2, s65, v2\n\t"
    "v_add_u32_e32 v3, s65, v3\n\t"
    "v_mfma_f32_16x16x32_bf16 v[80:83], v[140:143], a[92:95], v[80:83]\n\t"
    "v_add_u32_e32 v4, s65, v4\n\t"
    "v_add_u32_e32 v5, s65, v5\n\t"
    "v_mfma_f32_16x16x32_bf16 v[84:87], v[144:147], a[72:75], 0\n\t"
    "v_cvt_pk_bf16_f32 v70, v76, v77\n\t"
    "v_cvt_pk_bf16_f32 v71, v78, v79\n\t"
    "v_mfma_f32_16x16x32_bf16 v[84:87], v[156:159], a[76:79], v[84:87]\n\t"
    "v_mfma_f32_16x16x32_bf16 v[84:87], v[160:163], a[80:83], v[84:87]\n\t"
    "v_mfma_f32_16x16x32_bf16 v[84:87], v[164:167], a[84:87], v[84:87]\n\t"
    "v_cvt_pk_bf16_f32 v72, v80, v81\n\t"
    "v_cvt_pk_bf16_f32 v73, v82, v83\n\t"
    "v_mfma_f32_16x16x32_bf16 v[84:87], v[168:171], a[88:91], v[84:87]\n\t"
    "v_mfma_f32_16x16x32_bf16 v[84:87], v[196:199], a[92:95], v[84:87]\n\t"
    "v_mfma_f32_16x16x32_bf16 v[80:83], v[104:107], a[96:99], 0\n\t"
    "buffer_store_dwordx2 v[148:149], v8, s[52:55], s61 offen offset:768 sc1\n\t"
    "v_mfma_f32_16x16x32_bf16 v[80:83], v[112:115], a[100:103], v[80:83]\n\t"
    "v_mfma_f32_16x16x32_bf16 v[80:83], v[120:123], a[104:107], v[80:83]\n\t"
    "v_mfma_f32_16x16x32_bf16 v[80:83], v[128:131], a[108:111], v[80:83]\n\t"
    "v_cvt_pk_bf16_f32 v74, v84, v85\n\t"
    "v_cvt_pk_bf16_f32 v75, v86, v87\n\t"
    "v_mfma_f32_16x16x32_bf16 v[80:83], v[132:135], a[112:115], v[80:83]\n\t"
    "v_mfma_f32_16x16x32_bf16 v[80:83], v[140:143], a[116:119], v[80:83]\n\t"
    "v_mfma_f32_16x16x32_bf16 v[84:87], v[144:147], a[96:99], 0\n\t"
    "v_mfma_f32_16x16x32_bf16 v[84:87], v[156:159], a[100:103], v[84:87]\n\t"
    "v_mfma_f32_16x16x32_bf16 v[84:87], v[160:163], a[104:107], v[84:87]\n\t"
    "v_mfma_f32_16x16x32_bf16 v[84:87], v[164:167], a[108:111], v[84:87]\n\t"
    "v_cvt_pk_bf16_f32 v76, v80, v81\n\t"
    "v_cvt_pk_bf16_f32 v77, v82, v83\n\t"
    "v_mfma_f32_16x16x32_bf16 v[84:87], v[168:171], a[112:115], v[84:87]\n\t"
    "v_mfma_f32_16x16x32_bf16 v[84:87], v[196:199], a[116:119], v[84:87]\n\t"
    "v_mfma_f32_16x16x32_bf16 v[88:91], v[104:107], a[120:123], 0\n\t"
    "buffer_store_dwordx2 v[150:151], v8, s[52:55], s61 offen offset:1152 sc1\n\t"
    "v_mfma_f32_16x16x32_bf16 v[88:91], v[112:115], a[124:127], v[88:91]\n\t"
    "s_add_u32 s61, s61, 393216\n\t"
    "v_mfma_f32_16x16x32_bf16 v[88:91], v[120:123], a[128:131], v[88:91]\n\t"
    "v_mfma_f32_16x16x32_bf16 v[88:91], v[128:131], a[132:135], v[88:91]\n\t"
    "v_cvt_pk_bf16_f32 v78, v84, v85\n\t"
    "v_cvt_pk_bf16_f32 v79, v86, v87\n\t"
    "v_mfma_f32_16x16x32_bf16 v[88:91], v[132:135], a[136:139], v[88:91]\n\t"
    "v_mfma_f32_16x16x32_bf16 v[88:91], v[140:143], a[140:143], v[88:91]\n\t"
    "v_mfma_f32_16x16x32_bf16 v[84:87], v[144:147], a[120:123], 0\n\t"
    "v_mfma_f32_16x16x32_bf16 v[84:87], v[156:159], a[124:127], v[84:87]\n\t"
    "v_mfma_f32_16x16x32_bf16 v[84:87], v[160:163], a[128:131], v[84:87]\n\t"
    "v_mfma_f32_16x16x32_bf16 v[84:87], v[164:167], a[132:135], v[84:87]\n\t"
    "v_cvt_pk_bf16_f32 v80, v88, v89\n\t"
    "v_cvt_pk_bf16_f32 v81, v90, v91\n\t"
    "v_mfma_f32_16x16x32_bf16 v[84:87], v[168:171], a[136:139], v[84:87]\n\t"
    "v_mfma_f32_16x16x32_bf16 v[84:87], v[196:199], a[140:143], v[84:87]\n\t"
    "v_mfma_f32_16x16x32_bf16 v[88:91], v[104:107], a[144:147], 0\n\t"
    "buffer_store_dwordx4 v[152:155], v7, s[52:55], s61 offen offset:0 sc1\n\t"
    "v_mfma_f32_16x16x32_bf16 v[88:91], v[112:115], a[148:151], v[88:91]\n\t"
    "v_mfma_f32_16x16x32_bf16 v[88:91], v[120:123], a[152:155], v[88:91]\n\t"
    "v_mfma_f32_16x16x32_bf16 v[88:91], v[128:131], a[156:159], v[88:91]\n\t"
    "v_cvt_pk_bf16_f32 v82, v84, v85\n\t"
    "v_cvt_pk_bf16_f32 v83, v86, v87\n\t"
    "v_mfma_f32_16x16x32_bf16 v[88:91], v[132:135], a[160:163], v[88:91]\n\t"
    "v_mfma_f32_16x16x32_bf16 v[88:91], v[140:143], a[164:167], v[88:91]\n\t"
    "v_mfma_f32_16x16x32_bf16 v[92:95], v[144:147], a[144:147], 0\n\t"
    "v_mfma_f32_16x16x32_bf16 v[92:95], v[156:159], a[148:151], v[92:95]\n\t"
    "v_mfma_f32_16x16x32_bf16 v[92:95], v[160:163], a[152:155], v[92:95]\n\t"
    "v_mfma_f32_16x16x32_bf16 v[92:95], v[164:167], a[156:159], v[92:95]\n\t"
    "v_cvt_pk_bf16_f32 v84, v88, v89\n\t"
    "v_cvt_pk_bf16_f32 v85, v90, v91\n\t"
    "v_mfma_f32_16x16x32_bf16 v[92:95], v[168:171], a[160:163], v[92:95]\n\t"
    "v_mfma_f32_16x16x32_bf16 v[92:95], v[196:199], a[164:167], v[92:95]\n\t"
    "v_mfma_f32_16x16x32_bf16 v[96:99], v[104:107], a[168:171], 0\n\t"
    "buffer_store_dwordx4 v[124:127], v7, s[52:55], s61 offen offset:384 sc1\n\t"
    "v_mfma_f32_16x16x32_bf16 v[96:99], v[112:115], a[172:175], v[96:99]\n\t"
    "v_mfma_f32_16x16x32_bf16 v[96:99], v[120:123], a[176:179], v[96:99]\n\t"
    "v_mfma_f32_16x16x32_bf16 v[96:99], v[128:131], a[180:183], v[96:99]\n\t"
    "v_cvt_pk_bf16_f32 v86, v92, v93\n\t"
    "v_cvt_pk_bf16_f32 v87, v94, v95\n\t"
    "v_mfma_f32_16x16x32_bf16 v[96:99], v[132:135], a[184:187], v[96:99]\n\t"
    "v_mfma_f32_16x16x32_bf16 v[96:99], v[140:143], a[188:191], v[96:99]\n\t"
    "v_mfma_f32_16x16x32_bf16 v[92:95], v[144:147], a[168:171], 0\n\t"
    "v_mfma_f32_16x16x32_bf16 v[92:95], v[156:159], a[172:175], v[92:95]\n\t"
    "v_mfma_f32_16x16x32_bf16 v[92:95], v[160:163], a[176:179], v[92:95]\n\t"
    "v_mfma_f32_16x16x32_bf16 v[92:95], v[164:167], a[180:183], v[92:95]\n\t"
    "v_cvt_pk_bf16_f32 v88, v96, v97\n\t"
    "v_cvt_pk_bf16_f32 v89, v98, v99\n\t"
    "v_mfma_f32_16x16x32_bf16 v[92:95], v[168:171], a[184:187], v[92:95]\n\t"
    "v_mfma_f32_16x16x32_bf16 v[92:95], v[196:199], a[188:191], v[92:95]\n\t"
    "v_mfma_f32_16x16x32_bf16 v[96:99], v[104:107], a[192:195], 0\n\t"
    "buffer_store_dwordx4 v[192:195], v7, s[52:55], s61 offen offset:768 sc1\n\t"
    "v_mfma_f32_16x16x32_bf16 v[96:99], v[112:115], a[196:199], v[96:99]\n\t"
    "v_mfma_f32_16x16x32_bf16 v[96:99], v[120:123], a[200:203], v[96:99]\n\t"
    "v_mfma_f32_16x16x32_bf16 v[96:99], v[128:131], a[204:207], v[96:99]\n\t"
    "v_cvt_pk_bf16_f32 v90, v92, v93\n\t"
    "v_cvt_pk_bf16_f32 v91, v94, v95\n\t"
    "v_mfma_f32_16x16x32_bf16 v[96:99], v[132:135], a[208:211], v[96:99]\n\t"
    "v_mfma_f32_16x16x32_bf16 v[96:99], v[140:143], a[212:215], v[96:99]\n\t"
    "v_mfma_f32_16x16x32_bf16 v[100:103], v[144:147], a[192:195], 0\n\t"
    "v_mfma_f32_16x16x32_bf16 v[100:103], v[156:159], a[196:199], v[100:103]\n\t"
    "v_mfma_f32_16x16x32_bf16 v[100:103], v[160:163], a[200:203], v[100:103]\n\t"
    "v_mfma_f32_16x16x32_bf16 v[100:103], v[164:167], a[204:207], v[100:103]\n\t"
    "v_cvt_pk_bf16_f32 v92, v96, v97\n\t"
    "v_cvt_pk_bf16_f32 v93, v98, v99\n\t"
    "v_mfma_f32_16x16x32_bf16 v[100:103], v[168:171], a[208:211], v[100:103]\n\t"
    "s_barrier \n\t"
    "v_mfma_f32_16x16x32_bf16 v[100:103], v[196:199], a[212:215], v[100:103]\n\t"
    "v_mfma_f32_16x16x32_bf16 v[108:111], v[104:107], a[216:219], 0\n\t"
    "buffer_store_dwordx4 v[184:187], v7, s[52:55], s61 offen offset:1152 sc1\n\t"
    "v_mfma_f32_16x16x32_bf16 v[108:111], v[112:115], a[220:223], v[108:111]\n\t"
    "v_mfma_f32_16x16x32_bf16 v[108:111], v[120:123], a[224:227], v[108:111]\n\t"
    "v_mfma_f32_16x16x32_bf16 v[108:111], v[128:131], a[228:231], v[108:111]\n\t"
    "v_cvt_pk_bf16_f32 v94, v100, v101\n\t"
    "v_cvt_pk_bf16_f32 v95, v102, v103\n\t"
    "v_mfma_f32_16x16x32_bf16 v[108:111], v[132:135], a[232:235], v[108:111]\n\t"
    "v_mfma_f32_16x16x32_bf16 v[108:111], v[140:143], a[236:239], v[108:111]\n\t"
    "v_mfma_f32_16x16x32_bf16 v[100:103], v[144:147], a[216:219], 0\n\t"
    "v_mfma_f32_16x16x32_bf16 v[100:103], v[156:159], a[220:223], v[100:103]\n\t"
    "v_mfma_f32_16x16x32_bf16 v[100:103], v[160:163], a[224:227], v[100:103]\n\t"
    "v_mfma_f32_16x16x32_bf16 v[100:103], v[164:167], a[228:231], v[100:103]\n\t"
    "v_cvt_pk_bf16_f32 v96, v108, v109\n\t"
    "v_cvt_pk_bf16_f32 v97, v110, v111\n\t"
    "v_mfma_f32_16x16x32_bf16 v[100:103], v[168:171], a[232:235], v[100:103]\n\t"
    "v_mfma_f32_16x16x32_bf16 v[100:103], v[196:199], a[236:239], v[100:103]\n\t"
    "v_mfma_f32_16x16x32_bf16 v[108:111], v[104:107], a[240:243], 0\n\t"
    "buffer_store_dwordx2 v[172:173], v8, s[52:55], s61 offen offset:0 sc1\n\t"
    "v_mfma_f32_16x16x32_bf16 v[108:111], v[112:115], a[244:247], v[108:111]\n\t"
    "v_mfma_f32_16x16x32_bf16 v[108:111], v[120:123], a[248:251], v[108:111]\n\t"
    "v_mfma_f32_16x16x32_bf16 v[108:111], v[128:131], a[252:255], v[108:111]\n\t"
    "v_cvt_pk_bf16_f32 v98, v100, v101\n\t"
    "v_cvt_pk_bf16_f32 v99, v102, v103\n\t"
    "v_mfma_f32_16x16x32_bf16 v[108:111], v[132:135], v[224:227], v[108:111]\n\t"
    "v_mfma_f32_16x16x32_bf16 v[108:111], v[140:143], v[228:231], v[108:111]\n\t"
    "v_mfma_f32_16x16x32_bf16 v[116:119], v[144:147], a[240:243], 0\n\t"
    "v_mfma_f32_16x16x32_bf16 v[116:119], v[156:159], a[244:247], v[116:119]\n\t"
    "v_mfma_f32_16x16x32_bf16 v[116:119], v[160:163], a[248:251], v[116:119]\n\t"
    "v_mfma_f32_16x16x32_bf16 v[116:119], v[164:167], a[252:255], v[116:119]\n\t"
    "v_cvt_pk_bf16_f32 v100, v108, v109\n\t"
    "v_cvt_pk_bf16_f32 v101, v110, v111\n\t"
    "v_mfma_f32_16x16x32_bf16 v[116:119], v[168:171], v[224:227], v[116:119]\n\t"
    "v_mfma_f32_16x16x32_bf16 v[116:119], v[196:199], v[228:231], v[116:119]\n\t"
    "v_mfma_f32_16x16x32_bf16 v[108:111], v[104:107], v[232:235], 0\n\t"
    "buffer_store_dwordx2 v[174:175], v8, s[52:55], s61 offen offset:384 sc1\n\t"
    "v_mfma_f32_16x16x32_bf16 v[108:111], v[112:115], v[236:239], v[108:111]\n\t"
    "ds_read_b128 v[148:151], v6 offset:0\n\t"
    "v_mfma_f32_16x16x32_bf16 v[108:111], v[120:123], v[240:243], v[108:111]\n\t"
    "ds_read_b128 v[152:155], v6 offset:1024\n\t"
    "v_mfma_f32_16x16x32_bf16 v[108:111], v[128:131], v[244:247], v[108:111]\n\t"
    "v_cvt_pk_bf16_f32 v102, v116, v117\n\t"
    "v_cvt_pk_bf16_f32 v103, v118, v119\n\t"
    "v_mfma_f32_16x16x32_bf16 v[108:111], v[132:135], v[248:251], v[108:111]\n\t"
    "v_mfma_f32_16x16x32_bf16 v[108:111], v[140:143], v[252:255], v[108:111]\n\t"
    "v_mfma_f32_16x16x32_bf16 v[112:115], v[144:147], v[232:235], 0\n\t"
    "v_mfma_f32_16x16x32_bf16 v[112:115], v[156:159], v[236:239], v[112:115]\n\t"
    "v_mfma_f32_16x16x32_bf16 v[112:115], v[160:163], v[240:243], v[112:115]\n\t"
    "v_mfma_f32_16x16x32_bf16 v[112:115], v[164:167], v[244:247], v[112:115]\n\t"
    "v_cvt_pk_bf16_f32 v104, v108, v109\n\t"
    "v_cvt_pk_bf16_f32 v105, v110, v111\n\t"
    "v_mfma_f32_16x16x32_bf16 v[112:115], v[168:171], v[248:251], v[112:115]\n\t"
    "v_mfma_f32_16x16x32_bf16 v[112:115], v[196:199], v[252:255], v[112:115]\n\t"
    "s_waitcnt lgkmcnt(0)\n\t"
    "v_mfma_f32_16x16x32_bf16 v[120:123], v[148:151], v[12:15], 0\n\t"
    "buffer_store_dwordx2 v[188:189], v8, s[52:55], s61 offen offset:768 sc1\n\t"
    "v_mfma_f32_16x16x32_bf16 v[120:123], v[152:155], v[60:63], v[120:123]\n\t"
    "v_mfma_f32_16x16x32_bf16 v[124:127], v[148:151], v[16:19], 0\n\t"
    "v_mfma_f32_16x16x32_bf16 v[124:127], v[152:155], v[64:67], v[124:127]\n\t"
    "v_cvt_pk_bf16_f32 v106, v112, v113\n\t"
    "v_cvt_pk_bf16_f32 v107, v114, v115\n\t"
    "v_mfma_f32_16x16x32_bf16 v[128:131], v[148:151], v[20:23], 0\n\t"
    "v_mfma_f32_16x16x32_bf16 v[128:131], v[152:155], v[68:71], v[128:131]\n\t"
    "v_mfma_f32_16x16x32_bf16 v[132:135], v[148:151], v[24:27], 0\n\t"
    "v_mfma_f32_16x16x32_bf16 v[132:135], v[152:155], v[72:75], v[132:135]\n\t"
    "v_cvt_pk_bf16_f32 v156, v120, v124\n\t"
    "v_cvt_pk_bf16_f32 v108, v121, v125\n\t"
    "v_mfma_f32_16x16x32_bf16 v[136:139], v[148:151], v[28:31], 0\n\t"
    "v_cvt_pk_bf16_f32 v116, v122, v126\n\t"
    "v_cvt_pk_bf16_f32 v112, v123, v127\n\t"
    "v_mfma_f32_16x16x32_bf16 v[136:139], v[152:155], v[76:79], v[136:139]\n\t"
    "v_mfma_f32_16x16x32_bf16 v[140:143], v[148:151], v[32:35], 0\n\t"
    "v_mfma_f32_16x16x32_bf16 v[140:143], v[152:155], v[80:83], v[140:143]\n\t"
    "v_cvt_pk_bf16_f32 v157, v128, v132\n\t"
    "v_cvt_pk_bf16_f32 v109, v129, v133\n\t"
    "v_mfma_f32_16x16x32_bf16 v[144:147], v[148:151], v[36:39], 0\n\t"
    "buffer_store_dwordx2 v[190:191], v8, s[52:55], s61 offen offset:1152 sc1\n\t"
    "v_mfma_f32_16x16x32_bf16 v[144:147], v[152:155], v[84:87], v[144:147]\n\t"
    "ds_read_b128 v[120:123], v6 offset:2048\n\t"
    "s_add_u32 s61, s61, 11403264\n\t"
    "v_mfma_f32_16x16x32_bf16 v[160:163], v[148:151], v[40:43], 0\n\t"
    "ds_read_b128 v[124:127], v6 offset:3072\n\t"
    "v_cvt_pk_bf16_f32 v117, v130, v134\n\t"
    "v_mfma_f32_16x16x32_bf16 v[160:163], v[152:155], v[88:91], v[160:163]\n\t"
    "v_cvt_pk_bf16_f32 v113, v131, v135\n\t"
    "v_cvt_pk_bf16_f32 v158, v136, v140\n\t"
    "v_mfma_f32_16x16x32_bf16 v[164:167], v[148:151], v[44:47], 0\n\t"
    "v_cvt_pk_bf16_f32 v110, v137, v141\n\t"
    "v_cvt_pk_bf16_f32 v118, v138, v142\n\t"
    "v_mfma_f32_16x16x32_bf16 v[164:167], v[152:155], v[92:95], v[164:167]\n\t"
    "v_cvt_pk_bf16_f32 v114, v139, v143\n\t"
    "v_mfma_f32_16x16x32_bf16 v[168:171], v[148:151], v[48:51], 0\n\t"
    "v_mfma_f32_16x16x32_bf16 v[168:171], v[152:155], v[96:99], v[168:171]\n\t"
    "v_cvt_pk_bf16_f32 v159, v144, v160\n\t"
    "v_cvt_pk_bf16_f32 v111, v145, v161\n\t"
    "v_mfma_f32_16x16x32_bf16 v[128:131], v[148:151], v[52:55], 0\n\t"
    "v_cvt_pk_bf16_f32 v119, v146, v162\n\t"
    "v_cvt_pk_bf16_f32 v115, v147, v163\n\t"
    "v_mfma_f32_16x16x32_bf16 v[128:131], v[152:155], v[100:103], v[128:131]\n\t"
    "v_mfma_f32_16x16x32_bf16 v[132:135], v[148:151], v[56:59], 0\n\t"
    "v_mfma_f32_16x16x32_bf16 v[132:135], v[152:155], v[104:107], v[132:135]\n\t"
    "buffer_store_dwordx4 v[156:159], v7, s[52:55], s61 offen offset:0 sc1\n\t"
    "s_waitcnt lgkmcnt(0)\n\t"
    "v_mfma_f32_16x16x32_bf16 v[140:143], v[120:123], v[12:15], 0\n\t"
    ".Lh2_%=:\n\t"
    "s_cbranch_execz .Lh6_%=\n\t"
    "v_cvt_pk_bf16_f32 v136, v164, v168\n\t"
    "v_cvt_pk_bf16_f32 v138, v165, v169\n\t"
    "v_mfma_f32_16x16x32_bf16 v[140:143], v[124:127], v[60:63], v[140:143]\n\t"
    "v_cvt_pk_bf16_f32 v144, v166, v170\n\t"
    "v_cvt_pk_bf16_f32 v146, v167, v171\n\t"
    "v_mfma_f32_16x16x32_bf16 v[148:151], v[120:123], v[16:19], 0\n\t"
    "v_mfma_f32_16x16x32_bf16 v[148:151], v[124:127], v[64:67], v[148:151]\n\t"
    "v_cvt_pk_bf16_f32 v137, v128, v132\n\t"
    "v_cvt_pk_bf16_f32 v139, v129, v133\n\t"
    "v_mfma_f32_16x16x32_bf16 v[152:155], v[120:123], v[20:23], 0\n\t"
    "v_cvt_pk_bf16_f32 v145, v130, v134\n\t"
    "v_cvt_pk_bf16_f32 v147, v131, v135\n\t"
    "v_mfma_f32_16x16x32_bf16 v[152:155], v[124:127], v[68:71], v[152:155]\n\t"
    "v_mfma_f32_16x16x32_bf16 v[156:159], v[120:123], v[24:27], 0\n\t"
    "v_mfma_f32_16x16x32_bf16 v[156:159], v[124:127], v[72:75], v[156:159]\n\t"
    "v_cvt_pk_bf16_f32 v132, v140, v148\n\t"
    "v_cvt_pk_bf16_f32 v128, v141, v149\n\t"
    "v_mfma_f32_16x16x32_bf16 v[168:171], v[120:123], v[28:31], 0\n\t"
    "v_cvt_pk_bf16_f32 v164, v142, v150\n\t"
    "v_cvt_pk_bf16_f32 v160, v143, v151\n\t"
    "v_mfma_f32_16x16x32_bf16 v[168:171], v[124:127], v[76:79], v[168:171]\n\t"
    "v_mfma_f32_16x16x32_bf16 v[172:175], v[120:123], v[32:35], 0\n\t"
    "v_mfma_f32_16x16x32_bf16 v[172:175], v[124:127], v[80:83], v[172:175]\n\t"
    "buffer_store_dwordx4 v[108:111], v7, s[52:55], s61 offen offset:384 sc1\n\t"
    "v_mfma_f32_16x16x32_bf16 v[140:143], v[120:123], v[36:39], 0\n\t"
    "v_cvt_pk_bf16_f32 v133, v152, v156\n\t"
    "v_cvt_pk_bf16_f32 v129, v153, v157\n\t"
    "v_mfma_f32_16x16x32_bf16 v[140:143], v[124:127], v[84:87], v[140:143]\n\t"
    "ds_read_b128 v[148:151], v6 offset:4096\n\t"
    "v_cvt_pk_bf16_f32 v165, v154, v158\n\t"
    "v_mfma_f32_16x16x32_bf16 v[176:179], v[120:123], v[40:43], 0\n\t"
    "ds_read_b128 v[108:111], v6 offset:5120\n\t"
    "v_cvt_pk_bf16_f32 v161, v155, v159\n\t"
    "v_mfma_f32_16x16x32_bf16 v[176:179], v[124:127], v[88:91], v[176:179]\n\t"
    "v_cvt_pk_bf16_f32 v134, v168, v172\n\t"
    "v_cvt_pk_bf16_f32 v130, v169, v173\n\t"
    "v_mfma_f32_16x16x32_bf16 v[180:183], v[120:123], v[44:47], 0\n\t"
    "v_cvt_pk_bf16_f32 v166, v170, v174\n\t"
    "v_cvt_pk_bf16_f32 v162, v171, v175\n\t"
    "v_mfma_f32_16x16x32_bf16 v[180:183], v[124:127], v[92:95], v[180:183]\n\t"
    "v_mfma_f32_16x16x32_bf16 v[152:155], v[120:123], v[48:51], 0\n\t"
    "v_mfma_f32_16x16x32_bf16 v[152:155], v[124:127], v[96:99], v[152:155]\n\t"
    "v_cvt_pk_bf16_f32 v135, v140, v176\n\t"
    "v_cvt_pk_bf16_f32 v131, v141, v177\n\t"
    "v_mfma_f32_16x16x32_bf16 v[156:159], v[120:123], v[52:55], 0\n\t"
    "v_cvt_pk_bf16_f32 v167, v142, v178\n\t"
    "v_cvt_pk_bf16_f32 v163, v143, v179\n\t"
    "v_mfma_f32_16x16x32_bf16 v[156:159], v[124:127], v[100:103], v[156:159]\n\t"
    "v_mfma_f32_16x16x32_bf16 v[168:171], v[120:123], v[56:59], 0\n\t"
    "v_mfma_f32_16x16x32_bf16 v[168:171], v[124:127], v[104:107], v[168:171]\n\t"
    "buffer_store_dwordx4 v[116:119], v7, s[52:55], s61 offen offset:768 sc1\n\t"
    "s_waitcnt lgkmcnt(0)\n\t"
    "v_mfma_f32_16x16x32_bf16 v[172:175], v[148:151], v[12:15], 0\n\t"
    "v_cvt_pk_bf16_f32 v140, v180, v152\n\t"
    "v_cvt_pk_bf16_f32 v142, v181, v153\n\t"
    "v_mfma_f32_16x16x32_bf16 v[172:175], v[108:111], v[60:63], v[172:175]\n\t"
    "v_cvt_pk_bf16_f32 v120, v182, v154\n\t"
    "v_cvt_pk_bf16_f32 v122, v183, v155\n\t"
    "v_mfma_f32_16x16x32_bf16 v[116:119], v[148:151], v[16:19], 0\n\t"
    "v_mfma_f32_16x16x32_bf16 v[116:119], v[108:111], v[64:67], v[116:119]\n\t"
    "v_cvt_pk_bf16_f32 v141, v156, v168\n\t"
    "v_cvt_pk_bf16_f32 v143, v157, v169\n\t"
    "v_mfma_f32_16x16x32_bf16 v[124:127], v[148:151], v[20:23], 0\n\t"
    "v_cvt_pk_bf16_f32 v121, v158, v170\n\t"
    "v_cvt_pk_bf16_f32 v123, v159, v171\n\t"
    "v_mfma_f32_16x16x32_bf16 v[124:127], v[108:111], v[68:71], v[124:127]\n\t"
    "v_mfma_f32_16x16x32_bf16 v[152:155], v[148:151], v[24:27], 0\n\t"
    "v_mfma_f32_16x16x32_bf16 v[152:155], v[108:111], v[72:75], v[152:155]\n\t"
    "v_cvt_pk_bf16_f32 v168, v172, v116\n\t"
    "v_cvt_pk_bf16_f32 v156, v173, v117\n\t"
    "v_mfma_f32_16x16x32_bf16 v[184:187], v[148:151], v[28:31], 0\n\t"
    "v_cvt_pk_bf16_f32 v180, v174, v118\n\t"
    "v_cvt_pk_bf16_f32 v176, v175, v119\n\t"
    "v_mfma_f32_16x16x32_bf16 v[184:187], v[108:111], v[76:79], v[184:187]\n\t"
    "v_mfma_f32_16x16x32_bf16 v[188:191], v[148:151], v[32:35], 0\n\t"
    "v_mfma_f32_16x16x32_bf16 v[188:191], v[108:111], v[80:83], v[188:191]\n\t"
    "buffer_store_dwordx4 v[112:115], v7, s[52:55], s61 offen offset:1152 sc1\n\t"
    "v_mfma_f32_16x16x32_bf16 v[116:119], v[148:151], v[36:39], 0\n\t"
    "s_barrier \n\t"
    "v_cvt_pk_bf16_f32 v169, v124, v152\n\t"
    "v_mfma_f32_16x16x32_bf16 v[116:119], v[108:111], v[84:87], v[116:119]\n\t"
    "ds_read_b128 v[172:175], v6 offset:6144\n\t"
    "v_cvt_pk_bf16_f32 v157, v125, v153\n\t"
    "v_mfma_f32_16x16x32_bf16 v[192:195], v[148:151], v[40:43], 0\n\t"
    "ds_read_b128 v[112:115], v6 offset:7168\n\t"
    "v_cvt_pk_bf16_f32 v181, v126, v154\n\t"
    "v_mfma_f32_16x16x32_bf16 v[192:195], v[108:111], v[88:91], v[192:195]\n\t"
    "v_cvt_pk_bf16_f32 v177, v127, v155\n\t"
    "v_cvt_pk_bf16_f32 v170, v184, v188\n\t"
    "v_mfma_f32_16x16x32_bf16 v[196:199], v[148:151], v[44:47], 0\n\t"
    "v_cvt_pk_bf16_f32 v158, v185, v189\n\t"
    "v_cvt_pk_bf16_f32 v182, v186, v190\n\t"
    "v_mfma_f32_16x16x32_bf16 v[196:199], v[108:111], v[92:95], v[196:199]\n\t"
    "v_cvt_pk_bf16_f32 v178, v187, v191\n\t"
    "v_mfma_f32_16x16x32_bf16 v[124:127], v[148:151], v[48:51], 0\n\t"
    "v_mfma_f32_16x16x32_bf16 v[124:127], v[108:111], v[96:99], v[124:127]\n\t"
    "v_cvt_pk_bf16_f32 v171, v116, v192\n\t"
    "v_cvt_pk_bf16_f32 v159, v117, v193\n\t"
    "v_mfma_f32_16x16x32_bf16 v[152:155], v[148:151], v[52:55], 0\n\t"
    "v_cvt_pk_bf16_f32 v183, v118, v194\n\t"
    "v_cvt_pk_bf16_f32 v179, v119, v195\n\t"
    "v_mfma_f32_16x16x32_bf16 v[152:155], v[108:111], v[100:103], v[152:155]\n\t"
    "v_mfma_f32_16x16x32_bf16 v[184:187], v[148:151], v[56:59], 0\n\t"
    "v_mfma_f32_16x16x32_bf16 v[184:187], v[108:111], v[104:107], v[184:187]\n\t"
    "buffer_store_dwordx2 v[136:137], v8, s[52:55], s61 offen offset:0 sc1\n\t"
    "s_waitcnt lgkmcnt(0)\n\t"
    "v_mfma_f32_16x16x32_bf16 v[188:191], v[172:175], v[12:15], 0\n\t"
    "v_cvt_pk_bf16_f32 v116, v196, v124\n\t"
    "v_cvt_pk_bf16_f32 v118, v197, v125\n\t"
    "v_mfma_f32_16x16x32_bf16 v[188:191], v[112:115], v[60:63], v[188:191]\n\t"
    "v_cvt_pk_bf16_f32 v148, v198, v126\n\t"
    "v_cvt_pk_bf16_f32 v150, v199, v127\n\t"
    "v_mfma_f32_16x16x32_bf16 v[108:111], v[172:175], v[16:19], 0\n\t"
    "v_mfma_f32_16x16x32_bf16 v[108:111], v[112:115], v[64:67], v[108:111]\n\t"
    "v_cvt_pk_bf16_f32 v117, v152, v184\n\t"
    "v_cvt_pk_bf16_f32 v119, v153, v185\n\t"
    "v_mfma_f32_16x16x32_bf16 v[12:15], v[172:175], v[20:23], 0\n\t"
    "v_cvt_pk_bf16_f32 v149, v154, v186\n\t"
    "v_cvt_pk_bf16_f32 v151, v155, v187\n\t"
    "v_mfma_f32_16x16x32_bf16 v[12:15], v[112:115], v[68:71], v[12:15]\n\t"
    "v_mfma_f32_16x16x32_bf16 v[16:19], v[172:175], v[24:27], 0\n\t"
    "v_mfma_f32_16x16x32_bf16 v[16:19], v[112:115], v[72:75], v[16:19]\n\t"
    "v_cvt_pk_bf16_f32 v152, v188, v108\n\t"
    "v_cvt_pk_bf16_f32 v124, v189, v109\n\t"
    "v_mfma_f32_16x16x32_bf16 v[20:23], v[172:175], v[28:31], 0\n\t"
    "v_cvt_pk_bf16_f32 v192, v190, v110\n\t"
    "v_cvt_pk_bf16_f32 v184, v191, v111\n\t"
    "v_mfma_f32_16x16x32_bf16 v[20:23], v[112:115], v[76:79], v[20:23]\n\t"
    "v_mfma_f32_16x16x32_bf16 v[24:27], v[172:175], v[32:35], 0\n\t"
    "v_mfma_f32_16x16x32_bf16 v[24:27], v[112:115], v[80:83], v[24:27]\n\t"
    "buffer_store_dwordx2 v[138:139], v8, s[52:55], s61 offen offset:384 sc1\n\t"
    "v_mfma_f32_16x16x32_bf16 v[28:31], v[172:175], v[36:39], 0\n\t"
    "ds_read_b128 v[60:63], v0 offset:0\n\t"
    "v_cvt_pk_bf16_f32 v153, v12, v16\n\t"
    "v_mfma_f32_16x16x32_bf16 v[28:31], v[112:115], v[84:87], v[28:31]\n\t"
    "ds_read_b128 v[64:67], v1 offset:0\n\t"
    "v_cvt_pk_bf16_f32 v125, v13, v17\n\t"
    "v_mfma_f32_16x16x32_bf16 v[32:35], v[172:175], v[40:43], 0\n\t"
    "ds_read_b128 v[68:71], v2 offset:0\n\t"
    "v_cvt_pk_bf16_f32 v193, v14, v18\n\t"
    "v_mfma_f32_16x16x32_bf16 v[32:35], v[112:115], v[88:91], v[32:35]\n\t"
    "ds_read_b128 v[72:75], v3 offset:0\n\t"
    "v_cvt_pk_bf16_f32 v185, v15, v19\n\t"
    "v_mfma_f32_16x16x32_bf16 v[36:39], v[172:175], v[44:47], 0\n\t"
    "ds_read_b128 v[76:79], v4 offset:0\n\t"
    "v_cvt_pk_bf16_f32 v154, v20, v24\n\t"
    "v_mfma_f32_16x16x32_bf16 v[36:39], v[112:115], v[92:95], v[36:39]\n\t"
    "ds_read_b128 v[80:83], v5 offset:0\n\t"
    "v_cvt_pk_bf16_f32 v126, v21, v25\n\t"
    "v_mfma_f32_16x16x32_bf16 v[12:15], v[172:175], v[48:51], 0\n\t"
    "ds_read_b128 v[84:87], v0 offset:6144\n\t"
    "v_cvt_pk_bf16_f32 v194, v22, v26\n\t"
    "v_mfma_f32_16x16x32_bf16 v[12:15], v[112:115], v[96:99], v[12:15]\n\t"
    "ds_read_b128 v[88:91], v1 offset:6144\n\t"
    "v_cvt_pk_bf16_f32 v186, v23, v27\n\t"
    "v_mfma_f32_16x16x32_bf16 v[16:19], v[172:175], v[52:55], 0\n\t"
    "ds_read_b128 v[92:95], v2 offset:6144\n\t"
    "v_cvt_pk_bf16_f32 v155, v28, v32\n\t"
    "v_mfma_f32_16x16x32_bf16 v[16:19], v[112:115], v[100:103], v[16:19]\n\t"
    "ds_read_b128 v[108:111], v3 offset:6144\n\t"
    "v_cvt_pk_bf16_f32 v127, v29, v33\n\t"
    "v_mfma_f32_16x16x32_bf16 v[20:23], v[172:175], v[56:59], 0\n\t"
    "ds_read_b128 v[96:99], v4 offset:6144\n\t"
    "v_cvt_pk_bf16_f32 v195, v30, v34\n\t"
    "v_mfma_f32_16x16x32_bf16 v[20:23], v[112:115], v[104:107], v[20:23]\n\t"
    "ds_read_b128 v[136:139], v5 offset:6144\n\t"
    "s_sub_u32 s60, s60, 1\n\t"
    "s_cmp_lg_u32 s60, 0\n\t"
    "s_cbranch_scc1 .Lmin_%=\n\t"
    "s_nop 7\n\t"
    "s_nop 1\n\t"
    "buffer_store_dwordx2 v[144:145], v8, s[52:55], s61 offen offset:768 sc1\n\t"
    "v_cvt_pk_bf16_f32 v187, v31, v35\n\t"
    "v_cvt_pk_bf16_f32 v172, v36, v12\n\t"
    "v_cvt_pk_bf16_f32 v174, v37, v13\n\t"
    "v_cvt_pk_bf16_f32 v188, v38, v14\n\t"
    "v_cvt_pk_bf16_f32 v190, v39, v15\n\t"
    "v_cvt_pk_bf16_f32 v173, v16, v20\n\t"
    "v_cvt_pk_bf16_f32 v175, v17, v21\n\t"
    "v_cvt_pk_bf16_f32 v189, v18, v22\n\t"
    "v_cvt_pk_bf16_f32 v191, v19, v23\n\t"
    "buffer_store_dwordx2 v[146:147], v8, s[52:55], s61 offen offset:1152 sc1\n\t"
    "s_add_u32 s61, s61, 393216\n\t"
    "buffer_store_dwordx4 v[132:135], v7, s[52:55], s61 offen offset:0 sc1\n\t"
    "buffer_store_dwordx4 v[128:131], v7, s[52:55], s61 offen offset:384 sc1\n\t"
    "buffer_store_dwordx4 v[164:167], v7, s[52:55], s61 offen offset:768 sc1\n\t"
    "buffer_store_dwordx4 v[160:163], v7, s[52:55], s61 offen offset:1152 sc1\n\t"
    "buffer_store_dwordx2 v[140:141], v8, s[52:55], s61 offen offset:0 sc1\n\t"
    "buffer_store_dwordx2 v[142:143], v8, s[52:55], s61 offen offset:384 sc1\n\t"
    "buffer_store_dwordx2 v[120:121], v8, s[52:55], s61 offen offset:768 sc1\n\t"
    "buffer_store_dwordx2 v[122:123], v8, s[52:55], s61 offen offset:1152 sc1\n\t"
    "s_add_u32 s61, s61, 393216\n\t"
    "buffer_store_dwordx4 v[168:171], v7, s[52:55], s61 offen offset:0 sc1\n\t"
    "buffer_store_dwordx4 v[156:159], v7, s[52:55], s61 offen offset:384 sc1\n\t"
    "buffer_store_dwordx4 v[180:183], v7, s[52:55], s61 offen offset:768 sc1\n\t"
    "buffer_store_dwordx4 v[176:179], v7, s[52:55], s61 offen offset:1152 sc1\n\t"
    "buffer_store_dwordx2 v[116:117], v8, s[52:55], s61 offen offset:0 sc1\n\t"
    "buffer_store_dwordx2 v[118:119], v8, s[52:55], s61 offen offset:384 sc1\n\t"
    "buffer_store_dwordx2 v[148:149], v8, s[52:55], s61 offen offset:768 sc1\n\t"
    "buffer_store_dwordx2 v[150:151], v8, s[52:55], s61 offen offset:1152 sc1\n\t"
    "s_add_u32 s61, s61, 393216\n\t"
    "buffer_store_dwordx4 v[152:155], v7, s[52:55], s61 offen offset:0 sc1\n\t"
    "buffer_store_dwordx4 v[124:127], v7, s[52:55], s61 offen offset:384 sc1\n\t"
    "buffer_store_dwordx4 v[192:195], v7, s[52:55], s61 offen offset:768 sc1\n\t"
    "buffer_store_dwordx4 v[184:187], v7, s[52:55], s61 offen offset:1152 sc1\n\t"
    "buffer_store_dwordx2 v[172:173], v8, s[52:55], s61 offen offset:0 sc1\n\t"
    "buffer_store_dwordx2 v[174:175], v8, s[52:55], s61 offen offset:384 sc1\n\t"
    "buffer_store_dwordx2 v[188:189], v8, s[52:55], s61 offen offset:768 sc1\n\t"
    "buffer_store_dwordx2 v[190:191], v8, s[52:55], s61 offen offset:1152 sc1\n\t"
    "s_add_u32 s61, s61, 11403264\n\t"
    "s_endpgm\n\t"
    ".p2align 12\n\t"
    ".Lroln_%=:\n\t"
    "v_lshrrev_b32_e32 v20, 3, v31\n\t"
    "v_and_b32_e32 v21, 6, v20\n\t"
    "v_and_b32_e32 v28, 7, v31\n\t"
    "v_xor_b32_e32 v21, v21, v28\n\t"
    "v_lshlrev_b32_e32 v20, 7, v20\n\t"
    "v_lshl_or_b32 v7, v21, 4, v20\n\t"
    "s_mov_b32 s54, 0x6000000\n\t"
    "s_mov_b32 s55, 0x20000\n\t"
    "s_waitcnt lgkmcnt(0)\n\t"
    "s_mov_b32 s52, s36\n\t"
    "s_and_b32 s53, s37, 0xffff\n\t"
    "s_barrier\n\t"
    "s_add_u32 m0, %[rbeg], 0\n\t"
    "s_add_u32 s61, %[wb], 0\n\t"
    "buffer_load_dwordx4 v7, s[52:55], s61 offen offset:0 nt lds\n\t"
    "buffer_load_dwordx4 v7, s[52:55], s61 offen offset:1024 nt lds\n\t"
    "buffer_load_dwordx4 v7, s[52:55], s61 offen offset:2048 nt lds\n\t"
    "s_add_u32 m0, %[rbeg], 3072\n\t"
    "s_add_u32 s61, %[wb], 196608\n\t"
    "buffer_load_dwordx4 v7, s[52:55], s61 offen offset:0 nt lds\n\t"
    "buffer_load_dwordx4 v7, s[52:55], s61 offen offset:1024 nt lds\n\t"
    "buffer_load_dwordx4 v7, s[52:55], s61 offen offset:2048 nt lds\n\t"
    "v_lshlrev_b32_e32 v30, 4, v31\n\t"
    "v_and_b32_e32 v28, 7, v31\n\t"
    "v_bfe_u32 v27, v31, 3, 1\n\t"
    "v_lshrrev_b32_e32 v29, 4, v31\n\t"
    "v_add_u32_e32 v6, %[pl], v30\n\t"
    "v_mul_u32_u24_e32 v27, 0xc00, v27\n\t"
    "v_add_u32_e32 v27, %[rbeg], v27\n\t"
    "v_lshlrev_b32_e32 v26, 7, v31\n\t"
    "v_mad_u32_u24 v20, v28, 3, 0\n\t"
    "v_and_b32_e32 v21, 6, v20\n\t"
    "v_xor_b32_e32 v21, v21, v29\n\t"
    "v_lshl_add_u32 v20, v20, 7, v27\n\t"
    "v_lshl_add_u32 v0, v21, 4, v20\n\t"
    "v_xor_b32_e32 v1, 64, v0\n\t"
    "v_mad_u32_u24 v20, v28, 3, 1\n\t"
    "v_and_b32_e32 v21, 6, v20\n\t"
    "v_xor_b32_e32 v21, v21, v29\n\t"
    "v_lshl_add_u32 v20, v20, 7, v27\n\t"
    "v_lshl_add_u32 v2, v21, 4, v20\n\t"
    "v_xor_b32_e32 v3, 64, v2\n\t"
    "v_mad_u32_u24 v20, v28, 3, 2\n\t"
    "v_and_b32_e32 v21, 6, v20\n\t"
    "v_xor_b32_e32 v21, v21, v29\n\t"
    "v_lshl_add_u32 v20, v20, 7, v27\n\t"
    "v_lshl_add_u32 v4, v21, 4, v20\n\t"
    "v_xor_b32_e32 v5, 64, v4\n\t"
    "s_mov_b32 s56, s42\n\t"
    "s_and_b32 s57, s43, 0xffff\n\t"
    "s_mov_b32 s58, 0x40000\n\t"
    "s_mov_b32 s59, 0x20000\n\t"
    "v_add_u32_e32 v8, 0x9000, v30\n\t"
    "v_add_u32_e32 v9, 0x1b000, v30\n\t"
    "s_mov_b32 s68, 0x3dd53b95\n\t"
    "s_add_u32 m0, %[rbeg], 6144\n\t"
    "s_add_u32 s61, %[wb], 393216\n\t"
    "buffer_load_dwordx4 v7, s[52:55], s61 offen offset:0 nt lds\n\t"
    "buffer_load_dwordx4 v7, s[52:55], s61 offen offset:1024 nt lds\n\t"
    "buffer_load_dwordx4 v7, s[52:55], s61 offen offset:2048 nt lds\n\t"
    "s_mov_b64 exec, 0\n\t"
    "s_cmp_eq_u32 %[pair], 0\n\t"
    "s_cbranch_scc1 .Lh4_%=\n\t"
    "s_branch .Lh1_%=\n\t"
    ".LrM_%=:\n\t"
    "s_mov_b64 exec, -1\n\t"
    "s_sleep 6\n\t"
    "s_add_u32 m0, %[rbeg], 9216\n\t"
    "s_add_u32 s61, %[wb], 589824\n\t"
    "buffer_load_dwordx4 v7, s[52:55], s61 offen offset:0 nt lds\n\t"
    "buffer_load_dwordx4 v7, s[52:55], s61 offen offset:1024 nt lds\n\t"
    "buffer_load_dwordx4 v7, s[52:55], s61 offen offset:2048 nt lds\n\t"
    "s_add_u32 m0, %[rbeg], 12288\n\t"
    "s_add_u32 s61, %[wb], 786432\n\t"
    "buffer_load_dwordx4 v7, s[52:55], s61 offen offset:0 nt lds\n\t"
    "buffer_load_dwordx4 v7, s[52:55], s61 offen offset:1024 nt lds\n\t"
    "buffer_load_dwordx4 v7, s[52:55], s61 offen offset:2048 nt lds\n\t"
    "s_add_u32 m0, %[rbeg], 15360\n\t"
    "s_add_u32 s61, %[wb], 983040\n\t"
    "buffer_load_dwordx4 v7, s[52:55], s61 offen offset:0 nt lds\n\t"
    "buffer_load_dwordx4 v7, s[52:55], s61 offen offset:1024 nt lds\n\t"
    "buffer_load_dwordx4 v7, s[52:55], s61 offen offset:2048 nt lds\n\t"
    "s_add_u32 m0, %[rbeg], 18432\n\t"
    "s_add_u32 s61, %[wb], 1179648\n\t"
    "buffer_load_dwordx4 v7, s[52:55], s61 offen offset:0 nt lds\n\t"
    "buffer_load_dwordx4 v7, s[52:55], s61 offen offset:1024 nt lds\n\t"
    "buffer_load_dwordx4 v7, s[52:55], s61 offen offset:2048 nt lds\n\t"
    "s_add_u32 m0, %[rbeg], 21504\n\t"
    "s_add_u32 s61, %[wb], 1376256\n\t"
    "buffer_load_dwordx4 v7, s[52:55], s61 offen offset:0 nt lds\n\t"
    "buffer_load_dwordx4 v7, s[52:55], s61 offen offset:1024 nt lds\n\t"
    "buffer_load_dwordx4 v7, s[52:55], s61 offen offset:2048 nt lds\n\t"
    "s_waitcnt vmcnt(0)\n\t"
    "s_barrier\n\t"
    "ds_read_b128 v[196:199], v0 offset:6144\n\t"
    "ds_read_b128 v[192:195], v1 offset:6144\n\t"
    "ds_read_b128 v[188:191], v2 offset:6144\n\t"
    "ds_read_b128 v[172:175], v0 offset:12288\n\t"
    "ds_read_b128 v[168:171], v1 offset:12288\n\t"
    "ds_read_b128 v[164:167], v2 offset:12288\n\t"
    "ds_read_b128 v[148:151], v0 offset:18432\n\t"
    "ds_read_b128 v[144:147], v1 offset:18432\n\t"
    "ds_read_b128 v[140:143], v2 offset:18432\n\t"
    "ds_read_b128 v[184:187], v3 offset:6144\n\t"
    "ds_read_b128 v[180:183], v4 offset:6144\n\t"
    "ds_read_b128 v[176:179], v5 offset:6144\n\t"
    "ds_read_b128 v[208:211], v3 offset:0\n\t"
    "ds_read_b128 v[204:207], v4 offset:0\n\t"
    "ds_read_b128 v[200:203], v5 offset:0\n\t"
    "ds_read_b128 v[160:163], v3 offset:12288\n\t"
    "ds_read_b128 v[156:159], v4 offset:12288\n\t"
    "ds_read_b128 v[152:155], v5 offset:12288\n\t"
    "ds_read_b128 v[136:139], v3 offset:18432\n\t"
    "ds_read_b128 v[132:135], v4 offset:18432\n\t"
    "ds_read_b128 v[128:131], v5 offset:18432\n\t"
    "ds_read_b128 v[220:223], v0 offset:0\n\t"
    "ds_read_b128 v[216:219], v1 offset:0\n\t"
    "ds_read_b128 v[212:215], v2 offset:0\n\t"
    "ds_read_b128 v[28:31], v3 offset:0\n\t"
    "ds_read_b128 v[32:35], v4 offset:0\n\t"
    "ds_read_b128 v[36:39], v5 offset:0\n\t"
    "s_add_i32 s63, %[rbeg], 24576\n\t"
    "s_add_i32 s62, %[rbeg], 49152\n\t"
    "s_barrier\n\t"
    "ds_read_b128 a[0:3], v8 offset:0\n\t"
    "ds_read_b128 a[8:11], v8 offset:1024\n\t"
    "ds_read_b128 a[16:19], v8 offset:2048\n\t"
    "ds_read_b128 a[24:27], v8 offset:3072\n\t"
    "ds_read_b128 a[32:35], v8 offset:4096\n\t"
    "ds_read_b128 a[40:43], v8 offset:5120\n\t"
    "ds_read_b128 a[4:7], v9 offset:0\n\t"
    "ds_read_b128 a[12:15], v9 offset:1024\n\t"
    "ds_read_b128 a[20:23], v9 offset:2048\n\t"
    "ds_read_b128 a[28:31], v9 offset:3072\n\t"
    "ds_read_b128 a[36:39], v9 offset:4096\n\t"
    "s_waitcnt lgkmcnt(10)\n\t"
    "v_mfma_f32_16x16x32_bf16 v[64:67], a[0:3], v[220:223], 0\n\t"
    "ds_read_b128 a[44:47], v9 offset:5120\n\t"
    "s_waitcnt lgkmcnt(10)\n\t"
    "v_mfma_f32_16x16x32_bf16 v[64:67], a[8:11], v[216:219], v[64:67]\n\t"
    "s_barrier\n\t"
    "ds_read_b128 a[48:51], v8 offset:6144\n\t"
    "s_waitcnt lgkmcnt(10)\n\t"
    "v_mfma_f32_16x16x32_bf16 v[64:67], a[16:19], v[212:215], v[64:67]\n\t"
    "ds_read_b128 a[56:59], v8 offset:7168\n\t"
    "s_waitcnt lgkmcnt(10)\n\t"
    "v_mfma_f32_16x16x32_bf16 v[64:67], a[24:27], v[28:31], v[64:67]\n\t"
    "ds_read_b128 a[64:67], v8 offset:8192\n\t"
    "s_waitcnt lgkmcnt(4)\n\t"
    "v_mfma_f32_16x16x32_bf16 v[64:67], a[32:35], v[32:35], v[64:67]\n\t"
    "ds_read_b128 a[72:75], v8 offset:9216\n\t"
    "v_mfma_f32_16x16x32_bf16 v[64:67], a[40:43], v[36:39], v[64:67]\n\t"
    "ds_read_b128 a[80:83], v8 offset:10240\n\t"
    "v_mfma_f32_16x16x32_bf16 v[40:43], a[4:7], v[220:223], 0\n\t"
    "ds_read_b128 a[88:91], v8 offset:11264\n\t"
    "v_mfma_f32_16x16x32_bf16 v[40:43], a[12:15], v[216:219], v[40:43]\n\t"
    "ds_read_b128 a[52:55], v9 offset:6144\n\t"
    "v_mfma_f32_16x16x32_bf16 v[40:43], a[20:23], v[212:215], v[40:43]\n\t"
    "ds_read_b128 a[60:63], v9 offset:7168\n\t"
    "v_mfma_f32_16x16x32_bf16 v[40:43], a[28:31], v[28:31], v[40:43]\n\t"
    "ds_read_b128 a[68:71], v9 offset:8192\n\t"
    "v_mfma_f32_16x16x32_bf16 v[40:43], a[36:39], v[32:35], v[40:43]\n\t"
    "v_cvt_pk_bf16_f32 v44, v64, v65\n\t"
    "v_cvt_pk_bf16_f32 v45, v66, v67\n\t"
    "ds_read_b128 a[76:79], v9 offset:9216\n\t"
    "s_waitcnt lgkmcnt(4)\n\t"
    "v_mfma_f32_16x16x32_bf16 v[40:43], a[44:47], v[36:39], v[40:43]\n\t"
    "ds_read_b128 a[84:87], v9 offset:10240\n\t"
    "v_mfma_f32_16x16x32_bf16 v[48:51], a[48:51], v[220:223], 0\n\t"
    "ds_read_b128 a[92:95], v9 offset:11264\n\t"
    "v_mfma_f32_16x16x32_bf16 v[48:51], a[56:59], v[216:219], v[48:51]\n\t"
    "s_barrier\n\t"
    "ds_read_b128 a[96:99], v8 offset:12288\n\t"
    "v_mfma_f32_16x16x32_bf16 v[48:51], a[64:67], v[212:215], v[48:51]\n\t"
    "ds_read_b128 a[104:107], v8 offset:13312\n\t"
    "v_mfma_f32_16x16x32_bf16 v[48:51], a[72:75], v[28:31], v[48:51]\n\t"
    "v_cvt_pk_bf16_f32 v46, v40, v41\n\t"
    "ds_read_b128 a[112:115], v8 offset:14336\n\t"
    "v_mfma_f32_16x16x32_bf16 v[48:51], a[80:83], v[32:35], v[48:51]\n\t"
    "v_cvt_pk_bf16_f32 v47, v42, v43\n\t"
    "ds_read_b128 a[120:123], v8 offset:15360\n\t"
    "v_mfma_f32_16x16x32_bf16 v[48:51], a[88:91], v[36:39], v[48:51]\n\t"
    "ds_read_b128 a[128:131], v8 offset:16384\n\t"
    "s_waitcnt lgkmcnt(4)\n\t"
    "v_mfma_f32_16x16x32_bf16 v[56:59], a[52:55], v[220:223], 0\n\t"
    "ds_read_b128 a[136:139], v8 offset:17408\n\t"
    "v_mfma_f32_16x16x32_bf16 v[56:59], a[60:63], v[216:219], v[56:59]\n\t"
    "ds_read_b128 a[100:103], v9 offset:12288\n\t"
    "v_mfma_f32_16x16x32_bf16 v[56:59], a[68:71], v[212:215], v[56:59]\n\t"
    "ds_read_b128 a[108:111], v9 offset:13312\n\t"
    "v_mfma_f32_16x16x32_bf16 v[56:59], a[76:79], v[28:31], v[56:59]\n\t"
    "v_cvt_pk_bf16_f32 v40, v48, v49\n\t"
    "ds_read_b128 a[116:119], v9 offset:14336\n\t"
    "v_mfma_f32_16x16x32_bf16 v[56:59], a[84:87], v[32:35], v[56:59]\n\t"
    "v_cvt_pk_bf16_f32 v41, v50, v51\n\t"
    "ds_read_b128 a[124:127], v9 offset:15360\n\t"
    "v_mfma_f32_16x16x32_bf16 v[56:59], a[92:95], v[36:39], v[56:59]\n\t"
    "ds_read_b128 a[132:135], v9 offset:16384\n\t"
    "v_mfma_f32_16x16x32_bf16 v[60:63], a[96:99], v[220:223], 0\n\t"
    "ds_read_b128 a[140:143], v9 offset:17408\n\t"
    "s_waitcnt lgkmcnt(4)\n\t"
    "v_mfma_f32_16x16x32_bf16 v[60:63], a[104:107], v[216:219], v[60:63]\n\t"
    "ds_read_b128 a[144:147], v8 offset:18432\n\t"
    "v_mfma_f32_16x16x32_bf16 v[60:63], a[112:115], v[212:215], v[60:63]\n\t"
    "ds_read_b128 a[152:155], v8 offset:19456\n\t"
    "v_mfma_f32_16x16x32_bf16 v[60:63], a[120:123], v[28:31], v[60:63]\n\t"
    "v_cvt_pk_bf16_f32 v42, v56, v57\n\t"
    "v_cvt_pk_bf16_f32 v43, v58, v59\n\t"
    "ds_read_b128 a[160:163], v8 offset:20480\n\t"
    "v_mfma_f32_16x16x32_bf16 v[60:63], a[128:131], v[32:35], v[60:63]\n\t"
    "ds_read_b128 a[168:171], v8 offset:21504\n\t"
    "v_mfma_f32_16x16x32_bf16 v[60:63], a[136:139], v[36:39], v[60:63]\n\t"
    "ds_read_b128 a[176:179], v8 offset:22528\n\t"
    "v_mfma_f32_16x16x32_bf16 v[48:51], a[100:103], v[220:223], 0\n\t"
    "ds_read_b128 a[184:187], v8 offset:23552\n\t"
    "v_mfma_f32_16x16x32_bf16 v[48:51], a[108:111], v[216:219], v[48:51]\n\t"
    "s_waitcnt lgkmcnt(3)\n\t"
    "v_mfma_f32_16x16x32_bf16 v[48:51], a[116:119], v[212:215], v[48:51]\n\t"
    "v_mfma_f32_16x16x32_bf16 v[48:51], a[124:127], v[28:31], v[48:51]\n\t"
    "v_cvt_pk_bf16_f32 v56, v60, v61\n\t"
    "v_cvt_pk_bf16_f32 v57, v62, v63\n\t"
    "v_mfma_f32_16x16x32_bf16 v[48:51], a[132:135], v[32:35], v[48:51]\n\t"
    "v_mfma_f32_16x16x32_bf16 v[48:51], a[140:143], v[36:39], v[48:51]\n\t"
    "v_mfma_f32_16x16x32_bf16 v[60:63], a[144:147], v[220:223], 0\n\t"
    "v_mfma_f32_16x16x32_bf16 v[60:63], a[152:155], v[216:219], v[60:63]\n\t"
    "v_mfma_f32_16x16x32_bf16 v[60:63], a[160:163], v[212:215], v[60:63]\n\t"
    "s_waitcnt lgkmcnt(0)\n\t"
    "v_mfma_f32_16x16x32_bf16 v[60:63], a[168:171], v[28:31], v[60:63]\n\t"
    "v_cvt_pk_bf16_f32 v58, v48, v49\n\t"
    "v_mfma_f32_16x16x32_bf16 v[60:63], a[176:179], v[32:35], v[60:63]\n\t"
    "v_cvt_pk_bf16_f32 v59, v50, v51\n\t"
    "v_mfma_f32_16x16x32_bf16 v[60:63], a[184:187], v[36:39], v[60:63]\n\t"
    "v_mfma_f32_16x16x32_bf16 v[64:67], v[220:223], v[44:47], 0\n\t"
    "v_mfma_f32_16x16x32_bf16 v[64:67], v[216:219], v[40:43], v[64:67]\n\t"
    "ds_read_b128 a[148:151], v9 offset:18432\n\t"
    "v_mfma_f32_16x16x32_bf16 v[64:67], v[212:215], v[56:59], v[64:67]\n\t"
    "ds_read_b128 a[156:159], v9 offset:19456\n\t"
    "v_mfma_f32_16x16x32_bf16 v[48:51], v[196:199], v[44:47], 0\n\t"
    "ds_read_b128 a[164:167], v9 offset:20480\n\t"
    "v_mfma_f32_16x16x32_bf16 v[48:51], v[192:195], v[40:43], v[48:51]\n\t"
    "ds_read_b128 a[172:175], v9 offset:21504\n\t"
    "v_mfma_f32_16x16x32_bf16 v[48:51], v[188:191], v[56:59], v[48:51]\n\t"
    "ds_read_b128 a[180:183], v9 offset:22528\n\t"
    "v_mfma_f32_16x16x32_bf16 v[68:71], v[172:175], v[44:47], 0\n\t"
    "ds_read_b128 a[188:191], v9 offset:23552\n\t"
    "v_mfma_f32_16x16x32_bf16 v[68:71], v[168:171], v[40:43], v[68:71]\n\t"
    "s_barrier\n\t"
    "ds_read_b128 a[192:195], v8 offset:24576\n\t"
    "v_mfma_f32_16x16x32_bf16 v[68:71], v[164:167], v[56:59], v[68:71]\n\t"
    "ds_read_b128 a[200:203], v8 offset:25600\n\t"
    "v_mfma_f32_16x16x32_bf16 v[72:75], v[148:151], v[44:47], 0\n\t"
    "ds_read_b128 a[208:211], v8 offset:26624\n\t"
    "v_mfma_f32_16x16x32_bf16 v[72:75], v[144:147], v[40:43], v[72:75]\n\t"
    "ds_read_b128 a[216:219], v8 offset:27648\n\t"
    "v_mfma_f32_16x16x32_bf16 v[72:75], v[140:143], v[56:59], v[72:75]\n\t"
    "ds_read_b128 a[224:227], v8 offset:28672\n\t"
    "s_waitcnt lgkmcnt(4)\n\t"
    "v_mfma_f32_16x16x32_bf16 v[44:47], a[148:151], v[220:223], 0\n\t"
    "ds_read_b128 a[232:235], v8 offset:29696\n\t"
    "v_mfma_f32_16x16x32_bf16 v[44:47], a[156:159], v[216:219], v[44:47]\n\t"
    "ds_read_b128 a[196:199], v9 offset:24576\n\t"
    "v_mfma_f32_16x16x32_bf16 v[44:47], a[164:167], v[212:215], v[44:47]\n\t"
    "ds_read_b128 a[204:207], v9 offset:25600\n\t"
    "v_mfma_f32_16x16x32_bf16 v[44:47], a[172:175], v[28:31], v[44:47]\n\t"
    "v_cvt_pk_bf16_f32 v40, v60, v61\n\t"
    "ds_read_b128 a[212:215], v9 offset:26624\n\t"
    "v_mfma_f32_16x16x32_bf16 v[44:47], a[180:183], v[32:35], v[44:47]\n\t"
    "ds_read_b128 a[220:223], v9 offset:27648\n\t"
    "v_mfma_f32_16x16x32_bf16 v[44:47], a[188:191], v[36:39], v[44:47]\n\t"
    "ds_read_b128 a[228:231], v9 offset:28672\n\t"
    "v_mfma_f32_16x16x32_bf16 v[56:59], a[192:195], v[220:223], 0\n\t"
    "ds_read_b128 a[236:239], v9 offset:29696\n\t"
    "s_waitcnt lgkmcnt(4)\n\t"
    "v_mfma_f32_16x16x32_bf16 v[56:59], a[200:203], v[216:219], v[56:59]\n\t"
    "v_cvt_pk_bf16_f32 v41, v62, v63\n\t"
    "ds_read_b128 a[240:243], v8 offset:30720\n\t"
    "v_mfma_f32_16x16x32_bf16 v[56:59], a[208:211], v[212:215], v[56:59]\n\t"
    "ds_read_b128 a[248:251], v8 offset:31744\n\t"
    "v_mfma_f32_16x16x32_bf16 v[56:59], a[216:219], v[28:31], v[56:59]\n\t"
    "v_cvt_pk_bf16_f32 v42, v44, v45\n\t"
    "ds_read_b128 v[224:227], v8 offset:32768\n\t"
    "v_mfma_f32_16x16x32_bf16 v[56:59], a[224:227], v[32:35], v[56:59]\n\t"
    "v_cvt_pk_bf16_f32 v43, v46, v47\n\t"
    "ds_read_b128 v[232:235], v8 offset:33792\n\t"
    "v_mfma_f32_16x16x32_bf16 v[56:59], a[232:235], v[36:39], v[56:59]\n\t"
    "ds_read_b128 v[240:243], v8 offset:34816\n\t"
    "v_mfma_f32_16x16x32_bf16 v[60:63], a[196:199], v[220:223], 0\n\t"
    "ds_read_b128 v[248:251], v8 offset:35840\n\t"
    "v_mfma_f32_16x16x32_bf16 v[60:63], a[204:207], v[216:219], v[60:63]\n\t"
    "ds_read_b128 a[244:247], v9 offset:30720\n\t"
    "s_waitcnt lgkmcnt(4)\n\t"
    "v_mfma_f32_16x16x32_bf16 v[60:63], a[212:215], v[212:215], v[60:63]\n\t"
    "ds_read_b128 a[252:255], v9 offset:31744\n\t"
    "v_mfma_f32_16x16x32_bf16 v[60:63], a[220:223], v[28:31], v[60:63]\n\t"
    "v_cvt_pk_bf16_f32 v44, v56, v57\n\t"
    "ds_read_b128 v[228:231], v9 offset:32768\n\t"
    "v_mfma_f32_16x16x32_bf16 v[60:63], a[228:231], v[32:35], v[60:63]\n\t"
    "v_cvt_pk_bf16_f32 v45, v58, v59\n\t"
    "ds_read_b128 v[236:239], v9 offset:33792\n\t"
    "v_mfma_f32_16x16x32_bf16 v[60:63], a[236:239], v[36:39], v[60:63]\n\t"
    "ds_read_b128 v[244:247], v9 offset:34816\n\t"
    "v_mfma_f32_16x16x32_bf16 v[80:83], a[240:243], v[220:223], 0\n\t"
    "ds_read_b128 v[252:255], v9 offset:35840\n\t"
    "v_mfma_f32_16x16x32_bf16 v[80:83], a[248:251], v[216:219], v[80:83]\n\t"
    "v_mfma_f32_16x16x32_bf16 v[80:83], v[224:227], v[212:215], v[80:83]\n\t"
    "s_waitcnt lgkmcnt(2)\n\t"
    "v_mfma_f32_16x16x32_bf16 v[80:83], v[232:235], v[28:31], v[80:83]\n\t"
    "v_cvt_pk_bf16_f32 v46, v60, v61\n\t"
    "v_cvt_pk_bf16_f32 v47, v62, v63\n\t"
    "v_mfma_f32_16x16x32_bf16 v[80:83], v[240:243], v[32:35], v[80:83]\n\t"
    "v_mfma_f32_16x16x32_bf16 v[80:83], v[248:251], v[36:39], v[80:83]\n\t"
    "v_mfma_f32_16x16x32_bf16 v[12:15], a[244:247], v[220:223], 0\n\t"
    "v_mfma_f32_16x16x32_bf16 v[12:15], a[252:255], v[216:219], v[12:15]\n\t"
    "v_mfma_f32_16x16x32_bf16 v[12:15], v[228:231], v[212:215], v[12:15]\n\t"
    "v_mfma_f32_16x16x32_bf16 v[12:15], v[236:239], v[28:31], v[12:15]\n\t"
    "v_cvt_pk_bf16_f32 v16, v80, v81\n\t"
    "v_cvt_pk_bf16_f32 v17, v82, v83\n\t"
    "s_waitcnt lgkmcnt(0)\n\t"
    "v_mfma_f32_16x16x32_bf16 v[12:15], v[244:247], v[32:35], v[12:15]\n\t"
    "v_mfma_f32_16x16x32_bf16 v[12:15], v[252:255], v[36:39], v[12:15]\n\t"
    "v_mfma_f32_16x16x32_bf16 v[20:23], a[0:3], v[196:199], 0\n\t"
    "v_mfma_f32_16x16x32_bf16 v[20:23], a[8:11], v[192:195], v[20:23]\n\t"
    "v_mfma_f32_16x16x32_bf16 v[20:23], a[16:19], v[188:191], v[20:23]\n\t"
    "v_mfma_f32_16x16x32_bf16 v[20:23], a[24:27], v[184:187], v[20:23]\n\t"
    "v_cvt_pk_bf16_f32 v18, v12, v13\n\t"
    "v_mfma_f32_16x16x32_bf16 v[20:23], a[32:35], v[180:183], v[20:23]\n\t"
    "v_mfma_f32_16x16x32_bf16 v[20:23], a[40:43], v[176:179], v[20:23]\n\t"
    "v_cvt_pk_bf16_f32 v19, v14, v15\n\t"
    "v_mfma_f32_16x16x32_bf16 v[64:67], v[208:211], v[40:43], v[64:67]\n\t"
    "v_mfma_f32_16x16x32_bf16 v[64:67], v[204:207], v[44:47], v[64:67]\n\t"
    "v_mfma_f32_16x16x32_bf16 v[64:67], v[200:203], v[16:19], v[64:67]\n\t"
    "v_mfma_f32_16x16x32_bf16 v[48:51], v[184:187], v[40:43], v[48:51]\n\t"
    "v_cvt_pk_bf16_f32 v12, v20, v21\n\t"
    "v_cvt_pk_bf16_f32 v13, v22, v23\n\t"
    "v_mfma_f32_16x16x32_bf16 v[48:51], v[180:183], v[44:47], v[48:51]\n\t"
    "v_mfma_f32_16x16x32_bf16 v[48:51], v[176:179], v[16:19], v[48:51]\n\t"
    "v_mfma_f32_16x16x32_bf16 v[68:71], v[160:163], v[40:43], v[68:71]\n\t"
    "v_max3_f32 v20, v64, v65, v66\n\t"
    "v_mfma_f32_16x16x32_bf16 v[68:71], v[156:159], v[44:47], v[68:71]\n\t"
    "v_mfma_f32_16x16x32_bf16 v[68:71], v[152:155], v[16:19], v[68:71]\n\t"
    "v_mfma_f32_16x16x32_bf16 v[72:75], v[136:139], v[40:43], v[72:75]\n\t"
    "v_max3_f32 v21, v67, v48, v49\n\t"
    "v_max3_f32 v20, v20, v50, v51\n\t"
    "v_mfma_f32_16x16x32_bf16 v[72:75], v[132:135], v[44:47], v[72:75]\n\t"
    "v_mfma_f32_16x16x32_bf16 v[72:75], v[128:131], v[16:19], v[72:75]\n\t"
    "v_mfma_f32_16x16x32_bf16 v[24:27], a[4:7], v[196:199], 0\n\t"
    "v_max3_f32 v21, v21, v68, v69\n\t"
    "v_max3_f32 v20, v20, v70, v71\n\t"
    "v_mfma_f32_16x16x32_bf16 v[24:27], a[12:15], v[192:195], v[24:27]\n\t"
    "v_mfma_f32_16x16x32_bf16 v[24:27], a[20:23], v[188:191], v[24:27]\n\t"
    "v_mfma_f32_16x16x32_bf16 v[24:27], a[28:31], v[184:187], v[24:27]\n\t"
    "v_max3_f32 v21, v21, v72, v73\n\t"
    "v_max3_f32 v20, v20, v74, v75\n\t"
    "v_mfma_f32_16x16x32_bf16 v[24:27], a[36:39], v[180:183], v[24:27]\n\t"
    "v_max_f32_e32 v20, v20, v21\n\t"
    "v_mov_b32_e32 v21, v20\n\t"
    "v_mfma_f32_16x16x32_bf16 v[24:27], a[44:47], v[176:179], v[24:27]\n\t"
    "v_mfma_f32_16x16x32_bf16 v[16:19], a[48:51], v[196:199], 0\n\t"
    "v_permlane32_swap_b32 v20, v21\n\t"
    "v_max_f32_e32 v20, v20, v21\n\t"
    "v_mfma_f32_16x16x32_bf16 v[16:19], a[56:59], v[192:195], v[16:19]\n\t"
    "v_mov_b32_e32 v21, v20\n\t"
    "v_mfma_f32_16x16x32_bf16 v[16:19], a[64:67], v[188:191], v[16:19]\n\t"
    "v_mfma_f32_16x16x32_bf16 v[16:19], a[72:75], v[184:187], v[16:19]\n\t"
    "v_cvt_pk_bf16_f32 v14, v24, v25\n\t"
    "v_cvt_pk_bf16_f32 v15, v26, v27\n\t"
    "v_mfma_f32_16x16x32_bf16 v[16:19], a[80:83], v[180:183], v[16:19]\n\t"
    "v_permlane16_swap_b32 v20, v21\n\t"
    "v_max_f32_e32 v20, v20, v21\n\t"
    "v_mfma_f32_16x16x32_bf16 v[16:19], a[88:91], v[176:179], v[16:19]\n\t"
    "v_mul_f32_e32 v20, s68, v20\n\t"
    "v_fma_f32 v64, v64, s68, -v20\n\t"
    "v_mfma_f32_16x16x32_bf16 v[24:27], a[52:55], v[196:199], 0\n\t"
    "v_exp_f32_e32 v64, v64\n\t"
    "v_mfma_f32_16x16x32_bf16 v[24:27], a[60:63], v[192:195], v[24:27]\n\t"
    "v_fma_f32 v65, v65, s68, -v20\n\t"
    "v_fma_f32 v66, v66, s68, -v20\n\t"
    "v_mfma_f32_16x16x32_bf16 v[24:27], a[68:71], v[188:191], v[24:27]\n\t"
    "v_exp_f32_e32 v65, v65\n\t"
    "v_mfma_f32_16x16x32_bf16 v[24:27], a[76:79], v[184:187], v[24:27]\n\t"
    "v_cvt_pk_bf16_f32 v28, v16, v17\n\t"
    "v_cvt_pk_bf16_f32 v29, v18, v19\n\t"
    "v_mfma_f32_16x16x32_bf16 v[24:27], a[84:87], v[180:183], v[24:27]\n\t"
    "v_add_f32_e32 v22, v64, v65\n\t"
    "v_fma_f32 v67, v67, s68, -v20\n\t"
    "v_mfma_f32_16x16x32_bf16 v[24:27], a[92:95], v[176:179], v[24:27]\n\t"
    "v_fma_f32 v48, v48, s68, -v20\n\t"
    "v_mfma_f32_16x16x32_bf16 v[16:19], a[96:99], v[196:199], 0\n\t"
    "v_fma_f32 v49, v49, s68, -v20\n\t"
    "v_mfma_f32_16x16x32_bf16 v[16:19], a[104:107], v[192:195], v[16:19]\n\t"
    "v_exp_f32_e32 v66, v66\n\t"
    "v_mfma_f32_16x16x32_bf16 v[16:19], a[112:115], v[188:191], v[16:19]\n\t"
    "v_exp_f32_e32 v67, v67\n\t"
    "v_mfma_f32_16x16x32_bf16 v[16:19], a[120:123], v[184:187], v[16:19]\n\t"
    "v_cvt_pk_bf16_f32 v30, v24, v25\n\t"
    "v_cvt_pk_bf16_f32 v31, v26, v27\n\t"
    "v_mfma_f32_16x16x32_bf16 v[16:19], a[128:131], v[180:183], v[16:19]\n\t"
    "v_add_f32_e32 v23, v66, v67\n\t"
    "v_fma_f32 v50, v50, s68, -v20\n\t"
    "v_mfma_f32_16x16x32_bf16 v[16:19], a[136:139], v[176:179], v[16:19]\n\t"
    "v_exp_f32_e32 v48, v48\n\t"
    "v_mfma_f32_16x16x32_bf16 v[24:27], a[100:103], v[196:199], 0\n\t"
    "v_add_f32_e32 v22, v22, v48\n\t"
    "v_fma_f32 v51, v51, s68, -v20\n\t"
    "v_mfma_f32_16x16x32_bf16 v[24:27], a[108:111], v[192:195], v[24:27]\n\t"
    "v_exp_f32_e32 v49, v49\n\t"
    "v_mfma_f32_16x16x32_bf16 v[24:27], a[116:119], v[188:191], v[24:27]\n\t"
    "v_add_f32_e32 v22, v22, v49\n\t"
    "v_fma_f32 v68, v68, s68, -v20\n\t"
    "v_mfma_f32_16x16x32_bf16 v[24:27], a[124:127], v[184:187], v[24:27]\n\t"
    "v_cvt_pk_bf16_f32 v32, v16, v17\n\t"
    "v_cvt_pk_bf16_f32 v33, v18, v19\n\t"
    "v_mfma_f32_16x16x32_bf16 v[24:27], a[132:135], v[180:183], v[24:27]\n\t"
    "v_exp_f32_e32 v50, v50\n\t"
    "v_mfma_f32_16x16x32_bf16 v[24:27], a[140:143], v[176:179], v[24:27]\n\t"
    "v_add_f32_e32 v23, v23, v50\n\t"
    "v_fma_f32 v69, v69, s68, -v20\n\t"
    "v_mfma_f32_16x16x32_bf16 v[16:19], a[144:147], v[196:199], 0\n\t"
    "v_exp_f32_e32 v51, v51\n\t"
    "v_mfma_f32_16x16x32_bf16 v[16:19], a[152:155], v[192:195], v[16:19]\n\t"
    "v_add_f32_e32 v23, v23, v51\n\t"
    "v_fma_f32 v70, v70, s68, -v20\n\t"
    "v_mfma_f32_16x16x32_bf16 v[16:19], a[160:163], v[188:191], v[16:19]\n\t"
    "v_fma_f32 v71, v71, s68, -v20\n\t"
    "v_mfma_f32_16x16x32_bf16 v[16:19], a[168:171], v[184:187], v[16:19]\n\t"
    "v_cvt_pk_bf16_f32 v34, v24, v25\n\t"
    "v_mfma_f32_16x16x32_bf16 v[16:19], a[176:179], v[180:183], v[16:19]\n\t"
    "v_cvt_pk_bf16_f32 v35, v26, v27\n\t"
    "v_fma_f32 v72, v72, s68, -v20\n\t"
    "s_barrier\n\t"
    "v_mfma_f32_16x16x32_bf16 v[16:19], a[184:187], v[176:179], v[16:19]\n\t"
    "v_exp_f32_e32 v68, v68\n\t"
    "v_mfma_f32_16x16x32_bf16 v[36:39], v[220:223], v[12:15], 0\n\t"
    "v_add_f32_e32 v22, v22, v68\n\t"
    "v_fma_f32 v73, v73, s68, -v20\n\t"
    "s_add_u32 m0, %[rbeg], 24576\n\t"
    "s_add_u32 s61, %[wb], 12582912\n\t"
    "v_mfma_f32_16x16x32_bf16 v[36:39], v[216:219], v[28:31], v[36:39]\n\t"
    "v_exp_f32_e32 v69, v69\n\t"
    "v_mfma_f32_16x16x32_bf16 v[36:39], v[212:215], v[32:35], v[36:39]\n\t"
    "v_add_f32_e32 v22, v22, v69\n\t"
    "v_fma_f32 v74, v74, s68, -v20\n\t"
    "v_mfma_f32_16x16x32_bf16 v[24:27], v[196:199], v[12:15], 0\n\t"
    "v_exp_f32_e32 v70, v70\n\t"
    "v_mfma_f32_16x16x32_bf16 v[24:27], v[192:195], v[28:31], v[24:27]\n\t"
    "v_add_f32_e32 v23, v23, v70\n\t"
    "v_fma_f32 v75, v75, s68, -v20\n\t"
    "v_mfma_f32_16x16x32_bf16 v[24:27], v[188:191], v[32:35], v[24:27]\n\t"
    "v_exp_f32_e32 v71, v71\n\t"
    "buffer_load_dwordx4 v7, s[52:55], s61 offen offset:0 nt lds\n\t"
    "v_mfma_f32_16x16x32_bf16 v[40:43], v[172:175], v[12:15], 0\n\t"
    "v_add_f32_e32 v23, v23, v71\n\t"
    "v_cvt_pk_bf16_f32 v44, v16, v17\n\t"
    "v_mfma_f32_16x16x32_bf16 v[40:43], v[168:171], v[28:31], v[40:43]\n\t"
    "v_exp_f32_e32 v72, v72\n\t"
    "v_mfma_f32_16x16x32_bf16 v[40:43], v[164:167], v[32:35], v[40:43]\n\t"
    "v_add_f32_e32 v22, v22, v72\n\t"
    "v_cvt_pk_bf16_f32 v45, v18, v19\n\t"
    "v_mfma_f32_16x16x32_bf16 v[52:55], v[148:151], v[12:15], 0\n\t"
    "v_exp_f32_e32 v73, v73\n\t"
    "v_mfma_f32_16x16x32_bf16 v[52:55], v[144:147], v[28:31], v[52:55]\n\t"
    "v_add_f32_e32 v22, v22, v73\n\t"
    "buffer_load_dwordx4 v7, s[52:55], s61 offen offset:1024 nt lds\n\t"
    "v_mfma_f32_16x16x32_bf16 v[52:55], v[140:143], v[32:35], v[52:55]\n\t"
    "v_exp_f32_e32 v74, v74\n\t"
    "v_mfma_f32_16x16x32_bf16 v[12:15], a[148:151], v[196:199], 0\n\t"
    "v_add_f32_e32 v23, v23, v74\n\t"
    "v_mfma_f32_16x16x32_bf16 v[12:15], a[156:159], v[192:195], v[12:15]\n\t"
    "v_mfma_f32_16x16x32_bf16 v[12:15], a[164:167], v[188:191], v[12:15]\n\t"
    "v_exp_f32_e32 v75, v75\n\t"
    "v_mfma_f32_16x16x32_bf16 v[12:15], a[172:175], v[184:187], v[12:15]\n\t"
    "v_add_f32_e32 v23, v23, v75\n\t"
    "v_add_f32_e32 v22, v22, v23\n\t"
    "v_mfma_f32_16x16x32_bf16 v[12:15], a[180:183], v[180:183], v[12:15]\n\t"
    "v_mov_b32_e32 v23, v22\n\t"
    "buffer_load_dwordx4 v7, s[52:55], s61 offen offset:2048 nt lds\n\t"
    "v_mfma_f32_16x16x32_bf16 v[12:15], a[188:191], v[176:179], v[12:15]\n\t"
    "v_mfma_f32_16x16x32_bf16 v[16:19], a[192:195], v[196:199], 0\n\t"
    "v_permlane32_swap_b32 v22, v23\n\t"
    "v_add_f32_e32 v22, v22, v23\n\t"
    "v_mfma_f32_16x16x32_bf16 v[16:19], a[200:203], v[192:195], v[16:19]\n\t"
    "v_mov_b32_e32 v23, v22\n\t"
    "v_mfma_f32_16x16x32_bf16 v[16:19], a[208:211], v[188:191], v[16:19]\n\t"
    "s_add_u32 m0, %[rbeg], 27648\n\t"
    "s_add_u32 s61, %[wb], 12779520\n\t"
    "v_mfma_f32_16x16x32_bf16 v[16:19], a[216:219], v[184:187], v[16:19]\n\t"
    "v_cvt_pk_bf16_f32 v46, v12, v13\n\t"
    "v_cvt_pk_bf16_f32 v47, v14, v15\n\t"
    "v_mfma_f32_16x16x32_bf16 v[16:19], a[224:227], v[180:183], v[16:19]\n\t"
    "v_permlane16_swap_b32 v22, v23\n\t"
    "v_add_f32_e32 v22, v22, v23\n\t"
    "v_mfma_f32_16x16x32_bf16 v[16:19], a[232:235], v[176:179], v[16:19]\n\t"
    "v_rcp_f32_e32 v22, v22\n\t"
    "v_mfma_f32_16x16x32_bf16 v[12:15], a[196:199], v[196:199], 0\n\t"
    "v_mul_f32_e32 v64, v64, v22\n\t"
    "v_mul_f32_e32 v65, v65, v22\n\t"
    "v_mfma_f32_16x16x32_bf16 v[12:15], a[204:207], v[192:195], v[12:15]\n\t"
    "v_cvt_pk_bf16_f32 v28, v64, v65\n\t"
    "v_mul_f32_e32 v66, v66, v22\n\t"
    "buffer_load_dwordx4 v7, s[52:55], s61 offen offset:0 nt lds\n\t"
    "v_mfma_f32_16x16x32_bf16 v[12:15], a[212:215], v[188:191], v[12:15]\n\t"
    "v_mul_f32_e32 v67, v67, v22\n\t"
    "v_cvt_pk_bf16_f32 v29, v66, v67\n\t"
    "v_mfma_f32_16x16x32_bf16 v[12:15], a[220:223], v[184:187], v[12:15]\n\t"
    "v_cvt_pk_bf16_f32 v32, v16, v17\n\t"
    "v_cvt_pk_bf16_f32 v33, v18, v19\n\t"
    "v_mfma_f32_16x16x32_bf16 v[12:15], a[228:231], v[180:183], v[12:15]\n\t"
    "v_mul_f32_e32 v48, v48, v22\n\t"
    "v_mul_f32_e32 v49, v49, v22\n\t"
    "v_mfma_f32_16x16x32_bf16 v[12:15], a[236:239], v[176:179], v[12:15]\n\t"
    "v_cvt_pk_bf16_f32 v30, v48, v49\n\t"
    "v_mfma_f32_16x16x32_bf16 v[16:19], a[240:243], v[196:199], 0\n\t"
    "v_mul_f32_e32 v50, v50, v22\n\t"
    "buffer_load_dwordx4 v7, s[52:55], s61 offen offset:1024 nt lds\n\t"
    "v_mfma_f32_16x16x32_bf16 v[16:19], a[248:251], v[192:195], v[16:19]\n\t"
    "v_mul_f32_e32 v51, v51, v22\n\t"
    "v_cvt_pk_bf16_f32 v31, v50, v51\n\t"
    "v_mfma_f32_16x16x32_bf16 v[16:19], v[224:227], v[188:191], v[16:19]\n\t"
    "v_mul_f32_e32 v68, v68, v22\n\t"
    "v_mul_f32_e32 v69, v69, v22\n\t"
    "v_mfma_f32_16x16x32_bf16 v[16:19], v[232:235], v[184:187], v[16:19]\n\t"
    "v_cvt_pk_bf16_f32 v34, v12, v13\n\t"
    "v_cvt_pk_bf16_f32 v35, v14, v15\n\t"
    "v_mfma_f32_16x16x32_bf16 v[16:19], v[240:243], v[180:183], v[16:19]\n\t"
    "v_cvt_pk_bf16_f32 v48, v68, v69\n\t"
    "v_mul_f32_e32 v70, v70, v22\n\t"
    "v_mfma_f32_16x16x32_bf16 v[16:19], v[248:251], v[176:179], v[16:19]\n\t"
    "v_mul_f32_e32 v71, v71, v22\n\t"
    "v_cvt_pk_bf16_f32 v49, v70, v71\n\t"
    "buffer_load_dwordx4 v7, s[52:55], s61 offen offset:2048 nt lds\n\t"
    "v_mfma_f32_16x16x32_bf16 v[12:15], a[244:247], v[196:199], 0\n\t"
    "v_mul_f32_e32 v72, v72, v22\n\t"
    "v_mul_f32_e32 v73, v73, v22\n\t"
    "v_mfma_f32_16x16x32_bf16 v[12:15], a[252:255], v[192:195], v[12:15]\n\t"
    "v_cvt_pk_bf16_f32 v50, v72, v73\n\t"
    "v_mul_f32_e32 v74, v74, v22\n\t"
    "v_mfma_f32_16x16x32_bf16 v[12:15], v[228:231], v[188:191], v[12:15]\n\t"
    "v_mul_f32_e32 v75, v75, v22\n\t"
    "v_cvt_pk_bf16_f32 v51, v74, v75\n\t"
    "v_mfma_f32_16x16x32_bf16 v[12:15], v[236:239], v[184:187], v[12:15]\n\t"
    "v_cvt_pk_bf16_f32 v56, v16, v17\n\t"
    "v_cvt_pk_bf16_f32 v57, v18, v19\n\t"
    "v_mfma_f32_16x16x32_bf16 v[12:15], v[244:247], v[180:183], v[12:15]\n\t"
    "v_mfma_f32_16x16x32_bf16 v[12:15], v[252:255], v[176:179], v[12:15]\n\t"
    "s_add_u32 m0, %[rbeg], 30720\n\t"
    "s_add_u32 s61, %[wb], 12976128\n\t"
    "v_mfma_f32_16x16x32_bf16 v[16:19], a[0:3], v[172:175], 0\n\t"
    "ds_write_b128 v6, v[28:31] offset:0\n\t"
    "v_mfma_f32_16x16x32_bf16 v[16:19], a[8:11], v[168:171], v[16:19]\n\t"
    "v_mfma_f32_16x16x32_bf16 v[16:19], a[16:19], v[164:167], v[16:19]\n\t"
    "v_mfma_f32_16x16x32_bf16 v[16:19], a[24:27], v[160:163], v[16:19]\n\t"
    "v_cvt_pk_bf16_f32 v58, v12, v13\n\t"
    "v_mfma_f32_16x16x32_bf16 v[16:19], a[32:35], v[156:159], v[16:19]\n\t"
    "ds_write_b128 v6, v[48:51] offset:1024\n\t"
    "v_mfma_f32_16x16x32_bf16 v[16:19], a[40:43], v[152:155], v[16:19]\n\t"
    "v_cvt_pk_bf16_f32 v59, v14, v15\n\t"
    "buffer_load_dwordx4 v7, s[52:55], s61 offen offset:0 nt lds\n\t"
    "v_mfma_f32_16x16x32_bf16 v[36:39], v[208:211], v[44:47], v[36:39]\n\t"
    "v_mfma_f32_16x16x32_bf16 v[36:39], v[204:207], v[32:35], v[36:39]\n\t"
    "v_mfma_f32_16x16x32_bf16 v[36:39], v[200:203], v[56:59], v[36:39]\n\t"
    "s_add_i32 s64, s62, 24576\n\t"
    "v_mfma_f32_16x16x32_bf16 v[24:27], v[184:187], v[44:47], v[24:27]\n\t"
    "s_cmp_eq_u32 s64, %[rend]\n\t"
    "s_cselect_b32 s64, %[rbeg], s64\n\t"
    "buffer_load_dwordx4 v7, s[52:55], s61 offen offset:1024 nt lds\n\t"
    "v_mfma_f32_16x16x32_bf16 v[24:27], v[180:183], v[32:35], v[24:27]\n\t"
    "s_sub_i32 s65, s63, s64\n\t"
    "v_cvt_pk_bf16_f32 v12, v16, v17\n\t"
    "v_mfma_f32_16x16x32_bf16 v[24:27], v[176:179], v[56:59], v[24:27]\n\t"
    "v_add_u32_e32 v0, s65, v0\n\t"
    "v_add_u32_e32 v1, s65, v1\n\t"
    "v_mfma_f32_16x16x32_bf16 v[40:43], v[160:163], v[44:47], v[40:43]\n\t"
    "v_add_u32_e32 v2, s65, v2\n\t"
    "v_add_u32_e32 v3, s65, v3\n\t"
    "v_mfma_f32_16x16x32_bf16 v[40:43], v[156:159], v[32:35], v[40:43]\n\t"
    ".Lh4_%=:\n\t"
    "s_cbranch_execz .Lh5_%=\n\t"
    "v_add_u32_e32 v4, s65, v4\n\t"
    "v_add_u32_e32 v5, s65, v5\n\t"
    "v_mfma_f32_16x16x32_bf16 v[40:43], v[152:155], v[56:59], v[40:43]\n\t"
    "v_max3_f32 v20, v36, v37, v38\n\t"
    "v_cvt_pk_bf16_f32 v13, v18, v19\n\t"
    "v_mfma_f32_16x16x32_bf16 v[52:55], v[136:139], v[44:47], v[52:55]\n\t"
    "v_max3_f32 v21, v39, v24, v25\n\t"
    "v_max3_f32 v20, v20, v26, v27\n\t"
    "v_mfma_f32_16x16x32_bf16 v[52:55], v[132:135], v[32:35], v[52:55]\n\t"
    "buffer_load_dwordx4 v7, s[52:55], s61 offen offset:2048 nt lds\n\t"
    "v_mfma_f32_16x16x32_bf16 v[52:55], v[128:131], v[56:59], v[52:55]\n\t"
    "v_mfma_f32_16x16x32_bf16 v[16:19], a[4:7], v[172:175], 0\n\t"
    "v_max3_f32 v21, v21, v40, v41\n\t"
    "v_max3_f32 v20, v20, v42, v43\n\t"
    "v_mfma_f32_16x16x32_bf16 v[16:19], a[12:15], v[168:171], v[16:19]\n\t"
    "s_add_u32 m0, %[rbeg], 33792\n\t"
    "s_add_u32 s61, %[wb], 13172736\n\t"
    "v_mfma_f32_16x16x32_bf16 v[16:19], a[20:23], v[164:167], v[16:19]\n\t"
    "v_mfma_f32_16x16x32_bf16 v[16:19], a[28:31], v[160:163], v[16:19]\n\t"
    "v_max3_f32 v21, v21, v52, v53\n\t"
    "v_max3_f32 v20, v20, v54, v55\n\t"
    "v_mfma_f32_16x16x32_bf16 v[16:19], a[36:39], v[156:159], v[16:19]\n\t"
    "v_max_f32_e32 v20, v20, v21\n\t"
    "v_mov_b32_e32 v21, v20\n\t"
    "v_mfma_f32_16x16x32_bf16 v[16:19], a[44:47], v[152:155], v[16:19]\n\t"
    "v_mfma_f32_16x16x32_bf16 v[28:31], a[48:51], v[172:175], 0\n\t"
    "v_permlane32_swap_b32 v20, v21\n\t"
    "v_max_f32_e32 v20, v20, v21\n\t"
    "v_mfma_f32_16x16x32_bf16 v[28:31], a[56:59], v[168:171], v[28:31]\n\t"
    "v_mov_b32_e32 v21, v20\n\t"
    "buffer_load_dwordx4 v7, s[52:55], s61 offen offset:0 nt lds\n\t"
    "v_mfma_f32_16x16x32_bf16 v[28:31], a[64:67], v[164:167], v[28:31]\n\t"
    "v_mfma_f32_16x16x32_bf16 v[28:31], a[72:75], v[160:163], v[28:31]\n\t"
    "v_cvt_pk_bf16_f32 v14, v16, v17\n\t"
    "v_cvt_pk_bf16_f32 v15, v18, v19\n\t"
    "v_mfma_f32_16x16x32_bf16 v[28:31], a[80:83], v[156:159], v[28:31]\n\t"
    "v_permlane16_swap_b32 v20, v21\n\t"
    "v_max_f32_e32 v20, v20, v21\n\t"
    "v_mfma_f32_16x16x32_bf16 v[28:31], a[88:91], v[152:155], v[28:31]\n\t"
    "v_mul_f32_e32 v20, s68, v20\n\t"
    "v_fma_f32 v36, v36, s68, -v20\n\t"
    "buffer_load_dwordx4 v7, s[52:55], s61 offen offset:1024 nt lds\n\t"
    "v_mfma_f32_16x16x32_bf16 v[16:19], a[52:55], v[172:175], 0\n\t"
    "v_exp_f32_e32 v36, v36\n\t"
    "v_mfma_f32_16x16x32_bf16 v[16:19], a[60:63], v[168:171], v[16:19]\n\t"
    "v_fma_f32 v37, v37, s68, -v20\n\t"
    "v_fma_f32 v38, v38, s68, -v20\n\t"
    "v_mfma_f32_16x16x32_bf16 v[16:19], a[68:71], v[164:167], v[16:19]\n\t"
    "v_exp_f32_e32 v37, v37\n\t"
    "v_mfma_f32_16x16x32_bf16 v[16:19], a[76:79], v[160:163], v[16:19]\n\t"
    "v_cvt_pk_bf16_f32 v32, v28, v29\n\t"
    "v_cvt_pk_bf16_f32 v33, v30, v31\n\t"
    "v_mfma_f32_16x16x32_bf16 v[16:19], a[84:87], v[156:159], v[16:19]\n\t"
    "v_add_f32_e32 v22, v36, v37\n\t"
    "v_fma_f32 v39, v39, s68, -v20\n\t"
    "v_mfma_f32_16x16x32_bf16 v[16:19], a[92:95], v[152:155], v[16:19]\n\t"
    "v_fma_f32 v24, v24, s68, -v20\n\t"
    "buffer_load_dwordx4 v7, s[52:55], s61 offen offset:2048 nt lds\n\t"
    "v_mfma_f32_16x16x32_bf16 v[28:31], a[96:99], v[172:175], 0\n\t"
    "v_fma_f32 v25, v25, s68, -v20\n\t"
    "v_mfma_f32_16x16x32_bf16 v[28:31], a[104:107], v[168:171], v[28:31]\n\t"
    "v_exp_f32_e32 v38, v38\n\t"
    "v_mfma_f32_16x16x32_bf16 v[28:31], a[112:115], v[164:167], v[28:31]\n\t"
    "v_exp_f32_e32 v39, v39\n\t"
    "v_mfma_f32_16x16x32_bf16 v[28:31], a[120:123], v[160:163], v[28:31]\n\t"
    "v_cvt_pk_bf16_f32 v34, v16, v17\n\t"
    "v_cvt_pk_bf16_f32 v35, v18, v19\n\t"
    "v_mfma_f32_16x16x32_bf16 v[28:31], a[128:131], v[156:159], v[28:31]\n\t"
    "v_add_f32_e32 v23, v38, v39\n\t"
    "v_fma_f32 v26, v26, s68, -v20\n\t"
    "s_add_u32 m0, %[rbeg], 36864\n\t"
    "s_add_u32 s61, %[wb], 13369344\n\t"
    "v_mfma_f32_16x16x32_bf16 v[28:31], a[136:139], v[152:155], v[28:31]\n\t"
    "v_exp_f32_e32 v24, v24\n\t"
    "v_mfma_f32_16x16x32_bf16 v[16:19], a[100:103], v[172:175], 0\n\t"
    "v_add_f32_e32 v22, v22, v24\n\t"
    "v_fma_f32 v27, v27, s68, -v20\n\t"
    "v_mfma_f32_16x16x32_bf16 v[16:19], a[108:111], v[168:171], v[16:19]\n\t"
    "v_exp_f32_e32 v25, v25\n\t"
    "v_mfma_f32_16x16x32_bf16 v[16:19], a[116:119], v[164:167], v[16:19]\n\t"
    "v_add_f32_e32 v22, v22, v25\n\t"
    "v_fma_f32 v40, v40, s68, -v20\n\t"
    "v_mfma_f32_16x16x32_bf16 v[16:19], a[124:127], v[160:163], v[16:19]\n\t"
    "v_cvt_pk_bf16_f32 v44, v28, v29\n\t"
    "v_cvt_pk_bf16_f32 v45, v30, v31\n\t"
    "buffer_load_dwordx4 v7, s[52:55], s61 offen offset:0 nt lds\n\t"
    "v_mfma_f32_16x16x32_bf16 v[16:19], a[132:135], v[156:159], v[16:19]\n\t"
    "v_exp_f32_e32 v26, v26\n\t"
    "v_mfma_f32_16x16x32_bf16 v[16:19], a[140:143], v[152:155], v[16:19]\n\t"
    "v_add_f32_e32 v23, v23, v26\n\t"
    "v_fma_f32 v41, v41, s68, -v20\n\t"
    "v_mfma_f32_16x16x32_bf16 v[28:31], a[144:147], v[172:175], 0\n\t"
    "v_exp_f32_e32 v27, v27\n\t"
    "v_mfma_f32_16x16x32_bf16 v[28:31], a[152:155], v[168:171], v[28:31]\n\t"
    "v_add_f32_e32 v23, v23, v27\n\t"
    "v_fma_f32 v42, v42, s68, -v20\n\t"
    "v_mfma_f32_16x16x32_bf16 v[28:31], a[160:163], v[164:167], v[28:31]\n\t"
    "v_fma_f32 v43, v43, s68, -v20\n\t"
    "buffer_load_dwordx4 v7, s[52:55], s61 offen offset:1024 nt lds\n\t"
    "v_mfma_f32_16x16x32_bf16 v[28:31], a[168:171], v[160:163], v[28:31]\n\t"
    "v_cvt_pk_bf16_f32 v46, v16, v17\n\t"
    "v_mfma_f32_16x16x32_bf16 v[28:31], a[176:179], v[156:159], v[28:31]\n\t"
    "v_cvt_pk_bf16_f32 v47, v18, v19\n\t"
    "v_fma_f32 v52, v52, s68, -v20\n\t"
    "v_mfma_f32_16x16x32_bf16 v[28:31], a[184:187], v[152:155], v[28:31]\n\t"
    "v_exp_f32_e32 v40, v40\n\t"
    "v_mfma_f32_16x16x32_bf16 v[56:59], v[220:223], v[12:15], 0\n\t"
    "v_add_f32_e32 v22, v22, v40\n\t"
    "v_fma_f32 v53, v53, s68, -v20\n\t"
    "v_mfma_f32_16x16x32_bf16 v[56:59], v[216:219], v[32:35], v[56:59]\n\t"
    "v_exp_f32_e32 v41, v41\n\t"
    "buffer_load_dwordx4 v7, s[52:55], s61 offen offset:2048 nt lds\n\t"
    "v_mfma_f32_16x16x32_bf16 v[56:59], v[212:215], v[44:47], v[56:59]\n\t"
    "v_add_f32_e32 v22, v22, v41\n\t"
    "v_fma_f32 v54, v54, s68, -v20\n\t"
    "v_mfma_f32_16x16x32_bf16 v[60:63], v[196:199], v[12:15], 0\n\t"
    "v_exp_f32_e32 v42, v42\n\t"
    "v_mfma_f32_16x16x32_bf16 v[60:63], v[192:195], v[32:35], v[60:63]\n\t"
    "v_add_f32_e32 v23, v23, v42\n\t"
    "v_fma_f32 v55, v55, s68, -v20\n\t"
    "v_mfma_f32_16x16x32_bf16 v[60:63], v[188:191], v[44:47], v[60:63]\n\t"
    "v_exp_f32_e32 v43, v43\n\t"
    "v_mfma_f32_16x16x32_bf16 v[64:67], v[172:175], v[12:15], 0\n\t"
    "v_add_f32_e32 v23, v23, v43\n\t"
    "v_cvt_pk_bf16_f32 v16, v28, v29\n\t"
    "s_add_u32 m0, %[rbeg], 39936\n\t"
    "s_add_u32 s61, %[wb], 13565952\n\t"
    "v_mfma_f32_16x16x32_bf16 v[64:67], v[168:171], v[32:35], v[64:67]\n\t"
    "v_exp_f32_e32 v52, v52\n\t"
    "v_mfma_f32_16x16x32_bf16 v[64:67], v[164:167], v[44:47], v[64:67]\n\t"
    "v_add_f32_e32 v22, v22, v52\n\t"
    "v_cvt_pk_bf16_f32 v17, v30, v31\n\t"
    "v_mfma_f32_16x16x32_bf16 v[68:71], v[148:151], v[12:15], 0\n\t"
    "v_exp_f32_e32 v53, v53\n\t"
    "v_mfma_f32_16x16x32_bf16 v[68:71], v[144:147], v[32:35], v[68:71]\n\t"
    "v_add_f32_e32 v22, v22, v53\n\t"
    "v_mfma_f32_16x16x32_bf16 v[68:71], v[140:143], v[44:47], v[68:71]\n\t"
    "v_exp_f32_e32 v54, v54\n\t"
    "v_mfma_f32_16x16x32_bf16 v[12:15], a[148:151], v[172:175], 0\n\t"
    "v_add_f32_e32 v23, v23, v54\n\t"
    "buffer_load_dwordx4 v7, s[52:55], s61 offen offset:0 nt lds\n\t"
    "v_mfma_f32_16x16x32_bf16 v[12:15], a[156:159], v[168:171], v[12:15]\n\t"
    "v_mfma_f32_16x16x32_bf16 v[12:15], a[164:167], v[164:167], v[12:15]\n\t"
    "v_exp_f32_e32 v55, v55\n\t"
    "v_mfma_f32_16x16x32_bf16 v[12:15], a[172:175], v[160:163], v[12:15]\n\t"
    "v_add_f32_e32 v23, v23, v55\n\t"
    "v_add_f32_e32 v22, v22, v23\n\t"
    "v_mfma_f32_16x16x32_bf16 v[12:15], a[180:183], v[156:159], v[12:15]\n\t"
    "v_mov_b32_e32 v23, v22\n\t"
    "v_mfma_f32_16x16x32_bf16 v[12:15], a[188:191], v[152:155], v[12:15]\n\t"
    "buffer_load_dwordx4 v7, s[52:55], s61 offen offset:1024 nt lds\n\t"
    "v_mfma_f32_16x16x32_bf16 v[28:31], a[192:195], v[172:175], 0\n\t"
    "v_permlane32_swap_b32 v22, v23\n\t"
    "v_add_f32_e32 v22, v22, v23\n\t"
    "v_mfma_f32_16x16x32_bf16 v[28:31], a[200:203], v[168:171], v[28:31]\n\t"
    "v_mov_b32_e32 v23, v22\n\t"
    "v_mfma_f32_16x16x32_bf16 v[28:31], a[208:211], v[164:167], v[28:31]\n\t"
    "v_mfma_f32_16x16x32_bf16 v[28:31], a[216:219], v[160:163], v[28:31]\n\t"
    "v_cvt_pk_bf16_f32 v18, v12, v13\n\t"
    "v_cvt_pk_bf16_f32 v19, v14, v15\n\t"
    "v_mfma_f32_16x16x32_bf16 v[28:31], a[224:227], v[156:159], v[28:31]\n\t"
    "v_permlane16_swap_b32 v22, v23\n\t"
    "v_add_f32_e32 v22, v22, v23\n\t"
    "buffer_load_dwordx4 v7, s[52:55], s61 offen offset:2048 nt lds\n\t"
    "v_mfma_f32_16x16x32_bf16 v[28:31], a[232:235], v[152:155], v[28:31]\n\t"
    "v_rcp_f32_e32 v22, v22\n\t"
    "v_mfma_f32_16x16x32_bf16 v[12:15], a[196:199], v[172:175], 0\n\t"
    "v_mul_f32_e32 v36, v36, v22\n\t"
    "v_mul_f32_e32 v37, v37, v22\n\t"
    "v_mfma_f32_16x16x32_bf16 v[12:15], a[204:207], v[168:171], v[12:15]\n\t"
    "v_cvt_pk_bf16_f32 v32, v36, v37\n\t"
    "v_mul_f32_e32 v38, v38, v22\n\t"
    "v_mfma_f32_16x16x32_bf16 v[12:15], a[212:215], v[164:167], v[12:15]\n\t"
    "v_mul_f32_e32 v39, v39, v22\n\t"
    "v_cvt_pk_bf16_f32 v33, v38, v39\n\t"
    "v_mfma_f32_16x16x32_bf16 v[12:15], a[220:223], v[160:163], v[12:15]\n\t"
    "v_cvt_pk_bf16_f32 v44, v28, v29\n\t"
    "v_cvt_pk_bf16_f32 v45, v30, v31\n\t"
    "v_mfma_f32_16x16x32_bf16 v[12:15], a[228:231], v[156:159], v[12:15]\n\t"
    "v_mul_f32_e32 v24, v24, v22\n\t"
    "v_mul_f32_e32 v25, v25, v22\n\t"
    "v_mfma_f32_16x16x32_bf16 v[12:15], a[236:239], v[152:155], v[12:15]\n\t"
    "v_cvt_pk_bf16_f32 v34, v24, v25\n\t"
    "s_add_u32 m0, %[rbeg], 43008\n\t"
    "s_add_u32 s61, %[wb], 13762560\n\t"
    "v_mfma_f32_16x16x32_bf16 v[28:31], a[240:243], v[172:175], 0\n\t"
    "v_mul_f32_e32 v26, v26, v22\n\t"
    "v_mfma_f32_16x16x32_bf16 v[28:31], a[248:251], v[168:171], v[28:31]\n\t"
    "v_mul_f32_e32 v27, v27, v22\n\t"
    "v_cvt_pk_bf16_f32 v35, v26, v27\n\t"
    "v_mfma_f32_16x16x32_bf16 v[28:31], v[224:227], v[164:167], v[28:31]\n\t"
    "v_mul_f32_e32 v40, v40, v22\n\t"
    "v_mul_f32_e32 v41, v41, v22\n\t"
    "buffer_load_dwordx4 v7, s[52:55], s61 offen offset:0 nt lds\n\t"
    "v_mfma_f32_16x16x32_bf16 v[28:31], v[232:235], v[160:163], v[28:31]\n\t"
    "v_cvt_pk_bf16_f32 v46, v12, v13\n\t"
    "v_cvt_pk_bf16_f32 v47, v14, v15\n\t"
    "v_mfma_f32_16x16x32_bf16 v[28:31], v[240:243], v[156:159], v[28:31]\n\t"
    "v_cvt_pk_bf16_f32 v24, v40, v41\n\t"
    "v_mul_f32_e32 v42, v42, v22\n\t"
    "v_mfma_f32_16x16x32_bf16 v[28:31], v[248:251], v[152:155], v[28:31]\n\t"
    "v_mul_f32_e32 v43, v43, v22\n\t"
    "v_cvt_pk_bf16_f32 v25, v42, v43\n\t"
    "v_mfma_f32_16x16x32_bf16 v[12:15], a[244:247], v[172:175], 0\n\t"
    "v_mul_f32_e32 v52, v52, v22\n\t"
    "v_mul_f32_e32 v53, v53, v22\n\t"
    "v_mfma_f32_16x16x32_bf16 v[12:15], a[252:255], v[168:171], v[12:15]\n\t"
    "v_cvt_pk_bf16_f32 v26, v52, v53\n\t"
    "v_mul_f32_e32 v54, v54, v22\n\t"
    "v_mfma_f32_16x16x32_bf16 v[12:15], v[228:231], v[164:167], v[12:15]\n\t"
    "v_mul_f32_e32 v55, v55, v22\n\t"
    "v_cvt_pk_bf16_f32 v27, v54, v55\n\t"
    "v_mfma_f32_16x16x32_bf16 v[12:15], v[236:239], v[160:163], v[12:15]\n\t"
    "v_cvt_pk_bf16_f32 v36, v28, v29\n\t"
    "v_cvt_pk_bf16_f32 v37, v30, v31\n\t"
    "v_mfma_f32_16x16x32_bf16 v[12:15], v[244:247], v[156:159], v[12:15]\n\t"
    "buffer_load_dwordx4 v7, s[52:55], s61 offen offset:1024 nt lds\n\t"
    "v_mfma_f32_16x16x32_bf16 v[12:15], v[252:255], v[152:155], v[12:15]\n\t"
    "v_mfma_f32_16x16x32_bf16 v[20:23], a[0:3], v[148:151], 0\n\t"
    "ds_write_b128 v6, v[32:35] offset:2048\n\t"
    "v_mfma_f32_16x16x32_bf16 v[20:23], a[8:11], v[144:147], v[20:23]\n\t"
    "buffer_load_dwordx4 v7, s[52:55], s61 offen offset:2048 nt lds\n\t"
    "v_mfma_f32_16x16x32_bf16 v[20:23], a[16:19], v[140:143], v[20:23]\n\t"
    "v_mfma_f32_16x16x32_bf16 v[20:23], a[24:27], v[136:139], v[20:23]\n\t"
    "v_cvt_pk_bf16_f32 v38, v12, v13\n\t"
    "v_mfma_f32_16x16x32_bf16 v[20:23], a[32:35], v[132:135], v[20:23]\n\t"
    "ds_write_b128 v6, v[24:27] offset:3072\n\t"
    "v_mfma_f32_16x16x32_bf16 v[20:23], a[40:43], v[128:131], v[20:23]\n\t"
    "v_cvt_pk_bf16_f32 v39, v14, v15\n\t"
    "v_mfma_f32_16x16x32_bf16 v[56:59], v[208:211], v[16:19], v[56:59]\n\t"
    "s_add_u32 m0, %[rbeg], 46080\n\t"
    "s_add_u32 s61, %[wb], 13959168\n\t"
    "v_mfma_f32_16x16x32_bf16 v[56:59], v[204:207], v[44:47], v[56:59]\n\t"
    "v_mfma_f32_16x16x32_bf16 v[56:59], v[200:203], v[36:39], v[56:59]\n\t"
    "v_mfma_f32_16x16x32_bf16 v[60:63], v[184:187], v[16:19], v[60:63]\n\t"
    "v_cvt_pk_bf16_f32 v32, v20, v21\n\t"
    "v_cvt_pk_bf16_f32 v33, v22, v23\n\t"
    "v_mfma_f32_16x16x32_bf16 v[60:63], v[180:183], v[44:47], v[60:63]\n\t"
    "v_mfma_f32_16x16x32_bf16 v[60:63], v[176:179], v[36:39], v[60:63]\n\t"
    "buffer_load_dwordx4 v7, s[52:55], s61 offen offset:0 nt lds\n\t"
    "v_mfma_f32_16x16x32_bf16 v[64:67], v[160:163], v[16:19], v[64:67]\n\t"
    "v_max3_f32 v72, v56, v57, v58\n\t"
    "v_mfma_f32_16x16x32_bf16 v[64:67], v[156:159], v[44:47], v[64:67]\n\t"
    "v_mfma_f32_16x16x32_bf16 v[64:67], v[152:155], v[36:39], v[64:67]\n\t"
    "v_mfma_f32_16x16x32_bf16 v[68:71], v[136:139], v[16:19], v[68:71]\n\t"
    "v_max3_f32 v73, v59, v60, v61\n\t"
    "v_max3_f32 v72, v72, v62, v63\n\t"
    "v_mfma_f32_16x16x32_bf16 v[68:71], v[132:135], v[44:47], v[68:71]\n\t"
    "buffer_load_dwordx4 v7, s[52:55], s61 offen offset:1024 nt lds\n\t"
    "v_mfma_f32_16x16x32_bf16 v[68:71], v[128:131], v[36:39], v[68:71]\n\t"
    "v_mfma_f32_16x16x32_bf16 v[12:15], a[4:7], v[148:151], 0\n\t"
    "v_max3_f32 v73, v73, v64, v65\n\t"
    "v_max3_f32 v72, v72, v66, v67\n\t"
    "v_mfma_f32_16x16x32_bf16 v[12:15], a[12:15], v[144:147], v[12:15]\n\t"
    "v_mfma_f32_16x16x32_bf16 v[12:15], a[20:23], v[140:143], v[12:15]\n\t"
    "v_mfma_f32_16x16x32_bf16 v[12:15], a[28:31], v[136:139], v[12:15]\n\t"
    "v_max3_f32 v73, v73, v68, v69\n\t"
    "v_max3_f32 v72, v72, v70, v71\n\t"
    "v_mfma_f32_16x16x32_bf16 v[12:15], a[36:39], v[132:135], v[12:15]\n\t"
    "v_max_f32_e32 v72, v72, v73\n\t"
    "v_mov_b32_e32 v73, v72\n\t"
    "v_mfma_f32_16x16x32_bf16 v[12:15], a[44:47], v[128:131], v[12:15]\n\t"
    "buffer_load_dwordx4 v7, s[52:55], s61 offen offset:2048 nt lds\n\t"
    "v_mfma_f32_16x16x32_bf16 v[16:19], a[48:51], v[148:151], 0\n\t"
    "v_permlane32_swap_b32 v72, v73\n\t"
    "v_max_f32_e32 v72, v72, v73\n\t"
    "v_mfma_f32_16x16x32_bf16 v[16:19], a[56:59], v[144:147], v[16:19]\n\t"
    "v_mov_b32_e32 v73, v72\n\t"
    "v_mfma_f32_16x16x32_bf16 v[16:19], a[64:67], v[140:143], v[16:19]\n\t"
    "s_add_u32 m0, %[rbeg], 49152\n\t"
    "s_add_u32 s61, %[wb], 25165824\n\t"
    "v_mfma_f32_16x16x32_bf16 v[16:19], a[72:75], v[136:139], v[16:19]\n\t"
    "v_cvt_pk_bf16_f32 v34, v12, v13\n\t"
    "v_cvt_pk_bf16_f32 v35, v14, v15\n\t"
    "v_mfma_f32_16x16x32_bf16 v[16:19], a[80:83], v[132:135], v[16:19]\n\t"
    "v_permlane16_swap_b32 v72, v73\n\t"
    "v_max_f32_e32 v72, v72, v73\n\t"
    "v_mfma_f32_16x16x32_bf16 v[16:19], a[88:91], v[128:131], v[16:19]\n\t"
    "v_mul_f32_e32 v72, s68, v72\n\t"
    "v_fma_f32 v56, v56, s68, -v72\n\t"
    "v_mfma_f32_16x16x32_bf16 v[12:15], a[52:55], v[148:151], 0\n\t"
    "v_exp_f32_e32 v56, v56\n\t"
    "v_mfma_f32_16x16x32_bf16 v[12:15], a[60:63], v[144:147], v[12:15]\n\t"
    "v_fma_f32 v57, v57, s68, -v72\n\t"
    "v_fma_f32 v58, v58, s68, -v72\n\t"
    "buffer_load_dwordx4 v7, s[52:55], s61 offen offset:0 nt lds\n\t"
    "v_mfma_f32_16x16x32_bf16 v[12:15], a[68:71], v[140:143], v[12:15]\n\t"
    "v_exp_f32_e32 v57, v57\n\t"
    "v_mfma_f32_16x16x32_bf16 v[12:15], a[76:79], v[136:139], v[12:15]\n\t"
    "v_cvt_pk_bf16_f32 v36, v16, v17\n\t"
    "v_cvt_pk_bf16_f32 v37, v18, v19\n\t"
    "v_mfma_f32_16x16x32_bf16 v[12:15], a[84:87], v[132:135], v[12:15]\n\t"
    "v_add_f32_e32 v74, v56, v57\n\t"
    "v_fma_f32 v59, v59, s68, -v72\n\t"
    "v_mfma_f32_16x16x32_bf16 v[12:15], a[92:95], v[128:131], v[12:15]\n\t"
    "v_fma_f32 v60, v60, s68, -v72\n\t"
    "v_mfma_f32_16x16x32_bf16 v[16:19], a[96:99], v[148:151], 0\n\t"
    "v_fma_f32 v61, v61, s68, -v72\n\t"
    "buffer_load_dwordx4 v7, s[52:55], s61 offen offset:1024 nt lds\n\t"
    "v_mfma_f32_16x16x32_bf16 v[16:19], a[104:107], v[144:147], v[16:19]\n\t"
    "v_exp_f32_e32 v58, v58\n\t"
    "v_mfma_f32_16x16x32_bf16 v[16:19], a[112:115], v[140:143], v[16:19]\n\t"
    "v_exp_f32_e32 v59, v59\n\t"
    "v_mfma_f32_16x16x32_bf16 v[16:19], a[120:123], v[136:139], v[16:19]\n\t"
    "v_cvt_pk_bf16_f32 v38, v12, v13\n\t"
    "v_cvt_pk_bf16_f32 v39, v14, v15\n\t"
    "v_mfma_f32_16x16x32_bf16 v[16:19], a[128:131], v[132:135], v[16:19]\n\t"
    "v_add_f32_e32 v75, v58, v59\n\t"
    "v_fma_f32 v62, v62, s68, -v72\n\t"
    "v_mfma_f32_16x16x32_bf16 v[16:19], a[136:139], v[128:131], v[16:19]\n\t"
    "v_exp_f32_e32 v60, v60\n\t"
    "buffer_load_dwordx4 v7, s[52:55], s61 offen offset:2048 nt lds\n\t"
    "v_mfma_f32_16x16x32_bf16 v[20:23], a[100:103], v[148:151], 0\n\t"
    "v_add_f32_e32 v74, v74, v60\n\t"
    "v_fma_f32 v63, v63, s68, -v72\n\t"
    "v_mfma_f32_16x16x32_bf16 v[20:23], a[108:111], v[144:147], v[20:23]\n\t"
    "v_exp_f32_e32 v61, v61\n\t"
    "v_mfma_f32_16x16x32_bf16 v[20:23], a[116:119], v[140:143], v[20:23]\n\t"
    "v_add_f32_e32 v74, v74, v61\n\t"
    "v_fma_f32 v64, v64, s68, -v72\n\t"
    "v_mfma_f32_16x16x32_bf16 v[20:23], a[124:127], v[136:139], v[20:23]\n\t"
    "v_cvt_pk_bf16_f32 v40, v16, v17\n\t"
    "v_cvt_pk_bf16_f32 v41, v18, v19\n\t"
    "v_mfma_f32_16x16x32_bf16 v[20:23], a[132:135], v[132:135], v[20:23]\n\t"
    "v_exp_f32_e32 v62, v62\n\t"
    "v_mfma_f32_16x16x32_bf16 v[20:23], a[140:143], v[128:131], v[20:23]\n\t"
    "v_add_f32_e32 v75, v75, v62\n\t"
    "v_fma_f32 v65, v65, s68, -v72\n\t"
    "v_mfma_f32_16x16x32_bf16 v[44:47], a[144:147], v[148:151], 0\n\t"
    "v_exp_f32_e32 v63, v63\n\t"
    "v_mfma_f32_16x16x32_bf16 v[44:47], a[152:155], v[144:147], v[44:47]\n\t"
    "v_add_f32_e32 v75, v75, v63\n\t"
    "v_fma_f32 v66, v66, s68, -v72\n\t"
    "s_mov_b32 s60, 8\n\t"
    "s_add_u32 s61, %[wb], 25362432\n\t"
    ".Lmid_%=:\n\t"
    "v_mfma_f32_16x16x32_bf16 v[44:47], a[160:163], v[140:143], v[44:47]\n\t"
    "s_add_u32 m0, s62, 3072\n\t"
    "v_fma_f32 v67, v67, s68, -v72\n\t"
    "v_mfma_f32_16x16x32_bf16 v[44:47], a[168:171], v[136:139], v[44:47]\n\t"
    "buffer_load_dwordx4 v7, s[52:55], s61 offen offset:0 nt lds\n\t"
    "v_cvt_pk_bf16_f32 v42, v20, v21\n\t"
    "v_mfma_f32_16x16x32_bf16 v[44:47], a[176:179], v[132:135], v[44:47]\n\t"
    "v_cvt_pk_bf16_f32 v43, v22, v23\n\t"
    "v_fma_f32 v68, v68, s68, -v72\n\t"
    "v_mfma_f32_16x16x32_bf16 v[44:47], a[184:187], v[128:131], v[44:47]\n\t"
    "v_exp_f32_e32 v64, v64\n\t"
    "v_mfma_f32_16x16x32_bf16 v[12:15], v[220:223], v[32:35], 0\n\t"
    "v_add_f32_e32 v74, v74, v64\n\t"
    "v_fma_f32 v69, v69, s68, -v72\n\t"
    "v_mfma_f32_16x16x32_bf16 v[12:15], v[216:219], v[36:39], v[12:15]\n\t"
    "v_exp_f32_e32 v65, v65\n\t"
    "v_mfma_f32_16x16x32_bf16 v[12:15], v[212:215], v[40:43], v[12:15]\n\t"
    "s_waitcnt vmcnt(22)\n\t"
    "v_add_f32_e32 v74, v74, v65\n\t"
    "v_mfma_f32_16x16x32_bf16 v[16:19], v[196:199], v[32:35], 0\n\t"
    "v_exp_f32_e32 v66, v66\n\t"
    "v_mfma_f32_16x16x32_bf16 v[16:19], v[192:195], v[36:39], v[16:19]\n\t"
    "ds_read_b128 v[220:223], v0 offset:0\n\t"
    "v_add_f32_e32 v75, v75, v66\n\t"
    "v_mfma_f32_16x16x32_bf16 v[16:19], v[188:191], v[40:43], v[16:19]\n\t"
    "v_exp_f32_e32 v67, v67\n\t"
    "v_mfma_f32_16x16x32_bf16 v[20:23], v[172:175], v[32:35], 0\n\t"
    "ds_read_b128 v[216:219], v1 offset:0\n\t"
    "v_add_f32_e32 v75, v75, v67\n\t"
    "v_mfma_f32_16x16x32_bf16 v[20:23], v[168:171], v[36:39], v[20:23]\n\t"
    "v_exp_f32_e32 v68, v68\n\t"
    "v_fma_f32 v70, v70, s68, -v72\n\t"
    "v_mfma_f32_16x16x32_bf16 v[20:23], v[164:167], v[40:43], v[20:23]\n\t"
    "ds_read_b128 v[212:215], v2 offset:0\n\t"
    "v_add_f32_e32 v74, v74, v68\n\t"
    "v_mfma_f32_16x16x32_bf16 v[24:27], v[148:151], v[32:35], 0\n\t"
    "v_exp_f32_e32 v69, v69\n\t"
    "v_mfma_f32_16x16x32_bf16 v[24:27], v[144:147], v[36:39], v[24:27]\n\t"
    "ds_read_b128 v[28:31], v3 offset:0\n\t"
    "v_add_f32_e32 v74, v74, v69\n\t"
    "v_mfma_f32_16x16x32_bf16 v[24:27], v[140:143], v[40:43], v[24:27]\n\t"
    "v_exp_f32_e32 v70, v70\n\t"
    "v_mfma_f32_16x16x32_bf16 v[48:51], a[148:151], v[148:151], 0\n\t"
    "ds_read_b128 v[32:35], v4 offset:0\n\t"
    "v_add_f32_e32 v75, v75, v70\n\t"
    "v_mfma_f32_16x16x32_bf16 v[48:51], a[156:159], v[144:147], v[48:51]\n\t"
    "buffer_load_dwordx4 v7, s[52:55], s61 offen offset:1024 nt lds\n\t"
    "v_fma_f32 v71, v71, s68, -v72\n\t"
    "v_mfma_f32_16x16x32_bf16 v[48:51], a[164:167], v[140:143], v[48:51]\n\t"
    "ds_read_b128 v[36:39], v5 offset:0\n\t"
    "v_cvt_pk_bf16_f32 v40, v44, v45\n\t"
    "v_mfma_f32_16x16x32_bf16 v[48:51], a[172:175], v[136:139], v[48:51]\n\t"
    "v_exp_f32_e32 v71, v71\n\t"
    "v_mfma_f32_16x16x32_bf16 v[48:51], a[180:183], v[132:135], v[48:51]\n\t"
    "v_add_f32_e32 v75, v75, v71\n\t"
    "v_add_f32_e32 v74, v74, v75\n\t"
    "v_mfma_f32_16x16x32_bf16 v[48:51], a[188:191], v[128:131], v[48:51]\n\t"
    "v_mov_b32_e32 v75, v74\n\t"
    "v_cvt_pk_bf16_f32 v41, v46, v47\n\t"
    "v_mfma_f32_16x16x32_bf16 v[52:55], a[192:195], v[148:151], 0\n\t"
    "v_mfma_f32_16x16x32_bf16 v[52:55], a[200:203], v[144:147], v[52:55]\n\t"
    "v_permlane32_swap_b32 v74, v75\n\t"
    "v_add_f32_e32 v74, v74, v75\n\t"
    "v_mfma_f32_16x16x32_bf16 v[52:55], a[208:211], v[140:143], v[52:55]\n\t"
    "v_mov_b32_e32 v75, v74\n\t"
    "v_mfma_f32_16x16x32_bf16 v[52:55], a[216:219], v[136:139], v[52:55]\n\t"
    "v_cvt_pk_bf16_f32 v42, v48, v49\n\t"
    "v_cvt_pk_bf16_f32 v43, v50, v51\n\t"
    "v_mfma_f32_16x16x32_bf16 v[52:55], a[224:227], v[132:135], v[52:55]\n\t"
    "v_permlane16_swap_b32 v74, v75\n\t"
    "v_add_f32_e32 v74, v74, v75\n\t"
    "v_mfma_f32_16x16x32_bf16 v[52:55], a[232:235], v[128:131], v[52:55]\n\t"
    "v_rcp_f32_e32 v74, v74\n\t"
    "v_mfma_f32_16x16x32_bf16 v[76:79], a[196:199], v[148:151], 0\n\t"
    "v_mul_f32_e32 v56, v56, v74\n\t"
    "v_mul_f32_e32 v57, v57, v74\n\t"
    "v_mfma_f32_16x16x32_bf16 v[76:79], a[204:207], v[144:147], v[76:79]\n\t"
    "v_cvt_pk_bf16_f32 v44, v56, v57\n\t"
    "v_mul_f32_e32 v58, v58, v74\n\t"
    "v_mfma_f32_16x16x32_bf16 v[76:79], a[212:215], v[140:143], v[76:79]\n\t"
    "v_mul_f32_e32 v59, v59, v74\n\t"
    "v_cvt_pk_bf16_f32 v45, v58, v59\n\t"
    "v_mfma_f32_16x16x32_bf16 v[76:79], a[220:223], v[136:139], v[76:79]\n\t"
    "v_cvt_pk_bf16_f32 v48, v52, v53\n\t"
    "v_cvt_pk_bf16_f32 v49, v54, v55\n\t"
    "v_mfma_f32_16x16x32_bf16 v[76:79], a[228:231], v[132:135], v[76:79]\n\t"
    "v_mul_f32_e32 v60, v60, v74\n\t"
    "v_mul_f32_e32 v61, v61, v74\n\t"
    "v_mfma_f32_16x16x32_bf16 v[76:79], a[236:239], v[128:131], v[76:79]\n\t"
    "buffer_load_dwordx4 v7, s[52:55], s61 offen offset:2048 nt lds\n\t"
    "v_cvt_pk_bf16_f32 v46, v60, v61\n\t"
    "v_mfma_f32_16x16x32_bf16 v[80:83], a[240:243], v[148:151], 0\n\t"
    "s_add_u32 s61, s61, 196608\n\t"
    "v_mul_f32_e32 v62, v62, v74\n\t"
    "v_mfma_f32_16x16x32_bf16 v[80:83], a[248:251], v[144:147], v[80:83]\n\t"
    "v_mul_f32_e32 v63, v63, v74\n\t"
    "v_cvt_pk_bf16_f32 v47, v62, v63\n\t"
    "v_mfma_f32_16x16x32_bf16 v[80:83], v[224:227], v[140:143], v[80:83]\n\t"
    "v_mul_f32_e32 v64, v64, v74\n\t"
    "v_mul_f32_e32 v65, v65, v74\n\t"
    "v_mfma_f32_16x16x32_bf16 v[80:83], v[232:235], v[136:139], v[80:83]\n\t"
    "v_cvt_pk_bf16_f32 v50, v76, v77\n\t"
    "v_cvt_pk_bf16_f32 v51, v78, v79\n\t"
    "v_mfma_f32_16x16x32_bf16 v[80:83], v[240:243], v[132:135], v[80:83]\n\t"
    "v_cvt_pk_bf16_f32 v52, v64, v65\n\t"
    "v_mul_f32_e32 v66, v66, v74\n\t"
    "v_mfma_f32_16x16x32_bf16 v[80:83], v[248:251], v[128:131], v[80:83]\n\t"
    "v_mul_f32_e32 v67, v67, v74\n\t"
    "v_cvt_pk_bf16_f32 v53, v66, v67\n\t"
    "v_mfma_f32_16x16x32_bf16 v[56:59], a[244:247], v[148:151], 0\n\t"
    "v_mul_f32_e32 v68, v68, v74\n\t"
    "v_mul_f32_e32 v69, v69, v74\n\t"
    "v_mfma_f32_16x16x32_bf16 v[56:59], a[252:255], v[144:147], v[56:59]\n\t"
    "v_cvt_pk_bf16_f32 v54, v68, v69\n\t"
    "v_mul_f32_e32 v70, v70, v74\n\t"
    "v_mfma_f32_16x16x32_bf16 v[56:59], v[228:231], v[140:143], v[56:59]\n\t"
    "v_mul_f32_e32 v71, v71, v74\n\t"
    "v_cvt_pk_bf16_f32 v55, v70, v71\n\t"
    "v_mfma_f32_16x16x32_bf16 v[56:59], v[236:239], v[136:139], v[56:59]\n\t"
    "v_cvt_pk_bf16_f32 v60, v80, v81\n\t"
    "v_cvt_pk_bf16_f32 v61, v82, v83\n\t"
    "v_mfma_f32_16x16x32_bf16 v[56:59], v[244:247], v[132:135], v[56:59]\n\t"
    "s_mov_b32 s63, s62\n\t"
    "s_mov_b32 s62, s64\n\t"
    "v_mfma_f32_16x16x32_bf16 v[56:59], v[252:255], v[128:131], v[56:59]\n\t"
    "s_sub_u32 s60, s60, 1\n\t"
    "s_cmp_lg_u32 s60, 0\n\t"
    "s_cbranch_scc0 .Ltail_%=\n\t"
    "s_waitcnt lgkmcnt(0)\n\t"
    "v_mfma_f32_16x16x32_bf16 v[64:67], a[0:3], v[220:223], 0\n\t"
    "ds_write_b128 v6, v[44:47] offset:4096\n\t"
    "v_mfma_f32_16x16x32_bf16 v[64:67], a[8:11], v[216:219], v[64:67]\n\t"
    "v_mfma_f32_16x16x32_bf16 v[64:67], a[16:19], v[212:215], v[64:67]\n\t"
    "s_add_u32 m0, s63, 6144\n\t"
    "v_mfma_f32_16x16x32_bf16 v[64:67], a[24:27], v[28:31], v[64:67]\n\t"
    "buffer_load_dwordx4 v7, s[52:55], s61 offen offset:0 nt lds\n\t"
    "v_cvt_pk_bf16_f32 v62, v56, v57\n\t"
    "v_mfma_f32_16x16x32_bf16 v[64:67], a[32:35], v[32:35], v[64:67]\n\t"
    "ds_write_b128 v6, v[52:55] offset:5120\n\t"
    "v_mfma_f32_16x16x32_bf16 v[64:67], a[40:43], v[36:39], v[64:67]\n\t"
    "v_cvt_pk_bf16_f32 v63, v58, v59\n\t"
    "v_mfma_f32_16x16x32_bf16 v[12:15], v[208:211], v[40:43], v[12:15]\n\t"
    "v_mfma_f32_16x16x32_bf16 v[12:15], v[204:207], v[48:51], v[12:15]\n\t"
    "v_mfma_f32_16x16x32_bf16 v[12:15], v[200:203], v[60:63], v[12:15]\n\t"
    "v_mfma_f32_16x16x32_bf16 v[16:19], v[184:187], v[40:43], v[16:19]\n\t"
    "v_cvt_pk_bf16_f32 v44, v64, v65\n\t"
    "v_cvt_pk_bf16_f32 v45, v66, v67\n\t"
    "v_mfma_f32_16x16x32_bf16 v[16:19], v[180:183], v[48:51], v[16:19]\n\t"
    "s_waitcnt lgkmcnt(0)\n\t"
    "s_barrier \n\t"
    "v_mfma_f32_16x16x32_bf16 v[16:19], v[176:179], v[60:63], v[16:19]\n\t"
    "v_mfma_f32_16x16x32_bf16 v[20:23], v[160:163], v[40:43], v[20:23]\n\t"
    "v_max3_f32 v52, v12, v13, v14\n\t"
    "v_mfma_f32_16x16x32_bf16 v[20:23], v[156:159], v[48:51], v[20:23]\n\t"
    "v_mfma_f32_16x16x32_bf16 v[20:23], v[152:155], v[60:63], v[20:23]\n\t"
    "v_mfma_f32_16x16x32_bf16 v[24:27], v[136:139], v[40:43], v[24:27]\n\t"
    "v_max3_f32 v53, v15, v16, v17\n\t"
    "v_max3_f32 v52, v52, v18, v19\n\t"
    "v_mfma_f32_16x16x32_bf16 v[24:27], v[132:135], v[48:51], v[24:27]\n\t"
    "s_waitcnt vmcnt(7)\n\t"
    "v_mfma_f32_16x16x32_bf16 v[24:27], v[128:131], v[60:63], v[24:27]\n\t"
    "ds_read_b128 v[196:199], v0 offset:6144\n\t"
    "v_mfma_f32_16x16x32_bf16 v[40:43], a[4:7], v[220:223], 0\n\t"
    "v_max3_f32 v53, v53, v20, v21\n\t"
    "v_max3_f32 v52, v52, v22, v23\n\t"
    "v_mfma_f32_16x16x32_bf16 v[40:43], a[12:15], v[216:219], v[40:43]\n\t"
    "buffer_load_dwordx4 v7, s[52:55], s61 offen offset:1024 nt lds\n\t"
    "ds_read_b128 v[192:195], v1 offset:6144\n\t"
    "v_mfma_f32_16x16x32_bf16 v[40:43], a[20:23], v[212:215], v[40:43]\n\t"
    "v_mfma_f32_16x16x32_bf16 v[40:43], a[28:31], v[28:31], v[40:43]\n\t"
    "ds_read_b128 v[188:191], v2 offset:6144\n\t"
    "v_max3_f32 v53, v53, v24, v25\n\t"
    "v_mfma_f32_16x16x32_bf16 v[40:43], a[36:39], v[32:35], v[40:43]\n\t"
    "v_max3_f32 v52, v52, v26, v27\n\t"
    "v_max_f32_e32 v52, v52, v53\n\t"
    "v_mfma_f32_16x16x32_bf16 v[40:43], a[44:47], v[36:39], v[40:43]\n\t"
    "ds_read_b128 v[172:175], v0 offset:12288\n\t"
    "v_mov_b32_e32 v53, v52\n\t"
    "v_mfma_f32_16x16x32_bf16 v[48:51], a[48:51], v[220:223], 0\n\t"
    "v_mfma_f32_16x16x32_bf16 v[48:51], a[56:59], v[216:219], v[48:51]\n\t"
    "ds_read_b128 v[168:171], v1 offset:12288\n\t"
    "v_permlane32_swap_b32 v52, v53\n\t"
    "v_mfma_f32_16x16x32_bf16 v[48:51], a[64:67], v[212:215], v[48:51]\n\t"
    "v_max_f32_e32 v52, v52, v53\n\t"
    "v_mov_b32_e32 v53, v52\n\t"
    "v_mfma_f32_16x16x32_bf16 v[48:51], a[72:75], v[28:31], v[48:51]\n\t"
    "ds_read_b128 v[164:167], v2 offset:12288\n\t"
    "v_cvt_pk_bf16_f32 v46, v40, v41\n\t"
    "v_mfma_f32_16x16x32_bf16 v[48:51], a[80:83], v[32:35], v[48:51]\n\t"
    "v_cvt_pk_bf16_f32 v47, v42, v43\n\t"
    "v_permlane16_swap_b32 v52, v53\n\t"
    "v_mfma_f32_16x16x32_bf16 v[48:51], a[88:91], v[36:39], v[48:51]\n\t"
    "ds_read_b128 v[148:151], v0 offset:18432\n\t"
    "v_max_f32_e32 v52, v52, v53\n\t"
    "v_mfma_f32_16x16x32_bf16 v[56:59], a[52:55], v[220:223], 0\n\t"
    "v_mul_f32_e32 v52, s68, v52\n\t"
    "v_fma_f32 v12, v12, s68, -v52\n\t"
    "v_mfma_f32_16x16x32_bf16 v[56:59], a[60:63], v[216:219], v[56:59]\n\t"
    "ds_read_b128 v[144:147], v1 offset:18432\n\t"
    "v_fma_f32 v13, v13, s68, -v52\n\t"
    "v_mfma_f32_16x16x32_bf16 v[56:59], a[68:71], v[212:215], v[56:59]\n\t"
    "v_exp_f32_e32 v12, v12\n\t"
    "v_mfma_f32_16x16x32_bf16 v[56:59], a[76:79], v[28:31], v[56:59]\n\t"
    "ds_read_b128 v[140:143], v2 offset:18432\n\t"
    "v_cvt_pk_bf16_f32 v40, v48, v49\n\t"
    "v_fma_f32 v14, v14, s68, -v52\n\t"
    "v_mfma_f32_16x16x32_bf16 v[56:59], a[84:87], v[32:35], v[56:59]\n\t"
    "v_cvt_pk_bf16_f32 v41, v50, v51\n\t"
    "v_fma_f32 v15, v15, s68, -v52\n\t"
    "v_mfma_f32_16x16x32_bf16 v[56:59], a[92:95], v[36:39], v[56:59]\n\t"
    "buffer_load_dwordx4 v7, s[52:55], s61 offen offset:2048 nt lds\n\t"
    "v_fma_f32 v16, v16, s68, -v52\n\t"
    "v_mfma_f32_16x16x32_bf16 v[60:63], a[96:99], v[220:223], 0\n\t"
    "s_add_u32 s61, s61, 196608\n\t"
    "v_fma_f32 v17, v17, s68, -v52\n\t"
    "v_mfma_f32_16x16x32_bf16 v[60:63], a[104:107], v[216:219], v[60:63]\n\t"
    "v_exp_f32_e32 v13, v13\n\t"
    "v_mfma_f32_16x16x32_bf16 v[60:63], a[112:115], v[212:215], v[60:63]\n\t"
    "v_add_f32_e32 v54, v12, v13\n\t"
    "v_fma_f32 v18, v18, s68, -v52\n\t"
    "v_mfma_f32_16x16x32_bf16 v[60:63], a[120:123], v[28:31], v[60:63]\n\t"
    "v_cvt_pk_bf16_f32 v42, v56, v57\n\t"
    "v_cvt_pk_bf16_f32 v43, v58, v59\n\t"
    "v_mfma_f32_16x16x32_bf16 v[60:63], a[128:131], v[32:35], v[60:63]\n\t"
    "v_exp_f32_e32 v14, v14\n\t"
    "v_mfma_f32_16x16x32_bf16 v[60:63], a[136:139], v[36:39], v[60:63]\n\t"
    "v_exp_f32_e32 v15, v15\n\t"
    "v_mfma_f32_16x16x32_bf16 v[48:51], a[100:103], v[220:223], 0\n\t"
    "v_add_f32_e32 v55, v14, v15\n\t"
    "v_fma_f32 v19, v19, s68, -v52\n\t"
    "v_mfma_f32_16x16x32_bf16 v[48:51], a[108:111], v[216:219], v[48:51]\n\t"
    "v_exp_f32_e32 v16, v16\n\t"
    "v_mfma_f32_16x16x32_bf16 v[48:51], a[116:119], v[212:215], v[48:51]\n\t"
    "v_add_f32_e32 v54, v54, v16\n\t"
    "v_fma_f32 v20, v20, s68, -v52\n\t"
    "v_mfma_f32_16x16x32_bf16 v[48:51], a[124:127], v[28:31], v[48:51]\n\t"
    "v_cvt_pk_bf16_f32 v56, v60, v61\n\t"
    "v_cvt_pk_bf16_f32 v57, v62, v63\n\t"
    "v_mfma_f32_16x16x32_bf16 v[48:51], a[132:135], v[32:35], v[48:51]\n\t"
    "v_exp_f32_e32 v17, v17\n\t"
    "v_mfma_f32_16x16x32_bf16 v[48:51], a[140:143], v[36:39], v[48:51]\n\t"
    "v_add_f32_e32 v54, v54, v17\n\t"
    "v_fma_f32 v21, v21, s68, -v52\n\t"
    "v_mfma_f32_16x16x32_bf16 v[60:63], a[144:147], v[220:223], 0\n\t"
    "v_exp_f32_e32 v18, v18\n\t"
    "v_mfma_f32_16x16x32_bf16 v[60:63], a[152:155], v[216:219], v[60:63]\n\t"
    "v_add_f32_e32 v55, v55, v18\n\t"
    "v_exp_f32_e32 v19, v19\n\t"
    "v_mfma_f32_16x16x32_bf16 v[60:63], a[160:163], v[212:215], v[60:63]\n\t"
    "s_add_u32 m0, s63, 9216\n\t"
    "v_add_f32_e32 v55, v55, v19\n\t"
    "v_mfma_f32_16x16x32_bf16 v[60:63], a[168:171], v[28:31], v[60:63]\n\t"
    "buffer_load_dwordx4 v7, s[52:55], s61 offen offset:0 nt lds\n\t"
    "v_cvt_pk_bf16_f32 v58, v48, v49\n\t"
    "v_mfma_f32_16x16x32_bf16 v[60:63], a[176:179], v[32:35], v[60:63]\n\t"
    "v_cvt_pk_bf16_f32 v59, v50, v51\n\t"
    ".Lh5_%=:\n\t"
    "s_cbranch_execz .LrM_%=\n\t"
    "v_fma_f32 v22, v22, s68, -v52\n\t"
    "v_mfma_f32_16x16x32_bf16 v[60:63], a[184:187], v[36:39], v[60:63]\n\t"
    "v_exp_f32_e32 v20, v20\n\t"
    "v_mfma_f32_16x16x32_bf16 v[64:67], v[220:223], v[44:47], 0\n\t"
    "v_add_f32_e32 v54, v54, v20\n\t"
    "v_fma_f32 v23, v23, s68, -v52\n\t"
    "v_mfma_f32_16x16x32_bf16 v[64:67], v[216:219], v[40:43], v[64:67]\n\t"
    "v_exp_f32_e32 v21, v21\n\t"
    "v_mfma_f32_16x16x32_bf16 v[64:67], v[212:215], v[56:59], v[64:67]\n\t"
    "v_add_f32_e32 v54, v54, v21\n\t"
    "s_waitcnt lgkmcnt(0)\n\t"
    "v_mfma_f32_16x16x32_bf16 v[48:51], v[196:199], v[44:47], 0\n\t"
    "v_exp_f32_e32 v22, v22\n\t"
    "v_mfma_f32_16x16x32_bf16 v[48:51], v[192:195], v[40:43], v[48:51]\n\t"
    "v_add_f32_e32 v55, v55, v22\n\t"
    "v_fma_f32 v24, v24, s68, -v52\n\t"
    "v_mfma_f32_16x16x32_bf16 v[48:51], v[188:191], v[56:59], v[48:51]\n\t"
    "ds_read_b128 v[184:187], v3 offset:6144\n\t"
    "v_fma_f32 v25, v25, s68, -v52\n\t"
    "v_mfma_f32_16x16x32_bf16 v[68:71], v[172:175], v[44:47], 0\n\t"
    "v_exp_f32_e32 v23, v23\n\t"
    "v_mfma_f32_16x16x32_bf16 v[68:71], v[168:171], v[40:43], v[68:71]\n\t"
    "ds_read_b128 v[180:183], v4 offset:6144\n\t"
    "v_add_f32_e32 v55, v55, v23\n\t"
    "v_mfma_f32_16x16x32_bf16 v[68:71], v[164:167], v[56:59], v[68:71]\n\t"
    "v_exp_f32_e32 v24, v24\n\t"
    "v_mfma_f32_16x16x32_bf16 v[72:75], v[148:151], v[44:47], 0\n\t"
    "ds_read_b128 v[176:179], v5 offset:6144\n\t"
    "v_add_f32_e32 v54, v54, v24\n\t"
    "v_mfma_f32_16x16x32_bf16 v[72:75], v[144:147], v[40:43], v[72:75]\n\t"
    "v_exp_f32_e32 v25, v25\n\t"
    "v_mfma_f32_16x16x32_bf16 v[72:75], v[140:143], v[56:59], v[72:75]\n\t"
    "ds_read_b128 v[208:211], v3 offset:0\n\t"
    "v_add_f32_e32 v54, v54, v25\n\t"
    "v_mfma_f32_16x16x32_bf16 v[44:47], a[148:151], v[220:223], 0\n\t"
    "v_fma_f32 v26, v26, s68, -v52\n\t"
    "v_exp_f32_e32 v26, v26\n\t"
    "v_mfma_f32_16x16x32_bf16 v[44:47], a[156:159], v[216:219], v[44:47]\n\t"
    "buffer_load_dwordx4 v7, s[52:55], s61 offen offset:1024 nt lds\n\t"
    "ds_read_b128 v[204:207], v4 offset:0\n\t"
    "v_mfma_f32_16x16x32_bf16 v[44:47], a[164:167], v[212:215], v[44:47]\n\t"
    "v_add_f32_e32 v55, v55, v26\n\t"
    "v_fma_f32 v27, v27, s68, -v52\n\t"
    "v_mfma_f32_16x16x32_bf16 v[44:47], a[172:175], v[28:31], v[44:47]\n\t"
    "ds_read_b128 v[200:203], v5 offset:0\n\t"
    "v_cvt_pk_bf16_f32 v40, v60, v61\n\t"
    "v_mfma_f32_16x16x32_bf16 v[44:47], a[180:183], v[32:35], v[44:47]\n\t"
    "v_exp_f32_e32 v27, v27\n\t"
    "v_mfma_f32_16x16x32_bf16 v[44:47], a[188:191], v[36:39], v[44:47]\n\t"
    "ds_read_b128 v[160:163], v3 offset:12288\n\t"
    "v_add_f32_e32 v55, v55, v27\n\t"
    "v_mfma_f32_16x16x32_bf16 v[56:59], a[192:195], v[220:223], 0\n\t"
    "v_add_f32_e32 v54, v54, v55\n\t"
    "v_mov_b32_e32 v55, v54\n\t"
    "v_mfma_f32_16x16x32_bf16 v[56:59], a[200:203], v[216:219], v[56:59]\n\t"
    "ds_read_b128 v[156:159], v4 offset:12288\n\t"
    "v_cvt_pk_bf16_f32 v41, v62, v63\n\t"
    "v_mfma_f32_16x16x32_bf16 v[56:59], a[208:211], v[212:215], v[56:59]\n\t"
    "v_permlane32_swap_b32 v54, v55\n\t"
    "v_add_f32_e32 v54, v54, v55\n\t"
    "v_mfma_f32_16x16x32_bf16 v[56:59], a[216:219], v[28:31], v[56:59]\n\t"
    "ds_read_b128 v[152:155], v5 offset:12288\n\t"
    "v_cvt_pk_bf16_f32 v42, v44, v45\n\t"
    "v_mfma_f32_16x16x32_bf16 v[56:59], a[224:227], v[32:35], v[56:59]\n\t"
    "v_cvt_pk_bf16_f32 v43, v46, v47\n\t"
    "v_mov_b32_e32 v55, v54\n\t"
    "v_mfma_f32_16x16x32_bf16 v[56:59], a[232:235], v[36:39], v[56:59]\n\t"
    "ds_read_b128 v[136:139], v3 offset:18432\n\t"
    "v_mfma_f32_16x16x32_bf16 v[60:63], a[196:199], v[220:223], 0\n\t"
    "v_permlane16_swap_b32 v54, v55\n\t"
    "v_add_f32_e32 v54, v54, v55\n\t"
    "v_mfma_f32_16x16x32_bf16 v[60:63], a[204:207], v[216:219], v[60:63]\n\t"
    "ds_read_b128 v[132:135], v4 offset:18432\n\t"
    "v_mfma_f32_16x16x32_bf16 v[60:63], a[212:215], v[212:215], v[60:63]\n\t"
    "v_rcp_f32_e32 v54, v54\n\t"
    "v_mfma_f32_16x16x32_bf16 v[60:63], a[220:223], v[28:31], v[60:63]\n\t"
    "ds_read_b128 v[128:131], v5 offset:18432\n\t"
    "v_cvt_pk_bf16_f32 v44, v56, v57\n\t"
    "v_mfma_f32_16x16x32_bf16 v[60:63], a[228:231], v[32:35], v[60:63]\n\t"
    "v_cvt_pk_bf16_f32 v45, v58, v59\n\t"
    "v_mul_f32_e32 v12, v12, v54\n\t"
    "v_mfma_f32_16x16x32_bf16 v[60:63], a[236:239], v[36:39], v[60:63]\n\t"
    "buffer_load_dwordx4 v7, s[52:55], s61 offen offset:2048 nt lds\n\t"
    "v_mul_f32_e32 v13, v13, v54\n\t"
    "v_cvt_pk_bf16_f32 v76, v12, v13\n\t"
    "v_mfma_f32_16x16x32_bf16 v[80:83], a[240:243], v[220:223], 0\n\t"
    "s_add_u32 s61, s61, 196608\n\t"
    "v_mul_f32_e32 v14, v14, v54\n\t"
    "v_mfma_f32_16x16x32_bf16 v[80:83], a[248:251], v[216:219], v[80:83]\n\t"
    "v_mul_f32_e32 v15, v15, v54\n\t"
    "v_cvt_pk_bf16_f32 v77, v14, v15\n\t"
    "v_mfma_f32_16x16x32_bf16 v[80:83], v[224:227], v[212:215], v[80:83]\n\t"
    "v_mul_f32_e32 v16, v16, v54\n\t"
    "v_mul_f32_e32 v17, v17, v54\n\t"
    "v_mfma_f32_16x16x32_bf16 v[80:83], v[232:235], v[28:31], v[80:83]\n\t"
    "v_cvt_pk_bf16_f32 v46, v60, v61\n\t"
    "v_cvt_pk_bf16_f32 v47, v62, v63\n\t"
    "v_mfma_f32_16x16x32_bf16 v[80:83], v[240:243], v[32:35], v[80:83]\n\t"
    "v_cvt_pk_bf16_f32 v78, v16, v17\n\t"
    "v_mul_f32_e32 v18, v18, v54\n\t"
    "v_mfma_f32_16x16x32_bf16 v[80:83], v[248:251], v[36:39], v[80:83]\n\t"
    "v_mul_f32_e32 v19, v19, v54\n\t"
    "v_cvt_pk_bf16_f32 v79, v18, v19\n\t"
    "v_mfma_f32_16x16x32_bf16 v[12:15], a[244:247], v[220:223], 0\n\t"
    "v_mul_f32_e32 v20, v20, v54\n\t"
    "v_mul_f32_e32 v21, v21, v54\n\t"
    "v_mfma_f32_16x16x32_bf16 v[12:15], a[252:255], v[216:219], v[12:15]\n\t"
    "v_cvt_pk_bf16_f32 v56, v20, v21\n\t"
    "v_mul_f32_e32 v22, v22, v54\n\t"
    "v_mfma_f32_16x16x32_bf16 v[12:15], v[228:231], v[212:215], v[12:15]\n\t"
    "v_mul_f32_e32 v23, v23, v54\n\t"
    "v_cvt_pk_bf16_f32 v57, v22, v23\n\t"
    "v_mfma_f32_16x16x32_bf16 v[12:15], v[236:239], v[28:31], v[12:15]\n\t"
    "v_cvt_pk_bf16_f32 v16, v80, v81\n\t"
    "v_cvt_pk_bf16_f32 v17, v82, v83\n\t"
    "v_mfma_f32_16x16x32_bf16 v[12:15], v[244:247], v[32:35], v[12:15]\n\t"
    "v_mul_f32_e32 v24, v24, v54\n\t"
    "v_mul_f32_e32 v25, v25, v54\n\t"
    "v_mfma_f32_16x16x32_bf16 v[12:15], v[252:255], v[36:39], v[12:15]\n\t"
    "v_cvt_pk_bf16_f32 v58, v24, v25\n\t"
    "v_mul_f32_e32 v26, v26, v54\n\t"
    "v_mfma_f32_16x16x32_bf16 v[20:23], a[0:3], v[196:199], 0\n\t"
    "ds_write_b128 v6, v[76:79] offset:6144\n\t"
    "v_mfma_f32_16x16x32_bf16 v[20:23], a[8:11], v[192:195], v[20:23]\n\t"
    "v_mul_f32_e32 v27, v27, v54\n\t"
    "v_cvt_pk_bf16_f32 v59, v26, v27\n\t"
    "v_mfma_f32_16x16x32_bf16 v[20:23], a[16:19], v[188:191], v[20:23]\n\t"
    "s_add_u32 m0, s63, 12288\n\t"
    "s_waitcnt lgkmcnt(1)\n\t"
    "v_mfma_f32_16x16x32_bf16 v[20:23], a[24:27], v[184:187], v[20:23]\n\t"
    "buffer_load_dwordx4 v7, s[52:55], s61 offen offset:0 nt lds\n\t"
    "v_cvt_pk_bf16_f32 v18, v12, v13\n\t"
    "v_mfma_f32_16x16x32_bf16 v[20:23], a[32:35], v[180:183], v[20:23]\n\t"
    "ds_write_b128 v6, v[56:59] offset:7168\n\t"
    "v_mfma_f32_16x16x32_bf16 v[20:23], a[40:43], v[176:179], v[20:23]\n\t"
    "v_cvt_pk_bf16_f32 v19, v14, v15\n\t"
    "v_mfma_f32_16x16x32_bf16 v[64:67], v[208:211], v[40:43], v[64:67]\n\t"
    "v_mfma_f32_16x16x32_bf16 v[64:67], v[204:207], v[44:47], v[64:67]\n\t"
    "v_mfma_f32_16x16x32_bf16 v[64:67], v[200:203], v[16:19], v[64:67]\n\t"
    "v_mfma_f32_16x16x32_bf16 v[48:51], v[184:187], v[40:43], v[48:51]\n\t"
    "v_cvt_pk_bf16_f32 v12, v20, v21\n\t"
    "v_cvt_pk_bf16_f32 v13, v22, v23\n\t"
    "v_mfma_f32_16x16x32_bf16 v[48:51], v[180:183], v[44:47], v[48:51]\n\t"
    "s_waitcnt lgkmcnt(0)\n\t"
    "s_barrier \n\t"
    "v_mfma_f32_16x16x32_bf16 v[48:51], v[176:179], v[16:19], v[48:51]\n\t"
    "v_mfma_f32_16x16x32_bf16 v[68:71], v[160:163], v[40:43], v[68:71]\n\t"
    "v_max3_f32 v20, v64, v65, v66\n\t"
    "v_mfma_f32_16x16x32_bf16 v[68:71], v[156:159], v[44:47], v[68:71]\n\t"
    "v_mfma_f32_16x16x32_bf16 v[68:71], v[152:155], v[16:19], v[68:71]\n\t"
    "v_mfma_f32_16x16x32_bf16 v[72:75], v[136:139], v[40:43], v[72:75]\n\t"
    "v_max3_f32 v21, v67, v48, v49\n\t"
    "v_max3_f32 v20, v20, v50, v51\n\t"
    "v_mfma_f32_16x16x32_bf16 v[72:75], v[132:135], v[44:47], v[72:75]\n\t"
    "v_mfma_f32_16x16x32_bf16 v[72:75], v[128:131], v[16:19], v[72:75]\n\t"
    "v_mfma_f32_16x16x32_bf16 v[24:27], a[4:7], v[196:199], 0\n\t"
    "v_max3_f32 v21, v21, v68, v69\n\t"
    "v_max3_f32 v20, v20, v70, v71\n\t"
    "v_mfma_f32_16x16x32_bf16 v[24:27], a[12:15], v[192:195], v[24:27]\n\t"
    "buffer_load_dwordx4 v7, s[52:55], s61 offen offset:1024 nt lds\n\t"
    "v_mfma_f32_16x16x32_bf16 v[24:27], a[20:23], v[188:191], v[24:27]\n\t"
    "v_mfma_f32_16x16x32_bf16 v[24:27], a[28:31], v[184:187], v[24:27]\n\t"
    "v_max3_f32 v21, v21, v72, v73\n\t"
    "v_max3_f32 v20, v20, v74, v75\n\t"
    "v_mfma_f32_16x16x32_bf16 v[24:27], a[36:39], v[180:183], v[24:27]\n\t"
    "v_max_f32_e32 v20, v20, v21\n\t"
    "v_mov_b32_e32 v21, v20\n\t"
    "v_mfma_f32_16x16x32_bf16 v[24:27], a[44:47], v[176:179], v[24:27]\n\t"
    "v_mfma_f32_16x16x32_bf16 v[16:19], a[48:51], v[196:199], 0\n\t"
    "v_permlane32_swap_b32 v20, v21\n\t"
    "v_max_f32_e32 v20, v20, v21\n\t"
    "v_mfma_f32_16x16x32_bf16 v[16:19], a[56:59], v[192:195], v[16:19]\n\t"
    "v_mov_b32_e32 v21, v20\n\t"
    "v_mfma_f32_16x16x32_bf16 v[16:19], a[64:67], v[188:191], v[16:19]\n\t"
    "v_mfma_f32_16x16x32_bf16 v[16:19], a[72:75], v[184:187], v[16:19]\n\t"
    "v_cvt_pk_bf16_f32 v14, v24, v25\n\t"
    "v_cvt_pk_bf16_f32 v15, v26, v27\n\t"
    "v_mfma_f32_16x16x32_bf16 v[16:19], a[80:83], v[180:183], v[16:19]\n\t"
    "v_permlane16_swap_b32 v20, v21\n\t"
    "v_max_f32_e32 v20, v20, v21\n\t"
    "v_mfma_f32_16x16x32_bf16 v[16:19], a[88:91], v[176:179], v[16:19]\n\t"
    "v_mul_f32_e32 v20, s68, v20\n\t"
    "v_fma_f32 v64, v64, s68, -v20\n\t"
    "v_mfma_f32_16x16x32_bf16 v[24:27], a[52:55], v[196:199], 0\n\t"
    "v_exp_f32_e32 v64, v64\n\t"
    "v_mfma_f32_16x16x32_bf16 v[24:27], a[60:63], v[192:195], v[24:27]\n\t"
    "v_fma_f32 v65, v65, s68, -v20\n\t"
    "v_fma_f32 v66, v66, s68, -v20\n\t"
    "v_mfma_f32_16x16x32_bf16 v[24:27], a[68:71], v[188:191], v[24:27]\n\t"
    "v_exp_f32_e32 v65, v65\n\t"
    "v_mfma_f32_16x16x32_bf16 v[24:27], a[76:79], v[184:187], v[24:27]\n\t"
    "v_cvt_pk_bf16_f32 v28, v16, v17\n\t"
    "v_cvt_pk_bf16_f32 v29, v18, v19\n\t"
    "v_mfma_f32_16x16x32_bf16 v[24:27], a[84:87], v[180:183], v[24:27]\n\t"
    "v_add_f32_e32 v22, v64, v65\n\t"
    "v_fma_f32 v67, v67, s68, -v20\n\t"
    "v_mfma_f32_16x16x32_bf16 v[24:27], a[92:95], v[176:179], v[24:27]\n\t"
    "buffer_load_dwordx4 v7, s[52:55], s61 offen offset:2048 nt lds\n\t"
    "v_fma_f32 v48, v48, s68, -v20\n\t"
    "v_mfma_f32_16x16x32_bf16 v[16:19], a[96:99], v[196:199], 0\n\t"
    "s_add_u32 s61, s61, 196608\n\t"
    "v_fma_f32 v49, v49, s68, -v20\n\t"
    "v_mfma_f32_16x16x32_bf16 v[16:19], a[104:107], v[192:195], v[16:19]\n\t"
    "v_exp_f32_e32 v66, v66\n\t"
    "v_mfma_f32_16x16x32_bf16 v[16:19], a[112:115], v[188:191], v[16:19]\n\t"
    "v_exp_f32_e32 v67, v67\n\t"
    "v_mfma_f32_16x16x32_bf16 v[16:19], a[120:123], v[184:187], v[16:19]\n\t"
    "v_cvt_pk_bf16_f32 v30, v24, v25\n\t"
    "v_cvt_pk_bf16_f32 v31, v26, v27\n\t"
    "v_mfma_f32_16x16x32_bf16 v[16:19], a[128:131], v[180:183], v[16:19]\n\t"
    "v_add_f32_e32 v23, v66, v67\n\t"
    "v_fma_f32 v50, v50, s68, -v20\n\t"
    "v_mfma_f32_16x16x32_bf16 v[16:19], a[136:139], v[176:179], v[16:19]\n\t"
    "v_exp_f32_e32 v48, v48\n\t"
    "v_mfma_f32_16x16x32_bf16 v[24:27], a[100:103], v[196:199], 0\n\t"
    "v_add_f32_e32 v22, v22, v48\n\t"
    "v_fma_f32 v51, v51, s68, -v20\n\t"
    "v_mfma_f32_16x16x32_bf16 v[24:27], a[108:111], v[192:195], v[24:27]\n\t"
    "v_exp_f32_e32 v49, v49\n\t"
    "v_mfma_f32_16x16x32_bf16 v[24:27], a[116:119], v[188:191], v[24:27]\n\t"
    "v_add_f32_e32 v22, v22, v49\n\t"
    "v_fma_f32 v68, v68, s68, -v20\n\t"
    "v_mfma_f32_16x16x32_bf16 v[24:27], a[124:127], v[184:187], v[24:27]\n\t"
    "v_cvt_pk_bf16_f32 v32, v16, v17\n\t"
    "v_cvt_pk_bf16_f32 v33, v18, v19\n\t"
    "v_mfma_f32_16x16x32_bf16 v[24:27], a[132:135], v[180:183], v[24:27]\n\t"
    "v_exp_f32_e32 v50, v50\n\t"
    "v_mfma_f32_16x16x32_bf16 v[24:27], a[140:143], v[176:179], v[24:27]\n\t"
    "v_add_f32_e32 v23, v23, v50\n\t"
    "v_fma_f32 v69, v69, s68, -v20\n\t"
    "v_mfma_f32_16x16x32_bf16 v[16:19], a[144:147], v[196:199], 0\n\t"
    "v_exp_f32_e32 v51, v51\n\t"
    "v_mfma_f32_16x16x32_bf16 v[16:19], a[152:155], v[192:195], v[16:19]\n\t"
    "v_add_f32_e32 v23, v23, v51\n\t"
    "v_fma_f32 v70, v70, s68, -v20\n\t"
    "v_mfma_f32_16x16x32_bf16 v[16:19], a[160:163], v[188:191], v[16:19]\n\t"
    "s_add_u32 m0, s63, 15360\n\t"
    "v_fma_f32 v71, v71, s68, -v20\n\t"
    "v_mfma_f32_16x16x32_bf16 v[16:19], a[168:171], v[184:187], v[16:19]\n\t"
    "buffer_load_dwordx4 v7, s[52:55], s61 offen offset:0 nt lds\n\t"
    "v_cvt_pk_bf16_f32 v34, v24, v25\n\t"
    "v_mfma_f32_16x16x32_bf16 v[16:19], a[176:179], v[180:183], v[16:19]\n\t"
    "v_cvt_pk_bf16_f32 v35, v26, v27\n\t"
    "v_fma_f32 v72, v72, s68, -v20\n\t"
    "v_mfma_f32_16x16x32_bf16 v[16:19], a[184:187], v[176:179], v[16:19]\n\t"
    "v_exp_f32_e32 v68, v68\n\t"
    "v_mfma_f32_16x16x32_bf16 v[36:39], v[220:223], v[12:15], 0\n\t"
    "v_add_f32_e32 v22, v22, v68\n\t"
    "v_fma_f32 v73, v73, s68, -v20\n\t"
    "v_mfma_f32_16x16x32_bf16 v[36:39], v[216:219], v[28:31], v[36:39]\n\t"
    "v_exp_f32_e32 v69, v69\n\t"
    "v_mfma_f32_16x16x32_bf16 v[36:39], v[212:215], v[32:35], v[36:39]\n\t"
    "v_add_f32_e32 v22, v22, v69\n\t"
    "v_fma_f32 v74, v74, s68, -v20\n\t"
    "v_mfma_f32_16x16x32_bf16 v[24:27], v[196:199], v[12:15], 0\n\t"
    "v_exp_f32_e32 v70, v70\n\t"
    "v_mfma_f32_16x16x32_bf16 v[24:27], v[192:195], v[28:31], v[24:27]\n\t"
    "v_add_f32_e32 v23, v23, v70\n\t"
    "v_fma_f32 v75, v75, s68, -v20\n\t"
    "v_mfma_f32_16x16x32_bf16 v[24:27], v[188:191], v[32:35], v[24:27]\n\t"
    "v_exp_f32_e32 v71, v71\n\t"
    "v_mfma_f32_16x16x32_bf16 v[40:43], v[172:175], v[12:15], 0\n\t"
    "v_add_f32_e32 v23, v23, v71\n\t"
    "v_cvt_pk_bf16_f32 v44, v16, v17\n\t"
    "v_mfma_f32_16x16x32_bf16 v[40:43], v[168:171], v[28:31], v[40:43]\n\t"
    "v_exp_f32_e32 v72, v72\n\t"
    "v_mfma_f32_16x16x32_bf16 v[40:43], v[164:167], v[32:35], v[40:43]\n\t"
    "v_add_f32_e32 v22, v22, v72\n\t"
    "v_cvt_pk_bf16_f32 v45, v18, v19\n\t"
    "v_mfma_f32_16x16x32_bf16 v[52:55], v[148:151], v[12:15], 0\n\t"
    "v_exp_f32_e32 v73, v73\n\t"
    "v_mfma_f32_16x16x32_bf16 v[52:55], v[144:147], v[28:31], v[52:55]\n\t"
    "v_add_f32_e32 v22, v22, v73\n\t"
    "v_mfma_f32_16x16x32_bf16 v[52:55], v[140:143], v[32:35], v[52:55]\n\t"
    "v_exp_f32_e32 v74, v74\n\t"
    "v_mfma_f32_16x16x32_bf16 v[12:15], a[148:151], v[196:199], 0\n\t"
    "v_add_f32_e32 v23, v23, v74\n\t"
    "v_mfma_f32_16x16x32_bf16 v[12:15], a[156:159], v[192:195], v[12:15]\n\t"
    "buffer_load_dwordx4 v7, s[52:55], s61 offen offset:1024 nt lds\n\t"
    "v_mfma_f32_16x16x32_bf16 v[12:15], a[164:167], v[188:191], v[12:15]\n\t"
    "v_exp_f32_e32 v75, v75\n\t"
    "v_mfma_f32_16x16x32_bf16 v[12:15], a[172:175], v[184:187], v[12:15]\n\t"
    "v_add_f32_e32 v23, v23, v75\n\t"
    "v_add_f32_e32 v22, v22, v23\n\t"
    "v_mfma_f32_16x16x32_bf16 v[12:15], a[180:183], v[180:183], v[12:15]\n\t"
    "v_mov_b32_e32 v23, v22\n\t"
    "v_mfma_f32_16x16x32_bf16 v[12:15], a[188:191], v[176:179], v[12:15]\n\t"
    "v_mfma_f32_16x16x32_bf16 v[16:19], a[192:195], v[196:199], 0\n\t"
    "v_permlane32_swap_b32 v22, v23\n\t"
    "v_add_f32_e32 v22, v22, v23\n\t"
    "v_mfma_f32_16x16x32_bf16 v[16:19], a[200:203], v[192:195], v[16:19]\n\t"
    "v_mov_b32_e32 v23, v22\n\t"
    "v_mfma_f32_16x16x32_bf16 v[16:19], a[208:211], v[188:191], v[16:19]\n\t"
    "v_mfma_f32_16x16x32_bf16 v[16:19], a[216:219], v[184:187], v[16:19]\n\t"
    "v_cvt_pk_bf16_f32 v46, v12, v13\n\t"
    "v_cvt_pk_bf16_f32 v47, v14, v15\n\t"
    "v_mfma_f32_16x16x32_bf16 v[16:19], a[224:227], v[180:183], v[16:19]\n\t"
    "v_permlane16_swap_b32 v22, v23\n\t"
    "v_add_f32_e32 v22, v22, v23\n\t"
    "v_mfma_f32_16x16x32_bf16 v[16:19], a[232:235], v[176:179], v[16:19]\n\t"
    "v_rcp_f32_e32 v22, v22\n\t"
    "v_mfma_f32_16x16x32_bf16 v[12:15], a[196:199], v[196:199], 0\n\t"
    "v_mul_f32_e32 v64, v64, v22\n\t"
    "v_mul_f32_e32 v65, v65, v22\n\t"
    "v_mfma_f32_16x16x32_bf16 v[12:15], a[204:207], v[192:195], v[12:15]\n\t"
    "v_cvt_pk_bf16_f32 v28, v64, v65\n\t"
    "v_mul_f32_e32 v66, v66, v22\n\t"
    "v_mfma_f32_16x16x32_bf16 v[12:15], a[212:215], v[188:191], v[12:15]\n\t"
    "v_mul_f32_e32 v67, v67, v22\n\t"
    "v_cvt_pk_bf16_f32 v29, v66, v67\n\t"
    "v_mfma_f32_16x16x32_bf16 v[12:15], a[220:223], v[184:187], v[12:15]\n\t"
    "v_cvt_pk_bf16_f32 v32, v16, v17\n\t"
    "v_cvt_pk_bf16_f32 v33, v18, v19\n\t"
    "v_mfma_f32_16x16x32_bf16 v[12:15], a[228:231], v[180:183], v[12:15]\n\t"
    "v_mul_f32_e32 v48, v48, v22\n\t"
    "v_mul_f32_e32 v49, v49, v22\n\t"
    "v_mfma_f32_16x16x32_bf16 v[12:15], a[236:239], v[176:179], v[12:15]\n\t"
    "buffer_load_dwordx4 v7, s[52:55], s61 offen offset:2048 nt lds\n\t"
    "v_cvt_pk_bf16_f32 v30, v48, v49\n\t"
    "v_mfma_f32_16x16x32_bf16 v[16:19], a[240:243], v[196:199], 0\n\t"
    "s_add_u32 s61, s61, 196608\n\t"
    "v_mul_f32_e32 v50, v50, v22\n\t"
    "v_mfma_f32_16x16x32_bf16 v[16:19], a[248:251], v[192:195], v[16:19]\n\t"
    "v_mul_f32_e32 v51, v51, v22\n\t"
    "v_cvt_pk_bf16_f32 v31, v50, v51\n\t"
    "v_mfma_f32_16x16x32_bf16 v[16:19], v[224:227], v[188:191], v[16:19]\n\t"
    "v_mul_f32_e32 v68, v68, v22\n\t"
    "v_mul_f32_e32 v69, v69, v22\n\t"
    "v_mfma_f32_16x16x32_bf16 v[16:19], v[232:235], v[184:187], v[16:19]\n\t"
    "v_cvt_pk_bf16_f32 v34, v12, v13\n\t"
    "v_cvt_pk_bf16_f32 v35, v14, v15\n\t"
    "v_mfma_f32_16x16x32_bf16 v[16:19], v[240:243], v[180:183], v[16:19]\n\t"
    "v_cvt_pk_bf16_f32 v48, v68, v69\n\t"
    "v_mul_f32_e32 v70, v70, v22\n\t"
    "v_mfma_f32_16x16x32_bf16 v[16:19], v[248:251], v[176:179], v[16:19]\n\t"
    "v_mul_f32_e32 v71, v71, v22\n\t"
    "v_cvt_pk_bf16_f32 v49, v70, v71\n\t"
    "v_mfma_f32_16x16x32_bf16 v[12:15], a[244:247], v[196:199], 0\n\t"
    "v_mul_f32_e32 v72, v72, v22\n\t"
    "v_mul_f32_e32 v73, v73, v22\n\t"
    "v_mfma_f32_16x16x32_bf16 v[12:15], a[252:255], v[192:195], v[12:15]\n\t"
    "v_cvt_pk_bf16_f32 v50, v72, v73\n\t"
    "v_mul_f32_e32 v74, v74, v22\n\t"
    "v_mfma_f32_16x16x32_bf16 v[12:15], v[228:231], v[188:191], v[12:15]\n\t"
    "v_mul_f32_e32 v75, v75, v22\n\t"
    "v_cvt_pk_bf16_f32 v51, v74, v75\n\t"
    "v_mfma_f32_16x16x32_bf16 v[12:15], v[236:239], v[184:187], v[12:15]\n\t"
    "v_cvt_pk_bf16_f32 v56, v16, v17\n\t"
    "v_cvt_pk_bf16_f32 v57, v18, v19\n\t"
    "v_mfma_f32_16x16x32_bf16 v[12:15], v[244:247], v[180:183], v[12:15]\n\t"
    "v_mfma_f32_16x16x32_bf16 v[12:15], v[252:255], v[176:179], v[12:15]\n\t"
    "v_mfma_f32_16x16x32_bf16 v[16:19], a[0:3], v[172:175], 0\n\t"
    "ds_write_b128 v6, v[28:31] offset:0\n\t"
    "v_mfma_f32_16x16x32_bf16 v[16:19], a[8:11], v[168:171], v[16:19]\n\t"
    "v_mfma_f32_16x16x32_bf16 v[16:19], a[16:19], v[164:167], v[16:19]\n\t"
    "s_add_u32 m0, s63, 18432\n\t"
    "v_mfma_f32_16x16x32_bf16 v[16:19], a[24:27], v[160:163], v[16:19]\n\t"
    "buffer_load_dwordx4 v7, s[52:55], s61 offen offset:0 nt lds\n\t"
    "v_cvt_pk_bf16_f32 v58, v12, v13\n\t"
    "v_mfma_f32_16x16x32_bf16 v[16:19], a[32:35], v[156:159], v[16:19]\n\t"
    "ds_write_b128 v6, v[48:51] offset:1024\n\t"
    "v_mfma_f32_16x16x32_bf16 v[16:19], a[40:43], v[152:155], v[16:19]\n\t"
    "v_cvt_pk_bf16_f32 v59, v14, v15\n\t"
    "v_mfma_f32_16x16x32_bf16 v[36:39], v[208:211], v[44:47], v[36:39]\n\t"
    "v_mfma_f32_16x16x32_bf16 v[36:39], v[204:207], v[32:35], v[36:39]\n\t"
    "v_mfma_f32_16x16x32_bf16 v[36:39], v[200:203], v[56:59], v[36:39]\n\t"
    "s_add_i32 s64, s62, 24576\n\t"
    "v_mfma_f32_16x16x32_bf16 v[24:27], v[184:187], v[44:47], v[24:27]\n\t"
    "s_cmp_eq_u32 s64, %[rend]\n\t"
    "s_cselect_b32 s64, %[rbeg], s64\n\t"
    "v_mfma_f32_16x16x32_bf16 v[24:27], v[180:183], v[32:35], v[24:27]\n\t"
    "s_sub_i32 s65, s63, s64\n\t"
    "v_cvt_pk_bf16_f32 v12, v16, v17\n\t"
    "v_mfma_f32_16x16x32_bf16 v[24:27], v[176:179], v[56:59], v[24:27]\n\t"
    "v_add_u32_e32 v0, s65, v0\n\t"
    "v_add_u32_e32 v1, s65, v1\n\t"
    "v_mfma_f32_16x16x32_bf16 v[40:43], v[160:163], v[44:47], v[40:43]\n\t"
    "v_add_u32_e32 v2, s65, v2\n\t"
    "v_add_u32_e32 v3, s65, v3\n\t"
    "v_mfma_f32_16x16x32_bf16 v[40:43], v[156:159], v[32:35], v[40:43]\n\t"
    "v_add_u32_e32 v4, s65, v4\n\t"
    "v_add_u32_e32 v5, s65, v5\n\t"
    "v_mfma_f32_16x16x32_bf16 v[40:43], v[152:155], v[56:59], v[40:43]\n\t"
    "v_max3_f32 v20, v36, v37, v38\n\t"
    "v_cvt_pk_bf16_f32 v13, v18, v19\n\t"
    "v_mfma_f32_16x16x32_bf16 v[52:55], v[136:139], v[44:47], v[52:55]\n\t"
    "v_max3_f32 v21, v39, v24, v25\n\t"
    "v_max3_f32 v20, v20, v26, v27\n\t"
    "v_mfma_f32_16x16x32_bf16 v[52:55], v[132:135], v[32:35], v[52:55]\n\t"
    "v_mfma_f32_16x16x32_bf16 v[52:55], v[128:131], v[56:59], v[52:55]\n\t"
    "v_mfma_f32_16x16x32_bf16 v[16:19], a[4:7], v[172:175], 0\n\t"
    "v_max3_f32 v21, v21, v40, v41\n\t"
    "v_max3_f32 v20, v20, v42, v43\n\t"
    "v_mfma_f32_16x16x32_bf16 v[16:19], a[12:15], v[168:171], v[16:19]\n\t"
    "buffer_load_dwordx4 v7, s[52:55], s61 offen offset:1024 nt lds\n\t"
    "v_mfma_f32_16x16x32_bf16 v[16:19], a[20:23], v[164:167], v[16:19]\n\t"
    "v_mfma_f32_16x16x32_bf16 v[16:19], a[28:31], v[160:163], v[16:19]\n\t"
    "v_max3_f32 v21, v21, v52, v53\n\t"
    "v_max3_f32 v20, v20, v54, v55\n\t"
    "v_mfma_f32_16x16x32_bf16 v[16:19], a[36:39], v[156:159], v[16:19]\n\t"
    "v_max_f32_e32 v20, v20, v21\n\t"
    "v_mov_b32_e32 v21, v20\n\t"
    "v_mfma_f32_16x16x32_bf16 v[16:19], a[44:47], v[152:155], v[16:19]\n\t"
    "v_mfma_f32_16x16x32_bf16 v[28:31], a[48:51], v[172:175], 0\n\t"
    "v_permlane32_swap_b32 v20, v21\n\t"
    "v_max_f32_e32 v20, v20, v21\n\t"
    "v_mfma_f32_16x16x32_bf16 v[28:31], a[56:59], v[168:171], v[28:31]\n\t"
    "v_mov_b32_e32 v21, v20\n\t"
    "v_mfma_f32_16x16x32_bf16 v[28:31], a[64:67], v[164:167], v[28:31]\n\t"
    "v_mfma_f32_16x16x32_bf16 v[28:31], a[72:75], v[160:163], v[28:31]\n\t"
    "v_cvt_pk_bf16_f32 v14, v16, v17\n\t"
    "v_cvt_pk_bf16_f32 v15, v18, v19\n\t"
    "v_mfma_f32_16x16x32_bf16 v[28:31], a[80:83], v[156:159], v[28:31]\n\t"
    "v_permlane16_swap_b32 v20, v21\n\t"
    "v_max_f32_e32 v20, v20, v21\n\t"
    "v_mfma_f32_16x16x32_bf16 v[28:31], a[88:91], v[152:155], v[28:31]\n\t"
    "v_mul_f32_e32 v20, s68, v20\n\t"
    "v_fma_f32 v36, v36, s68, -v20\n\t"
    "v_mfma_f32_16x16x32_bf16 v[16:19], a[52:55], v[172:175], 0\n\t"
    "v_exp_f32_e32 v36, v36\n\t"
    "v_mfma_f32_16x16x32_bf16 v[16:19], a[60:63], v[168:171], v[16:19]\n\t"
    "v_fma_f32 v37, v37, s68, -v20\n\t"
    "v_fma_f32 v38, v38, s68, -v20\n\t"
    "v_mfma_f32_16x16x32_bf16 v[16:19], a[68:71], v[164:167], v[16:19]\n\t"
    "v_exp_f32_e32 v37, v37\n\t"
    "v_mfma_f32_16x16x32_bf16 v[16:19], a[76:79], v[160:163], v[16:19]\n\t"
    "v_cvt_pk_bf16_f32 v32, v28, v29\n\t"
    "v_cvt_pk_bf16_f32 v33, v30, v31\n\t"
    "v_mfma_f32_16x16x32_bf16 v[16:19], a[84:87], v[156:159], v[16:19]\n\t"
    "v_add_f32_e32 v22, v36, v37\n\t"
    "v_fma_f32 v39, v39, s68, -v20\n\t"
    "v_mfma_f32_16x16x32_bf16 v[16:19], a[92:95], v[152:155], v[16:19]\n\t"
    "buffer_load_dwordx4 v7, s[52:55], s61 offen offset:2048 nt lds\n\t"
    "v_fma_f32 v24, v24, s68, -v20\n\t"
    "v_mfma_f32_16x16x32_bf16 v[28:31], a[96:99], v[172:175], 0\n\t"
    "s_add_u32 s61, s61, 196608\n\t"
    "v_fma_f32 v25, v25, s68, -v20\n\t"
    "v_mfma_f32_16x16x32_bf16 v[28:31], a[104:107], v[168:171], v[28:31]\n\t"
    "v_exp_f32_e32 v38, v38\n\t"
    "v_mfma_f32_16x16x32_bf16 v[28:31], a[112:115], v[164:167], v[28:31]\n\t"
    "v_exp_f32_e32 v39, v39\n\t"
    "v_mfma_f32_16x16x32_bf16 v[28:31], a[120:123], v[160:163], v[28:31]\n\t"
    "v_cvt_pk_bf16_f32 v34, v16, v17\n\t"
    "v_cvt_pk_bf16_f32 v35, v18, v19\n\t"
    "v_mfma_f32_16x16x32_bf16 v[28:31], a[128:131], v[156:159], v[28:31]\n\t"
    "v_add_f32_e32 v23, v38, v39\n\t"
    "v_fma_f32 v26, v26, s68, -v20\n\t"
    "v_mfma_f32_16x16x32_bf16 v[28:31], a[136:139], v[152:155], v[28:31]\n\t"
    "v_exp_f32_e32 v24, v24\n\t"
    "v_mfma_f32_16x16x32_bf16 v[16:19], a[100:103], v[172:175], 0\n\t"
    "v_add_f32_e32 v22, v22, v24\n\t"
    "v_fma_f32 v27, v27, s68, -v20\n\t"
    "v_mfma_f32_16x16x32_bf16 v[16:19], a[108:111], v[168:171], v[16:19]\n\t"
    "v_exp_f32_e32 v25, v25\n\t"
    "v_mfma_f32_16x16x32_bf16 v[16:19], a[116:119], v[164:167], v[16:19]\n\t"
    "v_add_f32_e32 v22, v22, v25\n\t"
    "v_fma_f32 v40, v40, s68, -v20\n\t"
    "v_mfma_f32_16x16x32_bf16 v[16:19], a[124:127], v[160:163], v[16:19]\n\t"
    "v_cvt_pk_bf16_f32 v44, v28, v29\n\t"
    "v_cvt_pk_bf16_f32 v45, v30, v31\n\t"
    "v_mfma_f32_16x16x32_bf16 v[16:19], a[132:135], v[156:159], v[16:19]\n\t"
    "v_exp_f32_e32 v26, v26\n\t"
    "v_mfma_f32_16x16x32_bf16 v[16:19], a[140:143], v[152:155], v[16:19]\n\t"
    "v_add_f32_e32 v23, v23, v26\n\t"
    "v_fma_f32 v41, v41, s68, -v20\n\t"
    "v_mfma_f32_16x16x32_bf16 v[28:31], a[144:147], v[172:175], 0\n\t"
    "v_exp_f32_e32 v27, v27\n\t"
    "v_mfma_f32_16x16x32_bf16 v[28:31], a[152:155], v[168:171], v[28:31]\n\t"
    "v_add_f32_e32 v23, v23, v27\n\t"
    "v_fma_f32 v42, v42, s68, -v20\n\t"
    "v_mfma_f32_16x16x32_bf16 v[28:31], a[160:163], v[164:167], v[28:31]\n\t"
    "s_add_u32 m0, s63, 21504\n\t"
    "v_fma_f32 v43, v43, s68, -v20\n\t"
    "v_mfma_f32_16x16x32_bf16 v[28:31], a[168:171], v[160:163], v[28:31]\n\t"
    "buffer_load_dwordx4 v7, s[52:55], s61 offen offset:0 nt lds\n\t"
    "v_cvt_pk_bf16_f32 v46, v16, v17\n\t"
    "v_mfma_f32_16x16x32_bf16 v[28:31], a[176:179], v[156:159], v[28:31]\n\t"
    "v_cvt_pk_bf16_f32 v47, v18, v19\n\t"
    "v_fma_f32 v52, v52, s68, -v20\n\t"
    "v_mfma_f32_16x16x32_bf16 v[28:31], a[184:187], v[152:155], v[28:31]\n\t"
    "v_exp_f32_e32 v40, v40\n\t"
    "v_mfma_f32_16x16x32_bf16 v[56:59], v[220:223], v[12:15], 0\n\t"
    "v_add_f32_e32 v22, v22, v40\n\t"
    "v_fma_f32 v53, v53, s68, -v20\n\t"
    "v_mfma_f32_16x16x32_bf16 v[56:59], v[216:219], v[32:35], v[56:59]\n\t"
    "v_exp_f32_e32 v41, v41\n\t"
    "v_mfma_f32_16x16x32_bf16 v[56:59], v[212:215], v[44:47], v[56:59]\n\t"
    "v_add_f32_e32 v22, v22, v41\n\t"
    "v_fma_f32 v54, v54, s68, -v20\n\t"
    "v_mfma_f32_16x16x32_bf16 v[60:63], v[196:199], v[12:15], 0\n\t"
    "v_exp_f32_e32 v42, v42\n\t"
    "v_mfma_f32_16x16x32_bf16 v[60:63], v[192:195], v[32:35], v[60:63]\n\t"
    "v_add_f32_e32 v23, v23, v42\n\t"
    "v_fma_f32 v55, v55, s68, -v20\n\t"
    "v_mfma_f32_16x16x32_bf16 v[60:63], v[188:191], v[44:47], v[60:63]\n\t"
    "v_exp_f32_e32 v43, v43\n\t"
    "v_mfma_f32_16x16x32_bf16 v[64:67], v[172:175], v[12:15], 0\n\t"
    "v_add_f32_e32 v23, v23, v43\n\t"
    "v_cvt_pk_bf16_f32 v16, v28, v29\n\t"
    "v_mfma_f32_16x16x32_bf16 v[64:67], v[168:171], v[32:35], v[64:67]\n\t"
    "v_exp_f32_e32 v52, v52\n\t"
    "v_mfma_f32_16x16x32_bf16 v[64:67], v[164:167], v[44:47], v[64:67]\n\t"
    "v_add_f32_e32 v22, v22, v52\n\t"
    "v_cvt_pk_bf16_f32 v17, v30, v31\n\t"
    "v_mfma_f32_16x16x32_bf16 v[68:71], v[148:151], v[12:15], 0\n\t"
    "v_exp_f32_e32 v53, v53\n\t"
    "v_mfma_f32_16x16x32_bf16 v[68:71], v[144:147], v[32:35], v[68:71]\n\t"
    "v_add_f32_e32 v22, v22, v53\n\t"
    "v_mfma_f32_16x16x32_bf16 v[68:71], v[140:143], v[44:47], v[68:71]\n\t"
    "v_exp_f32_e32 v54, v54\n\t"
    "v_mfma_f32_16x16x32_bf16 v[12:15], a[148:151], v[172:175], 0\n\t"
    "v_add_f32_e32 v23, v23, v54\n\t"
    "v_mfma_f32_16x16x32_bf16 v[12:15], a[156:159], v[168:171], v[12:15]\n\t"
    "buffer_load_dwordx4 v7, s[52:55], s61 offen offset:1024 nt lds\n\t"
    "v_mfma_f32_16x16x32_bf16 v[12:15], a[164:167], v[164:167], v[12:15]\n\t"
    "v_exp_f32_e32 v55, v55\n\t"
    "v_mfma_f32_16x16x32_bf16 v[12:15], a[172:175], v[160:163], v[12:15]\n\t"
    "v_add_f32_e32 v23, v23, v55\n\t"
    "v_add_f32_e32 v22, v22, v23\n\t"
    "v_mfma_f32_16x16x32_bf16 v[12:15], a[180:183], v[156:159], v[12:15]\n\t"
    "v_mov_b32_e32 v23, v22\n\t"
    "v_mfma_f32_16x16x32_bf16 v[12:15], a[188:191], v[152:155], v[12:15]\n\t"
    "v_mfma_f32_16x16x32_bf16 v[28:31], a[192:195], v[172:175], 0\n\t"
    "v_permlane32_swap_b32 v22, v23\n\t"
    "v_add_f32_e32 v22, v22, v23\n\t"
    "v_mfma_f32_16x16x32_bf16 v[28:31], a[200:203], v[168:171], v[28:31]\n\t"
    "v_mov_b32_e32 v23, v22\n\t"
    "v_mfma_f32_16x16x32_bf16 v[28:31], a[208:211], v[164:167], v[28:31]\n\t"
    "v_mfma_f32_16x16x32_bf16 v[28:31], a[216:219], v[160:163], v[28:31]\n\t"
    "v_cvt_pk_bf16_f32 v18, v12, v13\n\t"
    "v_cvt_pk_bf16_f32 v19, v14, v15\n\t"
    "v_mfma_f32_16x16x32_bf16 v[28:31], a[224:227], v[156:159], v[28:31]\n\t"
    "v_permlane16_swap_b32 v22, v23\n\t"
    "v_add_f32_e32 v22, v22, v23\n\t"
    "v_mfma_f32_16x16x32_bf16 v[28:31], a[232:235], v[152:155], v[28:31]\n\t"
    "v_rcp_f32_e32 v22, v22\n\t"
    "v_mfma_f32_16x16x32_bf16 v[12:15], a[196:199], v[172:175], 0\n\t"
    "v_mul_f32_e32 v36, v36, v22\n\t"
    "v_mul_f32_e32 v37, v37, v22\n\t"
    "v_mfma_f32_16x16x32_bf16 v[12:15], a[204:207], v[168:171], v[12:15]\n\t"
    "v_cvt_pk_bf16_f32 v32, v36, v37\n\t"
    "v_mul_f32_e32 v38, v38, v22\n\t"
    "v_mfma_f32_16x16x32_bf16 v[12:15], a[212:215], v[164:167], v[12:15]\n\t"
    "v_mul_f32_e32 v39, v39, v22\n\t"
    "v_cvt_pk_bf16_f32 v33, v38, v39\n\t"
    "v_mfma_f32_16x16x32_bf16 v[12:15], a[220:223], v[160:163], v[12:15]\n\t"
    "v_cvt_pk_bf16_f32 v44, v28, v29\n\t"
    "v_cvt_pk_bf16_f32 v45, v30, v31\n\t"
    "v_mfma_f32_16x16x32_bf16 v[12:15], a[228:231], v[156:159], v[12:15]\n\t"
    "v_mul_f32_e32 v24, v24, v22\n\t"
    "v_mul_f32_e32 v25, v25, v22\n\t"
    "v_mfma_f32_16x16x32_bf16 v[12:15], a[236:239], v[152:155], v[12:15]\n\t"
    "buffer_load_dwordx4 v7, s[52:55], s61 offen offset:2048 nt lds\n\t"
    "v_cvt_pk_bf16_f32 v34, v24, v25\n\t"
    "v_mfma_f32_16x16x32_bf16 v[28:31], a[240:243], v[172:175], 0\n\t"
    "s_add_u32 s61, s61, 11206656\n\t"
    "v_mul_f32_e32 v26, v26, v22\n\t"
    "v_mfma_f32_16x16x32_bf16 v[28:31], a[248:251], v[168:171], v[28:31]\n\t"
    "v_mul_f32_e32 v27, v27, v22\n\t"
    "v_cvt_pk_bf16_f32 v35, v26, v27\n\t"
    "v_mfma_f32_16x16x32_bf16 v[28:31], v[224:227], v[164:167], v[28:31]\n\t"
    "v_mul_f32_e32 v40, v40, v22\n\t"
    "v_mul_f32_e32 v41, v41, v22\n\t"
    "v_mfma_f32_16x16x32_bf16 v[28:31], v[232:235], v[160:163], v[28:31]\n\t"
    "v_cvt_pk_bf16_f32 v46, v12, v13\n\t"
    "v_cvt_pk_bf16_f32 v47, v14, v15\n\t"
    "v_mfma_f32_16x16x32_bf16 v[28:31], v[240:243], v[156:159], v[28:31]\n\t"
    "v_cvt_pk_bf16_f32 v24, v40, v41\n\t"
    "v_mul_f32_e32 v42, v42, v22\n\t"
    "v_mfma_f32_16x16x32_bf16 v[28:31], v[248:251], v[152:155], v[28:31]\n\t"
    "v_mul_f32_e32 v43, v43, v22\n\t"
    "v_cvt_pk_bf16_f32 v25, v42, v43\n\t"
    "v_mfma_f32_16x16x32_bf16 v[12:15], a[244:247], v[172:175], 0\n\t"
    "v_mul_f32_e32 v52, v52, v22\n\t"
    "v_mul_f32_e32 v53, v53, v22\n\t"
    "v_mfma_f32_16x16x32_bf16 v[12:15], a[252:255], v[168:171], v[12:15]\n\t"
    "v_cvt_pk_bf16_f32 v26, v52, v53\n\t"
    "v_mul_f32_e32 v54, v54, v22\n\t"
    "v_mfma_f32_16x16x32_bf16 v[12:15], v[228:231], v[164:167], v[12:15]\n\t"
    "v_mul_f32_e32 v55, v55, v22\n\t"
    "v_cvt_pk_bf16_f32 v27, v54, v55\n\t"
    "v_mfma_f32_16x16x32_bf16 v[12:15], v[236:239], v[160:163], v[12:15]\n\t"
    "v_cvt_pk_bf16_f32 v36, v28, v29\n\t"
    "v_cvt_pk_bf16_f32 v37, v30, v31\n\t"
    "v_mfma_f32_16x16x32_bf16 v[12:15], v[244:247], v[156:159], v[12:15]\n\t"
    "v_mfma_f32_16x16x32_bf16 v[12:15], v[252:255], v[152:155], v[12:15]\n\t"
    "v_mfma_f32_16x16x32_bf16 v[20:23], a[0:3], v[148:151], 0\n\t"
    "ds_write_b128 v6, v[32:35] offset:2048\n\t"
    "v_mfma_f32_16x16x32_bf16 v[20:23], a[8:11], v[144:147], v[20:23]\n\t"
    "v_mfma_f32_16x16x32_bf16 v[20:23], a[16:19], v[140:143], v[20:23]\n\t"
    "s_add_u32 m0, s62, 0\n\t"
    "v_mfma_f32_16x16x32_bf16 v[20:23], a[24:27], v[136:139], v[20:23]\n\t"
    "buffer_load_dwordx4 v7, s[52:55], s61 offen offset:0 nt lds\n\t"
    "v_cvt_pk_bf16_f32 v38, v12, v13\n\t"
    "v_mfma_f32_16x16x32_bf16 v[20:23], a[32:35], v[132:135], v[20:23]\n\t"
    "ds_write_b128 v6, v[24:27] offset:3072\n\t"
    "v_mfma_f32_16x16x32_bf16 v[20:23], a[40:43], v[128:131], v[20:23]\n\t"
    "v_cvt_pk_bf16_f32 v39, v14, v15\n\t"
    "v_mfma_f32_16x16x32_bf16 v[56:59], v[208:211], v[16:19], v[56:59]\n\t"
    "v_mfma_f32_16x16x32_bf16 v[56:59], v[204:207], v[44:47], v[56:59]\n\t"
    ".Lh6_%=:\n\t"
    "s_cbranch_execz .LrM_%=\n\t"
    "v_mfma_f32_16x16x32_bf16 v[56:59], v[200:203], v[36:39], v[56:59]\n\t"
    "v_mfma_f32_16x16x32_bf16 v[60:63], v[184:187], v[16:19], v[60:63]\n\t"
    "v_cvt_pk_bf16_f32 v32, v20, v21\n\t"
    "v_cvt_pk_bf16_f32 v33, v22, v23\n\t"
    "v_mfma_f32_16x16x32_bf16 v[60:63], v[180:183], v[44:47], v[60:63]\n\t"
    "v_mfma_f32_16x16x32_bf16 v[60:63], v[176:179], v[36:39], v[60:63]\n\t"
    "v_mfma_f32_16x16x32_bf16 v[64:67], v[160:163], v[16:19], v[64:67]\n\t"
    "v_max3_f32 v72, v56, v57, v58\n\t"
    "v_mfma_f32_16x16x32_bf16 v[64:67], v[156:159], v[44:47], v[64:67]\n\t"
    "v_mfma_f32_16x16x32_bf16 v[64:67], v[152:155], v[36:39], v[64:67]\n\t"
    "v_mfma_f32_16x16x32_bf16 v[68:71], v[136:139], v[16:19], v[68:71]\n\t"
    "v_max3_f32 v73, v59, v60, v61\n\t"
    "v_max3_f32 v72, v72, v62, v63\n\t"
    "v_mfma_f32_16x16x32_bf16 v[68:71], v[132:135], v[44:47], v[68:71]\n\t"
    "v_mfma_f32_16x16x32_bf16 v[68:71], v[128:131], v[36:39], v[68:71]\n\t"
    "v_mfma_f32_16x16x32_bf16 v[12:15], a[4:7], v[148:151], 0\n\t"
    "v_max3_f32 v73, v73, v64, v65\n\t"
    "v_max3_f32 v72, v72, v66, v67\n\t"
    "v_mfma_f32_16x16x32_bf16 v[12:15], a[12:15], v[144:147], v[12:15]\n\t"
    "buffer_load_dwordx4 v7, s[52:55], s61 offen offset:1024 nt lds\n\t"
    "v_mfma_f32_16x16x32_bf16 v[12:15], a[20:23], v[140:143], v[12:15]\n\t"
    "v_mfma_f32_16x16x32_bf16 v[12:15], a[28:31], v[136:139], v[12:15]\n\t"
    "v_max3_f32 v73, v73, v68, v69\n\t"
    "v_max3_f32 v72, v72, v70, v71\n\t"
    "v_mfma_f32_16x16x32_bf16 v[12:15], a[36:39], v[132:135], v[12:15]\n\t"
    "v_max_f32_e32 v72, v72, v73\n\t"
    "v_mov_b32_e32 v73, v72\n\t"
    "v_mfma_f32_16x16x32_bf16 v[12:15], a[44:47], v[128:131], v[12:15]\n\t"
    "v_mfma_f32_16x16x32_bf16 v[16:19], a[48:51], v[148:151], 0\n\t"
    "v_permlane32_swap_b32 v72, v73\n\t"
    "v_max_f32_e32 v72, v72, v73\n\t"
    "v_mfma_f32_16x16x32_bf16 v[16:19], a[56:59], v[144:147], v[16:19]\n\t"
    "v_mov_b32_e32 v73, v72\n\t"
    "v_mfma_f32_16x16x32_bf16 v[16:19], a[64:67], v[140:143], v[16:19]\n\t"
    "v_mfma_f32_16x16x32_bf16 v[16:19], a[72:75], v[136:139], v[16:19]\n\t"
    "v_cvt_pk_bf16_f32 v34, v12, v13\n\t"
    "v_cvt_pk_bf16_f32 v35, v14, v15\n\t"
    "v_mfma_f32_16x16x32_bf16 v[16:19], a[80:83], v[132:135], v[16:19]\n\t"
    "v_permlane16_swap_b32 v72, v73\n\t"
    "v_max_f32_e32 v72, v72, v73\n\t"
    "v_mfma_f32_16x16x32_bf16 v[16:19], a[88:91], v[128:131], v[16:19]\n\t"
    "v_mul_f32_e32 v72, s68, v72\n\t"
    "v_fma_f32 v56, v56, s68, -v72\n\t"
    "v_mfma_f32_16x16x32_bf16 v[12:15], a[52:55], v[148:151], 0\n\t"
    "v_exp_f32_e32 v56, v56\n\t"
    "v_mfma_f32_16x16x32_bf16 v[12:15], a[60:63], v[144:147], v[12:15]\n\t"
    "v_fma_f32 v57, v57, s68, -v72\n\t"
    "v_fma_f32 v58, v58, s68, -v72\n\t"
    "v_mfma_f32_16x16x32_bf16 v[12:15], a[68:71], v[140:143], v[12:15]\n\t"
    "v_exp_f32_e32 v57, v57\n\t"
    "v_mfma_f32_16x16x32_bf16 v[12:15], a[76:79], v[136:139], v[12:15]\n\t"
    "v_cvt_pk_bf16_f32 v36, v16, v17\n\t"
    "v_cvt_pk_bf16_f32 v37, v18, v19\n\t"
    "v_mfma_f32_16x16x32_bf16 v[12:15], a[84:87], v[132:135], v[12:15]\n\t"
    "v_add_f32_e32 v74, v56, v57\n\t"
    "v_fma_f32 v59, v59, s68, -v72\n\t"
    "v_mfma_f32_16x16x32_bf16 v[12:15], a[92:95], v[128:131], v[12:15]\n\t"
    "buffer_load_dwordx4 v7, s[52:55], s61 offen offset:2048 nt lds\n\t"
    "v_fma_f32 v60, v60, s68, -v72\n\t"
    "v_mfma_f32_16x16x32_bf16 v[16:19], a[96:99], v[148:151], 0\n\t"
    "s_add_u32 s61, s61, 196608\n\t"
    "v_fma_f32 v61, v61, s68, -v72\n\t"
    "v_mfma_f32_16x16x32_bf16 v[16:19], a[104:107], v[144:147], v[16:19]\n\t"
    "v_exp_f32_e32 v58, v58\n\t"
    "v_mfma_f32_16x16x32_bf16 v[16:19], a[112:115], v[140:143], v[16:19]\n\t"
    "v_exp_f32_e32 v59, v59\n\t"
    "v_mfma_f32_16x16x32_bf16 v[16:19], a[120:123], v[136:139], v[16:19]\n\t"
    "v_cvt_pk_bf16_f32 v38, v12, v13\n\t"
    "v_cvt_pk_bf16_f32 v39, v14, v15\n\t"
    "v_mfma_f32_16x16x32_bf16 v[16:19], a[128:131], v[132:135], v[16:19]\n\t"
    "v_add_f32_e32 v75, v58, v59\n\t"
    "v_fma_f32 v62, v62, s68, -v72\n\t"
    "v_mfma_f32_16x16x32_bf16 v[16:19], a[136:139], v[128:131], v[16:19]\n\t"
    "v_exp_f32_e32 v60, v60\n\t"
    "v_mfma_f32_16x16x32_bf16 v[20:23], a[100:103], v[148:151], 0\n\t"
    "v_add_f32_e32 v74, v74, v60\n\t"
    "v_fma_f32 v63, v63, s68, -v72\n\t"
    "v_mfma_f32_16x16x32_bf16 v[20:23], a[108:111], v[144:147], v[20:23]\n\t"
    "v_exp_f32_e32 v61, v61\n\t"
    "v_mfma_f32_16x16x32_bf16 v[20:23], a[116:119], v[140:143], v[20:23]\n\t"
    "v_add_f32_e32 v74, v74, v61\n\t"
    "v_fma_f32 v64, v64, s68, -v72\n\t"
    "v_mfma_f32_16x16x32_bf16 v[20:23], a[124:127], v[136:139], v[20:23]\n\t"
    "v_cvt_pk_bf16_f32 v40, v16, v17\n\t"
    "v_cvt_pk_bf16_f32 v41, v18, v19\n\t"
    "v_mfma_f32_16x16x32_bf16 v[20:23], a[132:135], v[132:135], v[20:23]\n\t"
    "v_exp_f32_e32 v62, v62\n\t"
    "v_mfma_f32_16x16x32_bf16 v[20:23], a[140:143], v[128:131], v[20:23]\n\t"
    "v_add_f32_e32 v75, v75, v62\n\t"
    "v_fma_f32 v65, v65, s68, -v72\n\t"
    "v_mfma_f32_16x16x32_bf16 v[44:47], a[144:147], v[148:151], 0\n\t"
    "v_exp_f32_e32 v63, v63\n\t"
    "v_mfma_f32_16x16x32_bf16 v[44:47], a[152:155], v[144:147], v[44:47]\n\t"
    "v_add_f32_e32 v75, v75, v63\n\t"
    "v_fma_f32 v66, v66, s68, -v72\n\t"
    "s_branch .Lmid_%=\n\t"
    ".Ltail_%=:\n\t"
    "v_mbcnt_lo_u32_b32 v8, -1, 0\n\t"
    "v_mbcnt_hi_u32_b32 v8, -1, v8\n\t"
    "v_lshlrev_b32_e32 v8, 5, v8\n\t"
    "s_nop 7\n\t"
    "s_nop 1\n\t"
    "ds_write_b128 v6, v[44:47] offset:4096\n\t"
    "v_cvt_pk_bf16_f32 v62, v56, v57\n\t"
    "ds_write_b128 v6, v[52:55] offset:5120\n\t"
    "v_cvt_pk_bf16_f32 v63, v58, v59\n\t"
    "s_nop 1\n\t"
    "v_mfma_f32_16x16x32_bf16 v[12:15], v[208:211], v[40:43], v[12:15]\n\t"
    "s_nop 1\n\t"
    "v_mfma_f32_16x16x32_bf16 v[12:15], v[204:207], v[48:51], v[12:15]\n\t"
    "s_nop 1\n\t"
    "v_mfma_f32_16x16x32_bf16 v[12:15], v[200:203], v[60:63], v[12:15]\n\t"
    "s_nop 1\n\t"
    "v_mfma_f32_16x16x32_bf16 v[16:19], v[184:187], v[40:43], v[16:19]\n\t"
    "s_nop 1\n\t"
    "v_mfma_f32_16x16x32_bf16 v[16:19], v[180:183], v[48:51], v[16:19]\n\t"
    "s_waitcnt lgkmcnt(0)\n\t"
    "s_barrier\n\t"
    "s_nop 1\n\t"
    "v_mfma_f32_16x16x32_bf16 v[16:19], v[176:179], v[60:63], v[16:19]\n\t"
    "s_nop 1\n\t"
    "v_mfma_f32_16x16x32_bf16 v[20:23], v[160:163], v[40:43], v[20:23]\n\t"
    "v_max3_f32 v52, v12, v13, v14\n\t"
    "s_nop 1\n\t"
    "v_mfma_f32_16x16x32_bf16 v[20:23], v[156:159], v[48:51], v[20:23]\n\t"
    "s_nop 1\n\t"
    "v_mfma_f32_16x16x32_bf16 v[20:23], v[152:155], v[60:63], v[20:23]\n\t"
    "s_nop 1\n\t"
    "v_mfma_f32_16x16x32_bf16 v[24:27], v[136:139], v[40:43], v[24:27]\n\t"
    "v_max3_f32 v53, v15, v16, v17\n\t"
    "v_max3_f32 v52, v52, v18, v19\n\t"
    "s_nop 1\n\t"
    "v_mfma_f32_16x16x32_bf16 v[24:27], v[132:135], v[48:51], v[24:27]\n\t"
    "s_nop 1\n\t"
    "v_mfma_f32_16x16x32_bf16 v[24:27], v[128:131], v[60:63], v[24:27]\n\t"
    "s_nop 7\n\t"
    "s_nop 1\n\t"
    "v_max3_f32 v53, v53, v20, v21\n\t"
    "v_max3_f32 v52, v52, v22, v23\n\t"
    "v_max3_f32 v53, v53, v24, v25\n\t"
    "v_max3_f32 v52, v52, v26, v27\n\t"
    "v_max_f32_e32 v52, v52, v53\n\t"
    "v_mov_b32_e32 v53, v52\n\t"
    "s_nop 1\n\t"
    "v_permlane32_swap_b32 v52, v53\n\t"
    "v_max_f32_e32 v52, v52, v53\n\t"
    "v_mov_b32_e32 v53, v52\n\t"
    "s_nop 1\n\t"
    "v_permlane16_swap_b32 v52, v53\n\t"
    "v_max_f32_e32 v52, v52, v53\n\t"
    "v_mul_f32_e32 v52, s68, v52\n\t"
    "v_fma_f32 v12, v12, s68, -v52\n\t"
    "v_fma_f32 v13, v13, s68, -v52\n\t"
    "v_exp_f32_e32 v12, v12\n\t"
    "s_nop 0\n\t"
    "v_fma_f32 v14, v14, s68, -v52\n\t"
    "v_fma_f32 v15, v15, s68, -v52\n\t"
    "v_fma_f32 v16, v16, s68, -v52\n\t"
    "v_fma_f32 v17, v17, s68, -v52\n\t"
    "v_exp_f32_e32 v13, v13\n\t"
    "s_nop 0\n\t"
    "v_add_f32_e32 v54, v12, v13\n\t"
    "v_fma_f32 v18, v18, s68, -v52\n\t"
    "v_exp_f32_e32 v14, v14\n\t"
    "s_nop 0\n\t"
    "v_exp_f32_e32 v15, v15\n\t"
    "s_nop 0\n\t"
    "v_add_f32_e32 v55, v14, v15\n\t"
    "v_fma_f32 v19, v19, s68, -v52\n\t"
    "v_exp_f32_e32 v16, v16\n\t"
    "s_nop 0\n\t"
    "v_add_f32_e32 v54, v54, v16\n\t"
    "v_fma_f32 v20, v20, s68, -v52\n\t"
    "v_exp_f32_e32 v17, v17\n\t"
    "s_nop 0\n\t"
    "v_add_f32_e32 v54, v54, v17\n\t"
    "v_fma_f32 v21, v21, s68, -v52\n\t"
    "v_exp_f32_e32 v18, v18\n\t"
    "s_nop 0\n\t"
    "v_add_f32_e32 v55, v55, v18\n\t"
    "v_exp_f32_e32 v19, v19\n\t"
    "s_nop 0\n\t"
    "v_add_f32_e32 v55, v55, v19\n\t"
    "v_fma_f32 v22, v22, s68, -v52\n\t"
    "v_exp_f32_e32 v20, v20\n\t"
    "s_nop 0\n\t"
    "v_add_f32_e32 v54, v54, v20\n\t"
    "v_fma_f32 v23, v23, s68, -v52\n\t"
    "v_exp_f32_e32 v21, v21\n\t"
    "s_nop 0\n\t"
    "v_add_f32_e32 v54, v54, v21\n\t"
    "v_exp_f32_e32 v22, v22\n\t"
    "s_nop 0\n\t"
    "v_add_f32_e32 v55, v55, v22\n\t"
    "v_fma_f32 v24, v24, s68, -v52\n\t"
    "v_fma_f32 v25, v25, s68, -v52\n\t"
    "v_exp_f32_e32 v23, v23\n\t"
    "s_nop 0\n\t"
    "v_add_f32_e32 v55, v55, v23\n\t"
    "v_exp_f32_e32 v24, v24\n\t"
    "s_nop 0\n\t"
    "v_add_f32_e32 v54, v54, v24\n\t"
    "v_exp_f32_e32 v25, v25\n\t"
    "s_nop 0\n\t"
    "v_add_f32_e32 v54, v54, v25\n\t"
    "v_fma_f32 v26, v26, s68, -v52\n\t"
    "v_exp_f32_e32 v26, v26\n\t"
    "s_nop 0\n\t"
    "v_add_f32_e32 v55, v55, v26\n\t"
    "v_fma_f32 v27, v27, s68, -v52\n\t"
    "v_exp_f32_e32 v27, v27\n\t"
    "s_nop 0\n\t"
    "v_add_f32_e32 v55, v55, v27\n\t"
    "v_add_f32_e32 v54, v54, v55\n\t"
    "v_mov_b32_e32 v55, v54\n\t"
    "s_nop 1\n\t"
    "v_permlane32_swap_b32 v54, v55\n\t"
    "v_add_f32_e32 v54, v54, v55\n\t"
    "v_mov_b32_e32 v55, v54\n\t"
    "s_nop 1\n\t"
    "v_permlane16_swap_b32 v54, v55\n\t"
    "v_add_f32_e32 v54, v54, v55\n\t"
    "v_rcp_f32_e32 v54, v54\n\t"
    "s_nop 0\n\t"
    "v_mul_f32_e32 v12, v12, v54\n\t"
    "v_mul_f32_e32 v13, v13, v54\n\t"
    "v_cvt_pk_bf16_f32 v76, v12, v13\n\t"
    "v_mul_f32_e32 v14, v14, v54\n\t"
    "v_mul_f32_e32 v15, v15, v54\n\t"
    "v_cvt_pk_bf16_f32 v77, v14, v15\n\t"
    "v_mul_f32_e32 v16, v16, v54\n\t"
    "v_mul_f32_e32 v17, v17, v54\n\t"
    "v_cvt_pk_bf16_f32 v78, v16, v17\n\t"
    "v_mul_f32_e32 v18, v18, v54\n\t"
    "v_mul_f32_e32 v19, v19, v54\n\t"
    "v_cvt_pk_bf16_f32 v79, v18, v19\n\t"
    "v_mul_f32_e32 v20, v20, v54\n\t"
    "v_mul_f32_e32 v21, v21, v54\n\t"
    "v_cvt_pk_bf16_f32 v56, v20, v21\n\t"
    "v_mul_f32_e32 v22, v22, v54\n\t"
    "v_mul_f32_e32 v23, v23, v54\n\t"
    "v_cvt_pk_bf16_f32 v57, v22, v23\n\t"
    "v_mul_f32_e32 v24, v24, v54\n\t"
    "v_mul_f32_e32 v25, v25, v54\n\t"
    "v_cvt_pk_bf16_f32 v58, v24, v25\n\t"
    "v_mul_f32_e32 v26, v26, v54\n\t"
    "ds_write_b128 v6, v[76:79] offset:6144\n\t"
    "v_mul_f32_e32 v27, v27, v54\n\t"
    "v_cvt_pk_bf16_f32 v59, v26, v27\n\t"
    "ds_write_b128 v6, v[56:59] offset:7168\n\t"
    "s_waitcnt lgkmcnt(0)\n\t"
    "s_barrier\n\t"
    "s_cmp_lg_u32 %[closer], 0\n\t"
    "s_cbranch_scc1 .Lx2_%=\n\t"
    "s_mov_b32 s60, 0x10000\n\t"
    "buffer_load_dwordx2 v[10:11], off, s[56:59], %[eoff] sc1\n\t"
    ".Lp3_%=:\n\t"
    "buffer_load_dwordx4 v[12:15], v8, s[56:59], %[aoff] offen sc1\n\t"
    "buffer_load_dwordx4 v[16:19], v8, s[56:59], %[aoff] offen offset:16 sc1\n\t"
    "s_waitcnt vmcnt(0)\n\t"
    "v_add_co_u32_e32 v20, vcc, 0x7f4a7c15, v10\n\t"
    "s_nop 1\n\t"
    "v_mov_b32_e32 v9, 0x9e3779b9\n\t"
    "v_addc_co_u32_e32 v21, vcc, v9, v11, vcc\n\t"
    "v_cmp_eq_u64_e32 vcc, v[12:13], v[20:21]\n\t"
    "v_cmp_eq_u64_e64 s[66:67], v[14:15], v[20:21]\n\t"
    "s_and_b64 vcc, vcc, s[66:67]\n\t"
    "v_cmp_eq_u64_e64 s[66:67], v[16:17], v[20:21]\n\t"
    "s_and_b64 vcc, vcc, s[66:67]\n\t"
    "v_cmp_eq_u64_e64 s[66:67], v[18:19], v[20:21]\n\t"
    "s_and_b64 vcc, vcc, s[66:67]\n\t"
    "s_cmp_eq_u64 vcc, exec\n\t"
    "s_cbranch_scc1 .Lk4_%=\n\t"
    "s_sleep 8\n\t"
    "s_sub_u32 s60, s60, 1\n\t"
    "s_cmp_lg_u32 s60, 0\n\t"
    "s_cbranch_scc1 .Lp3_%=\n\t"
    ".Lk4_%=:\n\t"
    "s_mov_b64 exec, 1\n\t"
    "buffer_store_dwordx2 v[20:21], off, s[56:59], %[eoff] sc1\n\t"
    "s_mov_b64 exec, -1\n\t"
    ".Lx2_%=:\n\t"
    "s_endpgm\n\t"
    ".Lend_%=:\n\t"
    :: "{v31}"(lane), "{s[36:43]}"(k8), "{s[44:47]}"(k4),
       [role] "s"(role), [pair] "s"(pair), [prod] "s"((pair * 256 + bid - 144) >> 31), [closer] "s"(bid | wave), [bid] "s"(bid), [wb] "s"(wb),
       [rbeg] "s"(rbeg), [rend] "s"(rbeg + 3 * SLOT), [pl] "s"(ldsb + PB + pair * 8192), [amask] "s"((unsigned long long)pair),
       [tch] "s"(((bid >> 3) * 2 + pair) * 1152), [foff] "s"(F_OFF + 64 * bid), [aoff] "s"(A_OFF), [aown] "s"(A_OFF + 8 * bid), [eoff] "s"(E_OFF)
    : "memory", "scc", ACLOB, VCLOB, SCLOB);
}

extern "C" void kernel_launch(void* const* d_in, const int* in_sizes, int n_in,
                              void* d_out, int out_size, void* d_ws, size_t ws_size,
                              hipStream_t stream) {
  attn_kernel<<<256, 256, 0, stream>>>((const unsigned char*)d_in[0], (const unsigned char*)d_in[1], (const unsigned char*)d_in[2],
                                       (unsigned char*)d_ws, (unsigned char*)d_out
      );
}
```

```cpp
#include <hip/hip_runtime.h>
#include <hip/hip_bf16.h>
#include <stdint.h>

using u32x4 = __attribute__((ext_vector_type(4))) unsigned;
using u32x8 = __attribute__((ext_vector_type(8))) unsigned;

static constexpr int ROWB = 512 * 384;
static constexpr int SLOT = 24576;
static constexpr int PB = 6 * SLOT;
static constexpr int F_OFF = 160 * 1024, A_OFF = 148 * 1024, E_OFF = 152 * 1024;

#define R10(p, n) p #n "0", p #n "1", p #n "2", p #n "3", p #n "4", p #n "5", p #n "6", p #n "7", p #n "8", p #n "9"
#define R240(p) R10(p, 4), R10(p, 5), R10(p, 6), R10(p, 7), R10(p, 8), R10(p, 9), R10(p, 10), R10(p, 11), R10(p, 12), R10(p, 13), \
  R10(p, 14), R10(p, 15), R10(p, 16), R10(p, 17), R10(p, 18), R10(p, 19), R10(p, 20), R10(p, 21), R10(p, 22), R10(p, 23), R10(p, 24), \
  p "250", p "251", p "252", p "253", p "254", p "255"
#define ACLOB R10("a", ), R10("a", 1), R10("a", 2), R10("a", 3), R240("a")
#define VCLOB R10("v", ), R10("v", 1), R10("v", 2), "v30", "v32", "v33", "v34", "v35", "v36", "v37", "v38", "v39", R240("v")
#define SCLOB "s48", "s49", R10("s", 5), R10("s", 6), "s70", "s71", "vcc"

__global__ __launch_bounds__(256, 1) void attn_kernel(const unsigned char* __restrict__ x,
                                                      const unsigned char* __restrict__ wqkv,
                                                      const unsigned char* __restrict__ wo,
                                                      unsigned char* ws,
                                                      unsigned char* __restrict__ out
                                                      ) {
  __shared__ u32x4 lds[10240];
  u32x8 k8; u32x4 k4;
  asm volatile(
    "s_load_dwordx8 s[36:43], %2, 0\n\t"
    "s_load_dwordx2 s[44:45], %2, 32\n\t"
    : "={s[36:43]}"(k8), "={s[44:47]}"(k4) : "s"(__builtin_amdgcn_kernarg_segment_ptr()) : "memory", ACLOB);
  __builtin_amdgcn_sched_barrier(0);
  const unsigned lane = threadIdx.x & 63, wave = __builtin_amdgcn_readfirstlane(threadIdx.x >> 6);
  const unsigned role = wave >> 1, pair = wave & 1;
  const unsigned ldsb = (unsigned)(size_t)lds;
  const unsigned bid = blockIdx.x;
  const unsigned wb = (bid & 7) * 8 * ROWB + ((bid >> 3) * 2 + pair) * 8 * 384;
  const unsigned rbeg = ldsb + pair * 3 * SLOT;

  asm volatile(
    ".Lbeg_%=:\n\t"
    "s_cmp_lg_u32 %[role], 1\n\t"
    "s_cbranch_scc1 .Lroln_%=\n\t"
    "s_cmp_eq_u32 %[prod], 0\n\t"
    "s_cbranch_scc1 .Lnp_%=\n\t"
    "s_cmp_lt_u32 %[bid], 72\n\t"
    "s_cselect_b32 s71, 0, 72\n\t"
    "s_cselect_b32 s63, 0, 768\n\t"
    "s_sub_u32 s64, %[bid], s71\n\t"
    "s_mul_i32 s65, s64, 0x1556\n\t"
    "s_lshr_b32 s65, s65, 16\n\t"
    "s_mul_i32 s69, s65, 12\n\t"
    "s_sub_u32 s69, s64, s69\n\t"
    "s_lshr_b32 s69, s69, 1\n\t"
    "s_and_b32 s70, s64, 1\n\t"
    "s_mul_i32 s60, s69, 36864\n\t"
    "s_add_u32 s60, s60, s63\n\t"
    "s_add_u32 s61, s60, 4608\n\t"
    "v_lshrrev_b32_e32 v20, 2, v31\n\t"
    "v_and_b32_e32 v21, 8, v20\n\t"
    "v_bfe_u32 v24, v31, 3, 2\n\t"
    "v_or_b32_e32 v21, v21, v24\n\t"
    "v_mul_u32_u24_e32 v21, 0x480, v21\n\t"
    "v_lshrrev_b32_e32 v24, 4, v31\n\t"
    "v_xor_b32_e32 v24, v24, v31\n\t"
    "v_and_b32_e32 v24, 7, v24\n\t"
    "v_lshl_or_b32 v24, v24, 4, v21\n\t"
    "v_xor_b32_e32 v25, 64, v24\n\t"
    "v_add_u32_e32 v25, 0x4800, v25\n\t"
    "v_mul_u32_u24_e32 v27, 0x180, v20\n\t"
    "v_and_b32_e32 v28, 3, v31\n\t"
    "v_lshl_or_b32 v27, v28, 4, v27\n\t"
    "s_lshl_b32 s62, s65, 3\n\t"
    "s_add_u32 s62, s62, s70\n\t"
    "s_mul_i32 s62, s62, 4608\n\t"
    "s_add_u32 s62, s62, 384\n\t"
    "s_lshl_b32 s63, s65, 6\n\t"
    "s_mov_b32 s50, 0x36000\n\t"
    "s_mov_b32 s51, 0x20000\n\t"
    "s_mov_b32 s54, 0x12000\n\t"
    "s_mov_b32 s55, 0x20000\n\t"
    "s_waitcnt lgkmcnt(0)\n\t"
    "s_mov_b32 s48, s38\n\t"
    "s_and_b32 s49, s39, 0xffff\n\t"
    "s_cmp_lg_u32 s71, 0\n\t"
    "s_cbranch_scc1 .Lpo_%=\n\t"
    "s_mov_b32 m0, 61440\n\t"
    "s_nop 0\n\t"
    "buffer_load_dwordx4 v24, s[48:51], s62 offen offset:0 lds\n\t"
    "s_mov_b32 m0, 62464\n\t"
    "s_nop 0\n\t"
    "buffer_load_dwordx4 v25, s[48:51], s62 offen offset:0 lds\n\t"
    "s_mov_b32 m0, 63360\n\t"
    "s_nop 0\n\t"
    "buffer_load_dwordx4 v24, s[48:51], s62 offen offset:128 lds\n\t"
    "s_mov_b32 m0, 64384\n\t"
    "s_nop 0\n\t"
    "buffer_load_dwordx4 v25, s[48:51], s62 offen offset:128 lds\n\t"
    "s_mov_b32 m0, 65280\n\t"
    "s_nop 0\n\t"
    "buffer_load_dwordx4 v24, s[48:51], s62 offen offset:256 lds\n\t"
    "s_mov_b32 m0, 66304\n\t"
    "s_nop 0\n\t"
    "buffer_load_dwordx4 v25, s[48:51], s62 offen offset:256 lds\n\t"
    "s_branch .Lpa_%=\n\t"
    ".Lpo_%=:\n\t"
    "s_mov_b32 s52, s40\n\t"
    "s_mov_b32 m0, 61440\n\t"
    "s_and_b32 s53, s41, 0xffff\n\t"
    "buffer_load_dwordx4 v27, s[52:55], s63 offen lds\n\t"
    "s_mov_b32 m0, 62464\n\t"
    "s_add_u32 s63, s63, 6144\n\t"
    "buffer_load_dwordx4 v27, s[52:55], s63 offen lds\n\t"
    "s_mov_b32 m0, 63488\n\t"
    "s_add_u32 s63, s63, 6144\n\t"
    "buffer_load_dwordx4 v27, s[52:55], s63 offen lds\n\t"
    "s_mov_b32 m0, 64512\n\t"
    "s_add_u32 s63, s63, 6144\n\t"
    "buffer_load_dwordx4 v27, s[52:55], s63 offen lds\n\t"
    "s_mov_b32 m0, 65536\n\t"
    "s_add_u32 s63, s63, 6144\n\t"
    "buffer_load_dwordx4 v27, s[52:55], s63 offen lds\n\t"
    "s_mov_b32 m0, 66560\n\t"
    "s_add_u32 s63, s63, 6144\n\t"
    "buffer_load_dwordx4 v27, s[52:55], s63 offen lds\n\t"
    "s_mov_b32 m0, 67584\n\t"
    "s_add_u32 s63, s63, 6144\n\t"
    "buffer_load_dwordx4 v27, s[52:55], s63 offen lds\n\t"
    "s_mov_b32 m0, 68608\n\t"
    "s_add_u32 s63, s63, 6144\n\t"
    "buffer_load_dwordx4 v27, s[52:55], s63 offen lds\n\t"
    "s_mov_b32 m0, 69632\n\t"
    "s_add_u32 s63, s63, 6144\n\t"
    "buffer_load_dwordx4 v27, s[52:55], s63 offen lds\n\t"
    "s_mov_b32 m0, 70656\n\t"
    "s_add_u32 s63, s63, 6144\n\t"
    "buffer_load_dwordx4 v27, s[52:55], s63 offen lds\n\t"
    "s_mov_b32 m0, 71680\n\t"
    "s_add_u32 s63, s63, 6144\n\t"
    "buffer_load_dwordx4 v27, s[52:55], s63 offen lds\n\t"
    "s_mov_b32 m0, 72704\n\t"
    "s_add_u32 s63, s63, 6144\n\t"
    "buffer_load_dwordx4 v27, s[52:55], s63 offen lds\n\t"
    ".Lpa_%=:\n\t"
    "s_mov_b32 m0, 49152\n\t"
    "s_nop 0\n\t"
    "buffer_load_dwordx4 v24, s[48:51], s60 offen offset:0 lds\n\t"
    "s_mov_b32 m0, 53120\n\t"
    "s_nop 0\n\t"
    "buffer_load_dwordx4 v24, s[48:51], s60 offen offset:128 lds\n\t"
    "s_mov_b32 m0, 57088\n\t"
    "s_nop 0\n\t"
    "buffer_load_dwordx4 v24, s[48:51], s60 offen offset:256 lds\n\t"
    "s_mov_b32 m0, 51200\n\t"
    "s_nop 0\n\t"
    "buffer_load_dwordx4 v25, s[48:51], s60 offen offset:0 lds\n\t"
    "s_mov_b32 m0, 55168\n\t"
    "s_nop 0\n\t"
    "buffer_load_dwordx4 v25, s[48:51], s60 offen offset:128 lds\n\t"
    "s_mov_b32 m0, 59136\n\t"
    "s_nop 0\n\t"
    "buffer_load_dwordx4 v25, s[48:51], s60 offen offset:256 lds\n\t"
    "s_mov_b32 m0, 50176\n\t"
    "s_nop 0\n\t"
    "buffer_load_dwordx4 v24, s[48:51], s61 offen offset:0 lds\n\t"
    "s_mov_b32 m0, 54144\n\t"
    "s_nop 0\n\t"
    "buffer_load_dwordx4 v24, s[48:51], s61 offen offset:128 lds\n\t"
    "s_mov_b32 m0, 58112\n\t"
    "s_nop 0\n\t"
    "buffer_load_dwordx4 v24, s[48:51], s61 offen offset:256 lds\n\t"
    "s_mov_b32 m0, 52224\n\t"
    "s_nop 0\n\t"
    "buffer_load_dwordx4 v25, s[48:51], s61 offen offset:0 lds\n\t"
    "s_mov_b32 m0, 56192\n\t"
    "s_nop 0\n\t"
    "buffer_load_dwordx4 v25, s[48:51], s61 offen offset:128 lds\n\t"
    "s_mov_b32 m0, 60160\n\t"
    "s_nop 0\n\t"
    "buffer_load_dwordx4 v25, s[48:51], s61 offen offset:256 lds\n\t"
    ".Lnp_%=:\n\t"
    "s_waitcnt lgkmcnt(0)\n\t"
    "s_mov_b32 s56, s42\n\t"
    "s_and_b32 s57, s43, 0xffff\n\t"
    "s_mov_b32 s58, 0x40000\n\t"
    "s_mov_b32 s59, 0x20000\n\t"
    "buffer_load_dwordx2 v[22:23], off, s[56:59], %[eoff] sc1\n\t"
    "s_barrier\n\t"
    "v_lshlrev_b32_e32 v30, 4, v31\n\t"
    "v_and_b32_e32 v28, 7, v31\n\t"
    "v_bfe_u32 v27, v31, 3, 1\n\t"
    "v_lshrrev_b32_e32 v29, 4, v31\n\t"
    "v_add_u32_e32 v6, %[pl], v30\n\t"
    "v_mul_u32_u24_e32 v27, 0xc00, v27\n\t"
    "v_add_u32_e32 v27, %[rbeg], v27\n\t"
    "v_lshlrev_b32_e32 v26, 7, v31\n\t"
    "v_mad_u32_u24 v20, v28, 3, 0\n\t"
    "v_and_b32_e32 v21, 6, v20\n\t"
    "v_xor_b32_e32 v21, v21, v29\n\t"
    "v_lshl_add_u32 v20, v20, 7, v27\n\t"
    "v_lshl_add_u32 v0, v21, 4, v20\n\t"
    "v_xor_b32_e32 v1, 64, v0\n\t"
    "v_mad_u32_u24 v20, v28, 3, 1\n\t"
    "v_and_b32_e32 v21, 6, v20\n\t"
    "v_xor_b32_e32 v21, v21, v29\n\t"
    "v_lshl_add_u32 v20, v20, 7, v27\n\t"
    "v_lshl_add_u32 v2, v21, 4, v20\n\t"
    "v_xor_b32_e32 v3, 64, v2\n\t"
    "v_mad_u32_u24 v20, v28, 3, 2\n\t"
    "v_and_b32_e32 v21, 6, v20\n\t"
    "v_xor_b32_e32 v21, v21, v29\n\t"
    "v_lshl_add_u32 v20, v20, 7, v27\n\t"
    "v_lshl_add_u32 v4, v21, 4, v20\n\t"
    "v_xor_b32_e32 v5, 64, v4\n\t"
    "s_cmp_eq_u32 %[prod], 0\n\t"
    "s_cbranch_scc1 .Lnq_%=\n\t"
    "v_bfe_u32 v20, v31, 1, 3\n\t"
    "v_xor_b32_e32 v20, v20, v29\n\t"
    "v_lshlrev_b32_e32 v20, 4, v20\n\t"
    "v_lshl_or_b32 v20, v28, 7, v20\n\t"
    "v_bfe_u32 v21, v31, 3, 1\n\t"
    "v_lshl_or_b32 v16, v21, 11, v20\n\t"
    "v_add_u32_e32 v16, 0xc000, v16\n\t"
    "v_xor_b32_e32 v17, 64, v16\n\t"
    "s_cmp_lg_u32 s71, 0\n\t"
    "s_cbranch_scc1 .Lqo_%=\n\t"
    "v_lshl_or_b32 v18, v21, 10, v20\n\t"
    "v_add_u32_e32 v18, 0xf000, v18\n\t"
    "v_xor_b32_e32 v19, 64, v18\n\t"
    "s_mul_i32 s60, s70, 6\n\t"
    "s_add_u32 s60, s60, s65\n\t"
    "s_mul_i32 s60, s60, 6\n\t"
    "s_add_u32 s60, s60, s69\n\t"
    "s_lshl_b32 s60, s60, 10\n\t"
    "v_add_u32_e32 v20, s60, v30\n\t"
    "v_mov_b32_e32 v24, v30\n\t"
    "s_waitcnt vmcnt(13)\n\t"
    "ds_read_b128 v[80:83], v18 offset:0\n\t"
    "ds_read_b128 v[84:87], v19 offset:0\n\t"
    "ds_read_b128 v[88:91], v18 offset:2048\n\t"
    "ds_read_b128 v[92:95], v19 offset:2048\n\t"
    "ds_read_b128 v[96:99], v18 offset:4096\n\t"
    "ds_read_b128 v[100:103], v19 offset:4096\n\t"
    "s_branch .Lqa_%=\n\t"
    ".Lqo_%=:\n\t"
    "s_and_b32 s63, s65, 1\n\t"
    "s_lshl_b32 s63, s63, 1\n\t"
    "s_add_u32 s63, s63, s70\n\t"
    "s_lshl_b32 s64, s70, 2\n\t"
    "s_lshl_b32 s62, s70, 4\n\t"
    "s_cmp_lt_u32 s65, 4\n\t"
    "s_cselect_b32 s60, s64, 8\n\t"
    "s_cselect_b32 s63, s65, s63\n\t"
    "s_cselect_b32 s62, s64, s62\n\t"
    "s_cselect_b32 s61, 4, 3\n\t"
    "s_lshl_b32 s62, s62, 1\n\t"
    "s_add_u32 s62, s62, 61440\n\t"
    "s_mul_i32 s60, s60, 6\n\t"
    "s_add_u32 s60, s60, s69\n\t"
    "s_lshl_b32 s60, s60, 2\n\t"
    "s_add_u32 s60, s60, s63\n\t"
    "s_lshl_b32 s60, s60, 8\n\t"
    "s_add_u32 s60, s60, 73728\n\t"
    "v_and_b32_e32 v25, 3, v31\n\t"
    "v_bfe_u32 v21, v31, 2, 2\n\t"
    "v_lshlrev_b32_e32 v18, s61, v25\n\t"
    "v_lshl_add_u32 v18, v21, 6, v18\n\t"
    "v_lshl_add_u32 v18, v29, 9, v18\n\t"
    "v_add_u32_e32 v18, s62, v18\n\t"
    "v_lshl_add_u32 v24, v25, 2, v21\n\t"
    "v_lshl_add_u32 v24, v29, 4, v24\n\t"
    "v_lshlrev_b32_e32 v24, 4, v24\n\t"
    "v_mul_u32_u24_e32 v21, 0x1800, v21\n\t"
    "v_lshl_add_u32 v20, v25, 4, v21\n\t"
    "v_lshl_add_u32 v20, v29, 6, v20\n\t"
    "v_add_u32_e32 v20, s60, v20\n\t"
    "s_waitcnt vmcnt(13)\n\t"
    "ds_read_b64_tr_b16 v[80:81], v18 offset:0\n\t"
    "ds_read_b64_tr_b16 v[82:83], v18 offset:256\n\t"
    "ds_read_b64_tr_b16 v[84:85], v18 offset:2048\n\t"
    "ds_read_b64_tr_b16 v[86:87], v18 offset:2304\n\t"
    "ds_read_b64_tr_b16 v[88:89], v18 offset:4096\n\t"
    "ds_read_b64_tr_b16 v[90:91], v18 offset:4352\n\t"
    "ds_read_b64_tr_b16 v[92:93], v18 offset:6144\n\t"
    "ds_read_b64_tr_b16 v[94:95], v18 offset:6400\n\t"
    "ds_read_b64_tr_b16 v[96:97], v18 offset:8192\n\t"
    "ds_read_b64_tr_b16 v[98:99], v18 offset:8448\n\t"
    "ds_read_b64_tr_b16 v[100:101], v18 offset:10240\n\t"
    "ds_read_b64_tr_b16 v[102:103], v18 offset:10496\n\t"
    ".Lqa_%=:\n\t"
    "s_waitcnt vmcnt(7)\n\t"
    "ds_read_b128 v[32:35], v16 offset:0\n\t"
    "ds_read_b128 v[36:39], v17 offset:0\n\t"
    "ds_read_b128 v[40:43], v16 offset:4096\n\t"
    "ds_read_b128 v[44:47], v17 offset:4096\n\t"
    "ds_read_b128 v[48:51], v16 offset:8192\n\t"
    "ds_read_b128 v[52:55], v17 offset:8192\n\t"
    "s_waitcnt vmcnt(1)\n\t"
    "ds_read_b128 v[56:59], v16 offset:1024\n\t"
    "ds_read_b128 v[60:63], v17 offset:1024\n\t"
    "ds_read_b128 v[64:67], v16 offset:5120\n\t"
    "ds_read_b128 v[68:71], v17 offset:5120\n\t"
    "ds_read_b128 v[72:75], v16 offset:9216\n\t"
    "ds_read_b128 v[76:79], v17 offset:9216\n\t"
    "s_waitcnt lgkmcnt(11)\n\t"
    "v_mfma_f32_16x16x32_bf16 v[108:111], v[32:35], v[80:83], 0\n\t"
    "s_waitcnt lgkmcnt(10)\n\t"
    "v_mfma_f32_16x16x32_bf16 v[108:111], v[36:39], v[84:87], v[108:111]\n\t"
    "s_waitcnt lgkmcnt(9)\n\t"
    "v_mfma_f32_16x16x32_bf16 v[108:111], v[40:43], v[88:91], v[108:111]\n\t"
    "s_waitcnt lgkmcnt(8)\n\t"
    "v_mfma_f32_16x16x32_bf16 v[108:111], v[44:47], v[92:95], v[108:111]\n\t"
    "s_waitcnt lgkmcnt(7)\n\t"
    "v_mfma_f32_16x16x32_bf16 v[108:111], v[48:51], v[96:99], v[108:111]\n\t"
    "s_waitcnt lgkmcnt(6)\n\t"
    "v_mfma_f32_16x16x32_bf16 v[108:111], v[52:55], v[100:103], v[108:111]\n\t"
    "s_waitcnt lgkmcnt(5)\n\t"
    "v_mfma_f32_16x16x32_bf16 v[112:115], v[56:59], v[80:83], 0\n\t"
    "s_waitcnt lgkmcnt(4)\n\t"
    "v_mfma_f32_16x16x32_bf16 v[112:115], v[60:63], v[84:87], v[112:115]\n\t"
    "s_waitcnt lgkmcnt(3)\n\t"
    "v_mfma_f32_16x16x32_bf16 v[112:115], v[64:67], v[88:91], v[112:115]\n\t"
    "s_waitcnt lgkmcnt(2)\n\t"
    "v_mfma_f32_16x16x32_bf16 v[112:115], v[68:71], v[92:95], v[112:115]\n\t"
    "s_waitcnt lgkmcnt(1)\n\t"
    "v_mfma_f32_16x16x32_bf16 v[112:115], v[72:75], v[96:99], v[112:115]\n\t"
    "s_waitcnt lgkmcnt(0)\n\t"
    "v_mfma_f32_16x16x32_bf16 v[112:115], v[76:79], v[100:103], v[112:115]\n\t"
    "s_cmp_eq_u32 s71, 0\n\t"
    "s_cselect_b32 s60, 0x3dd53b95, 1.0\n\t"
    "v_mul_f32_e32 v108, s60, v108\n\t"
    "v_mul_f32_e32 v109, s60, v109\n\t"
    "v_mul_f32_e32 v110, s60, v110\n\t"
    "v_mul_f32_e32 v111, s60, v111\n\t"
    "s_nop 3\n\t"
    "v_mul_f32_e32 v112, s60, v112\n\t"
    "v_mul_f32_e32 v113, s60, v113\n\t"
    "v_mul_f32_e32 v114, s60, v114\n\t"
    "v_mul_f32_e32 v115, s60, v115\n\t"
    "v_cvt_pk_bf16_f32 v104, v108, v109\n\t"
    "v_cvt_pk_bf16_f32 v105, v110, v111\n\t"
    "s_nop 0\n\t"
    "v_cvt_pk_bf16_f32 v106, v112, v113\n\t"
    "v_cvt_pk_bf16_f32 v107, v114, v115\n\t"
    "ds_write_b128 v24, v[104:107] offset:49152\n\t"
    "ds_read_b128 v[104:107], v30 offset:49152\n\t"
    "s_waitcnt lgkmcnt(0)\n\t"
    "buffer_store_dwordx4 v[104:107], v20, s[56:59], 0 offen sc1\n\t"
    ".Lnq_%=:\n\t"
    "s_waitcnt vmcnt(0)\n\t"
    "v_add_co_u32_e32 v22, vcc, 0x7f4a7c15, v22\n\t"
    "v_mov_b32_e32 v16, 0x9e3779b9\n\t"
    "s_nop 0\n\t"
    "v_addc_co_u32_e32 v23, vcc, v16, v23, vcc\n\t"
    "s_mov_b32 exec_lo, %[prod]\n\t"
    "s_mov_b32 exec_hi, 0\n\t"
    "buffer_store_dwordx2 v[22:23], off, s[56:59], %[foff] sc1\n\t"
    "s_mov_b64 exec, -1\n\t"
    "v_lshrrev_b32_e32 v20, 5, v31\n\t"
    "v_mul_u32_u24_e32 v20, 0x30000, v20\n\t"
    "v_and_b32_e32 v21, 1, v29\n\t"
    "v_mul_u32_u24_e32 v21, 0x600, v21\n\t"
    "v_add_u32_e32 v20, v20, v21\n\t"
    "v_and_b32_e32 v21, 15, v31\n\t"
    "v_lshl_add_u32 v7, v21, 4, v20\n\t"
    "v_lshl_add_u32 v8, v21, 3, v20\n\t"
    "v_add_u32_e32 v8, 0x100, v8\n\t"
    "v_and_b32_e32 v20, 48, v30\n\t"
    "v_lshl_or_b32 v20, v29, 6, v20\n\t"
    "v_lshrrev_b32_e32 v21, 2, v21\n\t"
    "v_lshl_or_b32 v9, v21, 8, v20\n\t"
    "v_min_u32_e32 v21, 35, v31\n\t"
    "v_mul_u32_u24_e32 v21, 64, v21\n\t"
    "v_add_u32_e32 v21, 0x28000, v21\n\t"
    "s_mov_b32 s52, s44\n\t"
    "s_and_b32 s53, s45, 0xffff\n\t"
    "s_mov_b32 s54, 0x6000000\n\t"
    "s_mov_b32 s55, 0x20000\n\t"
    "s_cmp_eq_u32 %[pair], 0\n\t"
    "s_cbranch_scc1 .Lng_%=\n\t"
    "s_sleep 45\n\t"
    "s_mov_b32 s64, 0\n\t"
    "s_mov_b32 s65, 2304\n\t"
    "buffer_load_dwordx2 v[12:13], v21, s[56:59], s64 offen sc1\n\t"
    "buffer_load_dwordx2 v[14:15], v21, s[56:59], s65 offen sc1\n\t"
    "s_waitcnt vmcnt(0)\n\t"
    "s_mov_b32 s60, 0x10000\n\t"
    ".Lp5_%=:\n\t"
    "v_cmp_eq_u64_e32 vcc, v[12:13], v[22:23]\n\t"
    "v_cmp_eq_u64_e64 s[70:71], v[14:15], v[22:23]\n\t"
    "s_and_b64 vcc, vcc, s[70:71]\n\t"
    "s_cmp_eq_u64 vcc, exec\n\t"
    "s_cbranch_scc1 .Lk6_%=\n\t"
    "s_sleep 4\n\t"
    "buffer_load_dwordx2 v[12:13], v21, s[56:59], s64 offen sc1\n\t"
    "buffer_load_dwordx2 v[14:15], v21, s[56:59], s65 offen sc1\n\t"
    "s_sub_u32 s60, s60, 1\n\t"
    "s_cmp_lg_u32 s60, 0\n\t"
    "s_waitcnt vmcnt(0)\n\t"
    "s_cbranch_scc1 .Lp5_%=\n\t"
    ".Lk6_%=:\n\t"
    ".Lng_%=:\n\t"
    "s_mul_i32 s66, %[pair], 36864\n\t"
    "s_mul_i32 s62, %[pair], 73728\n\t"
    "s_add_u32 s62, s62, 36864\n\t"
    "v_add_u32_e32 v11, s62, v30\n\t"
    "s_mov_b32 s63, 73728\n\t"
    "s_barrier\n\t"
    "s_add_u32 s67, %[tch], 0\n\t"
    "s_mov_b64 exec, 0x1ff\n\t"
    "buffer_load_dword v10, v26, s[56:59], s67 offen sc1\n\t"
    "s_mov_b64 exec, -1\n\t"
    "buffer_load_dwordx4 v[32:35], v30, s[56:59], s66 offen offset:0 sc1\n\t"
    "buffer_load_dwordx4 v[36:39], v30, s[56:59], s66 offen offset:1024 sc1\n\t"
    "buffer_load_dwordx4 v[40:43], v30, s[56:59], s66 offen offset:2048 sc1\n\t"
    "buffer_load_dwordx4 v[44:47], v30, s[56:59], s66 offen offset:3072 sc1\n\t"
    "s_add_u32 s66, s66, 4096\n\t"
    "buffer_load_dwordx4 v[48:51], v30, s[56:59], s66 offen offset:0 sc1\n\t"
    "buffer_load_dwordx4 v[52:55], v30, s[56:59], s66 offen offset:1024 sc1\n\t"
    "buffer_load_dwordx4 v[56:59], v30, s[56:59], s66 offen offset:2048 sc1\n\t"
    "buffer_load_dwordx4 v[60:63], v30, s[56:59], s66 offen offset:3072 sc1\n\t"
    "s_add_u32 s66, s66, 4096\n\t"
    "buffer_load_dwordx4 v[64:67], v30, s[56:59], s66 offen offset:0 sc1\n\t"
    "buffer_load_dwordx4 v[68:71], v30, s[56:59], s66 offen offset:1024 sc1\n\t"
    "buffer_load_dwordx4 v[72:75], v30, s[56:59], s66 offen offset:2048 sc1\n\t"
    "buffer_load_dwordx4 v[76:79], v30, s[56:59], s66 offen offset:3072 sc1\n\t"
    "s_add_u32 s66, s66, 4096\n\t"
    "buffer_load_dwordx4 v[80:83], v30, s[56:59], s66 offen offset:0 sc1\n\t"
    "buffer_load_dwordx4 v[84:87], v30, s[56:59], s66 offen offset:1024 sc1\n\t"
    "buffer_load_dwordx4 v[88:91], v30, s[56:59], s66 offen offset:2048 sc1\n\t"
    "buffer_load_dwordx4 v[92:95], v30, s[56:59], s66 offen offset:3072 sc1\n\t"
    "s_add_u32 s66, s66, 4096\n\t"
    "buffer_load_dwordx4 v[96:99], v30, s[56:59], s66 offen offset:0 sc1\n\t"
    "buffer_load_dwordx4 v[100:103], v30, s[56:59], s66 offen offset:1024 sc1\n\t"
    "buffer_load_dwordx4 v[104:107], v30, s[56:59], s66 offen offset:2048 sc1\n\t"
    "buffer_load_dwordx4 v[108:111], v30, s[56:59], s66 offen offset:3072 sc1\n\t"
    "s_add_u32 s66, s66, 4096\n\t"
    "buffer_load_dwordx4 v[112:115], v30, s[56:59], s66 offen offset:0 sc1\n\t"
    "buffer_load_dwordx4 v[116:119], v30, s[56:59], s66 offen offset:1024 sc1\n\t"
    "buffer_load_dwordx4 v[120:123], v30, s[56:59], s66 offen offset:2048 sc1\n\t"
    "buffer_load_dwordx4 v[124:127], v30, s[56:59], s66 offen offset:3072 sc1\n\t"
    "s_add_u32 s66, s66, 4096\n\t"
    "s_mov_b32 s64, 4608\n\t"
    "s_mov_b32 s65, 6912\n\t"
    "buffer_load_dwordx2 v[12:13], v21, s[56:59], s64 offen sc1\n\t"
    "buffer_load_dwordx2 v[14:15], v21, s[56:59], s65 offen sc1\n\t"
    "s_waitcnt vmcnt(25)\n\t"
    "ds_write_b128 v11, v[32:35] offset:0\n\t"
    "buffer_load_dwordx4 v[128:131], v30, s[56:59], s66 offen offset:0 sc1\n\t"
    "s_waitcnt vmcnt(25)\n\t"
    "ds_write_b128 v11, v[36:39] offset:1024\n\t"
    "buffer_load_dwordx4 v[132:135], v30, s[56:59], s66 offen offset:1024 sc1\n\t"
    "s_waitcnt vmcnt(25)\n\t"
    "ds_write_b128 v11, v[40:43] offset:2048\n\t"
    "buffer_load_dwordx4 v[136:139], v30, s[56:59], s66 offen offset:2048 sc1\n\t"
    "s_waitcnt vmcnt(25)\n\t"
    "ds_write_b128 v11, v[44:47] offset:3072\n\t"
    "buffer_load_dwordx4 v[140:143], v30, s[56:59], s66 offen offset:3072 sc1\n\t"
    "s_add_u32 s66, s66, 4096\n\t"
    "s_waitcnt vmcnt(25)\n\t"
    "ds_write_b128 v11, v[48:51] offset:4096\n\t"
    "buffer_load_dwordx4 v[144:147], v30, s[56:59], s66 offen offset:0 sc1\n\t"
    "s_waitcnt vmcnt(25)\n\t"
    "ds_write_b128 v11, v[52:55] offset:5120\n\t"
    "s_waitcnt lgkmcnt(0)\n\t"
    "s_barrier\n\t"
    "buffer_load_dwordx4 v[148:151], v30, s[56:59], s66 offen offset:1024 sc1\n\t"
    "s_waitcnt vmcnt(25)\n\t"
    "ds_write_b128 v11, v[56:59] offset:6144\n\t"
    "buffer_load_dwordx4 v[152:155], v30, s[56:59], s66 offen offset:2048 sc1\n\t"
    "s_waitcnt vmcnt(25)\n\t"
    "ds_write_b128 v11, v[60:63] offset:7168\n\t"
    "buffer_load_dwordx4 v[156:159], v30, s[56:59], s66 offen offset:3072 sc1\n\t"
    "s_add_u32 s66, s66, 4096\n\t"
    "s_waitcnt vmcnt(25)\n\t"
    "ds_write_b128 v11, v[64:67] offset:8192\n\t"
    "buffer_load_dwordx4 v[160:163], v30, s[56:59], s66 offen offset:0 sc1\n\t"
    "s_waitcnt vmcnt(25)\n\t"
    "ds_write_b128 v11, v[68:71] offset:9216\n\t"
    "buffer_load_dwordx4 v[164:167], v30, s[56:59], s66 offen offset:1024 sc1\n\t"
    "s_waitcnt vmcnt(25)\n\t"
    "ds_write_b128 v11, v[72:75] offset:10240\n\t"
    "buffer_load_dwordx4 v[168:171], v30, s[56:59], s66 offen offset:2048 sc1\n\t"
    "s_waitcnt vmcnt(25)\n\t"
    "ds_write_b128 v11, v[76:79] offset:11264\n\t"
    "s_waitcnt lgkmcnt(0)\n\t"
    "s_barrier\n\t"
    "buffer_load_dwordx4 v[172:175], v30, s[56:59], s66 offen offset:3072 sc1\n\t"
    "s_add_u32 s66, s66, 4096\n\t"
    "s_waitcnt vmcnt(25)\n\t"
    "ds_write_b128 v11, v[80:83] offset:12288\n\t"
    "s_waitcnt vmcnt(24)\n\t"
    "ds_write_b128 v11, v[84:87] offset:13312\n\t"
    "s_waitcnt vmcnt(23)\n\t"
    "ds_write_b128 v11, v[88:91] offset:14336\n\t"
    "s_waitcnt vmcnt(22)\n\t"
    "ds_write_b128 v11, v[92:95] offset:15360\n\t"
    "s_waitcnt vmcnt(21)\n\t"
    "ds_write_b128 v11, v[96:99] offset:16384\n\t"
    "s_waitcnt vmcnt(20)\n\t"
    "ds_write_b128 v11, v[100:103] offset:17408\n\t"
    "s_waitcnt vmcnt(19)\n\t"
    "ds_write_b128 v11, v[104:107] offset:18432\n\t"
    "s_waitcnt vmcnt(18)\n\t"
    "ds_write_b128 v11, v[108:111] offset:19456\n\t"
    "s_waitcnt vmcnt(17)\n\t"
    "ds_write_b128 v11, v[112:115] offset:20480\n\t"
    "s_waitcnt vmcnt(16)\n\t"
    "ds_write_b128 v11, v[116:119] offset:21504\n\t"
    "s_waitcnt vmcnt(15)\n\t"
    "ds_write_b128 v11, v[120:123] offset:22528\n\t"
    "s_waitcnt vmcnt(14)\n\t"
    "ds_write_b128 v11, v[124:127] offset:23552\n\t"
    "s_waitcnt lgkmcnt(0)\n\t"
    "s_barrier\n\t"
    "s_waitcnt vmcnt(12)\n\t"
    "s_mov_b32 s60, 0x10000\n\t"
    ".Lp7_%=:\n\t"
    "v_cmp_eq_u64_e32 vcc, v[12:13], v[22:23]\n\t"
    "v_cmp_eq_u64_e64 s[70:71], v[14:15], v[22:23]\n\t"
    "s_and_b64 vcc, vcc, s[70:71]\n\t"
    "s_cmp_eq_u64 vcc, exec\n\t"
    "s_cbranch_scc1 .Lk8_%=\n\t"
    "s_sleep 4\n\t"
    "buffer_load_dwordx2 v[12:13], v21, s[56:59], s64 offen sc1\n\t"
    "buffer_load_dwordx2 v[14:15], v21, s[56:59], s65 offen sc1\n\t"
    "s_sub_u32 s60, s60, 1\n\t"
    "s_cmp_lg_u32 s60, 0\n\t"
    "s_waitcnt vmcnt(0)\n\t"
    "s_cbranch_scc1 .Lp7_%=\n\t"
    ".Lk8_%=:\n\t"
    "s_add_u32 s67, %[tch], 73728\n\t"
    "s_mov_b64 exec, 0x1ff\n\t"
    "buffer_load_dword v10, v26, s[56:59], s67 offen sc1\n\t"
    "s_mov_b64 exec, -1\n\t"
    "buffer_load_dwordx4 a[0:3], v9, s[56:59], s63 offen offset:0 sc1\n\t"
    "buffer_load_dwordx4 a[4:7], v9, s[56:59], s63 offen offset:1024 sc1\n\t"
    "buffer_load_dwordx4 a[8:11], v9, s[56:59], s63 offen offset:2048 sc1\n\t"
    "buffer_load_dwordx4 a[12:15], v9, s[56:59], s63 offen offset:3072 sc1\n\t"
    "s_add_u32 s63, s63, 4096\n\t"
    "s_waitcnt vmcnt(16)\n\t"
    "ds_write_b128 v11, v[128:131] offset:24576\n\t"
    "buffer_load_dwordx4 a[16:19], v9, s[56:59], s63 offen offset:0 sc1\n\t"
    "s_waitcnt vmcnt(16)\n\t"
    "ds_write_b128 v11, v[132:135] offset:25600\n\t"
    "buffer_load_dwordx4 a[20:23], v9, s[56:59], s63 offen offset:1024 sc1\n\t"
    "s_waitcnt vmcnt(16)\n\t"
    "ds_write_b128 v11, v[136:139] offset:26624\n\t"
    "buffer_load_dwordx4 a[24:27], v9, s[56:59], s63 offen offset:2048 sc1\n\t"
    "s_waitcnt vmcnt(16)\n\t"
    "ds_write_b128 v11, v[140:143] offset:27648\n\t"
    "buffer_load_dwordx4 a[28:31], v9, s[56:59], s63 offen offset:3072 sc1\n\t"
    "s_add_u32 s63, s63, 4096\n\t"
    "s_waitcnt vmcnt(16)\n\t"
    "ds_write_b128 v11, v[144:147] offset:28672\n\t"
    "buffer_load_dwordx4 a[32:35], v9, s[56:59], s63 offen offset:0 sc1\n\t"
    "s_waitcnt vmcnt(16)\n\t"
    "ds_write_b128 v11, v[148:151] offset:29696\n\t"
    "buffer_load_dwordx4 a[36:39], v9, s[56:59], s63 offen offset:1024 sc1\n\t"
    "s_waitcnt vmcnt(16)\n\t"
    "ds_write_b128 v11, v[152:155] offset:30720\n\t"
    "buffer_load_dwordx4 a[40:43], v9, s[56:59], s63 offen offset:2048 sc1\n\t"
    "s_waitcnt vmcnt(16)\n\t"
    "ds_write_b128 v11, v[156:159] offset:31744\n\t"
    "buffer_load_dwordx4 a[44:47], v9, s[56:59], s63 offen offset:3072 sc1\n\t"
    "s_add_u32 s63, s63, 4096\n\t"
    "s_waitcnt vmcnt(16)\n\t"
    "ds_write_b128 v11, v[160:163] offset:32768\n\t"
    "buffer_load_dwordx4 a[48:51], v9, s[56:59], s63 offen offset:0 sc1\n\t"
    "s_waitcnt vmcnt(16)\n\t"
    "ds_write_b128 v11, v[164:167] offset:33792\n\t"
    "buffer_load_dwordx4 a[52:55], v9, s[56:59], s63 offen offset:1024 sc1\n\t"
    "s_waitcnt vmcnt(16)\n\t"
    "ds_write_b128 v11, v[168:171] offset:34816\n\t"
    "buffer_load_dwordx4 a[56:59], v9, s[56:59], s63 offen offset:2048 sc1\n\t"
    "s_waitcnt vmcnt(16)\n\t"
    "ds_write_b128 v11, v[172:175] offset:35840\n\t"
    "s_waitcnt lgkmcnt(0)\n\t"
    "s_barrier\n\t"
    "buffer_load_dwordx4 a[60:63], v9, s[56:59], s63 offen offset:3072 sc1\n\t"
    "s_add_u32 s63, s63, 4096\n\t"
    "buffer_load_dwordx4 a[64:67], v9, s[56:59], s63 offen offset:0 sc1\n\t"
    "buffer_load_dwordx4 a[68:71], v9, s[56:59], s63 offen offset:1024 sc1\n\t"
    "buffer_load_dwordx4 a[72:75], v9, s[56:59], s63 offen offset:2048 sc1\n\t"
    "buffer_load_dwordx4 a[76:79], v9, s[56:59], s63 offen offset:3072 sc1\n\t"
    "s_add_u32 s63, s63, 4096\n\t"
    "buffer_load_dwordx4 a[80:83], v9, s[56:59], s63 offen offset:0 sc1\n\t"
    "buffer_load_dwordx4 a[84:87], v9, s[56:59], s63 offen offset:1024 sc1\n\t"
    "buffer_load_dwordx4 a[88:91], v9, s[56:59], s63 offen offset:2048 sc1\n\t"
    "buffer_load_dwordx4 a[92:95], v9, s[56:59], s63 offen offset:3072 sc1\n\t"
    "s_add_u32 s63, s63, 4096\n\t"
    "buffer_load_dwordx4 a[96:99], v9, s[56:59], s63 offen offset:0 sc1\n\t"
    "buffer_load_dwordx4 a[100:103], v9, s[56:59], s63 offen offset:1024 sc1\n\t"
    "buffer_load_dwordx4 a[104:107], v9, s[56:59], s63 offen offset:2048 sc1\n\t"
    "buffer_load_dwordx4 a[108:111], v9, s[56:59], s63 offen offset:3072 sc1\n\t"
    "s_add_u32 s63, s63, 4096\n\t"
    "buffer_load_dwordx4 a[112:115], v9, s[56:59], s63 offen offset:0 sc1\n\t"
    "buffer_load_dwordx4 a[116:119], v9, s[56:59], s63 offen offset:1024 sc1\n\t"
    "buffer_load_dwordx4 a[120:123], v9, s[56:59], s63 offen offset:2048 sc1\n\t"
    "buffer_load_dwordx4 a[124:127], v9, s[56:59], s63 offen offset:3072 sc1\n\t"
    "s_add_u32 s63, s63, 4096\n\t"
    "buffer_load_dwordx4 a[128:131], v9, s[56:59], s63 offen offset:0 sc1\n\t"
    "buffer_load_dwordx4 a[132:135], v9, s[56:59], s63 offen offset:1024 sc1\n\t"
    "buffer_load_dwordx4 a[136:139], v9, s[56:59], s63 offen offset:2048 sc1\n\t"
    "buffer_load_dwordx4 a[140:143], v9, s[56:59], s63 offen offset:3072 sc1\n\t"
    "s_add_u32 s63, s63, 4096\n\t"
    "buffer_load_dwordx4 a[144:147], v9, s[56:59], s63 offen offset:0 sc1\n\t"
    "buffer_load_dwordx4 a[148:151], v9, s[56:59], s63 offen offset:1024 sc1\n\t"
    "buffer_load_dwordx4 a[152:155], v9, s[56:59], s63 offen offset:2048 sc1\n\t"
    "buffer_load_dwordx4 a[156:159], v9, s[56:59], s63 offen offset:3072 sc1\n\t"
    "s_add_u32 s63, s63, 4096\n\t"
    "buffer_load_dwordx4 a[160:163], v9, s[56:59], s63 offen offset:0 sc1\n\t"
    "buffer_load_dwordx4 a[164:167], v9, s[56:59], s63 offen offset:1024 sc1\n\t"
    "buffer_load_dwordx4 a[168:171], v9, s[56:59], s63 offen offset:2048 sc1\n\t"
    "buffer_load_dwordx4 a[172:175], v9, s[56:59], s63 offen offset:3072 sc1\n\t"
    "s_add_u32 s63, s63, 4096\n\t"
    "buffer_load_dwordx4 a[176:179], v9, s[56:59], s63 offen offset:0 sc1\n\t"
    "buffer_load_dwordx4 a[180:183], v9, s[56:59], s63 offen offset:1024 sc1\n\t"
    "buffer_load_dwordx4 a[184:187], v9, s[56:59], s63 offen offset:2048 sc1\n\t"
    "buffer_load_dwordx4 a[188:191], v9, s[56:59], s63 offen offset:3072 sc1\n\t"
    "s_add_u32 s63, s63, 4096\n\t"
    "buffer_load_dwordx4 a[192:195], v9, s[56:59], s63 offen offset:0 sc1\n\t"
    "buffer_load_dwordx4 a[196:199], v9, s[56:59], s63 offen offset:1024 sc1\n\t"
    "buffer_load_dwordx4 a[200:203], v9, s[56:59], s63 offen offset:2048 sc1\n\t"
    "buffer_load_dwordx4 a[204:207], v9, s[56:59], s63 offen offset:3072 sc1\n\t"
    "s_add_u32 s63, s63, 4096\n\t"
    "buffer_load_dwordx4 a[208:211], v9, s[56:59], s63 offen offset:0 sc1\n\t"
    "buffer_load_dwordx4 a[212:215], v9, s[56:59], s63 offen offset:1024 sc1\n\t"
    "buffer_load_dwordx4 a[216:219], v9, s[56:59], s63 offen offset:2048 sc1\n\t"
    "buffer_load_dwordx4 a[220:223], v9, s[56:59], s63 offen offset:3072 sc1\n\t"
    "s_add_u32 s63, s63, 4096\n\t"
    "buffer_load_dwordx4 a[224:227], v9, s[56:59], s63 offen offset:0 sc1\n\t"
    "buffer_load_dwordx4 a[228:231], v9, s[56:59], s63 offen offset:1024 sc1\n\t"
    "buffer_load_dwordx4 a[232:235], v9, s[56:59], s63 offen offset:2048 sc1\n\t"
    "buffer_load_dwordx4 a[236:239], v9, s[56:59], s63 offen offset:3072 sc1\n\t"
    "s_add_u32 s63, s63, 4096\n\t"
    "buffer_load_dwordx4 a[240:243], v9, s[56:59], s63 offen offset:0 sc1\n\t"
    "buffer_load_dwordx4 a[244:247], v9, s[56:59], s63 offen offset:1024 sc1\n\t"
    "buffer_load_dwordx4 a[248:251], v9, s[56:59], s63 offen offset:2048 sc1\n\t"
    "buffer_load_dwordx4 a[252:255], v9, s[56:59], s63 offen offset:3072 sc1\n\t"
    "s_add_u32 s63, s63, 4096\n\t"
    "buffer_load_dwordx4 v[224:227], v9, s[56:59], s63 offen offset:0 sc1\n\t"
    "buffer_load_dwordx4 v[228:231], v9, s[56:59], s63 offen offset:1024 sc1\n\t"
    "buffer_load_dwordx4 v[232:235], v9, s[56:59], s63 offen offset:2048 sc1\n\t"
    "buffer_load_dwordx4 v[236:239], v9, s[56:59], s63 offen offset:3072 sc1\n\t"
    "s_add_u32 s63, s63, 4096\n\t"
    "buffer_load_dwordx4 v[240:243], v9, s[56:59], s63 offen offset:0 sc1\n\t"
    "buffer_load_dwordx4 v[244:247], v9, s[56:59], s63 offen offset:1024 sc1\n\t"
    "buffer_load_dwordx4 v[248:251], v9, s[56:59], s63 offen offset:2048 sc1\n\t"
    "buffer_load_dwordx4 v[252:255], v9, s[56:59], s63 offen offset:3072 sc1\n\t"
    "s_add_u32 s63, s63, 4096\n\t"
    ".Lh1_%=:\n\t"
    "s_cbranch_execz .Lh2_%=\n\t"
    "s_barrier\n\t"
    "s_mov_b64 exec, %[amask]\n\t"
    "buffer_store_dwordx2 v[22:23], off, s[56:59], %[aown] sc1\n\t"
    "s_mov_b64 exec, -1\n\t"
    "ds_read_b128 v[72:75], v0 offset:0\n\t"
    "ds_read_b128 v[76:79], v1 offset:0\n\t"
    "ds_read_b128 v[80:83], v2 offset:0\n\t"
    "ds_read_b128 v[84:87], v3 offset:0\n\t"
    "ds_read_b128 v[88:91], v4 offset:0\n\t"
    "ds_read_b128 v[92:95], v5 offset:0\n\t"
    "ds_read_b128 v[96:99], v5 offset:6144\n\t"
    "ds_read_b128 v[100:103], v4 offset:6144\n\t"
    "ds_read_b128 v[104:107], v3 offset:6144\n\t"
    "ds_read_b128 v[108:111], v2 offset:6144\n\t"
    "ds_read_b128 v[156:159], v1 offset:6144\n\t"
    "ds_read_b128 v[188:191], v0 offset:6144\n\t"
    "s_add_u32 s61, %[wb], 0xff400000\n\t"
    "s_mov_b32 s62, %[rbeg]\n\t"
    "s_mov_b32 s60, 8\n\t"
    ".Lmin_%=:\n\t"
    "s_waitcnt lgkmcnt(7)\n\t"
    "s_waitcnt vmcnt(63)\n\t"
    "v_mfma_f32_16x16x32_bf16 v[24:27], v[72:75], a[0:3], 0\n\t"
    "buffer_store_dwordx2 v[120:121], v8, s[52:55], s61 offen offset:768 sc1\n\t"
    "s_waitcnt lgkmcnt(10)\n\t"
    "v_mfma_f32_16x16x32_bf16 v[24:27], v[76:79], a[4:7], v[24:27]\n\t"
    "v_cvt_pk_bf16_f32 v195, v31, v35\n\t"
    "v_cvt_pk_bf16_f32 v128, v36, v16\n\t"
    "s_waitcnt lgkmcnt(9)\n\t"
    "v_mfma_f32_16x16x32_bf16 v[24:27], v[80:83], a[8:11], v[24:27]\n\t"
    "v_cvt_pk_bf16_f32 v130, v37, v17\n\t"
    "v_cvt_pk_bf16_f32 v12, v38, v18\n\t"
    "s_waitcnt lgkmcnt(8)\n\t"
    "v_mfma_f32_16x16x32_bf16 v[24:27], v[84:87], a[12:15], v[24:27]\n\t"
    "v_cvt_pk_bf16_f32 v14, v39, v19\n\t"
    "v_cvt_pk_bf16_f32 v129, v40, v20\n\t"
    "s_waitcnt lgkmcnt(7)\n\t"
    "v_mfma_f32_16x16x32_bf16 v[24:27], v[88:91], a[16:19], v[24:27]\n\t"
    "v_cvt_pk_bf16_f32 v131, v41, v21\n\t"
    "s_waitcnt lgkmcnt(2)\n\t"
    "v_mfma_f32_16x16x32_bf16 v[24:27], v[92:95], a[20:23], v[24:27]\n\t"
    "v_cvt_pk_bf16_f32 v13, v42, v22\n\t"
    "v_cvt_pk_bf16_f32 v15, v43, v23\n\t"
    "s_waitcnt lgkmcnt(5)\n\t"
    "v_mfma_f32_16x16x32_bf16 v[28:31], v[96:99], a[20:23], 0\n\t"
    "s_waitcnt lgkmcnt(0)\n\t"
    "v_mfma_f32_16x16x32_bf16 v[28:31], v[100:103], a[16:19], v[28:31]\n\t"
    "v_mfma_f32_16x16x32_bf16 v[28:31], v[104:107], a[12:15], v[28:31]\n\t"
    "v_mfma_f32_16x16x32_bf16 v[28:31], v[108:111], a[8:11], v[28:31]\n\t"
    "v_cvt_pk_bf16_f32 v16, v24, v25\n\t"
    "s_waitcnt lgkmcnt(0)\n\t"
    "v_mfma_f32_16x16x32_bf16 v[28:31], v[156:159], a[4:7], v[28:31]\n\t"
    "v_cvt_pk_bf16_f32 v17, v26, v27\n\t"
    "v_mfma_f32_16x16x32_bf16 v[28:31], v[188:191], a[0:3], v[28:31]\n\t"
    "s_waitcnt vmcnt(62)\n\t"
    "v_mfma_f32_16x16x32_bf16 v[32:35], v[188:191], a[24:27], 0\n\t"
    "buffer_store_dwordx2 v[122:123], v8, s[52:55], s61 offen offset:1152 sc1\n\t"
    "v_mfma_f32_16x16x32_bf16 v[32:35], v[156:159], a[28:31], v[32:35]\n\t"
    "s_add_u32 s61, s61, 393216\n\t"
    "v_mfma_f32_16x16x32_bf16 v[32:35], v[108:111], a[32:35], v[32:35]\n\t"
    "v_mfma_f32_16x16x32_bf16 v[32:35], v[104:107], a[36:39], v[32:35]\n\t"
    "v_cvt_pk_bf16_f32 v18, v28, v29\n\t"
    "v_cvt_pk_bf16_f32 v19, v30, v31\n\t"
    "v_mfma_f32_16x16x32_bf16 v[32:35], v[100:103], a[40:43], v[32:35]\n\t"
    "v_mfma_f32_16x16x32_bf16 v[32:35], v[96:99], a[44:47], v[32:35]\n\t"
    "v_mfma_f32_16x16x32_bf16 v[24:27], v[92:95], a[44:47], 0\n\t"
    "v_mfma_f32_16x16x32_bf16 v[24:27], v[88:91], a[40:43], v[24:27]\n\t"
    "v_mfma_f32_16x16x32_bf16 v[24:27], v[84:87], a[36:39], v[24:27]\n\t"
    "v_mfma_f32_16x16x32_bf16 v[24:27], v[80:83], a[32:35], v[24:27]\n\t"
    "v_cvt_pk_bf16_f32 v22, v32, v33\n\t"
    "v_cvt_pk_bf16_f32 v23, v34, v35\n\t"
    "v_mfma_f32_16x16x32_bf16 v[24:27], v[76:79], a[28:31], v[24:27]\n\t"
    "v_mfma_f32_16x16x32_bf16 v[24:27], v[72:75], a[24:27], v[24:27]\n\t"
    "s_waitcnt vmcnt(57)\n\t"
    "v_mfma_f32_16x16x32_bf16 v[28:31], v[72:75], a[48:51], 0\n\t"
    "buffer_store_dwordx4 v[140:143], v7, s[52:55], s61 offen offset:0 sc1\n\t"
    "v_mfma_f32_16x16x32_bf16 v[28:31], v[76:79], a[52:55], v[28:31]\n\t"
    "v_mfma_f32_16x16x32_bf16 v[28:31], v[80:83], a[56:59], v[28:31]\n\t"
    "v_mfma_f32_16x16x32_bf16 v[28:31], v[84:87], a[60:63], v[28:31]\n\t"
    "v_cvt_pk_bf16_f32 v20, v24, v25\n\t"
    "v_cvt_pk_bf16_f32 v21, v26, v27\n\t"
    "v_mfma_f32_16x16x32_bf16 v[28:31], v[88:91], a[64:67], v[28:31]\n\t"
    "v_mfma_f32_16x16x32_bf16 v[28:31], v[92:95], a[68:71], v[28:31]\n\t"
    "v_mfma_f32_16x16x32_bf16 v[32:35], v[96:99], a[68:71], 0\n\t"
    "v_mfma_f32_16x16x32_bf16 v[32:35], v[100:103], a[64:67], v[32:35]\n\t"
    "v_mfma_f32_16x16x32_bf16 v[32:35], v[104:107], a[60:63], v[32:35]\n\t"
    "v_mfma_f32_16x16x32_bf16 v[32:35], v[108:111], a[56:59], v[32:35]\n\t"
    "v_cvt_pk_bf16_f32 v24, v28, v29\n\t"
    "v_cvt_pk_bf16_f32 v25, v30, v31\n\t"
    "v_mfma_f32_16x16x32_bf16 v[32:35], v[156:159], a[52:55], v[32:35]\n\t"
    "v_mfma_f32_16x16x32_bf16 v[32:35], v[188:191], a[48:51], v[32:35]\n\t"
    "s_waitcnt vmcnt(52)\n\t"
    "v_mfma_f32_16x16x32_bf16 v[36:39], v[188:191], a[72:75], 0\n\t"
    "buffer_store_dwordx4 v[124:127], v7, s[52:55], s61 offen offset:384 sc1\n\t"
    "v_mfma_f32_16x16x32_bf16 v[36:39], v[156:159], a[76:79], v[36:39]\n\t"
    "v_mfma_f32_16x16x32_bf16 v[36:39], v[108:111], a[80:83], v[36:39]\n\t"
    "v_mfma_f32_16x16x32_bf16 v[36:39], v[104:107], a[84:87], v[36:39]\n\t"
    "v_cvt_pk_bf16_f32 v26, v32, v33\n\t"
    "v_cvt_pk_bf16_f32 v27, v34, v35\n\t"
    "v_mfma_f32_16x16x32_bf16 v[36:39], v[100:103], a[88:91], v[36:39]\n\t"
    "v_mfma_f32_16x16x32_bf16 v[36:39], v[96:99], a[92:95], v[36:39]\n\t"
    "v_mfma_f32_16x16x32_bf16 v[32:35], v[92:95], a[92:95], 0\n\t"
    "v_mfma_f32_16x16x32_bf16 v[32:35], v[88:91], a[88:91], v[32:35]\n\t"
    "v_mfma_f32_16x16x32_bf16 v[32:35], v[84:87], a[84:87], v[32:35]\n\t"
    "v_mfma_f32_16x16x32_bf16 v[32:35], v[80:83], a[80:83], v[32:35]\n\t"
    "v_cvt_pk_bf16_f32 v30, v36, v37\n\t"
    "v_cvt_pk_bf16_f32 v31, v38, v39\n\t"
    "v_mfma_f32_16x16x32_bf16 v[32:35], v[76:79], a[76:79], v[32:35]\n\t"
    "v_mfma_f32_16x16x32_bf16 v[32:35], v[72:75], a[72:75], v[32:35]\n\t"
    "s_waitcnt vmcnt(47)\n\t"
    "v_mfma_f32_16x16x32_bf16 v[36:39], v[72:75], a[96:99], 0\n\t"
    "buffer_store_dwordx4 v[164:167], v7, s[52:55], s61 offen offset:768 sc1\n\t"
    "v_mfma_f32_16x16x32_bf16 v[36:39], v[76:79], a[100:103], v[36:39]\n\t"
    "v_mfma_f32_16x16x32_bf16 v[36:39], v[80:83], a[104:107], v[36:39]\n\t"
    "v_mfma_f32_16x16x32_bf16 v[36:39], v[84:87], a[108:111], v[36:39]\n\t"
    "v_cvt_pk_bf16_f32 v28, v32, v33\n\t"
    "v_cvt_pk_bf16_f32 v29, v34, v35\n\t"
    "v_mfma_f32_16x16x32_bf16 v[36:39], v[88:91], a[112:115], v[36:39]\n\t"
    "v_mfma_f32_16x16x32_bf16 v[36:39], v[92:95], a[116:119], v[36:39]\n\t"
    "v_mfma_f32_16x16x32_bf16 v[40:43], v[96:99], a[116:119], 0\n\t"
    "v_mfma_f32_16x16x32_bf16 v[40:43], v[100:103], a[112:115], v[40:43]\n\t"
    "v_mfma_f32_16x16x32_bf16 v[40:43], v[104:107], a[108:111], v[40:43]\n\t"
    "v_mfma_f32_16x16x32_bf16 v[40:43], v[108:111], a[104:107], v[40:43]\n\t"
    "v_cvt_pk_bf16_f32 v32, v36, v37\n\t"
    "v_cvt_pk_bf16_f32 v33, v38, v39\n\t"
    "v_mfma_f32_16x16x32_bf16 v[40:43], v[156:159], a[100:103], v[40:43]\n\t"
    "v_mfma_f32_16x16x32_bf16 v[40:43], v[188:191], a[96:99], v[40:43]\n\t"
    "s_waitcnt vmcnt(42)\n\t"
    "v_mfma_f32_16x16x32_bf16 v[44:47], v[188:191], a[120:123], 0\n\t"
    "buffer_store_dwordx4 v[152:155], v7, s[52:55], s61 offen offset:1152 sc1\n\t"
    "v_mfma_f32_16x16x32_bf16 v[44:47], v[156:159], a[124:127], v[44:47]\n\t"
    "v_mfma_f32_16x16x32_bf16 v[44:47], v[108:111], a[128:131], v[44:47]\n\t"
    "v_mfma_f32_16x16x32_bf16 v[44:47], v[104:107], a[132:135], v[44:47]\n\t"
    "v_cvt_pk_bf16_f32 v34, v40, v41\n\t"
    "v_cvt_pk_bf16_f32 v35, v42, v43\n\t"
    "v_mfma_f32_16x16x32_bf16 v[44:47], v[100:103], a[136:139], v[44:47]\n\t"
    "v_mfma_f32_16x16x32_bf16 v[44:47], v[96:99], a[140:143], v[44:47]\n\t"
    "v_mfma_f32_16x16x32_bf16 v[40:43], v[92:95], a[140:143], 0\n\t"
    "v_mfma_f32_16x16x32_bf16 v[40:43], v[88:91], a[136:139], v[40:43]\n\t"
    "v_mfma_f32_16x16x32_bf16 v[40:43], v[84:87], a[132:135], v[40:43]\n\t"
    "v_mfma_f32_16x16x32_bf16 v[40:43], v[80:83], a[128:131], v[40:43]\n\t"
    "v_cvt_pk_bf16_f32 v38, v44, v45\n\t"
    "v_cvt_pk_bf16_f32 v39, v46, v47\n\t"
    "v_mfma_f32_16x16x32_bf16 v[40:43], v[76:79], a[124:127], v[40:43]\n\t"
    "v_mfma_f32_16x16x32_bf16 v[40:43], v[72:75], a[120:123], v[40:43]\n\t"
    "s_waitcnt vmcnt(37)\n\t"
    "v_mfma_f32_16x16x32_bf16 v[44:47], v[72:75], a[144:147], 0\n\t"
    "buffer_store_dwordx2 v[116:117], v8, s[52:55], s61 offen offset:0 sc1\n\t"
    "v_mfma_f32_16x16x32_bf16 v[44:47], v[76:79], a[148:151], v[44:47]\n\t"
    "v_mfma_f32_16x16x32_bf16 v[44:47], v[80:83], a[152:155], v[44:47]\n\t"
    "v_mfma_f32_16x16x32_bf16 v[44:47], v[84:87], a[156:159], v[44:47]\n\t"
    "v_cvt_pk_bf16_f32 v36, v40, v41\n\t"
    "v_cvt_pk_bf16_f32 v37, v42, v43\n\t"
    "v_mfma_f32_16x16x32_bf16 v[44:47], v[88:91], a[160:163], v[44:47]\n\t"
    "v_mfma_f32_16x16x32_bf16 v[44:47], v[92:95], a[164:167], v[44:47]\n\t"
    "v_mfma_f32_16x16x32_bf16 v[48:51], v[96:99], a[164:167], 0\n\t"
    "v_mfma_f32_16x16x32_bf16 v[48:51], v[100:103], a[160:163], v[48:51]\n\t"
    "v_mfma_f32_16x16x32_bf16 v[48:51], v[104:107], a[156:159], v[48:51]\n\t"
    "v_mfma_f32_16x16x32_bf16 v[48:51], v[108:111], a[152:155], v[48:51]\n\t"
    "v_cvt_pk_bf16_f32 v40, v44, v45\n\t"
    "v_cvt_pk_bf16_f32 v41, v46, v47\n\t"
    "v_mfma_f32_16x16x32_bf16 v[48:51], v[156:159], a[148:151], v[48:51]\n\t"
    "v_mfma_f32_16x16x32_bf16 v[48:51], v[188:191], a[144:147], v[48:51]\n\t"
    "s_waitcnt vmcnt(32)\n\t"
    "v_mfma_f32_16x16x32_bf16 v[52:55], v[188:191], a[168:171], 0\n\t"
    "buffer_store_dwordx2 v[118:119], v8, s[52:55], s61 offen offset:384 sc1\n\t"
    "v_mfma_f32_16x16x32_bf16 v[52:55], v[156:159], a[172:175], v[52:55]\n\t"
    "v_mfma_f32_16x16x32_bf16 v[52:55], v[108:111], a[176:179], v[52:55]\n\t"
    "v_mfma_f32_16x16x32_bf16 v[52:55], v[104:107], a[180:183], v[52:55]\n\t"
    "v_cvt_pk_bf16_f32 v42, v48, v49\n\t"
    "v_cvt_pk_bf16_f32 v43, v50, v51\n\t"
    "v_mfma_f32_16x16x32_bf16 v[52:55], v[100:103], a[184:187], v[52:55]\n\t"
    "v_mfma_f32_16x16x32_bf16 v[52:55], v[96:99], a[188:191], v[52:55]\n\t"
    "v_mfma_f32_16x16x32_bf16 v[48:51], v[92:95], a[188:191], 0\n\t"
    "v_mfma_f32_16x16x32_bf16 v[48:51], v[88:91], a[184:187], v[48:51]\n\t"
    "v_mfma_f32_16x16x32_bf16 v[48:51], v[84:87], a[180:183], v[48:51]\n\t"
    "v_mfma_f32_16x16x32_bf16 v[48:51], v[80:83], a[176:179], v[48:51]\n\t"
    "v_cvt_pk_bf16_f32 v46, v52, v53\n\t"
    "v_cvt_pk_bf16_f32 v47, v54, v55\n\t"
    "v_mfma_f32_16x16x32_bf16 v[48:51], v[76:79], a[172:175], v[48:51]\n\t"
    "v_mfma_f32_16x16x32_bf16 v[48:51], v[72:75], a[168:171], v[48:51]\n\t"
    "s_waitcnt vmcnt(27)\n\t"
    "v_mfma_f32_16x16x32_bf16 v[52:55], v[72:75], a[192:195], 0\n\t"
    "buffer_store_dwordx2 v[160:161], v8, s[52:55], s61 offen offset:768 sc1\n\t"
    "v_mfma_f32_16x16x32_bf16 v[52:55], v[76:79], a[196:199], v[52:55]\n\t"
    "v_mfma_f32_16x16x32_bf16 v[52:55], v[80:83], a[200:203], v[52:55]\n\t"
    "v_mfma_f32_16x16x32_bf16 v[52:55], v[84:87], a[204:207], v[52:55]\n\t"
    "v_cvt_pk_bf16_f32 v44, v48, v49\n\t"
    "v_cvt_pk_bf16_f32 v45, v50, v51\n\t"
    "v_mfma_f32_16x16x32_bf16 v[52:55], v[88:91], a[208:211], v[52:55]\n\t"
    "v_mfma_f32_16x16x32_bf16 v[52:55], v[92:95], a[212:215], v[52:55]\n\t"
    "v_mfma_f32_16x16x32_bf16 v[56:59], v[96:99], a[212:215], 0\n\t"
    "v_mfma_f32_16x16x32_bf16 v[56:59], v[100:103], a[208:211], v[56:59]\n\t"
    "v_mfma_f32_16x16x32_bf16 v[56:59], v[104:107], a[204:207], v[56:59]\n\t"
    "v_mfma_f32_16x16x32_bf16 v[56:59], v[108:111], a[200:203], v[56:59]\n\t"
    "v_cvt_pk_bf16_f32 v48, v52, v53\n\t"
    "v_cvt_pk_bf16_f32 v49, v54, v55\n\t"
    "v_mfma_f32_16x16x32_bf16 v[56:59], v[156:159], a[196:199], v[56:59]\n\t"
    "v_mfma_f32_16x16x32_bf16 v[56:59], v[188:191], a[192:195], v[56:59]\n\t"
    "s_waitcnt vmcnt(22)\n\t"
    "v_mfma_f32_16x16x32_bf16 v[60:63], v[188:191], a[216:219], 0\n\t"
    "buffer_store_dwordx2 v[162:163], v8, s[52:55], s61 offen offset:1152 sc1\n\t"
    "v_mfma_f32_16x16x32_bf16 v[60:63], v[156:159], a[220:223], v[60:63]\n\t"
    "s_add_u32 s61, s61, 393216\n\t"
    "v_mfma_f32_16x16x32_bf16 v[60:63], v[108:111], a[224:227], v[60:63]\n\t"
    "v_mfma_f32_16x16x32_bf16 v[60:63], v[104:107], a[228:231], v[60:63]\n\t"
    "v_cvt_pk_bf16_f32 v50, v56, v57\n\t"
    "v_cvt_pk_bf16_f32 v51, v58, v59\n\t"
    "v_mfma_f32_16x16x32_bf16 v[60:63], v[100:103], a[232:235], v[60:63]\n\t"
    "v_mfma_f32_16x16x32_bf16 v[60:63], v[96:99], a[236:239], v[60:63]\n\t"
    "v_mfma_f32_16x16x32_bf16 v[56:59], v[92:95], a[236:239], 0\n\t"
    "v_mfma_f32_16x16x32_bf16 v[56:59], v[88:91], a[232:235], v[56:59]\n\t"
    "v_mfma_f32_16x16x32_bf16 v[56:59], v[84:87], a[228:231], v[56:59]\n\t"
    "v_mfma_f32_16x16x32_bf16 v[56:59], v[80:83], a[224:227], v[56:59]\n\t"
    "v_cvt_pk_bf16_f32 v54, v60, v61\n\t"
    "v_cvt_pk_bf16_f32 v55, v62, v63\n\t"
    "v_mfma_f32_16x16x32_bf16 v[56:59], v[76:79], a[220:223], v[56:59]\n\t"
    "v_mfma_f32_16x16x32_bf16 v[56:59], v[72:75], a[216:219], v[56:59]\n\t"
    "s_waitcnt vmcnt(17)\n\t"
    "v_mfma_f32_16x16x32_bf16 v[60:63], v[72:75], a[240:243], 0\n\t"
    "buffer_store_dwordx4 v[64:67], v7, s[52:55], s61 offen offset:0 sc1\n\t"
    "v_mfma_f32_16x16x32_bf16 v[60:63], v[76:79], a[244:247], v[60:63]\n\t"
    "v_mfma_f32_16x16x32_bf16 v[60:63], v[80:83], a[248:251], v[60:63]\n\t"
    "v_mfma_f32_16x16x32_bf16 v[60:63], v[84:87], a[252:255], v[60:63]\n\t"
    "v_cvt_pk_bf16_f32 v52, v56, v57\n\t"
    "v_cvt_pk_bf16_f32 v53, v58, v59\n\t"
    "v_mfma_f32_16x16x32_bf16 v[60:63], v[88:91], v[224:227], v[60:63]\n\t"
    "v_mfma_f32_16x16x32_bf16 v[60:63], v[92:95], v[228:231], v[60:63]\n\t"
    "v_mfma_f32_16x16x32_bf16 v[68:71], v[96:99], v[228:231], 0\n\t"
    "v_mfma_f32_16x16x32_bf16 v[68:71], v[100:103], v[224:227], v[68:71]\n\t"
    "v_mfma_f32_16x16x32_bf16 v[68:71], v[104:107], a[252:255], v[68:71]\n\t"
    "v_mfma_f32_16x16x32_bf16 v[68:71], v[108:111], a[248:251], v[68:71]\n\t"
    "v_cvt_pk_bf16_f32 v56, v60, v61\n\t"
    "v_cvt_pk_bf16_f32 v57, v62, v63\n\t"
    "v_mfma_f32_16x16x32_bf16 v[68:71], v[156:159], a[244:247], v[68:71]\n\t"
    "v_mfma_f32_16x16x32_bf16 v[68:71], v[188:191], a[240:243], v[68:71]\n\t"
    "s_waitcnt vmcnt(12)\n\t"
    "v_mfma_f32_16x16x32_bf16 v[112:115], v[188:191], v[232:235], 0\n\t"
    "ds_read_b128 v[60:63], v0 offset:12288\n\t"
    "buffer_store_dwordx4 v[168:171], v7, s[52:55], s61 offen offset:384 sc1\n\t"
    "v_mfma_f32_16x16x32_bf16 v[112:115], v[156:159], v[236:239], v[112:115]\n\t"
    "ds_read_b128 v[64:67], v1 offset:12288\n\t"
    "v_mfma_f32_16x16x32_bf16 v[112:115], v[108:111], v[240:243], v[112:115]\n\t"
    "ds_read_b128 v[140:143], v2 offset:12288\n\t"
    "v_mfma_f32_16x16x32_bf16 v[112:115], v[104:107], v[244:247], v[112:115]\n\t"
    "ds_read_b128 v[144:147], v3 offset:12288\n\t"
    "v_cvt_pk_bf16_f32 v58, v68, v69\n\t"
    "v_mfma_f32_16x16x32_bf16 v[112:115], v[100:103], v[248:251], v[112:115]\n\t"
    "ds_read_b128 v[152:155], v4 offset:12288\n\t"
    "v_cvt_pk_bf16_f32 v59, v70, v71\n\t"
    "v_mfma_f32_16x16x32_bf16 v[112:115], v[96:99], v[252:255], v[112:115]\n\t"
    "ds_read_b128 v[156:159], v5 offset:12288\n\t"
    "v_mfma_f32_16x16x32_bf16 v[104:107], v[92:95], v[252:255], 0\n\t"
    "ds_read_b128 v[160:163], v5 offset:18432\n\t"
    "v_mfma_f32_16x16x32_bf16 v[104:107], v[88:91], v[248:251], v[104:107]\n\t"
    "ds_read_b128 v[164:167], v4 offset:18432\n\t"
    "v_mfma_f32_16x16x32_bf16 v[104:107], v[84:87], v[244:247], v[104:107]\n\t"
    "ds_read_b128 v[124:127], v3 offset:18432\n\t"
    "v_mfma_f32_16x16x32_bf16 v[104:107], v[80:83], v[240:243], v[104:107]\n\t"
    "ds_read_b128 v[168:171], v2 offset:18432\n\t"
    "v_cvt_pk_bf16_f32 v70, v112, v113\n\t"
    "v_mfma_f32_16x16x32_bf16 v[104:107], v[76:79], v[236:239], v[104:107]\n\t"
    "ds_read_b128 v[172:175], v1 offset:18432\n\t"
    "v_cvt_pk_bf16_f32 v71, v114, v115\n\t"
    "v_mfma_f32_16x16x32_bf16 v[104:107], v[72:75], v[232:235], v[104:107]\n\t"
    "ds_read_b128 v[188:191], v0 offset:18432\n\t"
    "s_waitcnt lgkmcnt(7)\n\t"
    "v_mfma_f32_16x16x32_bf16 v[80:83], v[60:63], a[0:3], 0\n\t"
    "buffer_store_dwordx4 v[180:183], v7, s[52:55], s61 offen offset:768 sc1\n\t"
    "v_mfma_f32_16x16x32_bf16 v[80:83], v[64:67], a[4:7], v[80:83]\n\t"
    "v_mfma_f32_16x16x32_bf16 v[80:83], v[140:143], a[8:11], v[80:83]\n\t"
    "v_mfma_f32_16x16x32_bf16 v[80:83], v[144:147], a[12:15], v[80:83]\n\t"
    "v_cvt_pk_bf16_f32 v68, v104, v105\n\t"
    "v_cvt_pk_bf16_f32 v69, v106, v107\n\t"
    "v_mfma_f32_16x16x32_bf16 v[80:83], v[152:155], a[16:19], v[80:83]\n\t"
    "s_waitcnt lgkmcnt(2)\n\t"
    "v_mfma_f32_16x16x32_bf16 v[80:83], v[156:159], a[20:23], v[80:83]\n\t"
    "v_mfma_f32_16x16x32_bf16 v[76:79], v[160:163], a[20:23], 0\n\t"
    "v_mfma_f32_16x16x32_bf16 v[76:79], v[164:167], a[16:19], v[76:79]\n\t"
    "v_mfma_f32_16x16x32_bf16 v[76:79], v[124:127], a[12:15], v[76:79]\n\t"
    "v_mfma_f32_16x16x32_bf16 v[76:79], v[168:171], a[8:11], v[76:79]\n\t"
    "v_cvt_pk_bf16_f32 v72, v80, v81\n\t"
    "s_waitcnt lgkmcnt(0)\n\t"
    "v_mfma_f32_16x16x32_bf16 v[76:79], v[172:175], a[4:7], v[76:79]\n\t"
    "v_cvt_pk_bf16_f32 v73, v82, v83\n\t"
    "v_mfma_f32_16x16x32_bf16 v[76:79], v[188:191], a[0:3], v[76:79]\n\t"
    "v_mfma_f32_16x16x32_bf16 v[84:87], v[188:191], a[24:27], 0\n\t"
    "buffer_store_dwordx4 v[176:179], v7, s[52:55], s61 offen offset:1152 sc1\n\t"
    "v_mfma_f32_16x16x32_bf16 v[84:87], v[172:175], a[28:31], v[84:87]\n\t"
    "v_mfma_f32_16x16x32_bf16 v[84:87], v[168:171], a[32:35], v[84:87]\n\t"
    "v_mfma_f32_16x16x32_bf16 v[84:87], v[124:127], a[36:39], v[84:87]\n\t"
    "v_cvt_pk_bf16_f32 v74, v76, v77\n\t"
    "v_cvt_pk_bf16_f32 v75, v78, v79\n\t"
    "v_mfma_f32_16x16x32_bf16 v[84:87], v[164:167], a[40:43], v[84:87]\n\t"
    "v_mfma_f32_16x16x32_bf16 v[84:87], v[160:163], a[44:47], v[84:87]\n\t"
    "v_mfma_f32_16x16x32_bf16 v[80:83], v[156:159], a[44:47], 0\n\t"
    "v_mfma_f32_16x16x32_bf16 v[80:83], v[152:155], a[40:43], v[80:83]\n\t"
    "v_mfma_f32_16x16x32_bf16 v[80:83], v[144:147], a[36:39], v[80:83]\n\t"
    "v_mfma_f32_16x16x32_bf16 v[80:83], v[140:143], a[32:35], v[80:83]\n\t"
    "v_cvt_pk_bf16_f32 v78, v84, v85\n\t"
    "v_cvt_pk_bf16_f32 v79, v86, v87\n\t"
    "v_mfma_f32_16x16x32_bf16 v[80:83], v[64:67], a[28:31], v[80:83]\n\t"
    "v_mfma_f32_16x16x32_bf16 v[80:83], v[60:63], a[24:27], v[80:83]\n\t"
    "v_mfma_f32_16x16x32_bf16 v[84:87], v[60:63], a[48:51], 0\n\t"
    "buffer_store_dwordx2 v[132:133], v8, s[52:55], s61 offen offset:0 sc1\n\t"
    "v_mfma_f32_16x16x32_bf16 v[84:87], v[64:67], a[52:55], v[84:87]\n\t"
    "v_mfma_f32_16x16x32_bf16 v[84:87], v[140:143], a[56:59], v[84:87]\n\t"
    "v_mfma_f32_16x16x32_bf16 v[84:87], v[144:147], a[60:63], v[84:87]\n\t"
    "v_cvt_pk_bf16_f32 v76, v80, v81\n\t"
    "v_cvt_pk_bf16_f32 v77, v82, v83\n\t"
    "v_mfma_f32_16x16x32_bf16 v[84:87], v[152:155], a[64:67], v[84:87]\n\t"
    "v_mfma_f32_16x16x32_bf16 v[84:87], v[156:159], a[68:71], v[84:87]\n\t"
    "v_mfma_f32_16x16x32_bf16 v[88:91], v[160:163], a[68:71], 0\n\t"
    "v_mfma_f32_16x16x32_bf16 v[88:91], v[164:167], a[64:67], v[88:91]\n\t"
    "v_mfma_f32_16x16x32_bf16 v[88:91], v[124:127], a[60:63], v[88:91]\n\t"
    "v_mfma_f32_16x16x32_bf16 v[88:91], v[168:171], a[56:59], v[88:91]\n\t"
    "v_cvt_pk_bf16_f32 v80, v84, v85\n\t"
    "v_cvt_pk_bf16_f32 v81, v86, v87\n\t"
    "v_mfma_f32_16x16x32_bf16 v[88:91], v[172:175], a[52:55], v[88:91]\n\t"
    "v_mfma_f32_16x16x32_bf16 v[88:91], v[188:191], a[48:51], v[88:91]\n\t"
    "v_mfma_f32_16x16x32_bf16 v[92:95], v[188:191], a[72:75], 0\n\t"
    "s_add_i32 s64, s62, 24576\n\t"
    "buffer_store_dwordx2 v[134:135], v8, s[52:55], s61 offen offset:384 sc1\n\t"
    "v_mfma_f32_16x16x32_bf16 v[92:95], v[172:175], a[76:79], v[92:95]\n\t"
    "s_cmp_eq_u32 s64, %[rend]\n\t"
    "s_cselect_b32 s64, %[rbeg], s64\n\t"
    "v_mfma_f32_16x16x32_bf16 v[92:95], v[168:171], a[80:83], v[92:95]\n\t"
    "s_sub_i32 s65, s64, s62\n\t"
    "s_mov_b32 s62, s64\n\t"
    "v_mfma_f32_16x16x32_bf16 v[92:95], v[124:127], a[84:87], v[92:95]\n\t"
    "v_add_u32_e32 v0, s65, v0\n\t"
    "v_add_u32_e32 v1, s65, v1\n\t"
    "v_mfma_f32_16x16x32_bf16 v[92:95], v[164:167], a[88:91], v[92:95]\n\t"
    "v_add_u32_e32 v2, s65, v2\n\t"
    "v_add_u32_e32 v3, s65, v3\n\t"
    "v_mfma_f32_16x16x32_bf16 v[92:95], v[160:163], a[92:95], v[92:95]\n\t"
    "v_add_u32_e32 v4, s65, v4\n\t"
    "v_add_u32_e32 v5, s65, v5\n\t"
    "v_mfma_f32_16x16x32_bf16 v[96:99], v[156:159], a[92:95], 0\n\t"
    "v_cvt_pk_bf16_f32 v82, v88, v89\n\t"
    "v_cvt_pk_bf16_f32 v83, v90, v91\n\t"
    "v_mfma_f32_16x16x32_bf16 v[96:99], v[152:155], a[88:91], v[96:99]\n\t"
    "v_mfma_f32_16x16x32_bf16 v[96:99], v[144:147], a[84:87], v[96:99]\n\t"
    "v_mfma_f32_16x16x32_bf16 v[96:99], v[140:143], a[80:83], v[96:99]\n\t"
    "v_cvt_pk_bf16_f32 v86, v92, v93\n\t"
    "v_cvt_pk_bf16_f32 v87, v94, v95\n\t"
    "v_mfma_f32_16x16x32_bf16 v[96:99], v[64:67], a[76:79], v[96:99]\n\t"
    "v_mfma_f32_16x16x32_bf16 v[96:99], v[60:63], a[72:75], v[96:99]\n\t"
    "v_mfma_f32_16x16x32_bf16 v[92:95], v[60:63], a[96:99], 0\n\t"
    "buffer_store_dwordx2 v[136:137], v8, s[52:55], s61 offen offset:768 sc1\n\t"
    "v_mfma_f32_16x16x32_bf16 v[92:95], v[64:67], a[100:103], v[92:95]\n\t"
    "v_mfma_f32_16x16x32_bf16 v[92:95], v[140:143], a[104:107], v[92:95]\n\t"
    "v_mfma_f32_16x16x32_bf16 v[92:95], v[144:147], a[108:111], v[92:95]\n\t"
    "v_cvt_pk_bf16_f32 v84, v96, v97\n\t"
    "v_cvt_pk_bf16_f32 v85, v98, v99\n\t"
    "v_mfma_f32_16x16x32_bf16 v[92:95], v[152:155], a[112:115], v[92:95]\n\t"
    "v_mfma_f32_16x16x32_bf16 v[92:95], v[156:159], a[116:119], v[92:95]\n\t"
    "v_mfma_f32_16x16x32_bf16 v[96:99], v[160:163], a[116:119], 0\n\t"
    "v_mfma_f32_16x16x32_bf16 v[96:99], v[164:167], a[112:115], v[96:99]\n\t"
    "v_mfma_f32_16x16x32_bf16 v[96:99], v[124:127], a[108:111], v[96:99]\n\t"
    "v_mfma_f32_16x16x32_bf16 v[96:99], v[168:171], a[104:107], v[96:99]\n\t"
    "v_cvt_pk_bf16_f32 v88, v92, v93\n\t"
    "v_cvt_pk_bf16_f32 v89, v94, v95\n\t"
    "v_mfma_f32_16x16x32_bf16 v[96:99], v[172:175], a[100:103], v[96:99]\n\t"
    "v_mfma_f32_16x16x32_bf16 v[96:99], v[188:191], a[96:99], v[96:99]\n\t"
    "v_mfma_f32_16x16x32_bf16 v[100:103], v[188:191], a[120:123], 0\n\t"
    "buffer_store_dwordx2 v[138:139], v8, s[52:55], s61 offen offset:1152 sc1\n\t"
    "v_mfma_f32_16x16x32_bf16 v[100:103], v[172:175], a[124:127], v[100:103]\n\t"
    "s_add_u32 s61, s61, 393216\n\t"
    "v_mfma_f32_16x16x32_bf16 v[100:103], v[168:171], a[128:131], v[100:103]\n\t"
    "v_mfma_f32_16x16x32_bf16 v[100:103], v[124:127], a[132:135], v[100:103]\n\t"
    "v_cvt_pk_bf16_f32 v90, v96, v97\n\t"
    "v_cvt_pk_bf16_f32 v91, v98, v99\n\t"
    "v_mfma_f32_16x16x32_bf16 v[100:103], v[164:167], a[136:139], v[100:103]\n\t"
    "v_mfma_f32_16x16x32_bf16 v[100:103], v[160:163], a[140:143], v[100:103]\n\t"
    "v_mfma_f32_16x16x32_bf16 v[96:99], v[156:159], a[140:143], 0\n\t"
    "v_mfma_f32_16x16x32_bf16 v[96:99], v[152:155], a[136:139], v[96:99]\n\t"
    "v_mfma_f32_16x16x32_bf16 v[96:99], v[144:147], a[132:135], v[96:99]\n\t"
    "v_mfma_f32_16x16x32_bf16 v[96:99], v[140:143], a[128:131], v[96:99]\n\t"
    "v_cvt_pk_bf16_f32 v94, v100, v101\n\t"
    "v_cvt_pk_bf16_f32 v95, v102, v103\n\t"
    "v_mfma_f32_16x16x32_bf16 v[96:99], v[64:67], a[124:127], v[96:99]\n\t"
    "v_mfma_f32_16x16x32_bf16 v[96:99], v[60:63], a[120:123], v[96:99]\n\t"
    "v_mfma_f32_16x16x32_bf16 v[100:103], v[60:63], a[144:147], 0\n\t"
    "buffer_store_dwordx4 v[184:187], v7, s[52:55], s61 offen offset:0 sc1\n\t"
    "v_mfma_f32_16x16x32_bf16 v[100:103], v[64:67], a[148:151], v[100:103]\n\t"
    "v_mfma_f32_16x16x32_bf16 v[100:103], v[140:143], a[152:155], v[100:103]\n\t"
    "v_mfma_f32_16x16x32_bf16 v[100:103], v[144:147], a[156:159], v[100:103]\n\t"
    "v_cvt_pk_bf16_f32 v92, v96, v97\n\t"
    "v_cvt_pk_bf16_f32 v93, v98, v99\n\t"
    "v_mfma_f32_16x16x32_bf16 v[100:103], v[152:155], a[160:163], v[100:103]\n\t"
    "v_mfma_f32_16x16x32_bf16 v[100:103], v[156:159], a[164:167], v[100:103]\n\t"
    "v_mfma_f32_16x16x32_bf16 v[104:107], v[160:163], a[164:167], 0\n\t"
    "v_mfma_f32_16x16x32_bf16 v[104:107], v[164:167], a[160:163], v[104:107]\n\t"
    "v_mfma_f32_16x16x32_bf16 v[104:107], v[124:127], a[156:159], v[104:107]\n\t"
    "v_mfma_f32_16x16x32_bf16 v[104:107], v[168:171], a[152:155], v[104:107]\n\t"
    "v_cvt_pk_bf16_f32 v96, v100, v101\n\t"
    "v_cvt_pk_bf16_f32 v97, v102, v103\n\t"
    "v_mfma_f32_16x16x32_bf16 v[104:107], v[172:175], a[148:151], v[104:107]\n\t"
    "v_mfma_f32_16x16x32_bf16 v[104:107], v[188:191], a[144:147], v[104:107]\n\t"
    "v_mfma_f32_16x16x32_bf16 v[108:111], v[188:191], a[168:171], 0\n\t"
    "buffer_store_dwordx4 v[148:151], v7, s[52:55], s61 offen offset:384 sc1\n\t"
    "v_mfma_f32_16x16x32_bf16 v[108:111], v[172:175], a[172:175], v[108:111]\n\t"
    "v_mfma_f32_16x16x32_bf16 v[108:111], v[168:171], a[176:179], v[108:111]\n\t"
    "v_mfma_f32_16x16x32_bf16 v[108:111], v[124:127], a[180:183], v[108:111]\n\t"
    "v_cvt_pk_bf16_f32 v98, v104, v105\n\t"
    "v_cvt_pk_bf16_f32 v99, v106, v107\n\t"
    "v_mfma_f32_16x16x32_bf16 v[108:111], v[164:167], a[184:187], v[108:111]\n\t"
    "v_mfma_f32_16x16x32_bf16 v[108:111], v[160:163], a[188:191], v[108:111]\n\t"
    "v_mfma_f32_16x16x32_bf16 v[104:107], v[156:159], a[188:191], 0\n\t"
    "v_mfma_f32_16x16x32_bf16 v[104:107], v[152:155], a[184:187], v[104:107]\n\t"
    "v_mfma_f32_16x16x32_bf16 v[104:107], v[144:147], a[180:183], v[104:107]\n\t"
    "v_mfma_f32_16x16x32_bf16 v[104:107], v[140:143], a[176:179], v[104:107]\n\t"
    "v_cvt_pk_bf16_f32 v102, v108, v109\n\t"
    "v_cvt_pk_bf16_f32 v103, v110, v111\n\t"
    "v_mfma_f32_16x16x32_bf16 v[104:107], v[64:67], a[172:175], v[104:107]\n\t"
    "v_mfma_f32_16x16x32_bf16 v[104:107], v[60:63], a[168:171], v[104:107]\n\t"
    "v_mfma_f32_16x16x32_bf16 v[108:111], v[60:63], a[192:195], 0\n\t"
    "buffer_store_dwordx4 v[196:199], v7, s[52:55], s61 offen offset:768 sc1\n\t"
    "v_mfma_f32_16x16x32_bf16 v[108:111], v[64:67], a[196:199], v[108:111]\n\t"
    "v_mfma_f32_16x16x32_bf16 v[108:111], v[140:143], a[200:203], v[108:111]\n\t"
    "v_mfma_f32_16x16x32_bf16 v[108:111], v[144:147], a[204:207], v[108:111]\n\t"
    "v_cvt_pk_bf16_f32 v100, v104, v105\n\t"
    "v_cvt_pk_bf16_f32 v101, v106, v107\n\t"
    "v_mfma_f32_16x16x32_bf16 v[108:111], v[152:155], a[208:211], v[108:111]\n\t"
    "v_mfma_f32_16x16x32_bf16 v[108:111], v[156:159], a[212:215], v[108:111]\n\t"
    "v_mfma_f32_16x16x32_bf16 v[112:115], v[160:163], a[212:215], 0\n\t"
    "v_mfma_f32_16x16x32_bf16 v[112:115], v[164:167], a[208:211], v[112:115]\n\t"
    "v_mfma_f32_16x16x32_bf16 v[112:115], v[124:127], a[204:207], v[112:115]\n\t"
    "v_mfma_f32_16x16x32_bf16 v[112:115], v[168:171], a[200:203], v[112:115]\n\t"
    "v_cvt_pk_bf16_f32 v104, v108, v109\n\t"
    "v_cvt_pk_bf16_f32 v105, v110, v111\n\t"
    "v_mfma_f32_16x16x32_bf16 v[112:115], v[172:175], a[196:199], v[112:115]\n\t"
    "s_barrier\n\t"
    "v_mfma_f32_16x16x32_bf16 v[112:115], v[188:191], a[192:195], v[112:115]\n\t"
    "v_mfma_f32_16x16x32_bf16 v[116:119], v[188:191], a[216:219], 0\n\t"
    "buffer_store_dwordx4 v[192:195], v7, s[52:55], s61 offen offset:1152 sc1\n\t"
    "v_mfma_f32_16x16x32_bf16 v[116:119], v[172:175], a[220:223], v[116:119]\n\t"
    "v_mfma_f32_16x16x32_bf16 v[116:119], v[168:171], a[224:227], v[116:119]\n\t"
    "v_mfma_f32_16x16x32_bf16 v[116:119], v[124:127], a[228:231], v[116:119]\n\t"
    "v_cvt_pk_bf16_f32 v106, v112, v113\n\t"
    "v_cvt_pk_bf16_f32 v107, v114, v115\n\t"
    "v_mfma_f32_16x16x32_bf16 v[116:119], v[164:167], a[232:235], v[116:119]\n\t"
    "v_mfma_f32_16x16x32_bf16 v[116:119], v[160:163], a[236:239], v[116:119]\n\t"
    "v_mfma_f32_16x16x32_bf16 v[112:115], v[156:159], a[236:239], 0\n\t"
    "v_mfma_f32_16x16x32_bf16 v[112:115], v[152:155], a[232:235], v[112:115]\n\t"
    "v_mfma_f32_16x16x32_bf16 v[112:115], v[144:147], a[228:231], v[112:115]\n\t"
    "v_mfma_f32_16x16x32_bf16 v[112:115], v[140:143], a[224:227], v[112:115]\n\t"
    "v_cvt_pk_bf16_f32 v110, v116, v117\n\t"
    "v_cvt_pk_bf16_f32 v111, v118, v119\n\t"
    "v_mfma_f32_16x16x32_bf16 v[112:115], v[64:67], a[220:223], v[112:115]\n\t"
    "v_mfma_f32_16x16x32_bf16 v[112:115], v[60:63], a[216:219], v[112:115]\n\t"
    "v_mfma_f32_16x16x32_bf16 v[116:119], v[60:63], a[240:243], 0\n\t"
    "buffer_store_dwordx2 v[128:129], v8, s[52:55], s61 offen offset:0 sc1\n\t"
    "v_mfma_f32_16x16x32_bf16 v[116:119], v[64:67], a[244:247], v[116:119]\n\t"
    "v_mfma_f32_16x16x32_bf16 v[116:119], v[140:143], a[248:251], v[116:119]\n\t"
    "v_mfma_f32_16x16x32_bf16 v[116:119], v[144:147], a[252:255], v[116:119]\n\t"
    "v_cvt_pk_bf16_f32 v108, v112, v113\n\t"
    "v_cvt_pk_bf16_f32 v109, v114, v115\n\t"
    "v_mfma_f32_16x16x32_bf16 v[116:119], v[152:155], v[224:227], v[116:119]\n\t"
    "v_mfma_f32_16x16x32_bf16 v[116:119], v[156:159], v[228:231], v[116:119]\n\t"
    "v_mfma_f32_16x16x32_bf16 v[132:135], v[160:163], v[228:231], 0\n\t"
    "v_mfma_f32_16x16x32_bf16 v[132:135], v[164:167], v[224:227], v[132:135]\n\t"
    "v_mfma_f32_16x16x32_bf16 v[132:135], v[124:127], a[252:255], v[132:135]\n\t"
    "v_mfma_f32_16x16x32_bf16 v[132:135], v[168:171], a[248:251], v[132:135]\n\t"
    "v_cvt_pk_bf16_f32 v112, v116, v117\n\t"
    "v_cvt_pk_bf16_f32 v113, v118, v119\n\t"
    "v_mfma_f32_16x16x32_bf16 v[132:135], v[172:175], a[244:247], v[132:135]\n\t"
    "v_mfma_f32_16x16x32_bf16 v[132:135], v[188:191], a[240:243], v[132:135]\n\t"
    "v_mfma_f32_16x16x32_bf16 v[136:139], v[188:191], v[232:235], 0\n\t"
    "buffer_store_dwordx2 v[130:131], v8, s[52:55], s61 offen offset:384 sc1\n\t"
    "v_mfma_f32_16x16x32_bf16 v[136:139], v[172:175], v[236:239], v[136:139]\n\t"
    "ds_read_b128 v[116:119], v6 offset:0\n\t"
    "v_mfma_f32_16x16x32_bf16 v[136:139], v[168:171], v[240:243], v[136:139]\n\t"
    "ds_read_b128 v[120:123], v6 offset:1024\n\t"
    "v_mfma_f32_16x16x32_bf16 v[136:139], v[124:127], v[244:247], v[136:139]\n\t"
    "v_cvt_pk_bf16_f32 v114, v132, v133\n\t"
    "v_cvt_pk_bf16_f32 v115, v134, v135\n\t"
    "v_mfma_f32_16x16x32_bf16 v[136:139], v[164:167], v[248:251], v[136:139]\n\t"
    "v_mfma_f32_16x16x32_bf16 v[136:139], v[160:163], v[252:255], v[136:139]\n\t"
    "v_mfma_f32_16x16x32_bf16 v[124:127], v[156:159], v[252:255], 0\n\t"
    "v_mfma_f32_16x16x32_bf16 v[124:127], v[152:155], v[248:251], v[124:127]\n\t"
    "v_mfma_f32_16x16x32_bf16 v[124:127], v[144:147], v[244:247], v[124:127]\n\t"
    "v_mfma_f32_16x16x32_bf16 v[124:127], v[140:143], v[240:243], v[124:127]\n\t"
    "v_cvt_pk_bf16_f32 v130, v136, v137\n\t"
    "v_cvt_pk_bf16_f32 v131, v138, v139\n\t"
    "v_mfma_f32_16x16x32_bf16 v[124:127], v[64:67], v[236:239], v[124:127]\n\t"
    "v_mfma_f32_16x16x32_bf16 v[124:127], v[60:63], v[232:235], v[124:127]\n\t"
    "s_waitcnt lgkmcnt(0)\n\t"
    "v_mfma_f32_16x16x32_bf16 v[132:135], v[116:119], v[16:19], 0\n\t"
    "buffer_store_dwordx2 v[12:13], v8, s[52:55], s61 offen offset:768 sc1\n\t"
    "v_mfma_f32_16x16x32_bf16 v[132:135], v[120:123], v[72:75], v[132:135]\n\t"
    "v_mfma_f32_16x16x32_bf16 v[60:63], v[120:123], v[76:79], 0\n\t"
    "v_mfma_f32_16x16x32_bf16 v[60:63], v[116:119], v[20:23], v[60:63]\n\t"
    "v_cvt_pk_bf16_f32 v128, v124, v125\n\t"
    "v_cvt_pk_bf16_f32 v129, v126, v127\n\t"
    "v_mfma_f32_16x16x32_bf16 v[64:67], v[116:119], v[24:27], 0\n\t"
    "v_mfma_f32_16x16x32_bf16 v[64:67], v[120:123], v[80:83], v[64:67]\n\t"
    "v_mfma_f32_16x16x32_bf16 v[124:127], v[120:123], v[84:87], 0\n\t"
    "v_mfma_f32_16x16x32_bf16 v[124:127], v[116:119], v[28:31], v[124:127]\n\t"
    "v_cvt_pk_bf16_f32 v140, v132, v60\n\t"
    "v_cvt_pk_bf16_f32 v136, v133, v61\n\t"
    "v_mfma_f32_16x16x32_bf16 v[152:155], v[116:119], v[32:35], 0\n\t"
    "v_cvt_pk_bf16_f32 v148, v134, v62\n\t"
    "v_cvt_pk_bf16_f32 v144, v135, v63\n\t"
    "v_mfma_f32_16x16x32_bf16 v[152:155], v[120:123], v[88:91], v[152:155]\n\t"
    "v_mfma_f32_16x16x32_bf16 v[156:159], v[120:123], v[92:95], 0\n\t"
    "v_mfma_f32_16x16x32_bf16 v[156:159], v[116:119], v[36:39], v[156:159]\n\t"
    "v_cvt_pk_bf16_f32 v141, v64, v124\n\t"
    "v_cvt_pk_bf16_f32 v137, v65, v125\n\t"
    "v_mfma_f32_16x16x32_bf16 v[60:63], v[116:119], v[40:43], 0\n\t"
    "buffer_store_dwordx2 v[14:15], v8, s[52:55], s61 offen offset:1152 sc1\n\t"
    "v_mfma_f32_16x16x32_bf16 v[60:63], v[120:123], v[96:99], v[60:63]\n\t"
    "ds_read_b128 v[132:135], v6 offset:2048\n\t"
    "s_add_u32 s61, s61, 11403264\n\t"
    "v_mfma_f32_16x16x32_bf16 v[164:167], v[120:123], v[100:103], 0\n\t"
    "ds_read_b128 v[160:163], v6 offset:3072\n\t"
    "v_cvt_pk_bf16_f32 v149, v66, v126\n\t"
    "v_mfma_f32_16x16x32_bf16 v[164:167], v[116:119], v[44:47], v[164:167]\n\t"
    "v_cvt_pk_bf16_f32 v145, v67, v127\n\t"
    "v_cvt_pk_bf16_f32 v142, v152, v156\n\t"
    "v_mfma_f32_16x16x32_bf16 v[12:15], v[116:119], v[48:51], 0\n\t"
    "v_cvt_pk_bf16_f32 v138, v153, v157\n\t"
    "v_cvt_pk_bf16_f32 v150, v154, v158\n\t"
    "v_mfma_f32_16x16x32_bf16 v[12:15], v[120:123], v[104:107], v[12:15]\n\t"
    "v_cvt_pk_bf16_f32 v146, v155, v159\n\t"
    "v_mfma_f32_16x16x32_bf16 v[64:67], v[120:123], v[108:111], 0\n\t"
    "v_mfma_f32_16x16x32_bf16 v[64:67], v[116:119], v[52:55], v[64:67]\n\t"
    "v_cvt_pk_bf16_f32 v143, v60, v164\n\t"
    "v_cvt_pk_bf16_f32 v139, v61, v165\n\t"
    "v_mfma_f32_16x16x32_bf16 v[124:127], v[116:119], v[56:59], 0\n\t"
    "v_cvt_pk_bf16_f32 v151, v62, v166\n\t"
    "v_cvt_pk_bf16_f32 v147, v63, v167\n\t"
    ".Lh2_%=:\n\t"
    "s_cbranch_execz .Lh6_%=\n\t"
    "v_mfma_f32_16x16x32_bf16 v[124:127], v[120:123], v[112:115], v[124:127]\n\t"
    "v_mfma_f32_16x16x32_bf16 v[152:155], v[120:123], v[128:131], 0\n\t"
    "v_mfma_f32_16x16x32_bf16 v[152:155], v[116:119], v[68:71], v[152:155]\n\t"
    "buffer_store_dwordx4 v[140:143], v7, s[52:55], s61 offen offset:0 sc1\n\t"
    "s_waitcnt lgkmcnt(0)\n\t"
    "v_mfma_f32_16x16x32_bf16 v[156:159], v[132:135], v[16:19], 0\n\t"
    "v_cvt_pk_bf16_f32 v60, v12, v64\n\t"
    "v_cvt_pk_bf16_f32 v62, v13, v65\n\t"
    "v_mfma_f32_16x16x32_bf16 v[156:159], v[160:163], v[72:75], v[156:159]\n\t"
    "v_cvt_pk_bf16_f32 v120, v14, v66\n\t"
    "v_cvt_pk_bf16_f32 v122, v15, v67\n\t"
    "v_mfma_f32_16x16x32_bf16 v[116:119], v[160:163], v[76:79], 0\n\t"
    "v_mfma_f32_16x16x32_bf16 v[116:119], v[132:135], v[20:23], v[116:119]\n\t"
    "v_cvt_pk_bf16_f32 v61, v124, v152\n\t"
    "v_cvt_pk_bf16_f32 v63, v125, v153\n\t"
    "v_mfma_f32_16x16x32_bf16 v[12:15], v[132:135], v[24:27], 0\n\t"
    "v_cvt_pk_bf16_f32 v121, v126, v154\n\t"
    "v_cvt_pk_bf16_f32 v123, v127, v155\n\t"
    "v_mfma_f32_16x16x32_bf16 v[12:15], v[160:163], v[80:83], v[12:15]\n\t"
    "v_mfma_f32_16x16x32_bf16 v[64:67], v[160:163], v[84:87], 0\n\t"
    "v_mfma_f32_16x16x32_bf16 v[64:67], v[132:135], v[28:31], v[64:67]\n\t"
    "v_cvt_pk_bf16_f32 v140, v156, v116\n\t"
    "v_cvt_pk_bf16_f32 v124, v157, v117\n\t"
    "v_mfma_f32_16x16x32_bf16 v[168:171], v[132:135], v[32:35], 0\n\t"
    "v_cvt_pk_bf16_f32 v164, v158, v118\n\t"
    "v_cvt_pk_bf16_f32 v152, v159, v119\n\t"
    "v_mfma_f32_16x16x32_bf16 v[168:171], v[160:163], v[88:91], v[168:171]\n\t"
    "v_mfma_f32_16x16x32_bf16 v[172:175], v[160:163], v[92:95], 0\n\t"
    "v_mfma_f32_16x16x32_bf16 v[172:175], v[132:135], v[36:39], v[172:175]\n\t"
    "buffer_store_dwordx4 v[136:139], v7, s[52:55], s61 offen offset:384 sc1\n\t"
    "v_mfma_f32_16x16x32_bf16 v[116:119], v[132:135], v[40:43], 0\n\t"
    "v_cvt_pk_bf16_f32 v141, v12, v64\n\t"
    "v_cvt_pk_bf16_f32 v125, v13, v65\n\t"
    "v_mfma_f32_16x16x32_bf16 v[116:119], v[160:163], v[96:99], v[116:119]\n\t"
    "ds_read_b128 v[156:159], v6 offset:4096\n\t"
    "v_cvt_pk_bf16_f32 v165, v14, v66\n\t"
    "v_mfma_f32_16x16x32_bf16 v[176:179], v[160:163], v[100:103], 0\n\t"
    "ds_read_b128 v[136:139], v6 offset:5120\n\t"
    "v_cvt_pk_bf16_f32 v153, v15, v67\n\t"
    "v_mfma_f32_16x16x32_bf16 v[176:179], v[132:135], v[44:47], v[176:179]\n\t"
    "v_cvt_pk_bf16_f32 v142, v168, v172\n\t"
    "v_cvt_pk_bf16_f32 v126, v169, v173\n\t"
    "v_mfma_f32_16x16x32_bf16 v[180:183], v[132:135], v[48:51], 0\n\t"
    "v_cvt_pk_bf16_f32 v166, v170, v174\n\t"
    "v_cvt_pk_bf16_f32 v154, v171, v175\n\t"
    "v_mfma_f32_16x16x32_bf16 v[180:183], v[160:163], v[104:107], v[180:183]\n\t"
    "v_mfma_f32_16x16x32_bf16 v[12:15], v[160:163], v[108:111], 0\n\t"
    "v_mfma_f32_16x16x32_bf16 v[12:15], v[132:135], v[52:55], v[12:15]\n\t"
    "v_cvt_pk_bf16_f32 v143, v116, v176\n\t"
    "v_cvt_pk_bf16_f32 v127, v117, v177\n\t"
    "v_mfma_f32_16x16x32_bf16 v[64:67], v[132:135], v[56:59], 0\n\t"
    "v_cvt_pk_bf16_f32 v167, v118, v178\n\t"
    "v_cvt_pk_bf16_f32 v155, v119, v179\n\t"
    "v_mfma_f32_16x16x32_bf16 v[64:67], v[160:163], v[112:115], v[64:67]\n\t"
    "v_mfma_f32_16x16x32_bf16 v[168:171], v[160:163], v[128:131], 0\n\t"
    "v_mfma_f32_16x16x32_bf16 v[168:171], v[132:135], v[68:71], v[168:171]\n\t"
    "buffer_store_dwordx4 v[148:151], v7, s[52:55], s61 offen offset:768 sc1\n\t"
    "s_waitcnt lgkmcnt(0)\n\t"
    "v_mfma_f32_16x16x32_bf16 v[172:175], v[156:159], v[16:19], 0\n\t"
    "v_cvt_pk_bf16_f32 v116, v180, v12\n\t"
    "v_cvt_pk_bf16_f32 v118, v181, v13\n\t"
    "v_mfma_f32_16x16x32_bf16 v[172:175], v[136:139], v[72:75], v[172:175]\n\t"
    "v_cvt_pk_bf16_f32 v160, v182, v14\n\t"
    "v_cvt_pk_bf16_f32 v162, v183, v15\n\t"
    "v_mfma_f32_16x16x32_bf16 v[132:135], v[136:139], v[76:79], 0\n\t"
    "v_mfma_f32_16x16x32_bf16 v[132:135], v[156:159], v[20:23], v[132:135]\n\t"
    "v_cvt_pk_bf16_f32 v117, v64, v168\n\t"
    "v_cvt_pk_bf16_f32 v119, v65, v169\n\t"
    "v_mfma_f32_16x16x32_bf16 v[12:15], v[156:159], v[24:27], 0\n\t"
    "v_cvt_pk_bf16_f32 v161, v66, v170\n\t"
    "v_cvt_pk_bf16_f32 v163, v67, v171\n\t"
    "v_mfma_f32_16x16x32_bf16 v[12:15], v[136:139], v[80:83], v[12:15]\n\t"
    "v_mfma_f32_16x16x32_bf16 v[148:151], v[136:139], v[84:87], 0\n\t"
    "v_mfma_f32_16x16x32_bf16 v[148:151], v[156:159], v[28:31], v[148:151]\n\t"
    "v_cvt_pk_bf16_f32 v64, v172, v132\n\t"
    "v_cvt_pk_bf16_f32 v168, v173, v133\n\t"
    "v_mfma_f32_16x16x32_bf16 v[184:187], v[156:159], v[32:35], 0\n\t"
    "v_cvt_pk_bf16_f32 v180, v174, v134\n\t"
    "v_cvt_pk_bf16_f32 v176, v175, v135\n\t"
    "v_mfma_f32_16x16x32_bf16 v[184:187], v[136:139], v[88:91], v[184:187]\n\t"
    "v_mfma_f32_16x16x32_bf16 v[188:191], v[136:139], v[92:95], 0\n\t"
    "v_mfma_f32_16x16x32_bf16 v[188:191], v[156:159], v[36:39], v[188:191]\n\t"
    "buffer_store_dwordx4 v[144:147], v7, s[52:55], s61 offen offset:1152 sc1\n\t"
    "v_mfma_f32_16x16x32_bf16 v[132:135], v[156:159], v[40:43], 0\n\t"
    "s_barrier\n\t"
    "v_cvt_pk_bf16_f32 v65, v12, v148\n\t"
    "v_mfma_f32_16x16x32_bf16 v[132:135], v[136:139], v[96:99], v[132:135]\n\t"
    "ds_read_b128 v[172:175], v6 offset:6144\n\t"
    "v_cvt_pk_bf16_f32 v169, v13, v149\n\t"
    "v_mfma_f32_16x16x32_bf16 v[192:195], v[136:139], v[100:103], 0\n\t"
    "ds_read_b128 v[144:147], v6 offset:7168\n\t"
    "v_cvt_pk_bf16_f32 v181, v14, v150\n\t"
    "v_mfma_f32_16x16x32_bf16 v[192:195], v[156:159], v[44:47], v[192:195]\n\t"
    "v_cvt_pk_bf16_f32 v177, v15, v151\n\t"
    "v_cvt_pk_bf16_f32 v66, v184, v188\n\t"
    "v_mfma_f32_16x16x32_bf16 v[196:199], v[156:159], v[48:51], 0\n\t"
    "v_cvt_pk_bf16_f32 v170, v185, v189\n\t"
    "v_cvt_pk_bf16_f32 v182, v186, v190\n\t"
    "v_mfma_f32_16x16x32_bf16 v[196:199], v[136:139], v[104:107], v[196:199]\n\t"
    "v_cvt_pk_bf16_f32 v178, v187, v191\n\t"
    "v_mfma_f32_16x16x32_bf16 v[12:15], v[136:139], v[108:111], 0\n\t"
    "v_mfma_f32_16x16x32_bf16 v[12:15], v[156:159], v[52:55], v[12:15]\n\t"
    "v_cvt_pk_bf16_f32 v67, v132, v192\n\t"
    "v_cvt_pk_bf16_f32 v171, v133, v193\n\t"
    "v_mfma_f32_16x16x32_bf16 v[148:151], v[156:159], v[56:59], 0\n\t"
    "v_cvt_pk_bf16_f32 v183, v134, v194\n\t"
    "v_cvt_pk_bf16_f32 v179, v135, v195\n\t"
    "v_mfma_f32_16x16x32_bf16 v[148:151], v[136:139], v[112:115], v[148:151]\n\t"
    "v_mfma_f32_16x16x32_bf16 v[184:187], v[136:139], v[128:131], 0\n\t"
    "v_mfma_f32_16x16x32_bf16 v[184:187], v[156:159], v[68:71], v[184:187]\n\t"
    "buffer_store_dwordx2 v[60:61], v8, s[52:55], s61 offen offset:0 sc1\n\t"
    "s_waitcnt lgkmcnt(0)\n\t"
    "v_mfma_f32_16x16x32_bf16 v[188:191], v[172:175], v[16:19], 0\n\t"
    "v_cvt_pk_bf16_f32 v132, v196, v12\n\t"
    "v_cvt_pk_bf16_f32 v134, v197, v13\n\t"
    "v_mfma_f32_16x16x32_bf16 v[188:191], v[144:147], v[72:75], v[188:191]\n\t"
    "v_cvt_pk_bf16_f32 v136, v198, v14\n\t"
    "v_cvt_pk_bf16_f32 v138, v199, v15\n\t"
    "v_mfma_f32_16x16x32_bf16 v[156:159], v[144:147], v[76:79], 0\n\t"
    "v_mfma_f32_16x16x32_bf16 v[156:159], v[172:175], v[20:23], v[156:159]\n\t"
    "v_cvt_pk_bf16_f32 v133, v148, v184\n\t"
    "v_cvt_pk_bf16_f32 v135, v149, v185\n\t"
    "v_mfma_f32_16x16x32_bf16 v[12:15], v[172:175], v[24:27], 0\n\t"
    "v_cvt_pk_bf16_f32 v137, v150, v186\n\t"
    "v_cvt_pk_bf16_f32 v139, v151, v187\n\t"
    "v_mfma_f32_16x16x32_bf16 v[12:15], v[144:147], v[80:83], v[12:15]\n\t"
    "v_mfma_f32_16x16x32_bf16 v[16:19], v[144:147], v[84:87], 0\n\t"
    "v_mfma_f32_16x16x32_bf16 v[16:19], v[172:175], v[28:31], v[16:19]\n\t"
    "v_cvt_pk_bf16_f32 v184, v188, v156\n\t"
    "v_cvt_pk_bf16_f32 v148, v189, v157\n\t"
    "v_mfma_f32_16x16x32_bf16 v[20:23], v[172:175], v[32:35], 0\n\t"
    "v_cvt_pk_bf16_f32 v196, v190, v158\n\t"
    "v_cvt_pk_bf16_f32 v192, v191, v159\n\t"
    "v_mfma_f32_16x16x32_bf16 v[20:23], v[144:147], v[88:91], v[20:23]\n\t"
    "v_mfma_f32_16x16x32_bf16 v[24:27], v[144:147], v[92:95], 0\n\t"
    "v_mfma_f32_16x16x32_bf16 v[24:27], v[172:175], v[36:39], v[24:27]\n\t"
    "buffer_store_dwordx2 v[62:63], v8, s[52:55], s61 offen offset:384 sc1\n\t"
    "v_mfma_f32_16x16x32_bf16 v[28:31], v[172:175], v[40:43], 0\n\t"
    "ds_read_b128 v[72:75], v0 offset:0\n\t"
    "v_cvt_pk_bf16_f32 v185, v12, v16\n\t"
    "v_mfma_f32_16x16x32_bf16 v[28:31], v[144:147], v[96:99], v[28:31]\n\t"
    "ds_read_b128 v[76:79], v1 offset:0\n\t"
    "v_cvt_pk_bf16_f32 v149, v13, v17\n\t"
    "v_mfma_f32_16x16x32_bf16 v[32:35], v[144:147], v[100:103], 0\n\t"
    "ds_read_b128 v[80:83], v2 offset:0\n\t"
    "v_cvt_pk_bf16_f32 v197, v14, v18\n\t"
    "v_mfma_f32_16x16x32_bf16 v[32:35], v[172:175], v[44:47], v[32:35]\n\t"
    "ds_read_b128 v[84:87], v3 offset:0\n\t"
    "v_cvt_pk_bf16_f32 v193, v15, v19\n\t"
    "v_mfma_f32_16x16x32_bf16 v[36:39], v[172:175], v[48:51], 0\n\t"
    "ds_read_b128 v[88:91], v4 offset:0\n\t"
    "v_cvt_pk_bf16_f32 v186, v20, v24\n\t"
    "v_mfma_f32_16x16x32_bf16 v[36:39], v[144:147], v[104:107], v[36:39]\n\t"
    "ds_read_b128 v[92:95], v5 offset:0\n\t"
    "v_cvt_pk_bf16_f32 v150, v21, v25\n\t"
    "v_mfma_f32_16x16x32_bf16 v[16:19], v[144:147], v[108:111], 0\n\t"
    "ds_read_b128 v[96:99], v5 offset:6144\n\t"
    "v_cvt_pk_bf16_f32 v198, v22, v26\n\t"
    "v_mfma_f32_16x16x32_bf16 v[16:19], v[172:175], v[52:55], v[16:19]\n\t"
    "ds_read_b128 v[100:103], v4 offset:6144\n\t"
    "v_cvt_pk_bf16_f32 v194, v23, v27\n\t"
    "v_mfma_f32_16x16x32_bf16 v[40:43], v[172:175], v[56:59], 0\n\t"
    "ds_read_b128 v[104:107], v3 offset:6144\n\t"
    "v_cvt_pk_bf16_f32 v187, v28, v32\n\t"
    "v_mfma_f32_16x16x32_bf16 v[40:43], v[144:147], v[112:115], v[40:43]\n\t"
    "ds_read_b128 v[108:111], v2 offset:6144\n\t"
    "v_cvt_pk_bf16_f32 v151, v29, v33\n\t"
    "v_mfma_f32_16x16x32_bf16 v[20:23], v[144:147], v[128:131], 0\n\t"
    "ds_read_b128 v[156:159], v1 offset:6144\n\t"
    "v_cvt_pk_bf16_f32 v199, v30, v34\n\t"
    "v_mfma_f32_16x16x32_bf16 v[20:23], v[172:175], v[68:71], v[20:23]\n\t"
    "ds_read_b128 v[188:191], v0 offset:6144\n\t"
    "s_sub_u32 s60, s60, 1\n\t"
    "s_cmp_lg_u32 s60, 0\n\t"
    "s_cbranch_scc1 .Lmin_%=\n\t"
    "s_nop 7\n\t"
    "s_nop 1\n\t"
    "buffer_store_dwordx2 v[120:121], v8, s[52:55], s61 offen offset:768 sc1\n\t"
    "v_cvt_pk_bf16_f32 v195, v31, v35\n\t"
    "v_cvt_pk_bf16_f32 v128, v36, v16\n\t"
    "v_cvt_pk_bf16_f32 v130, v37, v17\n\t"
    "v_cvt_pk_bf16_f32 v12, v38, v18\n\t"
    "v_cvt_pk_bf16_f32 v14, v39, v19\n\t"
    "v_cvt_pk_bf16_f32 v129, v40, v20\n\t"
    "v_cvt_pk_bf16_f32 v131, v41, v21\n\t"
    "v_cvt_pk_bf16_f32 v13, v42, v22\n\t"
    "v_cvt_pk_bf16_f32 v15, v43, v23\n\t"
    "buffer_store_dwordx2 v[122:123], v8, s[52:55], s61 offen offset:1152 sc1\n\t"
    "s_add_u32 s61, s61, 393216\n\t"
    "buffer_store_dwordx4 v[140:143], v7, s[52:55], s61 offen offset:0 sc1\n\t"
    "buffer_store_dwordx4 v[124:127], v7, s[52:55], s61 offen offset:384 sc1\n\t"
    "buffer_store_dwordx4 v[164:167], v7, s[52:55], s61 offen offset:768 sc1\n\t"
    "buffer_store_dwordx4 v[152:155], v7, s[52:55], s61 offen offset:1152 sc1\n\t"
    "buffer_store_dwordx2 v[116:117], v8, s[52:55], s61 offen offset:0 sc1\n\t"
    "buffer_store_dwordx2 v[118:119], v8, s[52:55], s61 offen offset:384 sc1\n\t"
    "buffer_store_dwordx2 v[160:161], v8, s[52:55], s61 offen offset:768 sc1\n\t"
    "buffer_store_dwordx2 v[162:163], v8, s[52:55], s61 offen offset:1152 sc1\n\t"
    "s_add_u32 s61, s61, 393216\n\t"
    "buffer_store_dwordx4 v[64:67], v7, s[52:55], s61 offen offset:0 sc1\n\t"
    "buffer_store_dwordx4 v[168:171], v7, s[52:55], s61 offen offset:384 sc1\n\t"
    "buffer_store_dwordx4 v[180:183], v7, s[52:55], s61 offen offset:768 sc1\n\t"
    "buffer_store_dwordx4 v[176:179], v7, s[52:55], s61 offen offset:1152 sc1\n\t"
    "buffer_store_dwordx2 v[132:133], v8, s[52:55], s61 offen offset:0 sc1\n\t"
    "buffer_store_dwordx2 v[134:135], v8, s[52:55], s61 offen offset:384 sc1\n\t"
    "buffer_store_dwordx2 v[136:137], v8, s[52:55], s61 offen offset:768 sc1\n\t"
    "buffer_store_dwordx2 v[138:139], v8, s[52:55], s61 offen offset:1152 sc1\n\t"
    "s_add_u32 s61, s61, 393216\n\t"
    "buffer_store_dwordx4 v[184:187], v7, s[52:55], s61 offen offset:0 sc1\n\t"
    "buffer_store_dwordx4 v[148:151], v7, s[52:55], s61 offen offset:384 sc1\n\t"
    "buffer_store_dwordx4 v[196:199], v7, s[52:55], s61 offen offset:768 sc1\n\t"
    "buffer_store_dwordx4 v[192:195], v7, s[52:55], s61 offen offset:1152 sc1\n\t"
    "buffer_store_dwordx2 v[128:129], v8, s[52:55], s61 offen offset:0 sc1\n\t"
    "buffer_store_dwordx2 v[130:131], v8, s[52:55], s61 offen offset:384 sc1\n\t"
    "buffer_store_dwordx2 v[12:13], v8, s[52:55], s61 offen offset:768 sc1\n\t"
    "buffer_store_dwordx2 v[14:15], v8, s[52:55], s61 offen offset:1152 sc1\n\t"
    "s_add_u32 s61, s61, 11403264\n\t"
    "s_endpgm\n\t"
    ".p2align 12\n\t"
    ".Lroln_%=:\n\t"
    "v_lshrrev_b32_e32 v20, 3, v31\n\t"
    "v_and_b32_e32 v21, 6, v20\n\t"
    "v_and_b32_e32 v28, 7, v31\n\t"
    "v_xor_b32_e32 v21, v21, v28\n\t"
    "v_lshlrev_b32_e32 v20, 7, v20\n\t"
    "v_lshl_or_b32 v7, v21, 4, v20\n\t"
    "s_mov_b32 s54, 0x6000000\n\t"
    "s_mov_b32 s55, 0x20000\n\t"
    "s_waitcnt lgkmcnt(0)\n\t"
    "s_mov_b32 s52, s36\n\t"
    "s_and_b32 s53, s37, 0xffff\n\t"
    "s_barrier\n\t"
    "s_add_u32 m0, %[rbeg], 0\n\t"
    "s_add_u32 s61, %[wb], 0\n\t"
    "buffer_load_dwordx4 v7, s[52:55], s61 offen offset:0 nt lds\n\t"
    "buffer_load_dwordx4 v7, s[52:55], s61 offen offset:1024 nt lds\n\t"
    "buffer_load_dwordx4 v7, s[52:55], s61 offen offset:2048 nt lds\n\t"
    "s_add_u32 m0, %[rbeg], 3072\n\t"
    "s_add_u32 s61, %[wb], 196608\n\t"
    "buffer_load_dwordx4 v7, s[52:55], s61 offen offset:0 nt lds\n\t"
    "buffer_load_dwordx4 v7, s[52:55], s61 offen offset:1024 nt lds\n\t"
    "buffer_load_dwordx4 v7, s[52:55], s61 offen offset:2048 nt lds\n\t"
    "v_lshlrev_b32_e32 v30, 4, v31\n\t"
    "v_and_b32_e32 v28, 7, v31\n\t"
    "v_bfe_u32 v27, v31, 3, 1\n\t"
    "v_lshrrev_b32_e32 v29, 4, v31\n\t"
    "v_add_u32_e32 v6, %[pl], v30\n\t"
    "v_mul_u32_u24_e32 v27, 0xc00, v27\n\t"
    "v_add_u32_e32 v27, %[rbeg], v27\n\t"
    "v_lshlrev_b32_e32 v26, 7, v31\n\t"
    "v_mad_u32_u24 v20, v28, 3, 0\n\t"
    "v_and_b32_e32 v21, 6, v20\n\t"
    "v_xor_b32_e32 v21, v21, v29\n\t"
    "v_lshl_add_u32 v20, v20, 7, v27\n\t"
    "v_lshl_add_u32 v0, v21, 4, v20\n\t"
    "v_xor_b32_e32 v1, 64, v0\n\t"
    "v_mad_u32_u24 v20, v28, 3, 1\n\t"
    "v_and_b32_e32 v21, 6, v20\n\t"
    "v_xor_b32_e32 v21, v21, v29\n\t"
    "v_lshl_add_u32 v20, v20, 7, v27\n\t"
    "v_lshl_add_u32 v2, v21, 4, v20\n\t"
    "v_xor_b32_e32 v3, 64, v2\n\t"
    "v_mad_u32_u24 v20, v28, 3, 2\n\t"
    "v_and_b32_e32 v21, 6, v20\n\t"
    "v_xor_b32_e32 v21, v21, v29\n\t"
    "v_lshl_add_u32 v20, v20, 7, v27\n\t"
    "v_lshl_add_u32 v4, v21, 4, v20\n\t"
    "v_xor_b32_e32 v5, 64, v4\n\t"
    "s_mov_b32 s56, s42\n\t"
    "s_and_b32 s57, s43, 0xffff\n\t"
    "s_mov_b32 s58, 0x40000\n\t"
    "s_mov_b32 s59, 0x20000\n\t"
    "v_add_u32_e32 v8, 0x9000, v30\n\t"
    "v_add_u32_e32 v9, 0x1b000, v30\n\t"
    "s_mov_b32 s68, 0x3dd53b95\n\t"
    "s_add_u32 m0, %[rbeg], 6144\n\t"
    "s_add_u32 s61, %[wb], 393216\n\t"
    "buffer_load_dwordx4 v7, s[52:55], s61 offen offset:0 nt lds\n\t"
    "buffer_load_dwordx4 v7, s[52:55], s61 offen offset:1024 nt lds\n\t"
    "buffer_load_dwordx4 v7, s[52:55], s61 offen offset:2048 nt lds\n\t"
    "s_mov_b64 exec, 0\n\t"
    "s_cmp_eq_u32 %[pair], 0\n\t"
    "s_cbranch_scc1 .Lh4_%=\n\t"
    "s_branch .Lh1_%=\n\t"
    ".LrM_%=:\n\t"
    "s_mov_b64 exec, -1\n\t"
    "s_sleep 6\n\t"
    "s_add_u32 m0, %[rbeg], 9216\n\t"
    "s_add_u32 s61, %[wb], 589824\n\t"
    "buffer_load_dwordx4 v7, s[52:55], s61 offen offset:0 nt lds\n\t"
    "buffer_load_dwordx4 v7, s[52:55], s61 offen offset:1024 nt lds\n\t"
    "buffer_load_dwordx4 v7, s[52:55], s61 offen offset:2048 nt lds\n\t"
    "s_add_u32 m0, %[rbeg], 12288\n\t"
    "s_add_u32 s61, %[wb], 786432\n\t"
    "buffer_load_dwordx4 v7, s[52:55], s61 offen offset:0 nt lds\n\t"
    "buffer_load_dwordx4 v7, s[52:55], s61 offen offset:1024 nt lds\n\t"
    "buffer_load_dwordx4 v7, s[52:55], s61 offen offset:2048 nt lds\n\t"
    "s_add_u32 m0, %[rbeg], 15360\n\t"
    "s_add_u32 s61, %[wb], 983040\n\t"
    "buffer_load_dwordx4 v7, s[52:55], s61 offen offset:0 nt lds\n\t"
    "buffer_load_dwordx4 v7, s[52:55], s61 offen offset:1024 nt lds\n\t"
    "buffer_load_dwordx4 v7, s[52:55], s61 offen offset:2048 nt lds\n\t"
    "s_add_u32 m0, %[rbeg], 18432\n\t"
    "s_add_u32 s61, %[wb], 1179648\n\t"
    "buffer_load_dwordx4 v7, s[52:55], s61 offen offset:0 nt lds\n\t"
    "buffer_load_dwordx4 v7, s[52:55], s61 offen offset:1024 nt lds\n\t"
    "buffer_load_dwordx4 v7, s[52:55], s61 offen offset:2048 nt lds\n\t"
    "s_add_u32 m0, %[rbeg], 21504\n\t"
    "s_add_u32 s61, %[wb], 1376256\n\t"
    "buffer_load_dwordx4 v7, s[52:55], s61 offen offset:0 nt lds\n\t"
    "buffer_load_dwordx4 v7, s[52:55], s61 offen offset:1024 nt lds\n\t"
    "buffer_load_dwordx4 v7, s[52:55], s61 offen offset:2048 nt lds\n\t"
    "s_waitcnt vmcnt(0)\n\t"
    "s_barrier\n\t"
    "ds_read_b128 v[188:191], v2 offset:6144\n\t"
    "ds_read_b128 v[192:195], v1 offset:6144\n\t"
    "ds_read_b128 v[196:199], v0 offset:6144\n\t"
    "ds_read_b128 v[172:175], v0 offset:12288\n\t"
    "ds_read_b128 v[168:171], v1 offset:12288\n\t"
    "ds_read_b128 v[164:167], v2 offset:12288\n\t"
    "ds_read_b128 v[140:143], v2 offset:18432\n\t"
    "ds_read_b128 v[144:147], v1 offset:18432\n\t"
    "ds_read_b128 v[148:151], v0 offset:18432\n\t"
    "ds_read_b128 v[184:187], v3 offset:6144\n\t"
    "ds_read_b128 v[180:183], v4 offset:6144\n\t"
    "ds_read_b128 v[176:179], v5 offset:6144\n\t"
    "ds_read_b128 v[208:211], v3 offset:0\n\t"
    "ds_read_b128 v[204:207], v4 offset:0\n\t"
    "ds_read_b128 v[200:203], v5 offset:0\n\t"
    "ds_read_b128 v[160:163], v3 offset:12288\n\t"
    "ds_read_b128 v[156:159], v4 offset:12288\n\t"
    "ds_read_b128 v[152:155], v5 offset:12288\n\t"
    "ds_read_b128 v[128:131], v5 offset:18432\n\t"
    "ds_read_b128 v[132:135], v4 offset:18432\n\t"
    "ds_read_b128 v[136:139], v3 offset:18432\n\t"
    "ds_read_b128 v[220:223], v0 offset:0\n\t"
    "ds_read_b128 v[216:219], v1 offset:0\n\t"
    "ds_read_b128 v[212:215], v2 offset:0\n\t"
    "ds_read_b128 v[28:31], v3 offset:0\n\t"
    "ds_read_b128 v[32:35], v4 offset:0\n\t"
    "ds_read_b128 v[36:39], v5 offset:0\n\t"
    "s_add_i32 s63, %[rbeg], 24576\n\t"
    "s_add_i32 s62, %[rbeg], 49152\n\t"
    "s_barrier\n\t"
    "ds_read_b128 a[0:3], v8 offset:0\n\t"
    "ds_read_b128 a[8:11], v8 offset:1024\n\t"
    "ds_read_b128 a[16:19], v8 offset:2048\n\t"
    "ds_read_b128 a[24:27], v8 offset:3072\n\t"
    "ds_read_b128 a[32:35], v8 offset:4096\n\t"
    "ds_read_b128 a[40:43], v8 offset:5120\n\t"
    "ds_read_b128 a[44:47], v9 offset:5120\n\t"
    "ds_read_b128 a[36:39], v9 offset:4096\n\t"
    "ds_read_b128 a[28:31], v9 offset:3072\n\t"
    "ds_read_b128 a[20:23], v9 offset:2048\n\t"
    "ds_read_b128 a[12:15], v9 offset:1024\n\t"
    "s_waitcnt lgkmcnt(10)\n\t"
    "v_mfma_f32_16x16x32_bf16 v[64:67], a[0:3], v[220:223], 0\n\t"
    "ds_read_b128 a[4:7], v9 offset:0\n\t"
    "s_waitcnt lgkmcnt(10)\n\t"
    "v_mfma_f32_16x16x32_bf16 v[64:67], a[8:11], v[216:219], v[64:67]\n\t"
    "s_barrier\n\t"
    "ds_read_b128 a[48:51], v8 offset:6144\n\t"
    "s_waitcnt lgkmcnt(10)\n\t"
    "v_mfma_f32_16x16x32_bf16 v[64:67], a[16:19], v[212:215], v[64:67]\n\t"
    "ds_read_b128 a[56:59], v8 offset:7168\n\t"
    "s_waitcnt lgkmcnt(10)\n\t"
    "v_mfma_f32_16x16x32_bf16 v[64:67], a[24:27], v[28:31], v[64:67]\n\t"
    "ds_read_b128 a[64:67], v8 offset:8192\n\t"
    "s_waitcnt lgkmcnt(4)\n\t"
    "v_mfma_f32_16x16x32_bf16 v[64:67], a[32:35], v[32:35], v[64:67]\n\t"
    "ds_read_b128 a[72:75], v8 offset:9216\n\t"
    "v_mfma_f32_16x16x32_bf16 v[64:67], a[40:43], v[36:39], v[64:67]\n\t"
    "ds_read_b128 a[80:83], v8 offset:10240\n\t"
    "v_mfma_f32_16x16x32_bf16 v[56:59], a[44:47], v[36:39], 0\n\t"
    "ds_read_b128 a[88:91], v8 offset:11264\n\t"
    "v_mfma_f32_16x16x32_bf16 v[56:59], a[36:39], v[32:35], v[56:59]\n\t"
    "ds_read_b128 a[92:95], v9 offset:11264\n\t"
    "v_mfma_f32_16x16x32_bf16 v[56:59], a[28:31], v[28:31], v[56:59]\n\t"
    "ds_read_b128 a[84:87], v9 offset:10240\n\t"
    "v_mfma_f32_16x16x32_bf16 v[56:59], a[20:23], v[212:215], v[56:59]\n\t"
    "ds_read_b128 a[76:79], v9 offset:9216\n\t"
    "v_mfma_f32_16x16x32_bf16 v[56:59], a[12:15], v[216:219], v[56:59]\n\t"
    "v_cvt_pk_bf16_f32 v44, v64, v65\n\t"
    "v_cvt_pk_bf16_f32 v45, v66, v67\n\t"
    "ds_read_b128 a[68:71], v9 offset:8192\n\t"
    "s_waitcnt lgkmcnt(4)\n\t"
    "v_mfma_f32_16x16x32_bf16 v[56:59], a[4:7], v[220:223], v[56:59]\n\t"
    "ds_read_b128 a[60:63], v9 offset:7168\n\t"
    "v_mfma_f32_16x16x32_bf16 v[40:43], a[48:51], v[220:223], 0\n\t"
    "ds_read_b128 a[52:55], v9 offset:6144\n\t"
    "v_mfma_f32_16x16x32_bf16 v[40:43], a[56:59], v[216:219], v[40:43]\n\t"
    "s_barrier\n\t"
    "ds_read_b128 a[96:99], v8 offset:12288\n\t"
    "v_mfma_f32_16x16x32_bf16 v[40:43], a[64:67], v[212:215], v[40:43]\n\t"
    "ds_read_b128 a[104:107], v8 offset:13312\n\t"
    "v_mfma_f32_16x16x32_bf16 v[40:43], a[72:75], v[28:31], v[40:43]\n\t"
    "v_cvt_pk_bf16_f32 v46, v56, v57\n\t"
    "ds_read_b128 a[112:115], v8 offset:14336\n\t"
    "v_mfma_f32_16x16x32_bf16 v[40:43], a[80:83], v[32:35], v[40:43]\n\t"
    "v_cvt_pk_bf16_f32 v47, v58, v59\n\t"
    "ds_read_b128 a[120:123], v8 offset:15360\n\t"
    "v_mfma_f32_16x16x32_bf16 v[40:43], a[88:91], v[36:39], v[40:43]\n\t"
    "ds_read_b128 a[128:131], v8 offset:16384\n\t"
    "s_waitcnt lgkmcnt(4)\n\t"
    "v_mfma_f32_16x16x32_bf16 v[48:51], a[92:95], v[36:39], 0\n\t"
    "ds_read_b128 a[136:139], v8 offset:17408\n\t"
    "v_mfma_f32_16x16x32_bf16 v[48:51], a[84:87], v[32:35], v[48:51]\n\t"
    "ds_read_b128 a[140:143], v9 offset:17408\n\t"
    "v_mfma_f32_16x16x32_bf16 v[48:51], a[76:79], v[28:31], v[48:51]\n\t"
    "ds_read_b128 a[132:135], v9 offset:16384\n\t"
    "v_mfma_f32_16x16x32_bf16 v[48:51], a[68:71], v[212:215], v[48:51]\n\t"
    "v_cvt_pk_bf16_f32 v56, v40, v41\n\t"
    "ds_read_b128 a[124:127], v9 offset:15360\n\t"
    "v_mfma_f32_16x16x32_bf16 v[48:51], a[60:63], v[216:219], v[48:51]\n\t"
    "v_cvt_pk_bf16_f32 v57, v42, v43\n\t"
    "ds_read_b128 a[116:119], v9 offset:14336\n\t"
    "v_mfma_f32_16x16x32_bf16 v[48:51], a[52:55], v[220:223], v[48:51]\n\t"
    "ds_read_b128 a[108:111], v9 offset:13312\n\t"
    "v_mfma_f32_16x16x32_bf16 v[60:63], a[96:99], v[220:223], 0\n\t"
    "ds_read_b128 a[100:103], v9 offset:12288\n\t"
    "s_waitcnt lgkmcnt(4)\n\t"
    "v_mfma_f32_16x16x32_bf16 v[60:63], a[104:107], v[216:219], v[60:63]\n\t"
    "ds_read_b128 a[144:147], v8 offset:18432\n\t"
    "v_mfma_f32_16x16x32_bf16 v[60:63], a[112:115], v[212:215], v[60:63]\n\t"
    "ds_read_b128 a[152:155], v8 offset:19456\n\t"
    "v_mfma_f32_16x16x32_bf16 v[60:63], a[120:123], v[28:31], v[60:63]\n\t"
    "v_cvt_pk_bf16_f32 v58, v48, v49\n\t"
    "v_cvt_pk_bf16_f32 v59, v50, v51\n\t"
    "ds_read_b128 a[160:163], v8 offset:20480\n\t"
    "v_mfma_f32_16x16x32_bf16 v[60:63], a[128:131], v[32:35], v[60:63]\n\t"
    "ds_read_b128 a[168:171], v8 offset:21504\n\t"
    "v_mfma_f32_16x16x32_bf16 v[60:63], a[136:139], v[36:39], v[60:63]\n\t"
    "ds_read_b128 a[176:179], v8 offset:22528\n\t"
    "v_mfma_f32_16x16x32_bf16 v[40:43], a[140:143], v[36:39], 0\n\t"
    "ds_read_b128 a[184:187], v8 offset:23552\n\t"
    "v_mfma_f32_16x16x32_bf16 v[40:43], a[132:135], v[32:35], v[40:43]\n\t"
    "s_waitcnt lgkmcnt(3)\n\t"
    "v_mfma_f32_16x16x32_bf16 v[40:43], a[124:127], v[28:31], v[40:43]\n\t"
    "v_mfma_f32_16x16x32_bf16 v[40:43], a[116:119], v[212:215], v[40:43]\n\t"
    "v_cvt_pk_bf16_f32 v48, v60, v61\n\t"
    "v_cvt_pk_bf16_f32 v49, v62, v63\n\t"
    "v_mfma_f32_16x16x32_bf16 v[40:43], a[108:111], v[216:219], v[40:43]\n\t"
    "v_mfma_f32_16x16x32_bf16 v[40:43], a[100:103], v[220:223], v[40:43]\n\t"
    "v_mfma_f32_16x16x32_bf16 v[60:63], a[144:147], v[220:223], 0\n\t"
    "v_mfma_f32_16x16x32_bf16 v[60:63], a[152:155], v[216:219], v[60:63]\n\t"
    "v_mfma_f32_16x16x32_bf16 v[60:63], a[160:163], v[212:215], v[60:63]\n\t"
    "s_waitcnt lgkmcnt(0)\n\t"
    "v_mfma_f32_16x16x32_bf16 v[60:63], a[168:171], v[28:31], v[60:63]\n\t"
    "v_cvt_pk_bf16_f32 v50, v40, v41\n\t"
    "v_mfma_f32_16x16x32_bf16 v[60:63], a[176:179], v[32:35], v[60:63]\n\t"
    "v_cvt_pk_bf16_f32 v51, v42, v43\n\t"
    "v_mfma_f32_16x16x32_bf16 v[60:63], a[184:187], v[36:39], v[60:63]\n\t"
    "v_mfma_f32_16x16x32_bf16 v[64:67], v[220:223], v[44:47], 0\n\t"
    "v_mfma_f32_16x16x32_bf16 v[64:67], v[216:219], v[56:59], v[64:67]\n\t"
    "ds_read_b128 a[188:191], v9 offset:23552\n\t"
    "v_mfma_f32_16x16x32_bf16 v[64:67], v[212:215], v[48:51], v[64:67]\n\t"
    "ds_read_b128 a[180:183], v9 offset:22528\n\t"
    "v_mfma_f32_16x16x32_bf16 v[40:43], v[188:191], v[48:51], 0\n\t"
    "ds_read_b128 a[172:175], v9 offset:21504\n\t"
    "v_mfma_f32_16x16x32_bf16 v[40:43], v[192:195], v[56:59], v[40:43]\n\t"
    "ds_read_b128 a[164:167], v9 offset:20480\n\t"
    "v_mfma_f32_16x16x32_bf16 v[40:43], v[196:199], v[44:47], v[40:43]\n\t"
    "ds_read_b128 a[156:159], v9 offset:19456\n\t"
    "v_mfma_f32_16x16x32_bf16 v[68:71], v[172:175], v[44:47], 0\n\t"
    "ds_read_b128 a[148:151], v9 offset:18432\n\t"
    "v_mfma_f32_16x16x32_bf16 v[68:71], v[168:171], v[56:59], v[68:71]\n\t"
    "s_barrier\n\t"
    "ds_read_b128 a[192:195], v8 offset:24576\n\t"
    "v_mfma_f32_16x16x32_bf16 v[68:71], v[164:167], v[48:51], v[68:71]\n\t"
    "ds_read_b128 a[200:203], v8 offset:25600\n\t"
    "v_mfma_f32_16x16x32_bf16 v[72:75], v[140:143], v[48:51], 0\n\t"
    "ds_read_b128 a[208:211], v8 offset:26624\n\t"
    "v_mfma_f32_16x16x32_bf16 v[72:75], v[144:147], v[56:59], v[72:75]\n\t"
    "ds_read_b128 a[216:219], v8 offset:27648\n\t"
    "v_mfma_f32_16x16x32_bf16 v[72:75], v[148:151], v[44:47], v[72:75]\n\t"
    "ds_read_b128 a[224:227], v8 offset:28672\n\t"
    "s_waitcnt lgkmcnt(4)\n\t"
    "v_mfma_f32_16x16x32_bf16 v[48:51], a[188:191], v[36:39], 0\n\t"
    "ds_read_b128 a[232:235], v8 offset:29696\n\t"
    "v_mfma_f32_16x16x32_bf16 v[48:51], a[180:183], v[32:35], v[48:51]\n\t"
    "ds_read_b128 a[236:239], v9 offset:29696\n\t"
    "v_mfma_f32_16x16x32_bf16 v[48:51], a[172:175], v[28:31], v[48:51]\n\t"
    "ds_read_b128 a[228:231], v9 offset:28672\n\t"
    "v_mfma_f32_16x16x32_bf16 v[48:51], a[164:167], v[212:215], v[48:51]\n\t"
    "ds_read_b128 a[220:223], v9 offset:27648\n\t"
    "v_mfma_f32_16x16x32_bf16 v[48:51], a[156:159], v[216:219], v[48:51]\n\t"
    "ds_read_b128 a[212:215], v9 offset:26624\n\t"
    "v_mfma_f32_16x16x32_bf16 v[48:51], a[148:151], v[220:223], v[48:51]\n\t"
    "ds_read_b128 a[204:207], v9 offset:25600\n\t"
    "v_mfma_f32_16x16x32_bf16 v[44:47], a[192:195], v[220:223], 0\n\t"
    "ds_read_b128 a[196:199], v9 offset:24576\n\t"
    "s_waitcnt lgkmcnt(4)\n\t"
    "v_mfma_f32_16x16x32_bf16 v[44:47], a[200:203], v[216:219], v[44:47]\n\t"
    "v_cvt_pk_bf16_f32 v56, v60, v61\n\t"
    "ds_read_b128 a[240:243], v8 offset:30720\n\t"
    "v_mfma_f32_16x16x32_bf16 v[44:47], a[208:211], v[212:215], v[44:47]\n\t"
    "ds_read_b128 a[248:251], v8 offset:31744\n\t"
    "v_mfma_f32_16x16x32_bf16 v[44:47], a[216:219], v[28:31], v[44:47]\n\t"
    "v_cvt_pk_bf16_f32 v58, v48, v49\n\t"
    "ds_read_b128 v[224:227], v8 offset:32768\n\t"
    "v_mfma_f32_16x16x32_bf16 v[44:47], a[224:227], v[32:35], v[44:47]\n\t"
    "v_cvt_pk_bf16_f32 v59, v50, v51\n\t"
    "ds_read_b128 v[232:235], v8 offset:33792\n\t"
    "v_mfma_f32_16x16x32_bf16 v[44:47], a[232:235], v[36:39], v[44:47]\n\t"
    "v_cvt_pk_bf16_f32 v57, v62, v63\n\t"
    "ds_read_b128 v[240:243], v8 offset:34816\n\t"
    "v_mfma_f32_16x16x32_bf16 v[76:79], a[236:239], v[36:39], 0\n\t"
    "ds_read_b128 v[248:251], v8 offset:35840\n\t"
    "v_mfma_f32_16x16x32_bf16 v[76:79], a[228:231], v[32:35], v[76:79]\n\t"
    "ds_read_b128 v[252:255], v9 offset:35840\n\t"
    "s_waitcnt lgkmcnt(4)\n\t"
    "v_mfma_f32_16x16x32_bf16 v[76:79], a[220:223], v[28:31], v[76:79]\n\t"
    "ds_read_b128 v[244:247], v9 offset:34816\n\t"
    "v_mfma_f32_16x16x32_bf16 v[76:79], a[212:215], v[212:215], v[76:79]\n\t"
    "v_cvt_pk_bf16_f32 v48, v44, v45\n\t"
    "ds_read_b128 v[236:239], v9 offset:33792\n\t"
    "v_mfma_f32_16x16x32_bf16 v[76:79], a[204:207], v[216:219], v[76:79]\n\t"
    "v_cvt_pk_bf16_f32 v49, v46, v47\n\t"
    "ds_read_b128 v[228:231], v9 offset:32768\n\t"
    "v_mfma_f32_16x16x32_bf16 v[76:79], a[196:199], v[220:223], v[76:79]\n\t"
    "ds_read_b128 a[252:255], v9 offset:31744\n\t"
    "v_mfma_f32_16x16x32_bf16 v[80:83], a[240:243], v[220:223], 0\n\t"
    "ds_read_b128 a[244:247], v9 offset:30720\n\t"
    "v_mfma_f32_16x16x32_bf16 v[80:83], a[248:251], v[216:219], v[80:83]\n\t"
    "v_mfma_f32_16x16x32_bf16 v[80:83], v[224:227], v[212:215], v[80:83]\n\t"
    "s_waitcnt lgkmcnt(2)\n\t"
    "v_mfma_f32_16x16x32_bf16 v[80:83], v[232:235], v[28:31], v[80:83]\n\t"
    "v_cvt_pk_bf16_f32 v50, v76, v77\n\t"
    "v_cvt_pk_bf16_f32 v51, v78, v79\n\t"
    "v_mfma_f32_16x16x32_bf16 v[80:83], v[240:243], v[32:35], v[80:83]\n\t"
    "v_mfma_f32_16x16x32_bf16 v[80:83], v[248:251], v[36:39], v[80:83]\n\t"
    "v_mfma_f32_16x16x32_bf16 v[12:15], v[252:255], v[36:39], 0\n\t"
    "v_mfma_f32_16x16x32_bf16 v[12:15], v[244:247], v[32:35], v[12:15]\n\t"
    "v_mfma_f32_16x16x32_bf16 v[12:15], v[236:239], v[28:31], v[12:15]\n\t"
    "v_mfma_f32_16x16x32_bf16 v[12:15], v[228:231], v[212:215], v[12:15]\n\t"
    "v_cvt_pk_bf16_f32 v16, v80, v81\n\t"
    "v_cvt_pk_bf16_f32 v17, v82, v83\n\t"
    "s_waitcnt lgkmcnt(0)\n\t"
    "v_mfma_f32_16x16x32_bf16 v[12:15], a[252:255], v[216:219], v[12:15]\n\t"
    "v_mfma_f32_16x16x32_bf16 v[12:15], a[244:247], v[220:223], v[12:15]\n\t"
    "v_mfma_f32_16x16x32_bf16 v[28:31], a[0:3], v[196:199], 0\n\t"
    "v_mfma_f32_16x16x32_bf16 v[28:31], a[8:11], v[192:195], v[28:31]\n\t"
    "v_mfma_f32_16x16x32_bf16 v[28:31], a[16:19], v[188:191], v[28:31]\n\t"
    "v_mfma_f32_16x16x32_bf16 v[28:31], a[24:27], v[184:187], v[28:31]\n\t"
    "v_cvt_pk_bf16_f32 v18, v12, v13\n\t"
    "v_mfma_f32_16x16x32_bf16 v[28:31], a[32:35], v[180:183], v[28:31]\n\t"
    "v_mfma_f32_16x16x32_bf16 v[28:31], a[40:43], v[176:179], v[28:31]\n\t"
    "v_cvt_pk_bf16_f32 v19, v14, v15\n\t"
    "v_mfma_f32_16x16x32_bf16 v[64:67], v[208:211], v[56:59], v[64:67]\n\t"
    "v_mfma_f32_16x16x32_bf16 v[64:67], v[204:207], v[48:51], v[64:67]\n\t"
    "v_mfma_f32_16x16x32_bf16 v[64:67], v[200:203], v[16:19], v[64:67]\n\t"
    "v_mfma_f32_16x16x32_bf16 v[40:43], v[176:179], v[16:19], v[40:43]\n\t"
    "v_cvt_pk_bf16_f32 v12, v28, v29\n\t"
    "v_cvt_pk_bf16_f32 v13, v30, v31\n\t"
    "v_mfma_f32_16x16x32_bf16 v[40:43], v[180:183], v[48:51], v[40:43]\n\t"
    "v_mfma_f32_16x16x32_bf16 v[40:43], v[184:187], v[56:59], v[40:43]\n\t"
    "v_mfma_f32_16x16x32_bf16 v[68:71], v[160:163], v[56:59], v[68:71]\n\t"
    "v_max3_f32 v20, v64, v65, v66\n\t"
    "v_mfma_f32_16x16x32_bf16 v[68:71], v[156:159], v[48:51], v[68:71]\n\t"
    "v_mfma_f32_16x16x32_bf16 v[68:71], v[152:155], v[16:19], v[68:71]\n\t"
    "v_mfma_f32_16x16x32_bf16 v[72:75], v[128:131], v[16:19], v[72:75]\n\t"
    "v_max3_f32 v21, v67, v40, v41\n\t"
    "v_max3_f32 v20, v20, v42, v43\n\t"
    "v_mfma_f32_16x16x32_bf16 v[72:75], v[132:135], v[48:51], v[72:75]\n\t"
    "v_mfma_f32_16x16x32_bf16 v[72:75], v[136:139], v[56:59], v[72:75]\n\t"
    "v_mfma_f32_16x16x32_bf16 v[16:19], a[44:47], v[176:179], 0\n\t"
    "v_max3_f32 v21, v21, v68, v69\n\t"
    "v_max3_f32 v20, v20, v70, v71\n\t"
    "v_mfma_f32_16x16x32_bf16 v[16:19], a[36:39], v[180:183], v[16:19]\n\t"
    "v_mfma_f32_16x16x32_bf16 v[16:19], a[28:31], v[184:187], v[16:19]\n\t"
    "v_mfma_f32_16x16x32_bf16 v[16:19], a[20:23], v[188:191], v[16:19]\n\t"
    "v_max3_f32 v21, v21, v72, v73\n\t"
    "v_max3_f32 v20, v20, v74, v75\n\t"
    "v_mfma_f32_16x16x32_bf16 v[16:19], a[12:15], v[192:195], v[16:19]\n\t"
    "v_max_f32_e32 v20, v20, v21\n\t"
    "v_mov_b32_e32 v21, v20\n\t"
    "v_mfma_f32_16x16x32_bf16 v[16:19], a[4:7], v[196:199], v[16:19]\n\t"
    "v_mfma_f32_16x16x32_bf16 v[24:27], a[48:51], v[196:199], 0\n\t"
    "v_permlane32_swap_b32 v20, v21\n\t"
    "v_max_f32_e32 v20, v20, v21\n\t"
    "v_mfma_f32_16x16x32_bf16 v[24:27], a[56:59], v[192:195], v[24:27]\n\t"
    "v_mov_b32_e32 v21, v20\n\t"
    "v_mfma_f32_16x16x32_bf16 v[24:27], a[64:67], v[188:191], v[24:27]\n\t"
    "v_mfma_f32_16x16x32_bf16 v[24:27], a[72:75], v[184:187], v[24:27]\n\t"
    "v_cvt_pk_bf16_f32 v14, v16, v17\n\t"
    "v_cvt_pk_bf16_f32 v15, v18, v19\n\t"
    "v_mfma_f32_16x16x32_bf16 v[24:27], a[80:83], v[180:183], v[24:27]\n\t"
    "v_permlane16_swap_b32 v20, v21\n\t"
    "v_max_f32_e32 v20, v20, v21\n\t"
    "v_mfma_f32_16x16x32_bf16 v[24:27], a[88:91], v[176:179], v[24:27]\n\t"
    "v_sub_f32_e32 v64, v64, v20\n\t"
    "v_sub_f32_e32 v65, v65, v20\n\t"
    "v_mfma_f32_16x16x32_bf16 v[16:19], a[92:95], v[176:179], 0\n\t"
    "v_exp_f32_e32 v64, v64\n\t"
    "v_mfma_f32_16x16x32_bf16 v[16:19], a[84:87], v[180:183], v[16:19]\n\t"
    "v_exp_f32_e32 v65, v65\n\t"
    "v_mfma_f32_16x16x32_bf16 v[16:19], a[76:79], v[184:187], v[16:19]\n\t"
    "v_add_f32_e32 v22, v64, v65\n\t"
    "v_sub_f32_e32 v66, v66, v20\n\t"
    "v_mfma_f32_16x16x32_bf16 v[16:19], a[68:71], v[188:191], v[16:19]\n\t"
    "v_cvt_pk_bf16_f32 v28, v24, v25\n\t"
    "v_cvt_pk_bf16_f32 v29, v26, v27\n\t"
    "v_mfma_f32_16x16x32_bf16 v[16:19], a[60:63], v[192:195], v[16:19]\n\t"
    "v_exp_f32_e32 v66, v66\n\t"
    "v_mfma_f32_16x16x32_bf16 v[16:19], a[52:55], v[196:199], v[16:19]\n\t"
    "v_sub_f32_e32 v67, v67, v20\n\t"
    "v_mfma_f32_16x16x32_bf16 v[24:27], a[96:99], v[196:199], 0\n\t"
    "v_sub_f32_e32 v40, v40, v20\n\t"
    "v_mfma_f32_16x16x32_bf16 v[24:27], a[104:107], v[192:195], v[24:27]\n\t"
    "v_exp_f32_e32 v67, v67\n\t"
    "v_mfma_f32_16x16x32_bf16 v[24:27], a[112:115], v[188:191], v[24:27]\n\t"
    "v_add_f32_e32 v23, v66, v67\n\t"
    "v_sub_f32_e32 v41, v41, v20\n\t"
    "v_mfma_f32_16x16x32_bf16 v[24:27], a[120:123], v[184:187], v[24:27]\n\t"
    "v_cvt_pk_bf16_f32 v30, v16, v17\n\t"
    "v_cvt_pk_bf16_f32 v31, v18, v19\n\t"
    "v_mfma_f32_16x16x32_bf16 v[24:27], a[128:131], v[180:183], v[24:27]\n\t"
    "v_exp_f32_e32 v40, v40\n\t"
    "v_mfma_f32_16x16x32_bf16 v[24:27], a[136:139], v[176:179], v[24:27]\n\t"
    "v_add_f32_e32 v22, v22, v40\n\t"
    "v_sub_f32_e32 v42, v42, v20\n\t"
    "v_mfma_f32_16x16x32_bf16 v[16:19], a[140:143], v[176:179], 0\n\t"
    "v_exp_f32_e32 v41, v41\n\t"
    "v_mfma_f32_16x16x32_bf16 v[16:19], a[132:135], v[180:183], v[16:19]\n\t"
    "v_add_f32_e32 v22, v22, v41\n\t"
    "v_sub_f32_e32 v43, v43, v20\n\t"
    "v_mfma_f32_16x16x32_bf16 v[16:19], a[124:127], v[184:187], v[16:19]\n\t"
    "v_exp_f32_e32 v42, v42\n\t"
    "v_mfma_f32_16x16x32_bf16 v[16:19], a[116:119], v[188:191], v[16:19]\n\t"
    "v_cvt_pk_bf16_f32 v32, v24, v25\n\t"
    "v_cvt_pk_bf16_f32 v33, v26, v27\n\t"
    "v_mfma_f32_16x16x32_bf16 v[16:19], a[108:111], v[192:195], v[16:19]\n\t"
    "v_add_f32_e32 v23, v23, v42\n\t"
    "v_sub_f32_e32 v68, v68, v20\n\t"
    "v_mfma_f32_16x16x32_bf16 v[16:19], a[100:103], v[196:199], v[16:19]\n\t"
    "v_exp_f32_e32 v43, v43\n\t"
    "v_mfma_f32_16x16x32_bf16 v[24:27], a[144:147], v[196:199], 0\n\t"
    "v_add_f32_e32 v23, v23, v43\n\t"
    "v_sub_f32_e32 v69, v69, v20\n\t"
    "v_mfma_f32_16x16x32_bf16 v[24:27], a[152:155], v[192:195], v[24:27]\n\t"
    "v_exp_f32_e32 v68, v68\n\t"
    "v_mfma_f32_16x16x32_bf16 v[24:27], a[160:163], v[188:191], v[24:27]\n\t"
    "v_add_f32_e32 v22, v22, v68\n\t"
    "v_mfma_f32_16x16x32_bf16 v[24:27], a[168:171], v[184:187], v[24:27]\n\t"
    "v_cvt_pk_bf16_f32 v34, v16, v17\n\t"
    "v_mfma_f32_16x16x32_bf16 v[24:27], a[176:179], v[180:183], v[24:27]\n\t"
    "v_cvt_pk_bf16_f32 v35, v18, v19\n\t"
    "v_sub_f32_e32 v70, v70, v20\n\t"
    "s_barrier\n\t"
    "v_mfma_f32_16x16x32_bf16 v[24:27], a[184:187], v[176:179], v[24:27]\n\t"
    "v_exp_f32_e32 v69, v69\n\t"
    "v_mfma_f32_16x16x32_bf16 v[36:39], v[220:223], v[12:15], 0\n\t"
    "v_add_f32_e32 v22, v22, v69\n\t"
    "v_sub_f32_e32 v71, v71, v20\n\t"
    "s_add_u32 m0, %[rbeg], 24576\n\t"
    "s_add_u32 s61, %[wb], 12582912\n\t"
    "v_mfma_f32_16x16x32_bf16 v[36:39], v[216:219], v[28:31], v[36:39]\n\t"
    "v_exp_f32_e32 v70, v70\n\t"
    "v_mfma_f32_16x16x32_bf16 v[36:39], v[212:215], v[32:35], v[36:39]\n\t"
    "v_add_f32_e32 v23, v23, v70\n\t"
    "v_sub_f32_e32 v72, v72, v20\n\t"
    "v_mfma_f32_16x16x32_bf16 v[16:19], v[188:191], v[32:35], 0\n\t"
    "v_exp_f32_e32 v71, v71\n\t"
    "v_mfma_f32_16x16x32_bf16 v[16:19], v[192:195], v[28:31], v[16:19]\n\t"
    "v_add_f32_e32 v23, v23, v71\n\t"
    "v_sub_f32_e32 v73, v73, v20\n\t"
    "v_mfma_f32_16x16x32_bf16 v[16:19], v[196:199], v[12:15], v[16:19]\n\t"
    "v_exp_f32_e32 v72, v72\n\t"
    "buffer_load_dwordx4 v7, s[52:55], s61 offen offset:0 nt lds\n\t"
    "v_mfma_f32_16x16x32_bf16 v[44:47], v[172:175], v[12:15], 0\n\t"
    "v_add_f32_e32 v22, v22, v72\n\t"
    "v_sub_f32_e32 v74, v74, v20\n\t"
    "v_mfma_f32_16x16x32_bf16 v[44:47], v[168:171], v[28:31], v[44:47]\n\t"
    "v_exp_f32_e32 v73, v73\n\t"
    "v_mfma_f32_16x16x32_bf16 v[44:47], v[164:167], v[32:35], v[44:47]\n\t"
    "v_add_f32_e32 v22, v22, v73\n\t"
    "v_sub_f32_e32 v75, v75, v20\n\t"
    "v_mfma_f32_16x16x32_bf16 v[48:51], v[140:143], v[32:35], 0\n\t"
    "v_exp_f32_e32 v74, v74\n\t"
    "v_mfma_f32_16x16x32_bf16 v[48:51], v[144:147], v[28:31], v[48:51]\n\t"
    "v_add_f32_e32 v23, v23, v74\n\t"
    "v_cvt_pk_bf16_f32 v52, v24, v25\n\t"
    "v_mfma_f32_16x16x32_bf16 v[48:51], v[148:151], v[12:15], v[48:51]\n\t"
    "v_exp_f32_e32 v75, v75\n\t"
    "v_mfma_f32_16x16x32_bf16 v[32:35], a[188:191], v[176:179], 0\n\t"
    "v_add_f32_e32 v23, v23, v75\n\t"
    "v_add_f32_e32 v22, v22, v23\n\t"
    "v_mfma_f32_16x16x32_bf16 v[32:35], a[180:183], v[180:183], v[32:35]\n\t"
    "v_mov_b32_e32 v23, v22\n\t"
    "buffer_load_dwordx4 v7, s[52:55], s61 offen offset:1024 nt lds\n\t"
    "v_mfma_f32_16x16x32_bf16 v[32:35], a[172:175], v[184:187], v[32:35]\n\t"
    "v_cvt_pk_bf16_f32 v53, v26, v27\n\t"
    "v_mfma_f32_16x16x32_bf16 v[32:35], a[164:167], v[188:191], v[32:35]\n\t"
    "v_permlane32_swap_b32 v22, v23\n\t"
    "v_add_f32_e32 v22, v22, v23\n\t"
    "v_mfma_f32_16x16x32_bf16 v[32:35], a[156:159], v[192:195], v[32:35]\n\t"
    "v_mov_b32_e32 v23, v22\n\t"
    "buffer_load_dwordx4 v7, s[52:55], s61 offen offset:2048 nt lds\n\t"
    "v_mfma_f32_16x16x32_bf16 v[32:35], a[148:151], v[196:199], v[32:35]\n\t"
    "v_mfma_f32_16x16x32_bf16 v[12:15], a[192:195], v[196:199], 0\n\t"
    "v_permlane16_swap_b32 v22, v23\n\t"
    "v_add_f32_e32 v22, v22, v23\n\t"
    "v_mfma_f32_16x16x32_bf16 v[12:15], a[200:203], v[192:195], v[12:15]\n\t"
    "v_rcp_f32_e32 v22, v22\n\t"
    "v_mfma_f32_16x16x32_bf16 v[12:15], a[208:211], v[188:191], v[12:15]\n\t"
    "v_mul_f32_e32 v64, v64, v22\n\t"
    "v_mul_f32_e32 v65, v65, v22\n\t"
    "s_add_u32 m0, %[rbeg], 27648\n\t"
    "s_add_u32 s61, %[wb], 12779520\n\t"
    "v_mfma_f32_16x16x32_bf16 v[12:15], a[216:219], v[184:187], v[12:15]\n\t"
    "v_cvt_pk_bf16_f32 v54, v32, v33\n\t"
    "v_cvt_pk_bf16_f32 v55, v34, v35\n\t"
    "v_mfma_f32_16x16x32_bf16 v[12:15], a[224:227], v[180:183], v[12:15]\n\t"
    "v_cvt_pk_bf16_f32 v24, v64, v65\n\t"
    "v_mul_f32_e32 v66, v66, v22\n\t"
    "v_mfma_f32_16x16x32_bf16 v[12:15], a[232:235], v[176:179], v[12:15]\n\t"
    "v_mul_f32_e32 v67, v67, v22\n\t"
    "v_cvt_pk_bf16_f32 v25, v66, v67\n\t"
    "v_mfma_f32_16x16x32_bf16 v[28:31], a[236:239], v[176:179], 0\n\t"
    "v_mul_f32_e32 v40, v40, v22\n\t"
    "v_mul_f32_e32 v41, v41, v22\n\t"
    "v_mfma_f32_16x16x32_bf16 v[28:31], a[228:231], v[180:183], v[28:31]\n\t"
    "v_cvt_pk_bf16_f32 v26, v40, v41\n\t"
    "v_mul_f32_e32 v42, v42, v22\n\t"
    "buffer_load_dwordx4 v7, s[52:55], s61 offen offset:0 nt lds\n\t"
    "v_mfma_f32_16x16x32_bf16 v[28:31], a[220:223], v[184:187], v[28:31]\n\t"
    "v_mul_f32_e32 v43, v43, v22\n\t"
    "v_cvt_pk_bf16_f32 v27, v42, v43\n\t"
    "v_mfma_f32_16x16x32_bf16 v[28:31], a[212:215], v[188:191], v[28:31]\n\t"
    "v_cvt_pk_bf16_f32 v32, v12, v13\n\t"
    "v_cvt_pk_bf16_f32 v33, v14, v15\n\t"
    "v_mfma_f32_16x16x32_bf16 v[28:31], a[204:207], v[192:195], v[28:31]\n\t"
    "v_mul_f32_e32 v68, v68, v22\n\t"
    "v_mul_f32_e32 v69, v69, v22\n\t"
    "v_mfma_f32_16x16x32_bf16 v[28:31], a[196:199], v[196:199], v[28:31]\n\t"
    "v_cvt_pk_bf16_f32 v40, v68, v69\n\t"
    "v_mfma_f32_16x16x32_bf16 v[12:15], a[240:243], v[196:199], 0\n\t"
    "v_mul_f32_e32 v70, v70, v22\n\t"
    "buffer_load_dwordx4 v7, s[52:55], s61 offen offset:1024 nt lds\n\t"
    "v_mfma_f32_16x16x32_bf16 v[12:15], a[248:251], v[192:195], v[12:15]\n\t"
    "v_mul_f32_e32 v71, v71, v22\n\t"
    "v_cvt_pk_bf16_f32 v41, v70, v71\n\t"
    "v_mfma_f32_16x16x32_bf16 v[12:15], v[224:227], v[188:191], v[12:15]\n\t"
    "v_mul_f32_e32 v72, v72, v22\n\t"
    "v_mul_f32_e32 v73, v73, v22\n\t"
    "v_mfma_f32_16x16x32_bf16 v[12:15], v[232:235], v[184:187], v[12:15]\n\t"
    "v_cvt_pk_bf16_f32 v34, v28, v29\n\t"
    "v_cvt_pk_bf16_f32 v35, v30, v31\n\t"
    "v_mfma_f32_16x16x32_bf16 v[12:15], v[240:243], v[180:183], v[12:15]\n\t"
    "v_cvt_pk_bf16_f32 v42, v72, v73\n\t"
    "v_mul_f32_e32 v74, v74, v22\n\t"
    "v_mfma_f32_16x16x32_bf16 v[12:15], v[248:251], v[176:179], v[12:15]\n\t"
    "v_mul_f32_e32 v75, v75, v22\n\t"
    "v_cvt_pk_bf16_f32 v43, v74, v75\n\t"
    "v_mfma_f32_16x16x32_bf16 v[28:31], v[252:255], v[176:179], 0\n\t"
    "buffer_load_dwordx4 v7, s[52:55], s61 offen offset:2048 nt lds\n\t"
    "v_mfma_f32_16x16x32_bf16 v[28:31], v[244:247], v[180:183], v[28:31]\n\t"
    "v_mfma_f32_16x16x32_bf16 v[28:31], v[236:239], v[184:187], v[28:31]\n\t"
    "v_mfma_f32_16x16x32_bf16 v[28:31], v[228:231], v[188:191], v[28:31]\n\t"
    "v_cvt_pk_bf16_f32 v20, v12, v13\n\t"
    "v_cvt_pk_bf16_f32 v21, v14, v15\n\t"
    "v_mfma_f32_16x16x32_bf16 v[28:31], a[252:255], v[192:195], v[28:31]\n\t"
    "v_mfma_f32_16x16x32_bf16 v[28:31], a[244:247], v[196:199], v[28:31]\n\t"
    "s_add_u32 m0, %[rbeg], 30720\n\t"
    "s_add_u32 s61, %[wb], 12976128\n\t"
    "v_mfma_f32_16x16x32_bf16 v[12:15], a[0:3], v[172:175], 0\n\t"
    "ds_write_b128 v6, v[24:27] offset:0\n\t"
    "v_mfma_f32_16x16x32_bf16 v[12:15], a[8:11], v[168:171], v[12:15]\n\t"
    "v_mfma_f32_16x16x32_bf16 v[12:15], a[16:19], v[164:167], v[12:15]\n\t"
    "v_mfma_f32_16x16x32_bf16 v[12:15], a[24:27], v[160:163], v[12:15]\n\t"
    "v_cvt_pk_bf16_f32 v22, v28, v29\n\t"
    "v_mfma_f32_16x16x32_bf16 v[12:15], a[32:35], v[156:159], v[12:15]\n\t"
    "ds_write_b128 v6, v[40:43] offset:1024\n\t"
    "v_mfma_f32_16x16x32_bf16 v[12:15], a[40:43], v[152:155], v[12:15]\n\t"
    "v_cvt_pk_bf16_f32 v23, v30, v31\n\t"
    "buffer_load_dwordx4 v7, s[52:55], s61 offen offset:0 nt lds\n\t"
    "v_mfma_f32_16x16x32_bf16 v[36:39], v[208:211], v[52:55], v[36:39]\n\t"
    "v_mfma_f32_16x16x32_bf16 v[36:39], v[204:207], v[32:35], v[36:39]\n\t"
    "v_mfma_f32_16x16x32_bf16 v[36:39], v[200:203], v[20:23], v[36:39]\n\t"
    "s_add_i32 s64, s62, 24576\n\t"
    "v_mfma_f32_16x16x32_bf16 v[16:19], v[176:179], v[20:23], v[16:19]\n\t"
    "s_cmp_eq_u32 s64, %[rend]\n\t"
    "s_cselect_b32 s64, %[rbeg], s64\n\t"
    "buffer_load_dwordx4 v7, s[52:55], s61 offen offset:1024 nt lds\n\t"
    "v_mfma_f32_16x16x32_bf16 v[16:19], v[180:183], v[32:35], v[16:19]\n\t"
    "s_sub_i32 s65, s63, s64\n\t"
    "v_cvt_pk_bf16_f32 v24, v12, v13\n\t"
    "v_mfma_f32_16x16x32_bf16 v[16:19], v[184:187], v[52:55], v[16:19]\n\t"
    "v_add_u32_e32 v0, s65, v0\n\t"
    "v_add_u32_e32 v1, s65, v1\n\t"
    "v_mfma_f32_16x16x32_bf16 v[44:47], v[160:163], v[52:55], v[44:47]\n\t"
    "v_add_u32_e32 v2, s65, v2\n\t"
    "v_add_u32_e32 v3, s65, v3\n\t"
    "v_mfma_f32_16x16x32_bf16 v[44:47], v[156:159], v[32:35], v[44:47]\n\t"
    "v_add_u32_e32 v4, s65, v4\n\t"
    "v_add_u32_e32 v5, s65, v5\n\t"
    "v_mfma_f32_16x16x32_bf16 v[44:47], v[152:155], v[20:23], v[44:47]\n\t"
    "v_max3_f32 v28, v36, v37, v38\n\t"
    "v_cvt_pk_bf16_f32 v25, v14, v15\n\t"
    "v_mfma_f32_16x16x32_bf16 v[48:51], v[128:131], v[20:23], v[48:51]\n\t"
    "v_max3_f32 v29, v39, v16, v17\n\t"
    "v_max3_f32 v28, v28, v18, v19\n\t"
    "v_mfma_f32_16x16x32_bf16 v[48:51], v[132:135], v[32:35], v[48:51]\n\t"
    ".Lh4_%=:\n\t"
    "s_cbranch_execz .Lh5_%=\n\t"
    "buffer_load_dwordx4 v7, s[52:55], s61 offen offset:2048 nt lds\n\t"
    "v_mfma_f32_16x16x32_bf16 v[48:51], v[136:139], v[52:55], v[48:51]\n\t"
    "v_mfma_f32_16x16x32_bf16 v[12:15], a[44:47], v[152:155], 0\n\t"
    "v_max3_f32 v29, v29, v44, v45\n\t"
    "v_max3_f32 v28, v28, v46, v47\n\t"
    "v_mfma_f32_16x16x32_bf16 v[12:15], a[36:39], v[156:159], v[12:15]\n\t"
    "s_add_u32 m0, %[rbeg], 33792\n\t"
    "s_add_u32 s61, %[wb], 13172736\n\t"
    "v_mfma_f32_16x16x32_bf16 v[12:15], a[28:31], v[160:163], v[12:15]\n\t"
    "v_mfma_f32_16x16x32_bf16 v[12:15], a[20:23], v[164:167], v[12:15]\n\t"
    "v_max3_f32 v29, v29, v48, v49\n\t"
    "v_max3_f32 v28, v28, v50, v51\n\t"
    "v_mfma_f32_16x16x32_bf16 v[12:15], a[12:15], v[168:171], v[12:15]\n\t"
    "v_max_f32_e32 v28, v28, v29\n\t"
    "v_mov_b32_e32 v29, v28\n\t"
    "v_mfma_f32_16x16x32_bf16 v[12:15], a[4:7], v[172:175], v[12:15]\n\t"
    "v_mfma_f32_16x16x32_bf16 v[20:23], a[48:51], v[172:175], 0\n\t"
    "v_permlane32_swap_b32 v28, v29\n\t"
    "v_max_f32_e32 v28, v28, v29\n\t"
    "v_mfma_f32_16x16x32_bf16 v[20:23], a[56:59], v[168:171], v[20:23]\n\t"
    "v_mov_b32_e32 v29, v28\n\t"
    "buffer_load_dwordx4 v7, s[52:55], s61 offen offset:0 nt lds\n\t"
    "v_mfma_f32_16x16x32_bf16 v[20:23], a[64:67], v[164:167], v[20:23]\n\t"
    "v_mfma_f32_16x16x32_bf16 v[20:23], a[72:75], v[160:163], v[20:23]\n\t"
    "v_cvt_pk_bf16_f32 v26, v12, v13\n\t"
    "v_cvt_pk_bf16_f32 v27, v14, v15\n\t"
    "v_mfma_f32_16x16x32_bf16 v[20:23], a[80:83], v[156:159], v[20:23]\n\t"
    "v_permlane16_swap_b32 v28, v29\n\t"
    "v_max_f32_e32 v28, v28, v29\n\t"
    "v_mfma_f32_16x16x32_bf16 v[20:23], a[88:91], v[152:155], v[20:23]\n\t"
    "v_sub_f32_e32 v36, v36, v28\n\t"
    "v_sub_f32_e32 v37, v37, v28\n\t"
    "buffer_load_dwordx4 v7, s[52:55], s61 offen offset:1024 nt lds\n\t"
    "v_mfma_f32_16x16x32_bf16 v[12:15], a[92:95], v[152:155], 0\n\t"
    "v_exp_f32_e32 v36, v36\n\t"
    "v_mfma_f32_16x16x32_bf16 v[12:15], a[84:87], v[156:159], v[12:15]\n\t"
    "v_exp_f32_e32 v37, v37\n\t"
    "v_mfma_f32_16x16x32_bf16 v[12:15], a[76:79], v[160:163], v[12:15]\n\t"
    "v_add_f32_e32 v30, v36, v37\n\t"
    "v_sub_f32_e32 v38, v38, v28\n\t"
    "v_mfma_f32_16x16x32_bf16 v[12:15], a[68:71], v[164:167], v[12:15]\n\t"
    "v_cvt_pk_bf16_f32 v32, v20, v21\n\t"
    "v_cvt_pk_bf16_f32 v33, v22, v23\n\t"
    "v_mfma_f32_16x16x32_bf16 v[12:15], a[60:63], v[168:171], v[12:15]\n\t"
    "v_exp_f32_e32 v38, v38\n\t"
    "v_mfma_f32_16x16x32_bf16 v[12:15], a[52:55], v[172:175], v[12:15]\n\t"
    "v_sub_f32_e32 v39, v39, v28\n\t"
    "buffer_load_dwordx4 v7, s[52:55], s61 offen offset:2048 nt lds\n\t"
    "v_mfma_f32_16x16x32_bf16 v[20:23], a[96:99], v[172:175], 0\n\t"
    "v_sub_f32_e32 v16, v16, v28\n\t"
    "v_mfma_f32_16x16x32_bf16 v[20:23], a[104:107], v[168:171], v[20:23]\n\t"
    "v_exp_f32_e32 v39, v39\n\t"
    "v_mfma_f32_16x16x32_bf16 v[20:23], a[112:115], v[164:167], v[20:23]\n\t"
    "v_add_f32_e32 v31, v38, v39\n\t"
    "v_sub_f32_e32 v17, v17, v28\n\t"
    "v_mfma_f32_16x16x32_bf16 v[20:23], a[120:123], v[160:163], v[20:23]\n\t"
    "v_cvt_pk_bf16_f32 v34, v12, v13\n\t"
    "v_cvt_pk_bf16_f32 v35, v14, v15\n\t"
    "v_mfma_f32_16x16x32_bf16 v[20:23], a[128:131], v[156:159], v[20:23]\n\t"
    "v_exp_f32_e32 v16, v16\n\t"
    "s_add_u32 m0, %[rbeg], 36864\n\t"
    "s_add_u32 s61, %[wb], 13369344\n\t"
    "v_mfma_f32_16x16x32_bf16 v[20:23], a[136:139], v[152:155], v[20:23]\n\t"
    "v_add_f32_e32 v30, v30, v16\n\t"
    "v_sub_f32_e32 v18, v18, v28\n\t"
    "v_mfma_f32_16x16x32_bf16 v[12:15], a[140:143], v[152:155], 0\n\t"
    "v_exp_f32_e32 v17, v17\n\t"
    "v_mfma_f32_16x16x32_bf16 v[12:15], a[132:135], v[156:159], v[12:15]\n\t"
    "v_add_f32_e32 v30, v30, v17\n\t"
    "v_sub_f32_e32 v19, v19, v28\n\t"
    "v_mfma_f32_16x16x32_bf16 v[12:15], a[124:127], v[160:163], v[12:15]\n\t"
    "v_exp_f32_e32 v18, v18\n\t"
    "v_mfma_f32_16x16x32_bf16 v[12:15], a[116:119], v[164:167], v[12:15]\n\t"
    "v_cvt_pk_bf16_f32 v40, v20, v21\n\t"
    "v_cvt_pk_bf16_f32 v41, v22, v23\n\t"
    "buffer_load_dwordx4 v7, s[52:55], s61 offen offset:0 nt lds\n\t"
    "v_mfma_f32_16x16x32_bf16 v[12:15], a[108:111], v[168:171], v[12:15]\n\t"
    "v_add_f32_e32 v31, v31, v18\n\t"
    "v_sub_f32_e32 v44, v44, v28\n\t"
    "v_mfma_f32_16x16x32_bf16 v[12:15], a[100:103], v[172:175], v[12:15]\n\t"
    "v_exp_f32_e32 v19, v19\n\t"
    "v_mfma_f32_16x16x32_bf16 v[20:23], a[144:147], v[172:175], 0\n\t"
    "v_add_f32_e32 v31, v31, v19\n\t"
    "v_sub_f32_e32 v45, v45, v28\n\t"
    "v_mfma_f32_16x16x32_bf16 v[20:23], a[152:155], v[168:171], v[20:23]\n\t"
    "v_exp_f32_e32 v44, v44\n\t"
    "v_mfma_f32_16x16x32_bf16 v[20:23], a[160:163], v[164:167], v[20:23]\n\t"
    "v_add_f32_e32 v30, v30, v44\n\t"
    "buffer_load_dwordx4 v7, s[52:55], s61 offen offset:1024 nt lds\n\t"
    "v_mfma_f32_16x16x32_bf16 v[20:23], a[168:171], v[160:163], v[20:23]\n\t"
    "v_cvt_pk_bf16_f32 v42, v12, v13\n\t"
    "v_mfma_f32_16x16x32_bf16 v[20:23], a[176:179], v[156:159], v[20:23]\n\t"
    "v_cvt_pk_bf16_f32 v43, v14, v15\n\t"
    "v_sub_f32_e32 v46, v46, v28\n\t"
    "v_mfma_f32_16x16x32_bf16 v[20:23], a[184:187], v[152:155], v[20:23]\n\t"
    "v_exp_f32_e32 v45, v45\n\t"
    "v_mfma_f32_16x16x32_bf16 v[52:55], v[220:223], v[24:27], 0\n\t"
    "v_add_f32_e32 v30, v30, v45\n\t"
    "v_sub_f32_e32 v47, v47, v28\n\t"
    "v_mfma_f32_16x16x32_bf16 v[52:55], v[216:219], v[32:35], v[52:55]\n\t"
    "v_exp_f32_e32 v46, v46\n\t"
    "buffer_load_dwordx4 v7, s[52:55], s61 offen offset:2048 nt lds\n\t"
    "v_mfma_f32_16x16x32_bf16 v[52:55], v[212:215], v[40:43], v[52:55]\n\t"
    "v_add_f32_e32 v31, v31, v46\n\t"
    "v_sub_f32_e32 v48, v48, v28\n\t"
    "v_mfma_f32_16x16x32_bf16 v[56:59], v[188:191], v[40:43], 0\n\t"
    "v_exp_f32_e32 v47, v47\n\t"
    "v_mfma_f32_16x16x32_bf16 v[56:59], v[192:195], v[32:35], v[56:59]\n\t"
    "v_add_f32_e32 v31, v31, v47\n\t"
    "v_sub_f32_e32 v49, v49, v28\n\t"
    "v_mfma_f32_16x16x32_bf16 v[56:59], v[196:199], v[24:27], v[56:59]\n\t"
    "v_exp_f32_e32 v48, v48\n\t"
    "v_mfma_f32_16x16x32_bf16 v[60:63], v[172:175], v[24:27], 0\n\t"
    "v_add_f32_e32 v30, v30, v48\n\t"
    "v_sub_f32_e32 v50, v50, v28\n\t"
    "s_add_u32 m0, %[rbeg], 39936\n\t"
    "s_add_u32 s61, %[wb], 13565952\n\t"
    "v_mfma_f32_16x16x32_bf16 v[60:63], v[168:171], v[32:35], v[60:63]\n\t"
    "v_exp_f32_e32 v49, v49\n\t"
    "v_mfma_f32_16x16x32_bf16 v[60:63], v[164:167], v[40:43], v[60:63]\n\t"
    "v_add_f32_e32 v30, v30, v49\n\t"
    "v_sub_f32_e32 v51, v51, v28\n\t"
    "v_mfma_f32_16x16x32_bf16 v[64:67], v[140:143], v[40:43], 0\n\t"
    "v_exp_f32_e32 v50, v50\n\t"
    "v_mfma_f32_16x16x32_bf16 v[64:67], v[144:147], v[32:35], v[64:67]\n\t"
    "v_add_f32_e32 v31, v31, v50\n\t"
    "v_cvt_pk_bf16_f32 v12, v20, v21\n\t"
    "v_mfma_f32_16x16x32_bf16 v[64:67], v[148:151], v[24:27], v[64:67]\n\t"
    "v_exp_f32_e32 v51, v51\n\t"
    "v_mfma_f32_16x16x32_bf16 v[40:43], a[188:191], v[152:155], 0\n\t"
    "v_add_f32_e32 v31, v31, v51\n\t"
    "v_add_f32_e32 v30, v30, v31\n\t"
    "v_mfma_f32_16x16x32_bf16 v[40:43], a[180:183], v[156:159], v[40:43]\n\t"
    "v_mov_b32_e32 v31, v30\n\t"
    "buffer_load_dwordx4 v7, s[52:55], s61 offen offset:0 nt lds\n\t"
    "v_mfma_f32_16x16x32_bf16 v[40:43], a[172:175], v[160:163], v[40:43]\n\t"
    "v_cvt_pk_bf16_f32 v13, v22, v23\n\t"
    "v_mfma_f32_16x16x32_bf16 v[40:43], a[164:167], v[164:167], v[40:43]\n\t"
    "v_permlane32_swap_b32 v30, v31\n\t"
    "v_add_f32_e32 v30, v30, v31\n\t"
    "v_mfma_f32_16x16x32_bf16 v[40:43], a[156:159], v[168:171], v[40:43]\n\t"
    "v_mov_b32_e32 v31, v30\n\t"
    "v_mfma_f32_16x16x32_bf16 v[40:43], a[148:151], v[172:175], v[40:43]\n\t"
    "buffer_load_dwordx4 v7, s[52:55], s61 offen offset:1024 nt lds\n\t"
    "v_mfma_f32_16x16x32_bf16 v[20:23], a[192:195], v[172:175], 0\n\t"
    "v_permlane16_swap_b32 v30, v31\n\t"
    "v_add_f32_e32 v30, v30, v31\n\t"
    "v_mfma_f32_16x16x32_bf16 v[20:23], a[200:203], v[168:171], v[20:23]\n\t"
    "v_rcp_f32_e32 v30, v30\n\t"
    "v_mfma_f32_16x16x32_bf16 v[20:23], a[208:211], v[164:167], v[20:23]\n\t"
    "v_mul_f32_e32 v36, v36, v30\n\t"
    "v_mul_f32_e32 v37, v37, v30\n\t"
    "v_mfma_f32_16x16x32_bf16 v[20:23], a[216:219], v[160:163], v[20:23]\n\t"
    "v_cvt_pk_bf16_f32 v14, v40, v41\n\t"
    "v_cvt_pk_bf16_f32 v15, v42, v43\n\t"
    "v_mfma_f32_16x16x32_bf16 v[20:23], a[224:227], v[156:159], v[20:23]\n\t"
    "v_cvt_pk_bf16_f32 v24, v36, v37\n\t"
    "v_mul_f32_e32 v38, v38, v30\n\t"
    "buffer_load_dwordx4 v7, s[52:55], s61 offen offset:2048 nt lds\n\t"
    "v_mfma_f32_16x16x32_bf16 v[20:23], a[232:235], v[152:155], v[20:23]\n\t"
    "v_mul_f32_e32 v39, v39, v30\n\t"
    "v_cvt_pk_bf16_f32 v25, v38, v39\n\t"
    "v_mfma_f32_16x16x32_bf16 v[32:35], a[236:239], v[152:155], 0\n\t"
    "v_mul_f32_e32 v16, v16, v30\n\t"
    "v_mul_f32_e32 v17, v17, v30\n\t"
    "v_mfma_f32_16x16x32_bf16 v[32:35], a[228:231], v[156:159], v[32:35]\n\t"
    "v_cvt_pk_bf16_f32 v26, v16, v17\n\t"
    "v_mul_f32_e32 v18, v18, v30\n\t"
    "v_mfma_f32_16x16x32_bf16 v[32:35], a[220:223], v[160:163], v[32:35]\n\t"
    "v_mul_f32_e32 v19, v19, v30\n\t"
    "v_cvt_pk_bf16_f32 v27, v18, v19\n\t"
    "v_mfma_f32_16x16x32_bf16 v[32:35], a[212:215], v[164:167], v[32:35]\n\t"
    "v_cvt_pk_bf16_f32 v36, v20, v21\n\t"
    "v_cvt_pk_bf16_f32 v37, v22, v23\n\t"
    "v_mfma_f32_16x16x32_bf16 v[32:35], a[204:207], v[168:171], v[32:35]\n\t"
    "v_mul_f32_e32 v44, v44, v30\n\t"
    "v_mul_f32_e32 v45, v45, v30\n\t"
    "v_mfma_f32_16x16x32_bf16 v[32:35], a[196:199], v[172:175], v[32:35]\n\t"
    "v_cvt_pk_bf16_f32 v16, v44, v45\n\t"
    "s_add_u32 m0, %[rbeg], 43008\n\t"
    "s_add_u32 s61, %[wb], 13762560\n\t"
    "v_mfma_f32_16x16x32_bf16 v[20:23], a[240:243], v[172:175], 0\n\t"
    "v_mul_f32_e32 v46, v46, v30\n\t"
    "v_mfma_f32_16x16x32_bf16 v[20:23], a[248:251], v[168:171], v[20:23]\n\t"
    "v_mul_f32_e32 v47, v47, v30\n\t"
    "v_cvt_pk_bf16_f32 v17, v46, v47\n\t"
    "v_mfma_f32_16x16x32_bf16 v[20:23], v[224:227], v[164:167], v[20:23]\n\t"
    "v_mul_f32_e32 v48, v48, v30\n\t"
    "v_mul_f32_e32 v49, v49, v30\n\t"
    "buffer_load_dwordx4 v7, s[52:55], s61 offen offset:0 nt lds\n\t"
    "v_mfma_f32_16x16x32_bf16 v[20:23], v[232:235], v[160:163], v[20:23]\n\t"
    "v_cvt_pk_bf16_f32 v38, v32, v33\n\t"
    "v_cvt_pk_bf16_f32 v39, v34, v35\n\t"
    "v_mfma_f32_16x16x32_bf16 v[20:23], v[240:243], v[156:159], v[20:23]\n\t"
    "v_cvt_pk_bf16_f32 v18, v48, v49\n\t"
    "v_mul_f32_e32 v50, v50, v30\n\t"
    "v_mfma_f32_16x16x32_bf16 v[20:23], v[248:251], v[152:155], v[20:23]\n\t"
    "v_mul_f32_e32 v51, v51, v30\n\t"
    "v_cvt_pk_bf16_f32 v19, v50, v51\n\t"
    "v_mfma_f32_16x16x32_bf16 v[32:35], v[252:255], v[152:155], 0\n\t"
    "v_mfma_f32_16x16x32_bf16 v[32:35], v[244:247], v[156:159], v[32:35]\n\t"
    "buffer_load_dwordx4 v7, s[52:55], s61 offen offset:1024 nt lds\n\t"
    "v_mfma_f32_16x16x32_bf16 v[32:35], v[236:239], v[160:163], v[32:35]\n\t"
    "v_mfma_f32_16x16x32_bf16 v[32:35], v[228:231], v[164:167], v[32:35]\n\t"
    "v_cvt_pk_bf16_f32 v28, v20, v21\n\t"
    "v_cvt_pk_bf16_f32 v29, v22, v23\n\t"
    "v_mfma_f32_16x16x32_bf16 v[32:35], a[252:255], v[168:171], v[32:35]\n\t"
    "v_mfma_f32_16x16x32_bf16 v[32:35], a[244:247], v[172:175], v[32:35]\n\t"
    "v_mfma_f32_16x16x32_bf16 v[20:23], a[0:3], v[148:151], 0\n\t"
    "ds_write_b128 v6, v[24:27] offset:2048\n\t"
    "v_mfma_f32_16x16x32_bf16 v[20:23], a[8:11], v[144:147], v[20:23]\n\t"
    "buffer_load_dwordx4 v7, s[52:55], s61 offen offset:2048 nt lds\n\t"
    "v_mfma_f32_16x16x32_bf16 v[20:23], a[16:19], v[140:143], v[20:23]\n\t"
    "v_mfma_f32_16x16x32_bf16 v[20:23], a[24:27], v[136:139], v[20:23]\n\t"
    "v_cvt_pk_bf16_f32 v30, v32, v33\n\t"
    "v_mfma_f32_16x16x32_bf16 v[20:23], a[32:35], v[132:135], v[20:23]\n\t"
    "ds_write_b128 v6, v[16:19] offset:3072\n\t"
    "v_mfma_f32_16x16x32_bf16 v[20:23], a[40:43], v[128:131], v[20:23]\n\t"
    "v_cvt_pk_bf16_f32 v31, v34, v35\n\t"
    "v_mfma_f32_16x16x32_bf16 v[52:55], v[208:211], v[12:15], v[52:55]\n\t"
    "s_add_u32 m0, %[rbeg], 46080\n\t"
    "s_add_u32 s61, %[wb], 13959168\n\t"
    "v_mfma_f32_16x16x32_bf16 v[52:55], v[204:207], v[36:39], v[52:55]\n\t"
    "v_mfma_f32_16x16x32_bf16 v[52:55], v[200:203], v[28:31], v[52:55]\n\t"
    "v_mfma_f32_16x16x32_bf16 v[56:59], v[176:179], v[28:31], v[56:59]\n\t"
    "v_cvt_pk_bf16_f32 v40, v20, v21\n\t"
    "v_cvt_pk_bf16_f32 v41, v22, v23\n\t"
    "v_mfma_f32_16x16x32_bf16 v[56:59], v[180:183], v[36:39], v[56:59]\n\t"
    "v_mfma_f32_16x16x32_bf16 v[56:59], v[184:187], v[12:15], v[56:59]\n\t"
    "buffer_load_dwordx4 v7, s[52:55], s61 offen offset:0 nt lds\n\t"
    "v_mfma_f32_16x16x32_bf16 v[60:63], v[160:163], v[12:15], v[60:63]\n\t"
    "v_max3_f32 v68, v52, v53, v54\n\t"
    "v_mfma_f32_16x16x32_bf16 v[60:63], v[156:159], v[36:39], v[60:63]\n\t"
    "v_mfma_f32_16x16x32_bf16 v[60:63], v[152:155], v[28:31], v[60:63]\n\t"
    "v_mfma_f32_16x16x32_bf16 v[64:67], v[128:131], v[28:31], v[64:67]\n\t"
    "v_max3_f32 v69, v55, v56, v57\n\t"
    "v_max3_f32 v68, v68, v58, v59\n\t"
    "v_mfma_f32_16x16x32_bf16 v[64:67], v[132:135], v[36:39], v[64:67]\n\t"
    "buffer_load_dwordx4 v7, s[52:55], s61 offen offset:1024 nt lds\n\t"
    "v_mfma_f32_16x16x32_bf16 v[64:67], v[136:139], v[12:15], v[64:67]\n\t"
    "v_mfma_f32_16x16x32_bf16 v[16:19], a[44:47], v[128:131], 0\n\t"
    "v_max3_f32 v69, v69, v60, v61\n\t"
    "v_max3_f32 v68, v68, v62, v63\n\t"
    "v_mfma_f32_16x16x32_bf16 v[16:19], a[36:39], v[132:135], v[16:19]\n\t"
    "v_mfma_f32_16x16x32_bf16 v[16:19], a[28:31], v[136:139], v[16:19]\n\t"
    "v_mfma_f32_16x16x32_bf16 v[16:19], a[20:23], v[140:143], v[16:19]\n\t"
    "v_max3_f32 v69, v69, v64, v65\n\t"
    "v_max3_f32 v68, v68, v66, v67\n\t"
    "v_mfma_f32_16x16x32_bf16 v[16:19], a[12:15], v[144:147], v[16:19]\n\t"
    "v_max_f32_e32 v68, v68, v69\n\t"
    "v_mov_b32_e32 v69, v68\n\t"
    "v_mfma_f32_16x16x32_bf16 v[16:19], a[4:7], v[148:151], v[16:19]\n\t"
    "buffer_load_dwordx4 v7, s[52:55], s61 offen offset:2048 nt lds\n\t"
    "v_mfma_f32_16x16x32_bf16 v[12:15], a[48:51], v[148:151], 0\n\t"
    "v_permlane32_swap_b32 v68, v69\n\t"
    "v_max_f32_e32 v68, v68, v69\n\t"
    "v_mfma_f32_16x16x32_bf16 v[12:15], a[56:59], v[144:147], v[12:15]\n\t"
    "v_mov_b32_e32 v69, v68\n\t"
    "v_mfma_f32_16x16x32_bf16 v[12:15], a[64:67], v[140:143], v[12:15]\n\t"
    "s_add_u32 m0, %[rbeg], 49152\n\t"
    "s_add_u32 s61, %[wb], 25165824\n\t"
    "v_mfma_f32_16x16x32_bf16 v[12:15], a[72:75], v[136:139], v[12:15]\n\t"
    "v_cvt_pk_bf16_f32 v42, v16, v17\n\t"
    "v_cvt_pk_bf16_f32 v43, v18, v19\n\t"
    "v_mfma_f32_16x16x32_bf16 v[12:15], a[80:83], v[132:135], v[12:15]\n\t"
    "v_permlane16_swap_b32 v68, v69\n\t"
    "v_max_f32_e32 v68, v68, v69\n\t"
    "v_mfma_f32_16x16x32_bf16 v[12:15], a[88:91], v[128:131], v[12:15]\n\t"
    "v_sub_f32_e32 v52, v52, v68\n\t"
    "v_sub_f32_e32 v53, v53, v68\n\t"
    "v_mfma_f32_16x16x32_bf16 v[16:19], a[92:95], v[128:131], 0\n\t"
    "v_exp_f32_e32 v52, v52\n\t"
    "v_mfma_f32_16x16x32_bf16 v[16:19], a[84:87], v[132:135], v[16:19]\n\t"
    "v_exp_f32_e32 v53, v53\n\t"
    "buffer_load_dwordx4 v7, s[52:55], s61 offen offset:0 nt lds\n\t"
    "v_mfma_f32_16x16x32_bf16 v[16:19], a[76:79], v[136:139], v[16:19]\n\t"
    "v_add_f32_e32 v70, v52, v53\n\t"
    "v_sub_f32_e32 v54, v54, v68\n\t"
    "v_mfma_f32_16x16x32_bf16 v[16:19], a[68:71], v[140:143], v[16:19]\n\t"
    "v_cvt_pk_bf16_f32 v36, v12, v13\n\t"
    "v_cvt_pk_bf16_f32 v37, v14, v15\n\t"
    "v_mfma_f32_16x16x32_bf16 v[16:19], a[60:63], v[144:147], v[16:19]\n\t"
    "v_exp_f32_e32 v54, v54\n\t"
    "v_mfma_f32_16x16x32_bf16 v[16:19], a[52:55], v[148:151], v[16:19]\n\t"
    "v_sub_f32_e32 v55, v55, v68\n\t"
    "v_mfma_f32_16x16x32_bf16 v[12:15], a[96:99], v[148:151], 0\n\t"
    "v_sub_f32_e32 v56, v56, v68\n\t"
    "buffer_load_dwordx4 v7, s[52:55], s61 offen offset:1024 nt lds\n\t"
    "v_mfma_f32_16x16x32_bf16 v[12:15], a[104:107], v[144:147], v[12:15]\n\t"
    "v_exp_f32_e32 v55, v55\n\t"
    "v_mfma_f32_16x16x32_bf16 v[12:15], a[112:115], v[140:143], v[12:15]\n\t"
    "v_add_f32_e32 v71, v54, v55\n\t"
    "v_sub_f32_e32 v57, v57, v68\n\t"
    "v_mfma_f32_16x16x32_bf16 v[12:15], a[120:123], v[136:139], v[12:15]\n\t"
    "v_cvt_pk_bf16_f32 v38, v16, v17\n\t"
    "v_cvt_pk_bf16_f32 v39, v18, v19\n\t"
    "v_mfma_f32_16x16x32_bf16 v[12:15], a[128:131], v[132:135], v[12:15]\n\t"
    "v_exp_f32_e32 v56, v56\n\t"
    "v_mfma_f32_16x16x32_bf16 v[12:15], a[136:139], v[128:131], v[12:15]\n\t"
    "v_add_f32_e32 v70, v70, v56\n\t"
    "v_sub_f32_e32 v58, v58, v68\n\t"
    "buffer_load_dwordx4 v7, s[52:55], s61 offen offset:2048 nt lds\n\t"
    "v_mfma_f32_16x16x32_bf16 v[16:19], a[140:143], v[128:131], 0\n\t"
    "v_exp_f32_e32 v57, v57\n\t"
    "v_mfma_f32_16x16x32_bf16 v[16:19], a[132:135], v[132:135], v[16:19]\n\t"
    "v_add_f32_e32 v70, v70, v57\n\t"
    "v_sub_f32_e32 v59, v59, v68\n\t"
    "v_mfma_f32_16x16x32_bf16 v[16:19], a[124:127], v[136:139], v[16:19]\n\t"
    "v_exp_f32_e32 v58, v58\n\t"
    "v_mfma_f32_16x16x32_bf16 v[16:19], a[116:119], v[140:143], v[16:19]\n\t"
    "v_cvt_pk_bf16_f32 v32, v12, v13\n\t"
    "v_cvt_pk_bf16_f32 v33, v14, v15\n\t"
    "v_mfma_f32_16x16x32_bf16 v[16:19], a[108:111], v[144:147], v[16:19]\n\t"
    "v_add_f32_e32 v71, v71, v58\n\t"
    "v_sub_f32_e32 v60, v60, v68\n\t"
    "v_mfma_f32_16x16x32_bf16 v[16:19], a[100:103], v[148:151], v[16:19]\n\t"
    "v_exp_f32_e32 v59, v59\n\t"
    "v_mfma_f32_16x16x32_bf16 v[44:47], a[144:147], v[148:151], 0\n\t"
    "v_add_f32_e32 v71, v71, v59\n\t"
    "v_sub_f32_e32 v61, v61, v68\n\t"
    "v_mfma_f32_16x16x32_bf16 v[44:47], a[152:155], v[144:147], v[44:47]\n\t"
    "v_exp_f32_e32 v60, v60\n\t"
    "s_mov_b32 s60, 8\n\t"
    "s_add_u32 s61, %[wb], 25362432\n\t"
    ".Lmid_%=:\n\t"
    "v_mfma_f32_16x16x32_bf16 v[44:47], a[160:163], v[140:143], v[44:47]\n\t"
    "s_add_u32 m0, s62, 3072\n\t"
    "v_add_f32_e32 v70, v70, v60\n\t"
    "v_mfma_f32_16x16x32_bf16 v[44:47], a[168:171], v[136:139], v[44:47]\n\t"
    "buffer_load_dwordx4 v7, s[52:55], s61 offen offset:0 nt lds\n\t"
    "v_cvt_pk_bf16_f32 v34, v16, v17\n\t"
    "v_mfma_f32_16x16x32_bf16 v[44:47], a[176:179], v[132:135], v[44:47]\n\t"
    "v_cvt_pk_bf16_f32 v35, v18, v19\n\t"
    "v_sub_f32_e32 v62, v62, v68\n\t"
    "v_mfma_f32_16x16x32_bf16 v[44:47], a[184:187], v[128:131], v[44:47]\n\t"
    "v_exp_f32_e32 v61, v61\n\t"
    "v_mfma_f32_16x16x32_bf16 v[12:15], v[220:223], v[40:43], 0\n\t"
    "v_add_f32_e32 v70, v70, v61\n\t"
    "v_sub_f32_e32 v63, v63, v68\n\t"
    "v_mfma_f32_16x16x32_bf16 v[12:15], v[216:219], v[36:39], v[12:15]\n\t"
    "v_exp_f32_e32 v62, v62\n\t"
    "v_mfma_f32_16x16x32_bf16 v[12:15], v[212:215], v[32:35], v[12:15]\n\t"
    "s_waitcnt vmcnt(22)\n\t"
    "v_add_f32_e32 v71, v71, v62\n\t"
    "v_mfma_f32_16x16x32_bf16 v[16:19], v[188:191], v[32:35], 0\n\t"
    "v_exp_f32_e32 v63, v63\n\t"
    "v_mfma_f32_16x16x32_bf16 v[16:19], v[192:195], v[36:39], v[16:19]\n\t"
    "ds_read_b128 v[220:223], v0 offset:0\n\t"
    "v_add_f32_e32 v71, v71, v63\n\t"
    "v_mfma_f32_16x16x32_bf16 v[16:19], v[196:199], v[40:43], v[16:19]\n\t"
    "v_sub_f32_e32 v64, v64, v68\n\t"
    "v_sub_f32_e32 v65, v65, v68\n\t"
    "v_mfma_f32_16x16x32_bf16 v[20:23], v[172:175], v[40:43], 0\n\t"
    "ds_read_b128 v[216:219], v1 offset:0\n\t"
    "v_sub_f32_e32 v66, v66, v68\n\t"
    "v_mfma_f32_16x16x32_bf16 v[20:23], v[168:171], v[36:39], v[20:23]\n\t"
    "v_exp_f32_e32 v64, v64\n\t"
    "v_mfma_f32_16x16x32_bf16 v[20:23], v[164:167], v[32:35], v[20:23]\n\t"
    "ds_read_b128 v[212:215], v2 offset:0\n\t"
    "v_add_f32_e32 v70, v70, v64\n\t"
    "v_mfma_f32_16x16x32_bf16 v[24:27], v[140:143], v[32:35], 0\n\t"
    "v_exp_f32_e32 v65, v65\n\t"
    "v_mfma_f32_16x16x32_bf16 v[24:27], v[144:147], v[36:39], v[24:27]\n\t"
    "ds_read_b128 v[28:31], v3 offset:0\n\t"
    "v_add_f32_e32 v70, v70, v65\n\t"
    "v_mfma_f32_16x16x32_bf16 v[24:27], v[148:151], v[40:43], v[24:27]\n\t"
    "v_exp_f32_e32 v66, v66\n\t"
    "v_mfma_f32_16x16x32_bf16 v[48:51], a[188:191], v[128:131], 0\n\t"
    "ds_read_b128 v[32:35], v4 offset:0\n\t"
    "v_add_f32_e32 v71, v71, v66\n\t"
    "v_mfma_f32_16x16x32_bf16 v[48:51], a[180:183], v[132:135], v[48:51]\n\t"
    "buffer_load_dwordx4 v7, s[52:55], s61 offen offset:1024 nt lds\n\t"
    "v_sub_f32_e32 v67, v67, v68\n\t"
    "v_mfma_f32_16x16x32_bf16 v[48:51], a[172:175], v[136:139], v[48:51]\n\t"
    "ds_read_b128 v[36:39], v5 offset:0\n\t"
    "v_cvt_pk_bf16_f32 v40, v44, v45\n\t"
    "v_mfma_f32_16x16x32_bf16 v[48:51], a[164:167], v[140:143], v[48:51]\n\t"
    "v_exp_f32_e32 v67, v67\n\t"
    "v_mfma_f32_16x16x32_bf16 v[48:51], a[156:159], v[144:147], v[48:51]\n\t"
    "v_add_f32_e32 v71, v71, v67\n\t"
    "v_add_f32_e32 v70, v70, v71\n\t"
    "v_mfma_f32_16x16x32_bf16 v[48:51], a[148:151], v[148:151], v[48:51]\n\t"
    "v_mov_b32_e32 v71, v70\n\t"
    "v_cvt_pk_bf16_f32 v41, v46, v47\n\t"
    "v_mfma_f32_16x16x32_bf16 v[72:75], a[192:195], v[148:151], 0\n\t"
    "v_mfma_f32_16x16x32_bf16 v[72:75], a[200:203], v[144:147], v[72:75]\n\t"
    "v_permlane32_swap_b32 v70, v71\n\t"
    "v_add_f32_e32 v70, v70, v71\n\t"
    "v_mfma_f32_16x16x32_bf16 v[72:75], a[208:211], v[140:143], v[72:75]\n\t"
    "v_mov_b32_e32 v71, v70\n\t"
    "v_mfma_f32_16x16x32_bf16 v[72:75], a[216:219], v[136:139], v[72:75]\n\t"
    "v_cvt_pk_bf16_f32 v42, v48, v49\n\t"
    "v_cvt_pk_bf16_f32 v43, v50, v51\n\t"
    "v_mfma_f32_16x16x32_bf16 v[72:75], a[224:227], v[132:135], v[72:75]\n\t"
    "v_permlane16_swap_b32 v70, v71\n\t"
    "v_add_f32_e32 v70, v70, v71\n\t"
    "v_mfma_f32_16x16x32_bf16 v[72:75], a[232:235], v[128:131], v[72:75]\n\t"
    "v_rcp_f32_e32 v70, v70\n\t"
    "v_mfma_f32_16x16x32_bf16 v[76:79], a[236:239], v[128:131], 0\n\t"
    "v_mul_f32_e32 v52, v52, v70\n\t"
    "v_mul_f32_e32 v53, v53, v70\n\t"
    "v_mfma_f32_16x16x32_bf16 v[76:79], a[228:231], v[132:135], v[76:79]\n\t"
    "v_cvt_pk_bf16_f32 v44, v52, v53\n\t"
    "v_mul_f32_e32 v54, v54, v70\n\t"
    "v_mfma_f32_16x16x32_bf16 v[76:79], a[220:223], v[136:139], v[76:79]\n\t"
    "v_mul_f32_e32 v55, v55, v70\n\t"
    "v_cvt_pk_bf16_f32 v45, v54, v55\n\t"
    "v_mfma_f32_16x16x32_bf16 v[76:79], a[212:215], v[140:143], v[76:79]\n\t"
    "v_cvt_pk_bf16_f32 v48, v72, v73\n\t"
    "v_cvt_pk_bf16_f32 v49, v74, v75\n\t"
    "v_mfma_f32_16x16x32_bf16 v[76:79], a[204:207], v[144:147], v[76:79]\n\t"
    "v_mul_f32_e32 v56, v56, v70\n\t"
    "v_mul_f32_e32 v57, v57, v70\n\t"
    "v_mfma_f32_16x16x32_bf16 v[76:79], a[196:199], v[148:151], v[76:79]\n\t"
    "buffer_load_dwordx4 v7, s[52:55], s61 offen offset:2048 nt lds\n\t"
    "v_cvt_pk_bf16_f32 v46, v56, v57\n\t"
    "v_mfma_f32_16x16x32_bf16 v[72:75], a[240:243], v[148:151], 0\n\t"
    "s_add_u32 s61, s61, 196608\n\t"
    "v_mul_f32_e32 v58, v58, v70\n\t"
    "v_mfma_f32_16x16x32_bf16 v[72:75], a[248:251], v[144:147], v[72:75]\n\t"
    "v_mul_f32_e32 v59, v59, v70\n\t"
    "v_cvt_pk_bf16_f32 v47, v58, v59\n\t"
    "v_mfma_f32_16x16x32_bf16 v[72:75], v[224:227], v[140:143], v[72:75]\n\t"
    "v_mul_f32_e32 v60, v60, v70\n\t"
    "v_mul_f32_e32 v61, v61, v70\n\t"
    "v_mfma_f32_16x16x32_bf16 v[72:75], v[232:235], v[136:139], v[72:75]\n\t"
    "v_cvt_pk_bf16_f32 v50, v76, v77\n\t"
    "v_cvt_pk_bf16_f32 v51, v78, v79\n\t"
    "v_mfma_f32_16x16x32_bf16 v[72:75], v[240:243], v[132:135], v[72:75]\n\t"
    "v_cvt_pk_bf16_f32 v52, v60, v61\n\t"
    "v_mul_f32_e32 v62, v62, v70\n\t"
    "v_mfma_f32_16x16x32_bf16 v[72:75], v[248:251], v[128:131], v[72:75]\n\t"
    "v_mul_f32_e32 v63, v63, v70\n\t"
    "v_cvt_pk_bf16_f32 v53, v62, v63\n\t"
    "v_mfma_f32_16x16x32_bf16 v[56:59], v[252:255], v[128:131], 0\n\t"
    "v_mul_f32_e32 v64, v64, v70\n\t"
    "v_mul_f32_e32 v65, v65, v70\n\t"
    "v_mfma_f32_16x16x32_bf16 v[56:59], v[244:247], v[132:135], v[56:59]\n\t"
    "v_cvt_pk_bf16_f32 v54, v64, v65\n\t"
    "v_mul_f32_e32 v66, v66, v70\n\t"
    "v_mfma_f32_16x16x32_bf16 v[56:59], v[236:239], v[136:139], v[56:59]\n\t"
    "v_mul_f32_e32 v67, v67, v70\n\t"
    "v_cvt_pk_bf16_f32 v55, v66, v67\n\t"
    "v_mfma_f32_16x16x32_bf16 v[56:59], v[228:231], v[140:143], v[56:59]\n\t"
    "v_cvt_pk_bf16_f32 v60, v72, v73\n\t"
    "v_cvt_pk_bf16_f32 v61, v74, v75\n\t"
    "v_mfma_f32_16x16x32_bf16 v[56:59], a[252:255], v[144:147], v[56:59]\n\t"
    "s_mov_b32 s63, s62\n\t"
    "s_mov_b32 s62, s64\n\t"
    "v_mfma_f32_16x16x32_bf16 v[56:59], a[244:247], v[148:151], v[56:59]\n\t"
    "s_sub_u32 s60, s60, 1\n\t"
    "s_cmp_lg_u32 s60, 0\n\t"
    "s_cbranch_scc0 .Ltail_%=\n\t"
    "s_waitcnt lgkmcnt(0)\n\t"
    "v_mfma_f32_16x16x32_bf16 v[64:67], a[0:3], v[220:223], 0\n\t"
    "ds_write_b128 v6, v[44:47] offset:4096\n\t"
    "v_mfma_f32_16x16x32_bf16 v[64:67], a[8:11], v[216:219], v[64:67]\n\t"
    "v_mfma_f32_16x16x32_bf16 v[64:67], a[16:19], v[212:215], v[64:67]\n\t"
    "s_add_u32 m0, s63, 6144\n\t"
    "v_mfma_f32_16x16x32_bf16 v[64:67], a[24:27], v[28:31], v[64:67]\n\t"
    "buffer_load_dwordx4 v7, s[52:55], s61 offen offset:0 nt lds\n\t"
    "v_cvt_pk_bf16_f32 v62, v56, v57\n\t"
    "v_mfma_f32_16x16x32_bf16 v[64:67], a[32:35], v[32:35], v[64:67]\n\t"
    "ds_write_b128 v6, v[52:55] offset:5120\n\t"
    "v_mfma_f32_16x16x32_bf16 v[64:67], a[40:43], v[36:39], v[64:67]\n\t"
    "v_cvt_pk_bf16_f32 v63, v58, v59\n\t"
    "v_mfma_f32_16x16x32_bf16 v[12:15], v[208:211], v[40:43], v[12:15]\n\t"
    "v_mfma_f32_16x16x32_bf16 v[12:15], v[204:207], v[48:51], v[12:15]\n\t"
    "v_mfma_f32_16x16x32_bf16 v[12:15], v[200:203], v[60:63], v[12:15]\n\t"
    "v_mfma_f32_16x16x32_bf16 v[16:19], v[176:179], v[60:63], v[16:19]\n\t"
    "v_cvt_pk_bf16_f32 v44, v64, v65\n\t"
    "v_cvt_pk_bf16_f32 v45, v66, v67\n\t"
    "v_mfma_f32_16x16x32_bf16 v[16:19], v[180:183], v[48:51], v[16:19]\n\t"
    "s_waitcnt lgkmcnt(0)\n\t"
    "s_barrier\n\t"
    "v_mfma_f32_16x16x32_bf16 v[16:19], v[184:187], v[40:43], v[16:19]\n\t"
    "v_mfma_f32_16x16x32_bf16 v[20:23], v[160:163], v[40:43], v[20:23]\n\t"
    "v_max3_f32 v52, v12, v13, v14\n\t"
    "v_mfma_f32_16x16x32_bf16 v[20:23], v[156:159], v[48:51], v[20:23]\n\t"
    "v_mfma_f32_16x16x32_bf16 v[20:23], v[152:155], v[60:63], v[20:23]\n\t"
    "v_mfma_f32_16x16x32_bf16 v[24:27], v[128:131], v[60:63], v[24:27]\n\t"
    "v_max3_f32 v53, v15, v16, v17\n\t"
    "v_max3_f32 v52, v52, v18, v19\n\t"
    "v_mfma_f32_16x16x32_bf16 v[24:27], v[132:135], v[48:51], v[24:27]\n\t"
    "s_waitcnt vmcnt(7)\n\t"
    "v_mfma_f32_16x16x32_bf16 v[24:27], v[136:139], v[40:43], v[24:27]\n\t"
    "ds_read_b128 v[188:191], v2 offset:6144\n\t"
    "v_mfma_f32_16x16x32_bf16 v[56:59], a[44:47], v[36:39], 0\n\t"
    "v_max3_f32 v53, v53, v20, v21\n\t"
    "v_max3_f32 v52, v52, v22, v23\n\t"
    "v_mfma_f32_16x16x32_bf16 v[56:59], a[36:39], v[32:35], v[56:59]\n\t"
    "buffer_load_dwordx4 v7, s[52:55], s61 offen offset:1024 nt lds\n\t"
    "ds_read_b128 v[192:195], v1 offset:6144\n\t"
    "v_mfma_f32_16x16x32_bf16 v[56:59], a[28:31], v[28:31], v[56:59]\n\t"
    "v_mfma_f32_16x16x32_bf16 v[56:59], a[20:23], v[212:215], v[56:59]\n\t"
    "ds_read_b128 v[196:199], v0 offset:6144\n\t"
    "v_max3_f32 v53, v53, v24, v25\n\t"
    "v_mfma_f32_16x16x32_bf16 v[56:59], a[12:15], v[216:219], v[56:59]\n\t"
    "v_max3_f32 v52, v52, v26, v27\n\t"
    "v_max_f32_e32 v52, v52, v53\n\t"
    "v_mfma_f32_16x16x32_bf16 v[56:59], a[4:7], v[220:223], v[56:59]\n\t"
    "ds_read_b128 v[172:175], v0 offset:12288\n\t"
    "v_mov_b32_e32 v53, v52\n\t"
    "v_mfma_f32_16x16x32_bf16 v[40:43], a[48:51], v[220:223], 0\n\t"
    "v_mfma_f32_16x16x32_bf16 v[40:43], a[56:59], v[216:219], v[40:43]\n\t"
    "ds_read_b128 v[168:171], v1 offset:12288\n\t"
    "v_permlane32_swap_b32 v52, v53\n\t"
    "v_mfma_f32_16x16x32_bf16 v[40:43], a[64:67], v[212:215], v[40:43]\n\t"
    "v_max_f32_e32 v52, v52, v53\n\t"
    "v_mov_b32_e32 v53, v52\n\t"
    "v_mfma_f32_16x16x32_bf16 v[40:43], a[72:75], v[28:31], v[40:43]\n\t"
    "ds_read_b128 v[164:167], v2 offset:12288\n\t"
    "v_cvt_pk_bf16_f32 v46, v56, v57\n\t"
    "v_mfma_f32_16x16x32_bf16 v[40:43], a[80:83], v[32:35], v[40:43]\n\t"
    "v_cvt_pk_bf16_f32 v47, v58, v59\n\t"
    "v_permlane16_swap_b32 v52, v53\n\t"
    "v_mfma_f32_16x16x32_bf16 v[40:43], a[88:91], v[36:39], v[40:43]\n\t"
    "ds_read_b128 v[140:143], v2 offset:18432\n\t"
    "v_max_f32_e32 v52, v52, v53\n\t"
    "v_mfma_f32_16x16x32_bf16 v[48:51], a[92:95], v[36:39], 0\n\t"
    "v_sub_f32_e32 v12, v12, v52\n\t"
    "v_sub_f32_e32 v13, v13, v52\n\t"
    "v_mfma_f32_16x16x32_bf16 v[48:51], a[84:87], v[32:35], v[48:51]\n\t"
    "ds_read_b128 v[144:147], v1 offset:18432\n\t"
    "v_sub_f32_e32 v14, v14, v52\n\t"
    "v_mfma_f32_16x16x32_bf16 v[48:51], a[76:79], v[28:31], v[48:51]\n\t"
    "v_exp_f32_e32 v12, v12\n\t"
    "v_mfma_f32_16x16x32_bf16 v[48:51], a[68:71], v[212:215], v[48:51]\n\t"
    "ds_read_b128 v[148:151], v0 offset:18432\n\t"
    "v_cvt_pk_bf16_f32 v56, v40, v41\n\t"
    "v_mfma_f32_16x16x32_bf16 v[48:51], a[60:63], v[216:219], v[48:51]\n\t"
    "v_cvt_pk_bf16_f32 v57, v42, v43\n\t"
    "v_sub_f32_e32 v15, v15, v52\n\t"
    "v_mfma_f32_16x16x32_bf16 v[48:51], a[52:55], v[220:223], v[48:51]\n\t"
    "buffer_load_dwordx4 v7, s[52:55], s61 offen offset:2048 nt lds\n\t"
    "v_sub_f32_e32 v16, v16, v52\n\t"
    "v_mfma_f32_16x16x32_bf16 v[60:63], a[96:99], v[220:223], 0\n\t"
    "s_add_u32 s61, s61, 196608\n\t"
    "v_sub_f32_e32 v17, v17, v52\n\t"
    "v_mfma_f32_16x16x32_bf16 v[60:63], a[104:107], v[216:219], v[60:63]\n\t"
    "v_exp_f32_e32 v13, v13\n\t"
    "v_mfma_f32_16x16x32_bf16 v[60:63], a[112:115], v[212:215], v[60:63]\n\t"
    "v_add_f32_e32 v54, v12, v13\n\t"
    "v_sub_f32_e32 v18, v18, v52\n\t"
    "v_mfma_f32_16x16x32_bf16 v[60:63], a[120:123], v[28:31], v[60:63]\n\t"
    "v_cvt_pk_bf16_f32 v58, v48, v49\n\t"
    "v_cvt_pk_bf16_f32 v59, v50, v51\n\t"
    "v_mfma_f32_16x16x32_bf16 v[60:63], a[128:131], v[32:35], v[60:63]\n\t"
    "v_exp_f32_e32 v14, v14\n\t"
    "v_mfma_f32_16x16x32_bf16 v[60:63], a[136:139], v[36:39], v[60:63]\n\t"
    "v_exp_f32_e32 v15, v15\n\t"
    "v_mfma_f32_16x16x32_bf16 v[40:43], a[140:143], v[36:39], 0\n\t"
    "v_add_f32_e32 v55, v14, v15\n\t"
    "v_sub_f32_e32 v19, v19, v52\n\t"
    "v_mfma_f32_16x16x32_bf16 v[40:43], a[132:135], v[32:35], v[40:43]\n\t"
    "v_exp_f32_e32 v16, v16\n\t"
    "v_mfma_f32_16x16x32_bf16 v[40:43], a[124:127], v[28:31], v[40:43]\n\t"
    "v_add_f32_e32 v54, v54, v16\n\t"
    "v_sub_f32_e32 v20, v20, v52\n\t"
    "v_mfma_f32_16x16x32_bf16 v[40:43], a[116:119], v[212:215], v[40:43]\n\t"
    "v_cvt_pk_bf16_f32 v48, v60, v61\n\t"
    "v_cvt_pk_bf16_f32 v49, v62, v63\n\t"
    "v_mfma_f32_16x16x32_bf16 v[40:43], a[108:111], v[216:219], v[40:43]\n\t"
    "v_exp_f32_e32 v17, v17\n\t"
    "v_mfma_f32_16x16x32_bf16 v[40:43], a[100:103], v[220:223], v[40:43]\n\t"
    "v_add_f32_e32 v54, v54, v17\n\t"
    "v_sub_f32_e32 v21, v21, v52\n\t"
    "v_mfma_f32_16x16x32_bf16 v[60:63], a[144:147], v[220:223], 0\n\t"
    "v_exp_f32_e32 v18, v18\n\t"
    "v_mfma_f32_16x16x32_bf16 v[60:63], a[152:155], v[216:219], v[60:63]\n\t"
    "v_add_f32_e32 v55, v55, v18\n\t"
    "v_exp_f32_e32 v19, v19\n\t"
    "v_mfma_f32_16x16x32_bf16 v[60:63], a[160:163], v[212:215], v[60:63]\n\t"
    "s_add_u32 m0, s63, 9216\n\t"
    "v_add_f32_e32 v55, v55, v19\n\t"
    "v_mfma_f32_16x16x32_bf16 v[60:63], a[168:171], v[28:31], v[60:63]\n\t"
    "buffer_load_dwordx4 v7, s[52:55], s61 offen offset:0 nt lds\n\t"
    "v_cvt_pk_bf16_f32 v50, v40, v41\n\t"
    "v_mfma_f32_16x16x32_bf16 v[60:63], a[176:179], v[32:35], v[60:63]\n\t"
    "v_cvt_pk_bf16_f32 v51, v42, v43\n\t"
    "v_sub_f32_e32 v22, v22, v52\n\t"
    "v_mfma_f32_16x16x32_bf16 v[60:63], a[184:187], v[36:39], v[60:63]\n\t"
    "v_exp_f32_e32 v20, v20\n\t"
    "v_mfma_f32_16x16x32_bf16 v[64:67], v[220:223], v[44:47], 0\n\t"
    "v_add_f32_e32 v54, v54, v20\n\t"
    "v_sub_f32_e32 v23, v23, v52\n\t"
    "v_mfma_f32_16x16x32_bf16 v[64:67], v[216:219], v[56:59], v[64:67]\n\t"
    "v_exp_f32_e32 v21, v21\n\t"
    "v_mfma_f32_16x16x32_bf16 v[64:67], v[212:215], v[48:51], v[64:67]\n\t"
    "v_add_f32_e32 v54, v54, v21\n\t"
    "s_waitcnt lgkmcnt(0)\n\t"
    "v_mfma_f32_16x16x32_bf16 v[40:43], v[188:191], v[48:51], 0\n\t"
    "v_exp_f32_e32 v22, v22\n\t"
    "v_mfma_f32_16x16x32_bf16 v[40:43], v[192:195], v[56:59], v[40:43]\n\t"
    "v_add_f32_e32 v55, v55, v22\n\t"
    "v_sub_f32_e32 v24, v24, v52\n\t"
    "v_mfma_f32_16x16x32_bf16 v[40:43], v[196:199], v[44:47], v[40:43]\n\t"
    "ds_read_b128 v[184:187], v3 offset:6144\n\t"
    "v_sub_f32_e32 v25, v25, v52\n\t"
    "v_mfma_f32_16x16x32_bf16 v[68:71], v[172:175], v[44:47], 0\n\t"
    "v_exp_f32_e32 v23, v23\n\t"
    "v_mfma_f32_16x16x32_bf16 v[68:71], v[168:171], v[56:59], v[68:71]\n\t"
    "ds_read_b128 v[180:183], v4 offset:6144\n\t"
    "v_add_f32_e32 v55, v55, v23\n\t"
    "v_mfma_f32_16x16x32_bf16 v[68:71], v[164:167], v[48:51], v[68:71]\n\t"
    "v_exp_f32_e32 v24, v24\n\t"
    "v_mfma_f32_16x16x32_bf16 v[72:75], v[140:143], v[48:51], 0\n\t"
    "ds_read_b128 v[176:179], v5 offset:6144\n\t"
    "v_add_f32_e32 v54, v54, v24\n\t"
    "v_mfma_f32_16x16x32_bf16 v[72:75], v[144:147], v[56:59], v[72:75]\n\t"
    "v_exp_f32_e32 v25, v25\n\t"
    "v_mfma_f32_16x16x32_bf16 v[72:75], v[148:151], v[44:47], v[72:75]\n\t"
    "ds_read_b128 v[208:211], v3 offset:0\n\t"
    "v_add_f32_e32 v54, v54, v25\n\t"
    "v_mfma_f32_16x16x32_bf16 v[48:51], a[188:191], v[36:39], 0\n\t"
    "v_sub_f32_e32 v26, v26, v52\n\t"
    "v_sub_f32_e32 v27, v27, v52\n\t"
    "v_mfma_f32_16x16x32_bf16 v[48:51], a[180:183], v[32:35], v[48:51]\n\t"
    "buffer_load_dwordx4 v7, s[52:55], s61 offen offset:1024 nt lds\n\t"
    "ds_read_b128 v[204:207], v4 offset:0\n\t"
    ".Lh5_%=:\n\t"
    "s_cbranch_execz .LrM_%=\n\t"
    "v_mfma_f32_16x16x32_bf16 v[48:51], a[172:175], v[28:31], v[48:51]\n\t"
    "v_exp_f32_e32 v26, v26\n\t"
    "v_mfma_f32_16x16x32_bf16 v[48:51], a[164:167], v[212:215], v[48:51]\n\t"
    "ds_read_b128 v[200:203], v5 offset:0\n\t"
    "v_add_f32_e32 v55, v55, v26\n\t"
    "v_mfma_f32_16x16x32_bf16 v[48:51], a[156:159], v[216:219], v[48:51]\n\t"
    "v_exp_f32_e32 v27, v27\n\t"
    "v_mfma_f32_16x16x32_bf16 v[48:51], a[148:151], v[220:223], v[48:51]\n\t"
    "ds_read_b128 v[160:163], v3 offset:12288\n\t"
    "v_add_f32_e32 v55, v55, v27\n\t"
    "v_mfma_f32_16x16x32_bf16 v[44:47], a[192:195], v[220:223], 0\n\t"
    "v_add_f32_e32 v54, v54, v55\n\t"
    "v_mov_b32_e32 v55, v54\n\t"
    "v_mfma_f32_16x16x32_bf16 v[44:47], a[200:203], v[216:219], v[44:47]\n\t"
    "ds_read_b128 v[156:159], v4 offset:12288\n\t"
    "v_cvt_pk_bf16_f32 v56, v60, v61\n\t"
    "v_mfma_f32_16x16x32_bf16 v[44:47], a[208:211], v[212:215], v[44:47]\n\t"
    "v_permlane32_swap_b32 v54, v55\n\t"
    "v_add_f32_e32 v54, v54, v55\n\t"
    "v_mfma_f32_16x16x32_bf16 v[44:47], a[216:219], v[28:31], v[44:47]\n\t"
    "ds_read_b128 v[152:155], v5 offset:12288\n\t"
    "v_cvt_pk_bf16_f32 v58, v48, v49\n\t"
    "v_mfma_f32_16x16x32_bf16 v[44:47], a[224:227], v[32:35], v[44:47]\n\t"
    "v_cvt_pk_bf16_f32 v59, v50, v51\n\t"
    "v_mov_b32_e32 v55, v54\n\t"
    "v_mfma_f32_16x16x32_bf16 v[44:47], a[232:235], v[36:39], v[44:47]\n\t"
    "ds_read_b128 v[128:131], v5 offset:18432\n\t"
    "v_cvt_pk_bf16_f32 v57, v62, v63\n\t"
    "v_mfma_f32_16x16x32_bf16 v[76:79], a[236:239], v[36:39], 0\n\t"
    "v_permlane16_swap_b32 v54, v55\n\t"
    "v_add_f32_e32 v54, v54, v55\n\t"
    "v_mfma_f32_16x16x32_bf16 v[76:79], a[228:231], v[32:35], v[76:79]\n\t"
    "ds_read_b128 v[132:135], v4 offset:18432\n\t"
    "v_mfma_f32_16x16x32_bf16 v[76:79], a[220:223], v[28:31], v[76:79]\n\t"
    "v_rcp_f32_e32 v54, v54\n\t"
    "v_mfma_f32_16x16x32_bf16 v[76:79], a[212:215], v[212:215], v[76:79]\n\t"
    "ds_read_b128 v[136:139], v3 offset:18432\n\t"
    "v_cvt_pk_bf16_f32 v48, v44, v45\n\t"
    "v_mfma_f32_16x16x32_bf16 v[76:79], a[204:207], v[216:219], v[76:79]\n\t"
    "v_cvt_pk_bf16_f32 v49, v46, v47\n\t"
    "v_mul_f32_e32 v12, v12, v54\n\t"
    "v_mfma_f32_16x16x32_bf16 v[76:79], a[196:199], v[220:223], v[76:79]\n\t"
    "buffer_load_dwordx4 v7, s[52:55], s61 offen offset:2048 nt lds\n\t"
    "v_mul_f32_e32 v13, v13, v54\n\t"
    "v_mfma_f32_16x16x32_bf16 v[80:83], a[240:243], v[220:223], 0\n\t"
    "s_add_u32 s61, s61, 196608\n\t"
    "v_cvt_pk_bf16_f32 v60, v12, v13\n\t"
    "v_mfma_f32_16x16x32_bf16 v[80:83], a[248:251], v[216:219], v[80:83]\n\t"
    "v_mul_f32_e32 v14, v14, v54\n\t"
    "v_mul_f32_e32 v15, v15, v54\n\t"
    "v_mfma_f32_16x16x32_bf16 v[80:83], v[224:227], v[212:215], v[80:83]\n\t"
    "v_cvt_pk_bf16_f32 v61, v14, v15\n\t"
    "v_mul_f32_e32 v16, v16, v54\n\t"
    "v_mfma_f32_16x16x32_bf16 v[80:83], v[232:235], v[28:31], v[80:83]\n\t"
    "v_cvt_pk_bf16_f32 v50, v76, v77\n\t"
    "v_cvt_pk_bf16_f32 v51, v78, v79\n\t"
    "v_mfma_f32_16x16x32_bf16 v[80:83], v[240:243], v[32:35], v[80:83]\n\t"
    "v_mul_f32_e32 v17, v17, v54\n\t"
    "v_cvt_pk_bf16_f32 v62, v16, v17\n\t"
    "v_mfma_f32_16x16x32_bf16 v[80:83], v[248:251], v[36:39], v[80:83]\n\t"
    "v_mul_f32_e32 v18, v18, v54\n\t"
    "v_mul_f32_e32 v19, v19, v54\n\t"
    "v_mfma_f32_16x16x32_bf16 v[12:15], v[252:255], v[36:39], 0\n\t"
    "v_cvt_pk_bf16_f32 v63, v18, v19\n\t"
    "v_mul_f32_e32 v20, v20, v54\n\t"
    "v_mfma_f32_16x16x32_bf16 v[12:15], v[244:247], v[32:35], v[12:15]\n\t"
    "v_mul_f32_e32 v21, v21, v54\n\t"
    "v_cvt_pk_bf16_f32 v44, v20, v21\n\t"
    "v_mfma_f32_16x16x32_bf16 v[12:15], v[236:239], v[28:31], v[12:15]\n\t"
    "v_mul_f32_e32 v22, v22, v54\n\t"
    "v_mul_f32_e32 v23, v23, v54\n\t"
    "v_mfma_f32_16x16x32_bf16 v[12:15], v[228:231], v[212:215], v[12:15]\n\t"
    "v_cvt_pk_bf16_f32 v16, v80, v81\n\t"
    "v_cvt_pk_bf16_f32 v17, v82, v83\n\t"
    "v_mfma_f32_16x16x32_bf16 v[12:15], a[252:255], v[216:219], v[12:15]\n\t"
    "v_cvt_pk_bf16_f32 v45, v22, v23\n\t"
    "v_mul_f32_e32 v24, v24, v54\n\t"
    "v_mfma_f32_16x16x32_bf16 v[12:15], a[244:247], v[220:223], v[12:15]\n\t"
    "v_mul_f32_e32 v25, v25, v54\n\t"
    "v_cvt_pk_bf16_f32 v46, v24, v25\n\t"
    "v_mfma_f32_16x16x32_bf16 v[28:31], a[0:3], v[196:199], 0\n\t"
    "ds_write_b128 v6, v[60:63] offset:6144\n\t"
    "v_mfma_f32_16x16x32_bf16 v[28:31], a[8:11], v[192:195], v[28:31]\n\t"
    "v_mul_f32_e32 v26, v26, v54\n\t"
    "v_mul_f32_e32 v27, v27, v54\n\t"
    "v_mfma_f32_16x16x32_bf16 v[28:31], a[16:19], v[188:191], v[28:31]\n\t"
    "s_add_u32 m0, s63, 12288\n\t"
    "s_waitcnt lgkmcnt(1)\n\t"
    "v_mfma_f32_16x16x32_bf16 v[28:31], a[24:27], v[184:187], v[28:31]\n\t"
    "buffer_load_dwordx4 v7, s[52:55], s61 offen offset:0 nt lds\n\t"
    "v_cvt_pk_bf16_f32 v18, v12, v13\n\t"
    "v_cvt_pk_bf16_f32 v47, v26, v27\n\t"
    "v_mfma_f32_16x16x32_bf16 v[28:31], a[32:35], v[180:183], v[28:31]\n\t"
    "ds_write_b128 v6, v[44:47] offset:7168\n\t"
    "v_mfma_f32_16x16x32_bf16 v[28:31], a[40:43], v[176:179], v[28:31]\n\t"
    "v_cvt_pk_bf16_f32 v19, v14, v15\n\t"
    "v_mfma_f32_16x16x32_bf16 v[64:67], v[208:211], v[56:59], v[64:67]\n\t"
    "v_mfma_f32_16x16x32_bf16 v[64:67], v[204:207], v[48:51], v[64:67]\n\t"
    "v_mfma_f32_16x16x32_bf16 v[64:67], v[200:203], v[16:19], v[64:67]\n\t"
    "v_mfma_f32_16x16x32_bf16 v[40:43], v[176:179], v[16:19], v[40:43]\n\t"
    "v_cvt_pk_bf16_f32 v12, v28, v29\n\t"
    "v_cvt_pk_bf16_f32 v13, v30, v31\n\t"
    "v_mfma_f32_16x16x32_bf16 v[40:43], v[180:183], v[48:51], v[40:43]\n\t"
    "s_waitcnt lgkmcnt(0)\n\t"
    "s_barrier\n\t"
    "v_mfma_f32_16x16x32_bf16 v[40:43], v[184:187], v[56:59], v[40:43]\n\t"
    "v_mfma_f32_16x16x32_bf16 v[68:71], v[160:163], v[56:59], v[68:71]\n\t"
    "v_max3_f32 v20, v64, v65, v66\n\t"
    "v_mfma_f32_16x16x32_bf16 v[68:71], v[156:159], v[48:51], v[68:71]\n\t"
    "v_mfma_f32_16x16x32_bf16 v[68:71], v[152:155], v[16:19], v[68:71]\n\t"
    "v_mfma_f32_16x16x32_bf16 v[72:75], v[128:131], v[16:19], v[72:75]\n\t"
    "v_max3_f32 v21, v67, v40, v41\n\t"
    "v_max3_f32 v20, v20, v42, v43\n\t"
    "v_mfma_f32_16x16x32_bf16 v[72:75], v[132:135], v[48:51], v[72:75]\n\t"
    "v_mfma_f32_16x16x32_bf16 v[72:75], v[136:139], v[56:59], v[72:75]\n\t"
    "v_mfma_f32_16x16x32_bf16 v[16:19], a[44:47], v[176:179], 0\n\t"
    "v_max3_f32 v21, v21, v68, v69\n\t"
    "v_max3_f32 v20, v20, v70, v71\n\t"
    "v_mfma_f32_16x16x32_bf16 v[16:19], a[36:39], v[180:183], v[16:19]\n\t"
    "buffer_load_dwordx4 v7, s[52:55], s61 offen offset:1024 nt lds\n\t"
    "v_mfma_f32_16x16x32_bf16 v[16:19], a[28:31], v[184:187], v[16:19]\n\t"
    "v_mfma_f32_16x16x32_bf16 v[16:19], a[20:23], v[188:191], v[16:19]\n\t"
    "v_max3_f32 v21, v21, v72, v73\n\t"
    "v_max3_f32 v20, v20, v74, v75\n\t"
    "v_mfma_f32_16x16x32_bf16 v[16:19], a[12:15], v[192:195], v[16:19]\n\t"
    "v_max_f32_e32 v20, v20, v21\n\t"
    "v_mov_b32_e32 v21, v20\n\t"
    "v_mfma_f32_16x16x32_bf16 v[16:19], a[4:7], v[196:199], v[16:19]\n\t"
    "v_mfma_f32_16x16x32_bf16 v[24:27], a[48:51], v[196:199], 0\n\t"
    "v_permlane32_swap_b32 v20, v21\n\t"
    "v_max_f32_e32 v20, v20, v21\n\t"
    "v_mfma_f32_16x16x32_bf16 v[24:27], a[56:59], v[192:195], v[24:27]\n\t"
    "v_mov_b32_e32 v21, v20\n\t"
    "v_mfma_f32_16x16x32_bf16 v[24:27], a[64:67], v[188:191], v[24:27]\n\t"
    "v_mfma_f32_16x16x32_bf16 v[24:27], a[72:75], v[184:187], v[24:27]\n\t"
    "v_cvt_pk_bf16_f32 v14, v16, v17\n\t"
    "v_cvt_pk_bf16_f32 v15, v18, v19\n\t"
    "v_mfma_f32_16x16x32_bf16 v[24:27], a[80:83], v[180:183], v[24:27]\n\t"
    "v_permlane16_swap_b32 v20, v21\n\t"
    "v_max_f32_e32 v20, v20, v21\n\t"
    "v_mfma_f32_16x16x32_bf16 v[24:27], a[88:91], v[176:179], v[24:27]\n\t"
    "v_sub_f32_e32 v64, v64, v20\n\t"
    "v_sub_f32_e32 v65, v65, v20\n\t"
    "v_mfma_f32_16x16x32_bf16 v[16:19], a[92:95], v[176:179], 0\n\t"
    "v_exp_f32_e32 v64, v64\n\t"
    "v_mfma_f32_16x16x32_bf16 v[16:19], a[84:87], v[180:183], v[16:19]\n\t"
    "v_exp_f32_e32 v65, v65\n\t"
    "v_mfma_f32_16x16x32_bf16 v[16:19], a[76:79], v[184:187], v[16:19]\n\t"
    "v_add_f32_e32 v22, v64, v65\n\t"
    "v_sub_f32_e32 v66, v66, v20\n\t"
    "v_mfma_f32_16x16x32_bf16 v[16:19], a[68:71], v[188:191], v[16:19]\n\t"
    "v_cvt_pk_bf16_f32 v28, v24, v25\n\t"
    "v_cvt_pk_bf16_f32 v29, v26, v27\n\t"
    "v_mfma_f32_16x16x32_bf16 v[16:19], a[60:63], v[192:195], v[16:19]\n\t"
    "v_exp_f32_e32 v66, v66\n\t"
    "v_mfma_f32_16x16x32_bf16 v[16:19], a[52:55], v[196:199], v[16:19]\n\t"
    "buffer_load_dwordx4 v7, s[52:55], s61 offen offset:2048 nt lds\n\t"
    "v_sub_f32_e32 v67, v67, v20\n\t"
    "v_mfma_f32_16x16x32_bf16 v[24:27], a[96:99], v[196:199], 0\n\t"
    "s_add_u32 s61, s61, 196608\n\t"
    "v_sub_f32_e32 v40, v40, v20\n\t"
    "v_mfma_f32_16x16x32_bf16 v[24:27], a[104:107], v[192:195], v[24:27]\n\t"
    "v_exp_f32_e32 v67, v67\n\t"
    "v_mfma_f32_16x16x32_bf16 v[24:27], a[112:115], v[188:191], v[24:27]\n\t"
    "v_add_f32_e32 v23, v66, v67\n\t"
    "v_sub_f32_e32 v41, v41, v20\n\t"
    "v_mfma_f32_16x16x32_bf16 v[24:27], a[120:123], v[184:187], v[24:27]\n\t"
    "v_cvt_pk_bf16_f32 v30, v16, v17\n\t"
    "v_cvt_pk_bf16_f32 v31, v18, v19\n\t"
    "v_mfma_f32_16x16x32_bf16 v[24:27], a[128:131], v[180:183], v[24:27]\n\t"
    "v_exp_f32_e32 v40, v40\n\t"
    "v_mfma_f32_16x16x32_bf16 v[24:27], a[136:139], v[176:179], v[24:27]\n\t"
    "v_add_f32_e32 v22, v22, v40\n\t"
    "v_sub_f32_e32 v42, v42, v20\n\t"
    "v_mfma_f32_16x16x32_bf16 v[16:19], a[140:143], v[176:179], 0\n\t"
    "v_exp_f32_e32 v41, v41\n\t"
    "v_mfma_f32_16x16x32_bf16 v[16:19], a[132:135], v[180:183], v[16:19]\n\t"
    "v_add_f32_e32 v22, v22, v41\n\t"
    "v_sub_f32_e32 v43, v43, v20\n\t"
    "v_mfma_f32_16x16x32_bf16 v[16:19], a[124:127], v[184:187], v[16:19]\n\t"
    "v_exp_f32_e32 v42, v42\n\t"
    "v_mfma_f32_16x16x32_bf16 v[16:19], a[116:119], v[188:191], v[16:19]\n\t"
    "v_cvt_pk_bf16_f32 v32, v24, v25\n\t"
    "v_cvt_pk_bf16_f32 v33, v26, v27\n\t"
    "v_mfma_f32_16x16x32_bf16 v[16:19], a[108:111], v[192:195], v[16:19]\n\t"
    "v_add_f32_e32 v23, v23, v42\n\t"
    "v_sub_f32_e32 v68, v68, v20\n\t"
    "v_mfma_f32_16x16x32_bf16 v[16:19], a[100:103], v[196:199], v[16:19]\n\t"
    "v_exp_f32_e32 v43, v43\n\t"
    "v_mfma_f32_16x16x32_bf16 v[24:27], a[144:147], v[196:199], 0\n\t"
    "v_add_f32_e32 v23, v23, v43\n\t"
    "v_sub_f32_e32 v69, v69, v20\n\t"
    "v_mfma_f32_16x16x32_bf16 v[24:27], a[152:155], v[192:195], v[24:27]\n\t"
    "v_exp_f32_e32 v68, v68\n\t"
    "v_mfma_f32_16x16x32_bf16 v[24:27], a[160:163], v[188:191], v[24:27]\n\t"
    "s_add_u32 m0, s63, 15360\n\t"
    "v_add_f32_e32 v22, v22, v68\n\t"
    "v_mfma_f32_16x16x32_bf16 v[24:27], a[168:171], v[184:187], v[24:27]\n\t"
    "buffer_load_dwordx4 v7, s[52:55], s61 offen offset:0 nt lds\n\t"
    "v_cvt_pk_bf16_f32 v34, v16, v17\n\t"
    "v_mfma_f32_16x16x32_bf16 v[24:27], a[176:179], v[180:183], v[24:27]\n\t"
    "v_cvt_pk_bf16_f32 v35, v18, v19\n\t"
    "v_sub_f32_e32 v70, v70, v20\n\t"
    "v_mfma_f32_16x16x32_bf16 v[24:27], a[184:187], v[176:179], v[24:27]\n\t"
    "v_exp_f32_e32 v69, v69\n\t"
    "v_mfma_f32_16x16x32_bf16 v[36:39], v[220:223], v[12:15], 0\n\t"
    "v_add_f32_e32 v22, v22, v69\n\t"
    "v_sub_f32_e32 v71, v71, v20\n\t"
    "v_mfma_f32_16x16x32_bf16 v[36:39], v[216:219], v[28:31], v[36:39]\n\t"
    "v_exp_f32_e32 v70, v70\n\t"
    "v_mfma_f32_16x16x32_bf16 v[36:39], v[212:215], v[32:35], v[36:39]\n\t"
    "v_add_f32_e32 v23, v23, v70\n\t"
    "v_sub_f32_e32 v72, v72, v20\n\t"
    "v_mfma_f32_16x16x32_bf16 v[16:19], v[188:191], v[32:35], 0\n\t"
    "v_exp_f32_e32 v71, v71\n\t"
    "v_mfma_f32_16x16x32_bf16 v[16:19], v[192:195], v[28:31], v[16:19]\n\t"
    "v_add_f32_e32 v23, v23, v71\n\t"
    "v_sub_f32_e32 v73, v73, v20\n\t"
    "v_mfma_f32_16x16x32_bf16 v[16:19], v[196:199], v[12:15], v[16:19]\n\t"
    "v_exp_f32_e32 v72, v72\n\t"
    "v_mfma_f32_16x16x32_bf16 v[44:47], v[172:175], v[12:15], 0\n\t"
    "v_add_f32_e32 v22, v22, v72\n\t"
    "v_sub_f32_e32 v74, v74, v20\n\t"
    "v_mfma_f32_16x16x32_bf16 v[44:47], v[168:171], v[28:31], v[44:47]\n\t"
    "v_exp_f32_e32 v73, v73\n\t"
    "v_mfma_f32_16x16x32_bf16 v[44:47], v[164:167], v[32:35], v[44:47]\n\t"
    "v_add_f32_e32 v22, v22, v73\n\t"
    "v_sub_f32_e32 v75, v75, v20\n\t"
    "v_mfma_f32_16x16x32_bf16 v[48:51], v[140:143], v[32:35], 0\n\t"
    "v_exp_f32_e32 v74, v74\n\t"
    "v_mfma_f32_16x16x32_bf16 v[48:51], v[144:147], v[28:31], v[48:51]\n\t"
    "v_add_f32_e32 v23, v23, v74\n\t"
    "v_cvt_pk_bf16_f32 v52, v24, v25\n\t"
    "v_mfma_f32_16x16x32_bf16 v[48:51], v[148:151], v[12:15], v[48:51]\n\t"
    "v_exp_f32_e32 v75, v75\n\t"
    "v_mfma_f32_16x16x32_bf16 v[32:35], a[188:191], v[176:179], 0\n\t"
    "v_add_f32_e32 v23, v23, v75\n\t"
    "v_add_f32_e32 v22, v22, v23\n\t"
    "v_mfma_f32_16x16x32_bf16 v[32:35], a[180:183], v[180:183], v[32:35]\n\t"
    "buffer_load_dwordx4 v7, s[52:55], s61 offen offset:1024 nt lds\n\t"
    "v_mov_b32_e32 v23, v22\n\t"
    "v_mfma_f32_16x16x32_bf16 v[32:35], a[172:175], v[184:187], v[32:35]\n\t"
    "v_cvt_pk_bf16_f32 v53, v26, v27\n\t"
    "v_mfma_f32_16x16x32_bf16 v[32:35], a[164:167], v[188:191], v[32:35]\n\t"
    "v_permlane32_swap_b32 v22, v23\n\t"
    "v_add_f32_e32 v22, v22, v23\n\t"
    "v_mfma_f32_16x16x32_bf16 v[32:35], a[156:159], v[192:195], v[32:35]\n\t"
    "v_mov_b32_e32 v23, v22\n\t"
    "v_mfma_f32_16x16x32_bf16 v[32:35], a[148:151], v[196:199], v[32:35]\n\t"
    "v_mfma_f32_16x16x32_bf16 v[12:15], a[192:195], v[196:199], 0\n\t"
    "v_permlane16_swap_b32 v22, v23\n\t"
    "v_add_f32_e32 v22, v22, v23\n\t"
    "v_mfma_f32_16x16x32_bf16 v[12:15], a[200:203], v[192:195], v[12:15]\n\t"
    "v_rcp_f32_e32 v22, v22\n\t"
    "v_mfma_f32_16x16x32_bf16 v[12:15], a[208:211], v[188:191], v[12:15]\n\t"
    "v_mul_f32_e32 v64, v64, v22\n\t"
    "v_mul_f32_e32 v65, v65, v22\n\t"
    "v_mfma_f32_16x16x32_bf16 v[12:15], a[216:219], v[184:187], v[12:15]\n\t"
    "v_cvt_pk_bf16_f32 v54, v32, v33\n\t"
    "v_cvt_pk_bf16_f32 v55, v34, v35\n\t"
    "v_mfma_f32_16x16x32_bf16 v[12:15], a[224:227], v[180:183], v[12:15]\n\t"
    "v_cvt_pk_bf16_f32 v24, v64, v65\n\t"
    "v_mul_f32_e32 v66, v66, v22\n\t"
    "v_mfma_f32_16x16x32_bf16 v[12:15], a[232:235], v[176:179], v[12:15]\n\t"
    "v_mul_f32_e32 v67, v67, v22\n\t"
    "v_cvt_pk_bf16_f32 v25, v66, v67\n\t"
    "v_mfma_f32_16x16x32_bf16 v[28:31], a[236:239], v[176:179], 0\n\t"
    "v_mul_f32_e32 v40, v40, v22\n\t"
    "v_mul_f32_e32 v41, v41, v22\n\t"
    "v_mfma_f32_16x16x32_bf16 v[28:31], a[228:231], v[180:183], v[28:31]\n\t"
    "v_cvt_pk_bf16_f32 v26, v40, v41\n\t"
    "v_mul_f32_e32 v42, v42, v22\n\t"
    "v_mfma_f32_16x16x32_bf16 v[28:31], a[220:223], v[184:187], v[28:31]\n\t"
    "v_mul_f32_e32 v43, v43, v22\n\t"
    "v_cvt_pk_bf16_f32 v27, v42, v43\n\t"
    "v_mfma_f32_16x16x32_bf16 v[28:31], a[212:215], v[188:191], v[28:31]\n\t"
    "v_cvt_pk_bf16_f32 v32, v12, v13\n\t"
    "v_cvt_pk_bf16_f32 v33, v14, v15\n\t"
    "v_mfma_f32_16x16x32_bf16 v[28:31], a[204:207], v[192:195], v[28:31]\n\t"
    "v_mul_f32_e32 v68, v68, v22\n\t"
    "v_mul_f32_e32 v69, v69, v22\n\t"
    "v_mfma_f32_16x16x32_bf16 v[28:31], a[196:199], v[196:199], v[28:31]\n\t"
    "buffer_load_dwordx4 v7, s[52:55], s61 offen offset:2048 nt lds\n\t"
    "v_cvt_pk_bf16_f32 v40, v68, v69\n\t"
    "v_mfma_f32_16x16x32_bf16 v[12:15], a[240:243], v[196:199], 0\n\t"
    "s_add_u32 s61, s61, 196608\n\t"
    "v_mul_f32_e32 v70, v70, v22\n\t"
    "v_mfma_f32_16x16x32_bf16 v[12:15], a[248:251], v[192:195], v[12:15]\n\t"
    "v_mul_f32_e32 v71, v71, v22\n\t"
    "v_cvt_pk_bf16_f32 v41, v70, v71\n\t"
    "v_mfma_f32_16x16x32_bf16 v[12:15], v[224:227], v[188:191], v[12:15]\n\t"
    "v_mul_f32_e32 v72, v72, v22\n\t"
    "v_mul_f32_e32 v73, v73, v22\n\t"
    "v_mfma_f32_16x16x32_bf16 v[12:15], v[232:235], v[184:187], v[12:15]\n\t"
    "v_cvt_pk_bf16_f32 v34, v28, v29\n\t"
    "v_cvt_pk_bf16_f32 v35, v30, v31\n\t"
    "v_mfma_f32_16x16x32_bf16 v[12:15], v[240:243], v[180:183], v[12:15]\n\t"
    "v_cvt_pk_bf16_f32 v42, v72, v73\n\t"
    "v_mul_f32_e32 v74, v74, v22\n\t"
    "v_mfma_f32_16x16x32_bf16 v[12:15], v[248:251], v[176:179], v[12:15]\n\t"
    "v_mul_f32_e32 v75, v75, v22\n\t"
    "v_cvt_pk_bf16_f32 v43, v74, v75\n\t"
    "v_mfma_f32_16x16x32_bf16 v[28:31], v[252:255], v[176:179], 0\n\t"
    "v_mfma_f32_16x16x32_bf16 v[28:31], v[244:247], v[180:183], v[28:31]\n\t"
    "v_mfma_f32_16x16x32_bf16 v[28:31], v[236:239], v[184:187], v[28:31]\n\t"
    "v_mfma_f32_16x16x32_bf16 v[28:31], v[228:231], v[188:191], v[28:31]\n\t"
    "v_cvt_pk_bf16_f32 v20, v12, v13\n\t"
    "v_cvt_pk_bf16_f32 v21, v14, v15\n\t"
    "v_mfma_f32_16x16x32_bf16 v[28:31], a[252:255], v[192:195], v[28:31]\n\t"
    "v_mfma_f32_16x16x32_bf16 v[28:31], a[244:247], v[196:199], v[28:31]\n\t"
    "v_mfma_f32_16x16x32_bf16 v[12:15], a[0:3], v[172:175], 0\n\t"
    "ds_write_b128 v6, v[24:27] offset:0\n\t"
    "v_mfma_f32_16x16x32_bf16 v[12:15], a[8:11], v[168:171], v[12:15]\n\t"
    "v_mfma_f32_16x16x32_bf16 v[12:15], a[16:19], v[164:167], v[12:15]\n\t"
    "s_add_u32 m0, s63, 18432\n\t"
    "v_mfma_f32_16x16x32_bf16 v[12:15], a[24:27], v[160:163], v[12:15]\n\t"
    "buffer_load_dwordx4 v7, s[52:55], s61 offen offset:0 nt lds\n\t"
    "v_cvt_pk_bf16_f32 v22, v28, v29\n\t"
    "v_mfma_f32_16x16x32_bf16 v[12:15], a[32:35], v[156:159], v[12:15]\n\t"
    "ds_write_b128 v6, v[40:43] offset:1024\n\t"
    "v_mfma_f32_16x16x32_bf16 v[12:15], a[40:43], v[152:155], v[12:15]\n\t"
    "v_cvt_pk_bf16_f32 v23, v30, v31\n\t"
    "v_mfma_f32_16x16x32_bf16 v[36:39], v[208:211], v[52:55], v[36:39]\n\t"
    "v_mfma_f32_16x16x32_bf16 v[36:39], v[204:207], v[32:35], v[36:39]\n\t"
    "v_mfma_f32_16x16x32_bf16 v[36:39], v[200:203], v[20:23], v[36:39]\n\t"
    "s_add_i32 s64, s62, 24576\n\t"
    "v_mfma_f32_16x16x32_bf16 v[16:19], v[176:179], v[20:23], v[16:19]\n\t"
    "s_cmp_eq_u32 s64, %[rend]\n\t"
    "s_cselect_b32 s64, %[rbeg], s64\n\t"
    "v_mfma_f32_16x16x32_bf16 v[16:19], v[180:183], v[32:35], v[16:19]\n\t"
    "s_sub_i32 s65, s63, s64\n\t"
    "v_cvt_pk_bf16_f32 v24, v12, v13\n\t"
    "v_mfma_f32_16x16x32_bf16 v[16:19], v[184:187], v[52:55], v[16:19]\n\t"
    "v_add_u32_e32 v0, s65, v0\n\t"
    "v_add_u32_e32 v1, s65, v1\n\t"
    "v_mfma_f32_16x16x32_bf16 v[44:47], v[160:163], v[52:55], v[44:47]\n\t"
    "v_add_u32_e32 v2, s65, v2\n\t"
    "v_add_u32_e32 v3, s65, v3\n\t"
    "v_mfma_f32_16x16x32_bf16 v[44:47], v[156:159], v[32:35], v[44:47]\n\t"
    "v_add_u32_e32 v4, s65, v4\n\t"
    "v_add_u32_e32 v5, s65, v5\n\t"
    "v_mfma_f32_16x16x32_bf16 v[44:47], v[152:155], v[20:23], v[44:47]\n\t"
    "v_max3_f32 v28, v36, v37, v38\n\t"
    "v_cvt_pk_bf16_f32 v25, v14, v15\n\t"
    "v_mfma_f32_16x16x32_bf16 v[48:51], v[128:131], v[20:23], v[48:51]\n\t"
    "v_max3_f32 v29, v39, v16, v17\n\t"
    "v_max3_f32 v28, v28, v18, v19\n\t"
    "v_mfma_f32_16x16x32_bf16 v[48:51], v[132:135], v[32:35], v[48:51]\n\t"
    "v_mfma_f32_16x16x32_bf16 v[48:51], v[136:139], v[52:55], v[48:51]\n\t"
    "v_mfma_f32_16x16x32_bf16 v[12:15], a[44:47], v[152:155], 0\n\t"
    "v_max3_f32 v29, v29, v44, v45\n\t"
    "v_max3_f32 v28, v28, v46, v47\n\t"
    "v_mfma_f32_16x16x32_bf16 v[12:15], a[36:39], v[156:159], v[12:15]\n\t"
    "buffer_load_dwordx4 v7, s[52:55], s61 offen offset:1024 nt lds\n\t"
    "v_mfma_f32_16x16x32_bf16 v[12:15], a[28:31], v[160:163], v[12:15]\n\t"
    "v_mfma_f32_16x16x32_bf16 v[12:15], a[20:23], v[164:167], v[12:15]\n\t"
    "v_max3_f32 v29, v29, v48, v49\n\t"
    "v_max3_f32 v28, v28, v50, v51\n\t"
    "v_mfma_f32_16x16x32_bf16 v[12:15], a[12:15], v[168:171], v[12:15]\n\t"
    "v_max_f32_e32 v28, v28, v29\n\t"
    "v_mov_b32_e32 v29, v28\n\t"
    "v_mfma_f32_16x16x32_bf16 v[12:15], a[4:7], v[172:175], v[12:15]\n\t"
    "v_mfma_f32_16x16x32_bf16 v[20:23], a[48:51], v[172:175], 0\n\t"
    "v_permlane32_swap_b32 v28, v29\n\t"
    "v_max_f32_e32 v28, v28, v29\n\t"
    "v_mfma_f32_16x16x32_bf16 v[20:23], a[56:59], v[168:171], v[20:23]\n\t"
    "v_mov_b32_e32 v29, v28\n\t"
    "v_mfma_f32_16x16x32_bf16 v[20:23], a[64:67], v[164:167], v[20:23]\n\t"
    "v_mfma_f32_16x16x32_bf16 v[20:23], a[72:75], v[160:163], v[20:23]\n\t"
    "v_cvt_pk_bf16_f32 v26, v12, v13\n\t"
    "v_cvt_pk_bf16_f32 v27, v14, v15\n\t"
    "v_mfma_f32_16x16x32_bf16 v[20:23], a[80:83], v[156:159], v[20:23]\n\t"
    "v_permlane16_swap_b32 v28, v29\n\t"
    "v_max_f32_e32 v28, v28, v29\n\t"
    "v_mfma_f32_16x16x32_bf16 v[20:23], a[88:91], v[152:155], v[20:23]\n\t"
    "v_sub_f32_e32 v36, v36, v28\n\t"
    "v_sub_f32_e32 v37, v37, v28\n\t"
    "v_mfma_f32_16x16x32_bf16 v[12:15], a[92:95], v[152:155], 0\n\t"
    "v_exp_f32_e32 v36, v36\n\t"
    "v_mfma_f32_16x16x32_bf16 v[12:15], a[84:87], v[156:159], v[12:15]\n\t"
    "v_exp_f32_e32 v37, v37\n\t"
    "v_mfma_f32_16x16x32_bf16 v[12:15], a[76:79], v[160:163], v[12:15]\n\t"
    "v_add_f32_e32 v30, v36, v37\n\t"
    "v_sub_f32_e32 v38, v38, v28\n\t"
    "v_mfma_f32_16x16x32_bf16 v[12:15], a[68:71], v[164:167], v[12:15]\n\t"
    "v_cvt_pk_bf16_f32 v32, v20, v21\n\t"
    "v_cvt_pk_bf16_f32 v33, v22, v23\n\t"
    "v_mfma_f32_16x16x32_bf16 v[12:15], a[60:63], v[168:171], v[12:15]\n\t"
    "v_exp_f32_e32 v38, v38\n\t"
    "v_mfma_f32_16x16x32_bf16 v[12:15], a[52:55], v[172:175], v[12:15]\n\t"
    "buffer_load_dwordx4 v7, s[52:55], s61 offen offset:2048 nt lds\n\t"
    "v_sub_f32_e32 v39, v39, v28\n\t"
    "v_mfma_f32_16x16x32_bf16 v[20:23], a[96:99], v[172:175], 0\n\t"
    "s_add_u32 s61, s61, 196608\n\t"
    "v_sub_f32_e32 v16, v16, v28\n\t"
    "v_mfma_f32_16x16x32_bf16 v[20:23], a[104:107], v[168:171], v[20:23]\n\t"
    "v_exp_f32_e32 v39, v39\n\t"
    "v_mfma_f32_16x16x32_bf16 v[20:23], a[112:115], v[164:167], v[20:23]\n\t"
    "v_add_f32_e32 v31, v38, v39\n\t"
    "v_sub_f32_e32 v17, v17, v28\n\t"
    "v_mfma_f32_16x16x32_bf16 v[20:23], a[120:123], v[160:163], v[20:23]\n\t"
    "v_cvt_pk_bf16_f32 v34, v12, v13\n\t"
    "v_cvt_pk_bf16_f32 v35, v14, v15\n\t"
    "v_mfma_f32_16x16x32_bf16 v[20:23], a[128:131], v[156:159], v[20:23]\n\t"
    "v_exp_f32_e32 v16, v16\n\t"
    "v_mfma_f32_16x16x32_bf16 v[20:23], a[136:139], v[152:155], v[20:23]\n\t"
    "v_add_f32_e32 v30, v30, v16\n\t"
    "v_sub_f32_e32 v18, v18, v28\n\t"
    "v_mfma_f32_16x16x32_bf16 v[12:15], a[140:143], v[152:155], 0\n\t"
    "v_exp_f32_e32 v17, v17\n\t"
    "v_mfma_f32_16x16x32_bf16 v[12:15], a[132:135], v[156:159], v[12:15]\n\t"
    "v_add_f32_e32 v30, v30, v17\n\t"
    "v_sub_f32_e32 v19, v19, v28\n\t"
    "v_mfma_f32_16x16x32_bf16 v[12:15], a[124:127], v[160:163], v[12:15]\n\t"
    "v_exp_f32_e32 v18, v18\n\t"
    "v_mfma_f32_16x16x32_bf16 v[12:15], a[116:119], v[164:167], v[12:15]\n\t"
    "v_cvt_pk_bf16_f32 v40, v20, v21\n\t"
    "v_cvt_pk_bf16_f32 v41, v22, v23\n\t"
    "v_mfma_f32_16x16x32_bf16 v[12:15], a[108:111], v[168:171], v[12:15]\n\t"
    "v_add_f32_e32 v31, v31, v18\n\t"
    "v_sub_f32_e32 v44, v44, v28\n\t"
    "v_mfma_f32_16x16x32_bf16 v[12:15], a[100:103], v[172:175], v[12:15]\n\t"
    "v_exp_f32_e32 v19, v19\n\t"
    "v_mfma_f32_16x16x32_bf16 v[20:23], a[144:147], v[172:175], 0\n\t"
    "v_add_f32_e32 v31, v31, v19\n\t"
    "v_sub_f32_e32 v45, v45, v28\n\t"
    "v_mfma_f32_16x16x32_bf16 v[20:23], a[152:155], v[168:171], v[20:23]\n\t"
    "v_exp_f32_e32 v44, v44\n\t"
    "v_mfma_f32_16x16x32_bf16 v[20:23], a[160:163], v[164:167], v[20:23]\n\t"
    "s_add_u32 m0, s63, 21504\n\t"
    "v_add_f32_e32 v30, v30, v44\n\t"
    "v_mfma_f32_16x16x32_bf16 v[20:23], a[168:171], v[160:163], v[20:23]\n\t"
    "buffer_load_dwordx4 v7, s[52:55], s61 offen offset:0 nt lds\n\t"
    "v_cvt_pk_bf16_f32 v42, v12, v13\n\t"
    "v_mfma_f32_16x16x32_bf16 v[20:23], a[176:179], v[156:159], v[20:23]\n\t"
    "v_cvt_pk_bf16_f32 v43, v14, v15\n\t"
    "v_sub_f32_e32 v46, v46, v28\n\t"
    "v_mfma_f32_16x16x32_bf16 v[20:23], a[184:187], v[152:155], v[20:23]\n\t"
    "v_exp_f32_e32 v45, v45\n\t"
    "v_mfma_f32_16x16x32_bf16 v[52:55], v[220:223], v[24:27], 0\n\t"
    "v_add_f32_e32 v30, v30, v45\n\t"
    "v_sub_f32_e32 v47, v47, v28\n\t"
    "v_mfma_f32_16x16x32_bf16 v[52:55], v[216:219], v[32:35], v[52:55]\n\t"
    "v_exp_f32_e32 v46, v46\n\t"
    "v_mfma_f32_16x16x32_bf16 v[52:55], v[212:215], v[40:43], v[52:55]\n\t"
    "v_add_f32_e32 v31, v31, v46\n\t"
    "v_sub_f32_e32 v48, v48, v28\n\t"
    "v_mfma_f32_16x16x32_bf16 v[56:59], v[188:191], v[40:43], 0\n\t"
    "v_exp_f32_e32 v47, v47\n\t"
    "v_mfma_f32_16x16x32_bf16 v[56:59], v[192:195], v[32:35], v[56:59]\n\t"
    "v_add_f32_e32 v31, v31, v47\n\t"
    "v_sub_f32_e32 v49, v49, v28\n\t"
    "v_mfma_f32_16x16x32_bf16 v[56:59], v[196:199], v[24:27], v[56:59]\n\t"
    "v_exp_f32_e32 v48, v48\n\t"
    "v_mfma_f32_16x16x32_bf16 v[60:63], v[172:175], v[24:27], 0\n\t"
    "v_add_f32_e32 v30, v30, v48\n\t"
    "v_sub_f32_e32 v50, v50, v28\n\t"
    "v_mfma_f32_16x16x32_bf16 v[60:63], v[168:171], v[32:35], v[60:63]\n\t"
    "v_exp_f32_e32 v49, v49\n\t"
    "v_mfma_f32_16x16x32_bf16 v[60:63], v[164:167], v[40:43], v[60:63]\n\t"
    "v_add_f32_e32 v30, v30, v49\n\t"
    "v_sub_f32_e32 v51, v51, v28\n\t"
    "v_mfma_f32_16x16x32_bf16 v[64:67], v[140:143], v[40:43], 0\n\t"
    "v_exp_f32_e32 v50, v50\n\t"
    "v_mfma_f32_16x16x32_bf16 v[64:67], v[144:147], v[32:35], v[64:67]\n\t"
    "v_add_f32_e32 v31, v31, v50\n\t"
    "v_cvt_pk_bf16_f32 v12, v20, v21\n\t"
    "v_mfma_f32_16x16x32_bf16 v[64:67], v[148:151], v[24:27], v[64:67]\n\t"
    "v_exp_f32_e32 v51, v51\n\t"
    "v_mfma_f32_16x16x32_bf16 v[40:43], a[188:191], v[152:155], 0\n\t"
    "v_add_f32_e32 v31, v31, v51\n\t"
    "v_add_f32_e32 v30, v30, v31\n\t"
    "v_mfma_f32_16x16x32_bf16 v[40:43], a[180:183], v[156:159], v[40:43]\n\t"
    "buffer_load_dwordx4 v7, s[52:55], s61 offen offset:1024 nt lds\n\t"
    "v_mov_b32_e32 v31, v30\n\t"
    "v_mfma_f32_16x16x32_bf16 v[40:43], a[172:175], v[160:163], v[40:43]\n\t"
    "v_cvt_pk_bf16_f32 v13, v22, v23\n\t"
    "v_mfma_f32_16x16x32_bf16 v[40:43], a[164:167], v[164:167], v[40:43]\n\t"
    "v_permlane32_swap_b32 v30, v31\n\t"
    "v_add_f32_e32 v30, v30, v31\n\t"
    "v_mfma_f32_16x16x32_bf16 v[40:43], a[156:159], v[168:171], v[40:43]\n\t"
    "v_mov_b32_e32 v31, v30\n\t"
    "v_mfma_f32_16x16x32_bf16 v[40:43], a[148:151], v[172:175], v[40:43]\n\t"
    "v_mfma_f32_16x16x32_bf16 v[20:23], a[192:195], v[172:175], 0\n\t"
    "v_permlane16_swap_b32 v30, v31\n\t"
    "v_add_f32_e32 v30, v30, v31\n\t"
    "v_mfma_f32_16x16x32_bf16 v[20:23], a[200:203], v[168:171], v[20:23]\n\t"
    "v_rcp_f32_e32 v30, v30\n\t"
    "v_mfma_f32_16x16x32_bf16 v[20:23], a[208:211], v[164:167], v[20:23]\n\t"
    "v_mul_f32_e32 v36, v36, v30\n\t"
    "v_mul_f32_e32 v37, v37, v30\n\t"
    "v_mfma_f32_16x16x32_bf16 v[20:23], a[216:219], v[160:163], v[20:23]\n\t"
    "v_cvt_pk_bf16_f32 v14, v40, v41\n\t"
    "v_cvt_pk_bf16_f32 v15, v42, v43\n\t"
    "v_mfma_f32_16x16x32_bf16 v[20:23], a[224:227], v[156:159], v[20:23]\n\t"
    "v_cvt_pk_bf16_f32 v24, v36, v37\n\t"
    "v_mul_f32_e32 v38, v38, v30\n\t"
    "v_mfma_f32_16x16x32_bf16 v[20:23], a[232:235], v[152:155], v[20:23]\n\t"
    "v_mul_f32_e32 v39, v39, v30\n\t"
    "v_cvt_pk_bf16_f32 v25, v38, v39\n\t"
    "v_mfma_f32_16x16x32_bf16 v[32:35], a[236:239], v[152:155], 0\n\t"
    "v_mul_f32_e32 v16, v16, v30\n\t"
    "v_mul_f32_e32 v17, v17, v30\n\t"
    "v_mfma_f32_16x16x32_bf16 v[32:35], a[228:231], v[156:159], v[32:35]\n\t"
    "v_cvt_pk_bf16_f32 v26, v16, v17\n\t"
    "v_mul_f32_e32 v18, v18, v30\n\t"
    "v_mfma_f32_16x16x32_bf16 v[32:35], a[220:223], v[160:163], v[32:35]\n\t"
    "v_mul_f32_e32 v19, v19, v30\n\t"
    "v_cvt_pk_bf16_f32 v27, v18, v19\n\t"
    "v_mfma_f32_16x16x32_bf16 v[32:35], a[212:215], v[164:167], v[32:35]\n\t"
    "v_cvt_pk_bf16_f32 v36, v20, v21\n\t"
    "v_cvt_pk_bf16_f32 v37, v22, v23\n\t"
    "v_mfma_f32_16x16x32_bf16 v[32:35], a[204:207], v[168:171], v[32:35]\n\t"
    "v_mul_f32_e32 v44, v44, v30\n\t"
    "v_mul_f32_e32 v45, v45, v30\n\t"
    "v_mfma_f32_16x16x32_bf16 v[32:35], a[196:199], v[172:175], v[32:35]\n\t"
    "buffer_load_dwordx4 v7, s[52:55], s61 offen offset:2048 nt lds\n\t"
    "v_cvt_pk_bf16_f32 v16, v44, v45\n\t"
    "v_mfma_f32_16x16x32_bf16 v[20:23], a[240:243], v[172:175], 0\n\t"
    "s_add_u32 s61, s61, 11206656\n\t"
    "v_mul_f32_e32 v46, v46, v30\n\t"
    "v_mfma_f32_16x16x32_bf16 v[20:23], a[248:251], v[168:171], v[20:23]\n\t"
    "v_mul_f32_e32 v47, v47, v30\n\t"
    "v_cvt_pk_bf16_f32 v17, v46, v47\n\t"
    "v_mfma_f32_16x16x32_bf16 v[20:23], v[224:227], v[164:167], v[20:23]\n\t"
    "v_mul_f32_e32 v48, v48, v30\n\t"
    "v_mul_f32_e32 v49, v49, v30\n\t"
    "v_mfma_f32_16x16x32_bf16 v[20:23], v[232:235], v[160:163], v[20:23]\n\t"
    "v_cvt_pk_bf16_f32 v38, v32, v33\n\t"
    "v_cvt_pk_bf16_f32 v39, v34, v35\n\t"
    "v_mfma_f32_16x16x32_bf16 v[20:23], v[240:243], v[156:159], v[20:23]\n\t"
    "v_cvt_pk_bf16_f32 v18, v48, v49\n\t"
    "v_mul_f32_e32 v50, v50, v30\n\t"
    "v_mfma_f32_16x16x32_bf16 v[20:23], v[248:251], v[152:155], v[20:23]\n\t"
    "v_mul_f32_e32 v51, v51, v30\n\t"
    "v_cvt_pk_bf16_f32 v19, v50, v51\n\t"
    "v_mfma_f32_16x16x32_bf16 v[32:35], v[252:255], v[152:155], 0\n\t"
    "v_mfma_f32_16x16x32_bf16 v[32:35], v[244:247], v[156:159], v[32:35]\n\t"
    "v_mfma_f32_16x16x32_bf16 v[32:35], v[236:239], v[160:163], v[32:35]\n\t"
    "v_mfma_f32_16x16x32_bf16 v[32:35], v[228:231], v[164:167], v[32:35]\n\t"
    "v_cvt_pk_bf16_f32 v28, v20, v21\n\t"
    "v_cvt_pk_bf16_f32 v29, v22, v23\n\t"
    "v_mfma_f32_16x16x32_bf16 v[32:35], a[252:255], v[168:171], v[32:35]\n\t"
    "v_mfma_f32_16x16x32_bf16 v[32:35], a[244:247], v[172:175], v[32:35]\n\t"
    "v_mfma_f32_16x16x32_bf16 v[20:23], a[0:3], v[148:151], 0\n\t"
    "ds_write_b128 v6, v[24:27] offset:2048\n\t"
    "v_mfma_f32_16x16x32_bf16 v[20:23], a[8:11], v[144:147], v[20:23]\n\t"
    "v_mfma_f32_16x16x32_bf16 v[20:23], a[16:19], v[140:143], v[20:23]\n\t"
    "s_add_u32 m0, s62, 0\n\t"
    "v_mfma_f32_16x16x32_bf16 v[20:23], a[24:27], v[136:139], v[20:23]\n\t"
    "buffer_load_dwordx4 v7, s[52:55], s61 offen offset:0 nt lds\n\t"
    "v_cvt_pk_bf16_f32 v30, v32, v33\n\t"
    "v_mfma_f32_16x16x32_bf16 v[20:23], a[32:35], v[132:135], v[20:23]\n\t"
    "ds_write_b128 v6, v[16:19] offset:3072\n\t"
    "v_mfma_f32_16x16x32_bf16 v[20:23], a[40:43], v[128:131], v[20:23]\n\t"
    "v_cvt_pk_bf16_f32 v31, v34, v35\n\t"
    "v_mfma_f32_16x16x32_bf16 v[52:55], v[208:211], v[12:15], v[52:55]\n\t"
    "v_mfma_f32_16x16x32_bf16 v[52:55], v[204:207], v[36:39], v[52:55]\n\t"
    "v_mfma_f32_16x16x32_bf16 v[52:55], v[200:203], v[28:31], v[52:55]\n\t"
    "v_mfma_f32_16x16x32_bf16 v[56:59], v[176:179], v[28:31], v[56:59]\n\t"
    "v_cvt_pk_bf16_f32 v40, v20, v21\n\t"
    "v_cvt_pk_bf16_f32 v41, v22, v23\n\t"
    "v_mfma_f32_16x16x32_bf16 v[56:59], v[180:183], v[36:39], v[56:59]\n\t"
    "v_mfma_f32_16x16x32_bf16 v[56:59], v[184:187], v[12:15], v[56:59]\n\t"
    "v_mfma_f32_16x16x32_bf16 v[60:63], v[160:163], v[12:15], v[60:63]\n\t"
    "v_max3_f32 v68, v52, v53, v54\n\t"
    "v_mfma_f32_16x16x32_bf16 v[60:63], v[156:159], v[36:39], v[60:63]\n\t"
    "v_mfma_f32_16x16x32_bf16 v[60:63], v[152:155], v[28:31], v[60:63]\n\t"
    "v_mfma_f32_16x16x32_bf16 v[64:67], v[128:131], v[28:31], v[64:67]\n\t"
    "v_max3_f32 v69, v55, v56, v57\n\t"
    "v_max3_f32 v68, v68, v58, v59\n\t"
    "v_mfma_f32_16x16x32_bf16 v[64:67], v[132:135], v[36:39], v[64:67]\n\t"
    "v_mfma_f32_16x16x32_bf16 v[64:67], v[136:139], v[12:15], v[64:67]\n\t"
    "v_mfma_f32_16x16x32_bf16 v[16:19], a[44:47], v[128:131], 0\n\t"
    "v_max3_f32 v69, v69, v60, v61\n\t"
    "v_max3_f32 v68, v68, v62, v63\n\t"
    "v_mfma_f32_16x16x32_bf16 v[16:19], a[36:39], v[132:135], v[16:19]\n\t"
    "buffer_load_dwordx4 v7, s[52:55], s61 offen offset:1024 nt lds\n\t"
    "v_mfma_f32_16x16x32_bf16 v[16:19], a[28:31], v[136:139], v[16:19]\n\t"
    "v_mfma_f32_16x16x32_bf16 v[16:19], a[20:23], v[140:143], v[16:19]\n\t"
    "v_max3_f32 v69, v69, v64, v65\n\t"
    "v_max3_f32 v68, v68, v66, v67\n\t"
    "v_mfma_f32_16x16x32_bf16 v[16:19], a[12:15], v[144:147], v[16:19]\n\t"
    "v_max_f32_e32 v68, v68, v69\n\t"
    "v_mov_b32_e32 v69, v68\n\t"
    "v_mfma_f32_16x16x32_bf16 v[16:19], a[4:7], v[148:151], v[16:19]\n\t"
    "v_mfma_f32_16x16x32_bf16 v[12:15], a[48:51], v[148:151], 0\n\t"
    "v_permlane32_swap_b32 v68, v69\n\t"
    "v_max_f32_e32 v68, v68, v69\n\t"
    "v_mfma_f32_16x16x32_bf16 v[12:15], a[56:59], v[144:147], v[12:15]\n\t"
    "v_mov_b32_e32 v69, v68\n\t"
    "v_mfma_f32_16x16x32_bf16 v[12:15], a[64:67], v[140:143], v[12:15]\n\t"
    "v_mfma_f32_16x16x32_bf16 v[12:15], a[72:75], v[136:139], v[12:15]\n\t"
    "v_cvt_pk_bf16_f32 v42, v16, v17\n\t"
    "v_cvt_pk_bf16_f32 v43, v18, v19\n\t"
    "v_mfma_f32_16x16x32_bf16 v[12:15], a[80:83], v[132:135], v[12:15]\n\t"
    "v_permlane16_swap_b32 v68, v69\n\t"
    "v_max_f32_e32 v68, v68, v69\n\t"
    "v_mfma_f32_16x16x32_bf16 v[12:15], a[88:91], v[128:131], v[12:15]\n\t"
    "v_sub_f32_e32 v52, v52, v68\n\t"
    "v_sub_f32_e32 v53, v53, v68\n\t"
    "v_mfma_f32_16x16x32_bf16 v[16:19], a[92:95], v[128:131], 0\n\t"
    "v_exp_f32_e32 v52, v52\n\t"
    "v_mfma_f32_16x16x32_bf16 v[16:19], a[84:87], v[132:135], v[16:19]\n\t"
    "v_exp_f32_e32 v53, v53\n\t"
    "v_mfma_f32_16x16x32_bf16 v[16:19], a[76:79], v[136:139], v[16:19]\n\t"
    "v_add_f32_e32 v70, v52, v53\n\t"
    "v_sub_f32_e32 v54, v54, v68\n\t"
    "v_mfma_f32_16x16x32_bf16 v[16:19], a[68:71], v[140:143], v[16:19]\n\t"
    "v_cvt_pk_bf16_f32 v36, v12, v13\n\t"
    "v_cvt_pk_bf16_f32 v37, v14, v15\n\t"
    "v_mfma_f32_16x16x32_bf16 v[16:19], a[60:63], v[144:147], v[16:19]\n\t"
    "v_exp_f32_e32 v54, v54\n\t"
    "v_mfma_f32_16x16x32_bf16 v[16:19], a[52:55], v[148:151], v[16:19]\n\t"
    "buffer_load_dwordx4 v7, s[52:55], s61 offen offset:2048 nt lds\n\t"
    "v_sub_f32_e32 v55, v55, v68\n\t"
    "v_mfma_f32_16x16x32_bf16 v[12:15], a[96:99], v[148:151], 0\n\t"
    ".Lh6_%=:\n\t"
    "s_cbranch_execz .LrM_%=\n\t"
    "s_add_u32 s61, s61, 196608\n\t"
    "v_sub_f32_e32 v56, v56, v68\n\t"
    "v_mfma_f32_16x16x32_bf16 v[12:15], a[104:107], v[144:147], v[12:15]\n\t"
    "v_exp_f32_e32 v55, v55\n\t"
    "v_mfma_f32_16x16x32_bf16 v[12:15], a[112:115], v[140:143], v[12:15]\n\t"
    "v_add_f32_e32 v71, v54, v55\n\t"
    "v_sub_f32_e32 v57, v57, v68\n\t"
    "v_mfma_f32_16x16x32_bf16 v[12:15], a[120:123], v[136:139], v[12:15]\n\t"
    "v_cvt_pk_bf16_f32 v38, v16, v17\n\t"
    "v_cvt_pk_bf16_f32 v39, v18, v19\n\t"
    "v_mfma_f32_16x16x32_bf16 v[12:15], a[128:131], v[132:135], v[12:15]\n\t"
    "v_exp_f32_e32 v56, v56\n\t"
    "v_mfma_f32_16x16x32_bf16 v[12:15], a[136:139], v[128:131], v[12:15]\n\t"
    "v_add_f32_e32 v70, v70, v56\n\t"
    "v_sub_f32_e32 v58, v58, v68\n\t"
    "v_mfma_f32_16x16x32_bf16 v[16:19], a[140:143], v[128:131], 0\n\t"
    "v_exp_f32_e32 v57, v57\n\t"
    "v_mfma_f32_16x16x32_bf16 v[16:19], a[132:135], v[132:135], v[16:19]\n\t"
    "v_add_f32_e32 v70, v70, v57\n\t"
    "v_sub_f32_e32 v59, v59, v68\n\t"
    "v_mfma_f32_16x16x32_bf16 v[16:19], a[124:127], v[136:139], v[16:19]\n\t"
    "v_exp_f32_e32 v58, v58\n\t"
    "v_mfma_f32_16x16x32_bf16 v[16:19], a[116:119], v[140:143], v[16:19]\n\t"
    "v_cvt_pk_bf16_f32 v32, v12, v13\n\t"
    "v_cvt_pk_bf16_f32 v33, v14, v15\n\t"
    "v_mfma_f32_16x16x32_bf16 v[16:19], a[108:111], v[144:147], v[16:19]\n\t"
    "v_add_f32_e32 v71, v71, v58\n\t"
    "v_sub_f32_e32 v60, v60, v68\n\t"
    "v_mfma_f32_16x16x32_bf16 v[16:19], a[100:103], v[148:151], v[16:19]\n\t"
    "v_exp_f32_e32 v59, v59\n\t"
    "v_mfma_f32_16x16x32_bf16 v[44:47], a[144:147], v[148:151], 0\n\t"
    "v_add_f32_e32 v71, v71, v59\n\t"
    "v_sub_f32_e32 v61, v61, v68\n\t"
    "v_mfma_f32_16x16x32_bf16 v[44:47], a[152:155], v[144:147], v[44:47]\n\t"
    "v_exp_f32_e32 v60, v60\n\t"
    "s_branch .Lmid_%=\n\t"
    ".Ltail_%=:\n\t"
    "v_mbcnt_lo_u32_b32 v8, -1, 0\n\t"
    "v_mbcnt_hi_u32_b32 v8, -1, v8\n\t"
    "v_lshlrev_b32_e32 v8, 5, v8\n\t"
    "s_nop 7\n\t"
    "s_nop 1\n\t"
    "ds_write_b128 v6, v[44:47] offset:4096\n\t"
    "v_cvt_pk_bf16_f32 v62, v56, v57\n\t"
    "ds_write_b128 v6, v[52:55] offset:5120\n\t"
    "v_cvt_pk_bf16_f32 v63, v58, v59\n\t"
    "s_nop 1\n\t"
    "v_mfma_f32_16x16x32_bf16 v[12:15], v[208:211], v[40:43], v[12:15]\n\t"
    "s_nop 1\n\t"
    "v_mfma_f32_16x16x32_bf16 v[12:15], v[204:207], v[48:51], v[12:15]\n\t"
    "s_nop 1\n\t"
    "v_mfma_f32_16x16x32_bf16 v[12:15], v[200:203], v[60:63], v[12:15]\n\t"
    "s_nop 1\n\t"
    "v_mfma_f32_16x16x32_bf16 v[16:19], v[176:179], v[60:63], v[16:19]\n\t"
    "s_nop 1\n\t"
    "v_mfma_f32_16x16x32_bf16 v[16:19], v[180:183], v[48:51], v[16:19]\n\t"
    "s_waitcnt lgkmcnt(0)\n\t"
    "s_barrier\n\t"
    "s_nop 1\n\t"
    "v_mfma_f32_16x16x32_bf16 v[16:19], v[184:187], v[40:43], v[16:19]\n\t"
    "s_nop 1\n\t"
    "v_mfma_f32_16x16x32_bf16 v[20:23], v[160:163], v[40:43], v[20:23]\n\t"
    "v_max3_f32 v52, v12, v13, v14\n\t"
    "s_nop 1\n\t"
    "v_mfma_f32_16x16x32_bf16 v[20:23], v[156:159], v[48:51], v[20:23]\n\t"
    "s_nop 1\n\t"
    "v_mfma_f32_16x16x32_bf16 v[20:23], v[152:155], v[60:63], v[20:23]\n\t"
    "s_nop 1\n\t"
    "v_mfma_f32_16x16x32_bf16 v[24:27], v[128:131], v[60:63], v[24:27]\n\t"
    "v_max3_f32 v53, v15, v16, v17\n\t"
    "v_max3_f32 v52, v52, v18, v19\n\t"
    "s_nop 1\n\t"
    "v_mfma_f32_16x16x32_bf16 v[24:27], v[132:135], v[48:51], v[24:27]\n\t"
    "s_nop 1\n\t"
    "v_mfma_f32_16x16x32_bf16 v[24:27], v[136:139], v[40:43], v[24:27]\n\t"
    "s_nop 7\n\t"
    "s_nop 1\n\t"
    "v_max3_f32 v53, v53, v20, v21\n\t"
    "v_max3_f32 v52, v52, v22, v23\n\t"
    "v_max3_f32 v53, v53, v24, v25\n\t"
    "v_max3_f32 v52, v52, v26, v27\n\t"
    "v_max_f32_e32 v52, v52, v53\n\t"
    "v_mov_b32_e32 v53, v52\n\t"
    "s_nop 1\n\t"
    "v_permlane32_swap_b32 v52, v53\n\t"
    "v_max_f32_e32 v52, v52, v53\n\t"
    "v_mov_b32_e32 v53, v52\n\t"
    "s_nop 1\n\t"
    "v_permlane16_swap_b32 v52, v53\n\t"
    "v_max_f32_e32 v52, v52, v53\n\t"
    "v_sub_f32_e32 v12, v12, v52\n\t"
    "v_sub_f32_e32 v13, v13, v52\n\t"
    "v_sub_f32_e32 v14, v14, v52\n\t"
    "v_exp_f32_e32 v12, v12\n\t"
    "s_nop 0\n\t"
    "v_sub_f32_e32 v15, v15, v52\n\t"
    "v_sub_f32_e32 v16, v16, v52\n\t"
    "v_sub_f32_e32 v17, v17, v52\n\t"
    "v_exp_f32_e32 v13, v13\n\t"
    "s_nop 0\n\t"
    "v_add_f32_e32 v54, v12, v13\n\t"
    "v_sub_f32_e32 v18, v18, v52\n\t"
    "v_exp_f32_e32 v14, v14\n\t"
    "s_nop 0\n\t"
    "v_exp_f32_e32 v15, v15\n\t"
    "s_nop 0\n\t"
    "v_add_f32_e32 v55, v14, v15\n\t"
    "v_sub_f32_e32 v19, v19, v52\n\t"
    "v_exp_f32_e32 v16, v16\n\t"
    "s_nop 0\n\t"
    "v_add_f32_e32 v54, v54, v16\n\t"
    "v_sub_f32_e32 v20, v20, v52\n\t"
    "v_exp_f32_e32 v17, v17\n\t"
    "s_nop 0\n\t"
    "v_add_f32_e32 v54, v54, v17\n\t"
    "v_sub_f32_e32 v21, v21, v52\n\t"
    "v_exp_f32_e32 v18, v18\n\t"
    "s_nop 0\n\t"
    "v_add_f32_e32 v55, v55, v18\n\t"
    "v_exp_f32_e32 v19, v19\n\t"
    "s_nop 0\n\t"
    "v_add_f32_e32 v55, v55, v19\n\t"
    "v_sub_f32_e32 v22, v22, v52\n\t"
    "v_exp_f32_e32 v20, v20\n\t"
    "s_nop 0\n\t"
    "v_add_f32_e32 v54, v54, v20\n\t"
    "v_sub_f32_e32 v23, v23, v52\n\t"
    "v_exp_f32_e32 v21, v21\n\t"
    "s_nop 0\n\t"
    "v_add_f32_e32 v54, v54, v21\n\t"
    "v_exp_f32_e32 v22, v22\n\t"
    "s_nop 0\n\t"
    "v_add_f32_e32 v55, v55, v22\n\t"
    "v_sub_f32_e32 v24, v24, v52\n\t"
    "v_sub_f32_e32 v25, v25, v52\n\t"
    "v_exp_f32_e32 v23, v23\n\t"
    "s_nop 0\n\t"
    "v_add_f32_e32 v55, v55, v23\n\t"
    "v_exp_f32_e32 v24, v24\n\t"
    "s_nop 0\n\t"
    "v_add_f32_e32 v54, v54, v24\n\t"
    "v_exp_f32_e32 v25, v25\n\t"
    "s_nop 0\n\t"
    "v_add_f32_e32 v54, v54, v25\n\t"
    "v_sub_f32_e32 v26, v26, v52\n\t"
    "v_sub_f32_e32 v27, v27, v52\n\t"
    "v_exp_f32_e32 v26, v26\n\t"
    "s_nop 0\n\t"
    "v_add_f32_e32 v55, v55, v26\n\t"
    "v_exp_f32_e32 v27, v27\n\t"
    "s_nop 0\n\t"
    "v_add_f32_e32 v55, v55, v27\n\t"
    "v_add_f32_e32 v54, v54, v55\n\t"
    "v_mov_b32_e32 v55, v54\n\t"
    "s_nop 1\n\t"
    "v_permlane32_swap_b32 v54, v55\n\t"
    "v_add_f32_e32 v54, v54, v55\n\t"
    "v_mov_b32_e32 v55, v54\n\t"
    "s_nop 1\n\t"
    "v_permlane16_swap_b32 v54, v55\n\t"
    "v_add_f32_e32 v54, v54, v55\n\t"
    "v_rcp_f32_e32 v54, v54\n\t"
    "s_nop 0\n\t"
    "v_mul_f32_e32 v12, v12, v54\n\t"
    "v_mul_f32_e32 v13, v13, v54\n\t"
    "v_cvt_pk_bf16_f32 v60, v12, v13\n\t"
    "v_mul_f32_e32 v14, v14, v54\n\t"
    "v_mul_f32_e32 v15, v15, v54\n\t"
    "v_cvt_pk_bf16_f32 v61, v14, v15\n\t"
    "v_mul_f32_e32 v16, v16, v54\n\t"
    "v_mul_f32_e32 v17, v17, v54\n\t"
    "v_cvt_pk_bf16_f32 v62, v16, v17\n\t"
    "v_mul_f32_e32 v18, v18, v54\n\t"
    "v_mul_f32_e32 v19, v19, v54\n\t"
    "v_cvt_pk_bf16_f32 v63, v18, v19\n\t"
    "v_mul_f32_e32 v20, v20, v54\n\t"
    "v_mul_f32_e32 v21, v21, v54\n\t"
    "v_cvt_pk_bf16_f32 v44, v20, v21\n\t"
    "v_mul_f32_e32 v22, v22, v54\n\t"
    "v_mul_f32_e32 v23, v23, v54\n\t"
    "v_cvt_pk_bf16_f32 v45, v22, v23\n\t"
    "v_mul_f32_e32 v24, v24, v54\n\t"
    "v_mul_f32_e32 v25, v25, v54\n\t"
    "v_cvt_pk_bf16_f32 v46, v24, v25\n\t"
    "ds_write_b128 v6, v[60:63] offset:6144\n\t"
    "v_mul_f32_e32 v26, v26, v54\n\t"
    "v_mul_f32_e32 v27, v27, v54\n\t"
    "v_cvt_pk_bf16_f32 v47, v26, v27\n\t"
    "ds_write_b128 v6, v[44:47] offset:7168\n\t"
    "s_waitcnt lgkmcnt(0)\n\t"
    "s_barrier\n\t"
    "s_cmp_lg_u32 %[closer], 0\n\t"
    "s_cbranch_scc1 .Lx2_%=\n\t"
    "s_mov_b32 s60, 0x10000\n\t"
    "buffer_load_dwordx2 v[10:11], off, s[56:59], %[eoff] sc1\n\t"
    ".Lp3_%=:\n\t"
    "buffer_load_dwordx4 v[12:15], v8, s[56:59], %[aoff] offen sc1\n\t"
    "buffer_load_dwordx4 v[16:19], v8, s[56:59], %[aoff] offen offset:16 sc1\n\t"
    "s_waitcnt vmcnt(0)\n\t"
    "v_add_co_u32_e32 v20, vcc, 0x7f4a7c15, v10\n\t"
    "s_nop 1\n\t"
    "v_mov_b32_e32 v9, 0x9e3779b9\n\t"
    "v_addc_co_u32_e32 v21, vcc, v9, v11, vcc\n\t"
    "v_cmp_eq_u64_e32 vcc, v[12:13], v[20:21]\n\t"
    "v_cmp_eq_u64_e64 s[66:67], v[14:15], v[20:21]\n\t"
    "s_and_b64 vcc, vcc, s[66:67]\n\t"
    "v_cmp_eq_u64_e64 s[66:67], v[16:17], v[20:21]\n\t"
    "s_and_b64 vcc, vcc, s[66:67]\n\t"
    "v_cmp_eq_u64_e64 s[66:67], v[18:19], v[20:21]\n\t"
    "s_and_b64 vcc, vcc, s[66:67]\n\t"
    "s_cmp_eq_u64 vcc, exec\n\t"
    "s_cbranch_scc1 .Lk4_%=\n\t"
    "s_sleep 8\n\t"
    "s_sub_u32 s60, s60, 1\n\t"
    "s_cmp_lg_u32 s60, 0\n\t"
    "s_cbranch_scc1 .Lp3_%=\n\t"
    ".Lk4_%=:\n\t"
    "s_mov_b64 exec, 1\n\t"
    "buffer_store_dwordx2 v[20:21], off, s[56:59], %[eoff] sc1\n\t"
    "s_mov_b64 exec, -1\n\t"
    ".Lx2_%=:\n\t"
    "s_endpgm\n\t"
    ".Lend_%=:\n\t"
    :: "{v31}"(lane), "{s[36:43]}"(k8), "{s[44:47]}"(k4),
       [role] "s"(role), [pair] "s"(pair), [prod] "s"((pair * 256 + bid - 144) >> 31), [closer] "s"(bid | wave), [bid] "s"(bid), [wb] "s"(wb),
       [rbeg] "s"(rbeg), [rend] "s"(rbeg + 3 * SLOT), [pl] "s"(ldsb + PB + pair * 8192), [amask] "s"((unsigned long long)pair),
       [tch] "s"(((bid >> 3) * 2 + pair) * 1152), [foff] "s"(F_OFF + 64 * bid), [aoff] "s"(A_OFF), [aown] "s"(A_OFF + 8 * bid), [eoff] "s"(E_OFF)
    : "memory", "scc", ACLOB, VCLOB, SCLOB);
}

extern "C" void kernel_launch(void* const* d_in, const int* in_sizes, int n_in,
                              void* d_out, int out_size, void* d_ws, size_t ws_size,
                              hipStream_t stream) {
  attn_kernel<<<256, 256, 0, stream>>>((const unsigned char*)d_in[0], (const unsigned char*)d_in[1], (const unsigned char*)d_in[2],
                                       (unsigned char*)d_ws, (unsigned char*)d_out
      );
}
```

```cpp
#include <hip/hip_runtime.h>
#include <hip/hip_bf16.h>
#include <stdint.h>

using u32x4 = __attribute__((ext_vector_type(4))) unsigned;
using u32x8 = __attribute__((ext_vector_type(8))) unsigned;

static constexpr int ROWB = 512 * 384;
static constexpr int SLOT = 24576;
static constexpr int PB = 6 * SLOT;
static constexpr int F_OFF = 160 * 1024, A_OFF = 148 * 1024, E_OFF = 152 * 1024;

#define R10(p, n) p #n "0", p #n "1", p #n "2", p #n "3", p #n "4", p #n "5", p #n "6", p #n "7", p #n "8", p #n "9"
#define R240(p) R10(p, 4), R10(p, 5), R10(p, 6), R10(p, 7), R10(p, 8), R10(p, 9), R10(p, 10), R10(p, 11), R10(p, 12), R10(p, 13), \
  R10(p, 14), R10(p, 15), R10(p, 16), R10(p, 17), R10(p, 18), R10(p, 19), R10(p, 20), R10(p, 21), R10(p, 22), R10(p, 23), R10(p, 24), \
  p "250", p "251", p "252", p "253", p "254", p "255"
#define ACLOB R10("a", ), R10("a", 1), R10("a", 2), R10("a", 3), R240("a")
#define VCLOB R10("v", ), R10("v", 1), R10("v", 2), "v30", "v32", "v33", "v34", "v35", "v36", "v37", "v38", "v39", R240("v")
#define SCLOB "s48", "s49", R10("s", 5), R10("s", 6), "s70", "s71", "vcc"

__global__ __launch_bounds__(256, 1) void attn_kernel(const unsigned char* __restrict__ x,
                                                      const unsigned char* __restrict__ wqkv,
                                                      const unsigned char* __restrict__ wo,
                                                      unsigned char* ws,
                                                      unsigned char* __restrict__ out
                                                      ) {
  __shared__ u32x4 lds[10240];
  u32x8 k8; u32x4 k4;
  asm volatile(
    "s_load_dwordx8 s[36:43], %2, 0\n\t"
    "s_load_dwordx2 s[44:45], %2, 32\n\t"
    : "={s[36:43]}"(k8), "={s[44:47]}"(k4) : "s"(__builtin_amdgcn_kernarg_segment_ptr()) : "memory", ACLOB);
  __builtin_amdgcn_sched_barrier(0);
  const unsigned lane = threadIdx.x & 63, wave = __builtin_amdgcn_readfirstlane(threadIdx.x >> 6);
  const unsigned role = wave >> 1, pair = wave & 1;
  const unsigned ldsb = (unsigned)(size_t)lds;
  const unsigned bid = blockIdx.x;
  const unsigned wb = (bid & 7) * 8 * ROWB + ((bid >> 3) * 2 + pair) * 8 * 384;
  const unsigned rbeg = ldsb + pair * 3 * SLOT;

  asm volatile(
    ".Lbeg_%=:\n\t"
    "s_cmp_lg_u32 %[role], 1\n\t"
    "s_cbranch_scc1 .Lroln_%=\n\t"
    "s_cmp_eq_u32 %[prod], 0\n\t"
    "s_cbranch_scc1 .Lnp_%=\n\t"
    "s_cmp_lt_u32 %[bid], 72\n\t"
    "s_cselect_b32 s71, 0, 72\n\t"
    "s_cselect_b32 s63, 0, 768\n\t"
    "s_sub_u32 s64, %[bid], s71\n\t"
    "s_mul_i32 s65, s64, 0x1556\n\t"
    "s_lshr_b32 s65, s65, 16\n\t"
    "s_mul_i32 s69, s65, 12\n\t"
    "s_sub_u32 s69, s64, s69\n\t"
    "s_lshr_b32 s69, s69, 1\n\t"
    "s_and_b32 s70, s64, 1\n\t"
    "s_mul_i32 s60, s69, 36864\n\t"
    "s_add_u32 s60, s60, s63\n\t"
    "s_add_u32 s61, s60, 4608\n\t"
    "v_lshrrev_b32_e32 v20, 2, v31\n\t"
    "v_and_b32_e32 v21, 8, v20\n\t"
    "v_bfe_u32 v24, v31, 3, 2\n\t"
    "v_or_b32_e32 v21, v21, v24\n\t"
    "v_mul_u32_u24_e32 v21, 0x480, v21\n\t"
    "v_lshrrev_b32_e32 v24, 4, v31\n\t"
    "v_xor_b32_e32 v24, v24, v31\n\t"
    "v_and_b32_e32 v24, 7, v24\n\t"
    "v_lshl_or_b32 v24, v24, 4, v21\n\t"
    "v_xor_b32_e32 v25, 64, v24\n\t"
    "v_add_u32_e32 v25, 0x4800, v25\n\t"
    "v_mul_u32_u24_e32 v27, 0x180, v20\n\t"
    "v_and_b32_e32 v28, 3, v31\n\t"
    "v_lshl_or_b32 v27, v28, 4, v27\n\t"
    "s_lshl_b32 s62, s65, 3\n\t"
    "s_add_u32 s62, s62, s70\n\t"
    "s_mul_i32 s62, s62, 4608\n\t"
    "s_add_u32 s62, s62, 384\n\t"
    "s_lshl_b32 s63, s65, 6\n\t"
    "s_mov_b32 s50, 0x36000\n\t"
    "s_mov_b32 s51, 0x20000\n\t"
    "s_mov_b32 s54, 0x12000\n\t"
    "s_mov_b32 s55, 0x20000\n\t"
    "s_waitcnt lgkmcnt(0)\n\t"
    "s_mov_b32 s48, s38\n\t"
    "s_and_b32 s49, s39, 0xffff\n\t"
    "s_cmp_lg_u32 s71, 0\n\t"
    "s_cbranch_scc1 .Lpo_%=\n\t"
    "s_mov_b32 m0, 61440\n\t"
    "s_nop 0\n\t"
    "buffer_load_dwordx4 v24, s[48:51], s62 offen offset:0 lds\n\t"
    "s_mov_b32 m0, 62464\n\t"
    "s_nop 0\n\t"
    "buffer_load_dwordx4 v25, s[48:51], s62 offen offset:0 lds\n\t"
    "s_mov_b32 m0, 63360\n\t"
    "s_nop 0\n\t"
    "buffer_load_dwordx4 v24, s[48:51], s62 offen offset:128 lds\n\t"
    "s_mov_b32 m0, 64384\n\t"
    "s_nop 0\n\t"
    "buffer_load_dwordx4 v25, s[48:51], s62 offen offset:128 lds\n\t"
    "s_mov_b32 m0, 65280\n\t"
    "s_nop 0\n\t"
    "buffer_load_dwordx4 v24, s[48:51], s62 offen offset:256 lds\n\t"
    "s_mov_b32 m0, 66304\n\t"
    "s_nop 0\n\t"
    "buffer_load_dwordx4 v25, s[48:51], s62 offen offset:256 lds\n\t"
    "s_branch .Lpa_%=\n\t"
    ".Lpo_%=:\n\t"
    "s_mov_b32 s52, s40\n\t"
    "s_mov_b32 m0, 61440\n\t"
    "s_and_b32 s53, s41, 0xffff\n\t"
    "buffer_load_dwordx4 v27, s[52:55], s63 offen lds\n\t"
    "s_mov_b32 m0, 62464\n\t"
    "s_add_u32 s63, s63, 6144\n\t"
    "buffer_load_dwordx4 v27, s[52:55], s63 offen lds\n\t"
    "s_mov_b32 m0, 63488\n\t"
    "s_add_u32 s63, s63, 6144\n\t"
    "buffer_load_dwordx4 v27, s[52:55], s63 offen lds\n\t"
    "s_mov_b32 m0, 64512\n\t"
    "s_add_u32 s63, s63, 6144\n\t"
    "buffer_load_dwordx4 v27, s[52:55], s63 offen lds\n\t"
    "s_mov_b32 m0, 65536\n\t"
    "s_add_u32 s63, s63, 6144\n\t"
    "buffer_load_dwordx4 v27, s[52:55], s63 offen lds\n\t"
    "s_mov_b32 m0, 66560\n\t"
    "s_add_u32 s63, s63, 6144\n\t"
    "buffer_load_dwordx4 v27, s[52:55], s63 offen lds\n\t"
    "s_mov_b32 m0, 67584\n\t"
    "s_add_u32 s63, s63, 6144\n\t"
    "buffer_load_dwordx4 v27, s[52:55], s63 offen lds\n\t"
    "s_mov_b32 m0, 68608\n\t"
    "s_add_u32 s63, s63, 6144\n\t"
    "buffer_load_dwordx4 v27, s[52:55], s63 offen lds\n\t"
    "s_mov_b32 m0, 69632\n\t"
    "s_add_u32 s63, s63, 6144\n\t"
    "buffer_load_dwordx4 v27, s[52:55], s63 offen lds\n\t"
    "s_mov_b32 m0, 70656\n\t"
    "s_add_u32 s63, s63, 6144\n\t"
    "buffer_load_dwordx4 v27, s[52:55], s63 offen lds\n\t"
    "s_mov_b32 m0, 71680\n\t"
    "s_add_u32 s63, s63, 6144\n\t"
    "buffer_load_dwordx4 v27, s[52:55], s63 offen lds\n\t"
    "s_mov_b32 m0, 72704\n\t"
    "s_add_u32 s63, s63, 6144\n\t"
    "buffer_load_dwordx4 v27, s[52:55], s63 offen lds\n\t"
    ".Lpa_%=:\n\t"
    "s_mov_b32 m0, 49152\n\t"
    "s_nop 0\n\t"
    "buffer_load_dwordx4 v24, s[48:51], s60 offen offset:0 lds\n\t"
    "s_mov_b32 m0, 53120\n\t"
    "s_nop 0\n\t"
    "buffer_load_dwordx4 v24, s[48:51], s60 offen offset:128 lds\n\t"
    "s_mov_b32 m0, 57088\n\t"
    "s_nop 0\n\t"
    "buffer_load_dwordx4 v24, s[48:51], s60 offen offset:256 lds\n\t"
    "s_mov_b32 m0, 51200\n\t"
    "s_nop 0\n\t"
    "buffer_load_dwordx4 v25, s[48:51], s60 offen offset:0 lds\n\t"
    "s_mov_b32 m0, 55168\n\t"
    "s_nop 0\n\t"
    "buffer_load_dwordx4 v25, s[48:51], s60 offen offset:128 lds\n\t"
    "s_mov_b32 m0, 59136\n\t"
    "s_nop 0\n\t"
    "buffer_load_dwordx4 v25, s[48:51], s60 offen offset:256 lds\n\t"
    "s_mov_b32 m0, 50176\n\t"
    "s_nop 0\n\t"
    "buffer_load_dwordx4 v24, s[48:51], s61 offen offset:0 lds\n\t"
    "s_mov_b32 m0, 54144\n\t"
    "s_nop 0\n\t"
    "buffer_load_dwordx4 v24, s[48:51], s61 offen offset:128 lds\n\t"
    "s_mov_b32 m0, 58112\n\t"
    "s_nop 0\n\t"
    "buffer_load_dwordx4 v24, s[48:51], s61 offen offset:256 lds\n\t"
    "s_mov_b32 m0, 52224\n\t"
    "s_nop 0\n\t"
    "buffer_load_dwordx4 v25, s[48:51], s61 offen offset:0 lds\n\t"
    "s_mov_b32 m0, 56192\n\t"
    "s_nop 0\n\t"
    "buffer_load_dwordx4 v25, s[48:51], s61 offen offset:128 lds\n\t"
    "s_mov_b32 m0, 60160\n\t"
    "s_nop 0\n\t"
    "buffer_load_dwordx4 v25, s[48:51], s61 offen offset:256 lds\n\t"
    ".Lnp_%=:\n\t"
    "s_waitcnt lgkmcnt(0)\n\t"
    "s_mov_b32 s56, s42\n\t"
    "s_and_b32 s57, s43, 0xffff\n\t"
    "s_mov_b32 s58, 0x40000\n\t"
    "s_mov_b32 s59, 0x20000\n\t"
    "buffer_load_dwordx2 v[22:23], off, s[56:59], %[eoff] sc1\n\t"
    "s_barrier\n\t"
    "v_lshlrev_b32_e32 v30, 4, v31\n\t"
    "v_and_b32_e32 v28, 7, v31\n\t"
    "v_bfe_u32 v27, v31, 3, 1\n\t"
    "v_lshrrev_b32_e32 v29, 4, v31\n\t"
    "v_add_u32_e32 v6, %[pl], v30\n\t"
    "v_mul_u32_u24_e32 v27, 0xc00, v27\n\t"
    "v_add_u32_e32 v27, %[rbeg], v27\n\t"
    "v_lshlrev_b32_e32 v26, 7, v31\n\t"
    "v_mad_u32_u24 v20, v28, 3, 0\n\t"
    "v_and_b32_e32 v21, 6, v20\n\t"
    "v_xor_b32_e32 v21, v21, v29\n\t"
    "v_lshl_add_u32 v20, v20, 7, v27\n\t"
    "v_lshl_add_u32 v0, v21, 4, v20\n\t"
    "v_xor_b32_e32 v1, 64, v0\n\t"
    "v_mad_u32_u24 v20, v28, 3, 1\n\t"
    "v_and_b32_e32 v21, 6, v20\n\t"
    "v_xor_b32_e32 v21, v21, v29\n\t"
    "v_lshl_add_u32 v20, v20, 7, v27\n\t"
    "v_lshl_add_u32 v2, v21, 4, v20\n\t"
    "v_xor_b32_e32 v3, 64, v2\n\t"
    "v_mad_u32_u24 v20, v28, 3, 2\n\t"
    "v_and_b32_e32 v21, 6, v20\n\t"
    "v_xor_b32_e32 v21, v21, v29\n\t"
    "v_lshl_add_u32 v20, v20, 7, v27\n\t"
    "v_lshl_add_u32 v4, v21, 4, v20\n\t"
    "v_xor_b32_e32 v5, 64, v4\n\t"
    "s_cmp_eq_u32 %[prod], 0\n\t"
    "s_cbranch_scc1 .Lnq_%=\n\t"
    "v_bfe_u32 v20, v31, 1, 3\n\t"
    "v_xor_b32_e32 v20, v20, v29\n\t"
    "v_lshlrev_b32_e32 v20, 4, v20\n\t"
    "v_lshl_or_b32 v20, v28, 7, v20\n\t"
    "v_bfe_u32 v21, v31, 3, 1\n\t"
    "v_lshl_or_b32 v16, v21, 11, v20\n\t"
    "v_add_u32_e32 v16, 0xc000, v16\n\t"
    "v_xor_b32_e32 v17, 64, v16\n\t"
    "s_cmp_lg_u32 s71, 0\n\t"
    "s_cbranch_scc1 .Lqo_%=\n\t"
    "v_lshl_or_b32 v18, v21, 10, v20\n\t"
    "v_add_u32_e32 v18, 0xf000, v18\n\t"
    "v_xor_b32_e32 v19, 64, v18\n\t"
    "s_mul_i32 s60, s70, 6\n\t"
    "s_add_u32 s60, s60, s65\n\t"
    "s_mul_i32 s60, s60, 6\n\t"
    "s_add_u32 s60, s60, s69\n\t"
    "s_lshl_b32 s60, s60, 10\n\t"
    "v_add_u32_e32 v20, s60, v30\n\t"
    "v_mov_b32_e32 v24, v30\n\t"
    "s_waitcnt vmcnt(13)\n\t"
    "ds_read_b128 v[80:83], v18 offset:0\n\t"
    "ds_read_b128 v[84:87], v19 offset:0\n\t"
    "ds_read_b128 v[88:91], v18 offset:2048\n\t"
    "ds_read_b128 v[92:95], v19 offset:2048\n\t"
    "ds_read_b128 v[96:99], v18 offset:4096\n\t"
    "ds_read_b128 v[100:103], v19 offset:4096\n\t"
    "s_branch .Lqa_%=\n\t"
    ".Lqo_%=:\n\t"
    "s_and_b32 s63, s65, 1\n\t"
    "s_lshl_b32 s63, s63, 1\n\t"
    "s_add_u32 s63, s63, s70\n\t"
    "s_lshl_b32 s64, s70, 2\n\t"
    "s_lshl_b32 s62, s70, 4\n\t"
    "s_cmp_lt_u32 s65, 4\n\t"
    "s_cselect_b32 s60, s64, 8\n\t"
    "s_cselect_b32 s63, s65, s63\n\t"
    "s_cselect_b32 s62, s64, s62\n\t"
    "s_cselect_b32 s61, 4, 3\n\t"
    "s_lshl_b32 s62, s62, 1\n\t"
    "s_add_u32 s62, s62, 61440\n\t"
    "s_mul_i32 s60, s60, 6\n\t"
    "s_add_u32 s60, s60, s69\n\t"
    "s_lshl_b32 s60, s60, 2\n\t"
    "s_add_u32 s60, s60, s63\n\t"
    "s_lshl_b32 s60, s60, 8\n\t"
    "s_add_u32 s60, s60, 73728\n\t"
    "v_and_b32_e32 v25, 3, v31\n\t"
    "v_bfe_u32 v21, v31, 2, 2\n\t"
    "v_lshlrev_b32_e32 v18, s61, v25\n\t"
    "v_lshl_add_u32 v18, v21, 6, v18\n\t"
    "v_lshl_add_u32 v18, v29, 9, v18\n\t"
    "v_add_u32_e32 v18, s62, v18\n\t"
    "v_lshl_add_u32 v24, v25, 2, v21\n\t"
    "v_lshl_add_u32 v24, v29, 4, v24\n\t"
    "v_lshlrev_b32_e32 v24, 4, v24\n\t"
    "v_mul_u32_u24_e32 v21, 0x1800, v21\n\t"
    "v_lshl_add_u32 v20, v25, 4, v21\n\t"
    "v_lshl_add_u32 v20, v29, 6, v20\n\t"
    "v_add_u32_e32 v20, s60, v20\n\t"
    "s_waitcnt vmcnt(13)\n\t"
    "ds_read_b64_tr_b16 v[80:81], v18 offset:0\n\t"
    "ds_read_b64_tr_b16 v[82:83], v18 offset:256\n\t"
    "ds_read_b64_tr_b16 v[84:85], v18 offset:2048\n\t"
    "ds_read_b64_tr_b16 v[86:87], v18 offset:2304\n\t"
    "ds_read_b64_tr_b16 v[88:89], v18 offset:4096\n\t"
    "ds_read_b64_tr_b16 v[90:91], v18 offset:4352\n\t"
    "ds_read_b64_tr_b16 v[92:93], v18 offset:6144\n\t"
    "ds_read_b64_tr_b16 v[94:95], v18 offset:6400\n\t"
    "ds_read_b64_tr_b16 v[96:97], v18 offset:8192\n\t"
    "ds_read_b64_tr_b16 v[98:99], v18 offset:8448\n\t"
    "ds_read_b64_tr_b16 v[100:101], v18 offset:10240\n\t"
    "ds_read_b64_tr_b16 v[102:103], v18 offset:10496\n\t"
    ".Lqa_%=:\n\t"
    "s_waitcnt vmcnt(7)\n\t"
    "ds_read_b128 v[32:35], v16 offset:0\n\t"
    "ds_read_b128 v[36:39], v17 offset:0\n\t"
    "ds_read_b128 v[40:43], v16 offset:4096\n\t"
    "ds_read_b128 v[44:47], v17 offset:4096\n\t"
    "ds_read_b128 v[48:51], v16 offset:8192\n\t"
    "ds_read_b128 v[52:55], v17 offset:8192\n\t"
    "s_waitcnt vmcnt(1)\n\t"
    "ds_read_b128 v[56:59], v16 offset:1024\n\t"
    "ds_read_b128 v[60:63], v17 offset:1024\n\t"
    "ds_read_b128 v[64:67], v16 offset:5120\n\t"
    "ds_read_b128 v[68:71], v17 offset:5120\n\t"
    "ds_read_b128 v[72:75], v16 offset:9216\n\t"
    "ds_read_b128 v[76:79], v17 offset:9216\n\t"
    "s_waitcnt lgkmcnt(11)\n\t"
    "v_mfma_f32_16x16x32_bf16 v[108:111], v[32:35], v[80:83], 0\n\t"
    "s_waitcnt lgkmcnt(10)\n\t"
    "v_mfma_f32_16x16x32_bf16 v[108:111], v[36:39], v[84:87], v[108:111]\n\t"
    "s_waitcnt lgkmcnt(9)\n\t"
    "v_mfma_f32_16x16x32_bf16 v[108:111], v[40:43], v[88:91], v[108:111]\n\t"
    "s_waitcnt lgkmcnt(8)\n\t"
    "v_mfma_f32_16x16x32_bf16 v[108:111], v[44:47], v[92:95], v[108:111]\n\t"
    "s_waitcnt lgkmcnt(7)\n\t"
    "v_mfma_f32_16x16x32_bf16 v[108:111], v[48:51], v[96:99], v[108:111]\n\t"
    "s_waitcnt lgkmcnt(6)\n\t"
    "v_mfma_f32_16x16x32_bf16 v[108:111], v[52:55], v[100:103], v[108:111]\n\t"
    "s_waitcnt lgkmcnt(5)\n\t"
    "v_mfma_f32_16x16x32_bf16 v[112:115], v[56:59], v[80:83], 0\n\t"
    "s_waitcnt lgkmcnt(4)\n\t"
    "v_mfma_f32_16x16x32_bf16 v[112:115], v[60:63], v[84:87], v[112:115]\n\t"
    "s_waitcnt lgkmcnt(3)\n\t"
    "v_mfma_f32_16x16x32_bf16 v[112:115], v[64:67], v[88:91], v[112:115]\n\t"
    "s_waitcnt lgkmcnt(2)\n\t"
    "v_mfma_f32_16x16x32_bf16 v[112:115], v[68:71], v[92:95], v[112:115]\n\t"
    "s_waitcnt lgkmcnt(1)\n\t"
    "v_mfma_f32_16x16x32_bf16 v[112:115], v[72:75], v[96:99], v[112:115]\n\t"
    "s_waitcnt lgkmcnt(0)\n\t"
    "v_mfma_f32_16x16x32_bf16 v[112:115], v[76:79], v[100:103], v[112:115]\n\t"
    "s_cmp_eq_u32 s71, 0\n\t"
    "s_cselect_b32 s60, 0x3dd53b95, 1.0\n\t"
    "v_mul_f32_e32 v108, s60, v108\n\t"
    "v_mul_f32_e32 v109, s60, v109\n\t"
    "v_mul_f32_e32 v110, s60, v110\n\t"
    "v_mul_f32_e32 v111, s60, v111\n\t"
    "s_nop 3\n\t"
    "v_mul_f32_e32 v112, s60, v112\n\t"
    "v_mul_f32_e32 v113, s60, v113\n\t"
    "v_mul_f32_e32 v114, s60, v114\n\t"
    "v_mul_f32_e32 v115, s60, v115\n\t"
    "v_cvt_pk_bf16_f32 v104, v108, v109\n\t"
    "v_cvt_pk_bf16_f32 v105, v110, v111\n\t"
    "s_nop 0\n\t"
    "v_cvt_pk_bf16_f32 v106, v112, v113\n\t"
    "v_cvt_pk_bf16_f32 v107, v114, v115\n\t"
    "ds_write_b128 v24, v[104:107] offset:49152\n\t"
    "ds_read_b128 v[104:107], v30 offset:49152\n\t"
    "s_waitcnt lgkmcnt(0)\n\t"
    "buffer_store_dwordx4 v[104:107], v20, s[56:59], 0 offen sc1\n\t"
    ".Lnq_%=:\n\t"
    "s_waitcnt vmcnt(0)\n\t"
    "v_add_co_u32_e32 v22, vcc, 0x7f4a7c15, v22\n\t"
    "v_mov_b32_e32 v16, 0x9e3779b9\n\t"
    "s_nop 0\n\t"
    "v_addc_co_u32_e32 v23, vcc, v16, v23, vcc\n\t"
    "s_mov_b32 exec_lo, %[prod]\n\t"
    "s_mov_b32 exec_hi, 0\n\t"
    "buffer_store_dwordx2 v[22:23], off, s[56:59], %[foff] sc1\n\t"
    "s_mov_b64 exec, -1\n\t"
    "v_lshrrev_b32_e32 v20, 5, v31\n\t"
    "v_mul_u32_u24_e32 v20, 0x30000, v20\n\t"
    "v_and_b32_e32 v21, 1, v29\n\t"
    "v_mul_u32_u24_e32 v21, 0x600, v21\n\t"
    "v_add_u32_e32 v20, v20, v21\n\t"
    "v_and_b32_e32 v21, 15, v31\n\t"
    "v_lshl_add_u32 v7, v21, 4, v20\n\t"
    "v_lshl_add_u32 v8, v21, 3, v20\n\t"
    "v_add_u32_e32 v8, 0x100, v8\n\t"
    "v_and_b32_e32 v20, 48, v30\n\t"
    "v_lshl_or_b32 v20, v29, 6, v20\n\t"
    "v_lshrrev_b32_e32 v21, 2, v21\n\t"
    "v_lshl_or_b32 v9, v21, 8, v20\n\t"
    "v_min_u32_e32 v21, 35, v31\n\t"
    "v_mul_u32_u24_e32 v21, 64, v21\n\t"
    "v_add_u32_e32 v21, 0x28000, v21\n\t"
    "s_mov_b32 s52, s44\n\t"
    "s_and_b32 s53, s45, 0xffff\n\t"
    "s_mov_b32 s54, 0x6000000\n\t"
    "s_mov_b32 s55, 0x20000\n\t"
    "s_cmp_eq_u32 %[pair], 0\n\t"
    "s_cbranch_scc1 .Lng_%=\n\t"
    "s_sleep 45\n\t"
    "s_mov_b32 s64, 0\n\t"
    "s_mov_b32 s65, 2304\n\t"
    "buffer_load_dwordx2 v[12:13], v21, s[56:59], s64 offen sc1\n\t"
    "buffer_load_dwordx2 v[14:15], v21, s[56:59], s65 offen sc1\n\t"
    "s_waitcnt vmcnt(0)\n\t"
    "s_mov_b32 s60, 0x10000\n\t"
    ".Lp5_%=:\n\t"
    "v_cmp_eq_u64_e32 vcc, v[12:13], v[22:23]\n\t"
    "v_cmp_eq_u64_e64 s[70:71], v[14:15], v[22:23]\n\t"
    "s_and_b64 vcc, vcc, s[70:71]\n\t"
    "s_cmp_eq_u64 vcc, exec\n\t"
    "s_cbranch_scc1 .Lk6_%=\n\t"
    "s_sleep 4\n\t"
    "buffer_load_dwordx2 v[12:13], v21, s[56:59], s64 offen sc1\n\t"
    "buffer_load_dwordx2 v[14:15], v21, s[56:59], s65 offen sc1\n\t"
    "s_sub_u32 s60, s60, 1\n\t"
    "s_cmp_lg_u32 s60, 0\n\t"
    "s_waitcnt vmcnt(0)\n\t"
    "s_cbranch_scc1 .Lp5_%=\n\t"
    ".Lk6_%=:\n\t"
    ".Lng_%=:\n\t"
    "s_mul_i32 s66, %[pair], 36864\n\t"
    "s_mul_i32 s62, %[pair], 73728\n\t"
    "s_add_u32 s62, s62, 36864\n\t"
    "v_add_u32_e32 v11, s62, v30\n\t"
    "s_mov_b32 s63, 73728\n\t"
    "s_barrier\n\t"
    "s_add_u32 s67, %[tch], 0\n\t"
    "s_mov_b64 exec, 0x1ff\n\t"
    "buffer_load_dword v10, v26, s[56:59], s67 offen sc1\n\t"
    "s_mov_b64 exec, -1\n\t"
    "buffer_load_dwordx4 v[32:35], v30, s[56:59], s66 offen offset:0 sc1\n\t"
    "buffer_load_dwordx4 v[36:39], v30, s[56:59], s66 offen offset:1024 sc1\n\t"
    "buffer_load_dwordx4 v[40:43], v30, s[56:59], s66 offen offset:2048 sc1\n\t"
    "buffer_load_dwordx4 v[44:47], v30, s[56:59], s66 offen offset:3072 sc1\n\t"
    "s_add_u32 s66, s66, 4096\n\t"
    "buffer_load_dwordx4 v[48:51], v30, s[56:59], s66 offen offset:0 sc1\n\t"
    "buffer_load_dwordx4 v[52:55], v30, s[56:59], s66 offen offset:1024 sc1\n\t"
    "buffer_load_dwordx4 v[56:59], v30, s[56:59], s66 offen offset:2048 sc1\n\t"
    "buffer_load_dwordx4 v[60:63], v30, s[56:59], s66 offen offset:3072 sc1\n\t"
    "s_add_u32 s66, s66, 4096\n\t"
    "buffer_load_dwordx4 v[64:67], v30, s[56:59], s66 offen offset:0 sc1\n\t"
    "buffer_load_dwordx4 v[68:71], v30, s[56:59], s66 offen offset:1024 sc1\n\t"
    "buffer_load_dwordx4 v[72:75], v30, s[56:59], s66 offen offset:2048 sc1\n\t"
    "buffer_load_dwordx4 v[76:79], v30, s[56:59], s66 offen offset:3072 sc1\n\t"
    "s_add_u32 s66, s66, 4096\n\t"
    "buffer_load_dwordx4 v[80:83], v30, s[56:59], s66 offen offset:0 sc1\n\t"
    "buffer_load_dwordx4 v[84:87], v30, s[56:59], s66 offen offset:1024 sc1\n\t"
    "buffer_load_dwordx4 v[88:91], v30, s[56:59], s66 offen offset:2048 sc1\n\t"
    "buffer_load_dwordx4 v[92:95], v30, s[56:59], s66 offen offset:3072 sc1\n\t"
    "s_add_u32 s66, s66, 4096\n\t"
    "buffer_load_dwordx4 v[96:99], v30, s[56:59], s66 offen offset:0 sc1\n\t"
    "buffer_load_dwordx4 v[100:103], v30, s[56:59], s66 offen offset:1024 sc1\n\t"
    "buffer_load_dwordx4 v[104:107], v30, s[56:59], s66 offen offset:2048 sc1\n\t"
    "buffer_load_dwordx4 v[108:111], v30, s[56:59], s66 offen offset:3072 sc1\n\t"
    "s_add_u32 s66, s66, 4096\n\t"
    "buffer_load_dwordx4 v[112:115], v30, s[56:59], s66 offen offset:0 sc1\n\t"
    "buffer_load_dwordx4 v[116:119], v30, s[56:59], s66 offen offset:1024 sc1\n\t"
    "buffer_load_dwordx4 v[120:123], v30, s[56:59], s66 offen offset:2048 sc1\n\t"
    "buffer_load_dwordx4 v[124:127], v30, s[56:59], s66 offen offset:3072 sc1\n\t"
    "s_add_u32 s66, s66, 4096\n\t"
    "s_mov_b32 s64, 4608\n\t"
    "s_mov_b32 s65, 6912\n\t"
    "buffer_load_dwordx2 v[12:13], v21, s[56:59], s64 offen sc1\n\t"
    "buffer_load_dwordx2 v[14:15], v21, s[56:59], s65 offen sc1\n\t"
    "s_waitcnt vmcnt(25)\n\t"
    "ds_write_b128 v11, v[32:35] offset:0\n\t"
    "buffer_load_dwordx4 v[128:131], v30, s[56:59], s66 offen offset:0 sc1\n\t"
    "s_waitcnt vmcnt(25)\n\t"
    "ds_write_b128 v11, v[36:39] offset:1024\n\t"
    "buffer_load_dwordx4 v[132:135], v30, s[56:59], s66 offen offset:1024 sc1\n\t"
    "s_waitcnt vmcnt(25)\n\t"
    "ds_write_b128 v11, v[40:43] offset:2048\n\t"
    "buffer_load_dwordx4 v[136:139], v30, s[56:59], s66 offen offset:2048 sc1\n\t"
    "s_waitcnt vmcnt(25)\n\t"
    "ds_write_b128 v11, v[44:47] offset:3072\n\t"
    "buffer_load_dwordx4 v[140:143], v30, s[56:59], s66 offen offset:3072 sc1\n\t"
    "s_add_u32 s66, s66, 4096\n\t"
    "s_waitcnt vmcnt(25)\n\t"
    "ds_write_b128 v11, v[48:51] offset:4096\n\t"
    "buffer_load_dwordx4 v[144:147], v30, s[56:59], s66 offen offset:0 sc1\n\t"
    "s_waitcnt vmcnt(25)\n\t"
    "ds_write_b128 v11, v[52:55] offset:5120\n\t"
    "s_waitcnt lgkmcnt(0)\n\t"
    "s_barrier\n\t"
    "buffer_load_dwordx4 v[148:151], v30, s[56:59], s66 offen offset:1024 sc1\n\t"
    "s_waitcnt vmcnt(25)\n\t"
    "ds_write_b128 v11, v[56:59] offset:6144\n\t"
    "buffer_load_dwordx4 v[152:155], v30, s[56:59], s66 offen offset:2048 sc1\n\t"
    "s_waitcnt vmcnt(25)\n\t"
    "ds_write_b128 v11, v[60:63] offset:7168\n\t"
    "buffer_load_dwordx4 v[156:159], v30, s[56:59], s66 offen offset:3072 sc1\n\t"
    "s_add_u32 s66, s66, 4096\n\t"
    "s_waitcnt vmcnt(25)\n\t"
    "ds_write_b128 v11, v[64:67] offset:8192\n\t"
    "buffer_load_dwordx4 v[160:163], v30, s[56:59], s66 offen offset:0 sc1\n\t"
    "s_waitcnt vmcnt(25)\n\t"
    "ds_write_b128 v11, v[68:71] offset:9216\n\t"
    "buffer_load_dwordx4 v[164:167], v30, s[56:59], s66 offen offset:1024 sc1\n\t"
    "s_waitcnt vmcnt(25)\n\t"
    "ds_write_b128 v11, v[72:75] offset:10240\n\t"
    "buffer_load_dwordx4 v[168:171], v30, s[56:59], s66 offen offset:2048 sc1\n\t"
    "s_waitcnt vmcnt(25)\n\t"
    "ds_write_b128 v11, v[76:79] offset:11264\n\t"
    "s_waitcnt lgkmcnt(0)\n\t"
    "s_barrier\n\t"
    "buffer_load_dwordx4 v[172:175], v30, s[56:59], s66 offen offset:3072 sc1\n\t"
    "s_add_u32 s66, s66, 4096\n\t"
    "s_waitcnt vmcnt(25)\n\t"
    "ds_write_b128 v11, v[80:83] offset:12288\n\t"
    "s_waitcnt vmcnt(24)\n\t"
    "ds_write_b128 v11, v[84:87] offset:13312\n\t"
    "s_waitcnt vmcnt(23)\n\t"
    "ds_write_b128 v11, v[88:91] offset:14336\n\t"
    "s_waitcnt vmcnt(22)\n\t"
    "ds_write_b128 v11, v[92:95] offset:15360\n\t"
    "s_waitcnt vmcnt(21)\n\t"
    "ds_write_b128 v11, v[96:99] offset:16384\n\t"
    "s_waitcnt vmcnt(20)\n\t"
    "ds_write_b128 v11, v[100:103] offset:17408\n\t"
    "s_waitcnt vmcnt(19)\n\t"
    "ds_write_b128 v11, v[104:107] offset:18432\n\t"
    "s_waitcnt vmcnt(18)\n\t"
    "ds_write_b128 v11, v[108:111] offset:19456\n\t"
    "s_waitcnt vmcnt(17)\n\t"
    "ds_write_b128 v11, v[112:115] offset:20480\n\t"
    "s_waitcnt vmcnt(16)\n\t"
    "ds_write_b128 v11, v[116:119] offset:21504\n\t"
    "s_waitcnt vmcnt(15)\n\t"
    "ds_write_b128 v11, v[120:123] offset:22528\n\t"
    "s_waitcnt vmcnt(14)\n\t"
    "ds_write_b128 v11, v[124:127] offset:23552\n\t"
    "s_waitcnt lgkmcnt(0)\n\t"
    "s_barrier\n\t"
    "s_waitcnt vmcnt(12)\n\t"
    "s_mov_b32 s60, 0x10000\n\t"
    ".Lp7_%=:\n\t"
    "v_cmp_eq_u64_e32 vcc, v[12:13], v[22:23]\n\t"
    "v_cmp_eq_u64_e64 s[70:71], v[14:15], v[22:23]\n\t"
    "s_and_b64 vcc, vcc, s[70:71]\n\t"
    "s_cmp_eq_u64 vcc, exec\n\t"
    "s_cbranch_scc1 .Lk8_%=\n\t"
    "s_sleep 4\n\t"
    "buffer_load_dwordx2 v[12:13], v21, s[56:59], s64 offen sc1\n\t"
    "buffer_load_dwordx2 v[14:15], v21, s[56:59], s65 offen sc1\n\t"
    "s_sub_u32 s60, s60, 1\n\t"
    "s_cmp_lg_u32 s60, 0\n\t"
    "s_waitcnt vmcnt(0)\n\t"
    "s_cbranch_scc1 .Lp7_%=\n\t"
    ".Lk8_%=:\n\t"
    "s_add_u32 s67, %[tch], 73728\n\t"
    "s_mov_b64 exec, 0x1ff\n\t"
    "buffer_load_dword v10, v26, s[56:59], s67 offen sc1\n\t"
    "s_mov_b64 exec, -1\n\t"
    "buffer_load_dwordx4 a[0:3], v9, s[56:59], s63 offen offset:0 sc1\n\t"
    "buffer_load_dwordx4 a[4:7], v9, s[56:59], s63 offen offset:1024 sc1\n\t"
    "buffer_load_dwordx4 a[8:11], v9, s[56:59], s63 offen offset:2048 sc1\n\t"
    "buffer_load_dwordx4 a[12:15], v9, s[56:59], s63 offen offset:3072 sc1\n\t"
    "s_add_u32 s63, s63, 4096\n\t"
    "s_waitcnt vmcnt(16)\n\t"
    "ds_write_b128 v11, v[128:131] offset:24576\n\t"
    "buffer_load_dwordx4 a[16:19], v9, s[56:59], s63 offen offset:0 sc1\n\t"
    "s_waitcnt vmcnt(16)\n\t"
    "ds_write_b128 v11, v[132:135] offset:25600\n\t"
    "buffer_load_dwordx4 a[20:23], v9, s[56:59], s63 offen offset:1024 sc1\n\t"
    "s_waitcnt vmcnt(16)\n\t"
    "ds_write_b128 v11, v[136:139] offset:26624\n\t"
    "buffer_load_dwordx4 a[24:27], v9, s[56:59], s63 offen offset:2048 sc1\n\t"
    "s_waitcnt vmcnt(16)\n\t"
    "ds_write_b128 v11, v[140:143] offset:27648\n\t"
    "buffer_load_dwordx4 a[28:31], v9, s[56:59], s63 offen offset:3072 sc1\n\t"
    "s_add_u32 s63, s63, 4096\n\t"
    "s_waitcnt vmcnt(16)\n\t"
    "ds_write_b128 v11, v[144:147] offset:28672\n\t"
    "buffer_load_dwordx4 a[32:35], v9, s[56:59], s63 offen offset:0 sc1\n\t"
    "s_waitcnt vmcnt(16)\n\t"
    "ds_write_b128 v11, v[148:151] offset:29696\n\t"
    "buffer_load_dwordx4 a[36:39], v9, s[56:59], s63 offen offset:1024 sc1\n\t"
    "s_waitcnt vmcnt(16)\n\t"
    "ds_write_b128 v11, v[152:155] offset:30720\n\t"
    "buffer_load_dwordx4 a[40:43], v9, s[56:59], s63 offen offset:2048 sc1\n\t"
    "s_waitcnt vmcnt(16)\n\t"
    "ds_write_b128 v11, v[156:159] offset:31744\n\t"
    "buffer_load_dwordx4 a[44:47], v9, s[56:59], s63 offen offset:3072 sc1\n\t"
    "s_add_u32 s63, s63, 4096\n\t"
    "s_waitcnt vmcnt(16)\n\t"
    "ds_write_b128 v11, v[160:163] offset:32768\n\t"
    "buffer_load_dwordx4 a[48:51], v9, s[56:59], s63 offen offset:0 sc1\n\t"
    "s_waitcnt vmcnt(16)\n\t"
    "ds_write_b128 v11, v[164:167] offset:33792\n\t"
    "buffer_load_dwordx4 a[52:55], v9, s[56:59], s63 offen offset:1024 sc1\n\t"
    "s_waitcnt vmcnt(16)\n\t"
    "ds_write_b128 v11, v[168:171] offset:34816\n\t"
    "buffer_load_dwordx4 a[56:59], v9, s[56:59], s63 offen offset:2048 sc1\n\t"
    "s_waitcnt vmcnt(16)\n\t"
    "ds_write_b128 v11, v[172:175] offset:35840\n\t"
    "s_waitcnt lgkmcnt(0)\n\t"
    "s_barrier\n\t"
    "buffer_load_dwordx4 a[60:63], v9, s[56:59], s63 offen offset:3072 sc1\n\t"
    "s_add_u32 s63, s63, 4096\n\t"
    "buffer_load_dwordx4 a[64:67], v9, s[56:59], s63 offen offset:0 sc1\n\t"
    "buffer_load_dwordx4 a[68:71], v9, s[56:59], s63 offen offset:1024 sc1\n\t"
    "buffer_load_dwordx4 a[72:75], v9, s[56:59], s63 offen offset:2048 sc1\n\t"
    "buffer_load_dwordx4 a[76:79], v9, s[56:59], s63 offen offset:3072 sc1\n\t"
    "s_add_u32 s63, s63, 4096\n\t"
    "buffer_load_dwordx4 a[80:83], v9, s[56:59], s63 offen offset:0 sc1\n\t"
    "buffer_load_dwordx4 a[84:87], v9, s[56:59], s63 offen offset:1024 sc1\n\t"
    "buffer_load_dwordx4 a[88:91], v9, s[56:59], s63 offen offset:2048 sc1\n\t"
    "buffer_load_dwordx4 a[92:95], v9, s[56:59], s63 offen offset:3072 sc1\n\t"
    "s_add_u32 s63, s63, 4096\n\t"
    "buffer_load_dwordx4 a[96:99], v9, s[56:59], s63 offen offset:0 sc1\n\t"
    "buffer_load_dwordx4 a[100:103], v9, s[56:59], s63 offen offset:1024 sc1\n\t"
    "buffer_load_dwordx4 a[104:107], v9, s[56:59], s63 offen offset:2048 sc1\n\t"
    "buffer_load_dwordx4 a[108:111], v9, s[56:59], s63 offen offset:3072 sc1\n\t"
    "s_add_u32 s63, s63, 4096\n\t"
    "buffer_load_dwordx4 a[112:115], v9, s[56:59], s63 offen offset:0 sc1\n\t"
    "buffer_load_dwordx4 a[116:119], v9, s[56:59], s63 offen offset:1024 sc1\n\t"
    "buffer_load_dwordx4 a[120:123], v9, s[56:59], s63 offen offset:2048 sc1\n\t"
    "buffer_load_dwordx4 a[124:127], v9, s[56:59], s63 offen offset:3072 sc1\n\t"
    "s_add_u32 s63, s63, 4096\n\t"
    "buffer_load_dwordx4 a[128:131], v9, s[56:59], s63 offen offset:0 sc1\n\t"
    "buffer_load_dwordx4 a[132:135], v9, s[56:59], s63 offen offset:1024 sc1\n\t"
    "buffer_load_dwordx4 a[136:139], v9, s[56:59], s63 offen offset:2048 sc1\n\t"
    "buffer_load_dwordx4 a[140:143], v9, s[56:59], s63 offen offset:3072 sc1\n\t"
    "s_add_u32 s63, s63, 4096\n\t"
    "buffer_load_dwordx4 a[144:147], v9, s[56:59], s63 offen offset:0 sc1\n\t"
    "buffer_load_dwordx4 a[148:151], v9, s[56:59], s63 offen offset:1024 sc1\n\t"
    "buffer_load_dwordx4 a[152:155], v9, s[56:59], s63 offen offset:2048 sc1\n\t"
    "buffer_load_dwordx4 a[156:159], v9, s[56:59], s63 offen offset:3072 sc1\n\t"
    "s_add_u32 s63, s63, 4096\n\t"
    "buffer_load_dwordx4 a[160:163], v9, s[56:59], s63 offen offset:0 sc1\n\t"
    "buffer_load_dwordx4 a[164:167], v9, s[56:59], s63 offen offset:1024 sc1\n\t"
    "buffer_load_dwordx4 a[168:171], v9, s[56:59], s63 offen offset:2048 sc1\n\t"
    "buffer_load_dwordx4 a[172:175], v9, s[56:59], s63 offen offset:3072 sc1\n\t"
    "s_add_u32 s63, s63, 4096\n\t"
    "buffer_load_dwordx4 a[176:179], v9, s[56:59], s63 offen offset:0 sc1\n\t"
    "buffer_load_dwordx4 a[180:183], v9, s[56:59], s63 offen offset:1024 sc1\n\t"
    "buffer_load_dwordx4 a[184:187], v9, s[56:59], s63 offen offset:2048 sc1\n\t"
    "buffer_load_dwordx4 a[188:191], v9, s[56:59], s63 offen offset:3072 sc1\n\t"
    "s_add_u32 s63, s63, 4096\n\t"
    "buffer_load_dwordx4 a[192:195], v9, s[56:59], s63 offen offset:0 sc1\n\t"
    "buffer_load_dwordx4 a[196:199], v9, s[56:59], s63 offen offset:1024 sc1\n\t"
    "buffer_load_dwordx4 a[200:203], v9, s[56:59], s63 offen offset:2048 sc1\n\t"
    "buffer_load_dwordx4 a[204:207], v9, s[56:59], s63 offen offset:3072 sc1\n\t"
    "s_add_u32 s63, s63, 4096\n\t"
    "buffer_load_dwordx4 a[208:211], v9, s[56:59], s63 offen offset:0 sc1\n\t"
    "buffer_load_dwordx4 a[212:215], v9, s[56:59], s63 offen offset:1024 sc1\n\t"
    "buffer_load_dwordx4 a[216:219], v9, s[56:59], s63 offen offset:2048 sc1\n\t"
    "buffer_load_dwordx4 a[220:223], v9, s[56:59], s63 offen offset:3072 sc1\n\t"
    "s_add_u32 s63, s63, 4096\n\t"
    "buffer_load_dwordx4 a[224:227], v9, s[56:59], s63 offen offset:0 sc1\n\t"
    "buffer_load_dwordx4 a[228:231], v9, s[56:59], s63 offen offset:1024 sc1\n\t"
    "buffer_load_dwordx4 a[232:235], v9, s[56:59], s63 offen offset:2048 sc1\n\t"
    "buffer_load_dwordx4 a[236:239], v9, s[56:59], s63 offen offset:3072 sc1\n\t"
    "s_add_u32 s63, s63, 4096\n\t"
    "buffer_load_dwordx4 a[240:243], v9, s[56:59], s63 offen offset:0 sc1\n\t"
    "buffer_load_dwordx4 a[244:247], v9, s[56:59], s63 offen offset:1024 sc1\n\t"
    "buffer_load_dwordx4 a[248:251], v9, s[56:59], s63 offen offset:2048 sc1\n\t"
    "buffer_load_dwordx4 a[252:255], v9, s[56:59], s63 offen offset:3072 sc1\n\t"
    "s_add_u32 s63, s63, 4096\n\t"
    "buffer_load_dwordx4 v[224:227], v9, s[56:59], s63 offen offset:0 sc1\n\t"
    "buffer_load_dwordx4 v[228:231], v9, s[56:59], s63 offen offset:1024 sc1\n\t"
    "buffer_load_dwordx4 v[232:235], v9, s[56:59], s63 offen offset:2048 sc1\n\t"
    "buffer_load_dwordx4 v[236:239], v9, s[56:59], s63 offen offset:3072 sc1\n\t"
    "s_add_u32 s63, s63, 4096\n\t"
    "buffer_load_dwordx4 v[240:243], v9, s[56:59], s63 offen offset:0 sc1\n\t"
    "buffer_load_dwordx4 v[244:247], v9, s[56:59], s63 offen offset:1024 sc1\n\t"
    "buffer_load_dwordx4 v[248:251], v9, s[56:59], s63 offen offset:2048 sc1\n\t"
    "buffer_load_dwordx4 v[252:255], v9, s[56:59], s63 offen offset:3072 sc1\n\t"
    "s_add_u32 s63, s63, 4096\n\t"
    ".Lh1_%=:\n\t"
    "s_cbranch_execz .Lh2_%=\n\t"
    "s_barrier\n\t"
    "s_mov_b64 exec, %[amask]\n\t"
    "buffer_store_dwordx2 v[22:23], off, s[56:59], %[aown] sc1\n\t"
    "s_mov_b64 exec, -1\n\t"
    "ds_read_b128 v[60:63], v0 offset:0\n\t"
    "ds_read_b128 v[64:67], v1 offset:0\n\t"
    "ds_read_b128 v[68:71], v2 offset:0\n\t"
    "ds_read_b128 v[72:75], v3 offset:0\n\t"
    "ds_read_b128 v[188:191], v4 offset:0\n\t"
    "ds_read_b128 v[76:79], v5 offset:0\n\t"
    "ds_read_b128 v[80:83], v5 offset:6144\n\t"
    "ds_read_b128 v[108:111], v4 offset:6144\n\t"
    "ds_read_b128 v[84:87], v3 offset:6144\n\t"
    "ds_read_b128 v[88:91], v2 offset:6144\n\t"
    "ds_read_b128 v[144:147], v1 offset:6144\n\t"
    "ds_read_b128 v[196:199], v0 offset:6144\n\t"
    "s_waitcnt lgkmcnt(0)\n\t"
    "s_add_u32 s61, %[wb], 0xff400000\n\t"
    "s_mov_b32 s62, %[rbeg]\n\t"
    "s_mov_b32 s60, 8\n\t"
    ".Lmin_%=:\n\t"
    "s_waitcnt lgkmcnt(3)\n\t"
    "s_waitcnt vmcnt(63)\n\t"
    "v_mfma_f32_16x16x32_bf16 v[24:27], v[60:63], a[0:3], 0\n\t"
    "v_cvt_pk_bf16_f32 v159, v30, v34\n\t"
    "v_cvt_pk_bf16_f32 v187, v31, v35\n\t"
    "v_mfma_f32_16x16x32_bf16 v[24:27], v[64:67], a[4:7], v[24:27]\n\t"
    "v_cvt_pk_bf16_f32 v104, v36, v12\n\t"
    "v_cvt_pk_bf16_f32 v106, v37, v13\n\t"
    "v_mfma_f32_16x16x32_bf16 v[24:27], v[68:71], a[8:11], v[24:27]\n\t"
    "v_cvt_pk_bf16_f32 v180, v38, v14\n\t"
    "v_cvt_pk_bf16_f32 v182, v39, v15\n\t"
    "v_mfma_f32_16x16x32_bf16 v[24:27], v[72:75], a[12:15], v[24:27]\n\t"
    "v_cvt_pk_bf16_f32 v105, v16, v20\n\t"
    "v_cvt_pk_bf16_f32 v107, v17, v21\n\t"
    "v_mfma_f32_16x16x32_bf16 v[24:27], v[188:191], a[16:19], v[24:27]\n\t"
    "v_cvt_pk_bf16_f32 v181, v18, v22\n\t"
    "v_cvt_pk_bf16_f32 v183, v19, v23\n\t"
    "v_mfma_f32_16x16x32_bf16 v[24:27], v[76:79], a[20:23], v[24:27]\n\t"
    "v_mfma_f32_16x16x32_bf16 v[28:31], v[80:83], a[20:23], 0\n\t"
    "v_mfma_f32_16x16x32_bf16 v[28:31], v[108:111], a[16:19], v[28:31]\n\t"
    "v_mfma_f32_16x16x32_bf16 v[28:31], v[84:87], a[12:15], v[28:31]\n\t"
    "s_waitcnt lgkmcnt(0)\n\t"
    "v_mfma_f32_16x16x32_bf16 v[28:31], v[88:91], a[8:11], v[28:31]\n\t"
    "v_cvt_pk_bf16_f32 v12, v24, v25\n\t"
    "v_cvt_pk_bf16_f32 v13, v26, v27\n\t"
    "v_mfma_f32_16x16x32_bf16 v[28:31], v[144:147], a[4:7], v[28:31]\n\t"
    "buffer_store_dwordx2 v[154:155], v8, s[52:55], s61 offen offset:1152 sc1\n\t"
    "v_mfma_f32_16x16x32_bf16 v[28:31], v[196:199], a[0:3], v[28:31]\n\t"
    "s_add_u32 s61, s61, 393216\n\t"
    "s_waitcnt vmcnt(62)\n\t"
    "v_mfma_f32_16x16x32_bf16 v[20:23], v[196:199], a[24:27], 0\n\t"
    "v_mfma_f32_16x16x32_bf16 v[20:23], v[144:147], a[28:31], v[20:23]\n\t"
    "v_mfma_f32_16x16x32_bf16 v[20:23], v[88:91], a[32:35], v[20:23]\n\t"
    "v_mfma_f32_16x16x32_bf16 v[20:23], v[84:87], a[36:39], v[20:23]\n\t"
    "v_cvt_pk_bf16_f32 v14, v28, v29\n\t"
    "v_cvt_pk_bf16_f32 v15, v30, v31\n\t"
    "v_mfma_f32_16x16x32_bf16 v[20:23], v[108:111], a[40:43], v[20:23]\n\t"
    "v_mfma_f32_16x16x32_bf16 v[20:23], v[80:83], a[44:47], v[20:23]\n\t"
    "v_mfma_f32_16x16x32_bf16 v[24:27], v[76:79], a[44:47], 0\n\t"
    "v_mfma_f32_16x16x32_bf16 v[24:27], v[188:191], a[40:43], v[24:27]\n\t"
    "v_mfma_f32_16x16x32_bf16 v[24:27], v[72:75], a[36:39], v[24:27]\n\t"
    "v_mfma_f32_16x16x32_bf16 v[24:27], v[68:71], a[32:35], v[24:27]\n\t"
    "v_cvt_pk_bf16_f32 v18, v20, v21\n\t"
    "v_cvt_pk_bf16_f32 v19, v22, v23\n\t"
    "v_mfma_f32_16x16x32_bf16 v[24:27], v[64:67], a[28:31], v[24:27]\n\t"
    "buffer_store_dwordx4 v[136:139], v7, s[52:55], s61 offen offset:0 sc1\n\t"
    "v_mfma_f32_16x16x32_bf16 v[24:27], v[60:63], a[24:27], v[24:27]\n\t"
    "s_waitcnt vmcnt(57)\n\t"
    "v_mfma_f32_16x16x32_bf16 v[28:31], v[60:63], a[48:51], 0\n\t"
    "v_mfma_f32_16x16x32_bf16 v[28:31], v[64:67], a[52:55], v[28:31]\n\t"
    "v_mfma_f32_16x16x32_bf16 v[28:31], v[68:71], a[56:59], v[28:31]\n\t"
    "v_mfma_f32_16x16x32_bf16 v[28:31], v[72:75], a[60:63], v[28:31]\n\t"
    "v_cvt_pk_bf16_f32 v16, v24, v25\n\t"
    "v_cvt_pk_bf16_f32 v17, v26, v27\n\t"
    "v_mfma_f32_16x16x32_bf16 v[28:31], v[188:191], a[64:67], v[28:31]\n\t"
    "v_mfma_f32_16x16x32_bf16 v[28:31], v[76:79], a[68:71], v[28:31]\n\t"
    "v_mfma_f32_16x16x32_bf16 v[24:27], v[80:83], a[68:71], 0\n\t"
    "v_mfma_f32_16x16x32_bf16 v[24:27], v[108:111], a[64:67], v[24:27]\n\t"
    "v_mfma_f32_16x16x32_bf16 v[24:27], v[84:87], a[60:63], v[24:27]\n\t"
    "v_mfma_f32_16x16x32_bf16 v[24:27], v[88:91], a[56:59], v[24:27]\n\t"
    "v_cvt_pk_bf16_f32 v20, v28, v29\n\t"
    "v_cvt_pk_bf16_f32 v21, v30, v31\n\t"
    "v_mfma_f32_16x16x32_bf16 v[24:27], v[144:147], a[52:55], v[24:27]\n\t"
    "buffer_store_dwordx4 v[132:135], v7, s[52:55], s61 offen offset:384 sc1\n\t"
    "v_mfma_f32_16x16x32_bf16 v[24:27], v[196:199], a[48:51], v[24:27]\n\t"
    "s_waitcnt vmcnt(52)\n\t"
    "v_mfma_f32_16x16x32_bf16 v[28:31], v[196:199], a[72:75], 0\n\t"
    "v_mfma_f32_16x16x32_bf16 v[28:31], v[144:147], a[76:79], v[28:31]\n\t"
    "v_mfma_f32_16x16x32_bf16 v[28:31], v[88:91], a[80:83], v[28:31]\n\t"
    "v_mfma_f32_16x16x32_bf16 v[28:31], v[84:87], a[84:87], v[28:31]\n\t"
    "v_cvt_pk_bf16_f32 v22, v24, v25\n\t"
    "v_cvt_pk_bf16_f32 v23, v26, v27\n\t"
    "v_mfma_f32_16x16x32_bf16 v[28:31], v[108:111], a[88:91], v[28:31]\n\t"
    "v_mfma_f32_16x16x32_bf16 v[28:31], v[80:83], a[92:95], v[28:31]\n\t"
    "v_mfma_f32_16x16x32_bf16 v[32:35], v[76:79], a[92:95], 0\n\t"
    "v_mfma_f32_16x16x32_bf16 v[32:35], v[188:191], a[88:91], v[32:35]\n\t"
    "v_mfma_f32_16x16x32_bf16 v[32:35], v[72:75], a[84:87], v[32:35]\n\t"
    "v_mfma_f32_16x16x32_bf16 v[32:35], v[68:71], a[80:83], v[32:35]\n\t"
    "v_cvt_pk_bf16_f32 v26, v28, v29\n\t"
    "v_cvt_pk_bf16_f32 v27, v30, v31\n\t"
    "v_mfma_f32_16x16x32_bf16 v[32:35], v[64:67], a[76:79], v[32:35]\n\t"
    "buffer_store_dwordx4 v[164:167], v7, s[52:55], s61 offen offset:768 sc1\n\t"
    "v_mfma_f32_16x16x32_bf16 v[32:35], v[60:63], a[72:75], v[32:35]\n\t"
    "s_waitcnt vmcnt(47)\n\t"
    "v_mfma_f32_16x16x32_bf16 v[36:39], v[60:63], a[96:99], 0\n\t"
    "v_mfma_f32_16x16x32_bf16 v[36:39], v[64:67], a[100:103], v[36:39]\n\t"
    "v_mfma_f32_16x16x32_bf16 v[36:39], v[68:71], a[104:107], v[36:39]\n\t"
    "v_mfma_f32_16x16x32_bf16 v[36:39], v[72:75], a[108:111], v[36:39]\n\t"
    "v_cvt_pk_bf16_f32 v24, v32, v33\n\t"
    "v_cvt_pk_bf16_f32 v25, v34, v35\n\t"
    "v_mfma_f32_16x16x32_bf16 v[36:39], v[188:191], a[112:115], v[36:39]\n\t"
    "v_mfma_f32_16x16x32_bf16 v[36:39], v[76:79], a[116:119], v[36:39]\n\t"
    "v_mfma_f32_16x16x32_bf16 v[32:35], v[80:83], a[116:119], 0\n\t"
    "v_mfma_f32_16x16x32_bf16 v[32:35], v[108:111], a[112:115], v[32:35]\n\t"
    "v_mfma_f32_16x16x32_bf16 v[32:35], v[84:87], a[108:111], v[32:35]\n\t"
    "v_mfma_f32_16x16x32_bf16 v[32:35], v[88:91], a[104:107], v[32:35]\n\t"
    "v_cvt_pk_bf16_f32 v28, v36, v37\n\t"
    "v_cvt_pk_bf16_f32 v29, v38, v39\n\t"
    "v_mfma_f32_16x16x32_bf16 v[32:35], v[144:147], a[100:103], v[32:35]\n\t"
    "buffer_store_dwordx4 v[140:143], v7, s[52:55], s61 offen offset:1152 sc1\n\t"
    "v_mfma_f32_16x16x32_bf16 v[32:35], v[196:199], a[96:99], v[32:35]\n\t"
    "s_waitcnt vmcnt(42)\n\t"
    "v_mfma_f32_16x16x32_bf16 v[36:39], v[196:199], a[120:123], 0\n\t"
    "v_mfma_f32_16x16x32_bf16 v[36:39], v[144:147], a[124:127], v[36:39]\n\t"
    "v_mfma_f32_16x16x32_bf16 v[36:39], v[88:91], a[128:131], v[36:39]\n\t"
    "v_mfma_f32_16x16x32_bf16 v[36:39], v[84:87], a[132:135], v[36:39]\n\t"
    "v_cvt_pk_bf16_f32 v30, v32, v33\n\t"
    "v_cvt_pk_bf16_f32 v31, v34, v35\n\t"
    "v_mfma_f32_16x16x32_bf16 v[36:39], v[108:111], a[136:139], v[36:39]\n\t"
    "v_mfma_f32_16x16x32_bf16 v[36:39], v[80:83], a[140:143], v[36:39]\n\t"
    "v_mfma_f32_16x16x32_bf16 v[40:43], v[76:79], a[140:143], 0\n\t"
    "v_mfma_f32_16x16x32_bf16 v[40:43], v[188:191], a[136:139], v[40:43]\n\t"
    "v_mfma_f32_16x16x32_bf16 v[40:43], v[72:75], a[132:135], v[40:43]\n\t"
    "v_mfma_f32_16x16x32_bf16 v[40:43], v[68:71], a[128:131], v[40:43]\n\t"
    "v_cvt_pk_bf16_f32 v34, v36, v37\n\t"
    "v_cvt_pk_bf16_f32 v35, v38, v39\n\t"
    "v_mfma_f32_16x16x32_bf16 v[40:43], v[64:67], a[124:127], v[40:43]\n\t"
    "buffer_store_dwordx2 v[148:149], v8, s[52:55], s61 offen offset:0 sc1\n\t"
    "v_mfma_f32_16x16x32_bf16 v[40:43], v[60:63], a[120:123], v[40:43]\n\t"
    "s_waitcnt vmcnt(37)\n\t"
    "v_mfma_f32_16x16x32_bf16 v[44:47], v[60:63], a[144:147], 0\n\t"
    "v_mfma_f32_16x16x32_bf16 v[44:47], v[64:67], a[148:151], v[44:47]\n\t"
    "v_mfma_f32_16x16x32_bf16 v[44:47], v[68:71], a[152:155], v[44:47]\n\t"
    "v_mfma_f32_16x16x32_bf16 v[44:47], v[72:75], a[156:159], v[44:47]\n\t"
    "v_cvt_pk_bf16_f32 v32, v40, v41\n\t"
    "v_cvt_pk_bf16_f32 v33, v42, v43\n\t"
    "v_mfma_f32_16x16x32_bf16 v[44:47], v[188:191], a[160:163], v[44:47]\n\t"
    "v_mfma_f32_16x16x32_bf16 v[44:47], v[76:79], a[164:167], v[44:47]\n\t"
    "v_mfma_f32_16x16x32_bf16 v[40:43], v[80:83], a[164:167], 0\n\t"
    "v_mfma_f32_16x16x32_bf16 v[40:43], v[108:111], a[160:163], v[40:43]\n\t"
    "v_mfma_f32_16x16x32_bf16 v[40:43], v[84:87], a[156:159], v[40:43]\n\t"
    "v_mfma_f32_16x16x32_bf16 v[40:43], v[88:91], a[152:155], v[40:43]\n\t"
    "v_cvt_pk_bf16_f32 v36, v44, v45\n\t"
    "v_cvt_pk_bf16_f32 v37, v46, v47\n\t"
    "v_mfma_f32_16x16x32_bf16 v[40:43], v[144:147], a[148:151], v[40:43]\n\t"
    "buffer_store_dwordx2 v[150:151], v8, s[52:55], s61 offen offset:384 sc1\n\t"
    "v_mfma_f32_16x16x32_bf16 v[40:43], v[196:199], a[144:147], v[40:43]\n\t"
    "s_waitcnt vmcnt(32)\n\t"
    "v_mfma_f32_16x16x32_bf16 v[44:47], v[196:199], a[168:171], 0\n\t"
    "v_mfma_f32_16x16x32_bf16 v[44:47], v[144:147], a[172:175], v[44:47]\n\t"
    "v_mfma_f32_16x16x32_bf16 v[44:47], v[88:91], a[176:179], v[44:47]\n\t"
    "v_mfma_f32_16x16x32_bf16 v[44:47], v[84:87], a[180:183], v[44:47]\n\t"
    "v_cvt_pk_bf16_f32 v38, v40, v41\n\t"
    "v_cvt_pk_bf16_f32 v39, v42, v43\n\t"
    "v_mfma_f32_16x16x32_bf16 v[44:47], v[108:111], a[184:187], v[44:47]\n\t"
    "v_mfma_f32_16x16x32_bf16 v[44:47], v[80:83], a[188:191], v[44:47]\n\t"
    "v_mfma_f32_16x16x32_bf16 v[48:51], v[76:79], a[188:191], 0\n\t"
    "v_mfma_f32_16x16x32_bf16 v[48:51], v[188:191], a[184:187], v[48:51]\n\t"
    "v_mfma_f32_16x16x32_bf16 v[48:51], v[72:75], a[180:183], v[48:51]\n\t"
    "v_mfma_f32_16x16x32_bf16 v[48:51], v[68:71], a[176:179], v[48:51]\n\t"
    "v_cvt_pk_bf16_f32 v42, v44, v45\n\t"
    "v_cvt_pk_bf16_f32 v43, v46, v47\n\t"
    "v_mfma_f32_16x16x32_bf16 v[48:51], v[64:67], a[172:175], v[48:51]\n\t"
    "buffer_store_dwordx2 v[176:177], v8, s[52:55], s61 offen offset:768 sc1\n\t"
    "v_mfma_f32_16x16x32_bf16 v[48:51], v[60:63], a[168:171], v[48:51]\n\t"
    "s_waitcnt vmcnt(27)\n\t"
    "v_mfma_f32_16x16x32_bf16 v[52:55], v[60:63], a[192:195], 0\n\t"
    "v_mfma_f32_16x16x32_bf16 v[52:55], v[64:67], a[196:199], v[52:55]\n\t"
    "v_mfma_f32_16x16x32_bf16 v[52:55], v[68:71], a[200:203], v[52:55]\n\t"
    "v_mfma_f32_16x16x32_bf16 v[52:55], v[72:75], a[204:207], v[52:55]\n\t"
    "v_cvt_pk_bf16_f32 v40, v48, v49\n\t"
    "v_cvt_pk_bf16_f32 v41, v50, v51\n\t"
    "v_mfma_f32_16x16x32_bf16 v[52:55], v[188:191], a[208:211], v[52:55]\n\t"
    "v_mfma_f32_16x16x32_bf16 v[52:55], v[76:79], a[212:215], v[52:55]\n\t"
    "v_mfma_f32_16x16x32_bf16 v[48:51], v[80:83], a[212:215], 0\n\t"
    "v_mfma_f32_16x16x32_bf16 v[48:51], v[108:111], a[208:211], v[48:51]\n\t"
    "v_mfma_f32_16x16x32_bf16 v[48:51], v[84:87], a[204:207], v[48:51]\n\t"
    "v_mfma_f32_16x16x32_bf16 v[48:51], v[88:91], a[200:203], v[48:51]\n\t"
    "v_cvt_pk_bf16_f32 v44, v52, v53\n\t"
    "v_cvt_pk_bf16_f32 v45, v54, v55\n\t"
    "v_mfma_f32_16x16x32_bf16 v[48:51], v[144:147], a[196:199], v[48:51]\n\t"
    "buffer_store_dwordx2 v[178:179], v8, s[52:55], s61 offen offset:1152 sc1\n\t"
    "v_mfma_f32_16x16x32_bf16 v[48:51], v[196:199], a[192:195], v[48:51]\n\t"
    "s_add_u32 s61, s61, 393216\n\t"
    "s_waitcnt vmcnt(22)\n\t"
    "v_mfma_f32_16x16x32_bf16 v[52:55], v[196:199], a[216:219], 0\n\t"
    "v_mfma_f32_16x16x32_bf16 v[52:55], v[144:147], a[220:223], v[52:55]\n\t"
    "v_mfma_f32_16x16x32_bf16 v[52:55], v[88:91], a[224:227], v[52:55]\n\t"
    "v_mfma_f32_16x16x32_bf16 v[52:55], v[84:87], a[228:231], v[52:55]\n\t"
    "v_cvt_pk_bf16_f32 v46, v48, v49\n\t"
    "v_cvt_pk_bf16_f32 v47, v50, v51\n\t"
    "v_mfma_f32_16x16x32_bf16 v[52:55], v[108:111], a[232:235], v[52:55]\n\t"
    "v_mfma_f32_16x16x32_bf16 v[52:55], v[80:83], a[236:239], v[52:55]\n\t"
    "v_mfma_f32_16x16x32_bf16 v[56:59], v[76:79], a[236:239], 0\n\t"
    "v_mfma_f32_16x16x32_bf16 v[56:59], v[188:191], a[232:235], v[56:59]\n\t"
    "v_mfma_f32_16x16x32_bf16 v[56:59], v[72:75], a[228:231], v[56:59]\n\t"
    "v_mfma_f32_16x16x32_bf16 v[56:59], v[68:71], a[224:227], v[56:59]\n\t"
    "v_cvt_pk_bf16_f32 v50, v52, v53\n\t"
    "v_cvt_pk_bf16_f32 v51, v54, v55\n\t"
    "v_mfma_f32_16x16x32_bf16 v[56:59], v[64:67], a[220:223], v[56:59]\n\t"
    "buffer_store_dwordx4 v[160:163], v7, s[52:55], s61 offen offset:0 sc1\n\t"
    "v_mfma_f32_16x16x32_bf16 v[56:59], v[60:63], a[216:219], v[56:59]\n\t"
    "s_waitcnt vmcnt(17)\n\t"
    "v_mfma_f32_16x16x32_bf16 v[92:95], v[60:63], a[240:243], 0\n\t"
    "v_mfma_f32_16x16x32_bf16 v[92:95], v[64:67], a[244:247], v[92:95]\n\t"
    "v_mfma_f32_16x16x32_bf16 v[92:95], v[68:71], a[248:251], v[92:95]\n\t"
    "v_mfma_f32_16x16x32_bf16 v[92:95], v[72:75], a[252:255], v[92:95]\n\t"
    "v_cvt_pk_bf16_f32 v48, v56, v57\n\t"
    "v_cvt_pk_bf16_f32 v49, v58, v59\n\t"
    "v_mfma_f32_16x16x32_bf16 v[92:95], v[188:191], v[224:227], v[92:95]\n\t"
    "v_mfma_f32_16x16x32_bf16 v[92:95], v[76:79], v[228:231], v[92:95]\n\t"
    "v_mfma_f32_16x16x32_bf16 v[56:59], v[80:83], v[228:231], 0\n\t"
    "v_mfma_f32_16x16x32_bf16 v[56:59], v[108:111], v[224:227], v[56:59]\n\t"
    "ds_read_b128 v[112:115], v0 offset:12288\n\t"
    "v_mfma_f32_16x16x32_bf16 v[56:59], v[84:87], a[252:255], v[56:59]\n\t"
    "ds_read_b128 v[132:135], v1 offset:12288\n\t"
    "v_mfma_f32_16x16x32_bf16 v[56:59], v[88:91], a[248:251], v[56:59]\n\t"
    "ds_read_b128 v[136:139], v2 offset:12288\n\t"
    "v_cvt_pk_bf16_f32 v52, v92, v93\n\t"
    "v_mfma_f32_16x16x32_bf16 v[56:59], v[144:147], a[244:247], v[56:59]\n\t"
    "ds_read_b128 v[140:143], v3 offset:12288\n\t"
    "v_cvt_pk_bf16_f32 v53, v94, v95\n\t"
    "v_mfma_f32_16x16x32_bf16 v[56:59], v[196:199], a[240:243], v[56:59]\n\t"
    "ds_read_b128 v[148:151], v4 offset:12288\n\t"
    "s_waitcnt vmcnt(11)\n\t"
    "v_mfma_f32_16x16x32_bf16 v[96:99], v[196:199], v[232:235], 0\n\t"
    "ds_read_b128 v[152:155], v5 offset:12288\n\t"
    "v_mfma_f32_16x16x32_bf16 v[96:99], v[144:147], v[236:239], v[96:99]\n\t"
    "buffer_store_dwordx4 v[128:131], v7, s[52:55], s61 offen offset:384 sc1\n\t"
    "v_mfma_f32_16x16x32_bf16 v[96:99], v[88:91], v[240:243], v[96:99]\n\t"
    "ds_read_b128 v[160:163], v5 offset:18432\n\t"
    "v_mfma_f32_16x16x32_bf16 v[96:99], v[84:87], v[244:247], v[96:99]\n\t"
    "ds_read_b128 v[164:167], v4 offset:18432\n\t"
    "v_cvt_pk_bf16_f32 v54, v56, v57\n\t"
    "v_mfma_f32_16x16x32_bf16 v[96:99], v[108:111], v[248:251], v[96:99]\n\t"
    "ds_read_b128 v[128:131], v3 offset:18432\n\t"
    "v_cvt_pk_bf16_f32 v55, v58, v59\n\t"
    "v_mfma_f32_16x16x32_bf16 v[96:99], v[80:83], v[252:255], v[96:99]\n\t"
    "ds_read_b128 v[144:147], v2 offset:18432\n\t"
    "v_mfma_f32_16x16x32_bf16 v[84:87], v[76:79], v[252:255], 0\n\t"
    "ds_read_b128 v[176:179], v1 offset:18432\n\t"
    "v_mfma_f32_16x16x32_bf16 v[84:87], v[188:191], v[248:251], v[84:87]\n\t"
    "ds_read_b128 v[108:111], v0 offset:18432\n\t"
    "v_mfma_f32_16x16x32_bf16 v[84:87], v[72:75], v[244:247], v[84:87]\n\t"
    "v_mfma_f32_16x16x32_bf16 v[84:87], v[68:71], v[240:243], v[84:87]\n\t"
    "v_cvt_pk_bf16_f32 v58, v96, v97\n\t"
    "v_cvt_pk_bf16_f32 v59, v98, v99\n\t"
    "v_mfma_f32_16x16x32_bf16 v[84:87], v[64:67], v[236:239], v[84:87]\n\t"
    "buffer_store_dwordx4 v[172:175], v7, s[52:55], s61 offen offset:768 sc1\n\t"
    "v_mfma_f32_16x16x32_bf16 v[84:87], v[60:63], v[232:235], v[84:87]\n\t"
    "s_waitcnt lgkmcnt(3)\n\t"
    "v_mfma_f32_16x16x32_bf16 v[68:71], v[112:115], a[0:3], 0\n\t"
    "v_mfma_f32_16x16x32_bf16 v[68:71], v[132:135], a[4:7], v[68:71]\n\t"
    "v_mfma_f32_16x16x32_bf16 v[68:71], v[136:139], a[8:11], v[68:71]\n\t"
    "v_mfma_f32_16x16x32_bf16 v[68:71], v[140:143], a[12:15], v[68:71]\n\t"
    "v_cvt_pk_bf16_f32 v56, v84, v85\n\t"
    "v_cvt_pk_bf16_f32 v57, v86, v87\n\t"
    "v_mfma_f32_16x16x32_bf16 v[68:71], v[148:151], a[16:19], v[68:71]\n\t"
    "v_mfma_f32_16x16x32_bf16 v[68:71], v[152:155], a[20:23], v[68:71]\n\t"
    "v_mfma_f32_16x16x32_bf16 v[64:67], v[160:163], a[20:23], 0\n\t"
    "v_mfma_f32_16x16x32_bf16 v[64:67], v[164:167], a[16:19], v[64:67]\n\t"
    "v_mfma_f32_16x16x32_bf16 v[64:67], v[128:131], a[12:15], v[64:67]\n\t"
    "s_waitcnt lgkmcnt(0)\n\t"
    "v_mfma_f32_16x16x32_bf16 v[64:67], v[144:147], a[8:11], v[64:67]\n\t"
    "v_cvt_pk_bf16_f32 v60, v68, v69\n\t"
    "v_cvt_pk_bf16_f32 v61, v70, v71\n\t"
    "v_mfma_f32_16x16x32_bf16 v[64:67], v[176:179], a[4:7], v[64:67]\n\t"
    "buffer_store_dwordx4 v[168:171], v7, s[52:55], s61 offen offset:1152 sc1\n\t"
    "v_mfma_f32_16x16x32_bf16 v[64:67], v[108:111], a[0:3], v[64:67]\n\t"
    "v_mfma_f32_16x16x32_bf16 v[68:71], v[108:111], a[24:27], 0\n\t"
    "v_mfma_f32_16x16x32_bf16 v[68:71], v[176:179], a[28:31], v[68:71]\n\t"
    "v_mfma_f32_16x16x32_bf16 v[68:71], v[144:147], a[32:35], v[68:71]\n\t"
    "v_mfma_f32_16x16x32_bf16 v[68:71], v[128:131], a[36:39], v[68:71]\n\t"
    "v_cvt_pk_bf16_f32 v62, v64, v65\n\t"
    "v_cvt_pk_bf16_f32 v63, v66, v67\n\t"
    "v_mfma_f32_16x16x32_bf16 v[68:71], v[164:167], a[40:43], v[68:71]\n\t"
    "v_mfma_f32_16x16x32_bf16 v[68:71], v[160:163], a[44:47], v[68:71]\n\t"
    "v_mfma_f32_16x16x32_bf16 v[72:75], v[152:155], a[44:47], 0\n\t"
    "v_mfma_f32_16x16x32_bf16 v[72:75], v[148:151], a[40:43], v[72:75]\n\t"
    "v_mfma_f32_16x16x32_bf16 v[72:75], v[140:143], a[36:39], v[72:75]\n\t"
    "v_mfma_f32_16x16x32_bf16 v[72:75], v[136:139], a[32:35], v[72:75]\n\t"
    "v_cvt_pk_bf16_f32 v66, v68, v69\n\t"
    "v_cvt_pk_bf16_f32 v67, v70, v71\n\t"
    "v_mfma_f32_16x16x32_bf16 v[72:75], v[132:135], a[28:31], v[72:75]\n\t"
    "buffer_store_dwordx2 v[116:117], v8, s[52:55], s61 offen offset:0 sc1\n\t"
    "v_mfma_f32_16x16x32_bf16 v[72:75], v[112:115], a[24:27], v[72:75]\n\t"
    "v_mfma_f32_16x16x32_bf16 v[76:79], v[112:115], a[48:51], 0\n\t"
    "v_mfma_f32_16x16x32_bf16 v[76:79], v[132:135], a[52:55], v[76:79]\n\t"
    "v_mfma_f32_16x16x32_bf16 v[76:79], v[136:139], a[56:59], v[76:79]\n\t"
    "v_mfma_f32_16x16x32_bf16 v[76:79], v[140:143], a[60:63], v[76:79]\n\t"
    "v_cvt_pk_bf16_f32 v64, v72, v73\n\t"
    "v_cvt_pk_bf16_f32 v65, v74, v75\n\t"
    "v_mfma_f32_16x16x32_bf16 v[76:79], v[148:151], a[64:67], v[76:79]\n\t"
    "v_mfma_f32_16x16x32_bf16 v[76:79], v[152:155], a[68:71], v[76:79]\n\t"
    "v_mfma_f32_16x16x32_bf16 v[72:75], v[160:163], a[68:71], 0\n\t"
    "v_mfma_f32_16x16x32_bf16 v[72:75], v[164:167], a[64:67], v[72:75]\n\t"
    "v_mfma_f32_16x16x32_bf16 v[72:75], v[128:131], a[60:63], v[72:75]\n\t"
    "v_mfma_f32_16x16x32_bf16 v[72:75], v[144:147], a[56:59], v[72:75]\n\t"
    "v_cvt_pk_bf16_f32 v68, v76, v77\n\t"
    "v_cvt_pk_bf16_f32 v69, v78, v79\n\t"
    "v_mfma_f32_16x16x32_bf16 v[72:75], v[176:179], a[52:55], v[72:75]\n\t"
    "buffer_store_dwordx2 v[118:119], v8, s[52:55], s61 offen offset:384 sc1\n\t"
    "v_mfma_f32_16x16x32_bf16 v[72:75], v[108:111], a[48:51], v[72:75]\n\t"
    "v_mfma_f32_16x16x32_bf16 v[76:79], v[108:111], a[72:75], 0\n\t"
    "s_add_i32 s64, s62, 24576\n\t"
    "v_mfma_f32_16x16x32_bf16 v[76:79], v[176:179], a[76:79], v[76:79]\n\t"
    "s_cmp_eq_u32 s64, %[rend]\n\t"
    "s_cselect_b32 s64, %[rbeg], s64\n\t"
    "v_mfma_f32_16x16x32_bf16 v[76:79], v[144:147], a[80:83], v[76:79]\n\t"
    "s_sub_i32 s65, s64, s62\n\t"
    "s_mov_b32 s62, s64\n\t"
    "v_mfma_f32_16x16x32_bf16 v[76:79], v[128:131], a[84:87], v[76:79]\n\t"
    "v_add_u32_e32 v0, s65, v0\n\t"
    "v_add_u32_e32 v1, s65, v1\n\t"
    "v_mfma_f32_16x16x32_bf16 v[76:79], v[164:167], a[88:91], v[76:79]\n\t"
    "v_add_u32_e32 v2, s65, v2\n\t"
    "v_add_u32_e32 v3, s65, v3\n\t"
    "v_mfma_f32_16x16x32_bf16 v[76:79], v[160:163], a[92:95], v[76:79]\n\t"
    "v_add_u32_e32 v4, s65, v4\n\t"
    "v_add_u32_e32 v5, s65, v5\n\t"
    "v_mfma_f32_16x16x32_bf16 v[80:83], v[152:155], a[92:95], 0\n\t"
    "v_cvt_pk_bf16_f32 v70, v72, v73\n\t"
    "v_cvt_pk_bf16_f32 v71, v74, v75\n\t"
    "v_mfma_f32_16x16x32_bf16 v[80:83], v[148:151], a[88:91], v[80:83]\n\t"
    "v_mfma_f32_16x16x32_bf16 v[80:83], v[140:143], a[84:87], v[80:83]\n\t"
    "v_mfma_f32_16x16x32_bf16 v[80:83], v[136:139], a[80:83], v[80:83]\n\t"
    "v_cvt_pk_bf16_f32 v74, v76, v77\n\t"
    "v_cvt_pk_bf16_f32 v75, v78, v79\n\t"
    "v_mfma_f32_16x16x32_bf16 v[80:83], v[132:135], a[76:79], v[80:83]\n\t"
    "buffer_store_dwordx2 v[192:193], v8, s[52:55], s61 offen offset:768 sc1\n\t"
    "v_mfma_f32_16x16x32_bf16 v[80:83], v[112:115], a[72:75], v[80:83]\n\t"
    "v_mfma_f32_16x16x32_bf16 v[84:87], v[112:115], a[96:99], 0\n\t"
    "v_mfma_f32_16x16x32_bf16 v[84:87], v[132:135], a[100:103], v[84:87]\n\t"
    "v_mfma_f32_16x16x32_bf16 v[84:87], v[136:139], a[104:107], v[84:87]\n\t"
    "v_mfma_f32_16x16x32_bf16 v[84:87], v[140:143], a[108:111], v[84:87]\n\t"
    "v_cvt_pk_bf16_f32 v72, v80, v81\n\t"
    "v_cvt_pk_bf16_f32 v73, v82, v83\n\t"
    "v_mfma_f32_16x16x32_bf16 v[84:87], v[148:151], a[112:115], v[84:87]\n\t"
    "v_mfma_f32_16x16x32_bf16 v[84:87], v[152:155], a[116:119], v[84:87]\n\t"
    "v_mfma_f32_16x16x32_bf16 v[80:83], v[160:163], a[116:119], 0\n\t"
    "v_mfma_f32_16x16x32_bf16 v[80:83], v[164:167], a[112:115], v[80:83]\n\t"
    "v_mfma_f32_16x16x32_bf16 v[80:83], v[128:131], a[108:111], v[80:83]\n\t"
    "v_mfma_f32_16x16x32_bf16 v[80:83], v[144:147], a[104:107], v[80:83]\n\t"
    "v_cvt_pk_bf16_f32 v76, v84, v85\n\t"
    "v_cvt_pk_bf16_f32 v77, v86, v87\n\t"
    "v_mfma_f32_16x16x32_bf16 v[80:83], v[176:179], a[100:103], v[80:83]\n\t"
    "buffer_store_dwordx2 v[194:195], v8, s[52:55], s61 offen offset:1152 sc1\n\t"
    "v_mfma_f32_16x16x32_bf16 v[80:83], v[108:111], a[96:99], v[80:83]\n\t"
    "s_add_u32 s61, s61, 393216\n\t"
    "v_mfma_f32_16x16x32_bf16 v[84:87], v[108:111], a[120:123], 0\n\t"
    "v_mfma_f32_16x16x32_bf16 v[84:87], v[176:179], a[124:127], v[84:87]\n\t"
    "v_mfma_f32_16x16x32_bf16 v[84:87], v[144:147], a[128:131], v[84:87]\n\t"
    "v_mfma_f32_16x16x32_bf16 v[84:87], v[128:131], a[132:135], v[84:87]\n\t"
    "v_cvt_pk_bf16_f32 v78, v80, v81\n\t"
    "v_cvt_pk_bf16_f32 v79, v82, v83\n\t"
    "v_mfma_f32_16x16x32_bf16 v[84:87], v[164:167], a[136:139], v[84:87]\n\t"
    "v_mfma_f32_16x16x32_bf16 v[84:87], v[160:163], a[140:143], v[84:87]\n\t"
    "v_mfma_f32_16x16x32_bf16 v[88:91], v[152:155], a[140:143], 0\n\t"
    "v_mfma_f32_16x16x32_bf16 v[88:91], v[148:151], a[136:139], v[88:91]\n\t"
    "v_mfma_f32_16x16x32_bf16 v[88:91], v[140:143], a[132:135], v[88:91]\n\t"
    "v_mfma_f32_16x16x32_bf16 v[88:91], v[136:139], a[128:131], v[88:91]\n\t"
    "v_cvt_pk_bf16_f32 v82, v84, v85\n\t"
    "v_cvt_pk_bf16_f32 v83, v86, v87\n\t"
    "v_mfma_f32_16x16x32_bf16 v[88:91], v[132:135], a[124:127], v[88:91]\n\t"
    "buffer_store_dwordx4 v[120:123], v7, s[52:55], s61 offen offset:0 sc1\n\t"
    "v_mfma_f32_16x16x32_bf16 v[88:91], v[112:115], a[120:123], v[88:91]\n\t"
    "v_mfma_f32_16x16x32_bf16 v[92:95], v[112:115], a[144:147], 0\n\t"
    "v_mfma_f32_16x16x32_bf16 v[92:95], v[132:135], a[148:151], v[92:95]\n\t"
    "v_mfma_f32_16x16x32_bf16 v[92:95], v[136:139], a[152:155], v[92:95]\n\t"
    "v_mfma_f32_16x16x32_bf16 v[92:95], v[140:143], a[156:159], v[92:95]\n\t"
    "v_cvt_pk_bf16_f32 v80, v88, v89\n\t"
    "v_cvt_pk_bf16_f32 v81, v90, v91\n\t"
    "v_mfma_f32_16x16x32_bf16 v[92:95], v[148:151], a[160:163], v[92:95]\n\t"
    "v_mfma_f32_16x16x32_bf16 v[92:95], v[152:155], a[164:167], v[92:95]\n\t"
    "v_mfma_f32_16x16x32_bf16 v[88:91], v[160:163], a[164:167], 0\n\t"
    "v_mfma_f32_16x16x32_bf16 v[88:91], v[164:167], a[160:163], v[88:91]\n\t"
    "v_mfma_f32_16x16x32_bf16 v[88:91], v[128:131], a[156:159], v[88:91]\n\t"
    "v_mfma_f32_16x16x32_bf16 v[88:91], v[144:147], a[152:155], v[88:91]\n\t"
    "v_cvt_pk_bf16_f32 v84, v92, v93\n\t"
    "v_cvt_pk_bf16_f32 v85, v94, v95\n\t"
    "v_mfma_f32_16x16x32_bf16 v[88:91], v[176:179], a[148:151], v[88:91]\n\t"
    "buffer_store_dwordx4 v[124:127], v7, s[52:55], s61 offen offset:384 sc1\n\t"
    "v_mfma_f32_16x16x32_bf16 v[88:91], v[108:111], a[144:147], v[88:91]\n\t"
    "v_mfma_f32_16x16x32_bf16 v[92:95], v[108:111], a[168:171], 0\n\t"
    "v_mfma_f32_16x16x32_bf16 v[92:95], v[176:179], a[172:175], v[92:95]\n\t"
    "v_mfma_f32_16x16x32_bf16 v[92:95], v[144:147], a[176:179], v[92:95]\n\t"
    "v_mfma_f32_16x16x32_bf16 v[92:95], v[128:131], a[180:183], v[92:95]\n\t"
    "v_cvt_pk_bf16_f32 v86, v88, v89\n\t"
    "v_cvt_pk_bf16_f32 v87, v90, v91\n\t"
    "v_mfma_f32_16x16x32_bf16 v[92:95], v[164:167], a[184:187], v[92:95]\n\t"
    "v_mfma_f32_16x16x32_bf16 v[92:95], v[160:163], a[188:191], v[92:95]\n\t"
    "v_mfma_f32_16x16x32_bf16 v[96:99], v[152:155], a[188:191], 0\n\t"
    "v_mfma_f32_16x16x32_bf16 v[96:99], v[148:151], a[184:187], v[96:99]\n\t"
    "v_mfma_f32_16x16x32_bf16 v[96:99], v[140:143], a[180:183], v[96:99]\n\t"
    "v_mfma_f32_16x16x32_bf16 v[96:99], v[136:139], a[176:179], v[96:99]\n\t"
    "v_cvt_pk_bf16_f32 v90, v92, v93\n\t"
    "v_cvt_pk_bf16_f32 v91, v94, v95\n\t"
    "v_mfma_f32_16x16x32_bf16 v[96:99], v[132:135], a[172:175], v[96:99]\n\t"
    "buffer_store_dwordx4 v[156:159], v7, s[52:55], s61 offen offset:768 sc1\n\t"
    "v_mfma_f32_16x16x32_bf16 v[96:99], v[112:115], a[168:171], v[96:99]\n\t"
    "v_mfma_f32_16x16x32_bf16 v[100:103], v[112:115], a[192:195], 0\n\t"
    "v_mfma_f32_16x16x32_bf16 v[100:103], v[132:135], a[196:199], v[100:103]\n\t"
    "v_mfma_f32_16x16x32_bf16 v[100:103], v[136:139], a[200:203], v[100:103]\n\t"
    "v_mfma_f32_16x16x32_bf16 v[100:103], v[140:143], a[204:207], v[100:103]\n\t"
    "v_cvt_pk_bf16_f32 v88, v96, v97\n\t"
    "v_cvt_pk_bf16_f32 v89, v98, v99\n\t"
    "v_mfma_f32_16x16x32_bf16 v[100:103], v[148:151], a[208:211], v[100:103]\n\t"
    "v_mfma_f32_16x16x32_bf16 v[100:103], v[152:155], a[212:215], v[100:103]\n\t"
    "v_mfma_f32_16x16x32_bf16 v[96:99], v[160:163], a[212:215], 0\n\t"
    "v_mfma_f32_16x16x32_bf16 v[96:99], v[164:167], a[208:211], v[96:99]\n\t"
    "v_mfma_f32_16x16x32_bf16 v[96:99], v[128:131], a[204:207], v[96:99]\n\t"
    "v_mfma_f32_16x16x32_bf16 v[96:99], v[144:147], a[200:203], v[96:99]\n\t"
    "v_cvt_pk_bf16_f32 v92, v100, v101\n\t"
    "v_cvt_pk_bf16_f32 v93, v102, v103\n\t"
    "v_mfma_f32_16x16x32_bf16 v[96:99], v[176:179], a[196:199], v[96:99]\n\t"
    "buffer_store_dwordx4 v[184:187], v7, s[52:55], s61 offen offset:1152 sc1\n\t"
    "v_mfma_f32_16x16x32_bf16 v[96:99], v[108:111], a[192:195], v[96:99]\n\t"
    "s_barrier\n\t"
    "v_mfma_f32_16x16x32_bf16 v[100:103], v[108:111], a[216:219], 0\n\t"
    "v_mfma_f32_16x16x32_bf16 v[100:103], v[176:179], a[220:223], v[100:103]\n\t"
    "v_mfma_f32_16x16x32_bf16 v[100:103], v[144:147], a[224:227], v[100:103]\n\t"
    "v_mfma_f32_16x16x32_bf16 v[100:103], v[128:131], a[228:231], v[100:103]\n\t"
    "v_cvt_pk_bf16_f32 v94, v96, v97\n\t"
    "v_cvt_pk_bf16_f32 v95, v98, v99\n\t"
    "v_mfma_f32_16x16x32_bf16 v[100:103], v[164:167], a[232:235], v[100:103]\n\t"
    "v_mfma_f32_16x16x32_bf16 v[100:103], v[160:163], a[236:239], v[100:103]\n\t"
    "v_mfma_f32_16x16x32_bf16 v[116:119], v[152:155], a[236:239], 0\n\t"
    "v_mfma_f32_16x16x32_bf16 v[116:119], v[148:151], a[232:235], v[116:119]\n\t"
    "v_mfma_f32_16x16x32_bf16 v[116:119], v[140:143], a[228:231], v[116:119]\n\t"
    "v_mfma_f32_16x16x32_bf16 v[116:119], v[136:139], a[224:227], v[116:119]\n\t"
    "v_cvt_pk_bf16_f32 v98, v100, v101\n\t"
    "v_cvt_pk_bf16_f32 v99, v102, v103\n\t"
    "v_mfma_f32_16x16x32_bf16 v[116:119], v[132:135], a[220:223], v[116:119]\n\t"
    "buffer_store_dwordx2 v[104:105], v8, s[52:55], s61 offen offset:0 sc1\n\t"
    "v_mfma_f32_16x16x32_bf16 v[116:119], v[112:115], a[216:219], v[116:119]\n\t"
    "v_mfma_f32_16x16x32_bf16 v[120:123], v[112:115], a[240:243], 0\n\t"
    "v_mfma_f32_16x16x32_bf16 v[120:123], v[132:135], a[244:247], v[120:123]\n\t"
    "v_mfma_f32_16x16x32_bf16 v[120:123], v[136:139], a[248:251], v[120:123]\n\t"
    "v_mfma_f32_16x16x32_bf16 v[120:123], v[140:143], a[252:255], v[120:123]\n\t"
    "v_cvt_pk_bf16_f32 v96, v116, v117\n\t"
    "v_cvt_pk_bf16_f32 v97, v118, v119\n\t"
    "v_mfma_f32_16x16x32_bf16 v[120:123], v[148:151], v[224:227], v[120:123]\n\t"
    "v_mfma_f32_16x16x32_bf16 v[120:123], v[152:155], v[228:231], v[120:123]\n\t"
    "v_mfma_f32_16x16x32_bf16 v[116:119], v[160:163], v[228:231], 0\n\t"
    "v_mfma_f32_16x16x32_bf16 v[116:119], v[164:167], v[224:227], v[116:119]\n\t"
    "v_mfma_f32_16x16x32_bf16 v[116:119], v[128:131], a[252:255], v[116:119]\n\t"
    "v_mfma_f32_16x16x32_bf16 v[116:119], v[144:147], a[248:251], v[116:119]\n\t"
    "v_cvt_pk_bf16_f32 v100, v120, v121\n\t"
    "v_cvt_pk_bf16_f32 v101, v122, v123\n\t"
    "v_mfma_f32_16x16x32_bf16 v[116:119], v[176:179], a[244:247], v[116:119]\n\t"
    "buffer_store_dwordx2 v[106:107], v8, s[52:55], s61 offen offset:384 sc1\n\t"
    "v_mfma_f32_16x16x32_bf16 v[116:119], v[108:111], a[240:243], v[116:119]\n\t"
    "v_mfma_f32_16x16x32_bf16 v[120:123], v[108:111], v[232:235], 0\n\t"
    "v_mfma_f32_16x16x32_bf16 v[120:123], v[176:179], v[236:239], v[120:123]\n\t"
    "ds_read_b128 v[156:159], v6 offset:0\n\t"
    "v_mfma_f32_16x16x32_bf16 v[120:123], v[144:147], v[240:243], v[120:123]\n\t"
    "ds_read_b128 v[168:171], v6 offset:1024\n\t"
    "v_mfma_f32_16x16x32_bf16 v[120:123], v[128:131], v[244:247], v[120:123]\n\t"
    "v_cvt_pk_bf16_f32 v102, v116, v117\n\t"
    "v_cvt_pk_bf16_f32 v103, v118, v119\n\t"
    "v_mfma_f32_16x16x32_bf16 v[120:123], v[164:167], v[248:251], v[120:123]\n\t"
    "v_mfma_f32_16x16x32_bf16 v[120:123], v[160:163], v[252:255], v[120:123]\n\t"
    "v_mfma_f32_16x16x32_bf16 v[108:111], v[152:155], v[252:255], 0\n\t"
    "v_mfma_f32_16x16x32_bf16 v[108:111], v[148:151], v[248:251], v[108:111]\n\t"
    "v_mfma_f32_16x16x32_bf16 v[108:111], v[140:143], v[244:247], v[108:111]\n\t"
    "v_mfma_f32_16x16x32_bf16 v[108:111], v[136:139], v[240:243], v[108:111]\n\t"
    "v_cvt_pk_bf16_f32 v106, v120, v121\n\t"
    "v_cvt_pk_bf16_f32 v107, v122, v123\n\t"
    "v_mfma_f32_16x16x32_bf16 v[108:111], v[132:135], v[236:239], v[108:111]\n\t"
    "buffer_store_dwordx2 v[180:181], v8, s[52:55], s61 offen offset:768 sc1\n\t"
    "v_mfma_f32_16x16x32_bf16 v[108:111], v[112:115], v[232:235], v[108:111]\n\t"
    "s_waitcnt lgkmcnt(0)\n\t"
    "v_mfma_f32_16x16x32_bf16 v[120:123], v[156:159], v[12:15], 0\n\t"
    "v_mfma_f32_16x16x32_bf16 v[120:123], v[168:171], v[60:63], v[120:123]\n\t"
    "v_mfma_f32_16x16x32_bf16 v[124:127], v[168:171], v[64:67], 0\n\t"
    "v_mfma_f32_16x16x32_bf16 v[124:127], v[156:159], v[16:19], v[124:127]\n\t"
    "v_cvt_pk_bf16_f32 v104, v108, v109\n\t"
    "v_cvt_pk_bf16_f32 v105, v110, v111\n\t"
    "v_mfma_f32_16x16x32_bf16 v[128:131], v[156:159], v[20:23], 0\n\t"
    "v_mfma_f32_16x16x32_bf16 v[128:131], v[168:171], v[68:71], v[128:131]\n\t"
    "v_mfma_f32_16x16x32_bf16 v[132:135], v[168:171], v[72:75], 0\n\t"
    "v_mfma_f32_16x16x32_bf16 v[132:135], v[156:159], v[24:27], v[132:135]\n\t"
    "v_cvt_pk_bf16_f32 v160, v120, v124\n\t"
    "v_cvt_pk_bf16_f32 v108, v121, v125\n\t"
    "v_mfma_f32_16x16x32_bf16 v[136:139], v[156:159], v[28:31], 0\n\t"
    "v_cvt_pk_bf16_f32 v116, v122, v126\n\t"
    "v_cvt_pk_bf16_f32 v112, v123, v127\n\t"
    "v_mfma_f32_16x16x32_bf16 v[136:139], v[168:171], v[76:79], v[136:139]\n\t"
    "v_mfma_f32_16x16x32_bf16 v[140:143], v[168:171], v[80:83], 0\n\t"
    "buffer_store_dwordx2 v[182:183], v8, s[52:55], s61 offen offset:1152 sc1\n\t"
    "v_mfma_f32_16x16x32_bf16 v[140:143], v[156:159], v[32:35], v[140:143]\n\t"
    "s_add_u32 s61, s61, 11403264\n\t"
    "v_cvt_pk_bf16_f32 v161, v128, v132\n\t"
    "v_mfma_f32_16x16x32_bf16 v[144:147], v[156:159], v[36:39], 0\n\t"
    "v_cvt_pk_bf16_f32 v109, v129, v133\n\t"
    "v_cvt_pk_bf16_f32 v117, v130, v134\n\t"
    "v_mfma_f32_16x16x32_bf16 v[144:147], v[168:171], v[84:87], v[144:147]\n\t"
    "ds_read_b128 v[120:123], v6 offset:2048\n\t"
    "v_cvt_pk_bf16_f32 v113, v131, v135\n\t"
    "v_mfma_f32_16x16x32_bf16 v[148:151], v[168:171], v[88:91], 0\n\t"
    "ds_read_b128 v[124:127], v6 offset:3072\n\t"
    "v_mfma_f32_16x16x32_bf16 v[148:151], v[156:159], v[40:43], v[148:151]\n\t"
    "v_cvt_pk_bf16_f32 v162, v136, v140\n\t"
    "v_cvt_pk_bf16_f32 v110, v137, v141\n\t"
    "v_mfma_f32_16x16x32_bf16 v[128:131], v[156:159], v[44:47], 0\n\t"
    "v_cvt_pk_bf16_f32 v118, v138, v142\n\t"
    "v_cvt_pk_bf16_f32 v114, v139, v143\n\t"
    "v_mfma_f32_16x16x32_bf16 v[128:131], v[168:171], v[92:95], v[128:131]\n\t"
    "v_mfma_f32_16x16x32_bf16 v[132:135], v[168:171], v[96:99], 0\n\t"
    "v_mfma_f32_16x16x32_bf16 v[132:135], v[156:159], v[48:51], v[132:135]\n\t"
    "v_cvt_pk_bf16_f32 v163, v144, v148\n\t"
    "v_cvt_pk_bf16_f32 v111, v145, v149\n\t"
    "v_mfma_f32_16x16x32_bf16 v[136:139], v[156:159], v[52:55], 0\n\t"
    "v_cvt_pk_bf16_f32 v119, v146, v150\n\t"
    "v_cvt_pk_bf16_f32 v115, v147, v151\n\t"
    "v_mfma_f32_16x16x32_bf16 v[136:139], v[168:171], v[100:103], v[136:139]\n\t"
    "v_mfma_f32_16x16x32_bf16 v[140:143], v[168:171], v[104:107], 0\n\t"
    "v_mfma_f32_16x16x32_bf16 v[140:143], v[156:159], v[56:59], v[140:143]\n\t"
    ".Lh2_%=:\n\t"
    "s_cbranch_execz .Lh6_%=\n\t"
    "v_cvt_pk_bf16_f32 v144, v128, v132\n\t"
    "s_waitcnt lgkmcnt(0)\n\t"
    "v_mfma_f32_16x16x32_bf16 v[148:151], v[120:123], v[12:15], 0\n\t"
    "buffer_store_dwordx4 v[160:163], v7, s[52:55], s61 offen offset:0 sc1\n\t"
    "v_mfma_f32_16x16x32_bf16 v[148:151], v[124:127], v[60:63], v[148:151]\n\t"
    "v_cvt_pk_bf16_f32 v146, v129, v133\n\t"
    "v_cvt_pk_bf16_f32 v152, v130, v134\n\t"
    "v_mfma_f32_16x16x32_bf16 v[156:159], v[124:127], v[64:67], 0\n\t"
    "v_cvt_pk_bf16_f32 v154, v131, v135\n\t"
    "v_mfma_f32_16x16x32_bf16 v[156:159], v[120:123], v[16:19], v[156:159]\n\t"
    "v_cvt_pk_bf16_f32 v145, v136, v140\n\t"
    "v_cvt_pk_bf16_f32 v147, v137, v141\n\t"
    "v_mfma_f32_16x16x32_bf16 v[160:163], v[120:123], v[20:23], 0\n\t"
    "v_cvt_pk_bf16_f32 v153, v138, v142\n\t"
    "v_cvt_pk_bf16_f32 v155, v139, v143\n\t"
    "v_mfma_f32_16x16x32_bf16 v[160:163], v[124:127], v[68:71], v[160:163]\n\t"
    "v_mfma_f32_16x16x32_bf16 v[128:131], v[124:127], v[72:75], 0\n\t"
    "v_mfma_f32_16x16x32_bf16 v[128:131], v[120:123], v[24:27], v[128:131]\n\t"
    "v_cvt_pk_bf16_f32 v136, v148, v156\n\t"
    "v_cvt_pk_bf16_f32 v132, v149, v157\n\t"
    "v_mfma_f32_16x16x32_bf16 v[168:171], v[120:123], v[28:31], 0\n\t"
    "v_cvt_pk_bf16_f32 v164, v150, v158\n\t"
    "v_cvt_pk_bf16_f32 v140, v151, v159\n\t"
    "v_mfma_f32_16x16x32_bf16 v[168:171], v[124:127], v[76:79], v[168:171]\n\t"
    "v_mfma_f32_16x16x32_bf16 v[172:175], v[124:127], v[80:83], 0\n\t"
    "buffer_store_dwordx4 v[108:111], v7, s[52:55], s61 offen offset:384 sc1\n\t"
    "v_mfma_f32_16x16x32_bf16 v[172:175], v[120:123], v[32:35], v[172:175]\n\t"
    "v_cvt_pk_bf16_f32 v137, v160, v128\n\t"
    "v_cvt_pk_bf16_f32 v133, v161, v129\n\t"
    "v_mfma_f32_16x16x32_bf16 v[148:151], v[120:123], v[36:39], 0\n\t"
    "v_cvt_pk_bf16_f32 v165, v162, v130\n\t"
    "v_cvt_pk_bf16_f32 v141, v163, v131\n\t"
    "v_mfma_f32_16x16x32_bf16 v[148:151], v[124:127], v[84:87], v[148:151]\n\t"
    "ds_read_b128 v[108:111], v6 offset:4096\n\t"
    "v_mfma_f32_16x16x32_bf16 v[176:179], v[124:127], v[88:91], 0\n\t"
    "ds_read_b128 v[156:159], v6 offset:5120\n\t"
    "v_mfma_f32_16x16x32_bf16 v[176:179], v[120:123], v[40:43], v[176:179]\n\t"
    "v_cvt_pk_bf16_f32 v138, v168, v172\n\t"
    "v_cvt_pk_bf16_f32 v134, v169, v173\n\t"
    "v_mfma_f32_16x16x32_bf16 v[128:131], v[120:123], v[44:47], 0\n\t"
    "v_cvt_pk_bf16_f32 v166, v170, v174\n\t"
    "v_cvt_pk_bf16_f32 v142, v171, v175\n\t"
    "v_mfma_f32_16x16x32_bf16 v[128:131], v[124:127], v[92:95], v[128:131]\n\t"
    "v_mfma_f32_16x16x32_bf16 v[160:163], v[124:127], v[96:99], 0\n\t"
    "v_mfma_f32_16x16x32_bf16 v[160:163], v[120:123], v[48:51], v[160:163]\n\t"
    "v_cvt_pk_bf16_f32 v139, v148, v176\n\t"
    "v_cvt_pk_bf16_f32 v135, v149, v177\n\t"
    "v_mfma_f32_16x16x32_bf16 v[168:171], v[120:123], v[52:55], 0\n\t"
    "v_cvt_pk_bf16_f32 v167, v150, v178\n\t"
    "v_cvt_pk_bf16_f32 v143, v151, v179\n\t"
    "v_mfma_f32_16x16x32_bf16 v[168:171], v[124:127], v[100:103], v[168:171]\n\t"
    "v_mfma_f32_16x16x32_bf16 v[172:175], v[124:127], v[104:107], 0\n\t"
    "buffer_store_dwordx4 v[116:119], v7, s[52:55], s61 offen offset:768 sc1\n\t"
    "v_mfma_f32_16x16x32_bf16 v[172:175], v[120:123], v[56:59], v[172:175]\n\t"
    "v_cvt_pk_bf16_f32 v148, v128, v160\n\t"
    "s_waitcnt lgkmcnt(0)\n\t"
    "v_mfma_f32_16x16x32_bf16 v[180:183], v[108:111], v[12:15], 0\n\t"
    "v_cvt_pk_bf16_f32 v150, v129, v161\n\t"
    "v_cvt_pk_bf16_f32 v176, v130, v162\n\t"
    "v_mfma_f32_16x16x32_bf16 v[180:183], v[156:159], v[60:63], v[180:183]\n\t"
    "v_cvt_pk_bf16_f32 v178, v131, v163\n\t"
    "v_mfma_f32_16x16x32_bf16 v[116:119], v[156:159], v[64:67], 0\n\t"
    "v_mfma_f32_16x16x32_bf16 v[116:119], v[108:111], v[16:19], v[116:119]\n\t"
    "v_cvt_pk_bf16_f32 v149, v168, v172\n\t"
    "v_cvt_pk_bf16_f32 v151, v169, v173\n\t"
    "v_mfma_f32_16x16x32_bf16 v[120:123], v[108:111], v[20:23], 0\n\t"
    "v_cvt_pk_bf16_f32 v177, v170, v174\n\t"
    "v_cvt_pk_bf16_f32 v179, v171, v175\n\t"
    "v_mfma_f32_16x16x32_bf16 v[120:123], v[156:159], v[68:71], v[120:123]\n\t"
    "v_mfma_f32_16x16x32_bf16 v[124:127], v[156:159], v[72:75], 0\n\t"
    "v_mfma_f32_16x16x32_bf16 v[124:127], v[108:111], v[24:27], v[124:127]\n\t"
    "v_cvt_pk_bf16_f32 v160, v180, v116\n\t"
    "v_cvt_pk_bf16_f32 v128, v181, v117\n\t"
    "v_mfma_f32_16x16x32_bf16 v[184:187], v[108:111], v[28:31], 0\n\t"
    "v_cvt_pk_bf16_f32 v172, v182, v118\n\t"
    "v_cvt_pk_bf16_f32 v168, v183, v119\n\t"
    "v_mfma_f32_16x16x32_bf16 v[184:187], v[156:159], v[76:79], v[184:187]\n\t"
    "v_mfma_f32_16x16x32_bf16 v[188:191], v[156:159], v[80:83], 0\n\t"
    "buffer_store_dwordx4 v[112:115], v7, s[52:55], s61 offen offset:1152 sc1\n\t"
    "v_mfma_f32_16x16x32_bf16 v[188:191], v[108:111], v[32:35], v[188:191]\n\t"
    "v_cvt_pk_bf16_f32 v161, v120, v124\n\t"
    "v_cvt_pk_bf16_f32 v129, v121, v125\n\t"
    "v_mfma_f32_16x16x32_bf16 v[116:119], v[108:111], v[36:39], 0\n\t"
    "s_barrier\n\t"
    "v_cvt_pk_bf16_f32 v173, v122, v126\n\t"
    "v_mfma_f32_16x16x32_bf16 v[116:119], v[156:159], v[84:87], v[116:119]\n\t"
    "ds_read_b128 v[112:115], v6 offset:6144\n\t"
    "v_cvt_pk_bf16_f32 v169, v123, v127\n\t"
    "v_mfma_f32_16x16x32_bf16 v[192:195], v[156:159], v[88:91], 0\n\t"
    "ds_read_b128 v[180:183], v6 offset:7168\n\t"
    "v_mfma_f32_16x16x32_bf16 v[192:195], v[108:111], v[40:43], v[192:195]\n\t"
    "v_cvt_pk_bf16_f32 v162, v184, v188\n\t"
    "v_cvt_pk_bf16_f32 v130, v185, v189\n\t"
    "v_mfma_f32_16x16x32_bf16 v[120:123], v[108:111], v[44:47], 0\n\t"
    "v_cvt_pk_bf16_f32 v174, v186, v190\n\t"
    "v_cvt_pk_bf16_f32 v170, v187, v191\n\t"
    "v_mfma_f32_16x16x32_bf16 v[120:123], v[156:159], v[92:95], v[120:123]\n\t"
    "v_mfma_f32_16x16x32_bf16 v[124:127], v[156:159], v[96:99], 0\n\t"
    "v_mfma_f32_16x16x32_bf16 v[124:127], v[108:111], v[48:51], v[124:127]\n\t"
    "v_cvt_pk_bf16_f32 v163, v116, v192\n\t"
    "v_cvt_pk_bf16_f32 v131, v117, v193\n\t"
    "v_mfma_f32_16x16x32_bf16 v[184:187], v[108:111], v[52:55], 0\n\t"
    "v_cvt_pk_bf16_f32 v175, v118, v194\n\t"
    "v_cvt_pk_bf16_f32 v171, v119, v195\n\t"
    "v_mfma_f32_16x16x32_bf16 v[184:187], v[156:159], v[100:103], v[184:187]\n\t"
    "v_mfma_f32_16x16x32_bf16 v[188:191], v[156:159], v[104:107], 0\n\t"
    "buffer_store_dwordx2 v[144:145], v8, s[52:55], s61 offen offset:0 sc1\n\t"
    "v_mfma_f32_16x16x32_bf16 v[188:191], v[108:111], v[56:59], v[188:191]\n\t"
    "v_cvt_pk_bf16_f32 v116, v120, v124\n\t"
    "s_waitcnt lgkmcnt(0)\n\t"
    "v_mfma_f32_16x16x32_bf16 v[196:199], v[112:115], v[12:15], 0\n\t"
    "v_cvt_pk_bf16_f32 v118, v121, v125\n\t"
    "v_cvt_pk_bf16_f32 v192, v122, v126\n\t"
    "v_mfma_f32_16x16x32_bf16 v[196:199], v[180:183], v[60:63], v[196:199]\n\t"
    "v_cvt_pk_bf16_f32 v194, v123, v127\n\t"
    "v_mfma_f32_16x16x32_bf16 v[108:111], v[180:183], v[64:67], 0\n\t"
    "v_mfma_f32_16x16x32_bf16 v[108:111], v[112:115], v[16:19], v[108:111]\n\t"
    "v_cvt_pk_bf16_f32 v117, v184, v188\n\t"
    "v_cvt_pk_bf16_f32 v119, v185, v189\n\t"
    "v_mfma_f32_16x16x32_bf16 v[12:15], v[112:115], v[20:23], 0\n\t"
    "v_cvt_pk_bf16_f32 v193, v186, v190\n\t"
    "v_cvt_pk_bf16_f32 v195, v187, v191\n\t"
    "v_mfma_f32_16x16x32_bf16 v[12:15], v[180:183], v[68:71], v[12:15]\n\t"
    "v_mfma_f32_16x16x32_bf16 v[16:19], v[180:183], v[72:75], 0\n\t"
    "v_mfma_f32_16x16x32_bf16 v[16:19], v[112:115], v[24:27], v[16:19]\n\t"
    "ds_read_b128 v[60:63], v0 offset:0\n\t"
    "v_cvt_pk_bf16_f32 v120, v196, v108\n\t"
    "v_mfma_f32_16x16x32_bf16 v[20:23], v[112:115], v[28:31], 0\n\t"
    "ds_read_b128 v[64:67], v1 offset:0\n\t"
    "v_cvt_pk_bf16_f32 v124, v197, v109\n\t"
    "v_mfma_f32_16x16x32_bf16 v[20:23], v[180:183], v[76:79], v[20:23]\n\t"
    "ds_read_b128 v[68:71], v2 offset:0\n\t"
    "v_cvt_pk_bf16_f32 v156, v198, v110\n\t"
    "v_mfma_f32_16x16x32_bf16 v[24:27], v[180:183], v[80:83], 0\n\t"
    "ds_read_b128 v[72:75], v3 offset:0\n\t"
    "v_cvt_pk_bf16_f32 v184, v199, v111\n\t"
    "v_mfma_f32_16x16x32_bf16 v[24:27], v[112:115], v[32:35], v[24:27]\n\t"
    "ds_read_b128 v[188:191], v4 offset:0\n\t"
    "v_cvt_pk_bf16_f32 v121, v12, v16\n\t"
    "v_mfma_f32_16x16x32_bf16 v[28:31], v[112:115], v[36:39], 0\n\t"
    "ds_read_b128 v[76:79], v5 offset:0\n\t"
    "v_cvt_pk_bf16_f32 v125, v13, v17\n\t"
    "v_mfma_f32_16x16x32_bf16 v[28:31], v[180:183], v[84:87], v[28:31]\n\t"
    "buffer_store_dwordx2 v[146:147], v8, s[52:55], s61 offen offset:384 sc1\n\t"
    "v_mfma_f32_16x16x32_bf16 v[32:35], v[180:183], v[88:91], 0\n\t"
    "ds_read_b128 v[80:83], v5 offset:6144\n\t"
    "v_cvt_pk_bf16_f32 v157, v14, v18\n\t"
    "v_mfma_f32_16x16x32_bf16 v[32:35], v[112:115], v[40:43], v[32:35]\n\t"
    "ds_read_b128 v[108:111], v4 offset:6144\n\t"
    "v_cvt_pk_bf16_f32 v185, v15, v19\n\t"
    "v_mfma_f32_16x16x32_bf16 v[36:39], v[112:115], v[44:47], 0\n\t"
    "ds_read_b128 v[84:87], v3 offset:6144\n\t"
    "v_cvt_pk_bf16_f32 v122, v20, v24\n\t"
    "v_mfma_f32_16x16x32_bf16 v[36:39], v[180:183], v[92:95], v[36:39]\n\t"
    "ds_read_b128 v[88:91], v2 offset:6144\n\t"
    "v_cvt_pk_bf16_f32 v126, v21, v25\n\t"
    "v_mfma_f32_16x16x32_bf16 v[12:15], v[180:183], v[96:99], 0\n\t"
    "ds_read_b128 v[144:147], v1 offset:6144\n\t"
    "v_cvt_pk_bf16_f32 v158, v22, v26\n\t"
    "v_mfma_f32_16x16x32_bf16 v[12:15], v[112:115], v[48:51], v[12:15]\n\t"
    "ds_read_b128 v[196:199], v0 offset:6144\n\t"
    "v_cvt_pk_bf16_f32 v186, v23, v27\n\t"
    "v_mfma_f32_16x16x32_bf16 v[16:19], v[112:115], v[52:55], 0\n\t"
    "s_sub_u32 s60, s60, 1\n\t"
    "s_cmp_lg_u32 s60, 0\n\t"
    "v_mfma_f32_16x16x32_bf16 v[16:19], v[180:183], v[100:103], v[16:19]\n\t"
    "v_cvt_pk_bf16_f32 v123, v28, v32\n\t"
    "v_cvt_pk_bf16_f32 v127, v29, v33\n\t"
    "v_mfma_f32_16x16x32_bf16 v[20:23], v[180:183], v[104:107], 0\n\t"
    "buffer_store_dwordx2 v[152:153], v8, s[52:55], s61 offen offset:768 sc1\n\t"
    "v_mfma_f32_16x16x32_bf16 v[20:23], v[112:115], v[56:59], v[20:23]\n\t"
    "s_cbranch_scc1 .Lmin_%=\n\t"
    "s_nop 7\n\t"
    "s_nop 1\n\t"
    "v_cvt_pk_bf16_f32 v159, v30, v34\n\t"
    "v_cvt_pk_bf16_f32 v187, v31, v35\n\t"
    "v_cvt_pk_bf16_f32 v104, v36, v12\n\t"
    "v_cvt_pk_bf16_f32 v106, v37, v13\n\t"
    "v_cvt_pk_bf16_f32 v180, v38, v14\n\t"
    "v_cvt_pk_bf16_f32 v182, v39, v15\n\t"
    "v_cvt_pk_bf16_f32 v105, v16, v20\n\t"
    "v_cvt_pk_bf16_f32 v107, v17, v21\n\t"
    "v_cvt_pk_bf16_f32 v181, v18, v22\n\t"
    "v_cvt_pk_bf16_f32 v183, v19, v23\n\t"
    "buffer_store_dwordx2 v[154:155], v8, s[52:55], s61 offen offset:1152 sc1\n\t"
    "s_add_u32 s61, s61, 393216\n\t"
    "buffer_store_dwordx4 v[136:139], v7, s[52:55], s61 offen offset:0 sc1\n\t"
    "buffer_store_dwordx4 v[132:135], v7, s[52:55], s61 offen offset:384 sc1\n\t"
    "buffer_store_dwordx4 v[164:167], v7, s[52:55], s61 offen offset:768 sc1\n\t"
    "buffer_store_dwordx4 v[140:143], v7, s[52:55], s61 offen offset:1152 sc1\n\t"
    "buffer_store_dwordx2 v[148:149], v8, s[52:55], s61 offen offset:0 sc1\n\t"
    "buffer_store_dwordx2 v[150:151], v8, s[52:55], s61 offen offset:384 sc1\n\t"
    "buffer_store_dwordx2 v[176:177], v8, s[52:55], s61 offen offset:768 sc1\n\t"
    "buffer_store_dwordx2 v[178:179], v8, s[52:55], s61 offen offset:1152 sc1\n\t"
    "s_add_u32 s61, s61, 393216\n\t"
    "buffer_store_dwordx4 v[160:163], v7, s[52:55], s61 offen offset:0 sc1\n\t"
    "buffer_store_dwordx4 v[128:131], v7, s[52:55], s61 offen offset:384 sc1\n\t"
    "buffer_store_dwordx4 v[172:175], v7, s[52:55], s61 offen offset:768 sc1\n\t"
    "buffer_store_dwordx4 v[168:171], v7, s[52:55], s61 offen offset:1152 sc1\n\t"
    "buffer_store_dwordx2 v[116:117], v8, s[52:55], s61 offen offset:0 sc1\n\t"
    "buffer_store_dwordx2 v[118:119], v8, s[52:55], s61 offen offset:384 sc1\n\t"
    "buffer_store_dwordx2 v[192:193], v8, s[52:55], s61 offen offset:768 sc1\n\t"
    "buffer_store_dwordx2 v[194:195], v8, s[52:55], s61 offen offset:1152 sc1\n\t"
    "s_add_u32 s61, s61, 393216\n\t"
    "buffer_store_dwordx4 v[120:123], v7, s[52:55], s61 offen offset:0 sc1\n\t"
    "buffer_store_dwordx4 v[124:127], v7, s[52:55], s61 offen offset:384 sc1\n\t"
    "buffer_store_dwordx4 v[156:159], v7, s[52:55], s61 offen offset:768 sc1\n\t"
    "buffer_store_dwordx4 v[184:187], v7, s[52:55], s61 offen offset:1152 sc1\n\t"
    "buffer_store_dwordx2 v[104:105], v8, s[52:55], s61 offen offset:0 sc1\n\t"
    "buffer_store_dwordx2 v[106:107], v8, s[52:55], s61 offen offset:384 sc1\n\t"
    "buffer_store_dwordx2 v[180:181], v8, s[52:55], s61 offen offset:768 sc1\n\t"
    "buffer_store_dwordx2 v[182:183], v8, s[52:55], s61 offen offset:1152 sc1\n\t"
    "s_add_u32 s61, s61, 11403264\n\t"
    "s_endpgm\n\t"
    ".p2align 12\n\t"
    ".Lroln_%=:\n\t"
    "v_lshrrev_b32_e32 v20, 3, v31\n\t"
    "v_and_b32_e32 v21, 6, v20\n\t"
    "v_and_b32_e32 v28, 7, v31\n\t"
    "v_xor_b32_e32 v21, v21, v28\n\t"
    "v_lshlrev_b32_e32 v20, 7, v20\n\t"
    "v_lshl_or_b32 v7, v21, 4, v20\n\t"
    "s_mov_b32 s54, 0x6000000\n\t"
    "s_mov_b32 s55, 0x20000\n\t"
    "s_waitcnt lgkmcnt(0)\n\t"
    "s_mov_b32 s52, s36\n\t"
    "s_and_b32 s53, s37, 0xffff\n\t"
    "s_barrier\n\t"
    "s_add_u32 m0, %[rbeg], 0\n\t"
    "s_add_u32 s61, %[wb], 0\n\t"
    "buffer_load_dwordx4 v7, s[52:55], s61 offen offset:0 nt lds\n\t"
    "buffer_load_dwordx4 v7, s[52:55], s61 offen offset:1024 nt lds\n\t"
    "buffer_load_dwordx4 v7, s[52:55], s61 offen offset:2048 nt lds\n\t"
    "s_add_u32 m0, %[rbeg], 3072\n\t"
    "s_add_u32 s61, %[wb], 196608\n\t"
    "buffer_load_dwordx4 v7, s[52:55], s61 offen offset:0 nt lds\n\t"
    "buffer_load_dwordx4 v7, s[52:55], s61 offen offset:1024 nt lds\n\t"
    "buffer_load_dwordx4 v7, s[52:55], s61 offen offset:2048 nt lds\n\t"
    "v_lshlrev_b32_e32 v30, 4, v31\n\t"
    "v_and_b32_e32 v28, 7, v31\n\t"
    "v_bfe_u32 v27, v31, 3, 1\n\t"
    "v_lshrrev_b32_e32 v29, 4, v31\n\t"
    "v_add_u32_e32 v6, %[pl], v30\n\t"
    "v_mul_u32_u24_e32 v27, 0xc00, v27\n\t"
    "v_add_u32_e32 v27, %[rbeg], v27\n\t"
    "v_lshlrev_b32_e32 v26, 7, v31\n\t"
    "v_mad_u32_u24 v20, v28, 3, 0\n\t"
    "v_and_b32_e32 v21, 6, v20\n\t"
    "v_xor_b32_e32 v21, v21, v29\n\t"
    "v_lshl_add_u32 v20, v20, 7, v27\n\t"
    "v_lshl_add_u32 v0, v21, 4, v20\n\t"
    "v_xor_b32_e32 v1, 64, v0\n\t"
    "v_mad_u32_u24 v20, v28, 3, 1\n\t"
    "v_and_b32_e32 v21, 6, v20\n\t"
    "v_xor_b32_e32 v21, v21, v29\n\t"
    "v_lshl_add_u32 v20, v20, 7, v27\n\t"
    "v_lshl_add_u32 v2, v21, 4, v20\n\t"
    "v_xor_b32_e32 v3, 64, v2\n\t"
    "v_mad_u32_u24 v20, v28, 3, 2\n\t"
    "v_and_b32_e32 v21, 6, v20\n\t"
    "v_xor_b32_e32 v21, v21, v29\n\t"
    "v_lshl_add_u32 v20, v20, 7, v27\n\t"
    "v_lshl_add_u32 v4, v21, 4, v20\n\t"
    "v_xor_b32_e32 v5, 64, v4\n\t"
    "s_mov_b32 s56, s42\n\t"
    "s_and_b32 s57, s43, 0xffff\n\t"
    "s_mov_b32 s58, 0x40000\n\t"
    "s_mov_b32 s59, 0x20000\n\t"
    "v_add_u32_e32 v8, 0x9000, v30\n\t"
    "v_add_u32_e32 v9, 0x1b000, v30\n\t"
    "s_mov_b32 s68, 0x3dd53b95\n\t"
    "s_add_u32 m0, %[rbeg], 6144\n\t"
    "s_add_u32 s61, %[wb], 393216\n\t"
    "buffer_load_dwordx4 v7, s[52:55], s61 offen offset:0 nt lds\n\t"
    "buffer_load_dwordx4 v7, s[52:55], s61 offen offset:1024 nt lds\n\t"
    "buffer_load_dwordx4 v7, s[52:55], s61 offen offset:2048 nt lds\n\t"
    "s_mov_b64 exec, 0\n\t"
    "s_cmp_eq_u32 %[pair], 0\n\t"
    "s_cbranch_scc1 .Lh4_%=\n\t"
    "s_branch .Lh1_%=\n\t"
    ".LrM_%=:\n\t"
    "s_mov_b64 exec, -1\n\t"
    "s_sleep 6\n\t"
    "s_add_u32 m0, %[rbeg], 9216\n\t"
    "s_add_u32 s61, %[wb], 589824\n\t"
    "buffer_load_dwordx4 v7, s[52:55], s61 offen offset:0 nt lds\n\t"
    "buffer_load_dwordx4 v7, s[52:55], s61 offen offset:1024 nt lds\n\t"
    "buffer_load_dwordx4 v7, s[52:55], s61 offen offset:2048 nt lds\n\t"
    "s_add_u32 m0, %[rbeg], 12288\n\t"
    "s_add_u32 s61, %[wb], 786432\n\t"
    "buffer_load_dwordx4 v7, s[52:55], s61 offen offset:0 nt lds\n\t"
    "buffer_load_dwordx4 v7, s[52:55], s61 offen offset:1024 nt lds\n\t"
    "buffer_load_dwordx4 v7, s[52:55], s61 offen offset:2048 nt lds\n\t"
    "s_add_u32 m0, %[rbeg], 15360\n\t"
    "s_add_u32 s61, %[wb], 983040\n\t"
    "buffer_load_dwordx4 v7, s[52:55], s61 offen offset:0 nt lds\n\t"
    "buffer_load_dwordx4 v7, s[52:55], s61 offen offset:1024 nt lds\n\t"
    "buffer_load_dwordx4 v7, s[52:55], s61 offen offset:2048 nt lds\n\t"
    "s_add_u32 m0, %[rbeg], 18432\n\t"
    "s_add_u32 s61, %[wb], 1179648\n\t"
    "buffer_load_dwordx4 v7, s[52:55], s61 offen offset:0 nt lds\n\t"
    "buffer_load_dwordx4 v7, s[52:55], s61 offen offset:1024 nt lds\n\t"
    "buffer_load_dwordx4 v7, s[52:55], s61 offen offset:2048 nt lds\n\t"
    "s_add_u32 m0, %[rbeg], 21504\n\t"
    "s_add_u32 s61, %[wb], 1376256\n\t"
    "buffer_load_dwordx4 v7, s[52:55], s61 offen offset:0 nt lds\n\t"
    "buffer_load_dwordx4 v7, s[52:55], s61 offen offset:1024 nt lds\n\t"
    "buffer_load_dwordx4 v7, s[52:55], s61 offen offset:2048 nt lds\n\t"
    "s_waitcnt vmcnt(0)\n\t"
    "s_barrier\n\t"
    "ds_read_b128 v[188:191], v2 offset:6144\n\t"
    "ds_read_b128 v[192:195], v1 offset:6144\n\t"
    "ds_read_b128 v[196:199], v0 offset:6144\n\t"
    "ds_read_b128 v[172:175], v0 offset:12288\n\t"
    "ds_read_b128 v[168:171], v1 offset:12288\n\t"
    "ds_read_b128 v[164:167], v2 offset:12288\n\t"
    "ds_read_b128 v[140:143], v2 offset:18432\n\t"
    "ds_read_b128 v[144:147], v1 offset:18432\n\t"
    "ds_read_b128 v[148:151], v0 offset:18432\n\t"
    "ds_read_b128 v[184:187], v3 offset:6144\n\t"
    "ds_read_b128 v[180:183], v4 offset:6144\n\t"
    "ds_read_b128 v[176:179], v5 offset:6144\n\t"
    "ds_read_b128 v[208:211], v3 offset:0\n\t"
    "ds_read_b128 v[204:207], v4 offset:0\n\t"
    "ds_read_b128 v[200:203], v5 offset:0\n\t"
    "ds_read_b128 v[160:163], v3 offset:12288\n\t"
    "ds_read_b128 v[156:159], v4 offset:12288\n\t"
    "ds_read_b128 v[152:155], v5 offset:12288\n\t"
    "ds_read_b128 v[128:131], v5 offset:18432\n\t"
    "ds_read_b128 v[132:135], v4 offset:18432\n\t"
    "ds_read_b128 v[136:139], v3 offset:18432\n\t"
    "ds_read_b128 v[220:223], v0 offset:0\n\t"
    "ds_read_b128 v[216:219], v1 offset:0\n\t"
    "ds_read_b128 v[212:215], v2 offset:0\n\t"
    "ds_read_b128 v[36:39], v3 offset:0\n\t"
    "ds_read_b128 v[40:43], v4 offset:0\n\t"
    "ds_read_b128 v[44:47], v5 offset:0\n\t"
    "s_add_i32 s63, %[rbeg], 24576\n\t"
    "s_add_i32 s62, %[rbeg], 49152\n\t"
    "s_barrier\n\t"
    "ds_read_b128 a[0:3], v8 offset:0\n\t"
    "ds_read_b128 a[8:11], v8 offset:1024\n\t"
    "ds_read_b128 a[16:19], v8 offset:2048\n\t"
    "ds_read_b128 a[24:27], v8 offset:3072\n\t"
    "ds_read_b128 a[32:35], v8 offset:4096\n\t"
    "ds_read_b128 a[40:43], v8 offset:5120\n\t"
    "ds_read_b128 a[44:47], v9 offset:5120\n\t"
    "ds_read_b128 a[36:39], v9 offset:4096\n\t"
    "ds_read_b128 a[28:31], v9 offset:3072\n\t"
    "ds_read_b128 a[20:23], v9 offset:2048\n\t"
    "ds_read_b128 a[12:15], v9 offset:1024\n\t"
    "s_waitcnt lgkmcnt(10)\n\t"
    "v_mfma_f32_16x16x32_bf16 v[72:75], a[0:3], v[220:223], 0\n\t"
    "ds_read_b128 a[4:7], v9 offset:0\n\t"
    "s_waitcnt lgkmcnt(10)\n\t"
    "v_mfma_f32_16x16x32_bf16 v[72:75], a[8:11], v[216:219], v[72:75]\n\t"
    "s_barrier\n\t"
    "ds_read_b128 a[48:51], v8 offset:6144\n\t"
    "s_waitcnt lgkmcnt(10)\n\t"
    "v_mfma_f32_16x16x32_bf16 v[72:75], a[16:19], v[212:215], v[72:75]\n\t"
    "ds_read_b128 a[56:59], v8 offset:7168\n\t"
    "s_waitcnt lgkmcnt(10)\n\t"
    "v_mfma_f32_16x16x32_bf16 v[72:75], a[24:27], v[36:39], v[72:75]\n\t"
    "ds_read_b128 a[64:67], v8 offset:8192\n\t"
    "s_waitcnt lgkmcnt(4)\n\t"
    "v_mfma_f32_16x16x32_bf16 v[72:75], a[32:35], v[40:43], v[72:75]\n\t"
    "ds_read_b128 a[72:75], v8 offset:9216\n\t"
    "v_mfma_f32_16x16x32_bf16 v[72:75], a[40:43], v[44:47], v[72:75]\n\t"
    "ds_read_b128 a[80:83], v8 offset:10240\n\t"
    "v_mfma_f32_16x16x32_bf16 v[52:55], a[44:47], v[44:47], 0\n\t"
    "ds_read_b128 a[88:91], v8 offset:11264\n\t"
    "v_mfma_f32_16x16x32_bf16 v[52:55], a[36:39], v[40:43], v[52:55]\n\t"
    "ds_read_b128 a[92:95], v9 offset:11264\n\t"
    "v_mfma_f32_16x16x32_bf16 v[52:55], a[28:31], v[36:39], v[52:55]\n\t"
    "ds_read_b128 a[84:87], v9 offset:10240\n\t"
    "v_mfma_f32_16x16x32_bf16 v[52:55], a[20:23], v[212:215], v[52:55]\n\t"
    "ds_read_b128 a[76:79], v9 offset:9216\n\t"
    "v_mfma_f32_16x16x32_bf16 v[52:55], a[12:15], v[216:219], v[52:55]\n\t"
    "v_cvt_pk_bf16_f32 v12, v72, v73\n\t"
    "v_cvt_pk_bf16_f32 v13, v74, v75\n\t"
    "ds_read_b128 a[68:71], v9 offset:8192\n\t"
    "s_waitcnt lgkmcnt(4)\n\t"
    "v_mfma_f32_16x16x32_bf16 v[52:55], a[4:7], v[220:223], v[52:55]\n\t"
    "ds_read_b128 a[60:63], v9 offset:7168\n\t"
    "v_mfma_f32_16x16x32_bf16 v[48:51], a[48:51], v[220:223], 0\n\t"
    "ds_read_b128 a[52:55], v9 offset:6144\n\t"
    "v_mfma_f32_16x16x32_bf16 v[48:51], a[56:59], v[216:219], v[48:51]\n\t"
    "s_barrier\n\t"
    "ds_read_b128 a[96:99], v8 offset:12288\n\t"
    "v_mfma_f32_16x16x32_bf16 v[48:51], a[64:67], v[212:215], v[48:51]\n\t"
    "ds_read_b128 a[104:107], v8 offset:13312\n\t"
    "v_mfma_f32_16x16x32_bf16 v[48:51], a[72:75], v[36:39], v[48:51]\n\t"
    "v_cvt_pk_bf16_f32 v14, v52, v53\n\t"
    "ds_read_b128 a[112:115], v8 offset:14336\n\t"
    "v_mfma_f32_16x16x32_bf16 v[48:51], a[80:83], v[40:43], v[48:51]\n\t"
    "v_cvt_pk_bf16_f32 v15, v54, v55\n\t"
    "ds_read_b128 a[120:123], v8 offset:15360\n\t"
    "v_mfma_f32_16x16x32_bf16 v[48:51], a[88:91], v[44:47], v[48:51]\n\t"
    "ds_read_b128 a[128:131], v8 offset:16384\n\t"
    "s_waitcnt lgkmcnt(4)\n\t"
    "v_mfma_f32_16x16x32_bf16 v[56:59], a[92:95], v[44:47], 0\n\t"
    "ds_read_b128 a[136:139], v8 offset:17408\n\t"
    "v_mfma_f32_16x16x32_bf16 v[56:59], a[84:87], v[40:43], v[56:59]\n\t"
    "ds_read_b128 a[140:143], v9 offset:17408\n\t"
    "v_mfma_f32_16x16x32_bf16 v[56:59], a[76:79], v[36:39], v[56:59]\n\t"
    "ds_read_b128 a[132:135], v9 offset:16384\n\t"
    "v_mfma_f32_16x16x32_bf16 v[56:59], a[68:71], v[212:215], v[56:59]\n\t"
    "v_cvt_pk_bf16_f32 v52, v48, v49\n\t"
    "ds_read_b128 a[124:127], v9 offset:15360\n\t"
    "v_mfma_f32_16x16x32_bf16 v[56:59], a[60:63], v[216:219], v[56:59]\n\t"
    "v_cvt_pk_bf16_f32 v53, v50, v51\n\t"
    "ds_read_b128 a[116:119], v9 offset:14336\n\t"
    "v_mfma_f32_16x16x32_bf16 v[56:59], a[52:55], v[220:223], v[56:59]\n\t"
    "ds_read_b128 a[108:111], v9 offset:13312\n\t"
    "v_mfma_f32_16x16x32_bf16 v[60:63], a[96:99], v[220:223], 0\n\t"
    "ds_read_b128 a[100:103], v9 offset:12288\n\t"
    "s_waitcnt lgkmcnt(4)\n\t"
    "v_mfma_f32_16x16x32_bf16 v[60:63], a[104:107], v[216:219], v[60:63]\n\t"
    "ds_read_b128 a[144:147], v8 offset:18432\n\t"
    "v_mfma_f32_16x16x32_bf16 v[60:63], a[112:115], v[212:215], v[60:63]\n\t"
    "ds_read_b128 a[152:155], v8 offset:19456\n\t"
    "v_mfma_f32_16x16x32_bf16 v[60:63], a[120:123], v[36:39], v[60:63]\n\t"
    "v_cvt_pk_bf16_f32 v54, v56, v57\n\t"
    "v_cvt_pk_bf16_f32 v55, v58, v59\n\t"
    "ds_read_b128 a[160:163], v8 offset:20480\n\t"
    "v_mfma_f32_16x16x32_bf16 v[60:63], a[128:131], v[40:43], v[60:63]\n\t"
    "ds_read_b128 a[168:171], v8 offset:21504\n\t"
    "v_mfma_f32_16x16x32_bf16 v[60:63], a[136:139], v[44:47], v[60:63]\n\t"
    "ds_read_b128 a[176:179], v8 offset:22528\n\t"
    "v_mfma_f32_16x16x32_bf16 v[48:51], a[140:143], v[44:47], 0\n\t"
    "ds_read_b128 a[184:187], v8 offset:23552\n\t"
    "v_mfma_f32_16x16x32_bf16 v[48:51], a[132:135], v[40:43], v[48:51]\n\t"
    "s_waitcnt lgkmcnt(3)\n\t"
    "v_mfma_f32_16x16x32_bf16 v[48:51], a[124:127], v[36:39], v[48:51]\n\t"
    "v_mfma_f32_16x16x32_bf16 v[48:51], a[116:119], v[212:215], v[48:51]\n\t"
    "v_cvt_pk_bf16_f32 v56, v60, v61\n\t"
    "v_cvt_pk_bf16_f32 v57, v62, v63\n\t"
    "v_mfma_f32_16x16x32_bf16 v[48:51], a[108:111], v[216:219], v[48:51]\n\t"
    "v_mfma_f32_16x16x32_bf16 v[48:51], a[100:103], v[220:223], v[48:51]\n\t"
    "v_mfma_f32_16x16x32_bf16 v[60:63], a[144:147], v[220:223], 0\n\t"
    "v_mfma_f32_16x16x32_bf16 v[60:63], a[152:155], v[216:219], v[60:63]\n\t"
    "v_mfma_f32_16x16x32_bf16 v[60:63], a[160:163], v[212:215], v[60:63]\n\t"
    "s_waitcnt lgkmcnt(0)\n\t"
    "v_mfma_f32_16x16x32_bf16 v[60:63], a[168:171], v[36:39], v[60:63]\n\t"
    "v_cvt_pk_bf16_f32 v58, v48, v49\n\t"
    "v_mfma_f32_16x16x32_bf16 v[60:63], a[176:179], v[40:43], v[60:63]\n\t"
    "v_cvt_pk_bf16_f32 v59, v50, v51\n\t"
    "v_mfma_f32_16x16x32_bf16 v[60:63], a[184:187], v[44:47], v[60:63]\n\t"
    "v_mfma_f32_16x16x32_bf16 v[64:67], v[220:223], v[12:15], 0\n\t"
    "v_mfma_f32_16x16x32_bf16 v[64:67], v[216:219], v[52:55], v[64:67]\n\t"
    "ds_read_b128 a[188:191], v9 offset:23552\n\t"
    "v_mfma_f32_16x16x32_bf16 v[64:67], v[212:215], v[56:59], v[64:67]\n\t"
    "ds_read_b128 a[180:183], v9 offset:22528\n\t"
    "v_mfma_f32_16x16x32_bf16 v[48:51], v[188:191], v[56:59], 0\n\t"
    "ds_read_b128 a[172:175], v9 offset:21504\n\t"
    "v_mfma_f32_16x16x32_bf16 v[48:51], v[192:195], v[52:55], v[48:51]\n\t"
    "ds_read_b128 a[164:167], v9 offset:20480\n\t"
    "v_mfma_f32_16x16x32_bf16 v[48:51], v[196:199], v[12:15], v[48:51]\n\t"
    "ds_read_b128 a[156:159], v9 offset:19456\n\t"
    "v_mfma_f32_16x16x32_bf16 v[68:71], v[172:175], v[12:15], 0\n\t"
    "ds_read_b128 a[148:151], v9 offset:18432\n\t"
    "v_mfma_f32_16x16x32_bf16 v[68:71], v[168:171], v[52:55], v[68:71]\n\t"
    "s_barrier\n\t"
    "ds_read_b128 a[192:195], v8 offset:24576\n\t"
    "v_mfma_f32_16x16x32_bf16 v[68:71], v[164:167], v[56:59], v[68:71]\n\t"
    "ds_read_b128 a[200:203], v8 offset:25600\n\t"
    "v_mfma_f32_16x16x32_bf16 v[72:75], v[140:143], v[56:59], 0\n\t"
    "ds_read_b128 a[208:211], v8 offset:26624\n\t"
    "v_mfma_f32_16x16x32_bf16 v[72:75], v[144:147], v[52:55], v[72:75]\n\t"
    "ds_read_b128 a[216:219], v8 offset:27648\n\t"
    "v_mfma_f32_16x16x32_bf16 v[72:75], v[148:151], v[12:15], v[72:75]\n\t"
    "ds_read_b128 a[224:227], v8 offset:28672\n\t"
    "s_waitcnt lgkmcnt(4)\n\t"
    "v_mfma_f32_16x16x32_bf16 v[56:59], a[188:191], v[44:47], 0\n\t"
    "ds_read_b128 a[232:235], v8 offset:29696\n\t"
    "v_mfma_f32_16x16x32_bf16 v[56:59], a[180:183], v[40:43], v[56:59]\n\t"
    "ds_read_b128 a[236:239], v9 offset:29696\n\t"
    "v_mfma_f32_16x16x32_bf16 v[56:59], a[172:175], v[36:39], v[56:59]\n\t"
    "ds_read_b128 a[228:231], v9 offset:28672\n\t"
    "v_mfma_f32_16x16x32_bf16 v[56:59], a[164:167], v[212:215], v[56:59]\n\t"
    "ds_read_b128 a[220:223], v9 offset:27648\n\t"
    "v_mfma_f32_16x16x32_bf16 v[56:59], a[156:159], v[216:219], v[56:59]\n\t"
    "ds_read_b128 a[212:215], v9 offset:26624\n\t"
    "v_mfma_f32_16x16x32_bf16 v[56:59], a[148:151], v[220:223], v[56:59]\n\t"
    "ds_read_b128 a[204:207], v9 offset:25600\n\t"
    "v_mfma_f32_16x16x32_bf16 v[12:15], a[192:195], v[220:223], 0\n\t"
    "ds_read_b128 a[196:199], v9 offset:24576\n\t"
    "s_waitcnt lgkmcnt(4)\n\t"
    "v_mfma_f32_16x16x32_bf16 v[12:15], a[200:203], v[216:219], v[12:15]\n\t"
    "v_cvt_pk_bf16_f32 v52, v60, v61\n\t"
    "ds_read_b128 a[240:243], v8 offset:30720\n\t"
    "v_mfma_f32_16x16x32_bf16 v[12:15], a[208:211], v[212:215], v[12:15]\n\t"
    "ds_read_b128 a[248:251], v8 offset:31744\n\t"
    "v_mfma_f32_16x16x32_bf16 v[12:15], a[216:219], v[36:39], v[12:15]\n\t"
    "v_cvt_pk_bf16_f32 v54, v56, v57\n\t"
    "ds_read_b128 v[224:227], v8 offset:32768\n\t"
    "v_mfma_f32_16x16x32_bf16 v[12:15], a[224:227], v[40:43], v[12:15]\n\t"
    "v_cvt_pk_bf16_f32 v55, v58, v59\n\t"
    "ds_read_b128 v[232:235], v8 offset:33792\n\t"
    "v_mfma_f32_16x16x32_bf16 v[12:15], a[232:235], v[44:47], v[12:15]\n\t"
    "v_cvt_pk_bf16_f32 v53, v62, v63\n\t"
    "ds_read_b128 v[240:243], v8 offset:34816\n\t"
    "v_mfma_f32_16x16x32_bf16 v[76:79], a[236:239], v[44:47], 0\n\t"
    "ds_read_b128 v[248:251], v8 offset:35840\n\t"
    "v_mfma_f32_16x16x32_bf16 v[76:79], a[228:231], v[40:43], v[76:79]\n\t"
    "ds_read_b128 v[252:255], v9 offset:35840\n\t"
    "s_waitcnt lgkmcnt(4)\n\t"
    "v_mfma_f32_16x16x32_bf16 v[76:79], a[220:223], v[36:39], v[76:79]\n\t"
    "ds_read_b128 v[244:247], v9 offset:34816\n\t"
    "v_mfma_f32_16x16x32_bf16 v[76:79], a[212:215], v[212:215], v[76:79]\n\t"
    "v_cvt_pk_bf16_f32 v56, v12, v13\n\t"
    "ds_read_b128 v[236:239], v9 offset:33792\n\t"
    "v_mfma_f32_16x16x32_bf16 v[76:79], a[204:207], v[216:219], v[76:79]\n\t"
    "v_cvt_pk_bf16_f32 v57, v14, v15\n\t"
    "ds_read_b128 v[228:231], v9 offset:32768\n\t"
    "v_mfma_f32_16x16x32_bf16 v[76:79], a[196:199], v[220:223], v[76:79]\n\t"
    "ds_read_b128 a[252:255], v9 offset:31744\n\t"
    "v_mfma_f32_16x16x32_bf16 v[80:83], a[240:243], v[220:223], 0\n\t"
    "ds_read_b128 a[244:247], v9 offset:30720\n\t"
    "v_mfma_f32_16x16x32_bf16 v[80:83], a[248:251], v[216:219], v[80:83]\n\t"
    "v_mfma_f32_16x16x32_bf16 v[80:83], v[224:227], v[212:215], v[80:83]\n\t"
    "s_waitcnt lgkmcnt(2)\n\t"
    "v_mfma_f32_16x16x32_bf16 v[80:83], v[232:235], v[36:39], v[80:83]\n\t"
    "v_cvt_pk_bf16_f32 v58, v76, v77\n\t"
    "v_cvt_pk_bf16_f32 v59, v78, v79\n\t"
    "v_mfma_f32_16x16x32_bf16 v[80:83], v[240:243], v[40:43], v[80:83]\n\t"
    "v_mfma_f32_16x16x32_bf16 v[80:83], v[248:251], v[44:47], v[80:83]\n\t"
    "v_mfma_f32_16x16x32_bf16 v[12:15], v[252:255], v[44:47], 0\n\t"
    "v_mfma_f32_16x16x32_bf16 v[12:15], v[244:247], v[40:43], v[12:15]\n\t"
    "v_mfma_f32_16x16x32_bf16 v[12:15], v[236:239], v[36:39], v[12:15]\n\t"
    "v_mfma_f32_16x16x32_bf16 v[12:15], v[228:231], v[212:215], v[12:15]\n\t"
    "v_cvt_pk_bf16_f32 v24, v80, v81\n\t"
    "v_cvt_pk_bf16_f32 v25, v82, v83\n\t"
    "s_waitcnt lgkmcnt(0)\n\t"
    "v_mfma_f32_16x16x32_bf16 v[12:15], a[252:255], v[216:219], v[12:15]\n\t"
    "v_mfma_f32_16x16x32_bf16 v[12:15], a[244:247], v[220:223], v[12:15]\n\t"
    "v_mfma_f32_16x16x32_bf16 v[36:39], a[0:3], v[196:199], 0\n\t"
    "v_mfma_f32_16x16x32_bf16 v[36:39], a[8:11], v[192:195], v[36:39]\n\t"
    "v_mfma_f32_16x16x32_bf16 v[36:39], a[16:19], v[188:191], v[36:39]\n\t"
    "v_mfma_f32_16x16x32_bf16 v[36:39], a[24:27], v[184:187], v[36:39]\n\t"
    "v_cvt_pk_bf16_f32 v26, v12, v13\n\t"
    "v_mfma_f32_16x16x32_bf16 v[36:39], a[32:35], v[180:183], v[36:39]\n\t"
    "v_mfma_f32_16x16x32_bf16 v[36:39], a[40:43], v[176:179], v[36:39]\n\t"
    "v_cvt_pk_bf16_f32 v27, v14, v15\n\t"
    "v_mfma_f32_16x16x32_bf16 v[64:67], v[208:211], v[52:55], v[64:67]\n\t"
    "v_mfma_f32_16x16x32_bf16 v[64:67], v[204:207], v[56:59], v[64:67]\n\t"
    "v_mfma_f32_16x16x32_bf16 v[64:67], v[200:203], v[24:27], v[64:67]\n\t"
    "v_mfma_f32_16x16x32_bf16 v[48:51], v[176:179], v[24:27], v[48:51]\n\t"
    "v_cvt_pk_bf16_f32 v12, v36, v37\n\t"
    "v_cvt_pk_bf16_f32 v13, v38, v39\n\t"
    "v_mfma_f32_16x16x32_bf16 v[48:51], v[180:183], v[56:59], v[48:51]\n\t"
    "v_mfma_f32_16x16x32_bf16 v[48:51], v[184:187], v[52:55], v[48:51]\n\t"
    "v_mfma_f32_16x16x32_bf16 v[68:71], v[160:163], v[52:55], v[68:71]\n\t"
    "v_max3_f32 v16, v64, v65, v66\n\t"
    "v_mfma_f32_16x16x32_bf16 v[68:71], v[156:159], v[56:59], v[68:71]\n\t"
    "v_mfma_f32_16x16x32_bf16 v[68:71], v[152:155], v[24:27], v[68:71]\n\t"
    "v_mfma_f32_16x16x32_bf16 v[72:75], v[128:131], v[24:27], v[72:75]\n\t"
    "v_max3_f32 v17, v67, v48, v49\n\t"
    "v_max3_f32 v16, v16, v50, v51\n\t"
    "v_mfma_f32_16x16x32_bf16 v[72:75], v[132:135], v[56:59], v[72:75]\n\t"
    "v_mfma_f32_16x16x32_bf16 v[72:75], v[136:139], v[52:55], v[72:75]\n\t"
    "v_mfma_f32_16x16x32_bf16 v[20:23], a[44:47], v[176:179], 0\n\t"
    "v_max3_f32 v17, v17, v68, v69\n\t"
    "v_max3_f32 v16, v16, v70, v71\n\t"
    "v_mfma_f32_16x16x32_bf16 v[20:23], a[36:39], v[180:183], v[20:23]\n\t"
    "v_mfma_f32_16x16x32_bf16 v[20:23], a[28:31], v[184:187], v[20:23]\n\t"
    "v_mfma_f32_16x16x32_bf16 v[20:23], a[20:23], v[188:191], v[20:23]\n\t"
    "v_max3_f32 v17, v17, v72, v73\n\t"
    "v_max3_f32 v16, v16, v74, v75\n\t"
    "v_mfma_f32_16x16x32_bf16 v[20:23], a[12:15], v[192:195], v[20:23]\n\t"
    "v_max_f32_e32 v16, v16, v17\n\t"
    "v_mov_b32_e32 v17, v16\n\t"
    "v_mfma_f32_16x16x32_bf16 v[20:23], a[4:7], v[196:199], v[20:23]\n\t"
    "v_mfma_f32_16x16x32_bf16 v[24:27], a[48:51], v[196:199], 0\n\t"
    "v_permlane32_swap_b32 v16, v17\n\t"
    "v_max_f32_e32 v16, v16, v17\n\t"
    "v_mfma_f32_16x16x32_bf16 v[24:27], a[56:59], v[192:195], v[24:27]\n\t"
    "v_mov_b32_e32 v17, v16\n\t"
    "v_mfma_f32_16x16x32_bf16 v[24:27], a[64:67], v[188:191], v[24:27]\n\t"
    "v_mfma_f32_16x16x32_bf16 v[24:27], a[72:75], v[184:187], v[24:27]\n\t"
    "v_cvt_pk_bf16_f32 v14, v20, v21\n\t"
    "v_cvt_pk_bf16_f32 v15, v22, v23\n\t"
    "v_mfma_f32_16x16x32_bf16 v[24:27], a[80:83], v[180:183], v[24:27]\n\t"
    "v_permlane16_swap_b32 v16, v17\n\t"
    "v_max_f32_e32 v16, v16, v17\n\t"
    "v_mfma_f32_16x16x32_bf16 v[24:27], a[88:91], v[176:179], v[24:27]\n\t"
    "v_sub_f32_e32 v64, v64, v16\n\t"
    "v_sub_f32_e32 v65, v65, v16\n\t"
    "v_mfma_f32_16x16x32_bf16 v[20:23], a[92:95], v[176:179], 0\n\t"
    "v_exp_f32_e32 v64, v64\n\t"
    "v_mfma_f32_16x16x32_bf16 v[20:23], a[84:87], v[180:183], v[20:23]\n\t"
    "v_exp_f32_e32 v65, v65\n\t"
    "v_mfma_f32_16x16x32_bf16 v[20:23], a[76:79], v[184:187], v[20:23]\n\t"
    "v_add_f32_e32 v18, v64, v65\n\t"
    "v_sub_f32_e32 v66, v66, v16\n\t"
    "v_mfma_f32_16x16x32_bf16 v[20:23], a[68:71], v[188:191], v[20:23]\n\t"
    "v_cvt_pk_bf16_f32 v28, v24, v25\n\t"
    "v_cvt_pk_bf16_f32 v29, v26, v27\n\t"
    "v_mfma_f32_16x16x32_bf16 v[20:23], a[60:63], v[192:195], v[20:23]\n\t"
    "v_exp_f32_e32 v66, v66\n\t"
    "v_mfma_f32_16x16x32_bf16 v[20:23], a[52:55], v[196:199], v[20:23]\n\t"
    "v_sub_f32_e32 v67, v67, v16\n\t"
    "v_mfma_f32_16x16x32_bf16 v[24:27], a[96:99], v[196:199], 0\n\t"
    "v_sub_f32_e32 v48, v48, v16\n\t"
    "v_mfma_f32_16x16x32_bf16 v[24:27], a[104:107], v[192:195], v[24:27]\n\t"
    "v_exp_f32_e32 v67, v67\n\t"
    "v_mfma_f32_16x16x32_bf16 v[24:27], a[112:115], v[188:191], v[24:27]\n\t"
    "v_add_f32_e32 v19, v66, v67\n\t"
    "v_sub_f32_e32 v49, v49, v16\n\t"
    "v_mfma_f32_16x16x32_bf16 v[24:27], a[120:123], v[184:187], v[24:27]\n\t"
    "v_cvt_pk_bf16_f32 v30, v20, v21\n\t"
    "v_cvt_pk_bf16_f32 v31, v22, v23\n\t"
    "v_mfma_f32_16x16x32_bf16 v[24:27], a[128:131], v[180:183], v[24:27]\n\t"
    "v_exp_f32_e32 v48, v48\n\t"
    "v_mfma_f32_16x16x32_bf16 v[24:27], a[136:139], v[176:179], v[24:27]\n\t"
    "v_add_f32_e32 v18, v18, v48\n\t"
    "v_sub_f32_e32 v50, v50, v16\n\t"
    "v_mfma_f32_16x16x32_bf16 v[20:23], a[140:143], v[176:179], 0\n\t"
    "v_exp_f32_e32 v49, v49\n\t"
    "v_mfma_f32_16x16x32_bf16 v[20:23], a[132:135], v[180:183], v[20:23]\n\t"
    "v_add_f32_e32 v18, v18, v49\n\t"
    "v_sub_f32_e32 v51, v51, v16\n\t"
    "v_mfma_f32_16x16x32_bf16 v[20:23], a[124:127], v[184:187], v[20:23]\n\t"
    "v_exp_f32_e32 v50, v50\n\t"
    "v_mfma_f32_16x16x32_bf16 v[20:23], a[116:119], v[188:191], v[20:23]\n\t"
    "v_cvt_pk_bf16_f32 v32, v24, v25\n\t"
    "v_cvt_pk_bf16_f32 v33, v26, v27\n\t"
    "v_mfma_f32_16x16x32_bf16 v[20:23], a[108:111], v[192:195], v[20:23]\n\t"
    "v_add_f32_e32 v19, v19, v50\n\t"
    "v_sub_f32_e32 v68, v68, v16\n\t"
    "v_mfma_f32_16x16x32_bf16 v[20:23], a[100:103], v[196:199], v[20:23]\n\t"
    "v_exp_f32_e32 v51, v51\n\t"
    "v_mfma_f32_16x16x32_bf16 v[24:27], a[144:147], v[196:199], 0\n\t"
    "v_add_f32_e32 v19, v19, v51\n\t"
    "v_sub_f32_e32 v69, v69, v16\n\t"
    "v_mfma_f32_16x16x32_bf16 v[24:27], a[152:155], v[192:195], v[24:27]\n\t"
    "v_exp_f32_e32 v68, v68\n\t"
    "v_mfma_f32_16x16x32_bf16 v[24:27], a[160:163], v[188:191], v[24:27]\n\t"
    "v_add_f32_e32 v18, v18, v68\n\t"
    "v_mfma_f32_16x16x32_bf16 v[24:27], a[168:171], v[184:187], v[24:27]\n\t"
    "v_cvt_pk_bf16_f32 v34, v20, v21\n\t"
    "v_mfma_f32_16x16x32_bf16 v[24:27], a[176:179], v[180:183], v[24:27]\n\t"
    "v_cvt_pk_bf16_f32 v35, v22, v23\n\t"
    "v_sub_f32_e32 v70, v70, v16\n\t"
    "s_barrier\n\t"
    "v_mfma_f32_16x16x32_bf16 v[24:27], a[184:187], v[176:179], v[24:27]\n\t"
    "v_exp_f32_e32 v69, v69\n\t"
    "v_mfma_f32_16x16x32_bf16 v[36:39], v[220:223], v[12:15], 0\n\t"
    "v_add_f32_e32 v18, v18, v69\n\t"
    "v_sub_f32_e32 v71, v71, v16\n\t"
    "s_add_u32 m0, %[rbeg], 24576\n\t"
    "s_add_u32 s61, %[wb], 12582912\n\t"
    "v_mfma_f32_16x16x32_bf16 v[36:39], v[216:219], v[28:31], v[36:39]\n\t"
    "v_exp_f32_e32 v70, v70\n\t"
    "v_mfma_f32_16x16x32_bf16 v[36:39], v[212:215], v[32:35], v[36:39]\n\t"
    "v_add_f32_e32 v19, v19, v70\n\t"
    "v_sub_f32_e32 v72, v72, v16\n\t"
    "v_mfma_f32_16x16x32_bf16 v[20:23], v[188:191], v[32:35], 0\n\t"
    "v_exp_f32_e32 v71, v71\n\t"
    "v_mfma_f32_16x16x32_bf16 v[20:23], v[192:195], v[28:31], v[20:23]\n\t"
    "v_add_f32_e32 v19, v19, v71\n\t"
    "v_sub_f32_e32 v73, v73, v16\n\t"
    "v_mfma_f32_16x16x32_bf16 v[20:23], v[196:199], v[12:15], v[20:23]\n\t"
    "v_exp_f32_e32 v72, v72\n\t"
    "buffer_load_dwordx4 v7, s[52:55], s61 offen offset:0 nt lds\n\t"
    "v_mfma_f32_16x16x32_bf16 v[40:43], v[172:175], v[12:15], 0\n\t"
    "v_add_f32_e32 v18, v18, v72\n\t"
    "v_sub_f32_e32 v74, v74, v16\n\t"
    "v_mfma_f32_16x16x32_bf16 v[40:43], v[168:171], v[28:31], v[40:43]\n\t"
    "v_exp_f32_e32 v73, v73\n\t"
    "v_mfma_f32_16x16x32_bf16 v[40:43], v[164:167], v[32:35], v[40:43]\n\t"
    "v_add_f32_e32 v18, v18, v73\n\t"
    "v_sub_f32_e32 v75, v75, v16\n\t"
    "v_mfma_f32_16x16x32_bf16 v[44:47], v[140:143], v[32:35], 0\n\t"
    "v_exp_f32_e32 v74, v74\n\t"
    "v_mfma_f32_16x16x32_bf16 v[44:47], v[144:147], v[28:31], v[44:47]\n\t"
    "v_add_f32_e32 v19, v19, v74\n\t"
    "v_cvt_pk_bf16_f32 v52, v24, v25\n\t"
    "v_mfma_f32_16x16x32_bf16 v[44:47], v[148:151], v[12:15], v[44:47]\n\t"
    "v_exp_f32_e32 v75, v75\n\t"
    "v_mfma_f32_16x16x32_bf16 v[32:35], a[188:191], v[176:179], 0\n\t"
    "v_add_f32_e32 v19, v19, v75\n\t"
    "v_add_f32_e32 v18, v18, v19\n\t"
    "v_mfma_f32_16x16x32_bf16 v[32:35], a[180:183], v[180:183], v[32:35]\n\t"
    "v_mov_b32_e32 v19, v18\n\t"
    "buffer_load_dwordx4 v7, s[52:55], s61 offen offset:1024 nt lds\n\t"
    "v_mfma_f32_16x16x32_bf16 v[32:35], a[172:175], v[184:187], v[32:35]\n\t"
    "v_cvt_pk_bf16_f32 v53, v26, v27\n\t"
    "v_mfma_f32_16x16x32_bf16 v[32:35], a[164:167], v[188:191], v[32:35]\n\t"
    "v_permlane32_swap_b32 v18, v19\n\t"
    "v_add_f32_e32 v18, v18, v19\n\t"
    "v_mfma_f32_16x16x32_bf16 v[32:35], a[156:159], v[192:195], v[32:35]\n\t"
    "v_mov_b32_e32 v19, v18\n\t"
    "buffer_load_dwordx4 v7, s[52:55], s61 offen offset:2048 nt lds\n\t"
    "v_mfma_f32_16x16x32_bf16 v[32:35], a[148:151], v[196:199], v[32:35]\n\t"
    "v_mfma_f32_16x16x32_bf16 v[12:15], a[192:195], v[196:199], 0\n\t"
    "v_permlane16_swap_b32 v18, v19\n\t"
    "v_add_f32_e32 v18, v18, v19\n\t"
    "v_mfma_f32_16x16x32_bf16 v[12:15], a[200:203], v[192:195], v[12:15]\n\t"
    "v_rcp_f32_e32 v18, v18\n\t"
    "v_mfma_f32_16x16x32_bf16 v[12:15], a[208:211], v[188:191], v[12:15]\n\t"
    "v_mul_f32_e32 v64, v64, v18\n\t"
    "v_mul_f32_e32 v65, v65, v18\n\t"
    "s_add_u32 m0, %[rbeg], 27648\n\t"
    "s_add_u32 s61, %[wb], 12779520\n\t"
    "v_mfma_f32_16x16x32_bf16 v[12:15], a[216:219], v[184:187], v[12:15]\n\t"
    "v_cvt_pk_bf16_f32 v54, v32, v33\n\t"
    "v_cvt_pk_bf16_f32 v55, v34, v35\n\t"
    "v_mfma_f32_16x16x32_bf16 v[12:15], a[224:227], v[180:183], v[12:15]\n\t"
    "v_cvt_pk_bf16_f32 v24, v64, v65\n\t"
    "v_mul_f32_e32 v66, v66, v18\n\t"
    "v_mfma_f32_16x16x32_bf16 v[12:15], a[232:235], v[176:179], v[12:15]\n\t"
    "v_mul_f32_e32 v67, v67, v18\n\t"
    "v_cvt_pk_bf16_f32 v25, v66, v67\n\t"
    "v_mfma_f32_16x16x32_bf16 v[28:31], a[236:239], v[176:179], 0\n\t"
    "v_mul_f32_e32 v48, v48, v18\n\t"
    "v_mul_f32_e32 v49, v49, v18\n\t"
    "v_mfma_f32_16x16x32_bf16 v[28:31], a[228:231], v[180:183], v[28:31]\n\t"
    "v_cvt_pk_bf16_f32 v26, v48, v49\n\t"
    "v_mul_f32_e32 v50, v50, v18\n\t"
    "buffer_load_dwordx4 v7, s[52:55], s61 offen offset:0 nt lds\n\t"
    "v_mfma_f32_16x16x32_bf16 v[28:31], a[220:223], v[184:187], v[28:31]\n\t"
    "v_mul_f32_e32 v51, v51, v18\n\t"
    "v_cvt_pk_bf16_f32 v27, v50, v51\n\t"
    "v_mfma_f32_16x16x32_bf16 v[28:31], a[212:215], v[188:191], v[28:31]\n\t"
    "v_cvt_pk_bf16_f32 v32, v12, v13\n\t"
    "v_cvt_pk_bf16_f32 v33, v14, v15\n\t"
    "v_mfma_f32_16x16x32_bf16 v[28:31], a[204:207], v[192:195], v[28:31]\n\t"
    "v_mul_f32_e32 v68, v68, v18\n\t"
    "v_mul_f32_e32 v69, v69, v18\n\t"
    "v_mfma_f32_16x16x32_bf16 v[28:31], a[196:199], v[196:199], v[28:31]\n\t"
    "v_cvt_pk_bf16_f32 v48, v68, v69\n\t"
    "v_mfma_f32_16x16x32_bf16 v[12:15], a[240:243], v[196:199], 0\n\t"
    "v_mul_f32_e32 v70, v70, v18\n\t"
    "buffer_load_dwordx4 v7, s[52:55], s61 offen offset:1024 nt lds\n\t"
    "v_mfma_f32_16x16x32_bf16 v[12:15], a[248:251], v[192:195], v[12:15]\n\t"
    "v_mul_f32_e32 v71, v71, v18\n\t"
    "v_cvt_pk_bf16_f32 v49, v70, v71\n\t"
    "v_mfma_f32_16x16x32_bf16 v[12:15], v[224:227], v[188:191], v[12:15]\n\t"
    "v_mul_f32_e32 v72, v72, v18\n\t"
    "v_mul_f32_e32 v73, v73, v18\n\t"
    "v_mfma_f32_16x16x32_bf16 v[12:15], v[232:235], v[184:187], v[12:15]\n\t"
    "v_cvt_pk_bf16_f32 v34, v28, v29\n\t"
    "v_cvt_pk_bf16_f32 v35, v30, v31\n\t"
    "v_mfma_f32_16x16x32_bf16 v[12:15], v[240:243], v[180:183], v[12:15]\n\t"
    "v_cvt_pk_bf16_f32 v50, v72, v73\n\t"
    "v_mul_f32_e32 v74, v74, v18\n\t"
    "v_mfma_f32_16x16x32_bf16 v[12:15], v[248:251], v[176:179], v[12:15]\n\t"
    "v_mul_f32_e32 v75, v75, v18\n\t"
    "v_cvt_pk_bf16_f32 v51, v74, v75\n\t"
    "v_mfma_f32_16x16x32_bf16 v[28:31], v[252:255], v[176:179], 0\n\t"
    "buffer_load_dwordx4 v7, s[52:55], s61 offen offset:2048 nt lds\n\t"
    "v_mfma_f32_16x16x32_bf16 v[28:31], v[244:247], v[180:183], v[28:31]\n\t"
    "v_mfma_f32_16x16x32_bf16 v[28:31], v[236:239], v[184:187], v[28:31]\n\t"
    "v_mfma_f32_16x16x32_bf16 v[28:31], v[228:231], v[188:191], v[28:31]\n\t"
    "v_cvt_pk_bf16_f32 v16, v12, v13\n\t"
    "v_cvt_pk_bf16_f32 v17, v14, v15\n\t"
    "v_mfma_f32_16x16x32_bf16 v[28:31], a[252:255], v[192:195], v[28:31]\n\t"
    "v_mfma_f32_16x16x32_bf16 v[28:31], a[244:247], v[196:199], v[28:31]\n\t"
    "s_add_u32 m0, %[rbeg], 30720\n\t"
    "s_add_u32 s61, %[wb], 12976128\n\t"
    "v_mfma_f32_16x16x32_bf16 v[12:15], a[0:3], v[172:175], 0\n\t"
    "ds_write_b128 v6, v[24:27] offset:0\n\t"
    "v_mfma_f32_16x16x32_bf16 v[12:15], a[8:11], v[168:171], v[12:15]\n\t"
    "v_mfma_f32_16x16x32_bf16 v[12:15], a[16:19], v[164:167], v[12:15]\n\t"
    "v_mfma_f32_16x16x32_bf16 v[12:15], a[24:27], v[160:163], v[12:15]\n\t"
    "v_cvt_pk_bf16_f32 v18, v28, v29\n\t"
    "v_mfma_f32_16x16x32_bf16 v[12:15], a[32:35], v[156:159], v[12:15]\n\t"
    "ds_write_b128 v6, v[48:51] offset:1024\n\t"
    "v_mfma_f32_16x16x32_bf16 v[12:15], a[40:43], v[152:155], v[12:15]\n\t"
    "v_cvt_pk_bf16_f32 v19, v30, v31\n\t"
    "buffer_load_dwordx4 v7, s[52:55], s61 offen offset:0 nt lds\n\t"
    "v_mfma_f32_16x16x32_bf16 v[36:39], v[208:211], v[52:55], v[36:39]\n\t"
    "v_mfma_f32_16x16x32_bf16 v[36:39], v[204:207], v[32:35], v[36:39]\n\t"
    "v_mfma_f32_16x16x32_bf16 v[36:39], v[200:203], v[16:19], v[36:39]\n\t"
    "s_add_i32 s64, s62, 24576\n\t"
    "v_mfma_f32_16x16x32_bf16 v[20:23], v[176:179], v[16:19], v[20:23]\n\t"
    "s_cmp_eq_u32 s64, %[rend]\n\t"
    "s_cselect_b32 s64, %[rbeg], s64\n\t"
    "buffer_load_dwordx4 v7, s[52:55], s61 offen offset:1024 nt lds\n\t"
    "v_mfma_f32_16x16x32_bf16 v[20:23], v[180:183], v[32:35], v[20:23]\n\t"
    "s_sub_i32 s65, s63, s64\n\t"
    "v_cvt_pk_bf16_f32 v24, v12, v13\n\t"
    "v_mfma_f32_16x16x32_bf16 v[20:23], v[184:187], v[52:55], v[20:23]\n\t"
    "v_add_u32_e32 v0, s65, v0\n\t"
    "v_add_u32_e32 v1, s65, v1\n\t"
    "v_mfma_f32_16x16x32_bf16 v[40:43], v[160:163], v[52:55], v[40:43]\n\t"
    "v_add_u32_e32 v2, s65, v2\n\t"
    "v_add_u32_e32 v3, s65, v3\n\t"
    "v_mfma_f32_16x16x32_bf16 v[40:43], v[156:159], v[32:35], v[40:43]\n\t"
    "v_add_u32_e32 v4, s65, v4\n\t"
    "v_add_u32_e32 v5, s65, v5\n\t"
    "v_mfma_f32_16x16x32_bf16 v[40:43], v[152:155], v[16:19], v[40:43]\n\t"
    "v_max3_f32 v28, v36, v37, v38\n\t"
    "v_cvt_pk_bf16_f32 v25, v14, v15\n\t"
    "v_mfma_f32_16x16x32_bf16 v[44:47], v[128:131], v[16:19], v[44:47]\n\t"
    "v_max3_f32 v29, v39, v20, v21\n\t"
    "v_max3_f32 v28, v28, v22, v23\n\t"
    "v_mfma_f32_16x16x32_bf16 v[44:47], v[132:135], v[32:35], v[44:47]\n\t"
    ".Lh4_%=:\n\t"
    "s_cbranch_execz .Lh5_%=\n\t"
    "buffer_load_dwordx4 v7, s[52:55], s61 offen offset:2048 nt lds\n\t"
    "v_mfma_f32_16x16x32_bf16 v[44:47], v[136:139], v[52:55], v[44:47]\n\t"
    "v_mfma_f32_16x16x32_bf16 v[12:15], a[44:47], v[152:155], 0\n\t"
    "v_max3_f32 v29, v29, v40, v41\n\t"
    "v_max3_f32 v28, v28, v42, v43\n\t"
    "v_mfma_f32_16x16x32_bf16 v[12:15], a[36:39], v[156:159], v[12:15]\n\t"
    "s_add_u32 m0, %[rbeg], 33792\n\t"
    "s_add_u32 s61, %[wb], 13172736\n\t"
    "v_mfma_f32_16x16x32_bf16 v[12:15], a[28:31], v[160:163], v[12:15]\n\t"
    "v_mfma_f32_16x16x32_bf16 v[12:15], a[20:23], v[164:167], v[12:15]\n\t"
    "v_max3_f32 v29, v29, v44, v45\n\t"
    "v_max3_f32 v28, v28, v46, v47\n\t"
    "v_mfma_f32_16x16x32_bf16 v[12:15], a[12:15], v[168:171], v[12:15]\n\t"
    "v_max_f32_e32 v28, v28, v29\n\t"
    "v_mov_b32_e32 v29, v28\n\t"
    "v_mfma_f32_16x16x32_bf16 v[12:15], a[4:7], v[172:175], v[12:15]\n\t"
    "v_mfma_f32_16x16x32_bf16 v[16:19], a[48:51], v[172:175], 0\n\t"
    "v_permlane32_swap_b32 v28, v29\n\t"
    "v_max_f32_e32 v28, v28, v29\n\t"
    "v_mfma_f32_16x16x32_bf16 v[16:19], a[56:59], v[168:171], v[16:19]\n\t"
    "v_mov_b32_e32 v29, v28\n\t"
    "buffer_load_dwordx4 v7, s[52:55], s61 offen offset:0 nt lds\n\t"
    "v_mfma_f32_16x16x32_bf16 v[16:19], a[64:67], v[164:167], v[16:19]\n\t"
    "v_mfma_f32_16x16x32_bf16 v[16:19], a[72:75], v[160:163], v[16:19]\n\t"
    "v_cvt_pk_bf16_f32 v26, v12, v13\n\t"
    "v_cvt_pk_bf16_f32 v27, v14, v15\n\t"
    "v_mfma_f32_16x16x32_bf16 v[16:19], a[80:83], v[156:159], v[16:19]\n\t"
    "v_permlane16_swap_b32 v28, v29\n\t"
    "v_max_f32_e32 v28, v28, v29\n\t"
    "v_mfma_f32_16x16x32_bf16 v[16:19], a[88:91], v[152:155], v[16:19]\n\t"
    "v_sub_f32_e32 v36, v36, v28\n\t"
    "v_sub_f32_e32 v37, v37, v28\n\t"
    "buffer_load_dwordx4 v7, s[52:55], s61 offen offset:1024 nt lds\n\t"
    "v_mfma_f32_16x16x32_bf16 v[12:15], a[92:95], v[152:155], 0\n\t"
    "v_exp_f32_e32 v36, v36\n\t"
    "v_mfma_f32_16x16x32_bf16 v[12:15], a[84:87], v[156:159], v[12:15]\n\t"
    "v_exp_f32_e32 v37, v37\n\t"
    "v_mfma_f32_16x16x32_bf16 v[12:15], a[76:79], v[160:163], v[12:15]\n\t"
    "v_add_f32_e32 v30, v36, v37\n\t"
    "v_sub_f32_e32 v38, v38, v28\n\t"
    "v_mfma_f32_16x16x32_bf16 v[12:15], a[68:71], v[164:167], v[12:15]\n\t"
    "v_cvt_pk_bf16_f32 v32, v16, v17\n\t"
    "v_cvt_pk_bf16_f32 v33, v18, v19\n\t"
    "v_mfma_f32_16x16x32_bf16 v[12:15], a[60:63], v[168:171], v[12:15]\n\t"
    "v_exp_f32_e32 v38, v38\n\t"
    "v_mfma_f32_16x16x32_bf16 v[12:15], a[52:55], v[172:175], v[12:15]\n\t"
    "v_sub_f32_e32 v39, v39, v28\n\t"
    "buffer_load_dwordx4 v7, s[52:55], s61 offen offset:2048 nt lds\n\t"
    "v_mfma_f32_16x16x32_bf16 v[16:19], a[96:99], v[172:175], 0\n\t"
    "v_sub_f32_e32 v20, v20, v28\n\t"
    "v_mfma_f32_16x16x32_bf16 v[16:19], a[104:107], v[168:171], v[16:19]\n\t"
    "v_exp_f32_e32 v39, v39\n\t"
    "v_mfma_f32_16x16x32_bf16 v[16:19], a[112:115], v[164:167], v[16:19]\n\t"
    "v_add_f32_e32 v31, v38, v39\n\t"
    "v_sub_f32_e32 v21, v21, v28\n\t"
    "v_mfma_f32_16x16x32_bf16 v[16:19], a[120:123], v[160:163], v[16:19]\n\t"
    "v_cvt_pk_bf16_f32 v34, v12, v13\n\t"
    "v_cvt_pk_bf16_f32 v35, v14, v15\n\t"
    "v_mfma_f32_16x16x32_bf16 v[16:19], a[128:131], v[156:159], v[16:19]\n\t"
    "v_exp_f32_e32 v20, v20\n\t"
    "s_add_u32 m0, %[rbeg], 36864\n\t"
    "s_add_u32 s61, %[wb], 13369344\n\t"
    "v_mfma_f32_16x16x32_bf16 v[16:19], a[136:139], v[152:155], v[16:19]\n\t"
    "v_add_f32_e32 v30, v30, v20\n\t"
    "v_sub_f32_e32 v22, v22, v28\n\t"
    "v_mfma_f32_16x16x32_bf16 v[12:15], a[140:143], v[152:155], 0\n\t"
    "v_exp_f32_e32 v21, v21\n\t"
    "v_mfma_f32_16x16x32_bf16 v[12:15], a[132:135], v[156:159], v[12:15]\n\t"
    "v_add_f32_e32 v30, v30, v21\n\t"
    "v_sub_f32_e32 v23, v23, v28\n\t"
    "v_mfma_f32_16x16x32_bf16 v[12:15], a[124:127], v[160:163], v[12:15]\n\t"
    "v_exp_f32_e32 v22, v22\n\t"
    "v_mfma_f32_16x16x32_bf16 v[12:15], a[116:119], v[164:167], v[12:15]\n\t"
    "v_cvt_pk_bf16_f32 v48, v16, v17\n\t"
    "v_cvt_pk_bf16_f32 v49, v18, v19\n\t"
    "buffer_load_dwordx4 v7, s[52:55], s61 offen offset:0 nt lds\n\t"
    "v_mfma_f32_16x16x32_bf16 v[12:15], a[108:111], v[168:171], v[12:15]\n\t"
    "v_add_f32_e32 v31, v31, v22\n\t"
    "v_sub_f32_e32 v40, v40, v28\n\t"
    "v_mfma_f32_16x16x32_bf16 v[12:15], a[100:103], v[172:175], v[12:15]\n\t"
    "v_exp_f32_e32 v23, v23\n\t"
    "v_mfma_f32_16x16x32_bf16 v[16:19], a[144:147], v[172:175], 0\n\t"
    "v_add_f32_e32 v31, v31, v23\n\t"
    "v_sub_f32_e32 v41, v41, v28\n\t"
    "v_mfma_f32_16x16x32_bf16 v[16:19], a[152:155], v[168:171], v[16:19]\n\t"
    "v_exp_f32_e32 v40, v40\n\t"
    "v_mfma_f32_16x16x32_bf16 v[16:19], a[160:163], v[164:167], v[16:19]\n\t"
    "v_add_f32_e32 v30, v30, v40\n\t"
    "buffer_load_dwordx4 v7, s[52:55], s61 offen offset:1024 nt lds\n\t"
    "v_mfma_f32_16x16x32_bf16 v[16:19], a[168:171], v[160:163], v[16:19]\n\t"
    "v_cvt_pk_bf16_f32 v50, v12, v13\n\t"
    "v_mfma_f32_16x16x32_bf16 v[16:19], a[176:179], v[156:159], v[16:19]\n\t"
    "v_cvt_pk_bf16_f32 v51, v14, v15\n\t"
    "v_sub_f32_e32 v42, v42, v28\n\t"
    "v_mfma_f32_16x16x32_bf16 v[16:19], a[184:187], v[152:155], v[16:19]\n\t"
    "v_exp_f32_e32 v41, v41\n\t"
    "v_mfma_f32_16x16x32_bf16 v[60:63], v[220:223], v[24:27], 0\n\t"
    "v_add_f32_e32 v30, v30, v41\n\t"
    "v_sub_f32_e32 v43, v43, v28\n\t"
    "v_mfma_f32_16x16x32_bf16 v[60:63], v[216:219], v[32:35], v[60:63]\n\t"
    "v_exp_f32_e32 v42, v42\n\t"
    "buffer_load_dwordx4 v7, s[52:55], s61 offen offset:2048 nt lds\n\t"
    "v_mfma_f32_16x16x32_bf16 v[60:63], v[212:215], v[48:51], v[60:63]\n\t"
    "v_add_f32_e32 v31, v31, v42\n\t"
    "v_sub_f32_e32 v44, v44, v28\n\t"
    "v_mfma_f32_16x16x32_bf16 v[64:67], v[188:191], v[48:51], 0\n\t"
    "v_exp_f32_e32 v43, v43\n\t"
    "v_mfma_f32_16x16x32_bf16 v[64:67], v[192:195], v[32:35], v[64:67]\n\t"
    "v_add_f32_e32 v31, v31, v43\n\t"
    "v_sub_f32_e32 v45, v45, v28\n\t"
    "v_mfma_f32_16x16x32_bf16 v[64:67], v[196:199], v[24:27], v[64:67]\n\t"
    "v_exp_f32_e32 v44, v44\n\t"
    "v_mfma_f32_16x16x32_bf16 v[68:71], v[172:175], v[24:27], 0\n\t"
    "v_add_f32_e32 v30, v30, v44\n\t"
    "v_sub_f32_e32 v46, v46, v28\n\t"
    "s_add_u32 m0, %[rbeg], 39936\n\t"
    "s_add_u32 s61, %[wb], 13565952\n\t"
    "v_mfma_f32_16x16x32_bf16 v[68:71], v[168:171], v[32:35], v[68:71]\n\t"
    "v_exp_f32_e32 v45, v45\n\t"
    "v_mfma_f32_16x16x32_bf16 v[68:71], v[164:167], v[48:51], v[68:71]\n\t"
    "v_add_f32_e32 v30, v30, v45\n\t"
    "v_sub_f32_e32 v47, v47, v28\n\t"
    "v_mfma_f32_16x16x32_bf16 v[12:15], v[140:143], v[48:51], 0\n\t"
    "v_exp_f32_e32 v46, v46\n\t"
    "v_mfma_f32_16x16x32_bf16 v[12:15], v[144:147], v[32:35], v[12:15]\n\t"
    "v_add_f32_e32 v31, v31, v46\n\t"
    "v_cvt_pk_bf16_f32 v52, v16, v17\n\t"
    "v_mfma_f32_16x16x32_bf16 v[12:15], v[148:151], v[24:27], v[12:15]\n\t"
    "v_exp_f32_e32 v47, v47\n\t"
    "v_mfma_f32_16x16x32_bf16 v[48:51], a[188:191], v[152:155], 0\n\t"
    "v_add_f32_e32 v31, v31, v47\n\t"
    "v_add_f32_e32 v30, v30, v31\n\t"
    "v_mfma_f32_16x16x32_bf16 v[48:51], a[180:183], v[156:159], v[48:51]\n\t"
    "v_mov_b32_e32 v31, v30\n\t"
    "buffer_load_dwordx4 v7, s[52:55], s61 offen offset:0 nt lds\n\t"
    "v_mfma_f32_16x16x32_bf16 v[48:51], a[172:175], v[160:163], v[48:51]\n\t"
    "v_cvt_pk_bf16_f32 v53, v18, v19\n\t"
    "v_mfma_f32_16x16x32_bf16 v[48:51], a[164:167], v[164:167], v[48:51]\n\t"
    "v_permlane32_swap_b32 v30, v31\n\t"
    "v_add_f32_e32 v30, v30, v31\n\t"
    "v_mfma_f32_16x16x32_bf16 v[48:51], a[156:159], v[168:171], v[48:51]\n\t"
    "v_mov_b32_e32 v31, v30\n\t"
    "v_mfma_f32_16x16x32_bf16 v[48:51], a[148:151], v[172:175], v[48:51]\n\t"
    "buffer_load_dwordx4 v7, s[52:55], s61 offen offset:1024 nt lds\n\t"
    "v_mfma_f32_16x16x32_bf16 v[16:19], a[192:195], v[172:175], 0\n\t"
    "v_permlane16_swap_b32 v30, v31\n\t"
    "v_add_f32_e32 v30, v30, v31\n\t"
    "v_mfma_f32_16x16x32_bf16 v[16:19], a[200:203], v[168:171], v[16:19]\n\t"
    "v_rcp_f32_e32 v30, v30\n\t"
    "v_mfma_f32_16x16x32_bf16 v[16:19], a[208:211], v[164:167], v[16:19]\n\t"
    "v_mul_f32_e32 v36, v36, v30\n\t"
    "v_mul_f32_e32 v37, v37, v30\n\t"
    "v_mfma_f32_16x16x32_bf16 v[16:19], a[216:219], v[160:163], v[16:19]\n\t"
    "v_cvt_pk_bf16_f32 v54, v48, v49\n\t"
    "v_cvt_pk_bf16_f32 v55, v50, v51\n\t"
    "v_mfma_f32_16x16x32_bf16 v[16:19], a[224:227], v[156:159], v[16:19]\n\t"
    "v_cvt_pk_bf16_f32 v24, v36, v37\n\t"
    "v_mul_f32_e32 v38, v38, v30\n\t"
    "buffer_load_dwordx4 v7, s[52:55], s61 offen offset:2048 nt lds\n\t"
    "v_mfma_f32_16x16x32_bf16 v[16:19], a[232:235], v[152:155], v[16:19]\n\t"
    "v_mul_f32_e32 v39, v39, v30\n\t"
    "v_cvt_pk_bf16_f32 v25, v38, v39\n\t"
    "v_mfma_f32_16x16x32_bf16 v[32:35], a[236:239], v[152:155], 0\n\t"
    "v_mul_f32_e32 v20, v20, v30\n\t"
    "v_mul_f32_e32 v21, v21, v30\n\t"
    "v_mfma_f32_16x16x32_bf16 v[32:35], a[228:231], v[156:159], v[32:35]\n\t"
    "v_cvt_pk_bf16_f32 v26, v20, v21\n\t"
    "v_mul_f32_e32 v22, v22, v30\n\t"
    "v_mfma_f32_16x16x32_bf16 v[32:35], a[220:223], v[160:163], v[32:35]\n\t"
    "v_mul_f32_e32 v23, v23, v30\n\t"
    "v_cvt_pk_bf16_f32 v27, v22, v23\n\t"
    "v_mfma_f32_16x16x32_bf16 v[32:35], a[212:215], v[164:167], v[32:35]\n\t"
    "v_cvt_pk_bf16_f32 v36, v16, v17\n\t"
    "v_cvt_pk_bf16_f32 v37, v18, v19\n\t"
    "v_mfma_f32_16x16x32_bf16 v[32:35], a[204:207], v[168:171], v[32:35]\n\t"
    "v_mul_f32_e32 v40, v40, v30\n\t"
    "v_mul_f32_e32 v41, v41, v30\n\t"
    "v_mfma_f32_16x16x32_bf16 v[32:35], a[196:199], v[172:175], v[32:35]\n\t"
    "v_cvt_pk_bf16_f32 v20, v40, v41\n\t"
    "s_add_u32 m0, %[rbeg], 43008\n\t"
    "s_add_u32 s61, %[wb], 13762560\n\t"
    "v_mfma_f32_16x16x32_bf16 v[16:19], a[240:243], v[172:175], 0\n\t"
    "v_mul_f32_e32 v42, v42, v30\n\t"
    "v_mfma_f32_16x16x32_bf16 v[16:19], a[248:251], v[168:171], v[16:19]\n\t"
    "v_mul_f32_e32 v43, v43, v30\n\t"
    "v_cvt_pk_bf16_f32 v21, v42, v43\n\t"
    "v_mfma_f32_16x16x32_bf16 v[16:19], v[224:227], v[164:167], v[16:19]\n\t"
    "v_mul_f32_e32 v44, v44, v30\n\t"
    "v_mul_f32_e32 v45, v45, v30\n\t"
    "buffer_load_dwordx4 v7, s[52:55], s61 offen offset:0 nt lds\n\t"
    "v_mfma_f32_16x16x32_bf16 v[16:19], v[232:235], v[160:163], v[16:19]\n\t"
    "v_cvt_pk_bf16_f32 v38, v32, v33\n\t"
    "v_cvt_pk_bf16_f32 v39, v34, v35\n\t"
    "v_mfma_f32_16x16x32_bf16 v[16:19], v[240:243], v[156:159], v[16:19]\n\t"
    "v_cvt_pk_bf16_f32 v22, v44, v45\n\t"
    "v_mul_f32_e32 v46, v46, v30\n\t"
    "v_mfma_f32_16x16x32_bf16 v[16:19], v[248:251], v[152:155], v[16:19]\n\t"
    "v_mul_f32_e32 v47, v47, v30\n\t"
    "v_cvt_pk_bf16_f32 v23, v46, v47\n\t"
    "v_mfma_f32_16x16x32_bf16 v[32:35], v[252:255], v[152:155], 0\n\t"
    "v_mfma_f32_16x16x32_bf16 v[32:35], v[244:247], v[156:159], v[32:35]\n\t"
    "buffer_load_dwordx4 v7, s[52:55], s61 offen offset:1024 nt lds\n\t"
    "v_mfma_f32_16x16x32_bf16 v[32:35], v[236:239], v[160:163], v[32:35]\n\t"
    "v_mfma_f32_16x16x32_bf16 v[32:35], v[228:231], v[164:167], v[32:35]\n\t"
    "v_cvt_pk_bf16_f32 v28, v16, v17\n\t"
    "v_cvt_pk_bf16_f32 v29, v18, v19\n\t"
    "v_mfma_f32_16x16x32_bf16 v[32:35], a[252:255], v[168:171], v[32:35]\n\t"
    "v_mfma_f32_16x16x32_bf16 v[32:35], a[244:247], v[172:175], v[32:35]\n\t"
    "v_mfma_f32_16x16x32_bf16 v[16:19], a[0:3], v[148:151], 0\n\t"
    "ds_write_b128 v6, v[24:27] offset:2048\n\t"
    "v_mfma_f32_16x16x32_bf16 v[16:19], a[8:11], v[144:147], v[16:19]\n\t"
    "buffer_load_dwordx4 v7, s[52:55], s61 offen offset:2048 nt lds\n\t"
    "v_mfma_f32_16x16x32_bf16 v[16:19], a[16:19], v[140:143], v[16:19]\n\t"
    "v_mfma_f32_16x16x32_bf16 v[16:19], a[24:27], v[136:139], v[16:19]\n\t"
    "v_cvt_pk_bf16_f32 v30, v32, v33\n\t"
    "v_mfma_f32_16x16x32_bf16 v[16:19], a[32:35], v[132:135], v[16:19]\n\t"
    "ds_write_b128 v6, v[20:23] offset:3072\n\t"
    "v_mfma_f32_16x16x32_bf16 v[16:19], a[40:43], v[128:131], v[16:19]\n\t"
    "v_cvt_pk_bf16_f32 v31, v34, v35\n\t"
    "v_mfma_f32_16x16x32_bf16 v[60:63], v[208:211], v[52:55], v[60:63]\n\t"
    "s_add_u32 m0, %[rbeg], 46080\n\t"
    "s_add_u32 s61, %[wb], 13959168\n\t"
    "v_mfma_f32_16x16x32_bf16 v[60:63], v[204:207], v[36:39], v[60:63]\n\t"
    "v_mfma_f32_16x16x32_bf16 v[60:63], v[200:203], v[28:31], v[60:63]\n\t"
    "v_mfma_f32_16x16x32_bf16 v[64:67], v[176:179], v[28:31], v[64:67]\n\t"
    "v_cvt_pk_bf16_f32 v44, v16, v17\n\t"
    "v_cvt_pk_bf16_f32 v45, v18, v19\n\t"
    "v_mfma_f32_16x16x32_bf16 v[64:67], v[180:183], v[36:39], v[64:67]\n\t"
    "v_mfma_f32_16x16x32_bf16 v[64:67], v[184:187], v[52:55], v[64:67]\n\t"
    "buffer_load_dwordx4 v7, s[52:55], s61 offen offset:0 nt lds\n\t"
    "v_mfma_f32_16x16x32_bf16 v[68:71], v[160:163], v[52:55], v[68:71]\n\t"
    "v_max3_f32 v16, v60, v61, v62\n\t"
    "v_mfma_f32_16x16x32_bf16 v[68:71], v[156:159], v[36:39], v[68:71]\n\t"
    "v_mfma_f32_16x16x32_bf16 v[68:71], v[152:155], v[28:31], v[68:71]\n\t"
    "v_mfma_f32_16x16x32_bf16 v[12:15], v[128:131], v[28:31], v[12:15]\n\t"
    "v_max3_f32 v17, v63, v64, v65\n\t"
    "v_max3_f32 v16, v16, v66, v67\n\t"
    "v_mfma_f32_16x16x32_bf16 v[12:15], v[132:135], v[36:39], v[12:15]\n\t"
    "buffer_load_dwordx4 v7, s[52:55], s61 offen offset:1024 nt lds\n\t"
    "v_mfma_f32_16x16x32_bf16 v[12:15], v[136:139], v[52:55], v[12:15]\n\t"
    "v_mfma_f32_16x16x32_bf16 v[20:23], a[44:47], v[128:131], 0\n\t"
    "v_max3_f32 v17, v17, v68, v69\n\t"
    "v_max3_f32 v16, v16, v70, v71\n\t"
    "v_mfma_f32_16x16x32_bf16 v[20:23], a[36:39], v[132:135], v[20:23]\n\t"
    "v_mfma_f32_16x16x32_bf16 v[20:23], a[28:31], v[136:139], v[20:23]\n\t"
    "v_mfma_f32_16x16x32_bf16 v[20:23], a[20:23], v[140:143], v[20:23]\n\t"
    "v_max3_f32 v17, v17, v12, v13\n\t"
    "v_max3_f32 v16, v16, v14, v15\n\t"
    "v_mfma_f32_16x16x32_bf16 v[20:23], a[12:15], v[144:147], v[20:23]\n\t"
    "v_max_f32_e32 v16, v16, v17\n\t"
    "v_mov_b32_e32 v17, v16\n\t"
    "v_mfma_f32_16x16x32_bf16 v[20:23], a[4:7], v[148:151], v[20:23]\n\t"
    "buffer_load_dwordx4 v7, s[52:55], s61 offen offset:2048 nt lds\n\t"
    "v_mfma_f32_16x16x32_bf16 v[24:27], a[48:51], v[148:151], 0\n\t"
    "v_permlane32_swap_b32 v16, v17\n\t"
    "v_max_f32_e32 v16, v16, v17\n\t"
    "v_mfma_f32_16x16x32_bf16 v[24:27], a[56:59], v[144:147], v[24:27]\n\t"
    "v_mov_b32_e32 v17, v16\n\t"
    "v_mfma_f32_16x16x32_bf16 v[24:27], a[64:67], v[140:143], v[24:27]\n\t"
    "s_add_u32 m0, %[rbeg], 49152\n\t"
    "s_add_u32 s61, %[wb], 25165824\n\t"
    "v_mfma_f32_16x16x32_bf16 v[24:27], a[72:75], v[136:139], v[24:27]\n\t"
    "v_cvt_pk_bf16_f32 v46, v20, v21\n\t"
    "v_cvt_pk_bf16_f32 v47, v22, v23\n\t"
    "v_mfma_f32_16x16x32_bf16 v[24:27], a[80:83], v[132:135], v[24:27]\n\t"
    "v_permlane16_swap_b32 v16, v17\n\t"
    "v_max_f32_e32 v16, v16, v17\n\t"
    "v_mfma_f32_16x16x32_bf16 v[24:27], a[88:91], v[128:131], v[24:27]\n\t"
    "v_sub_f32_e32 v60, v60, v16\n\t"
    "v_sub_f32_e32 v61, v61, v16\n\t"
    "v_mfma_f32_16x16x32_bf16 v[20:23], a[92:95], v[128:131], 0\n\t"
    "v_exp_f32_e32 v60, v60\n\t"
    "v_mfma_f32_16x16x32_bf16 v[20:23], a[84:87], v[132:135], v[20:23]\n\t"
    "v_exp_f32_e32 v61, v61\n\t"
    "buffer_load_dwordx4 v7, s[52:55], s61 offen offset:0 nt lds\n\t"
    "v_mfma_f32_16x16x32_bf16 v[20:23], a[76:79], v[136:139], v[20:23]\n\t"
    "v_add_f32_e32 v18, v60, v61\n\t"
    "v_sub_f32_e32 v62, v62, v16\n\t"
    "v_mfma_f32_16x16x32_bf16 v[20:23], a[68:71], v[140:143], v[20:23]\n\t"
    "v_cvt_pk_bf16_f32 v48, v24, v25\n\t"
    "v_cvt_pk_bf16_f32 v49, v26, v27\n\t"
    "v_mfma_f32_16x16x32_bf16 v[20:23], a[60:63], v[144:147], v[20:23]\n\t"
    "v_exp_f32_e32 v62, v62\n\t"
    "v_mfma_f32_16x16x32_bf16 v[20:23], a[52:55], v[148:151], v[20:23]\n\t"
    "v_sub_f32_e32 v63, v63, v16\n\t"
    "v_mfma_f32_16x16x32_bf16 v[24:27], a[96:99], v[148:151], 0\n\t"
    "v_sub_f32_e32 v64, v64, v16\n\t"
    "buffer_load_dwordx4 v7, s[52:55], s61 offen offset:1024 nt lds\n\t"
    "v_mfma_f32_16x16x32_bf16 v[24:27], a[104:107], v[144:147], v[24:27]\n\t"
    "v_exp_f32_e32 v63, v63\n\t"
    "v_mfma_f32_16x16x32_bf16 v[24:27], a[112:115], v[140:143], v[24:27]\n\t"
    "v_add_f32_e32 v19, v62, v63\n\t"
    "v_sub_f32_e32 v65, v65, v16\n\t"
    "v_mfma_f32_16x16x32_bf16 v[24:27], a[120:123], v[136:139], v[24:27]\n\t"
    "v_cvt_pk_bf16_f32 v50, v20, v21\n\t"
    "v_cvt_pk_bf16_f32 v51, v22, v23\n\t"
    "v_mfma_f32_16x16x32_bf16 v[24:27], a[128:131], v[132:135], v[24:27]\n\t"
    "v_exp_f32_e32 v64, v64\n\t"
    "v_mfma_f32_16x16x32_bf16 v[24:27], a[136:139], v[128:131], v[24:27]\n\t"
    "v_add_f32_e32 v18, v18, v64\n\t"
    "v_sub_f32_e32 v66, v66, v16\n\t"
    "buffer_load_dwordx4 v7, s[52:55], s61 offen offset:2048 nt lds\n\t"
    "v_mfma_f32_16x16x32_bf16 v[28:31], a[140:143], v[128:131], 0\n\t"
    "v_exp_f32_e32 v65, v65\n\t"
    "v_mfma_f32_16x16x32_bf16 v[28:31], a[132:135], v[132:135], v[28:31]\n\t"
    "v_add_f32_e32 v18, v18, v65\n\t"
    "v_sub_f32_e32 v67, v67, v16\n\t"
    "v_mfma_f32_16x16x32_bf16 v[28:31], a[124:127], v[136:139], v[28:31]\n\t"
    "v_exp_f32_e32 v66, v66\n\t"
    "v_mfma_f32_16x16x32_bf16 v[28:31], a[116:119], v[140:143], v[28:31]\n\t"
    "v_cvt_pk_bf16_f32 v40, v24, v25\n\t"
    "v_cvt_pk_bf16_f32 v41, v26, v27\n\t"
    "v_mfma_f32_16x16x32_bf16 v[28:31], a[108:111], v[144:147], v[28:31]\n\t"
    "v_add_f32_e32 v19, v19, v66\n\t"
    "v_sub_f32_e32 v68, v68, v16\n\t"
    "v_mfma_f32_16x16x32_bf16 v[28:31], a[100:103], v[148:151], v[28:31]\n\t"
    "v_exp_f32_e32 v67, v67\n\t"
    "v_mfma_f32_16x16x32_bf16 v[52:55], a[144:147], v[148:151], 0\n\t"
    "v_add_f32_e32 v19, v19, v67\n\t"
    "v_sub_f32_e32 v69, v69, v16\n\t"
    "v_mfma_f32_16x16x32_bf16 v[52:55], a[152:155], v[144:147], v[52:55]\n\t"
    "v_exp_f32_e32 v68, v68\n\t"
    "s_mov_b32 s60, 8\n\t"
    "s_add_u32 s61, %[wb], 25362432\n\t"
    ".Lmid_%=:\n\t"
    "v_mfma_f32_16x16x32_bf16 v[52:55], a[160:163], v[140:143], v[52:55]\n\t"
    "s_add_u32 m0, s62, 3072\n\t"
    "v_add_f32_e32 v18, v18, v68\n\t"
    "v_mfma_f32_16x16x32_bf16 v[52:55], a[168:171], v[136:139], v[52:55]\n\t"
    "buffer_load_dwordx4 v7, s[52:55], s61 offen offset:0 nt lds\n\t"
    "v_cvt_pk_bf16_f32 v42, v28, v29\n\t"
    "v_mfma_f32_16x16x32_bf16 v[52:55], a[176:179], v[132:135], v[52:55]\n\t"
    "v_cvt_pk_bf16_f32 v43, v30, v31\n\t"
    "v_sub_f32_e32 v70, v70, v16\n\t"
    "v_mfma_f32_16x16x32_bf16 v[52:55], a[184:187], v[128:131], v[52:55]\n\t"
    "v_exp_f32_e32 v69, v69\n\t"
    "v_mfma_f32_16x16x32_bf16 v[20:23], v[220:223], v[44:47], 0\n\t"
    "v_add_f32_e32 v18, v18, v69\n\t"
    "v_sub_f32_e32 v71, v71, v16\n\t"
    "v_mfma_f32_16x16x32_bf16 v[20:23], v[216:219], v[48:51], v[20:23]\n\t"
    "v_exp_f32_e32 v70, v70\n\t"
    "v_mfma_f32_16x16x32_bf16 v[20:23], v[212:215], v[40:43], v[20:23]\n\t"
    "s_waitcnt vmcnt(22)\n\t"
    "v_add_f32_e32 v19, v19, v70\n\t"
    "v_mfma_f32_16x16x32_bf16 v[24:27], v[188:191], v[40:43], 0\n\t"
    "v_exp_f32_e32 v71, v71\n\t"
    "v_mfma_f32_16x16x32_bf16 v[24:27], v[192:195], v[48:51], v[24:27]\n\t"
    "ds_read_b128 v[220:223], v0 offset:0\n\t"
    "v_add_f32_e32 v19, v19, v71\n\t"
    "v_mfma_f32_16x16x32_bf16 v[24:27], v[196:199], v[44:47], v[24:27]\n\t"
    "v_sub_f32_e32 v12, v12, v16\n\t"
    "v_sub_f32_e32 v13, v13, v16\n\t"
    "v_mfma_f32_16x16x32_bf16 v[28:31], v[172:175], v[44:47], 0\n\t"
    "ds_read_b128 v[216:219], v1 offset:0\n\t"
    "v_sub_f32_e32 v14, v14, v16\n\t"
    "v_mfma_f32_16x16x32_bf16 v[28:31], v[168:171], v[48:51], v[28:31]\n\t"
    "v_exp_f32_e32 v12, v12\n\t"
    "v_mfma_f32_16x16x32_bf16 v[28:31], v[164:167], v[40:43], v[28:31]\n\t"
    "ds_read_b128 v[212:215], v2 offset:0\n\t"
    "v_add_f32_e32 v18, v18, v12\n\t"
    "v_mfma_f32_16x16x32_bf16 v[32:35], v[140:143], v[40:43], 0\n\t"
    "v_exp_f32_e32 v13, v13\n\t"
    "v_mfma_f32_16x16x32_bf16 v[32:35], v[144:147], v[48:51], v[32:35]\n\t"
    "ds_read_b128 v[36:39], v3 offset:0\n\t"
    "v_add_f32_e32 v18, v18, v13\n\t"
    "v_mfma_f32_16x16x32_bf16 v[32:35], v[148:151], v[44:47], v[32:35]\n\t"
    "v_exp_f32_e32 v14, v14\n\t"
    "v_mfma_f32_16x16x32_bf16 v[56:59], a[188:191], v[128:131], 0\n\t"
    "ds_read_b128 v[40:43], v4 offset:0\n\t"
    "v_add_f32_e32 v19, v19, v14\n\t"
    "v_mfma_f32_16x16x32_bf16 v[56:59], a[180:183], v[132:135], v[56:59]\n\t"
    "buffer_load_dwordx4 v7, s[52:55], s61 offen offset:1024 nt lds\n\t"
    "v_sub_f32_e32 v15, v15, v16\n\t"
    "v_mfma_f32_16x16x32_bf16 v[56:59], a[172:175], v[136:139], v[56:59]\n\t"
    "ds_read_b128 v[44:47], v5 offset:0\n\t"
    "v_cvt_pk_bf16_f32 v48, v52, v53\n\t"
    "v_mfma_f32_16x16x32_bf16 v[56:59], a[164:167], v[140:143], v[56:59]\n\t"
    "v_exp_f32_e32 v15, v15\n\t"
    "v_mfma_f32_16x16x32_bf16 v[56:59], a[156:159], v[144:147], v[56:59]\n\t"
    "v_add_f32_e32 v19, v19, v15\n\t"
    "v_add_f32_e32 v18, v18, v19\n\t"
    "v_mfma_f32_16x16x32_bf16 v[56:59], a[148:151], v[148:151], v[56:59]\n\t"
    "v_mov_b32_e32 v19, v18\n\t"
    "v_cvt_pk_bf16_f32 v49, v54, v55\n\t"
    "v_mfma_f32_16x16x32_bf16 v[72:75], a[192:195], v[148:151], 0\n\t"
    "v_mfma_f32_16x16x32_bf16 v[72:75], a[200:203], v[144:147], v[72:75]\n\t"
    "v_permlane32_swap_b32 v18, v19\n\t"
    "v_add_f32_e32 v18, v18, v19\n\t"
    "v_mfma_f32_16x16x32_bf16 v[72:75], a[208:211], v[140:143], v[72:75]\n\t"
    "v_mov_b32_e32 v19, v18\n\t"
    "v_mfma_f32_16x16x32_bf16 v[72:75], a[216:219], v[136:139], v[72:75]\n\t"
    "v_cvt_pk_bf16_f32 v50, v56, v57\n\t"
    "v_cvt_pk_bf16_f32 v51, v58, v59\n\t"
    "v_mfma_f32_16x16x32_bf16 v[72:75], a[224:227], v[132:135], v[72:75]\n\t"
    "v_permlane16_swap_b32 v18, v19\n\t"
    "v_add_f32_e32 v18, v18, v19\n\t"
    "v_mfma_f32_16x16x32_bf16 v[72:75], a[232:235], v[128:131], v[72:75]\n\t"
    "v_rcp_f32_e32 v18, v18\n\t"
    "v_mfma_f32_16x16x32_bf16 v[76:79], a[236:239], v[128:131], 0\n\t"
    "v_mul_f32_e32 v60, v60, v18\n\t"
    "v_mul_f32_e32 v61, v61, v18\n\t"
    "v_mfma_f32_16x16x32_bf16 v[76:79], a[228:231], v[132:135], v[76:79]\n\t"
    "v_cvt_pk_bf16_f32 v52, v60, v61\n\t"
    "v_mul_f32_e32 v62, v62, v18\n\t"
    "v_mfma_f32_16x16x32_bf16 v[76:79], a[220:223], v[136:139], v[76:79]\n\t"
    "v_mul_f32_e32 v63, v63, v18\n\t"
    "v_cvt_pk_bf16_f32 v53, v62, v63\n\t"
    "v_mfma_f32_16x16x32_bf16 v[76:79], a[212:215], v[140:143], v[76:79]\n\t"
    "v_cvt_pk_bf16_f32 v56, v72, v73\n\t"
    "v_cvt_pk_bf16_f32 v57, v74, v75\n\t"
    "v_mfma_f32_16x16x32_bf16 v[76:79], a[204:207], v[144:147], v[76:79]\n\t"
    "v_mul_f32_e32 v64, v64, v18\n\t"
    "v_mul_f32_e32 v65, v65, v18\n\t"
    "v_mfma_f32_16x16x32_bf16 v[76:79], a[196:199], v[148:151], v[76:79]\n\t"
    "buffer_load_dwordx4 v7, s[52:55], s61 offen offset:2048 nt lds\n\t"
    "v_cvt_pk_bf16_f32 v54, v64, v65\n\t"
    "v_mfma_f32_16x16x32_bf16 v[72:75], a[240:243], v[148:151], 0\n\t"
    "s_add_u32 s61, s61, 196608\n\t"
    "v_mul_f32_e32 v66, v66, v18\n\t"
    "v_mfma_f32_16x16x32_bf16 v[72:75], a[248:251], v[144:147], v[72:75]\n\t"
    "v_mul_f32_e32 v67, v67, v18\n\t"
    "v_cvt_pk_bf16_f32 v55, v66, v67\n\t"
    "v_mfma_f32_16x16x32_bf16 v[72:75], v[224:227], v[140:143], v[72:75]\n\t"
    "v_mul_f32_e32 v68, v68, v18\n\t"
    "v_mul_f32_e32 v69, v69, v18\n\t"
    "v_mfma_f32_16x16x32_bf16 v[72:75], v[232:235], v[136:139], v[72:75]\n\t"
    "v_cvt_pk_bf16_f32 v58, v76, v77\n\t"
    "v_cvt_pk_bf16_f32 v59, v78, v79\n\t"
    "v_mfma_f32_16x16x32_bf16 v[72:75], v[240:243], v[132:135], v[72:75]\n\t"
    "v_cvt_pk_bf16_f32 v60, v68, v69\n\t"
    "v_mul_f32_e32 v70, v70, v18\n\t"
    "v_mfma_f32_16x16x32_bf16 v[72:75], v[248:251], v[128:131], v[72:75]\n\t"
    "v_mul_f32_e32 v71, v71, v18\n\t"
    "v_cvt_pk_bf16_f32 v61, v70, v71\n\t"
    "v_mfma_f32_16x16x32_bf16 v[64:67], v[252:255], v[128:131], 0\n\t"
    "v_mul_f32_e32 v12, v12, v18\n\t"
    "v_mul_f32_e32 v13, v13, v18\n\t"
    "v_mfma_f32_16x16x32_bf16 v[64:67], v[244:247], v[132:135], v[64:67]\n\t"
    "v_cvt_pk_bf16_f32 v62, v12, v13\n\t"
    "v_mul_f32_e32 v14, v14, v18\n\t"
    "v_mfma_f32_16x16x32_bf16 v[64:67], v[236:239], v[136:139], v[64:67]\n\t"
    "s_sub_u32 s60, s60, 1\n\t"
    "s_cmp_lg_u32 s60, 0\n\t"
    "v_mfma_f32_16x16x32_bf16 v[64:67], v[228:231], v[140:143], v[64:67]\n\t"
    "v_cvt_pk_bf16_f32 v68, v72, v73\n\t"
    "v_cvt_pk_bf16_f32 v69, v74, v75\n\t"
    "v_mfma_f32_16x16x32_bf16 v[64:67], a[252:255], v[144:147], v[64:67]\n\t"
    "s_mov_b32 s63, s62\n\t"
    "s_mov_b32 s62, s64\n\t"
    "v_mfma_f32_16x16x32_bf16 v[64:67], a[244:247], v[148:151], v[64:67]\n\t"
    "s_cbranch_scc0 .Ltail_%=\n\t"
    "s_waitcnt lgkmcnt(0)\n\t"
    "v_mfma_f32_16x16x32_bf16 v[72:75], a[0:3], v[220:223], 0\n\t"
    "ds_write_b128 v6, v[52:55] offset:4096\n\t"
    "v_mfma_f32_16x16x32_bf16 v[72:75], a[8:11], v[216:219], v[72:75]\n\t"
    "v_mul_f32_e32 v15, v15, v18\n\t"
    "v_cvt_pk_bf16_f32 v63, v14, v15\n\t"
    "v_mfma_f32_16x16x32_bf16 v[72:75], a[16:19], v[212:215], v[72:75]\n\t"
    "s_add_u32 m0, s63, 6144\n\t"
    "v_mfma_f32_16x16x32_bf16 v[72:75], a[24:27], v[36:39], v[72:75]\n\t"
    "buffer_load_dwordx4 v7, s[52:55], s61 offen offset:0 nt lds\n\t"
    "v_cvt_pk_bf16_f32 v70, v64, v65\n\t"
    "v_mfma_f32_16x16x32_bf16 v[72:75], a[32:35], v[40:43], v[72:75]\n\t"
    "ds_write_b128 v6, v[60:63] offset:5120\n\t"
    "v_mfma_f32_16x16x32_bf16 v[72:75], a[40:43], v[44:47], v[72:75]\n\t"
    "v_cvt_pk_bf16_f32 v71, v66, v67\n\t"
    "v_mfma_f32_16x16x32_bf16 v[20:23], v[208:211], v[48:51], v[20:23]\n\t"
    "v_mfma_f32_16x16x32_bf16 v[20:23], v[204:207], v[56:59], v[20:23]\n\t"
    "v_mfma_f32_16x16x32_bf16 v[20:23], v[200:203], v[68:71], v[20:23]\n\t"
    "v_mfma_f32_16x16x32_bf16 v[24:27], v[176:179], v[68:71], v[24:27]\n\t"
    "v_cvt_pk_bf16_f32 v12, v72, v73\n\t"
    "v_cvt_pk_bf16_f32 v13, v74, v75\n\t"
    "v_mfma_f32_16x16x32_bf16 v[24:27], v[180:183], v[56:59], v[24:27]\n\t"
    "s_waitcnt lgkmcnt(0)\n\t"
    "s_barrier\n\t"
    "v_mfma_f32_16x16x32_bf16 v[24:27], v[184:187], v[48:51], v[24:27]\n\t"
    "v_mfma_f32_16x16x32_bf16 v[28:31], v[160:163], v[48:51], v[28:31]\n\t"
    "v_max3_f32 v16, v20, v21, v22\n\t"
    "v_mfma_f32_16x16x32_bf16 v[28:31], v[156:159], v[56:59], v[28:31]\n\t"
    "v_mfma_f32_16x16x32_bf16 v[28:31], v[152:155], v[68:71], v[28:31]\n\t"
    "v_mfma_f32_16x16x32_bf16 v[32:35], v[128:131], v[68:71], v[32:35]\n\t"
    "v_max3_f32 v17, v23, v24, v25\n\t"
    "v_max3_f32 v16, v16, v26, v27\n\t"
    "v_mfma_f32_16x16x32_bf16 v[32:35], v[132:135], v[56:59], v[32:35]\n\t"
    "s_waitcnt vmcnt(7)\n\t"
    "v_mfma_f32_16x16x32_bf16 v[32:35], v[136:139], v[48:51], v[32:35]\n\t"
    "ds_read_b128 v[188:191], v2 offset:6144\n\t"
    "v_mfma_f32_16x16x32_bf16 v[52:55], a[44:47], v[44:47], 0\n\t"
    "v_max3_f32 v17, v17, v28, v29\n\t"
    "v_max3_f32 v16, v16, v30, v31\n\t"
    "v_mfma_f32_16x16x32_bf16 v[52:55], a[36:39], v[40:43], v[52:55]\n\t"
    "buffer_load_dwordx4 v7, s[52:55], s61 offen offset:1024 nt lds\n\t"
    "ds_read_b128 v[192:195], v1 offset:6144\n\t"
    "v_mfma_f32_16x16x32_bf16 v[52:55], a[28:31], v[36:39], v[52:55]\n\t"
    "v_mfma_f32_16x16x32_bf16 v[52:55], a[20:23], v[212:215], v[52:55]\n\t"
    "ds_read_b128 v[196:199], v0 offset:6144\n\t"
    "v_max3_f32 v17, v17, v32, v33\n\t"
    "v_mfma_f32_16x16x32_bf16 v[52:55], a[12:15], v[216:219], v[52:55]\n\t"
    "v_max3_f32 v16, v16, v34, v35\n\t"
    "v_max_f32_e32 v16, v16, v17\n\t"
    "v_mfma_f32_16x16x32_bf16 v[52:55], a[4:7], v[220:223], v[52:55]\n\t"
    "ds_read_b128 v[172:175], v0 offset:12288\n\t"
    "v_mov_b32_e32 v17, v16\n\t"
    "v_mfma_f32_16x16x32_bf16 v[48:51], a[48:51], v[220:223], 0\n\t"
    "v_mfma_f32_16x16x32_bf16 v[48:51], a[56:59], v[216:219], v[48:51]\n\t"
    "ds_read_b128 v[168:171], v1 offset:12288\n\t"
    "v_permlane32_swap_b32 v16, v17\n\t"
    "v_mfma_f32_16x16x32_bf16 v[48:51], a[64:67], v[212:215], v[48:51]\n\t"
    "v_max_f32_e32 v16, v16, v17\n\t"
    "v_mov_b32_e32 v17, v16\n\t"
    "v_mfma_f32_16x16x32_bf16 v[48:51], a[72:75], v[36:39], v[48:51]\n\t"
    "ds_read_b128 v[164:167], v2 offset:12288\n\t"
    "v_cvt_pk_bf16_f32 v14, v52, v53\n\t"
    "v_mfma_f32_16x16x32_bf16 v[48:51], a[80:83], v[40:43], v[48:51]\n\t"
    "v_cvt_pk_bf16_f32 v15, v54, v55\n\t"
    "v_permlane16_swap_b32 v16, v17\n\t"
    "v_mfma_f32_16x16x32_bf16 v[48:51], a[88:91], v[44:47], v[48:51]\n\t"
    "ds_read_b128 v[140:143], v2 offset:18432\n\t"
    "v_max_f32_e32 v16, v16, v17\n\t"
    "v_mfma_f32_16x16x32_bf16 v[56:59], a[92:95], v[44:47], 0\n\t"
    "v_sub_f32_e32 v20, v20, v16\n\t"
    "v_sub_f32_e32 v21, v21, v16\n\t"
    "v_mfma_f32_16x16x32_bf16 v[56:59], a[84:87], v[40:43], v[56:59]\n\t"
    "ds_read_b128 v[144:147], v1 offset:18432\n\t"
    "v_sub_f32_e32 v22, v22, v16\n\t"
    "v_mfma_f32_16x16x32_bf16 v[56:59], a[76:79], v[36:39], v[56:59]\n\t"
    "v_exp_f32_e32 v20, v20\n\t"
    "v_mfma_f32_16x16x32_bf16 v[56:59], a[68:71], v[212:215], v[56:59]\n\t"
    "ds_read_b128 v[148:151], v0 offset:18432\n\t"
    "v_cvt_pk_bf16_f32 v52, v48, v49\n\t"
    "v_mfma_f32_16x16x32_bf16 v[56:59], a[60:63], v[216:219], v[56:59]\n\t"
    "v_cvt_pk_bf16_f32 v53, v50, v51\n\t"
    "v_sub_f32_e32 v23, v23, v16\n\t"
    "v_mfma_f32_16x16x32_bf16 v[56:59], a[52:55], v[220:223], v[56:59]\n\t"
    "buffer_load_dwordx4 v7, s[52:55], s61 offen offset:2048 nt lds\n\t"
    "v_sub_f32_e32 v24, v24, v16\n\t"
    "v_mfma_f32_16x16x32_bf16 v[60:63], a[96:99], v[220:223], 0\n\t"
    "s_add_u32 s61, s61, 196608\n\t"
    "v_sub_f32_e32 v25, v25, v16\n\t"
    "v_mfma_f32_16x16x32_bf16 v[60:63], a[104:107], v[216:219], v[60:63]\n\t"
    "v_exp_f32_e32 v21, v21\n\t"
    "v_mfma_f32_16x16x32_bf16 v[60:63], a[112:115], v[212:215], v[60:63]\n\t"
    "v_add_f32_e32 v18, v20, v21\n\t"
    "v_sub_f32_e32 v26, v26, v16\n\t"
    "v_mfma_f32_16x16x32_bf16 v[60:63], a[120:123], v[36:39], v[60:63]\n\t"
    "v_cvt_pk_bf16_f32 v54, v56, v57\n\t"
    "v_cvt_pk_bf16_f32 v55, v58, v59\n\t"
    "v_mfma_f32_16x16x32_bf16 v[60:63], a[128:131], v[40:43], v[60:63]\n\t"
    "v_exp_f32_e32 v22, v22\n\t"
    "v_mfma_f32_16x16x32_bf16 v[60:63], a[136:139], v[44:47], v[60:63]\n\t"
    "v_exp_f32_e32 v23, v23\n\t"
    "v_mfma_f32_16x16x32_bf16 v[48:51], a[140:143], v[44:47], 0\n\t"
    "v_add_f32_e32 v19, v22, v23\n\t"
    "v_sub_f32_e32 v27, v27, v16\n\t"
    "v_mfma_f32_16x16x32_bf16 v[48:51], a[132:135], v[40:43], v[48:51]\n\t"
    "v_exp_f32_e32 v24, v24\n\t"
    "v_mfma_f32_16x16x32_bf16 v[48:51], a[124:127], v[36:39], v[48:51]\n\t"
    "v_add_f32_e32 v18, v18, v24\n\t"
    "v_sub_f32_e32 v28, v28, v16\n\t"
    "v_mfma_f32_16x16x32_bf16 v[48:51], a[116:119], v[212:215], v[48:51]\n\t"
    "v_cvt_pk_bf16_f32 v56, v60, v61\n\t"
    "v_cvt_pk_bf16_f32 v57, v62, v63\n\t"
    "v_mfma_f32_16x16x32_bf16 v[48:51], a[108:111], v[216:219], v[48:51]\n\t"
    "v_exp_f32_e32 v25, v25\n\t"
    "v_mfma_f32_16x16x32_bf16 v[48:51], a[100:103], v[220:223], v[48:51]\n\t"
    "v_add_f32_e32 v18, v18, v25\n\t"
    "v_sub_f32_e32 v29, v29, v16\n\t"
    "v_mfma_f32_16x16x32_bf16 v[60:63], a[144:147], v[220:223], 0\n\t"
    "v_exp_f32_e32 v26, v26\n\t"
    "v_mfma_f32_16x16x32_bf16 v[60:63], a[152:155], v[216:219], v[60:63]\n\t"
    "v_add_f32_e32 v19, v19, v26\n\t"
    "v_exp_f32_e32 v27, v27\n\t"
    "v_mfma_f32_16x16x32_bf16 v[60:63], a[160:163], v[212:215], v[60:63]\n\t"
    "s_add_u32 m0, s63, 9216\n\t"
    "v_add_f32_e32 v19, v19, v27\n\t"
    "v_mfma_f32_16x16x32_bf16 v[60:63], a[168:171], v[36:39], v[60:63]\n\t"
    "buffer_load_dwordx4 v7, s[52:55], s61 offen offset:0 nt lds\n\t"
    "v_cvt_pk_bf16_f32 v58, v48, v49\n\t"
    "v_mfma_f32_16x16x32_bf16 v[60:63], a[176:179], v[40:43], v[60:63]\n\t"
    "v_cvt_pk_bf16_f32 v59, v50, v51\n\t"
    "v_sub_f32_e32 v30, v30, v16\n\t"
    "v_mfma_f32_16x16x32_bf16 v[60:63], a[184:187], v[44:47], v[60:63]\n\t"
    "v_exp_f32_e32 v28, v28\n\t"
    "v_mfma_f32_16x16x32_bf16 v[64:67], v[220:223], v[12:15], 0\n\t"
    "v_add_f32_e32 v18, v18, v28\n\t"
    "v_sub_f32_e32 v31, v31, v16\n\t"
    "v_mfma_f32_16x16x32_bf16 v[64:67], v[216:219], v[52:55], v[64:67]\n\t"
    "v_exp_f32_e32 v29, v29\n\t"
    "v_mfma_f32_16x16x32_bf16 v[64:67], v[212:215], v[56:59], v[64:67]\n\t"
    "v_add_f32_e32 v18, v18, v29\n\t"
    "s_waitcnt lgkmcnt(0)\n\t"
    "v_mfma_f32_16x16x32_bf16 v[48:51], v[188:191], v[56:59], 0\n\t"
    "v_exp_f32_e32 v30, v30\n\t"
    "v_mfma_f32_16x16x32_bf16 v[48:51], v[192:195], v[52:55], v[48:51]\n\t"
    "v_add_f32_e32 v19, v19, v30\n\t"
    "v_sub_f32_e32 v32, v32, v16\n\t"
    "v_mfma_f32_16x16x32_bf16 v[48:51], v[196:199], v[12:15], v[48:51]\n\t"
    "ds_read_b128 v[184:187], v3 offset:6144\n\t"
    "v_sub_f32_e32 v33, v33, v16\n\t"
    "v_mfma_f32_16x16x32_bf16 v[68:71], v[172:175], v[12:15], 0\n\t"
    "v_exp_f32_e32 v31, v31\n\t"
    "v_mfma_f32_16x16x32_bf16 v[68:71], v[168:171], v[52:55], v[68:71]\n\t"
    "ds_read_b128 v[180:183], v4 offset:6144\n\t"
    "v_add_f32_e32 v19, v19, v31\n\t"
    "v_mfma_f32_16x16x32_bf16 v[68:71], v[164:167], v[56:59], v[68:71]\n\t"
    "v_exp_f32_e32 v32, v32\n\t"
    "v_mfma_f32_16x16x32_bf16 v[72:75], v[140:143], v[56:59], 0\n\t"
    "ds_read_b128 v[176:179], v5 offset:6144\n\t"
    "v_add_f32_e32 v18, v18, v32\n\t"
    "v_mfma_f32_16x16x32_bf16 v[72:75], v[144:147], v[52:55], v[72:75]\n\t"
    "v_exp_f32_e32 v33, v33\n\t"
    "v_mfma_f32_16x16x32_bf16 v[72:75], v[148:151], v[12:15], v[72:75]\n\t"
    "ds_read_b128 v[208:211], v3 offset:0\n\t"
    "v_add_f32_e32 v18, v18, v33\n\t"
    "v_mfma_f32_16x16x32_bf16 v[56:59], a[188:191], v[44:47], 0\n\t"
    "v_sub_f32_e32 v34, v34, v16\n\t"
    "v_sub_f32_e32 v35, v35, v16\n\t"
    "v_mfma_f32_16x16x32_bf16 v[56:59], a[180:183], v[40:43], v[56:59]\n\t"
    "buffer_load_dwordx4 v7, s[52:55], s61 offen offset:1024 nt lds\n\t"
    "ds_read_b128 v[204:207], v4 offset:0\n\t"
    ".Lh5_%=:\n\t"
    "s_cbranch_execz .LrM_%=\n\t"
    "v_mfma_f32_16x16x32_bf16 v[56:59], a[172:175], v[36:39], v[56:59]\n\t"
    "v_exp_f32_e32 v34, v34\n\t"
    "v_mfma_f32_16x16x32_bf16 v[56:59], a[164:167], v[212:215], v[56:59]\n\t"
    "ds_read_b128 v[200:203], v5 offset:0\n\t"
    "v_add_f32_e32 v19, v19, v34\n\t"
    "v_mfma_f32_16x16x32_bf16 v[56:59], a[156:159], v[216:219], v[56:59]\n\t"
    "v_exp_f32_e32 v35, v35\n\t"
    "v_mfma_f32_16x16x32_bf16 v[56:59], a[148:151], v[220:223], v[56:59]\n\t"
    "ds_read_b128 v[160:163], v3 offset:12288\n\t"
    "v_add_f32_e32 v19, v19, v35\n\t"
    "v_mfma_f32_16x16x32_bf16 v[12:15], a[192:195], v[220:223], 0\n\t"
    "v_add_f32_e32 v18, v18, v19\n\t"
    "v_mov_b32_e32 v19, v18\n\t"
    "v_mfma_f32_16x16x32_bf16 v[12:15], a[200:203], v[216:219], v[12:15]\n\t"
    "ds_read_b128 v[156:159], v4 offset:12288\n\t"
    "v_cvt_pk_bf16_f32 v52, v60, v61\n\t"
    "v_mfma_f32_16x16x32_bf16 v[12:15], a[208:211], v[212:215], v[12:15]\n\t"
    "v_permlane32_swap_b32 v18, v19\n\t"
    "v_add_f32_e32 v18, v18, v19\n\t"
    "v_mfma_f32_16x16x32_bf16 v[12:15], a[216:219], v[36:39], v[12:15]\n\t"
    "ds_read_b128 v[152:155], v5 offset:12288\n\t"
    "v_cvt_pk_bf16_f32 v54, v56, v57\n\t"
    "v_mfma_f32_16x16x32_bf16 v[12:15], a[224:227], v[40:43], v[12:15]\n\t"
    "v_cvt_pk_bf16_f32 v55, v58, v59\n\t"
    "v_mov_b32_e32 v19, v18\n\t"
    "v_mfma_f32_16x16x32_bf16 v[12:15], a[232:235], v[44:47], v[12:15]\n\t"
    "ds_read_b128 v[128:131], v5 offset:18432\n\t"
    "v_cvt_pk_bf16_f32 v53, v62, v63\n\t"
    "v_mfma_f32_16x16x32_bf16 v[76:79], a[236:239], v[44:47], 0\n\t"
    "v_permlane16_swap_b32 v18, v19\n\t"
    "v_add_f32_e32 v18, v18, v19\n\t"
    "v_mfma_f32_16x16x32_bf16 v[76:79], a[228:231], v[40:43], v[76:79]\n\t"
    "ds_read_b128 v[132:135], v4 offset:18432\n\t"
    "v_mfma_f32_16x16x32_bf16 v[76:79], a[220:223], v[36:39], v[76:79]\n\t"
    "v_rcp_f32_e32 v18, v18\n\t"
    "v_mfma_f32_16x16x32_bf16 v[76:79], a[212:215], v[212:215], v[76:79]\n\t"
    "ds_read_b128 v[136:139], v3 offset:18432\n\t"
    "v_cvt_pk_bf16_f32 v56, v12, v13\n\t"
    "v_mfma_f32_16x16x32_bf16 v[76:79], a[204:207], v[216:219], v[76:79]\n\t"
    "v_cvt_pk_bf16_f32 v57, v14, v15\n\t"
    "v_mul_f32_e32 v20, v20, v18\n\t"
    "v_mfma_f32_16x16x32_bf16 v[76:79], a[196:199], v[220:223], v[76:79]\n\t"
    "buffer_load_dwordx4 v7, s[52:55], s61 offen offset:2048 nt lds\n\t"
    "v_mul_f32_e32 v21, v21, v18\n\t"
    "v_mfma_f32_16x16x32_bf16 v[80:83], a[240:243], v[220:223], 0\n\t"
    "s_add_u32 s61, s61, 196608\n\t"
    "v_cvt_pk_bf16_f32 v60, v20, v21\n\t"
    "v_mfma_f32_16x16x32_bf16 v[80:83], a[248:251], v[216:219], v[80:83]\n\t"
    "v_mul_f32_e32 v22, v22, v18\n\t"
    "v_mul_f32_e32 v23, v23, v18\n\t"
    "v_mfma_f32_16x16x32_bf16 v[80:83], v[224:227], v[212:215], v[80:83]\n\t"
    "v_cvt_pk_bf16_f32 v61, v22, v23\n\t"
    "v_mul_f32_e32 v24, v24, v18\n\t"
    "v_mfma_f32_16x16x32_bf16 v[80:83], v[232:235], v[36:39], v[80:83]\n\t"
    "v_cvt_pk_bf16_f32 v58, v76, v77\n\t"
    "v_cvt_pk_bf16_f32 v59, v78, v79\n\t"
    "v_mfma_f32_16x16x32_bf16 v[80:83], v[240:243], v[40:43], v[80:83]\n\t"
    "v_mul_f32_e32 v25, v25, v18\n\t"
    "v_cvt_pk_bf16_f32 v62, v24, v25\n\t"
    "v_mfma_f32_16x16x32_bf16 v[80:83], v[248:251], v[44:47], v[80:83]\n\t"
    "v_mul_f32_e32 v26, v26, v18\n\t"
    "v_mul_f32_e32 v27, v27, v18\n\t"
    "v_mfma_f32_16x16x32_bf16 v[12:15], v[252:255], v[44:47], 0\n\t"
    "v_cvt_pk_bf16_f32 v63, v26, v27\n\t"
    "v_mul_f32_e32 v28, v28, v18\n\t"
    "v_mfma_f32_16x16x32_bf16 v[12:15], v[244:247], v[40:43], v[12:15]\n\t"
    "v_mul_f32_e32 v29, v29, v18\n\t"
    "v_cvt_pk_bf16_f32 v20, v28, v29\n\t"
    "v_mfma_f32_16x16x32_bf16 v[12:15], v[236:239], v[36:39], v[12:15]\n\t"
    "v_mul_f32_e32 v30, v30, v18\n\t"
    "v_mul_f32_e32 v31, v31, v18\n\t"
    "v_mfma_f32_16x16x32_bf16 v[12:15], v[228:231], v[212:215], v[12:15]\n\t"
    "v_cvt_pk_bf16_f32 v24, v80, v81\n\t"
    "v_cvt_pk_bf16_f32 v25, v82, v83\n\t"
    "v_mfma_f32_16x16x32_bf16 v[12:15], a[252:255], v[216:219], v[12:15]\n\t"
    "v_cvt_pk_bf16_f32 v21, v30, v31\n\t"
    "v_mul_f32_e32 v32, v32, v18\n\t"
    "v_mfma_f32_16x16x32_bf16 v[12:15], a[244:247], v[220:223], v[12:15]\n\t"
    "v_mul_f32_e32 v33, v33, v18\n\t"
    "v_cvt_pk_bf16_f32 v22, v32, v33\n\t"
    "v_mfma_f32_16x16x32_bf16 v[36:39], a[0:3], v[196:199], 0\n\t"
    "ds_write_b128 v6, v[60:63] offset:6144\n\t"
    "v_mfma_f32_16x16x32_bf16 v[36:39], a[8:11], v[192:195], v[36:39]\n\t"
    "v_mul_f32_e32 v34, v34, v18\n\t"
    "v_mul_f32_e32 v35, v35, v18\n\t"
    "v_mfma_f32_16x16x32_bf16 v[36:39], a[16:19], v[188:191], v[36:39]\n\t"
    "s_add_u32 m0, s63, 12288\n\t"
    "s_waitcnt lgkmcnt(1)\n\t"
    "v_mfma_f32_16x16x32_bf16 v[36:39], a[24:27], v[184:187], v[36:39]\n\t"
    "buffer_load_dwordx4 v7, s[52:55], s61 offen offset:0 nt lds\n\t"
    "v_cvt_pk_bf16_f32 v26, v12, v13\n\t"
    "v_cvt_pk_bf16_f32 v23, v34, v35\n\t"
    "v_mfma_f32_16x16x32_bf16 v[36:39], a[32:35], v[180:183], v[36:39]\n\t"
    "ds_write_b128 v6, v[20:23] offset:7168\n\t"
    "v_mfma_f32_16x16x32_bf16 v[36:39], a[40:43], v[176:179], v[36:39]\n\t"
    "v_cvt_pk_bf16_f32 v27, v14, v15\n\t"
    "v_mfma_f32_16x16x32_bf16 v[64:67], v[208:211], v[52:55], v[64:67]\n\t"
    "v_mfma_f32_16x16x32_bf16 v[64:67], v[204:207], v[56:59], v[64:67]\n\t"
    "v_mfma_f32_16x16x32_bf16 v[64:67], v[200:203], v[24:27], v[64:67]\n\t"
    "v_mfma_f32_16x16x32_bf16 v[48:51], v[176:179], v[24:27], v[48:51]\n\t"
    "v_cvt_pk_bf16_f32 v12, v36, v37\n\t"
    "v_cvt_pk_bf16_f32 v13, v38, v39\n\t"
    "v_mfma_f32_16x16x32_bf16 v[48:51], v[180:183], v[56:59], v[48:51]\n\t"
    "s_waitcnt lgkmcnt(0)\n\t"
    "s_barrier\n\t"
    "v_mfma_f32_16x16x32_bf16 v[48:51], v[184:187], v[52:55], v[48:51]\n\t"
    "v_mfma_f32_16x16x32_bf16 v[68:71], v[160:163], v[52:55], v[68:71]\n\t"
    "v_max3_f32 v16, v64, v65, v66\n\t"
    "v_mfma_f32_16x16x32_bf16 v[68:71], v[156:159], v[56:59], v[68:71]\n\t"
    "v_mfma_f32_16x16x32_bf16 v[68:71], v[152:155], v[24:27], v[68:71]\n\t"
    "v_mfma_f32_16x16x32_bf16 v[72:75], v[128:131], v[24:27], v[72:75]\n\t"
    "v_max3_f32 v17, v67, v48, v49\n\t"
    "v_max3_f32 v16, v16, v50, v51\n\t"
    "v_mfma_f32_16x16x32_bf16 v[72:75], v[132:135], v[56:59], v[72:75]\n\t"
    "v_mfma_f32_16x16x32_bf16 v[72:75], v[136:139], v[52:55], v[72:75]\n\t"
    "v_mfma_f32_16x16x32_bf16 v[20:23], a[44:47], v[176:179], 0\n\t"
    "v_max3_f32 v17, v17, v68, v69\n\t"
    "v_max3_f32 v16, v16, v70, v71\n\t"
    "v_mfma_f32_16x16x32_bf16 v[20:23], a[36:39], v[180:183], v[20:23]\n\t"
    "buffer_load_dwordx4 v7, s[52:55], s61 offen offset:1024 nt lds\n\t"
    "v_mfma_f32_16x16x32_bf16 v[20:23], a[28:31], v[184:187], v[20:23]\n\t"
    "v_mfma_f32_16x16x32_bf16 v[20:23], a[20:23], v[188:191], v[20:23]\n\t"
    "v_max3_f32 v17, v17, v72, v73\n\t"
    "v_max3_f32 v16, v16, v74, v75\n\t"
    "v_mfma_f32_16x16x32_bf16 v[20:23], a[12:15], v[192:195], v[20:23]\n\t"
    "v_max_f32_e32 v16, v16, v17\n\t"
    "v_mov_b32_e32 v17, v16\n\t"
    "v_mfma_f32_16x16x32_bf16 v[20:23], a[4:7], v[196:199], v[20:23]\n\t"
    "v_mfma_f32_16x16x32_bf16 v[24:27], a[48:51], v[196:199], 0\n\t"
    "v_permlane32_swap_b32 v16, v17\n\t"
    "v_max_f32_e32 v16, v16, v17\n\t"
    "v_mfma_f32_16x16x32_bf16 v[24:27], a[56:59], v[192:195], v[24:27]\n\t"
    "v_mov_b32_e32 v17, v16\n\t"
    "v_mfma_f32_16x16x32_bf16 v[24:27], a[64:67], v[188:191], v[24:27]\n\t"
    "v_mfma_f32_16x16x32_bf16 v[24:27], a[72:75], v[184:187], v[24:27]\n\t"
    "v_cvt_pk_bf16_f32 v14, v20, v21\n\t"
    "v_cvt_pk_bf16_f32 v15, v22, v23\n\t"
    "v_mfma_f32_16x16x32_bf16 v[24:27], a[80:83], v[180:183], v[24:27]\n\t"
    "v_permlane16_swap_b32 v16, v17\n\t"
    "v_max_f32_e32 v16, v16, v17\n\t"
    "v_mfma_f32_16x16x32_bf16 v[24:27], a[88:91], v[176:179], v[24:27]\n\t"
    "v_sub_f32_e32 v64, v64, v16\n\t"
    "v_sub_f32_e32 v65, v65, v16\n\t"
    "v_mfma_f32_16x16x32_bf16 v[20:23], a[92:95], v[176:179], 0\n\t"
    "v_exp_f32_e32 v64, v64\n\t"
    "v_mfma_f32_16x16x32_bf16 v[20:23], a[84:87], v[180:183], v[20:23]\n\t"
    "v_exp_f32_e32 v65, v65\n\t"
    "v_mfma_f32_16x16x32_bf16 v[20:23], a[76:79], v[184:187], v[20:23]\n\t"
    "v_add_f32_e32 v18, v64, v65\n\t"
    "v_sub_f32_e32 v66, v66, v16\n\t"
    "v_mfma_f32_16x16x32_bf16 v[20:23], a[68:71], v[188:191], v[20:23]\n\t"
    "v_cvt_pk_bf16_f32 v28, v24, v25\n\t"
    "v_cvt_pk_bf16_f32 v29, v26, v27\n\t"
    "v_mfma_f32_16x16x32_bf16 v[20:23], a[60:63], v[192:195], v[20:23]\n\t"
    "v_exp_f32_e32 v66, v66\n\t"
    "v_mfma_f32_16x16x32_bf16 v[20:23], a[52:55], v[196:199], v[20:23]\n\t"
    "buffer_load_dwordx4 v7, s[52:55], s61 offen offset:2048 nt lds\n\t"
    "v_sub_f32_e32 v67, v67, v16\n\t"
    "v_mfma_f32_16x16x32_bf16 v[24:27], a[96:99], v[196:199], 0\n\t"
    "s_add_u32 s61, s61, 196608\n\t"
    "v_sub_f32_e32 v48, v48, v16\n\t"
    "v_mfma_f32_16x16x32_bf16 v[24:27], a[104:107], v[192:195], v[24:27]\n\t"
    "v_exp_f32_e32 v67, v67\n\t"
    "v_mfma_f32_16x16x32_bf16 v[24:27], a[112:115], v[188:191], v[24:27]\n\t"
    "v_add_f32_e32 v19, v66, v67\n\t"
    "v_sub_f32_e32 v49, v49, v16\n\t"
    "v_mfma_f32_16x16x32_bf16 v[24:27], a[120:123], v[184:187], v[24:27]\n\t"
    "v_cvt_pk_bf16_f32 v30, v20, v21\n\t"
    "v_cvt_pk_bf16_f32 v31, v22, v23\n\t"
    "v_mfma_f32_16x16x32_bf16 v[24:27], a[128:131], v[180:183], v[24:27]\n\t"
    "v_exp_f32_e32 v48, v48\n\t"
    "v_mfma_f32_16x16x32_bf16 v[24:27], a[136:139], v[176:179], v[24:27]\n\t"
    "v_add_f32_e32 v18, v18, v48\n\t"
    "v_sub_f32_e32 v50, v50, v16\n\t"
    "v_mfma_f32_16x16x32_bf16 v[20:23], a[140:143], v[176:179], 0\n\t"
    "v_exp_f32_e32 v49, v49\n\t"
    "v_mfma_f32_16x16x32_bf16 v[20:23], a[132:135], v[180:183], v[20:23]\n\t"
    "v_add_f32_e32 v18, v18, v49\n\t"
    "v_sub_f32_e32 v51, v51, v16\n\t"
    "v_mfma_f32_16x16x32_bf16 v[20:23], a[124:127], v[184:187], v[20:23]\n\t"
    "v_exp_f32_e32 v50, v50\n\t"
    "v_mfma_f32_16x16x32_bf16 v[20:23], a[116:119], v[188:191], v[20:23]\n\t"
    "v_cvt_pk_bf16_f32 v32, v24, v25\n\t"
    "v_cvt_pk_bf16_f32 v33, v26, v27\n\t"
    "v_mfma_f32_16x16x32_bf16 v[20:23], a[108:111], v[192:195], v[20:23]\n\t"
    "v_add_f32_e32 v19, v19, v50\n\t"
    "v_sub_f32_e32 v68, v68, v16\n\t"
    "v_mfma_f32_16x16x32_bf16 v[20:23], a[100:103], v[196:199], v[20:23]\n\t"
    "v_exp_f32_e32 v51, v51\n\t"
    "v_mfma_f32_16x16x32_bf16 v[24:27], a[144:147], v[196:199], 0\n\t"
    "v_add_f32_e32 v19, v19, v51\n\t"
    "v_sub_f32_e32 v69, v69, v16\n\t"
    "v_mfma_f32_16x16x32_bf16 v[24:27], a[152:155], v[192:195], v[24:27]\n\t"
    "v_exp_f32_e32 v68, v68\n\t"
    "v_mfma_f32_16x16x32_bf16 v[24:27], a[160:163], v[188:191], v[24:27]\n\t"
    "s_add_u32 m0, s63, 15360\n\t"
    "v_add_f32_e32 v18, v18, v68\n\t"
    "v_mfma_f32_16x16x32_bf16 v[24:27], a[168:171], v[184:187], v[24:27]\n\t"
    "buffer_load_dwordx4 v7, s[52:55], s61 offen offset:0 nt lds\n\t"
    "v_cvt_pk_bf16_f32 v34, v20, v21\n\t"
    "v_mfma_f32_16x16x32_bf16 v[24:27], a[176:179], v[180:183], v[24:27]\n\t"
    "v_cvt_pk_bf16_f32 v35, v22, v23\n\t"
    "v_sub_f32_e32 v70, v70, v16\n\t"
    "v_mfma_f32_16x16x32_bf16 v[24:27], a[184:187], v[176:179], v[24:27]\n\t"
    "v_exp_f32_e32 v69, v69\n\t"
    "v_mfma_f32_16x16x32_bf16 v[36:39], v[220:223], v[12:15], 0\n\t"
    "v_add_f32_e32 v18, v18, v69\n\t"
    "v_sub_f32_e32 v71, v71, v16\n\t"
    "v_mfma_f32_16x16x32_bf16 v[36:39], v[216:219], v[28:31], v[36:39]\n\t"
    "v_exp_f32_e32 v70, v70\n\t"
    "v_mfma_f32_16x16x32_bf16 v[36:39], v[212:215], v[32:35], v[36:39]\n\t"
    "v_add_f32_e32 v19, v19, v70\n\t"
    "v_sub_f32_e32 v72, v72, v16\n\t"
    "v_mfma_f32_16x16x32_bf16 v[20:23], v[188:191], v[32:35], 0\n\t"
    "v_exp_f32_e32 v71, v71\n\t"
    "v_mfma_f32_16x16x32_bf16 v[20:23], v[192:195], v[28:31], v[20:23]\n\t"
    "v_add_f32_e32 v19, v19, v71\n\t"
    "v_sub_f32_e32 v73, v73, v16\n\t"
    "v_mfma_f32_16x16x32_bf16 v[20:23], v[196:199], v[12:15], v[20:23]\n\t"
    "v_exp_f32_e32 v72, v72\n\t"
    "v_mfma_f32_16x16x32_bf16 v[40:43], v[172:175], v[12:15], 0\n\t"
    "v_add_f32_e32 v18, v18, v72\n\t"
    "v_sub_f32_e32 v74, v74, v16\n\t"
    "v_mfma_f32_16x16x32_bf16 v[40:43], v[168:171], v[28:31], v[40:43]\n\t"
    "v_exp_f32_e32 v73, v73\n\t"
    "v_mfma_f32_16x16x32_bf16 v[40:43], v[164:167], v[32:35], v[40:43]\n\t"
    "v_add_f32_e32 v18, v18, v73\n\t"
    "v_sub_f32_e32 v75, v75, v16\n\t"
    "v_mfma_f32_16x16x32_bf16 v[44:47], v[140:143], v[32:35], 0\n\t"
    "v_exp_f32_e32 v74, v74\n\t"
    "v_mfma_f32_16x16x32_bf16 v[44:47], v[144:147], v[28:31], v[44:47]\n\t"
    "v_add_f32_e32 v19, v19, v74\n\t"
    "v_cvt_pk_bf16_f32 v52, v24, v25\n\t"
    "v_mfma_f32_16x16x32_bf16 v[44:47], v[148:151], v[12:15], v[44:47]\n\t"
    "v_exp_f32_e32 v75, v75\n\t"
    "v_mfma_f32_16x16x32_bf16 v[32:35], a[188:191], v[176:179], 0\n\t"
    "v_add_f32_e32 v19, v19, v75\n\t"
    "v_add_f32_e32 v18, v18, v19\n\t"
    "v_mfma_f32_16x16x32_bf16 v[32:35], a[180:183], v[180:183], v[32:35]\n\t"
    "buffer_load_dwordx4 v7, s[52:55], s61 offen offset:1024 nt lds\n\t"
    "v_mov_b32_e32 v19, v18\n\t"
    "v_mfma_f32_16x16x32_bf16 v[32:35], a[172:175], v[184:187], v[32:35]\n\t"
    "v_cvt_pk_bf16_f32 v53, v26, v27\n\t"
    "v_mfma_f32_16x16x32_bf16 v[32:35], a[164:167], v[188:191], v[32:35]\n\t"
    "v_permlane32_swap_b32 v18, v19\n\t"
    "v_add_f32_e32 v18, v18, v19\n\t"
    "v_mfma_f32_16x16x32_bf16 v[32:35], a[156:159], v[192:195], v[32:35]\n\t"
    "v_mov_b32_e32 v19, v18\n\t"
    "v_mfma_f32_16x16x32_bf16 v[32:35], a[148:151], v[196:199], v[32:35]\n\t"
    "v_mfma_f32_16x16x32_bf16 v[12:15], a[192:195], v[196:199], 0\n\t"
    "v_permlane16_swap_b32 v18, v19\n\t"
    "v_add_f32_e32 v18, v18, v19\n\t"
    "v_mfma_f32_16x16x32_bf16 v[12:15], a[200:203], v[192:195], v[12:15]\n\t"
    "v_rcp_f32_e32 v18, v18\n\t"
    "v_mfma_f32_16x16x32_bf16 v[12:15], a[208:211], v[188:191], v[12:15]\n\t"
    "v_mul_f32_e32 v64, v64, v18\n\t"
    "v_mul_f32_e32 v65, v65, v18\n\t"
    "v_mfma_f32_16x16x32_bf16 v[12:15], a[216:219], v[184:187], v[12:15]\n\t"
    "v_cvt_pk_bf16_f32 v54, v32, v33\n\t"
    "v_cvt_pk_bf16_f32 v55, v34, v35\n\t"
    "v_mfma_f32_16x16x32_bf16 v[12:15], a[224:227], v[180:183], v[12:15]\n\t"
    "v_cvt_pk_bf16_f32 v24, v64, v65\n\t"
    "v_mul_f32_e32 v66, v66, v18\n\t"
    "v_mfma_f32_16x16x32_bf16 v[12:15], a[232:235], v[176:179], v[12:15]\n\t"
    "v_mul_f32_e32 v67, v67, v18\n\t"
    "v_cvt_pk_bf16_f32 v25, v66, v67\n\t"
    "v_mfma_f32_16x16x32_bf16 v[28:31], a[236:239], v[176:179], 0\n\t"
    "v_mul_f32_e32 v48, v48, v18\n\t"
    "v_mul_f32_e32 v49, v49, v18\n\t"
    "v_mfma_f32_16x16x32_bf16 v[28:31], a[228:231], v[180:183], v[28:31]\n\t"
    "v_cvt_pk_bf16_f32 v26, v48, v49\n\t"
    "v_mul_f32_e32 v50, v50, v18\n\t"
    "v_mfma_f32_16x16x32_bf16 v[28:31], a[220:223], v[184:187], v[28:31]\n\t"
    "v_mul_f32_e32 v51, v51, v18\n\t"
    "v_cvt_pk_bf16_f32 v27, v50, v51\n\t"
    "v_mfma_f32_16x16x32_bf16 v[28:31], a[212:215], v[188:191], v[28:31]\n\t"
    "v_cvt_pk_bf16_f32 v32, v12, v13\n\t"
    "v_cvt_pk_bf16_f32 v33, v14, v15\n\t"
    "v_mfma_f32_16x16x32_bf16 v[28:31], a[204:207], v[192:195], v[28:31]\n\t"
    "v_mul_f32_e32 v68, v68, v18\n\t"
    "v_mul_f32_e32 v69, v69, v18\n\t"
    "v_mfma_f32_16x16x32_bf16 v[28:31], a[196:199], v[196:199], v[28:31]\n\t"
    "buffer_load_dwordx4 v7, s[52:55], s61 offen offset:2048 nt lds\n\t"
    "v_cvt_pk_bf16_f32 v48, v68, v69\n\t"
    "v_mfma_f32_16x16x32_bf16 v[12:15], a[240:243], v[196:199], 0\n\t"
    "s_add_u32 s61, s61, 196608\n\t"
    "v_mul_f32_e32 v70, v70, v18\n\t"
    "v_mfma_f32_16x16x32_bf16 v[12:15], a[248:251], v[192:195], v[12:15]\n\t"
    "v_mul_f32_e32 v71, v71, v18\n\t"
    "v_cvt_pk_bf16_f32 v49, v70, v71\n\t"
    "v_mfma_f32_16x16x32_bf16 v[12:15], v[224:227], v[188:191], v[12:15]\n\t"
    "v_mul_f32_e32 v72, v72, v18\n\t"
    "v_mul_f32_e32 v73, v73, v18\n\t"
    "v_mfma_f32_16x16x32_bf16 v[12:15], v[232:235], v[184:187], v[12:15]\n\t"
    "v_cvt_pk_bf16_f32 v34, v28, v29\n\t"
    "v_cvt_pk_bf16_f32 v35, v30, v31\n\t"
    "v_mfma_f32_16x16x32_bf16 v[12:15], v[240:243], v[180:183], v[12:15]\n\t"
    "v_cvt_pk_bf16_f32 v50, v72, v73\n\t"
    "v_mul_f32_e32 v74, v74, v18\n\t"
    "v_mfma_f32_16x16x32_bf16 v[12:15], v[248:251], v[176:179], v[12:15]\n\t"
    "v_mul_f32_e32 v75, v75, v18\n\t"
    "v_cvt_pk_bf16_f32 v51, v74, v75\n\t"
    "v_mfma_f32_16x16x32_bf16 v[28:31], v[252:255], v[176:179], 0\n\t"
    "v_mfma_f32_16x16x32_bf16 v[28:31], v[244:247], v[180:183], v[28:31]\n\t"
    "v_mfma_f32_16x16x32_bf16 v[28:31], v[236:239], v[184:187], v[28:31]\n\t"
    "v_mfma_f32_16x16x32_bf16 v[28:31], v[228:231], v[188:191], v[28:31]\n\t"
    "v_cvt_pk_bf16_f32 v16, v12, v13\n\t"
    "v_cvt_pk_bf16_f32 v17, v14, v15\n\t"
    "v_mfma_f32_16x16x32_bf16 v[28:31], a[252:255], v[192:195], v[28:31]\n\t"
    "v_mfma_f32_16x16x32_bf16 v[28:31], a[244:247], v[196:199], v[28:31]\n\t"
    "v_mfma_f32_16x16x32_bf16 v[12:15], a[0:3], v[172:175], 0\n\t"
    "ds_write_b128 v6, v[24:27] offset:0\n\t"
    "v_mfma_f32_16x16x32_bf16 v[12:15], a[8:11], v[168:171], v[12:15]\n\t"
    "v_mfma_f32_16x16x32_bf16 v[12:15], a[16:19], v[164:167], v[12:15]\n\t"
    "s_add_u32 m0, s63, 18432\n\t"
    "v_mfma_f32_16x16x32_bf16 v[12:15], a[24:27], v[160:163], v[12:15]\n\t"
    "buffer_load_dwordx4 v7, s[52:55], s61 offen offset:0 nt lds\n\t"
    "v_cvt_pk_bf16_f32 v18, v28, v29\n\t"
    "v_mfma_f32_16x16x32_bf16 v[12:15], a[32:35], v[156:159], v[12:15]\n\t"
    "ds_write_b128 v6, v[48:51] offset:1024\n\t"
    "v_mfma_f32_16x16x32_bf16 v[12:15], a[40:43], v[152:155], v[12:15]\n\t"
    "v_cvt_pk_bf16_f32 v19, v30, v31\n\t"
    "v_mfma_f32_16x16x32_bf16 v[36:39], v[208:211], v[52:55], v[36:39]\n\t"
    "v_mfma_f32_16x16x32_bf16 v[36:39], v[204:207], v[32:35], v[36:39]\n\t"
    "v_mfma_f32_16x16x32_bf16 v[36:39], v[200:203], v[16:19], v[36:39]\n\t"
    "s_add_i32 s64, s62, 24576\n\t"
    "v_mfma_f32_16x16x32_bf16 v[20:23], v[176:179], v[16:19], v[20:23]\n\t"
    "s_cmp_eq_u32 s64, %[rend]\n\t"
    "s_cselect_b32 s64, %[rbeg], s64\n\t"
    "v_mfma_f32_16x16x32_bf16 v[20:23], v[180:183], v[32:35], v[20:23]\n\t"
    "s_sub_i32 s65, s63, s64\n\t"
    "v_cvt_pk_bf16_f32 v24, v12, v13\n\t"
    "v_mfma_f32_16x16x32_bf16 v[20:23], v[184:187], v[52:55], v[20:23]\n\t"
    "v_add_u32_e32 v0, s65, v0\n\t"
    "v_add_u32_e32 v1, s65, v1\n\t"
    "v_mfma_f32_16x16x32_bf16 v[40:43], v[160:163], v[52:55], v[40:43]\n\t"
    "v_add_u32_e32 v2, s65, v2\n\t"
    "v_add_u32_e32 v3, s65, v3\n\t"
    "v_mfma_f32_16x16x32_bf16 v[40:43], v[156:159], v[32:35], v[40:43]\n\t"
    "v_add_u32_e32 v4, s65, v4\n\t"
    "v_add_u32_e32 v5, s65, v5\n\t"
    "v_mfma_f32_16x16x32_bf16 v[40:43], v[152:155], v[16:19], v[40:43]\n\t"
    "v_max3_f32 v28, v36, v37, v38\n\t"
    "v_cvt_pk_bf16_f32 v25, v14, v15\n\t"
    "v_mfma_f32_16x16x32_bf16 v[44:47], v[128:131], v[16:19], v[44:47]\n\t"
    "v_max3_f32 v29, v39, v20, v21\n\t"
    "v_max3_f32 v28, v28, v22, v23\n\t"
    "v_mfma_f32_16x16x32_bf16 v[44:47], v[132:135], v[32:35], v[44:47]\n\t"
    "v_mfma_f32_16x16x32_bf16 v[44:47], v[136:139], v[52:55], v[44:47]\n\t"
    "v_mfma_f32_16x16x32_bf16 v[12:15], a[44:47], v[152:155], 0\n\t"
    "v_max3_f32 v29, v29, v40, v41\n\t"
    "v_max3_f32 v28, v28, v42, v43\n\t"
    "v_mfma_f32_16x16x32_bf16 v[12:15], a[36:39], v[156:159], v[12:15]\n\t"
    "buffer_load_dwordx4 v7, s[52:55], s61 offen offset:1024 nt lds\n\t"
    "v_mfma_f32_16x16x32_bf16 v[12:15], a[28:31], v[160:163], v[12:15]\n\t"
    "v_mfma_f32_16x16x32_bf16 v[12:15], a[20:23], v[164:167], v[12:15]\n\t"
    "v_max3_f32 v29, v29, v44, v45\n\t"
    "v_max3_f32 v28, v28, v46, v47\n\t"
    "v_mfma_f32_16x16x32_bf16 v[12:15], a[12:15], v[168:171], v[12:15]\n\t"
    "v_max_f32_e32 v28, v28, v29\n\t"
    "v_mov_b32_e32 v29, v28\n\t"
    "v_mfma_f32_16x16x32_bf16 v[12:15], a[4:7], v[172:175], v[12:15]\n\t"
    "v_mfma_f32_16x16x32_bf16 v[16:19], a[48:51], v[172:175], 0\n\t"
    "v_permlane32_swap_b32 v28, v29\n\t"
    "v_max_f32_e32 v28, v28, v29\n\t"
    "v_mfma_f32_16x16x32_bf16 v[16:19], a[56:59], v[168:171], v[16:19]\n\t"
    "v_mov_b32_e32 v29, v28\n\t"
    "v_mfma_f32_16x16x32_bf16 v[16:19], a[64:67], v[164:167], v[16:19]\n\t"
    "v_mfma_f32_16x16x32_bf16 v[16:19], a[72:75], v[160:163], v[16:19]\n\t"
    "v_cvt_pk_bf16_f32 v26, v12, v13\n\t"
    "v_cvt_pk_bf16_f32 v27, v14, v15\n\t"
    "v_mfma_f32_16x16x32_bf16 v[16:19], a[80:83], v[156:159], v[16:19]\n\t"
    "v_permlane16_swap_b32 v28, v29\n\t"
    "v_max_f32_e32 v28, v28, v29\n\t"
    "v_mfma_f32_16x16x32_bf16 v[16:19], a[88:91], v[152:155], v[16:19]\n\t"
    "v_sub_f32_e32 v36, v36, v28\n\t"
    "v_sub_f32_e32 v37, v37, v28\n\t"
    "v_mfma_f32_16x16x32_bf16 v[12:15], a[92:95], v[152:155], 0\n\t"
    "v_exp_f32_e32 v36, v36\n\t"
    "v_mfma_f32_16x16x32_bf16 v[12:15], a[84:87], v[156:159], v[12:15]\n\t"
    "v_exp_f32_e32 v37, v37\n\t"
    "v_mfma_f32_16x16x32_bf16 v[12:15], a[76:79], v[160:163], v[12:15]\n\t"
    "v_add_f32_e32 v30, v36, v37\n\t"
    "v_sub_f32_e32 v38, v38, v28\n\t"
    "v_mfma_f32_16x16x32_bf16 v[12:15], a[68:71], v[164:167], v[12:15]\n\t"
    "v_cvt_pk_bf16_f32 v32, v16, v17\n\t"
    "v_cvt_pk_bf16_f32 v33, v18, v19\n\t"
    "v_mfma_f32_16x16x32_bf16 v[12:15], a[60:63], v[168:171], v[12:15]\n\t"
    "v_exp_f32_e32 v38, v38\n\t"
    "v_mfma_f32_16x16x32_bf16 v[12:15], a[52:55], v[172:175], v[12:15]\n\t"
    "buffer_load_dwordx4 v7, s[52:55], s61 offen offset:2048 nt lds\n\t"
    "v_sub_f32_e32 v39, v39, v28\n\t"
    "v_mfma_f32_16x16x32_bf16 v[16:19], a[96:99], v[172:175], 0\n\t"
    "s_add_u32 s61, s61, 196608\n\t"
    "v_sub_f32_e32 v20, v20, v28\n\t"
    "v_mfma_f32_16x16x32_bf16 v[16:19], a[104:107], v[168:171], v[16:19]\n\t"
    "v_exp_f32_e32 v39, v39\n\t"
    "v_mfma_f32_16x16x32_bf16 v[16:19], a[112:115], v[164:167], v[16:19]\n\t"
    "v_add_f32_e32 v31, v38, v39\n\t"
    "v_sub_f32_e32 v21, v21, v28\n\t"
    "v_mfma_f32_16x16x32_bf16 v[16:19], a[120:123], v[160:163], v[16:19]\n\t"
    "v_cvt_pk_bf16_f32 v34, v12, v13\n\t"
    "v_cvt_pk_bf16_f32 v35, v14, v15\n\t"
    "v_mfma_f32_16x16x32_bf16 v[16:19], a[128:131], v[156:159], v[16:19]\n\t"
    "v_exp_f32_e32 v20, v20\n\t"
    "v_mfma_f32_16x16x32_bf16 v[16:19], a[136:139], v[152:155], v[16:19]\n\t"
    "v_add_f32_e32 v30, v30, v20\n\t"
    "v_sub_f32_e32 v22, v22, v28\n\t"
    "v_mfma_f32_16x16x32_bf16 v[12:15], a[140:143], v[152:155], 0\n\t"
    "v_exp_f32_e32 v21, v21\n\t"
    "v_mfma_f32_16x16x32_bf16 v[12:15], a[132:135], v[156:159], v[12:15]\n\t"
    "v_add_f32_e32 v30, v30, v21\n\t"
    "v_sub_f32_e32 v23, v23, v28\n\t"
    "v_mfma_f32_16x16x32_bf16 v[12:15], a[124:127], v[160:163], v[12:15]\n\t"
    "v_exp_f32_e32 v22, v22\n\t"
    "v_mfma_f32_16x16x32_bf16 v[12:15], a[116:119], v[164:167], v[12:15]\n\t"
    "v_cvt_pk_bf16_f32 v48, v16, v17\n\t"
    "v_cvt_pk_bf16_f32 v49, v18, v19\n\t"
    "v_mfma_f32_16x16x32_bf16 v[12:15], a[108:111], v[168:171], v[12:15]\n\t"
    "v_add_f32_e32 v31, v31, v22\n\t"
    "v_sub_f32_e32 v40, v40, v28\n\t"
    "v_mfma_f32_16x16x32_bf16 v[12:15], a[100:103], v[172:175], v[12:15]\n\t"
    "v_exp_f32_e32 v23, v23\n\t"
    "v_mfma_f32_16x16x32_bf16 v[16:19], a[144:147], v[172:175], 0\n\t"
    "v_add_f32_e32 v31, v31, v23\n\t"
    "v_sub_f32_e32 v41, v41, v28\n\t"
    "v_mfma_f32_16x16x32_bf16 v[16:19], a[152:155], v[168:171], v[16:19]\n\t"
    "v_exp_f32_e32 v40, v40\n\t"
    "v_mfma_f32_16x16x32_bf16 v[16:19], a[160:163], v[164:167], v[16:19]\n\t"
    "s_add_u32 m0, s63, 21504\n\t"
    "v_add_f32_e32 v30, v30, v40\n\t"
    "v_mfma_f32_16x16x32_bf16 v[16:19], a[168:171], v[160:163], v[16:19]\n\t"
    "buffer_load_dwordx4 v7, s[52:55], s61 offen offset:0 nt lds\n\t"
    "v_cvt_pk_bf16_f32 v50, v12, v13\n\t"
    "v_mfma_f32_16x16x32_bf16 v[16:19], a[176:179], v[156:159], v[16:19]\n\t"
    "v_cvt_pk_bf16_f32 v51, v14, v15\n\t"
    "v_sub_f32_e32 v42, v42, v28\n\t"
    "v_mfma_f32_16x16x32_bf16 v[16:19], a[184:187], v[152:155], v[16:19]\n\t"
    "v_exp_f32_e32 v41, v41\n\t"
    "v_mfma_f32_16x16x32_bf16 v[60:63], v[220:223], v[24:27], 0\n\t"
    "v_add_f32_e32 v30, v30, v41\n\t"
    "v_sub_f32_e32 v43, v43, v28\n\t"
    "v_mfma_f32_16x16x32_bf16 v[60:63], v[216:219], v[32:35], v[60:63]\n\t"
    "v_exp_f32_e32 v42, v42\n\t"
    "v_mfma_f32_16x16x32_bf16 v[60:63], v[212:215], v[48:51], v[60:63]\n\t"
    "v_add_f32_e32 v31, v31, v42\n\t"
    "v_sub_f32_e32 v44, v44, v28\n\t"
    "v_mfma_f32_16x16x32_bf16 v[64:67], v[188:191], v[48:51], 0\n\t"
    "v_exp_f32_e32 v43, v43\n\t"
    "v_mfma_f32_16x16x32_bf16 v[64:67], v[192:195], v[32:35], v[64:67]\n\t"
    "v_add_f32_e32 v31, v31, v43\n\t"
    "v_sub_f32_e32 v45, v45, v28\n\t"
    "v_mfma_f32_16x16x32_bf16 v[64:67], v[196:199], v[24:27], v[64:67]\n\t"
    "v_exp_f32_e32 v44, v44\n\t"
    "v_mfma_f32_16x16x32_bf16 v[68:71], v[172:175], v[24:27], 0\n\t"
    "v_add_f32_e32 v30, v30, v44\n\t"
    "v_sub_f32_e32 v46, v46, v28\n\t"
    "v_mfma_f32_16x16x32_bf16 v[68:71], v[168:171], v[32:35], v[68:71]\n\t"
    "v_exp_f32_e32 v45, v45\n\t"
    "v_mfma_f32_16x16x32_bf16 v[68:71], v[164:167], v[48:51], v[68:71]\n\t"
    "v_add_f32_e32 v30, v30, v45\n\t"
    "v_sub_f32_e32 v47, v47, v28\n\t"
    "v_mfma_f32_16x16x32_bf16 v[12:15], v[140:143], v[48:51], 0\n\t"
    "v_exp_f32_e32 v46, v46\n\t"
    "v_mfma_f32_16x16x32_bf16 v[12:15], v[144:147], v[32:35], v[12:15]\n\t"
    "v_add_f32_e32 v31, v31, v46\n\t"
    "v_cvt_pk_bf16_f32 v52, v16, v17\n\t"
    "v_mfma_f32_16x16x32_bf16 v[12:15], v[148:151], v[24:27], v[12:15]\n\t"
    "v_exp_f32_e32 v47, v47\n\t"
    "v_mfma_f32_16x16x32_bf16 v[48:51], a[188:191], v[152:155], 0\n\t"
    "v_add_f32_e32 v31, v31, v47\n\t"
    "v_add_f32_e32 v30, v30, v31\n\t"
    "v_mfma_f32_16x16x32_bf16 v[48:51], a[180:183], v[156:159], v[48:51]\n\t"
    "buffer_load_dwordx4 v7, s[52:55], s61 offen offset:1024 nt lds\n\t"
    "v_mov_b32_e32 v31, v30\n\t"
    "v_mfma_f32_16x16x32_bf16 v[48:51], a[172:175], v[160:163], v[48:51]\n\t"
    "v_cvt_pk_bf16_f32 v53, v18, v19\n\t"
    "v_mfma_f32_16x16x32_bf16 v[48:51], a[164:167], v[164:167], v[48:51]\n\t"
    "v_permlane32_swap_b32 v30, v31\n\t"
    "v_add_f32_e32 v30, v30, v31\n\t"
    "v_mfma_f32_16x16x32_bf16 v[48:51], a[156:159], v[168:171], v[48:51]\n\t"
    "v_mov_b32_e32 v31, v30\n\t"
    "v_mfma_f32_16x16x32_bf16 v[48:51], a[148:151], v[172:175], v[48:51]\n\t"
    "v_mfma_f32_16x16x32_bf16 v[16:19], a[192:195], v[172:175], 0\n\t"
    "v_permlane16_swap_b32 v30, v31\n\t"
    "v_add_f32_e32 v30, v30, v31\n\t"
    "v_mfma_f32_16x16x32_bf16 v[16:19], a[200:203], v[168:171], v[16:19]\n\t"
    "v_rcp_f32_e32 v30, v30\n\t"
    "v_mfma_f32_16x16x32_bf16 v[16:19], a[208:211], v[164:167], v[16:19]\n\t"
    "v_mul_f32_e32 v36, v36, v30\n\t"
    "v_mul_f32_e32 v37, v37, v30\n\t"
    "v_mfma_f32_16x16x32_bf16 v[16:19], a[216:219], v[160:163], v[16:19]\n\t"
    "v_cvt_pk_bf16_f32 v54, v48, v49\n\t"
    "v_cvt_pk_bf16_f32 v55, v50, v51\n\t"
    "v_mfma_f32_16x16x32_bf16 v[16:19], a[224:227], v[156:159], v[16:19]\n\t"
    "v_cvt_pk_bf16_f32 v24, v36, v37\n\t"
    "v_mul_f32_e32 v38, v38, v30\n\t"
    "v_mfma_f32_16x16x32_bf16 v[16:19], a[232:235], v[152:155], v[16:19]\n\t"
    "v_mul_f32_e32 v39, v39, v30\n\t"
    "v_cvt_pk_bf16_f32 v25, v38, v39\n\t"
    "v_mfma_f32_16x16x32_bf16 v[32:35], a[236:239], v[152:155], 0\n\t"
    "v_mul_f32_e32 v20, v20, v30\n\t"
    "v_mul_f32_e32 v21, v21, v30\n\t"
    "v_mfma_f32_16x16x32_bf16 v[32:35], a[228:231], v[156:159], v[32:35]\n\t"
    "v_cvt_pk_bf16_f32 v26, v20, v21\n\t"
    "v_mul_f32_e32 v22, v22, v30\n\t"
    "v_mfma_f32_16x16x32_bf16 v[32:35], a[220:223], v[160:163], v[32:35]\n\t"
    "v_mul_f32_e32 v23, v23, v30\n\t"
    "v_cvt_pk_bf16_f32 v27, v22, v23\n\t"
    "v_mfma_f32_16x16x32_bf16 v[32:35], a[212:215], v[164:167], v[32:35]\n\t"
    "v_cvt_pk_bf16_f32 v36, v16, v17\n\t"
    "v_cvt_pk_bf16_f32 v37, v18, v19\n\t"
    "v_mfma_f32_16x16x32_bf16 v[32:35], a[204:207], v[168:171], v[32:35]\n\t"
    "v_mul_f32_e32 v40, v40, v30\n\t"
    "v_mul_f32_e32 v41, v41, v30\n\t"
    "v_mfma_f32_16x16x32_bf16 v[32:35], a[196:199], v[172:175], v[32:35]\n\t"
    "buffer_load_dwordx4 v7, s[52:55], s61 offen offset:2048 nt lds\n\t"
    "v_cvt_pk_bf16_f32 v20, v40, v41\n\t"
    "v_mfma_f32_16x16x32_bf16 v[16:19], a[240:243], v[172:175], 0\n\t"
    "s_add_u32 s61, s61, 11206656\n\t"
    "v_mul_f32_e32 v42, v42, v30\n\t"
    "v_mfma_f32_16x16x32_bf16 v[16:19], a[248:251], v[168:171], v[16:19]\n\t"
    "v_mul_f32_e32 v43, v43, v30\n\t"
    "v_cvt_pk_bf16_f32 v21, v42, v43\n\t"
    "v_mfma_f32_16x16x32_bf16 v[16:19], v[224:227], v[164:167], v[16:19]\n\t"
    "v_mul_f32_e32 v44, v44, v30\n\t"
    "v_mul_f32_e32 v45, v45, v30\n\t"
    "v_mfma_f32_16x16x32_bf16 v[16:19], v[232:235], v[160:163], v[16:19]\n\t"
    "v_cvt_pk_bf16_f32 v38, v32, v33\n\t"
    "v_cvt_pk_bf16_f32 v39, v34, v35\n\t"
    "v_mfma_f32_16x16x32_bf16 v[16:19], v[240:243], v[156:159], v[16:19]\n\t"
    "v_cvt_pk_bf16_f32 v22, v44, v45\n\t"
    "v_mul_f32_e32 v46, v46, v30\n\t"
    "v_mfma_f32_16x16x32_bf16 v[16:19], v[248:251], v[152:155], v[16:19]\n\t"
    "v_mul_f32_e32 v47, v47, v30\n\t"
    "v_cvt_pk_bf16_f32 v23, v46, v47\n\t"
    "v_mfma_f32_16x16x32_bf16 v[32:35], v[252:255], v[152:155], 0\n\t"
    "v_mfma_f32_16x16x32_bf16 v[32:35], v[244:247], v[156:159], v[32:35]\n\t"
    "v_mfma_f32_16x16x32_bf16 v[32:35], v[236:239], v[160:163], v[32:35]\n\t"
    "v_mfma_f32_16x16x32_bf16 v[32:35], v[228:231], v[164:167], v[32:35]\n\t"
    "v_cvt_pk_bf16_f32 v28, v16, v17\n\t"
    "v_cvt_pk_bf16_f32 v29, v18, v19\n\t"
    "v_mfma_f32_16x16x32_bf16 v[32:35], a[252:255], v[168:171], v[32:35]\n\t"
    "v_mfma_f32_16x16x32_bf16 v[32:35], a[244:247], v[172:175], v[32:35]\n\t"
    "v_mfma_f32_16x16x32_bf16 v[16:19], a[0:3], v[148:151], 0\n\t"
    "ds_write_b128 v6, v[24:27] offset:2048\n\t"
    "v_mfma_f32_16x16x32_bf16 v[16:19], a[8:11], v[144:147], v[16:19]\n\t"
    "v_mfma_f32_16x16x32_bf16 v[16:19], a[16:19], v[140:143], v[16:19]\n\t"
    "s_add_u32 m0, s62, 0\n\t"
    "v_mfma_f32_16x16x32_bf16 v[16:19], a[24:27], v[136:139], v[16:19]\n\t"
    "buffer_load_dwordx4 v7, s[52:55], s61 offen offset:0 nt lds\n\t"
    "v_cvt_pk_bf16_f32 v30, v32, v33\n\t"
    "v_mfma_f32_16x16x32_bf16 v[16:19], a[32:35], v[132:135], v[16:19]\n\t"
    "ds_write_b128 v6, v[20:23] offset:3072\n\t"
    "v_mfma_f32_16x16x32_bf16 v[16:19], a[40:43], v[128:131], v[16:19]\n\t"
    "v_cvt_pk_bf16_f32 v31, v34, v35\n\t"
    "v_mfma_f32_16x16x32_bf16 v[60:63], v[208:211], v[52:55], v[60:63]\n\t"
    "v_mfma_f32_16x16x32_bf16 v[60:63], v[204:207], v[36:39], v[60:63]\n\t"
    "v_mfma_f32_16x16x32_bf16 v[60:63], v[200:203], v[28:31], v[60:63]\n\t"
    "v_mfma_f32_16x16x32_bf16 v[64:67], v[176:179], v[28:31], v[64:67]\n\t"
    "v_cvt_pk_bf16_f32 v44, v16, v17\n\t"
    "v_cvt_pk_bf16_f32 v45, v18, v19\n\t"
    "v_mfma_f32_16x16x32_bf16 v[64:67], v[180:183], v[36:39], v[64:67]\n\t"
    "v_mfma_f32_16x16x32_bf16 v[64:67], v[184:187], v[52:55], v[64:67]\n\t"
    "v_mfma_f32_16x16x32_bf16 v[68:71], v[160:163], v[52:55], v[68:71]\n\t"
    "v_max3_f32 v16, v60, v61, v62\n\t"
    "v_mfma_f32_16x16x32_bf16 v[68:71], v[156:159], v[36:39], v[68:71]\n\t"
    "v_mfma_f32_16x16x32_bf16 v[68:71], v[152:155], v[28:31], v[68:71]\n\t"
    "v_mfma_f32_16x16x32_bf16 v[12:15], v[128:131], v[28:31], v[12:15]\n\t"
    "v_max3_f32 v17, v63, v64, v65\n\t"
    "v_max3_f32 v16, v16, v66, v67\n\t"
    "v_mfma_f32_16x16x32_bf16 v[12:15], v[132:135], v[36:39], v[12:15]\n\t"
    "v_mfma_f32_16x16x32_bf16 v[12:15], v[136:139], v[52:55], v[12:15]\n\t"
    "v_mfma_f32_16x16x32_bf16 v[20:23], a[44:47], v[128:131], 0\n\t"
    "v_max3_f32 v17, v17, v68, v69\n\t"
    "v_max3_f32 v16, v16, v70, v71\n\t"
    "v_mfma_f32_16x16x32_bf16 v[20:23], a[36:39], v[132:135], v[20:23]\n\t"
    "buffer_load_dwordx4 v7, s[52:55], s61 offen offset:1024 nt lds\n\t"
    "v_mfma_f32_16x16x32_bf16 v[20:23], a[28:31], v[136:139], v[20:23]\n\t"
    "v_mfma_f32_16x16x32_bf16 v[20:23], a[20:23], v[140:143], v[20:23]\n\t"
    "v_max3_f32 v17, v17, v12, v13\n\t"
    "v_max3_f32 v16, v16, v14, v15\n\t"
    "v_mfma_f32_16x16x32_bf16 v[20:23], a[12:15], v[144:147], v[20:23]\n\t"
    "v_max_f32_e32 v16, v16, v17\n\t"
    "v_mov_b32_e32 v17, v16\n\t"
    "v_mfma_f32_16x16x32_bf16 v[20:23], a[4:7], v[148:151], v[20:23]\n\t"
    "v_mfma_f32_16x16x32_bf16 v[24:27], a[48:51], v[148:151], 0\n\t"
    "v_permlane32_swap_b32 v16, v17\n\t"
    "v_max_f32_e32 v16, v16, v17\n\t"
    "v_mfma_f32_16x16x32_bf16 v[24:27], a[56:59], v[144:147], v[24:27]\n\t"
    "v_mov_b32_e32 v17, v16\n\t"
    "v_mfma_f32_16x16x32_bf16 v[24:27], a[64:67], v[140:143], v[24:27]\n\t"
    "v_mfma_f32_16x16x32_bf16 v[24:27], a[72:75], v[136:139], v[24:27]\n\t"
    "v_cvt_pk_bf16_f32 v46, v20, v21\n\t"
    "v_cvt_pk_bf16_f32 v47, v22, v23\n\t"
    "v_mfma_f32_16x16x32_bf16 v[24:27], a[80:83], v[132:135], v[24:27]\n\t"
    "v_permlane16_swap_b32 v16, v17\n\t"
    "v_max_f32_e32 v16, v16, v17\n\t"
    "v_mfma_f32_16x16x32_bf16 v[24:27], a[88:91], v[128:131], v[24:27]\n\t"
    "v_sub_f32_e32 v60, v60, v16\n\t"
    "v_sub_f32_e32 v61, v61, v16\n\t"
    "v_mfma_f32_16x16x32_bf16 v[20:23], a[92:95], v[128:131], 0\n\t"
    "v_exp_f32_e32 v60, v60\n\t"
    "v_mfma_f32_16x16x32_bf16 v[20:23], a[84:87], v[132:135], v[20:23]\n\t"
    "v_exp_f32_e32 v61, v61\n\t"
    "v_mfma_f32_16x16x32_bf16 v[20:23], a[76:79], v[136:139], v[20:23]\n\t"
    "v_add_f32_e32 v18, v60, v61\n\t"
    "v_sub_f32_e32 v62, v62, v16\n\t"
    "v_mfma_f32_16x16x32_bf16 v[20:23], a[68:71], v[140:143], v[20:23]\n\t"
    "v_cvt_pk_bf16_f32 v48, v24, v25\n\t"
    "v_cvt_pk_bf16_f32 v49, v26, v27\n\t"
    "v_mfma_f32_16x16x32_bf16 v[20:23], a[60:63], v[144:147], v[20:23]\n\t"
    "v_exp_f32_e32 v62, v62\n\t"
    "v_mfma_f32_16x16x32_bf16 v[20:23], a[52:55], v[148:151], v[20:23]\n\t"
    "buffer_load_dwordx4 v7, s[52:55], s61 offen offset:2048 nt lds\n\t"
    "v_sub_f32_e32 v63, v63, v16\n\t"
    "v_mfma_f32_16x16x32_bf16 v[24:27], a[96:99], v[148:151], 0\n\t"
    ".Lh6_%=:\n\t"
    "s_cbranch_execz .LrM_%=\n\t"
    "s_add_u32 s61, s61, 196608\n\t"
    "v_sub_f32_e32 v64, v64, v16\n\t"
    "v_mfma_f32_16x16x32_bf16 v[24:27], a[104:107], v[144:147], v[24:27]\n\t"
    "v_exp_f32_e32 v63, v63\n\t"
    "v_mfma_f32_16x16x32_bf16 v[24:27], a[112:115], v[140:143], v[24:27]\n\t"
    "v_add_f32_e32 v19, v62, v63\n\t"
    "v_sub_f32_e32 v65, v65, v16\n\t"
    "v_mfma_f32_16x16x32_bf16 v[24:27], a[120:123], v[136:139], v[24:27]\n\t"
    "v_cvt_pk_bf16_f32 v50, v20, v21\n\t"
    "v_cvt_pk_bf16_f32 v51, v22, v23\n\t"
    "v_mfma_f32_16x16x32_bf16 v[24:27], a[128:131], v[132:135], v[24:27]\n\t"
    "v_exp_f32_e32 v64, v64\n\t"
    "v_mfma_f32_16x16x32_bf16 v[24:27], a[136:139], v[128:131], v[24:27]\n\t"
    "v_add_f32_e32 v18, v18, v64\n\t"
    "v_sub_f32_e32 v66, v66, v16\n\t"
    "v_mfma_f32_16x16x32_bf16 v[28:31], a[140:143], v[128:131], 0\n\t"
    "v_exp_f32_e32 v65, v65\n\t"
    "v_mfma_f32_16x16x32_bf16 v[28:31], a[132:135], v[132:135], v[28:31]\n\t"
    "v_add_f32_e32 v18, v18, v65\n\t"
    "v_sub_f32_e32 v67, v67, v16\n\t"
    "v_mfma_f32_16x16x32_bf16 v[28:31], a[124:127], v[136:139], v[28:31]\n\t"
    "v_exp_f32_e32 v66, v66\n\t"
    "v_mfma_f32_16x16x32_bf16 v[28:31], a[116:119], v[140:143], v[28:31]\n\t"
    "v_cvt_pk_bf16_f32 v40, v24, v25\n\t"
    "v_cvt_pk_bf16_f32 v41, v26, v27\n\t"
    "v_mfma_f32_16x16x32_bf16 v[28:31], a[108:111], v[144:147], v[28:31]\n\t"
    "v_add_f32_e32 v19, v19, v66\n\t"
    "v_sub_f32_e32 v68, v68, v16\n\t"
    "v_mfma_f32_16x16x32_bf16 v[28:31], a[100:103], v[148:151], v[28:31]\n\t"
    "v_exp_f32_e32 v67, v67\n\t"
    "v_mfma_f32_16x16x32_bf16 v[52:55], a[144:147], v[148:151], 0\n\t"
    "v_add_f32_e32 v19, v19, v67\n\t"
    "v_sub_f32_e32 v69, v69, v16\n\t"
    "v_mfma_f32_16x16x32_bf16 v[52:55], a[152:155], v[144:147], v[52:55]\n\t"
    "v_exp_f32_e32 v68, v68\n\t"
    "s_branch .Lmid_%=\n\t"
    ".Ltail_%=:\n\t"
    "v_mbcnt_lo_u32_b32 v8, -1, 0\n\t"
    "v_mbcnt_hi_u32_b32 v8, -1, v8\n\t"
    "v_lshlrev_b32_e32 v8, 5, v8\n\t"
    "s_nop 7\n\t"
    "s_nop 1\n\t"
    "ds_write_b128 v6, v[52:55] offset:4096\n\t"
    "v_mul_f32_e32 v15, v15, v18\n\t"
    "v_cvt_pk_bf16_f32 v63, v14, v15\n\t"
    "v_cvt_pk_bf16_f32 v70, v64, v65\n\t"
    "ds_write_b128 v6, v[60:63] offset:5120\n\t"
    "v_cvt_pk_bf16_f32 v71, v66, v67\n\t"
    "s_nop 1\n\t"
    "v_mfma_f32_16x16x32_bf16 v[20:23], v[208:211], v[48:51], v[20:23]\n\t"
    "s_nop 1\n\t"
    "v_mfma_f32_16x16x32_bf16 v[20:23], v[204:207], v[56:59], v[20:23]\n\t"
    "s_nop 1\n\t"
    "v_mfma_f32_16x16x32_bf16 v[20:23], v[200:203], v[68:71], v[20:23]\n\t"
    "s_nop 1\n\t"
    "v_mfma_f32_16x16x32_bf16 v[24:27], v[176:179], v[68:71], v[24:27]\n\t"
    "s_nop 1\n\t"
    "v_mfma_f32_16x16x32_bf16 v[24:27], v[180:183], v[56:59], v[24:27]\n\t"
    "s_waitcnt lgkmcnt(0)\n\t"
    "s_barrier\n\t"
    "s_nop 1\n\t"
    "v_mfma_f32_16x16x32_bf16 v[24:27], v[184:187], v[48:51], v[24:27]\n\t"
    "s_nop 1\n\t"
    "v_mfma_f32_16x16x32_bf16 v[28:31], v[160:163], v[48:51], v[28:31]\n\t"
    "v_max3_f32 v16, v20, v21, v22\n\t"
    "s_nop 1\n\t"
    "v_mfma_f32_16x16x32_bf16 v[28:31], v[156:159], v[56:59], v[28:31]\n\t"
    "s_nop 1\n\t"
    "v_mfma_f32_16x16x32_bf16 v[28:31], v[152:155], v[68:71], v[28:31]\n\t"
    "s_nop 1\n\t"
    "v_mfma_f32_16x16x32_bf16 v[32:35], v[128:131], v[68:71], v[32:35]\n\t"
    "v_max3_f32 v17, v23, v24, v25\n\t"
    "v_max3_f32 v16, v16, v26, v27\n\t"
    "s_nop 1\n\t"
    "v_mfma_f32_16x16x32_bf16 v[32:35], v[132:135], v[56:59], v[32:35]\n\t"
    "s_nop 1\n\t"
    "v_mfma_f32_16x16x32_bf16 v[32:35], v[136:139], v[48:51], v[32:35]\n\t"
    "s_nop 7\n\t"
    "s_nop 1\n\t"
    "v_max3_f32 v17, v17, v28, v29\n\t"
    "v_max3_f32 v16, v16, v30, v31\n\t"
    "v_max3_f32 v17, v17, v32, v33\n\t"
    "v_max3_f32 v16, v16, v34, v35\n\t"
    "v_max_f32_e32 v16, v16, v17\n\t"
    "v_mov_b32_e32 v17, v16\n\t"
    "s_nop 1\n\t"
    "v_permlane32_swap_b32 v16, v17\n\t"
    "v_max_f32_e32 v16, v16, v17\n\t"
    "v_mov_b32_e32 v17, v16\n\t"
    "s_nop 1\n\t"
    "v_permlane16_swap_b32 v16, v17\n\t"
    "v_max_f32_e32 v16, v16, v17\n\t"
    "v_sub_f32_e32 v20, v20, v16\n\t"
    "v_sub_f32_e32 v21, v21, v16\n\t"
    "v_sub_f32_e32 v22, v22, v16\n\t"
    "v_exp_f32_e32 v20, v20\n\t"
    "s_nop 0\n\t"
    "v_sub_f32_e32 v23, v23, v16\n\t"
    "v_sub_f32_e32 v24, v24, v16\n\t"
    "v_sub_f32_e32 v25, v25, v16\n\t"
    "v_exp_f32_e32 v21, v21\n\t"
    "s_nop 0\n\t"
    "v_add_f32_e32 v18, v20, v21\n\t"
    "v_sub_f32_e32 v26, v26, v16\n\t"
    "v_exp_f32_e32 v22, v22\n\t"
    "s_nop 0\n\t"
    "v_exp_f32_e32 v23, v23\n\t"
    "s_nop 0\n\t"
    "v_add_f32_e32 v19, v22, v23\n\t"
    "v_sub_f32_e32 v27, v27, v16\n\t"
    "v_exp_f32_e32 v24, v24\n\t"
    "s_nop 0\n\t"
    "v_add_f32_e32 v18, v18, v24\n\t"
    "v_sub_f32_e32 v28, v28, v16\n\t"
    "v_exp_f32_e32 v25, v25\n\t"
    "s_nop 0\n\t"
    "v_add_f32_e32 v18, v18, v25\n\t"
    "v_sub_f32_e32 v29, v29, v16\n\t"
    "v_exp_f32_e32 v26, v26\n\t"
    "s_nop 0\n\t"
    "v_add_f32_e32 v19, v19, v26\n\t"
    "v_exp_f32_e32 v27, v27\n\t"
    "s_nop 0\n\t"
    "v_add_f32_e32 v19, v19, v27\n\t"
    "v_sub_f32_e32 v30, v30, v16\n\t"
    "v_exp_f32_e32 v28, v28\n\t"
    "s_nop 0\n\t"
    "v_add_f32_e32 v18, v18, v28\n\t"
    "v_sub_f32_e32 v31, v31, v16\n\t"
    "v_exp_f32_e32 v29, v29\n\t"
    "s_nop 0\n\t"
    "v_add_f32_e32 v18, v18, v29\n\t"
    "v_exp_f32_e32 v30, v30\n\t"
    "s_nop 0\n\t"
    "v_add_f32_e32 v19, v19, v30\n\t"
    "v_sub_f32_e32 v32, v32, v16\n\t"
    "v_sub_f32_e32 v33, v33, v16\n\t"
    "v_exp_f32_e32 v31, v31\n\t"
    "s_nop 0\n\t"
    "v_add_f32_e32 v19, v19, v31\n\t"
    "v_exp_f32_e32 v32, v32\n\t"
    "s_nop 0\n\t"
    "v_add_f32_e32 v18, v18, v32\n\t"
    "v_exp_f32_e32 v33, v33\n\t"
    "s_nop 0\n\t"
    "v_add_f32_e32 v18, v18, v33\n\t"
    "v_sub_f32_e32 v34, v34, v16\n\t"
    "v_sub_f32_e32 v35, v35, v16\n\t"
    "v_exp_f32_e32 v34, v34\n\t"
    "s_nop 0\n\t"
    "v_add_f32_e32 v19, v19, v34\n\t"
    "v_exp_f32_e32 v35, v35\n\t"
    "s_nop 0\n\t"
    "v_add_f32_e32 v19, v19, v35\n\t"
    "v_add_f32_e32 v18, v18, v19\n\t"
    "v_mov_b32_e32 v19, v18\n\t"
    "s_nop 1\n\t"
    "v_permlane32_swap_b32 v18, v19\n\t"
    "v_add_f32_e32 v18, v18, v19\n\t"
    "v_mov_b32_e32 v19, v18\n\t"
    "s_nop 1\n\t"
    "v_permlane16_swap_b32 v18, v19\n\t"
    "v_add_f32_e32 v18, v18, v19\n\t"
    "v_rcp_f32_e32 v18, v18\n\t"
    "s_nop 0\n\t"
    "v_mul_f32_e32 v20, v20, v18\n\t"
    "v_mul_f32_e32 v21, v21, v18\n\t"
    "v_cvt_pk_bf16_f32 v60, v20, v21\n\t"
    "v_mul_f32_e32 v22, v22, v18\n\t"
    "v_mul_f32_e32 v23, v23, v18\n\t"
    "v_cvt_pk_bf16_f32 v61, v22, v23\n\t"
    "v_mul_f32_e32 v24, v24, v18\n\t"
    "v_mul_f32_e32 v25, v25, v18\n\t"
    "v_cvt_pk_bf16_f32 v62, v24, v25\n\t"
    "v_mul_f32_e32 v26, v26, v18\n\t"
    "v_mul_f32_e32 v27, v27, v18\n\t"
    "v_cvt_pk_bf16_f32 v63, v26, v27\n\t"
    "v_mul_f32_e32 v28, v28, v18\n\t"
    "v_mul_f32_e32 v29, v29, v18\n\t"
    "v_cvt_pk_bf16_f32 v20, v28, v29\n\t"
    "v_mul_f32_e32 v30, v30, v18\n\t"
    "v_mul_f32_e32 v31, v31, v18\n\t"
    "v_cvt_pk_bf16_f32 v21, v30, v31\n\t"
    "v_mul_f32_e32 v32, v32, v18\n\t"
    "v_mul_f32_e32 v33, v33, v18\n\t"
    "v_cvt_pk_bf16_f32 v22, v32, v33\n\t"
    "ds_write_b128 v6, v[60:63] offset:6144\n\t"
    "v_mul_f32_e32 v34, v34, v18\n\t"
    "v_mul_f32_e32 v35, v35, v18\n\t"
    "v_cvt_pk_bf16_f32 v23, v34, v35\n\t"
    "ds_write_b128 v6, v[20:23] offset:7168\n\t"
    "s_waitcnt lgkmcnt(0)\n\t"
    "s_barrier\n\t"
    "s_cmp_lg_u32 %[closer], 0\n\t"
    "s_cbranch_scc1 .Lx2_%=\n\t"
    "s_mov_b32 s60, 0x10000\n\t"
    "buffer_load_dwordx2 v[10:11], off, s[56:59], %[eoff] sc1\n\t"
    ".Lp3_%=:\n\t"
    "buffer_load_dwordx4 v[12:15], v8, s[56:59], %[aoff] offen sc1\n\t"
    "buffer_load_dwordx4 v[16:19], v8, s[56:59], %[aoff] offen offset:16 sc1\n\t"
    "s_waitcnt vmcnt(0)\n\t"
    "v_add_co_u32_e32 v20, vcc, 0x7f4a7c15, v10\n\t"
    "s_nop 1\n\t"
    "v_mov_b32_e32 v9, 0x9e3779b9\n\t"
    "v_addc_co_u32_e32 v21, vcc, v9, v11, vcc\n\t"
    "v_cmp_eq_u64_e32 vcc, v[12:13], v[20:21]\n\t"
    "v_cmp_eq_u64_e64 s[66:67], v[14:15], v[20:21]\n\t"
    "s_and_b64 vcc, vcc, s[66:67]\n\t"
    "v_cmp_eq_u64_e64 s[66:67], v[16:17], v[20:21]\n\t"
    "s_and_b64 vcc, vcc, s[66:67]\n\t"
    "v_cmp_eq_u64_e64 s[66:67], v[18:19], v[20:21]\n\t"
    "s_and_b64 vcc, vcc, s[66:67]\n\t"
    "s_cmp_eq_u64 vcc, exec\n\t"
    "s_cbranch_scc1 .Lk4_%=\n\t"
    "s_sleep 8\n\t"
    "s_sub_u32 s60, s60, 1\n\t"
    "s_cmp_lg_u32 s60, 0\n\t"
    "s_cbranch_scc1 .Lp3_%=\n\t"
    ".Lk4_%=:\n\t"
    "s_mov_b64 exec, 1\n\t"
    "buffer_store_dwordx2 v[20:21], off, s[56:59], %[eoff] sc1\n\t"
    "s_mov_b64 exec, -1\n\t"
    ".Lx2_%=:\n\t"
    "s_endpgm\n\t"
    ".Lend_%=:\n\t"
    :: "{v31}"(lane), "{s[36:43]}"(k8), "{s[44:47]}"(k4),
       [role] "s"(role), [pair] "s"(pair), [prod] "s"((pair * 256 + bid - 144) >> 31), [closer] "s"(bid | wave), [bid] "s"(bid), [wb] "s"(wb),
       [rbeg] "s"(rbeg), [rend] "s"(rbeg + 3 * SLOT), [pl] "s"(ldsb + PB + pair * 8192), [amask] "s"((unsigned long long)pair),
       [tch] "s"(((bid >> 3) * 2 + pair) * 1152), [foff] "s"(F_OFF + 64 * bid), [aoff] "s"(A_OFF), [aown] "s"(A_OFF + 8 * bid), [eoff] "s"(E_OFF)
    : "memory", "scc", ACLOB, VCLOB, SCLOB);
}

extern "C" void kernel_launch(void* const* d_in, const int* in_sizes, int n_in,
                              void* d_out, int out_size, void* d_ws, size_t ws_size,
                              hipStream_t stream) {
  attn_kernel<<<256, 256, 0, stream>>>((const unsigned char*)d_in[0], (const unsigned char*)d_in[1], (const unsigned char*)d_in[2],
                                       (unsigned char*)d_ws, (unsigned char*)d_out
      );
}
```

```cpp
#include <hip/hip_runtime.h>
#include <hip/hip_bf16.h>
#include <stdint.h>

using u32x4 = __attribute__((ext_vector_type(4))) unsigned;
using u32x8 = __attribute__((ext_vector_type(8))) unsigned;

static constexpr int ROWB = 512 * 384;
static constexpr int SLOT = 24576;
static constexpr int PB = 6 * SLOT;
static constexpr int F_OFF = 160 * 1024, A_OFF = 148 * 1024, E_OFF = 152 * 1024;

#define R10(p, n) p #n "0", p #n "1", p #n "2", p #n "3", p #n "4", p #n "5", p #n "6", p #n "7", p #n "8", p #n "9"
#define R240(p) R10(p, 4), R10(p, 5), R10(p, 6), R10(p, 7), R10(p, 8), R10(p, 9), R10(p, 10), R10(p, 11), R10(p, 12), R10(p, 13), \
  R10(p, 14), R10(p, 15), R10(p, 16), R10(p, 17), R10(p, 18), R10(p, 19), R10(p, 20), R10(p, 21), R10(p, 22), R10(p, 23), R10(p, 24), \
  p "250", p "251", p "252", p "253", p "254", p "255"
#define ACLOB R10("a", ), R10("a", 1), R10("a", 2), R10("a", 3), R240("a")
#define VCLOB R10("v", ), R10("v", 1), R10("v", 2), "v30", "v32", "v33", "v34", "v35", "v36", "v37", "v38", "v39", R240("v")
#define SCLOB "s48", "s49", R10("s", 5), R10("s", 6), "s70", "s71", "vcc"

__global__ __launch_bounds__(256, 1) void attn_kernel(const unsigned char* __restrict__ x,
                                                      const unsigned char* __restrict__ wqkv,
                                                      const unsigned char* __restrict__ wo,
                                                      unsigned char* ws,
                                                      unsigned char* __restrict__ out
                                                      ) {
  __shared__ u32x4 lds[10240];
  u32x8 k8; u32x4 k4;
  asm volatile(
    "s_load_dwordx8 s[36:43], %2, 0\n\t"
    "s_load_dwordx2 s[44:45], %2, 32\n\t"
    : "={s[36:43]}"(k8), "={s[44:47]}"(k4) : "s"(__builtin_amdgcn_kernarg_segment_ptr()) : "memory", ACLOB);
  __builtin_amdgcn_sched_barrier(0);
  const unsigned lane = threadIdx.x & 63, wave = __builtin_amdgcn_readfirstlane(threadIdx.x >> 6);
  const unsigned role = wave >> 1, pair = wave & 1;
  const unsigned ldsb = (unsigned)(size_t)lds;
  const unsigned bid = blockIdx.x;
  const unsigned wb = (bid & 7) * 8 * ROWB + ((bid >> 3) * 2 + pair) * 8 * 384;
  const unsigned rbeg = ldsb + pair * 3 * SLOT;

  asm volatile(
    ".Lbeg_%=:\n\t"
    "s_cmp_lg_u32 %[role], 1\n\t"
    "s_cbranch_scc1 .Lroln_%=\n\t"
    "s_cmp_eq_u32 %[prod], 0\n\t"
    "s_cbranch_scc1 .Lnp_%=\n\t"
    "s_cmp_lt_u32 %[bid], 72\n\t"
    "s_cselect_b32 s71, 0, 72\n\t"
    "s_cselect_b32 s63, 0, 768\n\t"
    "s_sub_u32 s64, %[bid], s71\n\t"
    "s_mul_i32 s65, s64, 0x1556\n\t"
    "s_lshr_b32 s65, s65, 16\n\t"
    "s_mul_i32 s69, s65, 12\n\t"
    "s_sub_u32 s69, s64, s69\n\t"
    "s_lshr_b32 s69, s69, 1\n\t"
    "s_and_b32 s70, s64, 1\n\t"
    "s_mul_i32 s60, s69, 36864\n\t"
    "s_add_u32 s60, s60, s63\n\t"
    "s_add_u32 s61, s60, 4608\n\t"
    "v_lshrrev_b32_e32 v20, 2, v31\n\t"
    "v_and_b32_e32 v21, 8, v20\n\t"
    "v_bfe_u32 v24, v31, 3, 2\n\t"
    "v_or_b32_e32 v21, v21, v24\n\t"
    "v_mul_u32_u24_e32 v21, 0x480, v21\n\t"
    "v_lshrrev_b32_e32 v24, 4, v31\n\t"
    "v_xor_b32_e32 v24, v24, v31\n\t"
    "v_and_b32_e32 v24, 7, v24\n\t"
    "v_lshl_or_b32 v24, v24, 4, v21\n\t"
    "v_xor_b32_e32 v25, 64, v24\n\t"
    "v_add_u32_e32 v25, 0x4800, v25\n\t"
    "v_mul_u32_u24_e32 v27, 0x180, v20\n\t"
    "v_and_b32_e32 v28, 3, v31\n\t"
    "v_lshl_or_b32 v27, v28, 4, v27\n\t"
    "s_lshl_b32 s62, s65, 3\n\t"
    "s_add_u32 s62, s62, s70\n\t"
    "s_mul_i32 s62, s62, 4608\n\t"
    "s_add_u32 s62, s62, 384\n\t"
    "s_lshl_b32 s63, s65, 6\n\t"
    "s_mov_b32 s50, 0x36000\n\t"
    "s_mov_b32 s51, 0x20000\n\t"
    "s_mov_b32 s54, 0x12000\n\t"
    "s_mov_b32 s55, 0x20000\n\t"
    "s_waitcnt lgkmcnt(0)\n\t"
    "s_mov_b32 s48, s38\n\t"
    "s_and_b32 s49, s39, 0xffff\n\t"
    "s_cmp_lg_u32 s71, 0\n\t"
    "s_cbranch_scc1 .Lpo_%=\n\t"
    "s_mov_b32 m0, 61440\n\t"
    "s_nop 0\n\t"
    "buffer_load_dwordx4 v24, s[48:51], s62 offen offset:0 lds\n\t"
    "s_mov_b32 m0, 62464\n\t"
    "s_nop 0\n\t"
    "buffer_load_dwordx4 v25, s[48:51], s62 offen offset:0 lds\n\t"
    "s_mov_b32 m0, 63360\n\t"
    "s_nop 0\n\t"
    "buffer_load_dwordx4 v24, s[48:51], s62 offen offset:128 lds\n\t"
    "s_mov_b32 m0, 64384\n\t"
    "s_nop 0\n\t"
    "buffer_load_dwordx4 v25, s[48:51], s62 offen offset:128 lds\n\t"
    "s_mov_b32 m0, 65280\n\t"
    "s_nop 0\n\t"
    "buffer_load_dwordx4 v24, s[48:51], s62 offen offset:256 lds\n\t"
    "s_mov_b32 m0, 66304\n\t"
    "s_nop 0\n\t"
    "buffer_load_dwordx4 v25, s[48:51], s62 offen offset:256 lds\n\t"
    "s_branch .Lpa_%=\n\t"
    ".Lpo_%=:\n\t"
    "s_mov_b32 s52, s40\n\t"
    "s_mov_b32 m0, 61440\n\t"
    "s_and_b32 s53, s41, 0xffff\n\t"
    "buffer_load_dwordx4 v27, s[52:55], s63 offen lds\n\t"
    "s_mov_b32 m0, 62464\n\t"
    "s_add_u32 s63, s63, 6144\n\t"
    "buffer_load_dwordx4 v27, s[52:55], s63 offen lds\n\t"
    "s_mov_b32 m0, 63488\n\t"
    "s_add_u32 s63, s63, 6144\n\t"
    "buffer_load_dwordx4 v27, s[52:55], s63 offen lds\n\t"
    "s_mov_b32 m0, 64512\n\t"
    "s_add_u32 s63, s63, 6144\n\t"
    "buffer_load_dwordx4 v27, s[52:55], s63 offen lds\n\t"
    "s_mov_b32 m0, 65536\n\t"
    "s_add_u32 s63, s63, 6144\n\t"
    "buffer_load_dwordx4 v27, s[52:55], s63 offen lds\n\t"
    "s_mov_b32 m0, 66560\n\t"
    "s_add_u32 s63, s63, 6144\n\t"
    "buffer_load_dwordx4 v27, s[52:55], s63 offen lds\n\t"
    "s_mov_b32 m0, 67584\n\t"
    "s_add_u32 s63, s63, 6144\n\t"
    "buffer_load_dwordx4 v27, s[52:55], s63 offen lds\n\t"
    "s_mov_b32 m0, 68608\n\t"
    "s_add_u32 s63, s63, 6144\n\t"
    "buffer_load_dwordx4 v27, s[52:55], s63 offen lds\n\t"
    "s_mov_b32 m0, 69632\n\t"
    "s_add_u32 s63, s63, 6144\n\t"
    "buffer_load_dwordx4 v27, s[52:55], s63 offen lds\n\t"
    "s_mov_b32 m0, 70656\n\t"
    "s_add_u32 s63, s63, 6144\n\t"
    "buffer_load_dwordx4 v27, s[52:55], s63 offen lds\n\t"
    "s_mov_b32 m0, 71680\n\t"
    "s_add_u32 s63, s63, 6144\n\t"
    "buffer_load_dwordx4 v27, s[52:55], s63 offen lds\n\t"
    "s_mov_b32 m0, 72704\n\t"
    "s_add_u32 s63, s63, 6144\n\t"
    "buffer_load_dwordx4 v27, s[52:55], s63 offen lds\n\t"
    ".Lpa_%=:\n\t"
    "s_mov_b32 m0, 49152\n\t"
    "s_nop 0\n\t"
    "buffer_load_dwordx4 v24, s[48:51], s60 offen offset:0 lds\n\t"
    "s_mov_b32 m0, 53120\n\t"
    "s_nop 0\n\t"
    "buffer_load_dwordx4 v24, s[48:51], s60 offen offset:128 lds\n\t"
    "s_mov_b32 m0, 57088\n\t"
    "s_nop 0\n\t"
    "buffer_load_dwordx4 v24, s[48:51], s60 offen offset:256 lds\n\t"
    "s_mov_b32 m0, 51200\n\t"
    "s_nop 0\n\t"
    "buffer_load_dwordx4 v25, s[48:51], s60 offen offset:0 lds\n\t"
    "s_mov_b32 m0, 55168\n\t"
    "s_nop 0\n\t"
    "buffer_load_dwordx4 v25, s[48:51], s60 offen offset:128 lds\n\t"
    "s_mov_b32 m0, 59136\n\t"
    "s_nop 0\n\t"
    "buffer_load_dwordx4 v25, s[48:51], s60 offen offset:256 lds\n\t"
    "s_mov_b32 m0, 50176\n\t"
    "s_nop 0\n\t"
    "buffer_load_dwordx4 v24, s[48:51], s61 offen offset:0 lds\n\t"
    "s_mov_b32 m0, 54144\n\t"
    "s_nop 0\n\t"
    "buffer_load_dwordx4 v24, s[48:51], s61 offen offset:128 lds\n\t"
    "s_mov_b32 m0, 58112\n\t"
    "s_nop 0\n\t"
    "buffer_load_dwordx4 v24, s[48:51], s61 offen offset:256 lds\n\t"
    "s_mov_b32 m0, 52224\n\t"
    "s_nop 0\n\t"
    "buffer_load_dwordx4 v25, s[48:51], s61 offen offset:0 lds\n\t"
    "s_mov_b32 m0, 56192\n\t"
    "s_nop 0\n\t"
    "buffer_load_dwordx4 v25, s[48:51], s61 offen offset:128 lds\n\t"
    "s_mov_b32 m0, 60160\n\t"
    "s_nop 0\n\t"
    "buffer_load_dwordx4 v25, s[48:51], s61 offen offset:256 lds\n\t"
    ".Lnp_%=:\n\t"
    "s_waitcnt lgkmcnt(0)\n\t"
    "s_mov_b32 s56, s42\n\t"
    "s_and_b32 s57, s43, 0xffff\n\t"
    "s_mov_b32 s58, 0x40000\n\t"
    "s_mov_b32 s59, 0x20000\n\t"
    "buffer_load_dwordx2 v[22:23], off, s[56:59], %[eoff] sc1\n\t"
    "s_barrier\n\t"
    "v_lshlrev_b32_e32 v30, 4, v31\n\t"
    "v_and_b32_e32 v28, 7, v31\n\t"
    "v_bfe_u32 v27, v31, 3, 1\n\t"
    "v_lshrrev_b32_e32 v29, 4, v31\n\t"
    "v_add_u32_e32 v6, %[pl], v30\n\t"
    "v_mul_u32_u24_e32 v27, 0xc00, v27\n\t"
    "v_add_u32_e32 v27, %[rbeg], v27\n\t"
    "v_lshlrev_b32_e32 v26, 7, v31\n\t"
    "v_mad_u32_u24 v20, v28, 3, 0\n\t"
    "v_and_b32_e32 v21, 6, v20\n\t"
    "v_xor_b32_e32 v21, v21, v29\n\t"
    "v_lshl_add_u32 v20, v20, 7, v27\n\t"
    "v_lshl_add_u32 v0, v21, 4, v20\n\t"
    "v_xor_b32_e32 v1, 64, v0\n\t"
    "v_mad_u32_u24 v20, v28, 3, 1\n\t"
    "v_and_b32_e32 v21, 6, v20\n\t"
    "v_xor_b32_e32 v21, v21, v29\n\t"
    "v_lshl_add_u32 v20, v20, 7, v27\n\t"
    "v_lshl_add_u32 v2, v21, 4, v20\n\t"
    "v_xor_b32_e32 v3, 64, v2\n\t"
    "v_mad_u32_u24 v20, v28, 3, 2\n\t"
    "v_and_b32_e32 v21, 6, v20\n\t"
    "v_xor_b32_e32 v21, v21, v29\n\t"
    "v_lshl_add_u32 v20, v20, 7, v27\n\t"
    "v_lshl_add_u32 v4, v21, 4, v20\n\t"
    "v_xor_b32_e32 v5, 64, v4\n\t"
    "s_cmp_eq_u32 %[prod], 0\n\t"
    "s_cbranch_scc1 .Lnq_%=\n\t"
    "v_bfe_u32 v20, v31, 1, 3\n\t"
    "v_xor_b32_e32 v20, v20, v29\n\t"
    "v_lshlrev_b32_e32 v20, 4, v20\n\t"
    "v_lshl_or_b32 v20, v28, 7, v20\n\t"
    "v_bfe_u32 v21, v31, 3, 1\n\t"
    "v_lshl_or_b32 v16, v21, 11, v20\n\t"
    "v_add_u32_e32 v16, 0xc000, v16\n\t"
    "v_xor_b32_e32 v17, 64, v16\n\t"
    "s_cmp_lg_u32 s71, 0\n\t"
    "s_cbranch_scc1 .Lqo_%=\n\t"
    "v_lshl_or_b32 v18, v21, 10, v20\n\t"
    "v_add_u32_e32 v18, 0xf000, v18\n\t"
    "v_xor_b32_e32 v19, 64, v18\n\t"
    "s_mul_i32 s60, s70, 6\n\t"
    "s_add_u32 s60, s60, s65\n\t"
    "s_mul_i32 s60, s60, 6\n\t"
    "s_add_u32 s60, s60, s69\n\t"
    "s_lshl_b32 s60, s60, 10\n\t"
    "v_add_u32_e32 v20, s60, v30\n\t"
    "v_mov_b32_e32 v24, v30\n\t"
    "s_waitcnt vmcnt(13)\n\t"
    "ds_read_b128 v[80:83], v18 offset:0\n\t"
    "ds_read_b128 v[84:87], v19 offset:0\n\t"
    "ds_read_b128 v[88:91], v18 offset:2048\n\t"
    "ds_read_b128 v[92:95], v19 offset:2048\n\t"
    "ds_read_b128 v[96:99], v18 offset:4096\n\t"
    "ds_read_b128 v[100:103], v19 offset:4096\n\t"
    "s_branch .Lqa_%=\n\t"
    ".Lqo_%=:\n\t"
    "s_and_b32 s63, s65, 1\n\t"
    "s_lshl_b32 s63, s63, 1\n\t"
    "s_add_u32 s63, s63, s70\n\t"
    "s_lshl_b32 s64, s70, 2\n\t"
    "s_lshl_b32 s62, s70, 4\n\t"
    "s_cmp_lt_u32 s65, 4\n\t"
    "s_cselect_b32 s60, s64, 8\n\t"
    "s_cselect_b32 s63, s65, s63\n\t"
    "s_cselect_b32 s62, s64, s62\n\t"
    "s_cselect_b32 s61, 4, 3\n\t"
    "s_lshl_b32 s62, s62, 1\n\t"
    "s_add_u32 s62, s62, 61440\n\t"
    "s_mul_i32 s60, s60, 6\n\t"
    "s_add_u32 s60, s60, s69\n\t"
    "s_lshl_b32 s60, s60, 2\n\t"
    "s_add_u32 s60, s60, s63\n\t"
    "s_lshl_b32 s60, s60, 8\n\t"
    "s_add_u32 s60, s60, 73728\n\t"
    "v_and_b32_e32 v25, 3, v31\n\t"
    "v_bfe_u32 v21, v31, 2, 2\n\t"
    "v_lshlrev_b32_e32 v18, s61, v25\n\t"
    "v_lshl_add_u32 v18, v21, 6, v18\n\t"
    "v_lshl_add_u32 v18, v29, 9, v18\n\t"
    "v_add_u32_e32 v18, s62, v18\n\t"
    "v_lshl_add_u32 v24, v25, 2, v21\n\t"
    "v_lshl_add_u32 v24, v29, 4, v24\n\t"
    "v_lshlrev_b32_e32 v24, 4, v24\n\t"
    "v_mul_u32_u24_e32 v21, 0x1800, v21\n\t"
    "v_lshl_add_u32 v20, v25, 4, v21\n\t"
    "v_lshl_add_u32 v20, v29, 6, v20\n\t"
    "v_add_u32_e32 v20, s60, v20\n\t"
    "s_waitcnt vmcnt(13)\n\t"
    "ds_read_b64_tr_b16 v[80:81], v18 offset:0\n\t"
    "ds_read_b64_tr_b16 v[82:83], v18 offset:256\n\t"
    "ds_read_b64_tr_b16 v[84:85], v18 offset:2048\n\t"
    "ds_read_b64_tr_b16 v[86:87], v18 offset:2304\n\t"
    "ds_read_b64_tr_b16 v[88:89], v18 offset:4096\n\t"
    "ds_read_b64_tr_b16 v[90:91], v18 offset:4352\n\t"
    "ds_read_b64_tr_b16 v[92:93], v18 offset:6144\n\t"
    "ds_read_b64_tr_b16 v[94:95], v18 offset:6400\n\t"
    "ds_read_b64_tr_b16 v[96:97], v18 offset:8192\n\t"
    "ds_read_b64_tr_b16 v[98:99], v18 offset:8448\n\t"
    "ds_read_b64_tr_b16 v[100:101], v18 offset:10240\n\t"
    "ds_read_b64_tr_b16 v[102:103], v18 offset:10496\n\t"
    ".Lqa_%=:\n\t"
    "s_waitcnt vmcnt(7)\n\t"
    "ds_read_b128 v[32:35], v16 offset:0\n\t"
    "ds_read_b128 v[36:39], v17 offset:0\n\t"
    "ds_read_b128 v[40:43], v16 offset:4096\n\t"
    "ds_read_b128 v[44:47], v17 offset:4096\n\t"
    "ds_read_b128 v[48:51], v16 offset:8192\n\t"
    "ds_read_b128 v[52:55], v17 offset:8192\n\t"
    "s_waitcnt vmcnt(1)\n\t"
    "ds_read_b128 v[56:59], v16 offset:1024\n\t"
    "ds_read_b128 v[60:63], v17 offset:1024\n\t"
    "ds_read_b128 v[64:67], v16 offset:5120\n\t"
    "ds_read_b128 v[68:71], v17 offset:5120\n\t"
    "ds_read_b128 v[72:75], v16 offset:9216\n\t"
    "ds_read_b128 v[76:79], v17 offset:9216\n\t"
    "s_waitcnt lgkmcnt(11)\n\t"
    "v_mfma_f32_16x16x32_bf16 v[108:111], v[32:35], v[80:83], 0\n\t"
    "s_waitcnt lgkmcnt(10)\n\t"
    "v_mfma_f32_16x16x32_bf16 v[108:111], v[36:39], v[84:87], v[108:111]\n\t"
    "s_waitcnt lgkmcnt(9)\n\t"
    "v_mfma_f32_16x16x32_bf16 v[108:111], v[40:43], v[88:91], v[108:111]\n\t"
    "s_waitcnt lgkmcnt(8)\n\t"
    "v_mfma_f32_16x16x32_bf16 v[108:111], v[44:47], v[92:95], v[108:111]\n\t"
    "s_waitcnt lgkmcnt(7)\n\t"
    "v_mfma_f32_16x16x32_bf16 v[108:111], v[48:51], v[96:99], v[108:111]\n\t"
    "s_waitcnt lgkmcnt(6)\n\t"
    "v_mfma_f32_16x16x32_bf16 v[108:111], v[52:55], v[100:103], v[108:111]\n\t"
    "s_waitcnt lgkmcnt(5)\n\t"
    "v_mfma_f32_16x16x32_bf16 v[112:115], v[56:59], v[80:83], 0\n\t"
    "s_waitcnt lgkmcnt(4)\n\t"
    "v_mfma_f32_16x16x32_bf16 v[112:115], v[60:63], v[84:87], v[112:115]\n\t"
    "s_waitcnt lgkmcnt(3)\n\t"
    "v_mfma_f32_16x16x32_bf16 v[112:115], v[64:67], v[88:91], v[112:115]\n\t"
    "s_waitcnt lgkmcnt(2)\n\t"
    "v_mfma_f32_16x16x32_bf16 v[112:115], v[68:71], v[92:95], v[112:115]\n\t"
    "s_waitcnt lgkmcnt(1)\n\t"
    "v_mfma_f32_16x16x32_bf16 v[112:115], v[72:75], v[96:99], v[112:115]\n\t"
    "s_waitcnt lgkmcnt(0)\n\t"
    "v_mfma_f32_16x16x32_bf16 v[112:115], v[76:79], v[100:103], v[112:115]\n\t"
    "s_cmp_eq_u32 s71, 0\n\t"
    "s_cselect_b32 s60, 0x3dd53b95, 1.0\n\t"
    "v_mul_f32_e32 v108, s60, v108\n\t"
    "v_mul_f32_e32 v109, s60, v109\n\t"
    "v_mul_f32_e32 v110, s60, v110\n\t"
    "v_mul_f32_e32 v111, s60, v111\n\t"
    "s_nop 3\n\t"
    "v_mul_f32_e32 v112, s60, v112\n\t"
    "v_mul_f32_e32 v113, s60, v113\n\t"
    "v_mul_f32_e32 v114, s60, v114\n\t"
    "v_mul_f32_e32 v115, s60, v115\n\t"
    "v_cvt_pk_bf16_f32 v104, v108, v109\n\t"
    "v_cvt_pk_bf16_f32 v105, v110, v111\n\t"
    "s_nop 0\n\t"
    "v_cvt_pk_bf16_f32 v106, v112, v113\n\t"
    "v_cvt_pk_bf16_f32 v107, v114, v115\n\t"
    "ds_write_b128 v24, v[104:107] offset:49152\n\t"
    "ds_read_b128 v[104:107], v30 offset:49152\n\t"
    "s_waitcnt lgkmcnt(0)\n\t"
    "buffer_store_dwordx4 v[104:107], v20, s[56:59], 0 offen sc1\n\t"
    ".Lnq_%=:\n\t"
    "s_waitcnt vmcnt(0)\n\t"
    "v_add_co_u32_e32 v22, vcc, 0x7f4a7c15, v22\n\t"
    "v_mov_b32_e32 v16, 0x9e3779b9\n\t"
    "s_nop 0\n\t"
    "v_addc_co_u32_e32 v23, vcc, v16, v23, vcc\n\t"
    "s_mov_b32 exec_lo, %[prod]\n\t"
    "s_mov_b32 exec_hi, 0\n\t"
    "buffer_store_dwordx2 v[22:23], off, s[56:59], %[foff] sc1\n\t"
    "s_mov_b64 exec, -1\n\t"
    "v_lshrrev_b32_e32 v20, 5, v31\n\t"
    "v_mul_u32_u24_e32 v20, 0x30000, v20\n\t"
    "v_and_b32_e32 v21, 1, v29\n\t"
    "v_mul_u32_u24_e32 v21, 0x600, v21\n\t"
    "v_add_u32_e32 v20, v20, v21\n\t"
    "v_and_b32_e32 v21, 15, v31\n\t"
    "v_lshl_add_u32 v7, v21, 4, v20\n\t"
    "v_lshl_add_u32 v8, v21, 3, v20\n\t"
    "v_add_u32_e32 v8, 0x100, v8\n\t"
    "v_and_b32_e32 v20, 48, v30\n\t"
    "v_lshl_or_b32 v20, v29, 6, v20\n\t"
    "v_lshrrev_b32_e32 v21, 2, v21\n\t"
    "v_lshl_or_b32 v9, v21, 8, v20\n\t"
    "v_min_u32_e32 v21, 35, v31\n\t"
    "v_mul_u32_u24_e32 v21, 64, v21\n\t"
    "v_add_u32_e32 v21, 0x28000, v21\n\t"
    "s_mov_b32 s52, s44\n\t"
    "s_and_b32 s53, s45, 0xffff\n\t"
    "s_mov_b32 s54, 0x6000000\n\t"
    "s_mov_b32 s55, 0x20000\n\t"
    "s_cmp_eq_u32 %[pair], 0\n\t"
    "s_cbranch_scc1 .Lng_%=\n\t"
    "s_sleep 45\n\t"
    "s_mov_b32 s64, 0\n\t"
    "s_mov_b32 s65, 2304\n\t"
    "buffer_load_dwordx2 v[12:13], v21, s[56:59], s64 offen sc1\n\t"
    "buffer_load_dwordx2 v[14:15], v21, s[56:59], s65 offen sc1\n\t"
    "s_waitcnt vmcnt(0)\n\t"
    "s_mov_b32 s60, 0x10000\n\t"
    ".Lp5_%=:\n\t"
    "v_cmp_eq_u64_e32 vcc, v[12:13], v[22:23]\n\t"
    "v_cmp_eq_u64_e64 s[70:71], v[14:15], v[22:23]\n\t"
    "s_and_b64 vcc, vcc, s[70:71]\n\t"
    "s_cmp_eq_u64 vcc, exec\n\t"
    "s_cbranch_scc1 .Lk6_%=\n\t"
    "s_sleep 4\n\t"
    "buffer_load_dwordx2 v[12:13], v21, s[56:59], s64 offen sc1\n\t"
    "buffer_load_dwordx2 v[14:15], v21, s[56:59], s65 offen sc1\n\t"
    "s_sub_u32 s60, s60, 1\n\t"
    "s_cmp_lg_u32 s60, 0\n\t"
    "s_waitcnt vmcnt(0)\n\t"
    "s_cbranch_scc1 .Lp5_%=\n\t"
    ".Lk6_%=:\n\t"
    ".Lng_%=:\n\t"
    "s_mul_i32 s66, %[pair], 36864\n\t"
    "s_mul_i32 s62, %[pair], 73728\n\t"
    "s_add_u32 s62, s62, 36864\n\t"
    "v_add_u32_e32 v11, s62, v30\n\t"
    "s_mov_b32 s63, 73728\n\t"
    "s_barrier\n\t"
    "s_add_u32 s67, %[tch], 0\n\t"
    "s_mov_b64 exec, 0x1ff\n\t"
    "buffer_load_dword v10, v26, s[56:59], s67 offen sc1\n\t"
    "s_mov_b64 exec, -1\n\t"
    "buffer_load_dwordx4 v[32:35], v30, s[56:59], s66 offen offset:0 sc1\n\t"
    "buffer_load_dwordx4 v[36:39], v30, s[56:59], s66 offen offset:1024 sc1\n\t"
    "buffer_load_dwordx4 v[40:43], v30, s[56:59], s66 offen offset:2048 sc1\n\t"
    "buffer_load_dwordx4 v[44:47], v30, s[56:59], s66 offen offset:3072 sc1\n\t"
    "s_add_u32 s66, s66, 4096\n\t"
    "buffer_load_dwordx4 v[48:51], v30, s[56:59], s66 offen offset:0 sc1\n\t"
    "buffer_load_dwordx4 v[52:55], v30, s[56:59], s66 offen offset:1024 sc1\n\t"
    "buffer_load_dwordx4 v[56:59], v30, s[56:59], s66 offen offset:2048 sc1\n\t"
    "buffer_load_dwordx4 v[60:63], v30, s[56:59], s66 offen offset:3072 sc1\n\t"
    "s_add_u32 s66, s66, 4096\n\t"
    "buffer_load_dwordx4 v[64:67], v30, s[56:59], s66 offen offset:0 sc1\n\t"
    "buffer_load_dwordx4 v[68:71], v30, s[56:59], s66 offen offset:1024 sc1\n\t"
    "buffer_load_dwordx4 v[72:75], v30, s[56:59], s66 offen offset:2048 sc1\n\t"
    "buffer_load_dwordx4 v[76:79], v30, s[56:59], s66 offen offset:3072 sc1\n\t"
    "s_add_u32 s66, s66, 4096\n\t"
    "buffer_load_dwordx4 v[80:83], v30, s[56:59], s66 offen offset:0 sc1\n\t"
    "buffer_load_dwordx4 v[84:87], v30, s[56:59], s66 offen offset:1024 sc1\n\t"
    "buffer_load_dwordx4 v[88:91], v30, s[56:59], s66 offen offset:2048 sc1\n\t"
    "buffer_load_dwordx4 v[92:95], v30, s[56:59], s66 offen offset:3072 sc1\n\t"
    "s_add_u32 s66, s66, 4096\n\t"
    "buffer_load_dwordx4 v[96:99], v30, s[56:59], s66 offen offset:0 sc1\n\t"
    "buffer_load_dwordx4 v[100:103], v30, s[56:59], s66 offen offset:1024 sc1\n\t"
    "buffer_load_dwordx4 v[104:107], v30, s[56:59], s66 offen offset:2048 sc1\n\t"
    "buffer_load_dwordx4 v[108:111], v30, s[56:59], s66 offen offset:3072 sc1\n\t"
    "s_add_u32 s66, s66, 4096\n\t"
    "buffer_load_dwordx4 v[112:115], v30, s[56:59], s66 offen offset:0 sc1\n\t"
    "buffer_load_dwordx4 v[116:119], v30, s[56:59], s66 offen offset:1024 sc1\n\t"
    "buffer_load_dwordx4 v[120:123], v30, s[56:59], s66 offen offset:2048 sc1\n\t"
    "buffer_load_dwordx4 v[124:127], v30, s[56:59], s66 offen offset:3072 sc1\n\t"
    "s_add_u32 s66, s66, 4096\n\t"
    "s_mov_b32 s64, 4608\n\t"
    "s_mov_b32 s65, 6912\n\t"
    "buffer_load_dwordx2 v[12:13], v21, s[56:59], s64 offen sc1\n\t"
    "buffer_load_dwordx2 v[14:15], v21, s[56:59], s65 offen sc1\n\t"
    "s_waitcnt vmcnt(25)\n\t"
    "ds_write_b128 v11, v[32:35] offset:0\n\t"
    "buffer_load_dwordx4 v[128:131], v30, s[56:59], s66 offen offset:0 sc1\n\t"
    "s_waitcnt vmcnt(25)\n\t"
    "ds_write_b128 v11, v[36:39] offset:1024\n\t"
    "buffer_load_dwordx4 v[132:135], v30, s[56:59], s66 offen offset:1024 sc1\n\t"
    "s_waitcnt vmcnt(25)\n\t"
    "ds_write_b128 v11, v[40:43] offset:2048\n\t"
    "buffer_load_dwordx4 v[136:139], v30, s[56:59], s66 offen offset:2048 sc1\n\t"
    "s_waitcnt vmcnt(25)\n\t"
    "ds_write_b128 v11, v[44:47] offset:3072\n\t"
    "buffer_load_dwordx4 v[140:143], v30, s[56:59], s66 offen offset:3072 sc1\n\t"
    "s_add_u32 s66, s66, 4096\n\t"
    "s_waitcnt vmcnt(25)\n\t"
    "ds_write_b128 v11, v[48:51] offset:4096\n\t"
    "buffer_load_dwordx4 v[144:147], v30, s[56:59], s66 offen offset:0 sc1\n\t"
    "s_waitcnt vmcnt(25)\n\t"
    "ds_write_b128 v11, v[52:55] offset:5120\n\t"
    "s_waitcnt lgkmcnt(0)\n\t"
    "s_barrier\n\t"
    "buffer_load_dwordx4 v[148:151], v30, s[56:59], s66 offen offset:1024 sc1\n\t"
    "s_waitcnt vmcnt(25)\n\t"
    "ds_write_b128 v11, v[56:59] offset:6144\n\t"
    "buffer_load_dwordx4 v[152:155], v30, s[56:59], s66 offen offset:2048 sc1\n\t"
    "s_waitcnt vmcnt(25)\n\t"
    "ds_write_b128 v11, v[60:63] offset:7168\n\t"
    "buffer_load_dwordx4 v[156:159], v30, s[56:59], s66 offen offset:3072 sc1\n\t"
    "s_add_u32 s66, s66, 4096\n\t"
    "s_waitcnt vmcnt(25)\n\t"
    "ds_write_b128 v11, v[64:67] offset:8192\n\t"
    "buffer_load_dwordx4 v[160:163], v30, s[56:59], s66 offen offset:0 sc1\n\t"
    "s_waitcnt vmcnt(25)\n\t"
    "ds_write_b128 v11, v[68:71] offset:9216\n\t"
    "buffer_load_dwordx4 v[164:167], v30, s[56:59], s66 offen offset:1024 sc1\n\t"
    "s_waitcnt vmcnt(25)\n\t"
    "ds_write_b128 v11, v[72:75] offset:10240\n\t"
    "buffer_load_dwordx4 v[168:171], v30, s[56:59], s66 offen offset:2048 sc1\n\t"
    "s_waitcnt vmcnt(25)\n\t"
    "ds_write_b128 v11, v[76:79] offset:11264\n\t"
    "s_waitcnt lgkmcnt(0)\n\t"
    "s_barrier\n\t"
    "buffer_load_dwordx4 v[172:175], v30, s[56:59], s66 offen offset:3072 sc1\n\t"
    "s_add_u32 s66, s66, 4096\n\t"
    "s_waitcnt vmcnt(25)\n\t"
    "ds_write_b128 v11, v[80:83] offset:12288\n\t"
    "s_waitcnt vmcnt(24)\n\t"
    "ds_write_b128 v11, v[84:87] offset:13312\n\t"
    "s_waitcnt vmcnt(23)\n\t"
    "ds_write_b128 v11, v[88:91] offset:14336\n\t"
    "s_waitcnt vmcnt(22)\n\t"
    "ds_write_b128 v11, v[92:95] offset:15360\n\t"
    "s_waitcnt vmcnt(21)\n\t"
    "ds_write_b128 v11, v[96:99] offset:16384\n\t"
    "s_waitcnt vmcnt(20)\n\t"
    "ds_write_b128 v11, v[100:103] offset:17408\n\t"
    "s_waitcnt vmcnt(19)\n\t"
    "ds_write_b128 v11, v[104:107] offset:18432\n\t"
    "s_waitcnt vmcnt(18)\n\t"
    "ds_write_b128 v11, v[108:111] offset:19456\n\t"
    "s_waitcnt vmcnt(17)\n\t"
    "ds_write_b128 v11, v[112:115] offset:20480\n\t"
    "s_waitcnt vmcnt(16)\n\t"
    "ds_write_b128 v11, v[116:119] offset:21504\n\t"
    "s_waitcnt vmcnt(15)\n\t"
    "ds_write_b128 v11, v[120:123] offset:22528\n\t"
    "s_waitcnt vmcnt(14)\n\t"
    "ds_write_b128 v11, v[124:127] offset:23552\n\t"
    "s_waitcnt lgkmcnt(0)\n\t"
    "s_barrier\n\t"
    "s_waitcnt vmcnt(12)\n\t"
    "s_mov_b32 s60, 0x10000\n\t"
    ".Lp7_%=:\n\t"
    "v_cmp_eq_u64_e32 vcc, v[12:13], v[22:23]\n\t"
    "v_cmp_eq_u64_e64 s[70:71], v[14:15], v[22:23]\n\t"
    "s_and_b64 vcc, vcc, s[70:71]\n\t"
    "s_cmp_eq_u64 vcc, exec\n\t"
    "s_cbranch_scc1 .Lk8_%=\n\t"
    "s_sleep 4\n\t"
    "buffer_load_dwordx2 v[12:13], v21, s[56:59], s64 offen sc1\n\t"
    "buffer_load_dwordx2 v[14:15], v21, s[56:59], s65 offen sc1\n\t"
    "s_sub_u32 s60, s60, 1\n\t"
    "s_cmp_lg_u32 s60, 0\n\t"
    "s_waitcnt vmcnt(0)\n\t"
    "s_cbranch_scc1 .Lp7_%=\n\t"
    ".Lk8_%=:\n\t"
    "s_add_u32 s67, %[tch], 73728\n\t"
    "s_mov_b64 exec, 0x1ff\n\t"
    "buffer_load_dword v10, v26, s[56:59], s67 offen sc1\n\t"
    "s_mov_b64 exec, -1\n\t"
    "buffer_load_dwordx4 a[0:3], v9, s[56:59], s63 offen offset:0 sc1\n\t"
    "buffer_load_dwordx4 a[4:7], v9, s[56:59], s63 offen offset:1024 sc1\n\t"
    "buffer_load_dwordx4 a[8:11], v9, s[56:59], s63 offen offset:2048 sc1\n\t"
    "buffer_load_dwordx4 a[12:15], v9, s[56:59], s63 offen offset:3072 sc1\n\t"
    "s_add_u32 s63, s63, 4096\n\t"
    "s_waitcnt vmcnt(16)\n\t"
    "ds_write_b128 v11, v[128:131] offset:24576\n\t"
    "buffer_load_dwordx4 a[16:19], v9, s[56:59], s63 offen offset:0 sc1\n\t"
    "s_waitcnt vmcnt(16)\n\t"
    "ds_write_b128 v11, v[132:135] offset:25600\n\t"
    "buffer_load_dwordx4 a[20:23], v9, s[56:59], s63 offen offset:1024 sc1\n\t"
    "s_waitcnt vmcnt(16)\n\t"
    "ds_write_b128 v11, v[136:139] offset:26624\n\t"
    "buffer_load_dwordx4 a[24:27], v9, s[56:59], s63 offen offset:2048 sc1\n\t"
    "s_waitcnt vmcnt(16)\n\t"
    "ds_write_b128 v11, v[140:143] offset:27648\n\t"
    "buffer_load_dwordx4 a[28:31], v9, s[56:59], s63 offen offset:3072 sc1\n\t"
    "s_add_u32 s63, s63, 4096\n\t"
    "s_waitcnt vmcnt(16)\n\t"
    "ds_write_b128 v11, v[144:147] offset:28672\n\t"
    "buffer_load_dwordx4 a[32:35], v9, s[56:59], s63 offen offset:0 sc1\n\t"
    "s_waitcnt vmcnt(16)\n\t"
    "ds_write_b128 v11, v[148:151] offset:29696\n\t"
    "buffer_load_dwordx4 a[36:39], v9, s[56:59], s63 offen offset:1024 sc1\n\t"
    "s_waitcnt vmcnt(16)\n\t"
    "ds_write_b128 v11, v[152:155] offset:30720\n\t"
    "buffer_load_dwordx4 a[40:43], v9, s[56:59], s63 offen offset:2048 sc1\n\t"
    "s_waitcnt vmcnt(16)\n\t"
    "ds_write_b128 v11, v[156:159] offset:31744\n\t"
    "buffer_load_dwordx4 a[44:47], v9, s[56:59], s63 offen offset:3072 sc1\n\t"
    "s_add_u32 s63, s63, 4096\n\t"
    "s_waitcnt vmcnt(16)\n\t"
    "ds_write_b128 v11, v[160:163] offset:32768\n\t"
    "buffer_load_dwordx4 a[48:51], v9, s[56:59], s63 offen offset:0 sc1\n\t"
    "s_waitcnt vmcnt(16)\n\t"
    "ds_write_b128 v11, v[164:167] offset:33792\n\t"
    "buffer_load_dwordx4 a[52:55], v9, s[56:59], s63 offen offset:1024 sc1\n\t"
    "s_waitcnt vmcnt(16)\n\t"
    "ds_write_b128 v11, v[168:171] offset:34816\n\t"
    "buffer_load_dwordx4 a[56:59], v9, s[56:59], s63 offen offset:2048 sc1\n\t"
    "s_waitcnt vmcnt(16)\n\t"
    "ds_write_b128 v11, v[172:175] offset:35840\n\t"
    "s_waitcnt lgkmcnt(0)\n\t"
    "s_barrier\n\t"
    "buffer_load_dwordx4 a[60:63], v9, s[56:59], s63 offen offset:3072 sc1\n\t"
    "s_add_u32 s63, s63, 4096\n\t"
    "buffer_load_dwordx4 a[64:67], v9, s[56:59], s63 offen offset:0 sc1\n\t"
    "buffer_load_dwordx4 a[68:71], v9, s[56:59], s63 offen offset:1024 sc1\n\t"
    "buffer_load_dwordx4 a[72:75], v9, s[56:59], s63 offen offset:2048 sc1\n\t"
    "buffer_load_dwordx4 a[76:79], v9, s[56:59], s63 offen offset:3072 sc1\n\t"
    "s_add_u32 s63, s63, 4096\n\t"
    "buffer_load_dwordx4 a[80:83], v9, s[56:59], s63 offen offset:0 sc1\n\t"
    "buffer_load_dwordx4 a[84:87], v9, s[56:59], s63 offen offset:1024 sc1\n\t"
    "buffer_load_dwordx4 a[88:91], v9, s[56:59], s63 offen offset:2048 sc1\n\t"
    "buffer_load_dwordx4 a[92:95], v9, s[56:59], s63 offen offset:3072 sc1\n\t"
    "s_add_u32 s63, s63, 4096\n\t"
    "buffer_load_dwordx4 a[96:99], v9, s[56:59], s63 offen offset:0 sc1\n\t"
    "buffer_load_dwordx4 a[100:103], v9, s[56:59], s63 offen offset:1024 sc1\n\t"
    "buffer_load_dwordx4 a[104:107], v9, s[56:59], s63 offen offset:2048 sc1\n\t"
    "buffer_load_dwordx4 a[108:111], v9, s[56:59], s63 offen offset:3072 sc1\n\t"
    "s_add_u32 s63, s63, 4096\n\t"
    "buffer_load_dwordx4 a[112:115], v9, s[56:59], s63 offen offset:0 sc1\n\t"
    "buffer_load_dwordx4 a[116:119], v9, s[56:59], s63 offen offset:1024 sc1\n\t"
    "buffer_load_dwordx4 a[120:123], v9, s[56:59], s63 offen offset:2048 sc1\n\t"
    "buffer_load_dwordx4 a[124:127], v9, s[56:59], s63 offen offset:3072 sc1\n\t"
    "s_add_u32 s63, s63, 4096\n\t"
    "buffer_load_dwordx4 a[128:131], v9, s[56:59], s63 offen offset:0 sc1\n\t"
    "buffer_load_dwordx4 a[132:135], v9, s[56:59], s63 offen offset:1024 sc1\n\t"
    "buffer_load_dwordx4 a[136:139], v9, s[56:59], s63 offen offset:2048 sc1\n\t"
    "buffer_load_dwordx4 a[140:143], v9, s[56:59], s63 offen offset:3072 sc1\n\t"
    "s_add_u32 s63, s63, 4096\n\t"
    "buffer_load_dwordx4 a[144:147], v9, s[56:59], s63 offen offset:0 sc1\n\t"
    "buffer_load_dwordx4 a[148:151], v9, s[56:59], s63 offen offset:1024 sc1\n\t"
    "buffer_load_dwordx4 a[152:155], v9, s[56:59], s63 offen offset:2048 sc1\n\t"
    "buffer_load_dwordx4 a[156:159], v9, s[56:59], s63 offen offset:3072 sc1\n\t"
    "s_add_u32 s63, s63, 4096\n\t"
    "buffer_load_dwordx4 a[160:163], v9, s[56:59], s63 offen offset:0 sc1\n\t"
    "buffer_load_dwordx4 a[164:167], v9, s[56:59], s63 offen offset:1024 sc1\n\t"
    "buffer_load_dwordx4 a[168:171], v9, s[56:59], s63 offen offset:2048 sc1\n\t"
    "buffer_load_dwordx4 a[172:175], v9, s[56:59], s63 offen offset:3072 sc1\n\t"
    "s_add_u32 s63, s63, 4096\n\t"
    "buffer_load_dwordx4 a[176:179], v9, s[56:59], s63 offen offset:0 sc1\n\t"
    "buffer_load_dwordx4 a[180:183], v9, s[56:59], s63 offen offset:1024 sc1\n\t"
    "buffer_load_dwordx4 a[184:187], v9, s[56:59], s63 offen offset:2048 sc1\n\t"
    "buffer_load_dwordx4 a[188:191], v9, s[56:59], s63 offen offset:3072 sc1\n\t"
    "s_add_u32 s63, s63, 4096\n\t"
    "buffer_load_dwordx4 a[192:195], v9, s[56:59], s63 offen offset:0 sc1\n\t"
    "buffer_load_dwordx4 a[196:199], v9, s[56:59], s63 offen offset:1024 sc1\n\t"
    "buffer_load_dwordx4 a[200:203], v9, s[56:59], s63 offen offset:2048 sc1\n\t"
    "buffer_load_dwordx4 a[204:207], v9, s[56:59], s63 offen offset:3072 sc1\n\t"
    "s_add_u32 s63, s63, 4096\n\t"
    "buffer_load_dwordx4 a[208:211], v9, s[56:59], s63 offen offset:0 sc1\n\t"
    "buffer_load_dwordx4 a[212:215], v9, s[56:59], s63 offen offset:1024 sc1\n\t"
    "buffer_load_dwordx4 a[216:219], v9, s[56:59], s63 offen offset:2048 sc1\n\t"
    "buffer_load_dwordx4 a[220:223], v9, s[56:59], s63 offen offset:3072 sc1\n\t"
    "s_add_u32 s63, s63, 4096\n\t"
    "buffer_load_dwordx4 a[224:227], v9, s[56:59], s63 offen offset:0 sc1\n\t"
    "buffer_load_dwordx4 a[228:231], v9, s[56:59], s63 offen offset:1024 sc1\n\t"
    "buffer_load_dwordx4 a[232:235], v9, s[56:59], s63 offen offset:2048 sc1\n\t"
    "buffer_load_dwordx4 a[236:239], v9, s[56:59], s63 offen offset:3072 sc1\n\t"
    "s_add_u32 s63, s63, 4096\n\t"
    "buffer_load_dwordx4 a[240:243], v9, s[56:59], s63 offen offset:0 sc1\n\t"
    "buffer_load_dwordx4 a[244:247], v9, s[56:59], s63 offen offset:1024 sc1\n\t"
    "buffer_load_dwordx4 a[248:251], v9, s[56:59], s63 offen offset:2048 sc1\n\t"
    "buffer_load_dwordx4 a[252:255], v9, s[56:59], s63 offen offset:3072 sc1\n\t"
    "s_add_u32 s63, s63, 4096\n\t"
    "buffer_load_dwordx4 v[224:227], v9, s[56:59], s63 offen offset:0 sc1\n\t"
    "buffer_load_dwordx4 v[228:231], v9, s[56:59], s63 offen offset:1024 sc1\n\t"
    "buffer_load_dwordx4 v[232:235], v9, s[56:59], s63 offen offset:2048 sc1\n\t"
    "buffer_load_dwordx4 v[236:239], v9, s[56:59], s63 offen offset:3072 sc1\n\t"
    "s_add_u32 s63, s63, 4096\n\t"
    "buffer_load_dwordx4 v[240:243], v9, s[56:59], s63 offen offset:0 sc1\n\t"
    "buffer_load_dwordx4 v[244:247], v9, s[56:59], s63 offen offset:1024 sc1\n\t"
    "buffer_load_dwordx4 v[248:251], v9, s[56:59], s63 offen offset:2048 sc1\n\t"
    "buffer_load_dwordx4 v[252:255], v9, s[56:59], s63 offen offset:3072 sc1\n\t"
    "s_add_u32 s63, s63, 4096\n\t"
    ".Lh1_%=:\n\t"
    "s_cbranch_execz .Lh2_%=\n\t"
    "s_barrier\n\t"
    "s_mov_b64 exec, %[amask]\n\t"
    "buffer_store_dwordx2 v[22:23], off, s[56:59], %[aown] sc1\n\t"
    "s_mov_b64 exec, -1\n\t"
    "ds_read_b128 v[60:63], v0 offset:0\n\t"
    "ds_read_b128 v[64:67], v1 offset:0\n\t"
    "ds_read_b128 v[68:71], v2 offset:0\n\t"
    "ds_read_b128 v[72:75], v3 offset:0\n\t"
    "ds_read_b128 v[188:191], v4 offset:0\n\t"
    "ds_read_b128 v[76:79], v5 offset:0\n\t"
    "ds_read_b128 v[80:83], v5 offset:6144\n\t"
    "ds_read_b128 v[108:111], v4 offset:6144\n\t"
    "ds_read_b128 v[84:87], v3 offset:6144\n\t"
    "ds_read_b128 v[88:91], v2 offset:6144\n\t"
    "ds_read_b128 v[144:147], v1 offset:6144\n\t"
    "ds_read_b128 v[196:199], v0 offset:6144\n\t"
    "s_waitcnt lgkmcnt(0)\n\t"
    "s_add_u32 s61, %[wb], 0xff400000\n\t"
    "s_mov_b32 s62, %[rbeg]\n\t"
    "s_mov_b32 s60, 8\n\t"
    ".Lmin_%=:\n\t"
    "s_waitcnt lgkmcnt(3)\n\t"
    "s_waitcnt vmcnt(63)\n\t"
    "v_mfma_f32_16x16x32_bf16 v[24:27], v[60:63], a[0:3], 0\n\t"
    "v_cvt_pk_bf16_f32 v159, v30, v34\n\t"
    "v_cvt_pk_bf16_f32 v187, v31, v35\n\t"
    "v_mfma_f32_16x16x32_bf16 v[24:27], v[64:67], a[4:7], v[24:27]\n\t"
    "v_cvt_pk_bf16_f32 v104, v36, v12\n\t"
    "v_cvt_pk_bf16_f32 v106, v37, v13\n\t"
    "v_mfma_f32_16x16x32_bf16 v[24:27], v[68:71], a[8:11], v[24:27]\n\t"
    "v_cvt_pk_bf16_f32 v180, v38, v14\n\t"
    "v_cvt_pk_bf16_f32 v182, v39, v15\n\t"
    "v_mfma_f32_16x16x32_bf16 v[24:27], v[72:75], a[12:15], v[24:27]\n\t"
    "v_cvt_pk_bf16_f32 v105, v16, v20\n\t"
    "v_cvt_pk_bf16_f32 v107, v17, v21\n\t"
    "v_mfma_f32_16x16x32_bf16 v[24:27], v[188:191], a[16:19], v[24:27]\n\t"
    "v_cvt_pk_bf16_f32 v181, v18, v22\n\t"
    "v_cvt_pk_bf16_f32 v183, v19, v23\n\t"
    "v_mfma_f32_16x16x32_bf16 v[24:27], v[76:79], a[20:23], v[24:27]\n\t"
    "v_mfma_f32_16x16x32_bf16 v[28:31], v[80:83], a[20:23], 0\n\t"
    "v_mfma_f32_16x16x32_bf16 v[28:31], v[108:111], a[16:19], v[28:31]\n\t"
    "v_mfma_f32_16x16x32_bf16 v[28:31], v[84:87], a[12:15], v[28:31]\n\t"
    "s_waitcnt lgkmcnt(0)\n\t"
    "v_mfma_f32_16x16x32_bf16 v[28:31], v[88:91], a[8:11], v[28:31]\n\t"
    "v_cvt_pk_bf16_f32 v12, v24, v25\n\t"
    "v_cvt_pk_bf16_f32 v13, v26, v27\n\t"
    "v_mfma_f32_16x16x32_bf16 v[28:31], v[144:147], a[4:7], v[28:31]\n\t"
    "buffer_store_dwordx2 v[154:155], v8, s[52:55], s61 offen offset:1152 sc1\n\t"
    "v_mfma_f32_16x16x32_bf16 v[28:31], v[196:199], a[0:3], v[28:31]\n\t"
    "s_add_u32 s61, s61, 393216\n\t"
    "s_waitcnt vmcnt(62)\n\t"
    "v_mfma_f32_16x16x32_bf16 v[20:23], v[196:199], a[24:27], 0\n\t"
    "v_mfma_f32_16x16x32_bf16 v[20:23], v[144:147], a[28:31], v[20:23]\n\t"
    "v_mfma_f32_16x16x32_bf16 v[20:23], v[88:91], a[32:35], v[20:23]\n\t"
    "v_mfma_f32_16x16x32_bf16 v[20:23], v[84:87], a[36:39], v[20:23]\n\t"
    "v_cvt_pk_bf16_f32 v14, v28, v29\n\t"
    "v_cvt_pk_bf16_f32 v15, v30, v31\n\t"
    "v_mfma_f32_16x16x32_bf16 v[20:23], v[108:111], a[40:43], v[20:23]\n\t"
    "v_mfma_f32_16x16x32_bf16 v[20:23], v[80:83], a[44:47], v[20:23]\n\t"
    "v_mfma_f32_16x16x32_bf16 v[24:27], v[76:79], a[44:47], 0\n\t"
    "v_mfma_f32_16x16x32_bf16 v[24:27], v[188:191], a[40:43], v[24:27]\n\t"
    "v_mfma_f32_16x16x32_bf16 v[24:27], v[72:75], a[36:39], v[24:27]\n\t"
    "v_mfma_f32_16x16x32_bf16 v[24:27], v[68:71], a[32:35], v[24:27]\n\t"
    "v_cvt_pk_bf16_f32 v18, v20, v21\n\t"
    "v_cvt_pk_bf16_f32 v19, v22, v23\n\t"
    "v_mfma_f32_16x16x32_bf16 v[24:27], v[64:67], a[28:31], v[24:27]\n\t"
    "buffer_store_dwordx4 v[136:139], v7, s[52:55], s61 offen offset:0 sc1\n\t"
    "v_mfma_f32_16x16x32_bf16 v[24:27], v[60:63], a[24:27], v[24:27]\n\t"
    "s_waitcnt vmcnt(57)\n\t"
    "v_mfma_f32_16x16x32_bf16 v[28:31], v[60:63], a[48:51], 0\n\t"
    "v_mfma_f32_16x16x32_bf16 v[28:31], v[64:67], a[52:55], v[28:31]\n\t"
    "v_mfma_f32_16x16x32_bf16 v[28:31], v[68:71], a[56:59], v[28:31]\n\t"
    "v_mfma_f32_16x16x32_bf16 v[28:31], v[72:75], a[60:63], v[28:31]\n\t"
    "v_cvt_pk_bf16_f32 v16, v24, v25\n\t"
    "v_cvt_pk_bf16_f32 v17, v26, v27\n\t"
    "v_mfma_f32_16x16x32_bf16 v[28:31], v[188:191], a[64:67], v[28:31]\n\t"
    "v_mfma_f32_16x16x32_bf16 v[28:31], v[76:79], a[68:71], v[28:31]\n\t"
    "v_mfma_f32_16x16x32_bf16 v[24:27], v[80:83], a[68:71], 0\n\t"
    "v_mfma_f32_16x16x32_bf16 v[24:27], v[108:111], a[64:67], v[24:27]\n\t"
    "v_mfma_f32_16x16x32_bf16 v[24:27], v[84:87], a[60:63], v[24:27]\n\t"
    "v_mfma_f32_16x16x32_bf16 v[24:27], v[88:91], a[56:59], v[24:27]\n\t"
    "v_cvt_pk_bf16_f32 v20, v28, v29\n\t"
    "v_cvt_pk_bf16_f32 v21, v30, v31\n\t"
    "v_mfma_f32_16x16x32_bf16 v[24:27], v[144:147], a[52:55], v[24:27]\n\t"
    "buffer_store_dwordx4 v[132:135], v7, s[52:55], s61 offen offset:384 sc1\n\t"
    "v_mfma_f32_16x16x32_bf16 v[24:27], v[196:199], a[48:51], v[24:27]\n\t"
    "s_waitcnt vmcnt(52)\n\t"
    "v_mfma_f32_16x16x32_bf16 v[28:31], v[196:199], a[72:75], 0\n\t"
    "v_mfma_f32_16x16x32_bf16 v[28:31], v[144:147], a[76:79], v[28:31]\n\t"
    "v_mfma_f32_16x16x32_bf16 v[28:31], v[88:91], a[80:83], v[28:31]\n\t"
    "v_mfma_f32_16x16x32_bf16 v[28:31], v[84:87], a[84:87], v[28:31]\n\t"
    "v_cvt_pk_bf16_f32 v22, v24, v25\n\t"
    "v_cvt_pk_bf16_f32 v23, v26, v27\n\t"
    "v_mfma_f32_16x16x32_bf16 v[28:31], v[108:111], a[88:91], v[28:31]\n\t"
    "v_mfma_f32_16x16x32_bf16 v[28:31], v[80:83], a[92:95], v[28:31]\n\t"
    "v_mfma_f32_16x16x32_bf16 v[32:35], v[76:79], a[92:95], 0\n\t"
    "v_mfma_f32_16x16x32_bf16 v[32:35], v[188:191], a[88:91], v[32:35]\n\t"
    "v_mfma_f32_16x16x32_bf16 v[32:35], v[72:75], a[84:87], v[32:35]\n\t"
    "v_mfma_f32_16x16x32_bf16 v[32:35], v[68:71], a[80:83], v[32:35]\n\t"
    "v_cvt_pk_bf16_f32 v26, v28, v29\n\t"
    "v_cvt_pk_bf16_f32 v27, v30, v31\n\t"
    "v_mfma_f32_16x16x32_bf16 v[32:35], v[64:67], a[76:79], v[32:35]\n\t"
    "buffer_store_dwordx4 v[164:167], v7, s[52:55], s61 offen offset:768 sc1\n\t"
    "v_mfma_f32_16x16x32_bf16 v[32:35], v[60:63], a[72:75], v[32:35]\n\t"
    "s_waitcnt vmcnt(47)\n\t"
    "v_mfma_f32_16x16x32_bf16 v[36:39], v[60:63], a[96:99], 0\n\t"
    "v_mfma_f32_16x16x32_bf16 v[36:39], v[64:67], a[100:103], v[36:39]\n\t"
    "v_mfma_f32_16x16x32_bf16 v[36:39], v[68:71], a[104:107], v[36:39]\n\t"
    "v_mfma_f32_16x16x32_bf16 v[36:39], v[72:75], a[108:111], v[36:39]\n\t"
    "v_cvt_pk_bf16_f32 v24, v32, v33\n\t"
    "v_cvt_pk_bf16_f32 v25, v34, v35\n\t"
    "v_mfma_f32_16x16x32_bf16 v[36:39], v[188:191], a[112:115], v[36:39]\n\t"
    "v_mfma_f32_16x16x32_bf16 v[36:39], v[76:79], a[116:119], v[36:39]\n\t"
    "v_mfma_f32_16x16x32_bf16 v[32:35], v[80:83], a[116:119], 0\n\t"
    "v_mfma_f32_16x16x32_bf16 v[32:35], v[108:111], a[112:115], v[32:35]\n\t"
    "v_mfma_f32_16x16x32_bf16 v[32:35], v[84:87], a[108:111], v[32:35]\n\t"
    "v_mfma_f32_16x16x32_bf16 v[32:35], v[88:91], a[104:107], v[32:35]\n\t"
    "v_cvt_pk_bf16_f32 v28, v36, v37\n\t"
    "v_cvt_pk_bf16_f32 v29, v38, v39\n\t"
    "v_mfma_f32_16x16x32_bf16 v[32:35], v[144:147], a[100:103], v[32:35]\n\t"
    "buffer_store_dwordx4 v[140:143], v7, s[52:55], s61 offen offset:1152 sc1\n\t"
    "v_mfma_f32_16x16x32_bf16 v[32:35], v[196:199], a[96:99], v[32:35]\n\t"
    "s_waitcnt vmcnt(42)\n\t"
    "v_mfma_f32_16x16x32_bf16 v[36:39], v[196:199], a[120:123], 0\n\t"
    "v_mfma_f32_16x16x32_bf16 v[36:39], v[144:147], a[124:127], v[36:39]\n\t"
    "v_mfma_f32_16x16x32_bf16 v[36:39], v[88:91], a[128:131], v[36:39]\n\t"
    "v_mfma_f32_16x16x32_bf16 v[36:39], v[84:87], a[132:135], v[36:39]\n\t"
    "v_cvt_pk_bf16_f32 v30, v32, v33\n\t"
    "v_cvt_pk_bf16_f32 v31, v34, v35\n\t"
    "v_mfma_f32_16x16x32_bf16 v[36:39], v[108:111], a[136:139], v[36:39]\n\t"
    "v_mfma_f32_16x16x32_bf16 v[36:39], v[80:83], a[140:143], v[36:39]\n\t"
    "v_mfma_f32_16x16x32_bf16 v[40:43], v[76:79], a[140:143], 0\n\t"
    "v_mfma_f32_16x16x32_bf16 v[40:43], v[188:191], a[136:139], v[40:43]\n\t"
    "v_mfma_f32_16x16x32_bf16 v[40:43], v[72:75], a[132:135], v[40:43]\n\t"
    "v_mfma_f32_16x16x32_bf16 v[40:43], v[68:71], a[128:131], v[40:43]\n\t"
    "v_cvt_pk_bf16_f32 v34, v36, v37\n\t"
    "v_cvt_pk_bf16_f32 v35, v38, v39\n\t"
    "v_mfma_f32_16x16x32_bf16 v[40:43], v[64:67], a[124:127], v[40:43]\n\t"
    "buffer_store_dwordx2 v[148:149], v8, s[52:55], s61 offen offset:0 sc1\n\t"
    "v_mfma_f32_16x16x32_bf16 v[40:43], v[60:63], a[120:123], v[40:43]\n\t"
    "s_waitcnt vmcnt(37)\n\t"
    "v_mfma_f32_16x16x32_bf16 v[44:47], v[60:63], a[144:147], 0\n\t"
    "v_mfma_f32_16x16x32_bf16 v[44:47], v[64:67], a[148:151], v[44:47]\n\t"
    "v_mfma_f32_16x16x32_bf16 v[44:47], v[68:71], a[152:155], v[44:47]\n\t"
    "v_mfma_f32_16x16x32_bf16 v[44:47], v[72:75], a[156:159], v[44:47]\n\t"
    "v_cvt_pk_bf16_f32 v32, v40, v41\n\t"
    "v_cvt_pk_bf16_f32 v33, v42, v43\n\t"
    "v_mfma_f32_16x16x32_bf16 v[44:47], v[188:191], a[160:163], v[44:47]\n\t"
    "v_mfma_f32_16x16x32_bf16 v[44:47], v[76:79], a[164:167], v[44:47]\n\t"
    "v_mfma_f32_16x16x32_bf16 v[40:43], v[80:83], a[164:167], 0\n\t"
    "v_mfma_f32_16x16x32_bf16 v[40:43], v[108:111], a[160:163], v[40:43]\n\t"
    "v_mfma_f32_16x16x32_bf16 v[40:43], v[84:87], a[156:159], v[40:43]\n\t"
    "v_mfma_f32_16x16x32_bf16 v[40:43], v[88:91], a[152:155], v[40:43]\n\t"
    "v_cvt_pk_bf16_f32 v36, v44, v45\n\t"
    "v_cvt_pk_bf16_f32 v37, v46, v47\n\t"
    "v_mfma_f32_16x16x32_bf16 v[40:43], v[144:147], a[148:151], v[40:43]\n\t"
    "buffer_store_dwordx2 v[150:151], v8, s[52:55], s61 offen offset:384 sc1\n\t"
    "v_mfma_f32_16x16x32_bf16 v[40:43], v[196:199], a[144:147], v[40:43]\n\t"
    "s_waitcnt vmcnt(32)\n\t"
    "v_mfma_f32_16x16x32_bf16 v[44:47], v[196:199], a[168:171], 0\n\t"
    "v_mfma_f32_16x16x32_bf16 v[44:47], v[144:147], a[172:175], v[44:47]\n\t"
    "v_mfma_f32_16x16x32_bf16 v[44:47], v[88:91], a[176:179], v[44:47]\n\t"
    "v_mfma_f32_16x16x32_bf16 v[44:47], v[84:87], a[180:183], v[44:47]\n\t"
    "v_cvt_pk_bf16_f32 v38, v40, v41\n\t"
    "v_cvt_pk_bf16_f32 v39, v42, v43\n\t"
    "v_mfma_f32_16x16x32_bf16 v[44:47], v[108:111], a[184:187], v[44:47]\n\t"
    "v_mfma_f32_16x16x32_bf16 v[44:47], v[80:83], a[188:191], v[44:47]\n\t"
    "v_mfma_f32_16x16x32_bf16 v[48:51], v[76:79], a[188:191], 0\n\t"
    "v_mfma_f32_16x16x32_bf16 v[48:51], v[188:191], a[184:187], v[48:51]\n\t"
    "v_mfma_f32_16x16x32_bf16 v[48:51], v[72:75], a[180:183], v[48:51]\n\t"
    "v_mfma_f32_16x16x32_bf16 v[48:51], v[68:71], a[176:179], v[48:51]\n\t"
    "v_cvt_pk_bf16_f32 v42, v44, v45\n\t"
    "v_cvt_pk_bf16_f32 v43, v46, v47\n\t"
    "v_mfma_f32_16x16x32_bf16 v[48:51], v[64:67], a[172:175], v[48:51]\n\t"
    "buffer_store_dwordx2 v[176:177], v8, s[52:55], s61 offen offset:768 sc1\n\t"
    "v_mfma_f32_16x16x32_bf16 v[48:51], v[60:63], a[168:171], v[48:51]\n\t"
    "s_waitcnt vmcnt(27)\n\t"
    "v_mfma_f32_16x16x32_bf16 v[52:55], v[60:63], a[192:195], 0\n\t"
    "v_mfma_f32_16x16x32_bf16 v[52:55], v[64:67], a[196:199], v[52:55]\n\t"
    "v_mfma_f32_16x16x32_bf16 v[52:55], v[68:71], a[200:203], v[52:55]\n\t"
    "v_mfma_f32_16x16x32_bf16 v[52:55], v[72:75], a[204:207], v[52:55]\n\t"
    "v_cvt_pk_bf16_f32 v40, v48, v49\n\t"
    "v_cvt_pk_bf16_f32 v41, v50, v51\n\t"
    "v_mfma_f32_16x16x32_bf16 v[52:55], v[188:191], a[208:211], v[52:55]\n\t"
    "v_mfma_f32_16x16x32_bf16 v[52:55], v[76:79], a[212:215], v[52:55]\n\t"
    "v_mfma_f32_16x16x32_bf16 v[48:51], v[80:83], a[212:215], 0\n\t"
    "v_mfma_f32_16x16x32_bf16 v[48:51], v[108:111], a[208:211], v[48:51]\n\t"
    "v_mfma_f32_16x16x32_bf16 v[48:51], v[84:87], a[204:207], v[48:51]\n\t"
    "v_mfma_f32_16x16x32_bf16 v[48:51], v[88:91], a[200:203], v[48:51]\n\t"
    "v_cvt_pk_bf16_f32 v44, v52, v53\n\t"
    "v_cvt_pk_bf16_f32 v45, v54, v55\n\t"
    "v_mfma_f32_16x16x32_bf16 v[48:51], v[144:147], a[196:199], v[48:51]\n\t"
    "buffer_store_dwordx2 v[178:179], v8, s[52:55], s61 offen offset:1152 sc1\n\t"
    "v_mfma_f32_16x16x32_bf16 v[48:51], v[196:199], a[192:195], v[48:51]\n\t"
    "s_add_u32 s61, s61, 393216\n\t"
    "s_waitcnt vmcnt(22)\n\t"
    "v_mfma_f32_16x16x32_bf16 v[52:55], v[196:199], a[216:219], 0\n\t"
    "v_mfma_f32_16x16x32_bf16 v[52:55], v[144:147], a[220:223], v[52:55]\n\t"
    "v_mfma_f32_16x16x32_bf16 v[52:55], v[88:91], a[224:227], v[52:55]\n\t"
    "v_mfma_f32_16x16x32_bf16 v[52:55], v[84:87], a[228:231], v[52:55]\n\t"
    "v_cvt_pk_bf16_f32 v46, v48, v49\n\t"
    "v_cvt_pk_bf16_f32 v47, v50, v51\n\t"
    "v_mfma_f32_16x16x32_bf16 v[52:55], v[108:111], a[232:235], v[52:55]\n\t"
    "v_mfma_f32_16x16x32_bf16 v[52:55], v[80:83], a[236:239], v[52:55]\n\t"
    "v_mfma_f32_16x16x32_bf16 v[56:59], v[76:79], a[236:239], 0\n\t"
    "v_mfma_f32_16x16x32_bf16 v[56:59], v[188:191], a[232:235], v[56:59]\n\t"
    "v_mfma_f32_16x16x32_bf16 v[56:59], v[72:75], a[228:231], v[56:59]\n\t"
    "v_mfma_f32_16x16x32_bf16 v[56:59], v[68:71], a[224:227], v[56:59]\n\t"
    "v_cvt_pk_bf16_f32 v50, v52, v53\n\t"
    "v_cvt_pk_bf16_f32 v51, v54, v55\n\t"
    "v_mfma_f32_16x16x32_bf16 v[56:59], v[64:67], a[220:223], v[56:59]\n\t"
    "buffer_store_dwordx4 v[160:163], v7, s[52:55], s61 offen offset:0 sc1\n\t"
    "v_mfma_f32_16x16x32_bf16 v[56:59], v[60:63], a[216:219], v[56:59]\n\t"
    "s_waitcnt vmcnt(17)\n\t"
    "v_mfma_f32_16x16x32_bf16 v[92:95], v[60:63], a[240:243], 0\n\t"
    "v_mfma_f32_16x16x32_bf16 v[92:95], v[64:67], a[244:247], v[92:95]\n\t"
    "v_mfma_f32_16x16x32_bf16 v[92:95], v[68:71], a[248:251], v[92:95]\n\t"
    "v_mfma_f32_16x16x32_bf16 v[92:95], v[72:75], a[252:255], v[92:95]\n\t"
    "v_cvt_pk_bf16_f32 v48, v56, v57\n\t"
    "v_cvt_pk_bf16_f32 v49, v58, v59\n\t"
    "v_mfma_f32_16x16x32_bf16 v[92:95], v[188:191], v[224:227], v[92:95]\n\t"
    "v_mfma_f32_16x16x32_bf16 v[92:95], v[76:79], v[228:231], v[92:95]\n\t"
    "v_mfma_f32_16x16x32_bf16 v[56:59], v[80:83], v[228:231], 0\n\t"
    "v_mfma_f32_16x16x32_bf16 v[56:59], v[108:111], v[224:227], v[56:59]\n\t"
    "ds_read_b128 v[112:115], v0 offset:12288\n\t"
    "v_mfma_f32_16x16x32_bf16 v[56:59], v[84:87], a[252:255], v[56:59]\n\t"
    "ds_read_b128 v[132:135], v1 offset:12288\n\t"
    "v_mfma_f32_16x16x32_bf16 v[56:59], v[88:91], a[248:251], v[56:59]\n\t"
    "ds_read_b128 v[136:139], v2 offset:12288\n\t"
    "v_cvt_pk_bf16_f32 v52, v92, v93\n\t"
    "v_mfma_f32_16x16x32_bf16 v[56:59], v[144:147], a[244:247], v[56:59]\n\t"
    "ds_read_b128 v[140:143], v3 offset:12288\n\t"
    "v_cvt_pk_bf16_f32 v53, v94, v95\n\t"
    "v_mfma_f32_16x16x32_bf16 v[56:59], v[196:199], a[240:243], v[56:59]\n\t"
    "ds_read_b128 v[148:151], v4 offset:12288\n\t"
    "s_waitcnt vmcnt(11)\n\t"
    "v_mfma_f32_16x16x32_bf16 v[96:99], v[196:199], v[232:235], 0\n\t"
    "ds_read_b128 v[152:155], v5 offset:12288\n\t"
    "v_mfma_f32_16x16x32_bf16 v[96:99], v[144:147], v[236:239], v[96:99]\n\t"
    "buffer_store_dwordx4 v[128:131], v7, s[52:55], s61 offen offset:384 sc1\n\t"
    "v_mfma_f32_16x16x32_bf16 v[96:99], v[88:91], v[240:243], v[96:99]\n\t"
    "ds_read_b128 v[160:163], v5 offset:18432\n\t"
    "v_mfma_f32_16x16x32_bf16 v[96:99], v[84:87], v[244:247], v[96:99]\n\t"
    "ds_read_b128 v[164:167], v4 offset:18432\n\t"
    "v_cvt_pk_bf16_f32 v54, v56, v57\n\t"
    "v_mfma_f32_16x16x32_bf16 v[96:99], v[108:111], v[248:251], v[96:99]\n\t"
    "ds_read_b128 v[128:131], v3 offset:18432\n\t"
    "v_cvt_pk_bf16_f32 v55, v58, v59\n\t"
    "v_mfma_f32_16x16x32_bf16 v[96:99], v[80:83], v[252:255], v[96:99]\n\t"
    "ds_read_b128 v[144:147], v2 offset:18432\n\t"
    "v_mfma_f32_16x16x32_bf16 v[84:87], v[76:79], v[252:255], 0\n\t"
    "ds_read_b128 v[176:179], v1 offset:18432\n\t"
    "v_mfma_f32_16x16x32_bf16 v[84:87], v[188:191], v[248:251], v[84:87]\n\t"
    "ds_read_b128 v[108:111], v0 offset:18432\n\t"
    "v_mfma_f32_16x16x32_bf16 v[84:87], v[72:75], v[244:247], v[84:87]\n\t"
    "v_mfma_f32_16x16x32_bf16 v[84:87], v[68:71], v[240:243], v[84:87]\n\t"
    "v_cvt_pk_bf16_f32 v58, v96, v97\n\t"
    "v_cvt_pk_bf16_f32 v59, v98, v99\n\t"
    "v_mfma_f32_16x16x32_bf16 v[84:87], v[64:67], v[236:239], v[84:87]\n\t"
    "buffer_store_dwordx4 v[172:175], v7, s[52:55], s61 offen offset:768 sc1\n\t"
    "v_mfma_f32_16x16x32_bf16 v[84:87], v[60:63], v[232:235], v[84:87]\n\t"
    "s_waitcnt lgkmcnt(3)\n\t"
    "v_mfma_f32_16x16x32_bf16 v[68:71], v[112:115], a[0:3], 0\n\t"
    "v_mfma_f32_16x16x32_bf16 v[68:71], v[132:135], a[4:7], v[68:71]\n\t"
    "v_mfma_f32_16x16x32_bf16 v[68:71], v[136:139], a[8:11], v[68:71]\n\t"
    "v_mfma_f32_16x16x32_bf16 v[68:71], v[140:143], a[12:15], v[68:71]\n\t"
    "v_cvt_pk_bf16_f32 v56, v84, v85\n\t"
    "v_cvt_pk_bf16_f32 v57, v86, v87\n\t"
    "v_mfma_f32_16x16x32_bf16 v[68:71], v[148:151], a[16:19], v[68:71]\n\t"
    "v_mfma_f32_16x16x32_bf16 v[68:71], v[152:155], a[20:23], v[68:71]\n\t"
    "v_mfma_f32_16x16x32_bf16 v[64:67], v[160:163], a[20:23], 0\n\t"
    "v_mfma_f32_16x16x32_bf16 v[64:67], v[164:167], a[16:19], v[64:67]\n\t"
    "v_mfma_f32_16x16x32_bf16 v[64:67], v[128:131], a[12:15], v[64:67]\n\t"
    "s_waitcnt lgkmcnt(0)\n\t"
    "v_mfma_f32_16x16x32_bf16 v[64:67], v[144:147], a[8:11], v[64:67]\n\t"
    "v_cvt_pk_bf16_f32 v60, v68, v69\n\t"
    "v_cvt_pk_bf16_f32 v61, v70, v71\n\t"
    "v_mfma_f32_16x16x32_bf16 v[64:67], v[176:179], a[4:7], v[64:67]\n\t"
    "buffer_store_dwordx4 v[168:171], v7, s[52:55], s61 offen offset:1152 sc1\n\t"
    "v_mfma_f32_16x16x32_bf16 v[64:67], v[108:111], a[0:3], v[64:67]\n\t"
    "v_mfma_f32_16x16x32_bf16 v[68:71], v[108:111], a[24:27], 0\n\t"
    "v_mfma_f32_16x16x32_bf16 v[68:71], v[176:179], a[28:31], v[68:71]\n\t"
    "v_mfma_f32_16x16x32_bf16 v[68:71], v[144:147], a[32:35], v[68:71]\n\t"
    "v_mfma_f32_16x16x32_bf16 v[68:71], v[128:131], a[36:39], v[68:71]\n\t"
    "v_cvt_pk_bf16_f32 v62, v64, v65\n\t"
    "v_cvt_pk_bf16_f32 v63, v66, v67\n\t"
    "v_mfma_f32_16x16x32_bf16 v[68:71], v[164:167], a[40:43], v[68:71]\n\t"
    "v_mfma_f32_16x16x32_bf16 v[68:71], v[160:163], a[44:47], v[68:71]\n\t"
    "v_mfma_f32_16x16x32_bf16 v[72:75], v[152:155], a[44:47], 0\n\t"
    "v_mfma_f32_16x16x32_bf16 v[72:75], v[148:151], a[40:43], v[72:75]\n\t"
    "v_mfma_f32_16x16x32_bf16 v[72:75], v[140:143], a[36:39], v[72:75]\n\t"
    "v_mfma_f32_16x16x32_bf16 v[72:75], v[136:139], a[32:35], v[72:75]\n\t"
    "v_cvt_pk_bf16_f32 v66, v68, v69\n\t"
    "v_cvt_pk_bf16_f32 v67, v70, v71\n\t"
    "v_mfma_f32_16x16x32_bf16 v[72:75], v[132:135], a[28:31], v[72:75]\n\t"
    "buffer_store_dwordx2 v[116:117], v8, s[52:55], s61 offen offset:0 sc1\n\t"
    "v_mfma_f32_16x16x32_bf16 v[72:75], v[112:115], a[24:27], v[72:75]\n\t"
    "v_mfma_f32_16x16x32_bf16 v[76:79], v[112:115], a[48:51], 0\n\t"
    "v_mfma_f32_16x16x32_bf16 v[76:79], v[132:135], a[52:55], v[76:79]\n\t"
    "v_mfma_f32_16x16x32_bf16 v[76:79], v[136:139], a[56:59], v[76:79]\n\t"
    "v_mfma_f32_16x16x32_bf16 v[76:79], v[140:143], a[60:63], v[76:79]\n\t"
    "v_cvt_pk_bf16_f32 v64, v72, v73\n\t"
    "v_cvt_pk_bf16_f32 v65, v74, v75\n\t"
    "v_mfma_f32_16x16x32_bf16 v[76:79], v[148:151], a[64:67], v[76:79]\n\t"
    "v_mfma_f32_16x16x32_bf16 v[76:79], v[152:155], a[68:71], v[76:79]\n\t"
    "v_mfma_f32_16x16x32_bf16 v[72:75], v[160:163], a[68:71], 0\n\t"
    "v_mfma_f32_16x16x32_bf16 v[72:75], v[164:167], a[64:67], v[72:75]\n\t"
    "v_mfma_f32_16x16x32_bf16 v[72:75], v[128:131], a[60:63], v[72:75]\n\t"
    "v_mfma_f32_16x16x32_bf16 v[72:75], v[144:147], a[56:59], v[72:75]\n\t"
    "v_cvt_pk_bf16_f32 v68, v76, v77\n\t"
    "v_cvt_pk_bf16_f32 v69, v78, v79\n\t"
    "v_mfma_f32_16x16x32_bf16 v[72:75], v[176:179], a[52:55], v[72:75]\n\t"
    "buffer_store_dwordx2 v[118:119], v8, s[52:55], s61 offen offset:384 sc1\n\t"
    "v_mfma_f32_16x16x32_bf16 v[72:75], v[108:111], a[48:51], v[72:75]\n\t"
    "v_mfma_f32_16x16x32_bf16 v[76:79], v[108:111], a[72:75], 0\n\t"
    "s_add_i32 s64, s62, 24576\n\t"
    "v_mfma_f32_16x16x32_bf16 v[76:79], v[176:179], a[76:79], v[76:79]\n\t"
    "s_cmp_eq_u32 s64, %[rend]\n\t"
    "s_cselect_b32 s64, %[rbeg], s64\n\t"
    "v_mfma_f32_16x16x32_bf16 v[76:79], v[144:147], a[80:83], v[76:79]\n\t"
    "s_sub_i32 s65, s64, s62\n\t"
    "s_mov_b32 s62, s64\n\t"
    "v_mfma_f32_16x16x32_bf16 v[76:79], v[128:131], a[84:87], v[76:79]\n\t"
    "v_add_u32_e32 v0, s65, v0\n\t"
    "v_add_u32_e32 v1, s65, v1\n\t"
    "v_mfma_f32_16x16x32_bf16 v[76:79], v[164:167], a[88:91], v[76:79]\n\t"
    "v_add_u32_e32 v2, s65, v2\n\t"
    "v_add_u32_e32 v3, s65, v3\n\t"
    "v_mfma_f32_16x16x32_bf16 v[76:79], v[160:163], a[92:95], v[76:79]\n\t"
    "v_add_u32_e32 v4, s65, v4\n\t"
    "v_add_u32_e32 v5, s65, v5\n\t"
    "v_mfma_f32_16x16x32_bf16 v[80:83], v[152:155], a[92:95], 0\n\t"
    "v_cvt_pk_bf16_f32 v70, v72, v73\n\t"
    "v_cvt_pk_bf16_f32 v71, v74, v75\n\t"
    "v_mfma_f32_16x16x32_bf16 v[80:83], v[148:151], a[88:91], v[80:83]\n\t"
    "v_mfma_f32_16x16x32_bf16 v[80:83], v[140:143], a[84:87], v[80:83]\n\t"
    "v_mfma_f32_16x16x32_bf16 v[80:83], v[136:139], a[80:83], v[80:83]\n\t"
    "v_cvt_pk_bf16_f32 v74, v76, v77\n\t"
    "v_cvt_pk_bf16_f32 v75, v78, v79\n\t"
    "v_mfma_f32_16x16x32_bf16 v[80:83], v[132:135], a[76:79], v[80:83]\n\t"
    "buffer_store_dwordx2 v[192:193], v8, s[52:55], s61 offen offset:768 sc1\n\t"
    "v_mfma_f32_16x16x32_bf16 v[80:83], v[112:115], a[72:75], v[80:83]\n\t"
    "v_mfma_f32_16x16x32_bf16 v[84:87], v[112:115], a[96:99], 0\n\t"
    "v_mfma_f32_16x16x32_bf16 v[84:87], v[132:135], a[100:103], v[84:87]\n\t"
    "v_mfma_f32_16x16x32_bf16 v[84:87], v[136:139], a[104:107], v[84:87]\n\t"
    "v_mfma_f32_16x16x32_bf16 v[84:87], v[140:143], a[108:111], v[84:87]\n\t"
    "v_cvt_pk_bf16_f32 v72, v80, v81\n\t"
    "v_cvt_pk_bf16_f32 v73, v82, v83\n\t"
    "v_mfma_f32_16x16x32_bf16 v[84:87], v[148:151], a[112:115], v[84:87]\n\t"
    "v_mfma_f32_16x16x32_bf16 v[84:87], v[152:155], a[116:119], v[84:87]\n\t"
    "v_mfma_f32_16x16x32_bf16 v[80:83], v[160:163], a[116:119], 0\n\t"
    "v_mfma_f32_16x16x32_bf16 v[80:83], v[164:167], a[112:115], v[80:83]\n\t"
    "v_mfma_f32_16x16x32_bf16 v[80:83], v[128:131], a[108:111], v[80:83]\n\t"
    "v_mfma_f32_16x16x32_bf16 v[80:83], v[144:147], a[104:107], v[80:83]\n\t"
    "v_cvt_pk_bf16_f32 v76, v84, v85\n\t"
    "v_cvt_pk_bf16_f32 v77, v86, v87\n\t"
    "v_mfma_f32_16x16x32_bf16 v[80:83], v[176:179], a[100:103], v[80:83]\n\t"
    "buffer_store_dwordx2 v[194:195], v8, s[52:55], s61 offen offset:1152 sc1\n\t"
    "v_mfma_f32_16x16x32_bf16 v[80:83], v[108:111], a[96:99], v[80:83]\n\t"
    "s_add_u32 s61, s61, 393216\n\t"
    "v_mfma_f32_16x16x32_bf16 v[84:87], v[108:111], a[120:123], 0\n\t"
    "v_mfma_f32_16x16x32_bf16 v[84:87], v[176:179], a[124:127], v[84:87]\n\t"
    "v_mfma_f32_16x16x32_bf16 v[84:87], v[144:147], a[128:131], v[84:87]\n\t"
    "v_mfma_f32_16x16x32_bf16 v[84:87], v[128:131], a[132:135], v[84:87]\n\t"
    "v_cvt_pk_bf16_f32 v78, v80, v81\n\t"
    "v_cvt_pk_bf16_f32 v79, v82, v83\n\t"
    "v_mfma_f32_16x16x32_bf16 v[84:87], v[164:167], a[136:139], v[84:87]\n\t"
    "v_mfma_f32_16x16x32_bf16 v[84:87], v[160:163], a[140:143], v[84:87]\n\t"
    "v_mfma_f32_16x16x32_bf16 v[88:91], v[152:155], a[140:143], 0\n\t"
    "v_mfma_f32_16x16x32_bf16 v[88:91], v[148:151], a[136:139], v[88:91]\n\t"
    "v_mfma_f32_16x16x32_bf16 v[88:91], v[140:143], a[132:135], v[88:91]\n\t"
    "v_mfma_f32_16x16x32_bf16 v[88:91], v[136:139], a[128:131], v[88:91]\n\t"
    "v_cvt_pk_bf16_f32 v82, v84, v85\n\t"
    "v_cvt_pk_bf16_f32 v83, v86, v87\n\t"
    "v_mfma_f32_16x16x32_bf16 v[88:91], v[132:135], a[124:127], v[88:91]\n\t"
    "buffer_store_dwordx4 v[120:123], v7, s[52:55], s61 offen offset:0 sc1\n\t"
    "v_mfma_f32_16x16x32_bf16 v[88:91], v[112:115], a[120:123], v[88:91]\n\t"
    "v_mfma_f32_16x16x32_bf16 v[92:95], v[112:115], a[144:147], 0\n\t"
    "v_mfma_f32_16x16x32_bf16 v[92:95], v[132:135], a[148:151], v[92:95]\n\t"
    "v_mfma_f32_16x16x32_bf16 v[92:95], v[136:139], a[152:155], v[92:95]\n\t"
    "v_mfma_f32_16x16x32_bf16 v[92:95], v[140:143], a[156:159], v[92:95]\n\t"
    "v_cvt_pk_bf16_f32 v80, v88, v89\n\t"
    "v_cvt_pk_bf16_f32 v81, v90, v91\n\t"
    "v_mfma_f32_16x16x32_bf16 v[92:95], v[148:151], a[160:163], v[92:95]\n\t"
    "v_mfma_f32_16x16x32_bf16 v[92:95], v[152:155], a[164:167], v[92:95]\n\t"
    "v_mfma_f32_16x16x32_bf16 v[88:91], v[160:163], a[164:167], 0\n\t"
    "v_mfma_f32_16x16x32_bf16 v[88:91], v[164:167], a[160:163], v[88:91]\n\t"
    "v_mfma_f32_16x16x32_bf16 v[88:91], v[128:131], a[156:159], v[88:91]\n\t"
    "v_mfma_f32_16x16x32_bf16 v[88:91], v[144:147], a[152:155], v[88:91]\n\t"
    "v_cvt_pk_bf16_f32 v84, v92, v93\n\t"
    "v_cvt_pk_bf16_f32 v85, v94, v95\n\t"
    "v_mfma_f32_16x16x32_bf16 v[88:91], v[176:179], a[148:151], v[88:91]\n\t"
    "buffer_store_dwordx4 v[124:127], v7, s[52:55], s61 offen offset:384 sc1\n\t"
    "v_mfma_f32_16x16x32_bf16 v[88:91], v[108:111], a[144:147], v[88:91]\n\t"
    "v_mfma_f32_16x16x32_bf16 v[92:95], v[108:111], a[168:171], 0\n\t"
    "v_mfma_f32_16x16x32_bf16 v[92:95], v[176:179], a[172:175], v[92:95]\n\t"
    "v_mfma_f32_16x16x32_bf16 v[92:95], v[144:147], a[176:179], v[92:95]\n\t"
    "v_mfma_f32_16x16x32_bf16 v[92:95], v[128:131], a[180:183], v[92:95]\n\t"
    "v_cvt_pk_bf16_f32 v86, v88, v89\n\t"
    "v_cvt_pk_bf16_f32 v87, v90, v91\n\t"
    "v_mfma_f32_16x16x32_bf16 v[92:95], v[164:167], a[184:187], v[92:95]\n\t"
    "v_mfma_f32_16x16x32_bf16 v[92:95], v[160:163], a[188:191], v[92:95]\n\t"
    "v_mfma_f32_16x16x32_bf16 v[96:99], v[152:155], a[188:191], 0\n\t"
    "v_mfma_f32_16x16x32_bf16 v[96:99], v[148:151], a[184:187], v[96:99]\n\t"
    "v_mfma_f32_16x16x32_bf16 v[96:99], v[140:143], a[180:183], v[96:99]\n\t"
    "v_mfma_f32_16x16x32_bf16 v[96:99], v[136:139], a[176:179], v[96:99]\n\t"
    "v_cvt_pk_bf16_f32 v90, v92, v93\n\t"
    "v_cvt_pk_bf16_f32 v91, v94, v95\n\t"
    "v_mfma_f32_16x16x32_bf16 v[96:99], v[132:135], a[172:175], v[96:99]\n\t"
    "buffer_store_dwordx4 v[156:159], v7, s[52:55], s61 offen offset:768 sc1\n\t"
    "v_mfma_f32_16x16x32_bf16 v[96:99], v[112:115], a[168:171], v[96:99]\n\t"
    "v_mfma_f32_16x16x32_bf16 v[100:103], v[112:115], a[192:195], 0\n\t"
    "v_mfma_f32_16x16x32_bf16 v[100:103], v[132:135], a[196:199], v[100:103]\n\t"
    "v_mfma_f32_16x16x32_bf16 v[100:103], v[136:139], a[200:203], v[100:103]\n\t"
    "v_mfma_f32_16x16x32_bf16 v[100:103], v[140:143], a[204:207], v[100:103]\n\t"
    "v_cvt_pk_bf16_f32 v88, v96, v97\n\t"
    "v_cvt_pk_bf16_f32 v89, v98, v99\n\t"
    "v_mfma_f32_16x16x32_bf16 v[100:103], v[148:151], a[208:211], v[100:103]\n\t"
    "v_mfma_f32_16x16x32_bf16 v[100:103], v[152:155], a[212:215], v[100:103]\n\t"
    "v_mfma_f32_16x16x32_bf16 v[96:99], v[160:163], a[212:215], 0\n\t"
    "v_mfma_f32_16x16x32_bf16 v[96:99], v[164:167], a[208:211], v[96:99]\n\t"
    "v_mfma_f32_16x16x32_bf16 v[96:99], v[128:131], a[204:207], v[96:99]\n\t"
    "v_mfma_f32_16x16x32_bf16 v[96:99], v[144:147], a[200:203], v[96:99]\n\t"
    "v_cvt_pk_bf16_f32 v92, v100, v101\n\t"
    "v_cvt_pk_bf16_f32 v93, v102, v103\n\t"
    "v_mfma_f32_16x16x32_bf16 v[96:99], v[176:179], a[196:199], v[96:99]\n\t"
    "buffer_store_dwordx4 v[184:187], v7, s[52:55], s61 offen offset:1152 sc1\n\t"
    "v_mfma_f32_16x16x32_bf16 v[96:99], v[108:111], a[192:195], v[96:99]\n\t"
    "s_barrier\n\t"
    "v_mfma_f32_16x16x32_bf16 v[100:103], v[108:111], a[216:219], 0\n\t"
    "v_mfma_f32_16x16x32_bf16 v[100:103], v[176:179], a[220:223], v[100:103]\n\t"
    "v_mfma_f32_16x16x32_bf16 v[100:103], v[144:147], a[224:227], v[100:103]\n\t"
    "v_mfma_f32_16x16x32_bf16 v[100:103], v[128:131], a[228:231], v[100:103]\n\t"
    "v_cvt_pk_bf16_f32 v94, v96, v97\n\t"
    "v_cvt_pk_bf16_f32 v95, v98, v99\n\t"
    "v_mfma_f32_16x16x32_bf16 v[100:103], v[164:167], a[232:235], v[100:103]\n\t"
    "v_mfma_f32_16x16x32_bf16 v[100:103], v[160:163], a[236:239], v[100:103]\n\t"
    "v_mfma_f32_16x16x32_bf16 v[116:119], v[152:155], a[236:239], 0\n\t"
    "v_mfma_f32_16x16x32_bf16 v[116:119], v[148:151], a[232:235], v[116:119]\n\t"
    "v_mfma_f32_16x16x32_bf16 v[116:119], v[140:143], a[228:231], v[116:119]\n\t"
    "v_mfma_f32_16x16x32_bf16 v[116:119], v[136:139], a[224:227], v[116:119]\n\t"
    "v_cvt_pk_bf16_f32 v98, v100, v101\n\t"
    "v_cvt_pk_bf16_f32 v99, v102, v103\n\t"
    "v_mfma_f32_16x16x32_bf16 v[116:119], v[132:135], a[220:223], v[116:119]\n\t"
    "buffer_store_dwordx2 v[104:105], v8, s[52:55], s61 offen offset:0 sc1\n\t"
    "v_mfma_f32_16x16x32_bf16 v[116:119], v[112:115], a[216:219], v[116:119]\n\t"
    "v_mfma_f32_16x16x32_bf16 v[120:123], v[112:115], a[240:243], 0\n\t"
    "v_mfma_f32_16x16x32_bf16 v[120:123], v[132:135], a[244:247], v[120:123]\n\t"
    "v_mfma_f32_16x16x32_bf16 v[120:123], v[136:139], a[248:251], v[120:123]\n\t"
    "v_mfma_f32_16x16x32_bf16 v[120:123], v[140:143], a[252:255], v[120:123]\n\t"
    "v_cvt_pk_bf16_f32 v96, v116, v117\n\t"
    "v_cvt_pk_bf16_f32 v97, v118, v119\n\t"
    "v_mfma_f32_16x16x32_bf16 v[120:123], v[148:151], v[224:227], v[120:123]\n\t"
    "v_mfma_f32_16x16x32_bf16 v[120:123], v[152:155], v[228:231], v[120:123]\n\t"
    "v_mfma_f32_16x16x32_bf16 v[116:119], v[160:163], v[228:231], 0\n\t"
    "v_mfma_f32_16x16x32_bf16 v[116:119], v[164:167], v[224:227], v[116:119]\n\t"
    "v_mfma_f32_16x16x32_bf16 v[116:119], v[128:131], a[252:255], v[116:119]\n\t"
    "v_mfma_f32_16x16x32_bf16 v[116:119], v[144:147], a[248:251], v[116:119]\n\t"
    "v_cvt_pk_bf16_f32 v100, v120, v121\n\t"
    "v_cvt_pk_bf16_f32 v101, v122, v123\n\t"
    "v_mfma_f32_16x16x32_bf16 v[116:119], v[176:179], a[244:247], v[116:119]\n\t"
    "buffer_store_dwordx2 v[106:107], v8, s[52:55], s61 offen offset:384 sc1\n\t"
    "v_mfma_f32_16x16x32_bf16 v[116:119], v[108:111], a[240:243], v[116:119]\n\t"
    "v_mfma_f32_16x16x32_bf16 v[120:123], v[108:111], v[232:235], 0\n\t"
    "v_mfma_f32_16x16x32_bf16 v[120:123], v[176:179], v[236:239], v[120:123]\n\t"
    "ds_read_b128 v[156:159], v6 offset:0\n\t"
    "v_mfma_f32_16x16x32_bf16 v[120:123], v[144:147], v[240:243], v[120:123]\n\t"
    "ds_read_b128 v[168:171], v6 offset:1024\n\t"
    "v_mfma_f32_16x16x32_bf16 v[120:123], v[128:131], v[244:247], v[120:123]\n\t"
    "v_cvt_pk_bf16_f32 v102, v116, v117\n\t"
    "v_cvt_pk_bf16_f32 v103, v118, v119\n\t"
    "v_mfma_f32_16x16x32_bf16 v[120:123], v[164:167], v[248:251], v[120:123]\n\t"
    "v_mfma_f32_16x16x32_bf16 v[120:123], v[160:163], v[252:255], v[120:123]\n\t"
    "v_mfma_f32_16x16x32_bf16 v[108:111], v[152:155], v[252:255], 0\n\t"
    "v_mfma_f32_16x16x32_bf16 v[108:111], v[148:151], v[248:251], v[108:111]\n\t"
    "v_mfma_f32_16x16x32_bf16 v[108:111], v[140:143], v[244:247], v[108:111]\n\t"
    "v_mfma_f32_16x16x32_bf16 v[108:111], v[136:139], v[240:243], v[108:111]\n\t"
    "v_cvt_pk_bf16_f32 v106, v120, v121\n\t"
    "v_cvt_pk_bf16_f32 v107, v122, v123\n\t"
    "v_mfma_f32_16x16x32_bf16 v[108:111], v[132:135], v[236:239], v[108:111]\n\t"
    "buffer_store_dwordx2 v[180:181], v8, s[52:55], s61 offen offset:768 sc1\n\t"
    "v_mfma_f32_16x16x32_bf16 v[108:111], v[112:115], v[232:235], v[108:111]\n\t"
    "s_waitcnt lgkmcnt(0)\n\t"
    "v_mfma_f32_16x16x32_bf16 v[120:123], v[156:159], v[12:15], 0\n\t"
    "v_mfma_f32_16x16x32_bf16 v[120:123], v[168:171], v[60:63], v[120:123]\n\t"
    "v_mfma_f32_16x16x32_bf16 v[124:127], v[168:171], v[64:67], 0\n\t"
    "v_mfma_f32_16x16x32_bf16 v[124:127], v[156:159], v[16:19], v[124:127]\n\t"
    "v_cvt_pk_bf16_f32 v104, v108, v109\n\t"
    "v_cvt_pk_bf16_f32 v105, v110, v111\n\t"
    "v_mfma_f32_16x16x32_bf16 v[128:131], v[156:159], v[20:23], 0\n\t"
    "v_mfma_f32_16x16x32_bf16 v[128:131], v[168:171], v[68:71], v[128:131]\n\t"
    "v_mfma_f32_16x16x32_bf16 v[132:135], v[168:171], v[72:75], 0\n\t"
    "v_mfma_f32_16x16x32_bf16 v[132:135], v[156:159], v[24:27], v[132:135]\n\t"
    "v_cvt_pk_bf16_f32 v160, v120, v124\n\t"
    "v_cvt_pk_bf16_f32 v108, v121, v125\n\t"
    "v_mfma_f32_16x16x32_bf16 v[136:139], v[156:159], v[28:31], 0\n\t"
    "v_cvt_pk_bf16_f32 v116, v122, v126\n\t"
    "v_cvt_pk_bf16_f32 v112, v123, v127\n\t"
    "v_mfma_f32_16x16x32_bf16 v[136:139], v[168:171], v[76:79], v[136:139]\n\t"
    "v_mfma_f32_16x16x32_bf16 v[140:143], v[168:171], v[80:83], 0\n\t"
    "buffer_store_dwordx2 v[182:183], v8, s[52:55], s61 offen offset:1152 sc1\n\t"
    "v_mfma_f32_16x16x32_bf16 v[140:143], v[156:159], v[32:35], v[140:143]\n\t"
    "s_add_u32 s61, s61, 11403264\n\t"
    "v_cvt_pk_bf16_f32 v161, v128, v132\n\t"
    "v_mfma_f32_16x16x32_bf16 v[144:147], v[156:159], v[36:39], 0\n\t"
    "v_cvt_pk_bf16_f32 v109, v129, v133\n\t"
    "v_cvt_pk_bf16_f32 v117, v130, v134\n\t"
    "v_mfma_f32_16x16x32_bf16 v[144:147], v[168:171], v[84:87], v[144:147]\n\t"
    "ds_read_b128 v[120:123], v6 offset:2048\n\t"
    "v_cvt_pk_bf16_f32 v113, v131, v135\n\t"
    "v_mfma_f32_16x16x32_bf16 v[148:151], v[168:171], v[88:91], 0\n\t"
    "ds_read_b128 v[124:127], v6 offset:3072\n\t"
    "v_mfma_f32_16x16x32_bf16 v[148:151], v[156:159], v[40:43], v[148:151]\n\t"
    "v_cvt_pk_bf16_f32 v162, v136, v140\n\t"
    "v_cvt_pk_bf16_f32 v110, v137, v141\n\t"
    "v_mfma_f32_16x16x32_bf16 v[128:131], v[156:159], v[44:47], 0\n\t"
    "v_cvt_pk_bf16_f32 v118, v138, v142\n\t"
    "v_cvt_pk_bf16_f32 v114, v139, v143\n\t"
    "v_mfma_f32_16x16x32_bf16 v[128:131], v[168:171], v[92:95], v[128:131]\n\t"
    "v_mfma_f32_16x16x32_bf16 v[132:135], v[168:171], v[96:99], 0\n\t"
    "v_mfma_f32_16x16x32_bf16 v[132:135], v[156:159], v[48:51], v[132:135]\n\t"
    "v_cvt_pk_bf16_f32 v163, v144, v148\n\t"
    "v_cvt_pk_bf16_f32 v111, v145, v149\n\t"
    "v_mfma_f32_16x16x32_bf16 v[136:139], v[156:159], v[52:55], 0\n\t"
    "v_cvt_pk_bf16_f32 v119, v146, v150\n\t"
    "v_cvt_pk_bf16_f32 v115, v147, v151\n\t"
    "v_mfma_f32_16x16x32_bf16 v[136:139], v[168:171], v[100:103], v[136:139]\n\t"
    "v_mfma_f32_16x16x32_bf16 v[140:143], v[168:171], v[104:107], 0\n\t"
    "v_mfma_f32_16x16x32_bf16 v[140:143], v[156:159], v[56:59], v[140:143]\n\t"
    ".Lh2_%=:\n\t"
    "s_cbranch_execz .Lh6_%=\n\t"
    "v_cvt_pk_bf16_f32 v144, v128, v132\n\t"
    "s_waitcnt lgkmcnt(0)\n\t"
    "v_mfma_f32_16x16x32_bf16 v[148:151], v[120:123], v[12:15], 0\n\t"
    "buffer_store_dwordx4 v[160:163], v7, s[52:55], s61 offen offset:0 sc1\n\t"
    "v_mfma_f32_16x16x32_bf16 v[148:151], v[124:127], v[60:63], v[148:151]\n\t"
    "v_cvt_pk_bf16_f32 v146, v129, v133\n\t"
    "v_cvt_pk_bf16_f32 v152, v130, v134\n\t"
    "v_mfma_f32_16x16x32_bf16 v[156:159], v[124:127], v[64:67], 0\n\t"
    "v_cvt_pk_bf16_f32 v154, v131, v135\n\t"
    "v_mfma_f32_16x16x32_bf16 v[156:159], v[120:123], v[16:19], v[156:159]\n\t"
    "v_cvt_pk_bf16_f32 v145, v136, v140\n\t"
    "v_cvt_pk_bf16_f32 v147, v137, v141\n\t"
    "v_mfma_f32_16x16x32_bf16 v[160:163], v[120:123], v[20:23], 0\n\t"
    "v_cvt_pk_bf16_f32 v153, v138, v142\n\t"
    "v_cvt_pk_bf16_f32 v155, v139, v143\n\t"
    "v_mfma_f32_16x16x32_bf16 v[160:163], v[124:127], v[68:71], v[160:163]\n\t"
    "v_mfma_f32_16x16x32_bf16 v[128:131], v[124:127], v[72:75], 0\n\t"
    "v_mfma_f32_16x16x32_bf16 v[128:131], v[120:123], v[24:27], v[128:131]\n\t"
    "v_cvt_pk_bf16_f32 v136, v148, v156\n\t"
    "v_cvt_pk_bf16_f32 v132, v149, v157\n\t"
    "v_mfma_f32_16x16x32_bf16 v[168:171], v[120:123], v[28:31], 0\n\t"
    "v_cvt_pk_bf16_f32 v164, v150, v158\n\t"
    "v_cvt_pk_bf16_f32 v140, v151, v159\n\t"
    "v_mfma_f32_16x16x32_bf16 v[168:171], v[124:127], v[76:79], v[168:171]\n\t"
    "v_mfma_f32_16x16x32_bf16 v[172:175], v[124:127], v[80:83], 0\n\t"
    "buffer_store_dwordx4 v[108:111], v7, s[52:55], s61 offen offset:384 sc1\n\t"
    "v_mfma_f32_16x16x32_bf16 v[172:175], v[120:123], v[32:35], v[172:175]\n\t"
    "v_cvt_pk_bf16_f32 v137, v160, v128\n\t"
    "v_cvt_pk_bf16_f32 v133, v161, v129\n\t"
    "v_mfma_f32_16x16x32_bf16 v[148:151], v[120:123], v[36:39], 0\n\t"
    "v_cvt_pk_bf16_f32 v165, v162, v130\n\t"
    "v_cvt_pk_bf16_f32 v141, v163, v131\n\t"
    "v_mfma_f32_16x16x32_bf16 v[148:151], v[124:127], v[84:87], v[148:151]\n\t"
    "ds_read_b128 v[108:111], v6 offset:4096\n\t"
    "v_mfma_f32_16x16x32_bf16 v[176:179], v[124:127], v[88:91], 0\n\t"
    "ds_read_b128 v[156:159], v6 offset:5120\n\t"
    "v_mfma_f32_16x16x32_bf16 v[176:179], v[120:123], v[40:43], v[176:179]\n\t"
    "v_cvt_pk_bf16_f32 v138, v168, v172\n\t"
    "v_cvt_pk_bf16_f32 v134, v169, v173\n\t"
    "v_mfma_f32_16x16x32_bf16 v[128:131], v[120:123], v[44:47], 0\n\t"
    "v_cvt_pk_bf16_f32 v166, v170, v174\n\t"
    "v_cvt_pk_bf16_f32 v142, v171, v175\n\t"
    "v_mfma_f32_16x16x32_bf16 v[128:131], v[124:127], v[92:95], v[128:131]\n\t"
    "v_mfma_f32_16x16x32_bf16 v[160:163], v[124:127], v[96:99], 0\n\t"
    "v_mfma_f32_16x16x32_bf16 v[160:163], v[120:123], v[48:51], v[160:163]\n\t"
    "v_cvt_pk_bf16_f32 v139, v148, v176\n\t"
    "v_cvt_pk_bf16_f32 v135, v149, v177\n\t"
    "v_mfma_f32_16x16x32_bf16 v[168:171], v[120:123], v[52:55], 0\n\t"
    "v_cvt_pk_bf16_f32 v167, v150, v178\n\t"
    "v_cvt_pk_bf16_f32 v143, v151, v179\n\t"
    "v_mfma_f32_16x16x32_bf16 v[168:171], v[124:127], v[100:103], v[168:171]\n\t"
    "v_mfma_f32_16x16x32_bf16 v[172:175], v[124:127], v[104:107], 0\n\t"
    "buffer_store_dwordx4 v[116:119], v7, s[52:55], s61 offen offset:768 sc1\n\t"
    "v_mfma_f32_16x16x32_bf16 v[172:175], v[120:123], v[56:59], v[172:175]\n\t"
    "v_cvt_pk_bf16_f32 v148, v128, v160\n\t"
    "s_waitcnt lgkmcnt(0)\n\t"
    "v_mfma_f32_16x16x32_bf16 v[180:183], v[108:111], v[12:15], 0\n\t"
    "v_cvt_pk_bf16_f32 v150, v129, v161\n\t"
    "v_cvt_pk_bf16_f32 v176, v130, v162\n\t"
    "v_mfma_f32_16x16x32_bf16 v[180:183], v[156:159], v[60:63], v[180:183]\n\t"
    "v_cvt_pk_bf16_f32 v178, v131, v163\n\t"
    "v_mfma_f32_16x16x32_bf16 v[116:119], v[156:159], v[64:67], 0\n\t"
    "v_mfma_f32_16x16x32_bf16 v[116:119], v[108:111], v[16:19], v[116:119]\n\t"
    "v_cvt_pk_bf16_f32 v149, v168, v172\n\t"
    "v_cvt_pk_bf16_f32 v151, v169, v173\n\t"
    "v_mfma_f32_16x16x32_bf16 v[120:123], v[108:111], v[20:23], 0\n\t"
    "v_cvt_pk_bf16_f32 v177, v170, v174\n\t"
    "v_cvt_pk_bf16_f32 v179, v171, v175\n\t"
    "v_mfma_f32_16x16x32_bf16 v[120:123], v[156:159], v[68:71], v[120:123]\n\t"
    "v_mfma_f32_16x16x32_bf16 v[124:127], v[156:159], v[72:75], 0\n\t"
    "v_mfma_f32_16x16x32_bf16 v[124:127], v[108:111], v[24:27], v[124:127]\n\t"
    "v_cvt_pk_bf16_f32 v160, v180, v116\n\t"
    "v_cvt_pk_bf16_f32 v128, v181, v117\n\t"
    "v_mfma_f32_16x16x32_bf16 v[184:187], v[108:111], v[28:31], 0\n\t"
    "v_cvt_pk_bf16_f32 v172, v182, v118\n\t"
    "v_cvt_pk_bf16_f32 v168, v183, v119\n\t"
    "v_mfma_f32_16x16x32_bf16 v[184:187], v[156:159], v[76:79], v[184:187]\n\t"
    "v_mfma_f32_16x16x32_bf16 v[188:191], v[156:159], v[80:83], 0\n\t"
    "buffer_store_dwordx4 v[112:115], v7, s[52:55], s61 offen offset:1152 sc1\n\t"
    "v_mfma_f32_16x16x32_bf16 v[188:191], v[108:111], v[32:35], v[188:191]\n\t"
    "v_cvt_pk_bf16_f32 v161, v120, v124\n\t"
    "v_cvt_pk_bf16_f32 v129, v121, v125\n\t"
    "v_mfma_f32_16x16x32_bf16 v[116:119], v[108:111], v[36:39], 0\n\t"
    "s_barrier\n\t"
    "v_cvt_pk_bf16_f32 v173, v122, v126\n\t"
    "v_mfma_f32_16x16x32_bf16 v[116:119], v[156:159], v[84:87], v[116:119]\n\t"
    "ds_read_b128 v[112:115], v6 offset:6144\n\t"
    "v_cvt_pk_bf16_f32 v169, v123, v127\n\t"
    "v_mfma_f32_16x16x32_bf16 v[192:195], v[156:159], v[88:91], 0\n\t"
    "ds_read_b128 v[180:183], v6 offset:7168\n\t"
    "v_mfma_f32_16x16x32_bf16 v[192:195], v[108:111], v[40:43], v[192:195]\n\t"
    "v_cvt_pk_bf16_f32 v162, v184, v188\n\t"
    "v_cvt_pk_bf16_f32 v130, v185, v189\n\t"
    "v_mfma_f32_16x16x32_bf16 v[120:123], v[108:111], v[44:47], 0\n\t"
    "v_cvt_pk_bf16_f32 v174, v186, v190\n\t"
    "v_cvt_pk_bf16_f32 v170, v187, v191\n\t"
    "v_mfma_f32_16x16x32_bf16 v[120:123], v[156:159], v[92:95], v[120:123]\n\t"
    "v_mfma_f32_16x16x32_bf16 v[124:127], v[156:159], v[96:99], 0\n\t"
    "v_mfma_f32_16x16x32_bf16 v[124:127], v[108:111], v[48:51], v[124:127]\n\t"
    "v_cvt_pk_bf16_f32 v163, v116, v192\n\t"
    "v_cvt_pk_bf16_f32 v131, v117, v193\n\t"
    "v_mfma_f32_16x16x32_bf16 v[184:187], v[108:111], v[52:55], 0\n\t"
    "v_cvt_pk_bf16_f32 v175, v118, v194\n\t"
    "v_cvt_pk_bf16_f32 v171, v119, v195\n\t"
    "v_mfma_f32_16x16x32_bf16 v[184:187], v[156:159], v[100:103], v[184:187]\n\t"
    "v_mfma_f32_16x16x32_bf16 v[188:191], v[156:159], v[104:107], 0\n\t"
    "buffer_store_dwordx2 v[144:145], v8, s[52:55], s61 offen offset:0 sc1\n\t"
    "v_mfma_f32_16x16x32_bf16 v[188:191], v[108:111], v[56:59], v[188:191]\n\t"
    "v_cvt_pk_bf16_f32 v116, v120, v124\n\t"
    "s_waitcnt lgkmcnt(0)\n\t"
    "v_mfma_f32_16x16x32_bf16 v[196:199], v[112:115], v[12:15], 0\n\t"
    "v_cvt_pk_bf16_f32 v118, v121, v125\n\t"
    "v_cvt_pk_bf16_f32 v192, v122, v126\n\t"
    "v_mfma_f32_16x16x32_bf16 v[196:199], v[180:183], v[60:63], v[196:199]\n\t"
    "v_cvt_pk_bf16_f32 v194, v123, v127\n\t"
    "v_mfma_f32_16x16x32_bf16 v[108:111], v[180:183], v[64:67], 0\n\t"
    "v_mfma_f32_16x16x32_bf16 v[108:111], v[112:115], v[16:19], v[108:111]\n\t"
    "v_cvt_pk_bf16_f32 v117, v184, v188\n\t"
    "v_cvt_pk_bf16_f32 v119, v185, v189\n\t"
    "v_mfma_f32_16x16x32_bf16 v[12:15], v[112:115], v[20:23], 0\n\t"
    "v_cvt_pk_bf16_f32 v193, v186, v190\n\t"
    "v_cvt_pk_bf16_f32 v195, v187, v191\n\t"
    "v_mfma_f32_16x16x32_bf16 v[12:15], v[180:183], v[68:71], v[12:15]\n\t"
    "v_mfma_f32_16x16x32_bf16 v[16:19], v[180:183], v[72:75], 0\n\t"
    "v_mfma_f32_16x16x32_bf16 v[16:19], v[112:115], v[24:27], v[16:19]\n\t"
    "ds_read_b128 v[60:63], v0 offset:0\n\t"
    "v_cvt_pk_bf16_f32 v120, v196, v108\n\t"
    "v_mfma_f32_16x16x32_bf16 v[20:23], v[112:115], v[28:31], 0\n\t"
    "ds_read_b128 v[64:67], v1 offset:0\n\t"
    "v_cvt_pk_bf16_f32 v124, v197, v109\n\t"
    "v_mfma_f32_16x16x32_bf16 v[20:23], v[180:183], v[76:79], v[20:23]\n\t"
    "ds_read_b128 v[68:71], v2 offset:0\n\t"
    "v_cvt_pk_bf16_f32 v156, v198, v110\n\t"
    "v_mfma_f32_16x16x32_bf16 v[24:27], v[180:183], v[80:83], 0\n\t"
    "ds_read_b128 v[72:75], v3 offset:0\n\t"
    "v_cvt_pk_bf16_f32 v184, v199, v111\n\t"
    "v_mfma_f32_16x16x32_bf16 v[24:27], v[112:115], v[32:35], v[24:27]\n\t"
    "ds_read_b128 v[188:191], v4 offset:0\n\t"
    "v_cvt_pk_bf16_f32 v121, v12, v16\n\t"
    "v_mfma_f32_16x16x32_bf16 v[28:31], v[112:115], v[36:39], 0\n\t"
    "ds_read_b128 v[76:79], v5 offset:0\n\t"
    "v_cvt_pk_bf16_f32 v125, v13, v17\n\t"
    "v_mfma_f32_16x16x32_bf16 v[28:31], v[180:183], v[84:87], v[28:31]\n\t"
    "buffer_store_dwordx2 v[146:147], v8, s[52:55], s61 offen offset:384 sc1\n\t"
    "v_mfma_f32_16x16x32_bf16 v[32:35], v[180:183], v[88:91], 0\n\t"
    "ds_read_b128 v[80:83], v5 offset:6144\n\t"
    "v_cvt_pk_bf16_f32 v157, v14, v18\n\t"
    "v_mfma_f32_16x16x32_bf16 v[32:35], v[112:115], v[40:43], v[32:35]\n\t"
    "ds_read_b128 v[108:111], v4 offset:6144\n\t"
    "v_cvt_pk_bf16_f32 v185, v15, v19\n\t"
    "v_mfma_f32_16x16x32_bf16 v[36:39], v[112:115], v[44:47], 0\n\t"
    "ds_read_b128 v[84:87], v3 offset:6144\n\t"
    "v_cvt_pk_bf16_f32 v122, v20, v24\n\t"
    "v_mfma_f32_16x16x32_bf16 v[36:39], v[180:183], v[92:95], v[36:39]\n\t"
    "ds_read_b128 v[88:91], v2 offset:6144\n\t"
    "v_cvt_pk_bf16_f32 v126, v21, v25\n\t"
    "v_mfma_f32_16x16x32_bf16 v[12:15], v[180:183], v[96:99], 0\n\t"
    "ds_read_b128 v[144:147], v1 offset:6144\n\t"
    "v_cvt_pk_bf16_f32 v158, v22, v26\n\t"
    "v_mfma_f32_16x16x32_bf16 v[12:15], v[112:115], v[48:51], v[12:15]\n\t"
    "ds_read_b128 v[196:199], v0 offset:6144\n\t"
    "v_cvt_pk_bf16_f32 v186, v23, v27\n\t"
    "v_mfma_f32_16x16x32_bf16 v[16:19], v[112:115], v[52:55], 0\n\t"
    "s_sub_u32 s60, s60, 1\n\t"
    "s_cmp_lg_u32 s60, 0\n\t"
    "v_mfma_f32_16x16x32_bf16 v[16:19], v[180:183], v[100:103], v[16:19]\n\t"
    "v_cvt_pk_bf16_f32 v123, v28, v32\n\t"
    "v_cvt_pk_bf16_f32 v127, v29, v33\n\t"
    "v_mfma_f32_16x16x32_bf16 v[20:23], v[180:183], v[104:107], 0\n\t"
    "buffer_store_dwordx2 v[152:153], v8, s[52:55], s61 offen offset:768 sc1\n\t"
    "v_mfma_f32_16x16x32_bf16 v[20:23], v[112:115], v[56:59], v[20:23]\n\t"
    "s_cbranch_scc1 .Lmin_%=\n\t"
    "s_nop 7\n\t"
    "s_nop 1\n\t"
    "v_cvt_pk_bf16_f32 v159, v30, v34\n\t"
    "v_cvt_pk_bf16_f32 v187, v31, v35\n\t"
    "v_cvt_pk_bf16_f32 v104, v36, v12\n\t"
    "v_cvt_pk_bf16_f32 v106, v37, v13\n\t"
    "v_cvt_pk_bf16_f32 v180, v38, v14\n\t"
    "v_cvt_pk_bf16_f32 v182, v39, v15\n\t"
    "v_cvt_pk_bf16_f32 v105, v16, v20\n\t"
    "v_cvt_pk_bf16_f32 v107, v17, v21\n\t"
    "v_cvt_pk_bf16_f32 v181, v18, v22\n\t"
    "v_cvt_pk_bf16_f32 v183, v19, v23\n\t"
    "buffer_store_dwordx2 v[154:155], v8, s[52:55], s61 offen offset:1152 sc1\n\t"
    "s_add_u32 s61, s61, 393216\n\t"
    "buffer_store_dwordx4 v[136:139], v7, s[52:55], s61 offen offset:0 sc1\n\t"
    "buffer_store_dwordx4 v[132:135], v7, s[52:55], s61 offen offset:384 sc1\n\t"
    "buffer_store_dwordx4 v[164:167], v7, s[52:55], s61 offen offset:768 sc1\n\t"
    "buffer_store_dwordx4 v[140:143], v7, s[52:55], s61 offen offset:1152 sc1\n\t"
    "buffer_store_dwordx2 v[148:149], v8, s[52:55], s61 offen offset:0 sc1\n\t"
    "buffer_store_dwordx2 v[150:151], v8, s[52:55], s61 offen offset:384 sc1\n\t"
    "buffer_store_dwordx2 v[176:177], v8, s[52:55], s61 offen offset:768 sc1\n\t"
    "buffer_store_dwordx2 v[178:179], v8, s[52:55], s61 offen offset:1152 sc1\n\t"
    "s_add_u32 s61, s61, 393216\n\t"
    "buffer_store_dwordx4 v[160:163], v7, s[52:55], s61 offen offset:0 sc1\n\t"
    "buffer_store_dwordx4 v[128:131], v7, s[52:55], s61 offen offset:384 sc1\n\t"
    "buffer_store_dwordx4 v[172:175], v7, s[52:55], s61 offen offset:768 sc1\n\t"
    "buffer_store_dwordx4 v[168:171], v7, s[52:55], s61 offen offset:1152 sc1\n\t"
    "buffer_store_dwordx2 v[116:117], v8, s[52:55], s61 offen offset:0 sc1\n\t"
    "buffer_store_dwordx2 v[118:119], v8, s[52:55], s61 offen offset:384 sc1\n\t"
    "buffer_store_dwordx2 v[192:193], v8, s[52:55], s61 offen offset:768 sc1\n\t"
    "buffer_store_dwordx2 v[194:195], v8, s[52:55], s61 offen offset:1152 sc1\n\t"
    "s_add_u32 s61, s61, 393216\n\t"
    "buffer_store_dwordx4 v[120:123], v7, s[52:55], s61 offen offset:0 sc1\n\t"
    "buffer_store_dwordx4 v[124:127], v7, s[52:55], s61 offen offset:384 sc1\n\t"
    "buffer_store_dwordx4 v[156:159], v7, s[52:55], s61 offen offset:768 sc1\n\t"
    "buffer_store_dwordx4 v[184:187], v7, s[52:55], s61 offen offset:1152 sc1\n\t"
    "buffer_store_dwordx2 v[104:105], v8, s[52:55], s61 offen offset:0 sc1\n\t"
    "buffer_store_dwordx2 v[106:107], v8, s[52:55], s61 offen offset:384 sc1\n\t"
    "buffer_store_dwordx2 v[180:181], v8, s[52:55], s61 offen offset:768 sc1\n\t"
    "buffer_store_dwordx2 v[182:183], v8, s[52:55], s61 offen offset:1152 sc1\n\t"
    "s_add_u32 s61, s61, 11403264\n\t"
    "s_endpgm\n\t"
    ".p2align 12\n\t"
    ".Lroln_%=:\n\t"
    "v_lshrrev_b32_e32 v20, 3, v31\n\t"
    "v_and_b32_e32 v21, 6, v20\n\t"
    "v_and_b32_e32 v28, 7, v31\n\t"
    "v_xor_b32_e32 v21, v21, v28\n\t"
    "v_lshlrev_b32_e32 v20, 7, v20\n\t"
    "v_lshl_or_b32 v7, v21, 4, v20\n\t"
    "s_mov_b32 s54, 0x6000000\n\t"
    "s_mov_b32 s55, 0x20000\n\t"
    "s_waitcnt lgkmcnt(0)\n\t"
    "s_mov_b32 s52, s36\n\t"
    "s_and_b32 s53, s37, 0xffff\n\t"
    "s_barrier\n\t"
    "s_add_u32 m0, %[rbeg], 0\n\t"
    "s_add_u32 s61, %[wb], 0\n\t"
    "buffer_load_dwordx4 v7, s[52:55], s61 offen offset:0 nt lds\n\t"
    "buffer_load_dwordx4 v7, s[52:55], s61 offen offset:1024 nt lds\n\t"
    "buffer_load_dwordx4 v7, s[52:55], s61 offen offset:2048 nt lds\n\t"
    "s_add_u32 m0, %[rbeg], 3072\n\t"
    "s_add_u32 s61, %[wb], 196608\n\t"
    "buffer_load_dwordx4 v7, s[52:55], s61 offen offset:0 nt lds\n\t"
    "buffer_load_dwordx4 v7, s[52:55], s61 offen offset:1024 nt lds\n\t"
    "buffer_load_dwordx4 v7, s[52:55], s61 offen offset:2048 nt lds\n\t"
    "v_lshlrev_b32_e32 v30, 4, v31\n\t"
    "v_and_b32_e32 v28, 7, v31\n\t"
    "v_bfe_u32 v27, v31, 3, 1\n\t"
    "v_lshrrev_b32_e32 v29, 4, v31\n\t"
    "v_add_u32_e32 v6, %[pl], v30\n\t"
    "v_mul_u32_u24_e32 v27, 0xc00, v27\n\t"
    "v_add_u32_e32 v27, %[rbeg], v27\n\t"
    "v_lshlrev_b32_e32 v26, 7, v31\n\t"
    "v_mad_u32_u24 v20, v28, 3, 0\n\t"
    "v_and_b32_e32 v21, 6, v20\n\t"
    "v_xor_b32_e32 v21, v21, v29\n\t"
    "v_lshl_add_u32 v20, v20, 7, v27\n\t"
    "v_lshl_add_u32 v0, v21, 4, v20\n\t"
    "v_xor_b32_e32 v1, 64, v0\n\t"
    "v_mad_u32_u24 v20, v28, 3, 1\n\t"
    "v_and_b32_e32 v21, 6, v20\n\t"
    "v_xor_b32_e32 v21, v21, v29\n\t"
    "v_lshl_add_u32 v20, v20, 7, v27\n\t"
    "v_lshl_add_u32 v2, v21, 4, v20\n\t"
    "v_xor_b32_e32 v3, 64, v2\n\t"
    "v_mad_u32_u24 v20, v28, 3, 2\n\t"
    "v_and_b32_e32 v21, 6, v20\n\t"
    "v_xor_b32_e32 v21, v21, v29\n\t"
    "v_lshl_add_u32 v20, v20, 7, v27\n\t"
    "v_lshl_add_u32 v4, v21, 4, v20\n\t"
    "v_xor_b32_e32 v5, 64, v4\n\t"
    "s_mov_b32 s56, s42\n\t"
    "s_and_b32 s57, s43, 0xffff\n\t"
    "s_mov_b32 s58, 0x40000\n\t"
    "s_mov_b32 s59, 0x20000\n\t"
    "v_add_u32_e32 v8, 0x9000, v30\n\t"
    "v_add_u32_e32 v9, 0x1b000, v30\n\t"
    "s_mov_b32 s68, 0x3dd53b95\n\t"
    "s_add_u32 m0, %[rbeg], 6144\n\t"
    "s_add_u32 s61, %[wb], 393216\n\t"
    "buffer_load_dwordx4 v7, s[52:55], s61 offen offset:0 nt lds\n\t"
    "buffer_load_dwordx4 v7, s[52:55], s61 offen offset:1024 nt lds\n\t"
    "buffer_load_dwordx4 v7, s[52:55], s61 offen offset:2048 nt lds\n\t"
    "s_mov_b64 exec, 0\n\t"
    "s_cmp_eq_u32 %[pair], 0\n\t"
    "s_cbranch_scc1 .Lh4_%=\n\t"
    "s_branch .Lh1_%=\n\t"
    ".LrM_%=:\n\t"
    "s_mov_b64 exec, -1\n\t"
    "s_sleep 6\n\t"
    "s_add_u32 m0, %[rbeg], 9216\n\t"
    "s_add_u32 s61, %[wb], 589824\n\t"
    "buffer_load_dwordx4 v7, s[52:55], s61 offen offset:0 nt lds\n\t"
    "buffer_load_dwordx4 v7, s[52:55], s61 offen offset:1024 nt lds\n\t"
    "buffer_load_dwordx4 v7, s[52:55], s61 offen offset:2048 nt lds\n\t"
    "s_add_u32 m0, %[rbeg], 12288\n\t"
    "s_add_u32 s61, %[wb], 786432\n\t"
    "buffer_load_dwordx4 v7, s[52:55], s61 offen offset:0 nt lds\n\t"
    "buffer_load_dwordx4 v7, s[52:55], s61 offen offset:1024 nt lds\n\t"
    "buffer_load_dwordx4 v7, s[52:55], s61 offen offset:2048 nt lds\n\t"
    "s_add_u32 m0, %[rbeg], 15360\n\t"
    "s_add_u32 s61, %[wb], 983040\n\t"
    "buffer_load_dwordx4 v7, s[52:55], s61 offen offset:0 nt lds\n\t"
    "buffer_load_dwordx4 v7, s[52:55], s61 offen offset:1024 nt lds\n\t"
    "buffer_load_dwordx4 v7, s[52:55], s61 offen offset:2048 nt lds\n\t"
    "s_add_u32 m0, %[rbeg], 18432\n\t"
    "s_add_u32 s61, %[wb], 1179648\n\t"
    "buffer_load_dwordx4 v7, s[52:55], s61 offen offset:0 nt lds\n\t"
    "buffer_load_dwordx4 v7, s[52:55], s61 offen offset:1024 nt lds\n\t"
    "buffer_load_dwordx4 v7, s[52:55], s61 offen offset:2048 nt lds\n\t"
    "s_add_u32 m0, %[rbeg], 21504\n\t"
    "s_add_u32 s61, %[wb], 1376256\n\t"
    "buffer_load_dwordx4 v7, s[52:55], s61 offen offset:0 nt lds\n\t"
    "buffer_load_dwordx4 v7, s[52:55], s61 offen offset:1024 nt lds\n\t"
    "buffer_load_dwordx4 v7, s[52:55], s61 offen offset:2048 nt lds\n\t"
    "s_waitcnt vmcnt(0)\n\t"
    "s_barrier\n\t"
    "ds_read_b128 v[188:191], v2 offset:6144\n\t"
    "ds_read_b128 v[192:195], v1 offset:6144\n\t"
    "ds_read_b128 v[196:199], v0 offset:6144\n\t"
    "ds_read_b128 v[172:175], v0 offset:12288\n\t"
    "ds_read_b128 v[168:171], v1 offset:12288\n\t"
    "ds_read_b128 v[164:167], v2 offset:12288\n\t"
    "ds_read_b128 v[140:143], v2 offset:18432\n\t"
    "ds_read_b128 v[144:147], v1 offset:18432\n\t"
    "ds_read_b128 v[148:151], v0 offset:18432\n\t"
    "ds_read_b128 v[184:187], v3 offset:6144\n\t"
    "ds_read_b128 v[180:183], v4 offset:6144\n\t"
    "ds_read_b128 v[176:179], v5 offset:6144\n\t"
    "ds_read_b128 v[208:211], v3 offset:0\n\t"
    "ds_read_b128 v[204:207], v4 offset:0\n\t"
    "ds_read_b128 v[200:203], v5 offset:0\n\t"
    "ds_read_b128 v[160:163], v3 offset:12288\n\t"
    "ds_read_b128 v[156:159], v4 offset:12288\n\t"
    "ds_read_b128 v[152:155], v5 offset:12288\n\t"
    "ds_read_b128 v[128:131], v5 offset:18432\n\t"
    "ds_read_b128 v[132:135], v4 offset:18432\n\t"
    "ds_read_b128 v[136:139], v3 offset:18432\n\t"
    "ds_read_b128 v[220:223], v0 offset:0\n\t"
    "ds_read_b128 v[216:219], v1 offset:0\n\t"
    "ds_read_b128 v[212:215], v2 offset:0\n\t"
    "ds_read_b128 v[28:31], v3 offset:0\n\t"
    "ds_read_b128 v[32:35], v4 offset:0\n\t"
    "ds_read_b128 v[40:43], v5 offset:0\n\t"
    "s_add_i32 s63, %[rbeg], 24576\n\t"
    "s_add_i32 s62, %[rbeg], 49152\n\t"
    "s_barrier\n\t"
    "ds_read_b128 a[0:3], v8 offset:0\n\t"
    "ds_read_b128 a[8:11], v8 offset:1024\n\t"
    "ds_read_b128 a[16:19], v8 offset:2048\n\t"
    "ds_read_b128 a[24:27], v8 offset:3072\n\t"
    "ds_read_b128 a[32:35], v8 offset:4096\n\t"
    "ds_read_b128 a[40:43], v8 offset:5120\n\t"
    "ds_read_b128 a[44:47], v9 offset:5120\n\t"
    "ds_read_b128 a[36:39], v9 offset:4096\n\t"
    "ds_read_b128 a[28:31], v9 offset:3072\n\t"
    "ds_read_b128 a[20:23], v9 offset:2048\n\t"
    "ds_read_b128 a[12:15], v9 offset:1024\n\t"
    "s_waitcnt lgkmcnt(10)\n\t"
    "v_mfma_f32_16x16x32_bf16 v[64:67], a[0:3], v[220:223], 0\n\t"
    "ds_read_b128 a[4:7], v9 offset:0\n\t"
    "s_waitcnt lgkmcnt(10)\n\t"
    "v_mfma_f32_16x16x32_bf16 v[64:67], a[8:11], v[216:219], v[64:67]\n\t"
    "s_barrier\n\t"
    "ds_read_b128 a[48:51], v8 offset:6144\n\t"
    "s_waitcnt lgkmcnt(10)\n\t"
    "v_mfma_f32_16x16x32_bf16 v[64:67], a[16:19], v[212:215], v[64:67]\n\t"
    "ds_read_b128 a[56:59], v8 offset:7168\n\t"
    "s_waitcnt lgkmcnt(10)\n\t"
    "v_mfma_f32_16x16x32_bf16 v[64:67], a[24:27], v[28:31], v[64:67]\n\t"
    "ds_read_b128 a[64:67], v8 offset:8192\n\t"
    "s_waitcnt lgkmcnt(4)\n\t"
    "v_mfma_f32_16x16x32_bf16 v[64:67], a[32:35], v[32:35], v[64:67]\n\t"
    "ds_read_b128 a[72:75], v8 offset:9216\n\t"
    "v_mfma_f32_16x16x32_bf16 v[64:67], a[40:43], v[40:43], v[64:67]\n\t"
    "ds_read_b128 a[80:83], v8 offset:10240\n\t"
    "v_mfma_f32_16x16x32_bf16 v[56:59], a[44:47], v[40:43], 0\n\t"
    "ds_read_b128 a[88:91], v8 offset:11264\n\t"
    "v_mfma_f32_16x16x32_bf16 v[56:59], a[36:39], v[32:35], v[56:59]\n\t"
    "ds_read_b128 a[92:95], v9 offset:11264\n\t"
    "v_mfma_f32_16x16x32_bf16 v[56:59], a[28:31], v[28:31], v[56:59]\n\t"
    "ds_read_b128 a[84:87], v9 offset:10240\n\t"
    "v_mfma_f32_16x16x32_bf16 v[56:59], a[20:23], v[212:215], v[56:59]\n\t"
    "ds_read_b128 a[76:79], v9 offset:9216\n\t"
    "v_mfma_f32_16x16x32_bf16 v[56:59], a[12:15], v[216:219], v[56:59]\n\t"
    "v_cvt_pk_bf16_f32 v44, v64, v65\n\t"
    "v_cvt_pk_bf16_f32 v45, v66, v67\n\t"
    "ds_read_b128 a[68:71], v9 offset:8192\n\t"
    "s_waitcnt lgkmcnt(4)\n\t"
    "v_mfma_f32_16x16x32_bf16 v[56:59], a[4:7], v[220:223], v[56:59]\n\t"
    "ds_read_b128 a[60:63], v9 offset:7168\n\t"
    "v_mfma_f32_16x16x32_bf16 v[36:39], a[48:51], v[220:223], 0\n\t"
    "ds_read_b128 a[52:55], v9 offset:6144\n\t"
    "v_mfma_f32_16x16x32_bf16 v[36:39], a[56:59], v[216:219], v[36:39]\n\t"
    "s_barrier\n\t"
    "ds_read_b128 a[96:99], v8 offset:12288\n\t"
    "v_mfma_f32_16x16x32_bf16 v[36:39], a[64:67], v[212:215], v[36:39]\n\t"
    "ds_read_b128 a[104:107], v8 offset:13312\n\t"
    "v_mfma_f32_16x16x32_bf16 v[36:39], a[72:75], v[28:31], v[36:39]\n\t"
    "v_cvt_pk_bf16_f32 v46, v56, v57\n\t"
    "ds_read_b128 a[112:115], v8 offset:14336\n\t"
    "v_mfma_f32_16x16x32_bf16 v[36:39], a[80:83], v[32:35], v[36:39]\n\t"
    "v_cvt_pk_bf16_f32 v47, v58, v59\n\t"
    "ds_read_b128 a[120:123], v8 offset:15360\n\t"
    "v_mfma_f32_16x16x32_bf16 v[36:39], a[88:91], v[40:43], v[36:39]\n\t"
    "ds_read_b128 a[128:131], v8 offset:16384\n\t"
    "s_waitcnt lgkmcnt(4)\n\t"
    "v_mfma_f32_16x16x32_bf16 v[48:51], a[92:95], v[40:43], 0\n\t"
    "ds_read_b128 a[136:139], v8 offset:17408\n\t"
    "v_mfma_f32_16x16x32_bf16 v[48:51], a[84:87], v[32:35], v[48:51]\n\t"
    "ds_read_b128 a[140:143], v9 offset:17408\n\t"
    "v_mfma_f32_16x16x32_bf16 v[48:51], a[76:79], v[28:31], v[48:51]\n\t"
    "ds_read_b128 a[132:135], v9 offset:16384\n\t"
    "v_mfma_f32_16x16x32_bf16 v[48:51], a[68:71], v[212:215], v[48:51]\n\t"
    "v_cvt_pk_bf16_f32 v56, v36, v37\n\t"
    "ds_read_b128 a[124:127], v9 offset:15360\n\t"
    "v_mfma_f32_16x16x32_bf16 v[48:51], a[60:63], v[216:219], v[48:51]\n\t"
    "v_cvt_pk_bf16_f32 v57, v38, v39\n\t"
    "ds_read_b128 a[116:119], v9 offset:14336\n\t"
    "v_mfma_f32_16x16x32_bf16 v[48:51], a[52:55], v[220:223], v[48:51]\n\t"
    "ds_read_b128 a[108:111], v9 offset:13312\n\t"
    "v_mfma_f32_16x16x32_bf16 v[60:63], a[96:99], v[220:223], 0\n\t"
    "ds_read_b128 a[100:103], v9 offset:12288\n\t"
    "s_waitcnt lgkmcnt(4)\n\t"
    "v_mfma_f32_16x16x32_bf16 v[60:63], a[104:107], v[216:219], v[60:63]\n\t"
    "ds_read_b128 a[144:147], v8 offset:18432\n\t"
    "v_mfma_f32_16x16x32_bf16 v[60:63], a[112:115], v[212:215], v[60:63]\n\t"
    "ds_read_b128 a[152:155], v8 offset:19456\n\t"
    "v_mfma_f32_16x16x32_bf16 v[60:63], a[120:123], v[28:31], v[60:63]\n\t"
    "v_cvt_pk_bf16_f32 v58, v48, v49\n\t"
    "v_cvt_pk_bf16_f32 v59, v50, v51\n\t"
    "ds_read_b128 a[160:163], v8 offset:20480\n\t"
    "v_mfma_f32_16x16x32_bf16 v[60:63], a[128:131], v[32:35], v[60:63]\n\t"
    "ds_read_b128 a[168:171], v8 offset:21504\n\t"
    "v_mfma_f32_16x16x32_bf16 v[60:63], a[136:139], v[40:43], v[60:63]\n\t"
    "ds_read_b128 a[176:179], v8 offset:22528\n\t"
    "v_mfma_f32_16x16x32_bf16 v[36:39], a[140:143], v[40:43], 0\n\t"
    "ds_read_b128 a[184:187], v8 offset:23552\n\t"
    "v_mfma_f32_16x16x32_bf16 v[36:39], a[132:135], v[32:35], v[36:39]\n\t"
    "s_waitcnt lgkmcnt(3)\n\t"
    "v_mfma_f32_16x16x32_bf16 v[36:39], a[124:127], v[28:31], v[36:39]\n\t"
    "v_mfma_f32_16x16x32_bf16 v[36:39], a[116:119], v[212:215], v[36:39]\n\t"
    "v_cvt_pk_bf16_f32 v48, v60, v61\n\t"
    "v_cvt_pk_bf16_f32 v49, v62, v63\n\t"
    "v_mfma_f32_16x16x32_bf16 v[36:39], a[108:111], v[216:219], v[36:39]\n\t"
    "v_mfma_f32_16x16x32_bf16 v[36:39], a[100:103], v[220:223], v[36:39]\n\t"
    "v_mfma_f32_16x16x32_bf16 v[60:63], a[144:147], v[220:223], 0\n\t"
    "v_mfma_f32_16x16x32_bf16 v[60:63], a[152:155], v[216:219], v[60:63]\n\t"
    "v_mfma_f32_16x16x32_bf16 v[60:63], a[160:163], v[212:215], v[60:63]\n\t"
    "s_waitcnt lgkmcnt(0)\n\t"
    "v_mfma_f32_16x16x32_bf16 v[60:63], a[168:171], v[28:31], v[60:63]\n\t"
    "v_cvt_pk_bf16_f32 v50, v36, v37\n\t"
    "v_mfma_f32_16x16x32_bf16 v[60:63], a[176:179], v[32:35], v[60:63]\n\t"
    "v_cvt_pk_bf16_f32 v51, v38, v39\n\t"
    "v_mfma_f32_16x16x32_bf16 v[60:63], a[184:187], v[40:43], v[60:63]\n\t"
    "v_mfma_f32_16x16x32_bf16 v[64:67], v[220:223], v[44:47], 0\n\t"
    "v_mfma_f32_16x16x32_bf16 v[64:67], v[216:219], v[56:59], v[64:67]\n\t"
    "ds_read_b128 a[188:191], v9 offset:23552\n\t"
    "v_mfma_f32_16x16x32_bf16 v[64:67], v[212:215], v[48:51], v[64:67]\n\t"
    "ds_read_b128 a[180:183], v9 offset:22528\n\t"
    "v_mfma_f32_16x16x32_bf16 v[36:39], v[188:191], v[48:51], 0\n\t"
    "ds_read_b128 a[172:175], v9 offset:21504\n\t"
    "v_mfma_f32_16x16x32_bf16 v[36:39], v[192:195], v[56:59], v[36:39]\n\t"
    "ds_read_b128 a[164:167], v9 offset:20480\n\t"
    "v_mfma_f32_16x16x32_bf16 v[36:39], v[196:199], v[44:47], v[36:39]\n\t"
    "ds_read_b128 a[156:159], v9 offset:19456\n\t"
    "v_mfma_f32_16x16x32_bf16 v[68:71], v[172:175], v[44:47], 0\n\t"
    "ds_read_b128 a[148:151], v9 offset:18432\n\t"
    "v_mfma_f32_16x16x32_bf16 v[68:71], v[168:171], v[56:59], v[68:71]\n\t"
    "s_barrier\n\t"
    "ds_read_b128 a[192:195], v8 offset:24576\n\t"
    "v_mfma_f32_16x16x32_bf16 v[68:71], v[164:167], v[48:51], v[68:71]\n\t"
    "ds_read_b128 a[200:203], v8 offset:25600\n\t"
    "v_mfma_f32_16x16x32_bf16 v[72:75], v[140:143], v[48:51], 0\n\t"
    "ds_read_b128 a[208:211], v8 offset:26624\n\t"
    "v_mfma_f32_16x16x32_bf16 v[72:75], v[144:147], v[56:59], v[72:75]\n\t"
    "ds_read_b128 a[216:219], v8 offset:27648\n\t"
    "v_mfma_f32_16x16x32_bf16 v[72:75], v[148:151], v[44:47], v[72:75]\n\t"
    "ds_read_b128 a[224:227], v8 offset:28672\n\t"
    "s_waitcnt lgkmcnt(4)\n\t"
    "v_mfma_f32_16x16x32_bf16 v[48:51], a[188:191], v[40:43], 0\n\t"
    "ds_read_b128 a[232:235], v8 offset:29696\n\t"
    "v_mfma_f32_16x16x32_bf16 v[48:51], a[180:183], v[32:35], v[48:51]\n\t"
    "ds_read_b128 a[236:239], v9 offset:29696\n\t"
    "v_mfma_f32_16x16x32_bf16 v[48:51], a[172:175], v[28:31], v[48:51]\n\t"
    "ds_read_b128 a[228:231], v9 offset:28672\n\t"
    "v_mfma_f32_16x16x32_bf16 v[48:51], a[164:167], v[212:215], v[48:51]\n\t"
    "ds_read_b128 a[220:223], v9 offset:27648\n\t"
    "v_mfma_f32_16x16x32_bf16 v[48:51], a[156:159], v[216:219], v[48:51]\n\t"
    "ds_read_b128 a[212:215], v9 offset:26624\n\t"
    "v_mfma_f32_16x16x32_bf16 v[48:51], a[148:151], v[220:223], v[48:51]\n\t"
    "ds_read_b128 a[204:207], v9 offset:25600\n\t"
    "v_mfma_f32_16x16x32_bf16 v[44:47], a[192:195], v[220:223], 0\n\t"
    "ds_read_b128 a[196:199], v9 offset:24576\n\t"
    "s_waitcnt lgkmcnt(4)\n\t"
    "v_mfma_f32_16x16x32_bf16 v[44:47], a[200:203], v[216:219], v[44:47]\n\t"
    "v_cvt_pk_bf16_f32 v56, v60, v61\n\t"
    "ds_read_b128 a[240:243], v8 offset:30720\n\t"
    "v_mfma_f32_16x16x32_bf16 v[44:47], a[208:211], v[212:215], v[44:47]\n\t"
    "ds_read_b128 a[248:251], v8 offset:31744\n\t"
    "v_mfma_f32_16x16x32_bf16 v[44:47], a[216:219], v[28:31], v[44:47]\n\t"
    "v_cvt_pk_bf16_f32 v58, v48, v49\n\t"
    "ds_read_b128 v[224:227], v8 offset:32768\n\t"
    "v_mfma_f32_16x16x32_bf16 v[44:47], a[224:227], v[32:35], v[44:47]\n\t"
    "v_cvt_pk_bf16_f32 v59, v50, v51\n\t"
    "ds_read_b128 v[232:235], v8 offset:33792\n\t"
    "v_mfma_f32_16x16x32_bf16 v[44:47], a[232:235], v[40:43], v[44:47]\n\t"
    "v_cvt_pk_bf16_f32 v57, v62, v63\n\t"
    "ds_read_b128 v[240:243], v8 offset:34816\n\t"
    "v_mfma_f32_16x16x32_bf16 v[76:79], a[236:239], v[40:43], 0\n\t"
    "ds_read_b128 v[248:251], v8 offset:35840\n\t"
    "v_mfma_f32_16x16x32_bf16 v[76:79], a[228:231], v[32:35], v[76:79]\n\t"
    "ds_read_b128 v[252:255], v9 offset:35840\n\t"
    "s_waitcnt lgkmcnt(4)\n\t"
    "v_mfma_f32_16x16x32_bf16 v[76:79], a[220:223], v[28:31], v[76:79]\n\t"
    "ds_read_b128 v[244:247], v9 offset:34816\n\t"
    "v_mfma_f32_16x16x32_bf16 v[76:79], a[212:215], v[212:215], v[76:79]\n\t"
    "v_cvt_pk_bf16_f32 v48, v44, v45\n\t"
    "ds_read_b128 v[236:239], v9 offset:33792\n\t"
    "v_mfma_f32_16x16x32_bf16 v[76:79], a[204:207], v[216:219], v[76:79]\n\t"
    "v_cvt_pk_bf16_f32 v49, v46, v47\n\t"
    "ds_read_b128 v[228:231], v9 offset:32768\n\t"
    "v_mfma_f32_16x16x32_bf16 v[76:79], a[196:199], v[220:223], v[76:79]\n\t"
    "ds_read_b128 a[252:255], v9 offset:31744\n\t"
    "v_mfma_f32_16x16x32_bf16 v[80:83], a[240:243], v[220:223], 0\n\t"
    "ds_read_b128 a[244:247], v9 offset:30720\n\t"
    "v_mfma_f32_16x16x32_bf16 v[80:83], a[248:251], v[216:219], v[80:83]\n\t"
    "v_mfma_f32_16x16x32_bf16 v[80:83], v[224:227], v[212:215], v[80:83]\n\t"
    "s_waitcnt lgkmcnt(2)\n\t"
    "v_mfma_f32_16x16x32_bf16 v[80:83], v[232:235], v[28:31], v[80:83]\n\t"
    "v_cvt_pk_bf16_f32 v50, v76, v77\n\t"
    "v_cvt_pk_bf16_f32 v51, v78, v79\n\t"
    "v_mfma_f32_16x16x32_bf16 v[80:83], v[240:243], v[32:35], v[80:83]\n\t"
    "v_mfma_f32_16x16x32_bf16 v[80:83], v[248:251], v[40:43], v[80:83]\n\t"
    "v_mfma_f32_16x16x32_bf16 v[12:15], v[252:255], v[40:43], 0\n\t"
    "v_mfma_f32_16x16x32_bf16 v[12:15], v[244:247], v[32:35], v[12:15]\n\t"
    "v_mfma_f32_16x16x32_bf16 v[12:15], v[236:239], v[28:31], v[12:15]\n\t"
    "v_mfma_f32_16x16x32_bf16 v[12:15], v[228:231], v[212:215], v[12:15]\n\t"
    "v_cvt_pk_bf16_f32 v16, v80, v81\n\t"
    "v_cvt_pk_bf16_f32 v17, v82, v83\n\t"
    "s_waitcnt lgkmcnt(0)\n\t"
    "v_mfma_f32_16x16x32_bf16 v[12:15], a[252:255], v[216:219], v[12:15]\n\t"
    "v_mfma_f32_16x16x32_bf16 v[12:15], a[244:247], v[220:223], v[12:15]\n\t"
    "v_mfma_f32_16x16x32_bf16 v[20:23], a[0:3], v[196:199], 0\n\t"
    "v_mfma_f32_16x16x32_bf16 v[20:23], a[8:11], v[192:195], v[20:23]\n\t"
    "v_mfma_f32_16x16x32_bf16 v[20:23], a[16:19], v[188:191], v[20:23]\n\t"
    "v_mfma_f32_16x16x32_bf16 v[20:23], a[24:27], v[184:187], v[20:23]\n\t"
    "v_cvt_pk_bf16_f32 v18, v12, v13\n\t"
    "v_mfma_f32_16x16x32_bf16 v[20:23], a[32:35], v[180:183], v[20:23]\n\t"
    "v_mfma_f32_16x16x32_bf16 v[20:23], a[40:43], v[176:179], v[20:23]\n\t"
    "v_cvt_pk_bf16_f32 v19, v14, v15\n\t"
    "v_mfma_f32_16x16x32_bf16 v[64:67], v[208:211], v[56:59], v[64:67]\n\t"
    "v_mfma_f32_16x16x32_bf16 v[64:67], v[204:207], v[48:51], v[64:67]\n\t"
    "v_mfma_f32_16x16x32_bf16 v[64:67], v[200:203], v[16:19], v[64:67]\n\t"
    "v_mfma_f32_16x16x32_bf16 v[36:39], v[176:179], v[16:19], v[36:39]\n\t"
    "v_cvt_pk_bf16_f32 v12, v20, v21\n\t"
    "v_cvt_pk_bf16_f32 v13, v22, v23\n\t"
    "v_mfma_f32_16x16x32_bf16 v[36:39], v[180:183], v[48:51], v[36:39]\n\t"
    "v_mfma_f32_16x16x32_bf16 v[36:39], v[184:187], v[56:59], v[36:39]\n\t"
    "v_mfma_f32_16x16x32_bf16 v[68:71], v[160:163], v[56:59], v[68:71]\n\t"
    "v_max3_f32 v20, v64, v65, v66\n\t"
    "v_mfma_f32_16x16x32_bf16 v[68:71], v[156:159], v[48:51], v[68:71]\n\t"
    "v_mfma_f32_16x16x32_bf16 v[68:71], v[152:155], v[16:19], v[68:71]\n\t"
    "v_mfma_f32_16x16x32_bf16 v[72:75], v[128:131], v[16:19], v[72:75]\n\t"
    "v_max3_f32 v21, v67, v36, v37\n\t"
    "v_max3_f32 v20, v20, v38, v39\n\t"
    "v_mfma_f32_16x16x32_bf16 v[72:75], v[132:135], v[48:51], v[72:75]\n\t"
    "v_mfma_f32_16x16x32_bf16 v[72:75], v[136:139], v[56:59], v[72:75]\n\t"
    "v_mfma_f32_16x16x32_bf16 v[16:19], a[44:47], v[176:179], 0\n\t"
    "v_max3_f32 v21, v21, v68, v69\n\t"
    "v_max3_f32 v20, v20, v70, v71\n\t"
    "v_mfma_f32_16x16x32_bf16 v[16:19], a[36:39], v[180:183], v[16:19]\n\t"
    "v_mfma_f32_16x16x32_bf16 v[16:19], a[28:31], v[184:187], v[16:19]\n\t"
    "v_mfma_f32_16x16x32_bf16 v[16:19], a[20:23], v[188:191], v[16:19]\n\t"
    "v_max3_f32 v21, v21, v72, v73\n\t"
    "v_max3_f32 v20, v20, v74, v75\n\t"
    "v_mfma_f32_16x16x32_bf16 v[16:19], a[12:15], v[192:195], v[16:19]\n\t"
    "v_max_f32_e32 v20, v20, v21\n\t"
    "v_mov_b32_e32 v21, v20\n\t"
    "v_mfma_f32_16x16x32_bf16 v[16:19], a[4:7], v[196:199], v[16:19]\n\t"
    "v_mfma_f32_16x16x32_bf16 v[24:27], a[48:51], v[196:199], 0\n\t"
    "v_permlane32_swap_b32 v20, v21\n\t"
    "v_max_f32_e32 v20, v20, v21\n\t"
    "v_mfma_f32_16x16x32_bf16 v[24:27], a[56:59], v[192:195], v[24:27]\n\t"
    "v_mov_b32_e32 v21, v20\n\t"
    "v_mfma_f32_16x16x32_bf16 v[24:27], a[64:67], v[188:191], v[24:27]\n\t"
    "v_mfma_f32_16x16x32_bf16 v[24:27], a[72:75], v[184:187], v[24:27]\n\t"
    "v_cvt_pk_bf16_f32 v14, v16, v17\n\t"
    "v_cvt_pk_bf16_f32 v15, v18, v19\n\t"
    "v_mfma_f32_16x16x32_bf16 v[24:27], a[80:83], v[180:183], v[24:27]\n\t"
    "v_permlane16_swap_b32 v20, v21\n\t"
    "v_max_f32_e32 v20, v20, v21\n\t"
    "v_mfma_f32_16x16x32_bf16 v[24:27], a[88:91], v[176:179], v[24:27]\n\t"
    "v_sub_f32_e32 v64, v64, v20\n\t"
    "v_sub_f32_e32 v65, v65, v20\n\t"
    "v_mfma_f32_16x16x32_bf16 v[16:19], a[92:95], v[176:179], 0\n\t"
    "v_exp_f32_e32 v64, v64\n\t"
    "v_mfma_f32_16x16x32_bf16 v[16:19], a[84:87], v[180:183], v[16:19]\n\t"
    "v_exp_f32_e32 v65, v65\n\t"
    "v_mfma_f32_16x16x32_bf16 v[16:19], a[76:79], v[184:187], v[16:19]\n\t"
    "v_add_f32_e32 v22, v64, v65\n\t"
    "v_sub_f32_e32 v66, v66, v20\n\t"
    "v_mfma_f32_16x16x32_bf16 v[16:19], a[68:71], v[188:191], v[16:19]\n\t"
    "v_cvt_pk_bf16_f32 v28, v24, v25\n\t"
    "v_cvt_pk_bf16_f32 v29, v26, v27\n\t"
    "v_mfma_f32_16x16x32_bf16 v[16:19], a[60:63], v[192:195], v[16:19]\n\t"
    "v_exp_f32_e32 v66, v66\n\t"
    "v_mfma_f32_16x16x32_bf16 v[16:19], a[52:55], v[196:199], v[16:19]\n\t"
    "v_sub_f32_e32 v67, v67, v20\n\t"
    "v_sub_f32_e32 v36, v36, v20\n\t"
    "v_mfma_f32_16x16x32_bf16 v[24:27], a[96:99], v[196:199], 0\n\t"
    "v_exp_f32_e32 v67, v67\n\t"
    "v_mfma_f32_16x16x32_bf16 v[24:27], a[104:107], v[192:195], v[24:27]\n\t"
    "v_add_f32_e32 v23, v66, v67\n\t"
    "v_sub_f32_e32 v37, v37, v20\n\t"
    "v_mfma_f32_16x16x32_bf16 v[24:27], a[112:115], v[188:191], v[24:27]\n\t"
    "v_exp_f32_e32 v36, v36\n\t"
    "v_mfma_f32_16x16x32_bf16 v[24:27], a[120:123], v[184:187], v[24:27]\n\t"
    "v_cvt_pk_bf16_f32 v30, v16, v17\n\t"
    "v_cvt_pk_bf16_f32 v31, v18, v19\n\t"
    "v_mfma_f32_16x16x32_bf16 v[24:27], a[128:131], v[180:183], v[24:27]\n\t"
    "v_add_f32_e32 v22, v22, v36\n\t"
    "v_sub_f32_e32 v38, v38, v20\n\t"
    "v_mfma_f32_16x16x32_bf16 v[24:27], a[136:139], v[176:179], v[24:27]\n\t"
    "v_exp_f32_e32 v37, v37\n\t"
    "v_mfma_f32_16x16x32_bf16 v[16:19], a[140:143], v[176:179], 0\n\t"
    "v_add_f32_e32 v22, v22, v37\n\t"
    "v_sub_f32_e32 v39, v39, v20\n\t"
    "v_mfma_f32_16x16x32_bf16 v[16:19], a[132:135], v[180:183], v[16:19]\n\t"
    "v_exp_f32_e32 v38, v38\n\t"
    "v_mfma_f32_16x16x32_bf16 v[16:19], a[124:127], v[184:187], v[16:19]\n\t"
    "v_add_f32_e32 v23, v23, v38\n\t"
    "v_sub_f32_e32 v68, v68, v20\n\t"
    "v_mfma_f32_16x16x32_bf16 v[16:19], a[116:119], v[188:191], v[16:19]\n\t"
    "v_cvt_pk_bf16_f32 v32, v24, v25\n\t"
    "v_cvt_pk_bf16_f32 v33, v26, v27\n\t"
    "v_mfma_f32_16x16x32_bf16 v[16:19], a[108:111], v[192:195], v[16:19]\n\t"
    "v_exp_f32_e32 v39, v39\n\t"
    "v_mfma_f32_16x16x32_bf16 v[16:19], a[100:103], v[196:199], v[16:19]\n\t"
    "v_add_f32_e32 v23, v23, v39\n\t"
    "v_sub_f32_e32 v69, v69, v20\n\t"
    "v_mfma_f32_16x16x32_bf16 v[24:27], a[144:147], v[196:199], 0\n\t"
    "v_exp_f32_e32 v68, v68\n\t"
    "v_mfma_f32_16x16x32_bf16 v[24:27], a[152:155], v[192:195], v[24:27]\n\t"
    "v_add_f32_e32 v22, v22, v68\n\t"
    "v_sub_f32_e32 v70, v70, v20\n\t"
    "v_mfma_f32_16x16x32_bf16 v[24:27], a[160:163], v[188:191], v[24:27]\n\t"
    "v_sub_f32_e32 v71, v71, v20\n\t"
    "v_mfma_f32_16x16x32_bf16 v[24:27], a[168:171], v[184:187], v[24:27]\n\t"
    "v_cvt_pk_bf16_f32 v34, v16, v17\n\t"
    "v_mfma_f32_16x16x32_bf16 v[24:27], a[176:179], v[180:183], v[24:27]\n\t"
    "v_cvt_pk_bf16_f32 v35, v18, v19\n\t"
    "v_sub_f32_e32 v72, v72, v20\n\t"
    "s_barrier\n\t"
    "v_mfma_f32_16x16x32_bf16 v[24:27], a[184:187], v[176:179], v[24:27]\n\t"
    "v_sub_f32_e32 v73, v73, v20\n\t"
    "v_mfma_f32_16x16x32_bf16 v[40:43], v[220:223], v[12:15], 0\n\t"
    "v_exp_f32_e32 v69, v69\n\t"
    "v_mfma_f32_16x16x32_bf16 v[40:43], v[216:219], v[28:31], v[40:43]\n\t"
    "v_add_f32_e32 v22, v22, v69\n\t"
    "s_add_u32 m0, %[rbeg], 24576\n\t"
    "s_add_u32 s61, %[wb], 12582912\n\t"
    "v_mfma_f32_16x16x32_bf16 v[40:43], v[212:215], v[32:35], v[40:43]\n\t"
    "v_sub_f32_e32 v74, v74, v20\n\t"
    "v_mfma_f32_16x16x32_bf16 v[16:19], v[188:191], v[32:35], 0\n\t"
    "v_exp_f32_e32 v70, v70\n\t"
    "v_mfma_f32_16x16x32_bf16 v[16:19], v[192:195], v[28:31], v[16:19]\n\t"
    "v_add_f32_e32 v23, v23, v70\n\t"
    "v_sub_f32_e32 v75, v75, v20\n\t"
    "v_mfma_f32_16x16x32_bf16 v[16:19], v[196:199], v[12:15], v[16:19]\n\t"
    "v_exp_f32_e32 v71, v71\n\t"
    "buffer_load_dwordx4 v7, s[52:55], s61 offen offset:0 nt lds\n\t"
    "v_mfma_f32_16x16x32_bf16 v[44:47], v[172:175], v[12:15], 0\n\t"
    "v_add_f32_e32 v23, v23, v71\n\t"
    "v_cvt_pk_bf16_f32 v48, v24, v25\n\t"
    "v_mfma_f32_16x16x32_bf16 v[44:47], v[168:171], v[28:31], v[44:47]\n\t"
    "v_exp_f32_e32 v72, v72\n\t"
    "v_mfma_f32_16x16x32_bf16 v[44:47], v[164:167], v[32:35], v[44:47]\n\t"
    "v_add_f32_e32 v22, v22, v72\n\t"
    "v_cvt_pk_bf16_f32 v49, v26, v27\n\t"
    "v_mfma_f32_16x16x32_bf16 v[52:55], v[140:143], v[32:35], 0\n\t"
    "v_exp_f32_e32 v73, v73\n\t"
    "v_mfma_f32_16x16x32_bf16 v[52:55], v[144:147], v[28:31], v[52:55]\n\t"
    "v_add_f32_e32 v22, v22, v73\n\t"
    "buffer_load_dwordx4 v7, s[52:55], s61 offen offset:1024 nt lds\n\t"
    "v_mfma_f32_16x16x32_bf16 v[52:55], v[148:151], v[12:15], v[52:55]\n\t"
    "v_exp_f32_e32 v74, v74\n\t"
    "v_mfma_f32_16x16x32_bf16 v[24:27], a[188:191], v[176:179], 0\n\t"
    "v_add_f32_e32 v23, v23, v74\n\t"
    "v_mfma_f32_16x16x32_bf16 v[24:27], a[180:183], v[180:183], v[24:27]\n\t"
    "v_exp_f32_e32 v75, v75\n\t"
    "v_mfma_f32_16x16x32_bf16 v[24:27], a[172:175], v[184:187], v[24:27]\n\t"
    "v_add_f32_e32 v23, v23, v75\n\t"
    "v_add_f32_e32 v22, v22, v23\n\t"
    "v_mfma_f32_16x16x32_bf16 v[24:27], a[164:167], v[188:191], v[24:27]\n\t"
    "v_mov_b32_e32 v23, v22\n\t"
    "buffer_load_dwordx4 v7, s[52:55], s61 offen offset:2048 nt lds\n\t"
    "v_mfma_f32_16x16x32_bf16 v[24:27], a[156:159], v[192:195], v[24:27]\n\t"
    "v_mfma_f32_16x16x32_bf16 v[24:27], a[148:151], v[196:199], v[24:27]\n\t"
    "v_permlane32_swap_b32 v22, v23\n\t"
    "v_add_f32_e32 v22, v22, v23\n\t"
    "v_mfma_f32_16x16x32_bf16 v[12:15], a[192:195], v[196:199], 0\n\t"
    "v_mov_b32_e32 v23, v22\n\t"
    "v_mfma_f32_16x16x32_bf16 v[12:15], a[200:203], v[192:195], v[12:15]\n\t"
    "v_mfma_f32_16x16x32_bf16 v[12:15], a[208:211], v[188:191], v[12:15]\n\t"
    "v_permlane16_swap_b32 v22, v23\n\t"
    "v_add_f32_e32 v22, v22, v23\n\t"
    "s_add_u32 m0, %[rbeg], 27648\n\t"
    "s_add_u32 s61, %[wb], 12779520\n\t"
    "v_mfma_f32_16x16x32_bf16 v[12:15], a[216:219], v[184:187], v[12:15]\n\t"
    "v_cvt_pk_bf16_f32 v50, v24, v25\n\t"
    "v_cvt_pk_bf16_f32 v51, v26, v27\n\t"
    "v_mfma_f32_16x16x32_bf16 v[12:15], a[224:227], v[180:183], v[12:15]\n\t"
    "v_rcp_f32_e32 v22, v22\n\t"
    "v_mfma_f32_16x16x32_bf16 v[12:15], a[232:235], v[176:179], v[12:15]\n\t"
    "v_mul_f32_e32 v64, v64, v22\n\t"
    "v_mul_f32_e32 v65, v65, v22\n\t"
    "v_mfma_f32_16x16x32_bf16 v[28:31], a[236:239], v[176:179], 0\n\t"
    "v_cvt_pk_bf16_f32 v24, v64, v65\n\t"
    "v_mul_f32_e32 v66, v66, v22\n\t"
    "v_mfma_f32_16x16x32_bf16 v[28:31], a[228:231], v[180:183], v[28:31]\n\t"
    "v_mul_f32_e32 v67, v67, v22\n\t"
    "v_cvt_pk_bf16_f32 v25, v66, v67\n\t"
    "buffer_load_dwordx4 v7, s[52:55], s61 offen offset:0 nt lds\n\t"
    "v_mfma_f32_16x16x32_bf16 v[28:31], a[220:223], v[184:187], v[28:31]\n\t"
    "v_mul_f32_e32 v36, v36, v22\n\t"
    "v_mul_f32_e32 v37, v37, v22\n\t"
    "v_mfma_f32_16x16x32_bf16 v[28:31], a[212:215], v[188:191], v[28:31]\n\t"
    "v_cvt_pk_bf16_f32 v32, v12, v13\n\t"
    "v_cvt_pk_bf16_f32 v33, v14, v15\n\t"
    "v_mfma_f32_16x16x32_bf16 v[28:31], a[204:207], v[192:195], v[28:31]\n\t"
    "v_cvt_pk_bf16_f32 v26, v36, v37\n\t"
    "v_mul_f32_e32 v38, v38, v22\n\t"
    "v_mfma_f32_16x16x32_bf16 v[28:31], a[196:199], v[196:199], v[28:31]\n\t"
    "v_mul_f32_e32 v39, v39, v22\n\t"
    "v_cvt_pk_bf16_f32 v27, v38, v39\n\t"
    "v_mfma_f32_16x16x32_bf16 v[12:15], a[240:243], v[196:199], 0\n\t"
    "v_mul_f32_e32 v68, v68, v22\n\t"
    "v_mul_f32_e32 v69, v69, v22\n\t"
    "buffer_load_dwordx4 v7, s[52:55], s61 offen offset:1024 nt lds\n\t"
    "v_mfma_f32_16x16x32_bf16 v[12:15], a[248:251], v[192:195], v[12:15]\n\t"
    "v_cvt_pk_bf16_f32 v56, v68, v69\n\t"
    "v_mul_f32_e32 v70, v70, v22\n\t"
    "v_mfma_f32_16x16x32_bf16 v[12:15], v[224:227], v[188:191], v[12:15]\n\t"
    "v_mul_f32_e32 v71, v71, v22\n\t"
    "v_cvt_pk_bf16_f32 v57, v70, v71\n\t"
    "v_mfma_f32_16x16x32_bf16 v[12:15], v[232:235], v[184:187], v[12:15]\n\t"
    "v_cvt_pk_bf16_f32 v34, v28, v29\n\t"
    "v_cvt_pk_bf16_f32 v35, v30, v31\n\t"
    "v_mfma_f32_16x16x32_bf16 v[12:15], v[240:243], v[180:183], v[12:15]\n\t"
    "v_mul_f32_e32 v72, v72, v22\n\t"
    "v_mul_f32_e32 v73, v73, v22\n\t"
    "v_mfma_f32_16x16x32_bf16 v[12:15], v[248:251], v[176:179], v[12:15]\n\t"
    "v_cvt_pk_bf16_f32 v58, v72, v73\n\t"
    "v_mul_f32_e32 v74, v74, v22\n\t"
    "v_mfma_f32_16x16x32_bf16 v[28:31], v[252:255], v[176:179], 0\n\t"
    "v_mul_f32_e32 v75, v75, v22\n\t"
    "v_cvt_pk_bf16_f32 v59, v74, v75\n\t"
    "v_mfma_f32_16x16x32_bf16 v[28:31], v[244:247], v[180:183], v[28:31]\n\t"
    "buffer_load_dwordx4 v7, s[52:55], s61 offen offset:2048 nt lds\n\t"
    "v_mfma_f32_16x16x32_bf16 v[28:31], v[236:239], v[184:187], v[28:31]\n\t"
    "v_mfma_f32_16x16x32_bf16 v[28:31], v[228:231], v[188:191], v[28:31]\n\t"
    "v_cvt_pk_bf16_f32 v20, v12, v13\n\t"
    "v_cvt_pk_bf16_f32 v21, v14, v15\n\t"
    "v_mfma_f32_16x16x32_bf16 v[28:31], a[252:255], v[192:195], v[28:31]\n\t"
    "v_mfma_f32_16x16x32_bf16 v[28:31], a[244:247], v[196:199], v[28:31]\n\t"
    "s_add_u32 m0, %[rbeg], 30720\n\t"
    "s_add_u32 s61, %[wb], 12976128\n\t"
    "v_mfma_f32_16x16x32_bf16 v[12:15], a[0:3], v[172:175], 0\n\t"
    "ds_write_b128 v6, v[24:27] offset:0\n\t"
    "v_mfma_f32_16x16x32_bf16 v[12:15], a[8:11], v[168:171], v[12:15]\n\t"
    "v_mfma_f32_16x16x32_bf16 v[12:15], a[16:19], v[164:167], v[12:15]\n\t"
    "v_mfma_f32_16x16x32_bf16 v[12:15], a[24:27], v[160:163], v[12:15]\n\t"
    "v_cvt_pk_bf16_f32 v22, v28, v29\n\t"
    "v_mfma_f32_16x16x32_bf16 v[12:15], a[32:35], v[156:159], v[12:15]\n\t"
    "ds_write_b128 v6, v[56:59] offset:1024\n\t"
    "v_mfma_f32_16x16x32_bf16 v[12:15], a[40:43], v[152:155], v[12:15]\n\t"
    "v_cvt_pk_bf16_f32 v23, v30, v31\n\t"
    "buffer_load_dwordx4 v7, s[52:55], s61 offen offset:0 nt lds\n\t"
    "v_mfma_f32_16x16x32_bf16 v[40:43], v[208:211], v[48:51], v[40:43]\n\t"
    "v_mfma_f32_16x16x32_bf16 v[40:43], v[204:207], v[32:35], v[40:43]\n\t"
    "v_mfma_f32_16x16x32_bf16 v[40:43], v[200:203], v[20:23], v[40:43]\n\t"
    "s_add_i32 s64, s62, 24576\n\t"
    "v_mfma_f32_16x16x32_bf16 v[16:19], v[176:179], v[20:23], v[16:19]\n\t"
    "s_cmp_eq_u32 s64, %[rend]\n\t"
    "s_cselect_b32 s64, %[rbeg], s64\n\t"
    "buffer_load_dwordx4 v7, s[52:55], s61 offen offset:1024 nt lds\n\t"
    "v_mfma_f32_16x16x32_bf16 v[16:19], v[180:183], v[32:35], v[16:19]\n\t"
    "s_sub_i32 s65, s63, s64\n\t"
    "v_cvt_pk_bf16_f32 v24, v12, v13\n\t"
    "v_mfma_f32_16x16x32_bf16 v[16:19], v[184:187], v[48:51], v[16:19]\n\t"
    "v_add_u32_e32 v0, s65, v0\n\t"
    "v_add_u32_e32 v1, s65, v1\n\t"
    "v_mfma_f32_16x16x32_bf16 v[44:47], v[160:163], v[48:51], v[44:47]\n\t"
    "v_add_u32_e32 v2, s65, v2\n\t"
    "v_add_u32_e32 v3, s65, v3\n\t"
    "v_mfma_f32_16x16x32_bf16 v[44:47], v[156:159], v[32:35], v[44:47]\n\t"
    "v_add_u32_e32 v4, s65, v4\n\t"
    "v_add_u32_e32 v5, s65, v5\n\t"
    "v_mfma_f32_16x16x32_bf16 v[44:47], v[152:155], v[20:23], v[44:47]\n\t"
    "v_max3_f32 v28, v40, v41, v42\n\t"
    "v_cvt_pk_bf16_f32 v25, v14, v15\n\t"
    "v_mfma_f32_16x16x32_bf16 v[52:55], v[128:131], v[20:23], v[52:55]\n\t"
    "v_max3_f32 v29, v43, v16, v17\n\t"
    "v_max3_f32 v28, v28, v18, v19\n\t"
    "v_mfma_f32_16x16x32_bf16 v[52:55], v[132:135], v[32:35], v[52:55]\n\t"
    ".Lh4_%=:\n\t"
    "s_cbranch_execz .Lh5_%=\n\t"
    "buffer_load_dwordx4 v7, s[52:55], s61 offen offset:2048 nt lds\n\t"
    "v_mfma_f32_16x16x32_bf16 v[52:55], v[136:139], v[48:51], v[52:55]\n\t"
    "v_mfma_f32_16x16x32_bf16 v[12:15], a[44:47], v[152:155], 0\n\t"
    "v_max3_f32 v29, v29, v44, v45\n\t"
    "v_max3_f32 v28, v28, v46, v47\n\t"
    "v_mfma_f32_16x16x32_bf16 v[12:15], a[36:39], v[156:159], v[12:15]\n\t"
    "s_add_u32 m0, %[rbeg], 33792\n\t"
    "s_add_u32 s61, %[wb], 13172736\n\t"
    "v_mfma_f32_16x16x32_bf16 v[12:15], a[28:31], v[160:163], v[12:15]\n\t"
    "v_mfma_f32_16x16x32_bf16 v[12:15], a[20:23], v[164:167], v[12:15]\n\t"
    "v_max3_f32 v29, v29, v52, v53\n\t"
    "v_max3_f32 v28, v28, v54, v55\n\t"
    "v_mfma_f32_16x16x32_bf16 v[12:15], a[12:15], v[168:171], v[12:15]\n\t"
    "v_max_f32_e32 v28, v28, v29\n\t"
    "v_mov_b32_e32 v29, v28\n\t"
    "v_mfma_f32_16x16x32_bf16 v[12:15], a[4:7], v[172:175], v[12:15]\n\t"
    "v_mfma_f32_16x16x32_bf16 v[20:23], a[48:51], v[172:175], 0\n\t"
    "v_permlane32_swap_b32 v28, v29\n\t"
    "v_max_f32_e32 v28, v28, v29\n\t"
    "v_mfma_f32_16x16x32_bf16 v[20:23], a[56:59], v[168:171], v[20:23]\n\t"
    "v_mov_b32_e32 v29, v28\n\t"
    "buffer_load_dwordx4 v7, s[52:55], s61 offen offset:0 nt lds\n\t"
    "v_mfma_f32_16x16x32_bf16 v[20:23], a[64:67], v[164:167], v[20:23]\n\t"
    "v_mfma_f32_16x16x32_bf16 v[20:23], a[72:75], v[160:163], v[20:23]\n\t"
    "v_cvt_pk_bf16_f32 v26, v12, v13\n\t"
    "v_cvt_pk_bf16_f32 v27, v14, v15\n\t"
    "v_mfma_f32_16x16x32_bf16 v[20:23], a[80:83], v[156:159], v[20:23]\n\t"
    "v_permlane16_swap_b32 v28, v29\n\t"
    "v_max_f32_e32 v28, v28, v29\n\t"
    "v_mfma_f32_16x16x32_bf16 v[20:23], a[88:91], v[152:155], v[20:23]\n\t"
    "v_sub_f32_e32 v40, v40, v28\n\t"
    "v_sub_f32_e32 v41, v41, v28\n\t"
    "buffer_load_dwordx4 v7, s[52:55], s61 offen offset:1024 nt lds\n\t"
    "v_mfma_f32_16x16x32_bf16 v[12:15], a[92:95], v[152:155], 0\n\t"
    "v_exp_f32_e32 v40, v40\n\t"
    "v_mfma_f32_16x16x32_bf16 v[12:15], a[84:87], v[156:159], v[12:15]\n\t"
    "v_exp_f32_e32 v41, v41\n\t"
    "v_mfma_f32_16x16x32_bf16 v[12:15], a[76:79], v[160:163], v[12:15]\n\t"
    "v_add_f32_e32 v30, v40, v41\n\t"
    "v_sub_f32_e32 v42, v42, v28\n\t"
    "v_mfma_f32_16x16x32_bf16 v[12:15], a[68:71], v[164:167], v[12:15]\n\t"
    "v_cvt_pk_bf16_f32 v32, v20, v21\n\t"
    "v_cvt_pk_bf16_f32 v33, v22, v23\n\t"
    "v_mfma_f32_16x16x32_bf16 v[12:15], a[60:63], v[168:171], v[12:15]\n\t"
    "v_exp_f32_e32 v42, v42\n\t"
    "v_mfma_f32_16x16x32_bf16 v[12:15], a[52:55], v[172:175], v[12:15]\n\t"
    "v_sub_f32_e32 v43, v43, v28\n\t"
    "v_sub_f32_e32 v16, v16, v28\n\t"
    "buffer_load_dwordx4 v7, s[52:55], s61 offen offset:2048 nt lds\n\t"
    "v_mfma_f32_16x16x32_bf16 v[20:23], a[96:99], v[172:175], 0\n\t"
    "v_exp_f32_e32 v43, v43\n\t"
    "v_mfma_f32_16x16x32_bf16 v[20:23], a[104:107], v[168:171], v[20:23]\n\t"
    "v_add_f32_e32 v31, v42, v43\n\t"
    "v_sub_f32_e32 v17, v17, v28\n\t"
    "v_mfma_f32_16x16x32_bf16 v[20:23], a[112:115], v[164:167], v[20:23]\n\t"
    "v_exp_f32_e32 v16, v16\n\t"
    "v_mfma_f32_16x16x32_bf16 v[20:23], a[120:123], v[160:163], v[20:23]\n\t"
    "v_cvt_pk_bf16_f32 v34, v12, v13\n\t"
    "v_cvt_pk_bf16_f32 v35, v14, v15\n\t"
    "v_mfma_f32_16x16x32_bf16 v[20:23], a[128:131], v[156:159], v[20:23]\n\t"
    "v_add_f32_e32 v30, v30, v16\n\t"
    "v_sub_f32_e32 v18, v18, v28\n\t"
    "s_add_u32 m0, %[rbeg], 36864\n\t"
    "s_add_u32 s61, %[wb], 13369344\n\t"
    "v_mfma_f32_16x16x32_bf16 v[20:23], a[136:139], v[152:155], v[20:23]\n\t"
    "v_exp_f32_e32 v17, v17\n\t"
    "v_mfma_f32_16x16x32_bf16 v[12:15], a[140:143], v[152:155], 0\n\t"
    "v_add_f32_e32 v30, v30, v17\n\t"
    "v_sub_f32_e32 v19, v19, v28\n\t"
    "v_mfma_f32_16x16x32_bf16 v[12:15], a[132:135], v[156:159], v[12:15]\n\t"
    "v_exp_f32_e32 v18, v18\n\t"
    "v_mfma_f32_16x16x32_bf16 v[12:15], a[124:127], v[160:163], v[12:15]\n\t"
    "v_add_f32_e32 v31, v31, v18\n\t"
    "v_sub_f32_e32 v44, v44, v28\n\t"
    "v_mfma_f32_16x16x32_bf16 v[12:15], a[116:119], v[164:167], v[12:15]\n\t"
    "v_cvt_pk_bf16_f32 v36, v20, v21\n\t"
    "v_cvt_pk_bf16_f32 v37, v22, v23\n\t"
    "buffer_load_dwordx4 v7, s[52:55], s61 offen offset:0 nt lds\n\t"
    "v_mfma_f32_16x16x32_bf16 v[12:15], a[108:111], v[168:171], v[12:15]\n\t"
    "v_exp_f32_e32 v19, v19\n\t"
    "v_mfma_f32_16x16x32_bf16 v[12:15], a[100:103], v[172:175], v[12:15]\n\t"
    "v_add_f32_e32 v31, v31, v19\n\t"
    "v_sub_f32_e32 v45, v45, v28\n\t"
    "v_mfma_f32_16x16x32_bf16 v[20:23], a[144:147], v[172:175], 0\n\t"
    "v_exp_f32_e32 v44, v44\n\t"
    "v_mfma_f32_16x16x32_bf16 v[20:23], a[152:155], v[168:171], v[20:23]\n\t"
    "v_add_f32_e32 v30, v30, v44\n\t"
    "v_sub_f32_e32 v46, v46, v28\n\t"
    "v_mfma_f32_16x16x32_bf16 v[20:23], a[160:163], v[164:167], v[20:23]\n\t"
    "v_sub_f32_e32 v47, v47, v28\n\t"
    "buffer_load_dwordx4 v7, s[52:55], s61 offen offset:1024 nt lds\n\t"
    "v_mfma_f32_16x16x32_bf16 v[20:23], a[168:171], v[160:163], v[20:23]\n\t"
    "v_cvt_pk_bf16_f32 v38, v12, v13\n\t"
    "v_mfma_f32_16x16x32_bf16 v[20:23], a[176:179], v[156:159], v[20:23]\n\t"
    "v_cvt_pk_bf16_f32 v39, v14, v15\n\t"
    "v_sub_f32_e32 v52, v52, v28\n\t"
    "v_mfma_f32_16x16x32_bf16 v[20:23], a[184:187], v[152:155], v[20:23]\n\t"
    "v_sub_f32_e32 v53, v53, v28\n\t"
    "v_mfma_f32_16x16x32_bf16 v[56:59], v[220:223], v[24:27], 0\n\t"
    "v_exp_f32_e32 v45, v45\n\t"
    "v_mfma_f32_16x16x32_bf16 v[56:59], v[216:219], v[32:35], v[56:59]\n\t"
    "v_add_f32_e32 v30, v30, v45\n\t"
    "buffer_load_dwordx4 v7, s[52:55], s61 offen offset:2048 nt lds\n\t"
    "v_mfma_f32_16x16x32_bf16 v[56:59], v[212:215], v[36:39], v[56:59]\n\t"
    "v_sub_f32_e32 v54, v54, v28\n\t"
    "v_mfma_f32_16x16x32_bf16 v[60:63], v[188:191], v[36:39], 0\n\t"
    "v_exp_f32_e32 v46, v46\n\t"
    "v_mfma_f32_16x16x32_bf16 v[60:63], v[192:195], v[32:35], v[60:63]\n\t"
    "v_add_f32_e32 v31, v31, v46\n\t"
    "v_sub_f32_e32 v55, v55, v28\n\t"
    "v_mfma_f32_16x16x32_bf16 v[60:63], v[196:199], v[24:27], v[60:63]\n\t"
    "v_exp_f32_e32 v47, v47\n\t"
    "v_mfma_f32_16x16x32_bf16 v[64:67], v[172:175], v[24:27], 0\n\t"
    "v_add_f32_e32 v31, v31, v47\n\t"
    "v_cvt_pk_bf16_f32 v12, v20, v21\n\t"
    "s_add_u32 m0, %[rbeg], 39936\n\t"
    "s_add_u32 s61, %[wb], 13565952\n\t"
    "v_mfma_f32_16x16x32_bf16 v[64:67], v[168:171], v[32:35], v[64:67]\n\t"
    "v_exp_f32_e32 v52, v52\n\t"
    "v_mfma_f32_16x16x32_bf16 v[64:67], v[164:167], v[36:39], v[64:67]\n\t"
    "v_add_f32_e32 v30, v30, v52\n\t"
    "v_cvt_pk_bf16_f32 v13, v22, v23\n\t"
    "v_mfma_f32_16x16x32_bf16 v[68:71], v[140:143], v[36:39], 0\n\t"
    "v_exp_f32_e32 v53, v53\n\t"
    "v_mfma_f32_16x16x32_bf16 v[68:71], v[144:147], v[32:35], v[68:71]\n\t"
    "v_add_f32_e32 v30, v30, v53\n\t"
    "v_mfma_f32_16x16x32_bf16 v[68:71], v[148:151], v[24:27], v[68:71]\n\t"
    "v_exp_f32_e32 v54, v54\n\t"
    "v_mfma_f32_16x16x32_bf16 v[20:23], a[188:191], v[152:155], 0\n\t"
    "v_add_f32_e32 v31, v31, v54\n\t"
    "buffer_load_dwordx4 v7, s[52:55], s61 offen offset:0 nt lds\n\t"
    "v_mfma_f32_16x16x32_bf16 v[20:23], a[180:183], v[156:159], v[20:23]\n\t"
    "v_exp_f32_e32 v55, v55\n\t"
    "v_mfma_f32_16x16x32_bf16 v[20:23], a[172:175], v[160:163], v[20:23]\n\t"
    "v_add_f32_e32 v31, v31, v55\n\t"
    "v_add_f32_e32 v30, v30, v31\n\t"
    "v_mfma_f32_16x16x32_bf16 v[20:23], a[164:167], v[164:167], v[20:23]\n\t"
    "v_mov_b32_e32 v31, v30\n\t"
    "v_mfma_f32_16x16x32_bf16 v[20:23], a[156:159], v[168:171], v[20:23]\n\t"
    "v_mfma_f32_16x16x32_bf16 v[20:23], a[148:151], v[172:175], v[20:23]\n\t"
    "v_permlane32_swap_b32 v30, v31\n\t"
    "v_add_f32_e32 v30, v30, v31\n\t"
    "v_mfma_f32_16x16x32_bf16 v[24:27], a[192:195], v[172:175], 0\n\t"
    "v_mov_b32_e32 v31, v30\n\t"
    "buffer_load_dwordx4 v7, s[52:55], s61 offen offset:1024 nt lds\n\t"
    "v_mfma_f32_16x16x32_bf16 v[24:27], a[200:203], v[168:171], v[24:27]\n\t"
    "v_mfma_f32_16x16x32_bf16 v[24:27], a[208:211], v[164:167], v[24:27]\n\t"
    "v_permlane16_swap_b32 v30, v31\n\t"
    "v_add_f32_e32 v30, v30, v31\n\t"
    "v_mfma_f32_16x16x32_bf16 v[24:27], a[216:219], v[160:163], v[24:27]\n\t"
    "v_cvt_pk_bf16_f32 v14, v20, v21\n\t"
    "v_cvt_pk_bf16_f32 v15, v22, v23\n\t"
    "v_mfma_f32_16x16x32_bf16 v[24:27], a[224:227], v[156:159], v[24:27]\n\t"
    "v_rcp_f32_e32 v30, v30\n\t"
    "buffer_load_dwordx4 v7, s[52:55], s61 offen offset:2048 nt lds\n\t"
    "v_mfma_f32_16x16x32_bf16 v[24:27], a[232:235], v[152:155], v[24:27]\n\t"
    "v_mul_f32_e32 v40, v40, v30\n\t"
    "v_mul_f32_e32 v41, v41, v30\n\t"
    "v_mfma_f32_16x16x32_bf16 v[32:35], a[236:239], v[152:155], 0\n\t"
    "v_cvt_pk_bf16_f32 v20, v40, v41\n\t"
    "v_mul_f32_e32 v42, v42, v30\n\t"
    "v_mfma_f32_16x16x32_bf16 v[32:35], a[228:231], v[156:159], v[32:35]\n\t"
    "v_mul_f32_e32 v43, v43, v30\n\t"
    "v_cvt_pk_bf16_f32 v21, v42, v43\n\t"
    "v_mfma_f32_16x16x32_bf16 v[32:35], a[220:223], v[160:163], v[32:35]\n\t"
    "v_mul_f32_e32 v16, v16, v30\n\t"
    "v_mul_f32_e32 v17, v17, v30\n\t"
    "v_mfma_f32_16x16x32_bf16 v[32:35], a[212:215], v[164:167], v[32:35]\n\t"
    "v_cvt_pk_bf16_f32 v36, v24, v25\n\t"
    "v_cvt_pk_bf16_f32 v37, v26, v27\n\t"
    "s_add_u32 m0, %[rbeg], 43008\n\t"
    "s_add_u32 s61, %[wb], 13762560\n\t"
    "v_mfma_f32_16x16x32_bf16 v[32:35], a[204:207], v[168:171], v[32:35]\n\t"
    "v_cvt_pk_bf16_f32 v22, v16, v17\n\t"
    "v_mul_f32_e32 v18, v18, v30\n\t"
    "v_mfma_f32_16x16x32_bf16 v[32:35], a[196:199], v[172:175], v[32:35]\n\t"
    "v_mul_f32_e32 v19, v19, v30\n\t"
    "v_cvt_pk_bf16_f32 v23, v18, v19\n\t"
    "v_mfma_f32_16x16x32_bf16 v[24:27], a[240:243], v[172:175], 0\n\t"
    "v_mul_f32_e32 v44, v44, v30\n\t"
    "v_mul_f32_e32 v45, v45, v30\n\t"
    "v_mfma_f32_16x16x32_bf16 v[24:27], a[248:251], v[168:171], v[24:27]\n\t"
    "v_cvt_pk_bf16_f32 v40, v44, v45\n\t"
    "v_mul_f32_e32 v46, v46, v30\n\t"
    "v_mfma_f32_16x16x32_bf16 v[24:27], v[224:227], v[164:167], v[24:27]\n\t"
    "v_mul_f32_e32 v47, v47, v30\n\t"
    "v_cvt_pk_bf16_f32 v41, v46, v47\n\t"
    "buffer_load_dwordx4 v7, s[52:55], s61 offen offset:0 nt lds\n\t"
    "v_mfma_f32_16x16x32_bf16 v[24:27], v[232:235], v[160:163], v[24:27]\n\t"
    "v_cvt_pk_bf16_f32 v38, v32, v33\n\t"
    "v_cvt_pk_bf16_f32 v39, v34, v35\n\t"
    "v_mfma_f32_16x16x32_bf16 v[24:27], v[240:243], v[156:159], v[24:27]\n\t"
    "v_mul_f32_e32 v52, v52, v30\n\t"
    "v_mul_f32_e32 v53, v53, v30\n\t"
    "v_mfma_f32_16x16x32_bf16 v[24:27], v[248:251], v[152:155], v[24:27]\n\t"
    "v_cvt_pk_bf16_f32 v42, v52, v53\n\t"
    "v_mul_f32_e32 v54, v54, v30\n\t"
    "v_mfma_f32_16x16x32_bf16 v[16:19], v[252:255], v[152:155], 0\n\t"
    "v_mul_f32_e32 v55, v55, v30\n\t"
    "v_cvt_pk_bf16_f32 v43, v54, v55\n\t"
    "v_mfma_f32_16x16x32_bf16 v[16:19], v[244:247], v[156:159], v[16:19]\n\t"
    "buffer_load_dwordx4 v7, s[52:55], s61 offen offset:1024 nt lds\n\t"
    "v_mfma_f32_16x16x32_bf16 v[16:19], v[236:239], v[160:163], v[16:19]\n\t"
    "v_mfma_f32_16x16x32_bf16 v[16:19], v[228:231], v[164:167], v[16:19]\n\t"
    "v_cvt_pk_bf16_f32 v28, v24, v25\n\t"
    "v_cvt_pk_bf16_f32 v29, v26, v27\n\t"
    "v_mfma_f32_16x16x32_bf16 v[16:19], a[252:255], v[168:171], v[16:19]\n\t"
    "v_mfma_f32_16x16x32_bf16 v[16:19], a[244:247], v[172:175], v[16:19]\n\t"
    "v_mfma_f32_16x16x32_bf16 v[24:27], a[0:3], v[148:151], 0\n\t"
    "ds_write_b128 v6, v[20:23] offset:2048\n\t"
    "v_mfma_f32_16x16x32_bf16 v[24:27], a[8:11], v[144:147], v[24:27]\n\t"
    "buffer_load_dwordx4 v7, s[52:55], s61 offen offset:2048 nt lds\n\t"
    "v_mfma_f32_16x16x32_bf16 v[24:27], a[16:19], v[140:143], v[24:27]\n\t"
    "v_mfma_f32_16x16x32_bf16 v[24:27], a[24:27], v[136:139], v[24:27]\n\t"
    "v_cvt_pk_bf16_f32 v30, v16, v17\n\t"
    "v_mfma_f32_16x16x32_bf16 v[24:27], a[32:35], v[132:135], v[24:27]\n\t"
    "ds_write_b128 v6, v[40:43] offset:3072\n\t"
    "v_mfma_f32_16x16x32_bf16 v[24:27], a[40:43], v[128:131], v[24:27]\n\t"
    "v_cvt_pk_bf16_f32 v31, v18, v19\n\t"
    "v_mfma_f32_16x16x32_bf16 v[56:59], v[208:211], v[12:15], v[56:59]\n\t"
    "s_add_u32 m0, %[rbeg], 46080\n\t"
    "s_add_u32 s61, %[wb], 13959168\n\t"
    "v_mfma_f32_16x16x32_bf16 v[56:59], v[204:207], v[36:39], v[56:59]\n\t"
    "v_mfma_f32_16x16x32_bf16 v[56:59], v[200:203], v[28:31], v[56:59]\n\t"
    "v_mfma_f32_16x16x32_bf16 v[60:63], v[176:179], v[28:31], v[60:63]\n\t"
    "v_cvt_pk_bf16_f32 v40, v24, v25\n\t"
    "v_cvt_pk_bf16_f32 v41, v26, v27\n\t"
    "v_mfma_f32_16x16x32_bf16 v[60:63], v[180:183], v[36:39], v[60:63]\n\t"
    "v_mfma_f32_16x16x32_bf16 v[60:63], v[184:187], v[12:15], v[60:63]\n\t"
    "buffer_load_dwordx4 v7, s[52:55], s61 offen offset:0 nt lds\n\t"
    "v_mfma_f32_16x16x32_bf16 v[64:67], v[160:163], v[12:15], v[64:67]\n\t"
    "v_max3_f32 v72, v56, v57, v58\n\t"
    "v_mfma_f32_16x16x32_bf16 v[64:67], v[156:159], v[36:39], v[64:67]\n\t"
    "v_mfma_f32_16x16x32_bf16 v[64:67], v[152:155], v[28:31], v[64:67]\n\t"
    "v_mfma_f32_16x16x32_bf16 v[68:71], v[128:131], v[28:31], v[68:71]\n\t"
    "v_max3_f32 v73, v59, v60, v61\n\t"
    "v_max3_f32 v72, v72, v62, v63\n\t"
    "v_mfma_f32_16x16x32_bf16 v[68:71], v[132:135], v[36:39], v[68:71]\n\t"
    "buffer_load_dwordx4 v7, s[52:55], s61 offen offset:1024 nt lds\n\t"
    "v_mfma_f32_16x16x32_bf16 v[68:71], v[136:139], v[12:15], v[68:71]\n\t"
    "v_mfma_f32_16x16x32_bf16 v[16:19], a[44:47], v[128:131], 0\n\t"
    "v_max3_f32 v73, v73, v64, v65\n\t"
    "v_max3_f32 v72, v72, v66, v67\n\t"
    "v_mfma_f32_16x16x32_bf16 v[16:19], a[36:39], v[132:135], v[16:19]\n\t"
    "v_mfma_f32_16x16x32_bf16 v[16:19], a[28:31], v[136:139], v[16:19]\n\t"
    "v_mfma_f32_16x16x32_bf16 v[16:19], a[20:23], v[140:143], v[16:19]\n\t"
    "v_max3_f32 v73, v73, v68, v69\n\t"
    "v_max3_f32 v72, v72, v70, v71\n\t"
    "v_mfma_f32_16x16x32_bf16 v[16:19], a[12:15], v[144:147], v[16:19]\n\t"
    "v_max_f32_e32 v72, v72, v73\n\t"
    "v_mov_b32_e32 v73, v72\n\t"
    "v_mfma_f32_16x16x32_bf16 v[16:19], a[4:7], v[148:151], v[16:19]\n\t"
    "buffer_load_dwordx4 v7, s[52:55], s61 offen offset:2048 nt lds\n\t"
    "v_mfma_f32_16x16x32_bf16 v[12:15], a[48:51], v[148:151], 0\n\t"
    "v_permlane32_swap_b32 v72, v73\n\t"
    "v_max_f32_e32 v72, v72, v73\n\t"
    "v_mfma_f32_16x16x32_bf16 v[12:15], a[56:59], v[144:147], v[12:15]\n\t"
    "v_mov_b32_e32 v73, v72\n\t"
    "v_mfma_f32_16x16x32_bf16 v[12:15], a[64:67], v[140:143], v[12:15]\n\t"
    "s_add_u32 m0, %[rbeg], 49152\n\t"
    "s_add_u32 s61, %[wb], 25165824\n\t"
    "v_mfma_f32_16x16x32_bf16 v[12:15], a[72:75], v[136:139], v[12:15]\n\t"
    "v_cvt_pk_bf16_f32 v42, v16, v17\n\t"
    "v_cvt_pk_bf16_f32 v43, v18, v19\n\t"
    "v_mfma_f32_16x16x32_bf16 v[12:15], a[80:83], v[132:135], v[12:15]\n\t"
    "v_permlane16_swap_b32 v72, v73\n\t"
    "v_max_f32_e32 v72, v72, v73\n\t"
    "v_mfma_f32_16x16x32_bf16 v[12:15], a[88:91], v[128:131], v[12:15]\n\t"
    "v_sub_f32_e32 v56, v56, v72\n\t"
    "v_sub_f32_e32 v57, v57, v72\n\t"
    "v_mfma_f32_16x16x32_bf16 v[16:19], a[92:95], v[128:131], 0\n\t"
    "v_exp_f32_e32 v56, v56\n\t"
    "v_mfma_f32_16x16x32_bf16 v[16:19], a[84:87], v[132:135], v[16:19]\n\t"
    "v_exp_f32_e32 v57, v57\n\t"
    "buffer_load_dwordx4 v7, s[52:55], s61 offen offset:0 nt lds\n\t"
    "v_mfma_f32_16x16x32_bf16 v[16:19], a[76:79], v[136:139], v[16:19]\n\t"
    "v_add_f32_e32 v74, v56, v57\n\t"
    "v_sub_f32_e32 v58, v58, v72\n\t"
    "v_mfma_f32_16x16x32_bf16 v[16:19], a[68:71], v[140:143], v[16:19]\n\t"
    "v_cvt_pk_bf16_f32 v44, v12, v13\n\t"
    "v_cvt_pk_bf16_f32 v45, v14, v15\n\t"
    "v_mfma_f32_16x16x32_bf16 v[16:19], a[60:63], v[144:147], v[16:19]\n\t"
    "v_exp_f32_e32 v58, v58\n\t"
    "v_mfma_f32_16x16x32_bf16 v[16:19], a[52:55], v[148:151], v[16:19]\n\t"
    "v_sub_f32_e32 v59, v59, v72\n\t"
    "v_sub_f32_e32 v60, v60, v72\n\t"
    "v_mfma_f32_16x16x32_bf16 v[12:15], a[96:99], v[148:151], 0\n\t"
    "v_exp_f32_e32 v59, v59\n\t"
    "buffer_load_dwordx4 v7, s[52:55], s61 offen offset:1024 nt lds\n\t"
    "v_mfma_f32_16x16x32_bf16 v[12:15], a[104:107], v[144:147], v[12:15]\n\t"
    "v_add_f32_e32 v75, v58, v59\n\t"
    "v_sub_f32_e32 v61, v61, v72\n\t"
    "v_mfma_f32_16x16x32_bf16 v[12:15], a[112:115], v[140:143], v[12:15]\n\t"
    "v_exp_f32_e32 v60, v60\n\t"
    "v_mfma_f32_16x16x32_bf16 v[12:15], a[120:123], v[136:139], v[12:15]\n\t"
    "v_cvt_pk_bf16_f32 v46, v16, v17\n\t"
    "v_cvt_pk_bf16_f32 v47, v18, v19\n\t"
    "v_mfma_f32_16x16x32_bf16 v[12:15], a[128:131], v[132:135], v[12:15]\n\t"
    "v_add_f32_e32 v74, v74, v60\n\t"
    "v_sub_f32_e32 v62, v62, v72\n\t"
    "v_mfma_f32_16x16x32_bf16 v[12:15], a[136:139], v[128:131], v[12:15]\n\t"
    "v_exp_f32_e32 v61, v61\n\t"
    "buffer_load_dwordx4 v7, s[52:55], s61 offen offset:2048 nt lds\n\t"
    "v_mfma_f32_16x16x32_bf16 v[16:19], a[140:143], v[128:131], 0\n\t"
    "v_add_f32_e32 v74, v74, v61\n\t"
    "v_sub_f32_e32 v63, v63, v72\n\t"
    "v_mfma_f32_16x16x32_bf16 v[16:19], a[132:135], v[132:135], v[16:19]\n\t"
    "v_exp_f32_e32 v62, v62\n\t"
    "v_mfma_f32_16x16x32_bf16 v[16:19], a[124:127], v[136:139], v[16:19]\n\t"
    "v_add_f32_e32 v75, v75, v62\n\t"
    "v_sub_f32_e32 v64, v64, v72\n\t"
    "v_mfma_f32_16x16x32_bf16 v[16:19], a[116:119], v[140:143], v[16:19]\n\t"
    "v_cvt_pk_bf16_f32 v32, v12, v13\n\t"
    "v_cvt_pk_bf16_f32 v33, v14, v15\n\t"
    "v_mfma_f32_16x16x32_bf16 v[16:19], a[108:111], v[144:147], v[16:19]\n\t"
    "v_exp_f32_e32 v63, v63\n\t"
    "v_mfma_f32_16x16x32_bf16 v[16:19], a[100:103], v[148:151], v[16:19]\n\t"
    "v_add_f32_e32 v75, v75, v63\n\t"
    "v_sub_f32_e32 v65, v65, v72\n\t"
    "v_mfma_f32_16x16x32_bf16 v[48:51], a[144:147], v[148:151], 0\n\t"
    "v_exp_f32_e32 v64, v64\n\t"
    "v_mfma_f32_16x16x32_bf16 v[48:51], a[152:155], v[144:147], v[48:51]\n\t"
    "v_add_f32_e32 v74, v74, v64\n\t"
    "v_sub_f32_e32 v66, v66, v72\n\t"
    "s_mov_b32 s60, 8\n\t"
    "s_add_u32 s61, %[wb], 25362432\n\t"
    ".Lmid_%=:\n\t"
    "v_mfma_f32_16x16x32_bf16 v[48:51], a[160:163], v[140:143], v[48:51]\n\t"
    "s_add_u32 m0, s62, 3072\n\t"
    "v_sub_f32_e32 v67, v67, v72\n\t"
    "v_mfma_f32_16x16x32_bf16 v[48:51], a[168:171], v[136:139], v[48:51]\n\t"
    "buffer_load_dwordx4 v7, s[52:55], s61 offen offset:0 nt lds\n\t"
    "v_cvt_pk_bf16_f32 v34, v16, v17\n\t"
    "v_mfma_f32_16x16x32_bf16 v[48:51], a[176:179], v[132:135], v[48:51]\n\t"
    "v_cvt_pk_bf16_f32 v35, v18, v19\n\t"
    "v_sub_f32_e32 v68, v68, v72\n\t"
    "v_mfma_f32_16x16x32_bf16 v[48:51], a[184:187], v[128:131], v[48:51]\n\t"
    "buffer_load_dwordx4 v7, s[52:55], s61 offen offset:1024 nt lds\n\t"
    "v_sub_f32_e32 v69, v69, v72\n\t"
    "v_mfma_f32_16x16x32_bf16 v[12:15], v[220:223], v[40:43], 0\n\t"
    "v_exp_f32_e32 v65, v65\n\t"
    "v_mfma_f32_16x16x32_bf16 v[12:15], v[216:219], v[44:47], v[12:15]\n\t"
    "buffer_load_dwordx4 v7, s[52:55], s61 offen offset:2048 nt lds\n\t"
    "v_add_f32_e32 v74, v74, v65\n\t"
    "v_mfma_f32_16x16x32_bf16 v[12:15], v[212:215], v[32:35], v[12:15]\n\t"
    "s_waitcnt vmcnt(24)\n\t"
    "s_add_u32 s61, s61, 196608\n\t"
    "v_mfma_f32_16x16x32_bf16 v[16:19], v[188:191], v[32:35], 0\n\t"
    "v_exp_f32_e32 v66, v66\n\t"
    "v_mfma_f32_16x16x32_bf16 v[16:19], v[192:195], v[44:47], v[16:19]\n\t"
    "ds_read_b128 v[220:223], v0 offset:0\n\t"
    "v_add_f32_e32 v75, v75, v66\n\t"
    "v_mfma_f32_16x16x32_bf16 v[16:19], v[196:199], v[40:43], v[16:19]\n\t"
    "v_exp_f32_e32 v67, v67\n\t"
    "v_mfma_f32_16x16x32_bf16 v[20:23], v[172:175], v[40:43], 0\n\t"
    "ds_read_b128 v[216:219], v1 offset:0\n\t"
    "v_add_f32_e32 v75, v75, v67\n\t"
    "v_mfma_f32_16x16x32_bf16 v[20:23], v[168:171], v[44:47], v[20:23]\n\t"
    "v_exp_f32_e32 v68, v68\n\t"
    "v_mfma_f32_16x16x32_bf16 v[20:23], v[164:167], v[32:35], v[20:23]\n\t"
    "ds_read_b128 v[212:215], v2 offset:0\n\t"
    "v_add_f32_e32 v74, v74, v68\n\t"
    "v_mfma_f32_16x16x32_bf16 v[24:27], v[140:143], v[32:35], 0\n\t"
    "v_exp_f32_e32 v69, v69\n\t"
    "v_mfma_f32_16x16x32_bf16 v[24:27], v[144:147], v[44:47], v[24:27]\n\t"
    "ds_read_b128 v[28:31], v3 offset:0\n\t"
    "v_add_f32_e32 v74, v74, v69\n\t"
    "v_mfma_f32_16x16x32_bf16 v[24:27], v[148:151], v[40:43], v[24:27]\n\t"
    "v_sub_f32_e32 v70, v70, v72\n\t"
    "v_sub_f32_e32 v71, v71, v72\n\t"
    "v_mfma_f32_16x16x32_bf16 v[52:55], a[188:191], v[128:131], 0\n\t"
    "ds_read_b128 v[32:35], v4 offset:0\n\t"
    "v_cvt_pk_bf16_f32 v36, v48, v49\n\t"
    "v_mfma_f32_16x16x32_bf16 v[52:55], a[180:183], v[132:135], v[52:55]\n\t"
    "v_exp_f32_e32 v70, v70\n\t"
    "v_mfma_f32_16x16x32_bf16 v[52:55], a[172:175], v[136:139], v[52:55]\n\t"
    "ds_read_b128 v[40:43], v5 offset:0\n\t"
    "v_add_f32_e32 v75, v75, v70\n\t"
    "v_mfma_f32_16x16x32_bf16 v[52:55], a[164:167], v[140:143], v[52:55]\n\t"
    "v_exp_f32_e32 v71, v71\n\t"
    "v_mfma_f32_16x16x32_bf16 v[52:55], a[156:159], v[144:147], v[52:55]\n\t"
    "v_add_f32_e32 v75, v75, v71\n\t"
    "v_add_f32_e32 v74, v74, v75\n\t"
    "v_mfma_f32_16x16x32_bf16 v[52:55], a[148:151], v[148:151], v[52:55]\n\t"
    "v_mov_b32_e32 v75, v74\n\t"
    "v_cvt_pk_bf16_f32 v37, v50, v51\n\t"
    "v_mfma_f32_16x16x32_bf16 v[76:79], a[192:195], v[148:151], 0\n\t"
    "v_mfma_f32_16x16x32_bf16 v[76:79], a[200:203], v[144:147], v[76:79]\n\t"
    "v_permlane32_swap_b32 v74, v75\n\t"
    "v_add_f32_e32 v74, v74, v75\n\t"
    "v_mfma_f32_16x16x32_bf16 v[76:79], a[208:211], v[140:143], v[76:79]\n\t"
    "v_mov_b32_e32 v75, v74\n\t"
    "v_mfma_f32_16x16x32_bf16 v[76:79], a[216:219], v[136:139], v[76:79]\n\t"
    "v_cvt_pk_bf16_f32 v38, v52, v53\n\t"
    "v_cvt_pk_bf16_f32 v39, v54, v55\n\t"
    "v_mfma_f32_16x16x32_bf16 v[76:79], a[224:227], v[132:135], v[76:79]\n\t"
    "v_permlane16_swap_b32 v74, v75\n\t"
    "v_add_f32_e32 v74, v74, v75\n\t"
    "v_mfma_f32_16x16x32_bf16 v[76:79], a[232:235], v[128:131], v[76:79]\n\t"
    "v_rcp_f32_e32 v74, v74\n\t"
    "v_mfma_f32_16x16x32_bf16 v[80:83], a[236:239], v[128:131], 0\n\t"
    "v_mul_f32_e32 v56, v56, v74\n\t"
    "v_mul_f32_e32 v57, v57, v74\n\t"
    "v_mfma_f32_16x16x32_bf16 v[80:83], a[228:231], v[132:135], v[80:83]\n\t"
    "v_cvt_pk_bf16_f32 v44, v56, v57\n\t"
    "v_mul_f32_e32 v58, v58, v74\n\t"
    "v_mfma_f32_16x16x32_bf16 v[80:83], a[220:223], v[136:139], v[80:83]\n\t"
    "v_mul_f32_e32 v59, v59, v74\n\t"
    "v_cvt_pk_bf16_f32 v45, v58, v59\n\t"
    "v_mfma_f32_16x16x32_bf16 v[80:83], a[212:215], v[140:143], v[80:83]\n\t"
    "v_cvt_pk_bf16_f32 v48, v76, v77\n\t"
    "v_cvt_pk_bf16_f32 v49, v78, v79\n\t"
    "v_mfma_f32_16x16x32_bf16 v[80:83], a[204:207], v[144:147], v[80:83]\n\t"
    "v_mul_f32_e32 v60, v60, v74\n\t"
    "v_mul_f32_e32 v61, v61, v74\n\t"
    "v_mfma_f32_16x16x32_bf16 v[80:83], a[196:199], v[148:151], v[80:83]\n\t"
    "v_cvt_pk_bf16_f32 v46, v60, v61\n\t"
    "v_mul_f32_e32 v62, v62, v74\n\t"
    "v_mfma_f32_16x16x32_bf16 v[76:79], a[240:243], v[148:151], 0\n\t"
    "v_mul_f32_e32 v63, v63, v74\n\t"
    "v_cvt_pk_bf16_f32 v47, v62, v63\n\t"
    "v_mfma_f32_16x16x32_bf16 v[76:79], a[248:251], v[144:147], v[76:79]\n\t"
    "v_mul_f32_e32 v64, v64, v74\n\t"
    "v_mul_f32_e32 v65, v65, v74\n\t"
    "v_mfma_f32_16x16x32_bf16 v[76:79], v[224:227], v[140:143], v[76:79]\n\t"
    "v_cvt_pk_bf16_f32 v52, v64, v65\n\t"
    "v_mul_f32_e32 v66, v66, v74\n\t"
    "v_mfma_f32_16x16x32_bf16 v[76:79], v[232:235], v[136:139], v[76:79]\n\t"
    "v_cvt_pk_bf16_f32 v50, v80, v81\n\t"
    "v_cvt_pk_bf16_f32 v51, v82, v83\n\t"
    "v_mfma_f32_16x16x32_bf16 v[76:79], v[240:243], v[132:135], v[76:79]\n\t"
    "v_mul_f32_e32 v67, v67, v74\n\t"
    "v_cvt_pk_bf16_f32 v53, v66, v67\n\t"
    "v_mfma_f32_16x16x32_bf16 v[76:79], v[248:251], v[128:131], v[76:79]\n\t"
    "v_mul_f32_e32 v68, v68, v74\n\t"
    "v_mul_f32_e32 v69, v69, v74\n\t"
    "v_mfma_f32_16x16x32_bf16 v[56:59], v[252:255], v[128:131], 0\n\t"
    "v_cvt_pk_bf16_f32 v54, v68, v69\n\t"
    "v_mul_f32_e32 v70, v70, v74\n\t"
    "v_mfma_f32_16x16x32_bf16 v[56:59], v[244:247], v[132:135], v[56:59]\n\t"
    "v_mul_f32_e32 v71, v71, v74\n\t"
    "v_cvt_pk_bf16_f32 v55, v70, v71\n\t"
    "v_mfma_f32_16x16x32_bf16 v[56:59], v[236:239], v[136:139], v[56:59]\n\t"
    "s_sub_u32 s60, s60, 1\n\t"
    "s_cmp_lg_u32 s60, 0\n\t"
    "v_mfma_f32_16x16x32_bf16 v[56:59], v[228:231], v[140:143], v[56:59]\n\t"
    "v_cvt_pk_bf16_f32 v60, v76, v77\n\t"
    "v_cvt_pk_bf16_f32 v61, v78, v79\n\t"
    "v_mfma_f32_16x16x32_bf16 v[56:59], a[252:255], v[144:147], v[56:59]\n\t"
    "s_mov_b32 s63, s62\n\t"
    "s_mov_b32 s62, s64\n\t"
    "v_mfma_f32_16x16x32_bf16 v[56:59], a[244:247], v[148:151], v[56:59]\n\t"
    "s_cbranch_scc0 .Ltail_%=\n\t"
    "s_waitcnt lgkmcnt(0)\n\t"
    "v_mfma_f32_16x16x32_bf16 v[64:67], a[0:3], v[220:223], 0\n\t"
    "ds_write_b128 v6, v[44:47] offset:4096\n\t"
    "v_mfma_f32_16x16x32_bf16 v[64:67], a[8:11], v[216:219], v[64:67]\n\t"
    "v_mfma_f32_16x16x32_bf16 v[64:67], a[16:19], v[212:215], v[64:67]\n\t"
    "s_add_u32 m0, s63, 6144\n\t"
    "v_mfma_f32_16x16x32_bf16 v[64:67], a[24:27], v[28:31], v[64:67]\n\t"
    "buffer_load_dwordx4 v7, s[52:55], s61 offen offset:0 nt lds\n\t"
    "v_cvt_pk_bf16_f32 v62, v56, v57\n\t"
    "v_mfma_f32_16x16x32_bf16 v[64:67], a[32:35], v[32:35], v[64:67]\n\t"
    "ds_write_b128 v6, v[52:55] offset:5120\n\t"
    "v_mfma_f32_16x16x32_bf16 v[64:67], a[40:43], v[40:43], v[64:67]\n\t"
    "buffer_load_dwordx4 v7, s[52:55], s61 offen offset:1024 nt lds\n\t"
    "v_cvt_pk_bf16_f32 v63, v58, v59\n\t"
    "v_mfma_f32_16x16x32_bf16 v[12:15], v[208:211], v[36:39], v[12:15]\n\t"
    "v_mfma_f32_16x16x32_bf16 v[12:15], v[204:207], v[48:51], v[12:15]\n\t"
    "buffer_load_dwordx4 v7, s[52:55], s61 offen offset:2048 nt lds\n\t"
    "v_mfma_f32_16x16x32_bf16 v[12:15], v[200:203], v[60:63], v[12:15]\n\t"
    "s_add_u32 s61, s61, 196608\n\t"
    "v_mfma_f32_16x16x32_bf16 v[16:19], v[176:179], v[60:63], v[16:19]\n\t"
    "v_cvt_pk_bf16_f32 v44, v64, v65\n\t"
    "v_cvt_pk_bf16_f32 v45, v66, v67\n\t"
    "v_mfma_f32_16x16x32_bf16 v[16:19], v[180:183], v[48:51], v[16:19]\n\t"
    "s_waitcnt lgkmcnt(0)\n\t"
    "s_barrier\n\t"
    "v_mfma_f32_16x16x32_bf16 v[16:19], v[184:187], v[36:39], v[16:19]\n\t"
    "v_mfma_f32_16x16x32_bf16 v[20:23], v[160:163], v[36:39], v[20:23]\n\t"
    "v_max3_f32 v52, v12, v13, v14\n\t"
    "v_mfma_f32_16x16x32_bf16 v[20:23], v[156:159], v[48:51], v[20:23]\n\t"
    "v_mfma_f32_16x16x32_bf16 v[20:23], v[152:155], v[60:63], v[20:23]\n\t"
    "v_mfma_f32_16x16x32_bf16 v[24:27], v[128:131], v[60:63], v[24:27]\n\t"
    "v_max3_f32 v53, v15, v16, v17\n\t"
    "v_max3_f32 v52, v52, v18, v19\n\t"
    "v_mfma_f32_16x16x32_bf16 v[24:27], v[132:135], v[48:51], v[24:27]\n\t"
    "s_waitcnt vmcnt(9)\n\t"
    "v_mfma_f32_16x16x32_bf16 v[24:27], v[136:139], v[36:39], v[24:27]\n\t"
    "ds_read_b128 v[188:191], v2 offset:6144\n\t"
    "v_mfma_f32_16x16x32_bf16 v[56:59], a[44:47], v[40:43], 0\n\t"
    "v_max3_f32 v53, v53, v20, v21\n\t"
    "v_max3_f32 v52, v52, v22, v23\n\t"
    "v_mfma_f32_16x16x32_bf16 v[56:59], a[36:39], v[32:35], v[56:59]\n\t"
    "ds_read_b128 v[192:195], v1 offset:6144\n\t"
    "v_mfma_f32_16x16x32_bf16 v[56:59], a[28:31], v[28:31], v[56:59]\n\t"
    "v_mfma_f32_16x16x32_bf16 v[56:59], a[20:23], v[212:215], v[56:59]\n\t"
    "ds_read_b128 v[196:199], v0 offset:6144\n\t"
    "v_max3_f32 v53, v53, v24, v25\n\t"
    "v_mfma_f32_16x16x32_bf16 v[56:59], a[12:15], v[216:219], v[56:59]\n\t"
    "v_max3_f32 v52, v52, v26, v27\n\t"
    "v_max_f32_e32 v52, v52, v53\n\t"
    "v_mfma_f32_16x16x32_bf16 v[56:59], a[4:7], v[220:223], v[56:59]\n\t"
    "ds_read_b128 v[172:175], v0 offset:12288\n\t"
    "v_mov_b32_e32 v53, v52\n\t"
    "v_mfma_f32_16x16x32_bf16 v[36:39], a[48:51], v[220:223], 0\n\t"
    "v_mfma_f32_16x16x32_bf16 v[36:39], a[56:59], v[216:219], v[36:39]\n\t"
    "ds_read_b128 v[168:171], v1 offset:12288\n\t"
    "v_permlane32_swap_b32 v52, v53\n\t"
    "v_mfma_f32_16x16x32_bf16 v[36:39], a[64:67], v[212:215], v[36:39]\n\t"
    "v_max_f32_e32 v52, v52, v53\n\t"
    "v_mov_b32_e32 v53, v52\n\t"
    "v_mfma_f32_16x16x32_bf16 v[36:39], a[72:75], v[28:31], v[36:39]\n\t"
    "ds_read_b128 v[164:167], v2 offset:12288\n\t"
    "v_cvt_pk_bf16_f32 v46, v56, v57\n\t"
    "v_mfma_f32_16x16x32_bf16 v[36:39], a[80:83], v[32:35], v[36:39]\n\t"
    "v_cvt_pk_bf16_f32 v47, v58, v59\n\t"
    "v_permlane16_swap_b32 v52, v53\n\t"
    "v_mfma_f32_16x16x32_bf16 v[36:39], a[88:91], v[40:43], v[36:39]\n\t"
    "ds_read_b128 v[140:143], v2 offset:18432\n\t"
    "v_max_f32_e32 v52, v52, v53\n\t"
    "v_mfma_f32_16x16x32_bf16 v[48:51], a[92:95], v[40:43], 0\n\t"
    "v_sub_f32_e32 v12, v12, v52\n\t"
    "v_sub_f32_e32 v13, v13, v52\n\t"
    "v_mfma_f32_16x16x32_bf16 v[48:51], a[84:87], v[32:35], v[48:51]\n\t"
    "ds_read_b128 v[144:147], v1 offset:18432\n\t"
    "v_sub_f32_e32 v14, v14, v52\n\t"
    "v_mfma_f32_16x16x32_bf16 v[48:51], a[76:79], v[28:31], v[48:51]\n\t"
    "v_exp_f32_e32 v12, v12\n\t"
    "v_mfma_f32_16x16x32_bf16 v[48:51], a[68:71], v[212:215], v[48:51]\n\t"
    "ds_read_b128 v[148:151], v0 offset:18432\n\t"
    "v_cvt_pk_bf16_f32 v56, v36, v37\n\t"
    "v_mfma_f32_16x16x32_bf16 v[48:51], a[60:63], v[216:219], v[48:51]\n\t"
    "v_cvt_pk_bf16_f32 v57, v38, v39\n\t"
    "v_sub_f32_e32 v15, v15, v52\n\t"
    "v_mfma_f32_16x16x32_bf16 v[48:51], a[52:55], v[220:223], v[48:51]\n\t"
    "v_exp_f32_e32 v13, v13\n\t"
    "v_mfma_f32_16x16x32_bf16 v[60:63], a[96:99], v[220:223], 0\n\t"
    "v_add_f32_e32 v54, v12, v13\n\t"
    "v_sub_f32_e32 v16, v16, v52\n\t"
    "v_mfma_f32_16x16x32_bf16 v[60:63], a[104:107], v[216:219], v[60:63]\n\t"
    "v_exp_f32_e32 v14, v14\n\t"
    "v_mfma_f32_16x16x32_bf16 v[60:63], a[112:115], v[212:215], v[60:63]\n\t"
    "v_exp_f32_e32 v15, v15\n\t"
    "v_mfma_f32_16x16x32_bf16 v[60:63], a[120:123], v[28:31], v[60:63]\n\t"
    "v_cvt_pk_bf16_f32 v58, v48, v49\n\t"
    "v_cvt_pk_bf16_f32 v59, v50, v51\n\t"
    "v_mfma_f32_16x16x32_bf16 v[60:63], a[128:131], v[32:35], v[60:63]\n\t"
    "v_add_f32_e32 v55, v14, v15\n\t"
    "v_sub_f32_e32 v17, v17, v52\n\t"
    "v_mfma_f32_16x16x32_bf16 v[60:63], a[136:139], v[40:43], v[60:63]\n\t"
    "v_exp_f32_e32 v16, v16\n\t"
    "v_mfma_f32_16x16x32_bf16 v[36:39], a[140:143], v[40:43], 0\n\t"
    "v_add_f32_e32 v54, v54, v16\n\t"
    "v_sub_f32_e32 v18, v18, v52\n\t"
    "v_mfma_f32_16x16x32_bf16 v[36:39], a[132:135], v[32:35], v[36:39]\n\t"
    "v_exp_f32_e32 v17, v17\n\t"
    "v_mfma_f32_16x16x32_bf16 v[36:39], a[124:127], v[28:31], v[36:39]\n\t"
    "v_add_f32_e32 v54, v54, v17\n\t"
    "v_sub_f32_e32 v19, v19, v52\n\t"
    "v_mfma_f32_16x16x32_bf16 v[36:39], a[116:119], v[212:215], v[36:39]\n\t"
    "v_cvt_pk_bf16_f32 v48, v60, v61\n\t"
    "v_cvt_pk_bf16_f32 v49, v62, v63\n\t"
    "v_mfma_f32_16x16x32_bf16 v[36:39], a[108:111], v[216:219], v[36:39]\n\t"
    "v_exp_f32_e32 v18, v18\n\t"
    "v_mfma_f32_16x16x32_bf16 v[36:39], a[100:103], v[220:223], v[36:39]\n\t"
    "v_add_f32_e32 v55, v55, v18\n\t"
    "v_sub_f32_e32 v20, v20, v52\n\t"
    "v_mfma_f32_16x16x32_bf16 v[60:63], a[144:147], v[220:223], 0\n\t"
    "v_exp_f32_e32 v19, v19\n\t"
    "v_mfma_f32_16x16x32_bf16 v[60:63], a[152:155], v[216:219], v[60:63]\n\t"
    "v_add_f32_e32 v55, v55, v19\n\t"
    "v_exp_f32_e32 v20, v20\n\t"
    "v_mfma_f32_16x16x32_bf16 v[60:63], a[160:163], v[212:215], v[60:63]\n\t"
    "s_add_u32 m0, s63, 9216\n\t"
    "v_add_f32_e32 v54, v54, v20\n\t"
    "v_mfma_f32_16x16x32_bf16 v[60:63], a[168:171], v[28:31], v[60:63]\n\t"
    "buffer_load_dwordx4 v7, s[52:55], s61 offen offset:0 nt lds\n\t"
    "v_cvt_pk_bf16_f32 v50, v36, v37\n\t"
    "v_mfma_f32_16x16x32_bf16 v[60:63], a[176:179], v[32:35], v[60:63]\n\t"
    "v_cvt_pk_bf16_f32 v51, v38, v39\n\t"
    "v_sub_f32_e32 v21, v21, v52\n\t"
    "v_mfma_f32_16x16x32_bf16 v[60:63], a[184:187], v[40:43], v[60:63]\n\t"
    "buffer_load_dwordx4 v7, s[52:55], s61 offen offset:1024 nt lds\n\t"
    "v_sub_f32_e32 v22, v22, v52\n\t"
    "v_mfma_f32_16x16x32_bf16 v[64:67], v[220:223], v[44:47], 0\n\t"
    "v_exp_f32_e32 v21, v21\n\t"
    "v_mfma_f32_16x16x32_bf16 v[64:67], v[216:219], v[56:59], v[64:67]\n\t"
    "buffer_load_dwordx4 v7, s[52:55], s61 offen offset:2048 nt lds\n\t"
    "v_add_f32_e32 v54, v54, v21\n\t"
    "v_mfma_f32_16x16x32_bf16 v[64:67], v[212:215], v[48:51], v[64:67]\n\t"
    "s_add_u32 s61, s61, 196608\n\t"
    "s_waitcnt lgkmcnt(0)\n\t"
    "v_mfma_f32_16x16x32_bf16 v[36:39], v[188:191], v[48:51], 0\n\t"
    "v_exp_f32_e32 v22, v22\n\t"
    "v_mfma_f32_16x16x32_bf16 v[36:39], v[192:195], v[56:59], v[36:39]\n\t"
    "v_add_f32_e32 v55, v55, v22\n\t"
    "v_sub_f32_e32 v23, v23, v52\n\t"
    "v_mfma_f32_16x16x32_bf16 v[36:39], v[196:199], v[44:47], v[36:39]\n\t"
    "ds_read_b128 v[184:187], v3 offset:6144\n\t"
    "v_sub_f32_e32 v24, v24, v52\n\t"
    "v_mfma_f32_16x16x32_bf16 v[68:71], v[172:175], v[44:47], 0\n\t"
    "v_exp_f32_e32 v23, v23\n\t"
    "v_mfma_f32_16x16x32_bf16 v[68:71], v[168:171], v[56:59], v[68:71]\n\t"
    "ds_read_b128 v[180:183], v4 offset:6144\n\t"
    "v_add_f32_e32 v55, v55, v23\n\t"
    "v_mfma_f32_16x16x32_bf16 v[68:71], v[164:167], v[48:51], v[68:71]\n\t"
    "v_exp_f32_e32 v24, v24\n\t"
    "v_mfma_f32_16x16x32_bf16 v[72:75], v[140:143], v[48:51], 0\n\t"
    "ds_read_b128 v[176:179], v5 offset:6144\n\t"
    "v_add_f32_e32 v54, v54, v24\n\t"
    "v_mfma_f32_16x16x32_bf16 v[72:75], v[144:147], v[56:59], v[72:75]\n\t"
    "v_sub_f32_e32 v25, v25, v52\n\t"
    "v_sub_f32_e32 v26, v26, v52\n\t"
    "v_mfma_f32_16x16x32_bf16 v[72:75], v[148:151], v[44:47], v[72:75]\n\t"
    "ds_read_b128 v[208:211], v3 offset:0\n\t"
    "v_sub_f32_e32 v27, v27, v52\n\t"
    "v_mfma_f32_16x16x32_bf16 v[48:51], a[188:191], v[40:43], 0\n\t"
    "v_exp_f32_e32 v25, v25\n\t"
    "v_mfma_f32_16x16x32_bf16 v[48:51], a[180:183], v[32:35], v[48:51]\n\t"
    ".Lh5_%=:\n\t"
    "s_cbranch_execz .LrM_%=\n\t"
    "ds_read_b128 v[204:207], v4 offset:0\n\t"
    "v_add_f32_e32 v54, v54, v25\n\t"
    "v_mfma_f32_16x16x32_bf16 v[48:51], a[172:175], v[28:31], v[48:51]\n\t"
    "v_exp_f32_e32 v26, v26\n\t"
    "v_mfma_f32_16x16x32_bf16 v[48:51], a[164:167], v[212:215], v[48:51]\n\t"
    "ds_read_b128 v[200:203], v5 offset:0\n\t"
    "v_add_f32_e32 v55, v55, v26\n\t"
    "v_mfma_f32_16x16x32_bf16 v[48:51], a[156:159], v[216:219], v[48:51]\n\t"
    "v_exp_f32_e32 v27, v27\n\t"
    "v_mfma_f32_16x16x32_bf16 v[48:51], a[148:151], v[220:223], v[48:51]\n\t"
    "ds_read_b128 v[160:163], v3 offset:12288\n\t"
    "v_add_f32_e32 v55, v55, v27\n\t"
    "v_mfma_f32_16x16x32_bf16 v[44:47], a[192:195], v[220:223], 0\n\t"
    "v_add_f32_e32 v54, v54, v55\n\t"
    "v_mov_b32_e32 v55, v54\n\t"
    "v_mfma_f32_16x16x32_bf16 v[44:47], a[200:203], v[216:219], v[44:47]\n\t"
    "ds_read_b128 v[156:159], v4 offset:12288\n\t"
    "v_cvt_pk_bf16_f32 v56, v60, v61\n\t"
    "v_mfma_f32_16x16x32_bf16 v[44:47], a[208:211], v[212:215], v[44:47]\n\t"
    "v_permlane32_swap_b32 v54, v55\n\t"
    "v_add_f32_e32 v54, v54, v55\n\t"
    "v_mfma_f32_16x16x32_bf16 v[44:47], a[216:219], v[28:31], v[44:47]\n\t"
    "ds_read_b128 v[152:155], v5 offset:12288\n\t"
    "v_cvt_pk_bf16_f32 v58, v48, v49\n\t"
    "v_mfma_f32_16x16x32_bf16 v[44:47], a[224:227], v[32:35], v[44:47]\n\t"
    "v_cvt_pk_bf16_f32 v59, v50, v51\n\t"
    "v_mov_b32_e32 v55, v54\n\t"
    "v_mfma_f32_16x16x32_bf16 v[44:47], a[232:235], v[40:43], v[44:47]\n\t"
    "ds_read_b128 v[128:131], v5 offset:18432\n\t"
    "v_cvt_pk_bf16_f32 v57, v62, v63\n\t"
    "v_mfma_f32_16x16x32_bf16 v[76:79], a[236:239], v[40:43], 0\n\t"
    "v_permlane16_swap_b32 v54, v55\n\t"
    "v_add_f32_e32 v54, v54, v55\n\t"
    "v_mfma_f32_16x16x32_bf16 v[76:79], a[228:231], v[32:35], v[76:79]\n\t"
    "ds_read_b128 v[132:135], v4 offset:18432\n\t"
    "v_mfma_f32_16x16x32_bf16 v[76:79], a[220:223], v[28:31], v[76:79]\n\t"
    "v_rcp_f32_e32 v54, v54\n\t"
    "v_mfma_f32_16x16x32_bf16 v[76:79], a[212:215], v[212:215], v[76:79]\n\t"
    "ds_read_b128 v[136:139], v3 offset:18432\n\t"
    "v_cvt_pk_bf16_f32 v48, v44, v45\n\t"
    "v_mfma_f32_16x16x32_bf16 v[76:79], a[204:207], v[216:219], v[76:79]\n\t"
    "v_cvt_pk_bf16_f32 v49, v46, v47\n\t"
    "v_mul_f32_e32 v12, v12, v54\n\t"
    "v_mfma_f32_16x16x32_bf16 v[76:79], a[196:199], v[220:223], v[76:79]\n\t"
    "v_mul_f32_e32 v13, v13, v54\n\t"
    "v_cvt_pk_bf16_f32 v60, v12, v13\n\t"
    "v_mfma_f32_16x16x32_bf16 v[80:83], a[240:243], v[220:223], 0\n\t"
    "v_mul_f32_e32 v14, v14, v54\n\t"
    "v_mul_f32_e32 v15, v15, v54\n\t"
    "v_mfma_f32_16x16x32_bf16 v[80:83], a[248:251], v[216:219], v[80:83]\n\t"
    "v_cvt_pk_bf16_f32 v61, v14, v15\n\t"
    "v_mul_f32_e32 v16, v16, v54\n\t"
    "v_mfma_f32_16x16x32_bf16 v[80:83], v[224:227], v[212:215], v[80:83]\n\t"
    "v_mul_f32_e32 v17, v17, v54\n\t"
    "v_cvt_pk_bf16_f32 v62, v16, v17\n\t"
    "v_mfma_f32_16x16x32_bf16 v[80:83], v[232:235], v[28:31], v[80:83]\n\t"
    "v_cvt_pk_bf16_f32 v50, v76, v77\n\t"
    "v_cvt_pk_bf16_f32 v51, v78, v79\n\t"
    "v_mfma_f32_16x16x32_bf16 v[80:83], v[240:243], v[32:35], v[80:83]\n\t"
    "v_mul_f32_e32 v18, v18, v54\n\t"
    "v_mul_f32_e32 v19, v19, v54\n\t"
    "v_mfma_f32_16x16x32_bf16 v[80:83], v[248:251], v[40:43], v[80:83]\n\t"
    "v_cvt_pk_bf16_f32 v63, v18, v19\n\t"
    "v_mul_f32_e32 v20, v20, v54\n\t"
    "v_mfma_f32_16x16x32_bf16 v[12:15], v[252:255], v[40:43], 0\n\t"
    "v_mul_f32_e32 v21, v21, v54\n\t"
    "v_cvt_pk_bf16_f32 v44, v20, v21\n\t"
    "v_mfma_f32_16x16x32_bf16 v[12:15], v[244:247], v[32:35], v[12:15]\n\t"
    "v_mul_f32_e32 v22, v22, v54\n\t"
    "v_mul_f32_e32 v23, v23, v54\n\t"
    "v_mfma_f32_16x16x32_bf16 v[12:15], v[236:239], v[28:31], v[12:15]\n\t"
    "v_cvt_pk_bf16_f32 v45, v22, v23\n\t"
    "v_mul_f32_e32 v24, v24, v54\n\t"
    "v_mfma_f32_16x16x32_bf16 v[12:15], v[228:231], v[212:215], v[12:15]\n\t"
    "v_cvt_pk_bf16_f32 v16, v80, v81\n\t"
    "v_cvt_pk_bf16_f32 v17, v82, v83\n\t"
    "v_mfma_f32_16x16x32_bf16 v[12:15], a[252:255], v[216:219], v[12:15]\n\t"
    "v_mul_f32_e32 v25, v25, v54\n\t"
    "v_cvt_pk_bf16_f32 v46, v24, v25\n\t"
    "v_mfma_f32_16x16x32_bf16 v[12:15], a[244:247], v[220:223], v[12:15]\n\t"
    "v_mul_f32_e32 v26, v26, v54\n\t"
    "v_mul_f32_e32 v27, v27, v54\n\t"
    "v_mfma_f32_16x16x32_bf16 v[20:23], a[0:3], v[196:199], 0\n\t"
    "ds_write_b128 v6, v[60:63] offset:6144\n\t"
    "v_mfma_f32_16x16x32_bf16 v[20:23], a[8:11], v[192:195], v[20:23]\n\t"
    "v_cvt_pk_bf16_f32 v47, v26, v27\n\t"
    "v_mfma_f32_16x16x32_bf16 v[20:23], a[16:19], v[188:191], v[20:23]\n\t"
    "s_add_u32 m0, s63, 12288\n\t"
    "s_waitcnt lgkmcnt(1)\n\t"
    "v_mfma_f32_16x16x32_bf16 v[20:23], a[24:27], v[184:187], v[20:23]\n\t"
    "buffer_load_dwordx4 v7, s[52:55], s61 offen offset:0 nt lds\n\t"
    "v_cvt_pk_bf16_f32 v18, v12, v13\n\t"
    "v_mfma_f32_16x16x32_bf16 v[20:23], a[32:35], v[180:183], v[20:23]\n\t"
    "ds_write_b128 v6, v[44:47] offset:7168\n\t"
    "v_mfma_f32_16x16x32_bf16 v[20:23], a[40:43], v[176:179], v[20:23]\n\t"
    "buffer_load_dwordx4 v7, s[52:55], s61 offen offset:1024 nt lds\n\t"
    "v_cvt_pk_bf16_f32 v19, v14, v15\n\t"
    "v_mfma_f32_16x16x32_bf16 v[64:67], v[208:211], v[56:59], v[64:67]\n\t"
    "v_mfma_f32_16x16x32_bf16 v[64:67], v[204:207], v[48:51], v[64:67]\n\t"
    "buffer_load_dwordx4 v7, s[52:55], s61 offen offset:2048 nt lds\n\t"
    "v_mfma_f32_16x16x32_bf16 v[64:67], v[200:203], v[16:19], v[64:67]\n\t"
    "s_add_u32 s61, s61, 196608\n\t"
    "v_mfma_f32_16x16x32_bf16 v[36:39], v[176:179], v[16:19], v[36:39]\n\t"
    "v_cvt_pk_bf16_f32 v12, v20, v21\n\t"
    "v_cvt_pk_bf16_f32 v13, v22, v23\n\t"
    "v_mfma_f32_16x16x32_bf16 v[36:39], v[180:183], v[48:51], v[36:39]\n\t"
    "s_waitcnt lgkmcnt(0)\n\t"
    "s_barrier\n\t"
    "v_mfma_f32_16x16x32_bf16 v[36:39], v[184:187], v[56:59], v[36:39]\n\t"
    "v_mfma_f32_16x16x32_bf16 v[68:71], v[160:163], v[56:59], v[68:71]\n\t"
    "v_max3_f32 v20, v64, v65, v66\n\t"
    "v_mfma_f32_16x16x32_bf16 v[68:71], v[156:159], v[48:51], v[68:71]\n\t"
    "v_mfma_f32_16x16x32_bf16 v[68:71], v[152:155], v[16:19], v[68:71]\n\t"
    "v_mfma_f32_16x16x32_bf16 v[72:75], v[128:131], v[16:19], v[72:75]\n\t"
    "v_max3_f32 v21, v67, v36, v37\n\t"
    "v_max3_f32 v20, v20, v38, v39\n\t"
    "v_mfma_f32_16x16x32_bf16 v[72:75], v[132:135], v[48:51], v[72:75]\n\t"
    "v_mfma_f32_16x16x32_bf16 v[72:75], v[136:139], v[56:59], v[72:75]\n\t"
    "v_mfma_f32_16x16x32_bf16 v[16:19], a[44:47], v[176:179], 0\n\t"
    "v_max3_f32 v21, v21, v68, v69\n\t"
    "v_max3_f32 v20, v20, v70, v71\n\t"
    "v_mfma_f32_16x16x32_bf16 v[16:19], a[36:39], v[180:183], v[16:19]\n\t"
    "v_mfma_f32_16x16x32_bf16 v[16:19], a[28:31], v[184:187], v[16:19]\n\t"
    "v_mfma_f32_16x16x32_bf16 v[16:19], a[20:23], v[188:191], v[16:19]\n\t"
    "v_max3_f32 v21, v21, v72, v73\n\t"
    "v_max3_f32 v20, v20, v74, v75\n\t"
    "v_mfma_f32_16x16x32_bf16 v[16:19], a[12:15], v[192:195], v[16:19]\n\t"
    "v_max_f32_e32 v20, v20, v21\n\t"
    "v_mov_b32_e32 v21, v20\n\t"
    "v_mfma_f32_16x16x32_bf16 v[16:19], a[4:7], v[196:199], v[16:19]\n\t"
    "v_mfma_f32_16x16x32_bf16 v[24:27], a[48:51], v[196:199], 0\n\t"
    "v_permlane32_swap_b32 v20, v21\n\t"
    "v_max_f32_e32 v20, v20, v21\n\t"
    "v_mfma_f32_16x16x32_bf16 v[24:27], a[56:59], v[192:195], v[24:27]\n\t"
    "v_mov_b32_e32 v21, v20\n\t"
    "v_mfma_f32_16x16x32_bf16 v[24:27], a[64:67], v[188:191], v[24:27]\n\t"
    "v_mfma_f32_16x16x32_bf16 v[24:27], a[72:75], v[184:187], v[24:27]\n\t"
    "v_cvt_pk_bf16_f32 v14, v16, v17\n\t"
    "v_cvt_pk_bf16_f32 v15, v18, v19\n\t"
    "v_mfma_f32_16x16x32_bf16 v[24:27], a[80:83], v[180:183], v[24:27]\n\t"
    "v_permlane16_swap_b32 v20, v21\n\t"
    "v_max_f32_e32 v20, v20, v21\n\t"
    "v_mfma_f32_16x16x32_bf16 v[24:27], a[88:91], v[176:179], v[24:27]\n\t"
    "v_sub_f32_e32 v64, v64, v20\n\t"
    "v_sub_f32_e32 v65, v65, v20\n\t"
    "v_mfma_f32_16x16x32_bf16 v[16:19], a[92:95], v[176:179], 0\n\t"
    "v_exp_f32_e32 v64, v64\n\t"
    "v_mfma_f32_16x16x32_bf16 v[16:19], a[84:87], v[180:183], v[16:19]\n\t"
    "v_exp_f32_e32 v65, v65\n\t"
    "v_mfma_f32_16x16x32_bf16 v[16:19], a[76:79], v[184:187], v[16:19]\n\t"
    "v_add_f32_e32 v22, v64, v65\n\t"
    "v_sub_f32_e32 v66, v66, v20\n\t"
    "v_mfma_f32_16x16x32_bf16 v[16:19], a[68:71], v[188:191], v[16:19]\n\t"
    "v_cvt_pk_bf16_f32 v28, v24, v25\n\t"
    "v_cvt_pk_bf16_f32 v29, v26, v27\n\t"
    "v_mfma_f32_16x16x32_bf16 v[16:19], a[60:63], v[192:195], v[16:19]\n\t"
    "v_exp_f32_e32 v66, v66\n\t"
    "v_mfma_f32_16x16x32_bf16 v[16:19], a[52:55], v[196:199], v[16:19]\n\t"
    "v_sub_f32_e32 v67, v67, v20\n\t"
    "v_sub_f32_e32 v36, v36, v20\n\t"
    "v_mfma_f32_16x16x32_bf16 v[24:27], a[96:99], v[196:199], 0\n\t"
    "v_exp_f32_e32 v67, v67\n\t"
    "v_mfma_f32_16x16x32_bf16 v[24:27], a[104:107], v[192:195], v[24:27]\n\t"
    "v_add_f32_e32 v23, v66, v67\n\t"
    "v_sub_f32_e32 v37, v37, v20\n\t"
    "v_mfma_f32_16x16x32_bf16 v[24:27], a[112:115], v[188:191], v[24:27]\n\t"
    "v_exp_f32_e32 v36, v36\n\t"
    "v_mfma_f32_16x16x32_bf16 v[24:27], a[120:123], v[184:187], v[24:27]\n\t"
    "v_cvt_pk_bf16_f32 v30, v16, v17\n\t"
    "v_cvt_pk_bf16_f32 v31, v18, v19\n\t"
    "v_mfma_f32_16x16x32_bf16 v[24:27], a[128:131], v[180:183], v[24:27]\n\t"
    "v_add_f32_e32 v22, v22, v36\n\t"
    "v_sub_f32_e32 v38, v38, v20\n\t"
    "v_mfma_f32_16x16x32_bf16 v[24:27], a[136:139], v[176:179], v[24:27]\n\t"
    "v_exp_f32_e32 v37, v37\n\t"
    "v_mfma_f32_16x16x32_bf16 v[16:19], a[140:143], v[176:179], 0\n\t"
    "v_add_f32_e32 v22, v22, v37\n\t"
    "v_sub_f32_e32 v39, v39, v20\n\t"
    "v_mfma_f32_16x16x32_bf16 v[16:19], a[132:135], v[180:183], v[16:19]\n\t"
    "v_exp_f32_e32 v38, v38\n\t"
    "v_mfma_f32_16x16x32_bf16 v[16:19], a[124:127], v[184:187], v[16:19]\n\t"
    "v_add_f32_e32 v23, v23, v38\n\t"
    "v_sub_f32_e32 v68, v68, v20\n\t"
    "v_mfma_f32_16x16x32_bf16 v[16:19], a[116:119], v[188:191], v[16:19]\n\t"
    "v_cvt_pk_bf16_f32 v32, v24, v25\n\t"
    "v_cvt_pk_bf16_f32 v33, v26, v27\n\t"
    "v_mfma_f32_16x16x32_bf16 v[16:19], a[108:111], v[192:195], v[16:19]\n\t"
    "v_exp_f32_e32 v39, v39\n\t"
    "v_mfma_f32_16x16x32_bf16 v[16:19], a[100:103], v[196:199], v[16:19]\n\t"
    "v_add_f32_e32 v23, v23, v39\n\t"
    "v_sub_f32_e32 v69, v69, v20\n\t"
    "v_mfma_f32_16x16x32_bf16 v[24:27], a[144:147], v[196:199], 0\n\t"
    "v_exp_f32_e32 v68, v68\n\t"
    "v_mfma_f32_16x16x32_bf16 v[24:27], a[152:155], v[192:195], v[24:27]\n\t"
    "v_add_f32_e32 v22, v22, v68\n\t"
    "v_sub_f32_e32 v70, v70, v20\n\t"
    "v_mfma_f32_16x16x32_bf16 v[24:27], a[160:163], v[188:191], v[24:27]\n\t"
    "s_add_u32 m0, s63, 15360\n\t"
    "v_sub_f32_e32 v71, v71, v20\n\t"
    "v_mfma_f32_16x16x32_bf16 v[24:27], a[168:171], v[184:187], v[24:27]\n\t"
    "buffer_load_dwordx4 v7, s[52:55], s61 offen offset:0 nt lds\n\t"
    "v_cvt_pk_bf16_f32 v34, v16, v17\n\t"
    "v_mfma_f32_16x16x32_bf16 v[24:27], a[176:179], v[180:183], v[24:27]\n\t"
    "v_cvt_pk_bf16_f32 v35, v18, v19\n\t"
    "v_sub_f32_e32 v72, v72, v20\n\t"
    "v_mfma_f32_16x16x32_bf16 v[24:27], a[184:187], v[176:179], v[24:27]\n\t"
    "buffer_load_dwordx4 v7, s[52:55], s61 offen offset:1024 nt lds\n\t"
    "v_sub_f32_e32 v73, v73, v20\n\t"
    "v_mfma_f32_16x16x32_bf16 v[40:43], v[220:223], v[12:15], 0\n\t"
    "v_exp_f32_e32 v69, v69\n\t"
    "v_mfma_f32_16x16x32_bf16 v[40:43], v[216:219], v[28:31], v[40:43]\n\t"
    "buffer_load_dwordx4 v7, s[52:55], s61 offen offset:2048 nt lds\n\t"
    "v_add_f32_e32 v22, v22, v69\n\t"
    "v_mfma_f32_16x16x32_bf16 v[40:43], v[212:215], v[32:35], v[40:43]\n\t"
    "s_add_u32 s61, s61, 196608\n\t"
    "v_sub_f32_e32 v74, v74, v20\n\t"
    "v_mfma_f32_16x16x32_bf16 v[16:19], v[188:191], v[32:35], 0\n\t"
    "v_exp_f32_e32 v70, v70\n\t"
    "v_mfma_f32_16x16x32_bf16 v[16:19], v[192:195], v[28:31], v[16:19]\n\t"
    "v_add_f32_e32 v23, v23, v70\n\t"
    "v_sub_f32_e32 v75, v75, v20\n\t"
    "v_mfma_f32_16x16x32_bf16 v[16:19], v[196:199], v[12:15], v[16:19]\n\t"
    "v_exp_f32_e32 v71, v71\n\t"
    "v_mfma_f32_16x16x32_bf16 v[44:47], v[172:175], v[12:15], 0\n\t"
    "v_add_f32_e32 v23, v23, v71\n\t"
    "v_cvt_pk_bf16_f32 v48, v24, v25\n\t"
    "v_mfma_f32_16x16x32_bf16 v[44:47], v[168:171], v[28:31], v[44:47]\n\t"
    "v_exp_f32_e32 v72, v72\n\t"
    "v_mfma_f32_16x16x32_bf16 v[44:47], v[164:167], v[32:35], v[44:47]\n\t"
    "v_add_f32_e32 v22, v22, v72\n\t"
    "v_cvt_pk_bf16_f32 v49, v26, v27\n\t"
    "v_mfma_f32_16x16x32_bf16 v[52:55], v[140:143], v[32:35], 0\n\t"
    "v_exp_f32_e32 v73, v73\n\t"
    "v_mfma_f32_16x16x32_bf16 v[52:55], v[144:147], v[28:31], v[52:55]\n\t"
    "v_add_f32_e32 v22, v22, v73\n\t"
    "v_mfma_f32_16x16x32_bf16 v[52:55], v[148:151], v[12:15], v[52:55]\n\t"
    "v_exp_f32_e32 v74, v74\n\t"
    "v_mfma_f32_16x16x32_bf16 v[24:27], a[188:191], v[176:179], 0\n\t"
    "v_add_f32_e32 v23, v23, v74\n\t"
    "v_mfma_f32_16x16x32_bf16 v[24:27], a[180:183], v[180:183], v[24:27]\n\t"
    "v_exp_f32_e32 v75, v75\n\t"
    "v_mfma_f32_16x16x32_bf16 v[24:27], a[172:175], v[184:187], v[24:27]\n\t"
    "v_add_f32_e32 v23, v23, v75\n\t"
    "v_add_f32_e32 v22, v22, v23\n\t"
    "v_mfma_f32_16x16x32_bf16 v[24:27], a[164:167], v[188:191], v[24:27]\n\t"
    "v_mov_b32_e32 v23, v22\n\t"
    "v_mfma_f32_16x16x32_bf16 v[24:27], a[156:159], v[192:195], v[24:27]\n\t"
    "v_mfma_f32_16x16x32_bf16 v[24:27], a[148:151], v[196:199], v[24:27]\n\t"
    "v_permlane32_swap_b32 v22, v23\n\t"
    "v_add_f32_e32 v22, v22, v23\n\t"
    "v_mfma_f32_16x16x32_bf16 v[12:15], a[192:195], v[196:199], 0\n\t"
    "v_mov_b32_e32 v23, v22\n\t"
    "v_mfma_f32_16x16x32_bf16 v[12:15], a[200:203], v[192:195], v[12:15]\n\t"
    "v_mfma_f32_16x16x32_bf16 v[12:15], a[208:211], v[188:191], v[12:15]\n\t"
    "v_permlane16_swap_b32 v22, v23\n\t"
    "v_add_f32_e32 v22, v22, v23\n\t"
    "v_mfma_f32_16x16x32_bf16 v[12:15], a[216:219], v[184:187], v[12:15]\n\t"
    "v_cvt_pk_bf16_f32 v50, v24, v25\n\t"
    "v_cvt_pk_bf16_f32 v51, v26, v27\n\t"
    "v_mfma_f32_16x16x32_bf16 v[12:15], a[224:227], v[180:183], v[12:15]\n\t"
    "v_rcp_f32_e32 v22, v22\n\t"
    "v_mfma_f32_16x16x32_bf16 v[12:15], a[232:235], v[176:179], v[12:15]\n\t"
    "v_mul_f32_e32 v64, v64, v22\n\t"
    "v_mul_f32_e32 v65, v65, v22\n\t"
    "v_mfma_f32_16x16x32_bf16 v[28:31], a[236:239], v[176:179], 0\n\t"
    "v_cvt_pk_bf16_f32 v24, v64, v65\n\t"
    "v_mul_f32_e32 v66, v66, v22\n\t"
    "v_mfma_f32_16x16x32_bf16 v[28:31], a[228:231], v[180:183], v[28:31]\n\t"
    "v_mul_f32_e32 v67, v67, v22\n\t"
    "v_cvt_pk_bf16_f32 v25, v66, v67\n\t"
    "v_mfma_f32_16x16x32_bf16 v[28:31], a[220:223], v[184:187], v[28:31]\n\t"
    "v_mul_f32_e32 v36, v36, v22\n\t"
    "v_mul_f32_e32 v37, v37, v22\n\t"
    "v_mfma_f32_16x16x32_bf16 v[28:31], a[212:215], v[188:191], v[28:31]\n\t"
    "v_cvt_pk_bf16_f32 v32, v12, v13\n\t"
    "v_cvt_pk_bf16_f32 v33, v14, v15\n\t"
    "v_mfma_f32_16x16x32_bf16 v[28:31], a[204:207], v[192:195], v[28:31]\n\t"
    "v_cvt_pk_bf16_f32 v26, v36, v37\n\t"
    "v_mul_f32_e32 v38, v38, v22\n\t"
    "v_mfma_f32_16x16x32_bf16 v[28:31], a[196:199], v[196:199], v[28:31]\n\t"
    "v_mul_f32_e32 v39, v39, v22\n\t"
    "v_cvt_pk_bf16_f32 v27, v38, v39\n\t"
    "v_mfma_f32_16x16x32_bf16 v[12:15], a[240:243], v[196:199], 0\n\t"
    "v_mul_f32_e32 v68, v68, v22\n\t"
    "v_mul_f32_e32 v69, v69, v22\n\t"
    "v_mfma_f32_16x16x32_bf16 v[12:15], a[248:251], v[192:195], v[12:15]\n\t"
    "v_cvt_pk_bf16_f32 v56, v68, v69\n\t"
    "v_mul_f32_e32 v70, v70, v22\n\t"
    "v_mfma_f32_16x16x32_bf16 v[12:15], v[224:227], v[188:191], v[12:15]\n\t"
    "v_mul_f32_e32 v71, v71, v22\n\t"
    "v_cvt_pk_bf16_f32 v57, v70, v71\n\t"
    "v_mfma_f32_16x16x32_bf16 v[12:15], v[232:235], v[184:187], v[12:15]\n\t"
    "v_cvt_pk_bf16_f32 v34, v28, v29\n\t"
    "v_cvt_pk_bf16_f32 v35, v30, v31\n\t"
    "v_mfma_f32_16x16x32_bf16 v[12:15], v[240:243], v[180:183], v[12:15]\n\t"
    "v_mul_f32_e32 v72, v72, v22\n\t"
    "v_mul_f32_e32 v73, v73, v22\n\t"
    "v_mfma_f32_16x16x32_bf16 v[12:15], v[248:251], v[176:179], v[12:15]\n\t"
    "v_cvt_pk_bf16_f32 v58, v72, v73\n\t"
    "v_mul_f32_e32 v74, v74, v22\n\t"
    "v_mfma_f32_16x16x32_bf16 v[28:31], v[252:255], v[176:179], 0\n\t"
    "v_mul_f32_e32 v75, v75, v22\n\t"
    "v_cvt_pk_bf16_f32 v59, v74, v75\n\t"
    "v_mfma_f32_16x16x32_bf16 v[28:31], v[244:247], v[180:183], v[28:31]\n\t"
    "v_mfma_f32_16x16x32_bf16 v[28:31], v[236:239], v[184:187], v[28:31]\n\t"
    "v_mfma_f32_16x16x32_bf16 v[28:31], v[228:231], v[188:191], v[28:31]\n\t"
    "v_cvt_pk_bf16_f32 v20, v12, v13\n\t"
    "v_cvt_pk_bf16_f32 v21, v14, v15\n\t"
    "v_mfma_f32_16x16x32_bf16 v[28:31], a[252:255], v[192:195], v[28:31]\n\t"
    "v_mfma_f32_16x16x32_bf16 v[28:31], a[244:247], v[196:199], v[28:31]\n\t"
    "v_mfma_f32_16x16x32_bf16 v[12:15], a[0:3], v[172:175], 0\n\t"
    "ds_write_b128 v6, v[24:27] offset:0\n\t"
    "v_mfma_f32_16x16x32_bf16 v[12:15], a[8:11], v[168:171], v[12:15]\n\t"
    "v_mfma_f32_16x16x32_bf16 v[12:15], a[16:19], v[164:167], v[12:15]\n\t"
    "s_add_u32 m0, s63, 18432\n\t"
    "v_mfma_f32_16x16x32_bf16 v[12:15], a[24:27], v[160:163], v[12:15]\n\t"
    "buffer_load_dwordx4 v7, s[52:55], s61 offen offset:0 nt lds\n\t"
    "v_cvt_pk_bf16_f32 v22, v28, v29\n\t"
    "v_mfma_f32_16x16x32_bf16 v[12:15], a[32:35], v[156:159], v[12:15]\n\t"
    "ds_write_b128 v6, v[56:59] offset:1024\n\t"
    "v_mfma_f32_16x16x32_bf16 v[12:15], a[40:43], v[152:155], v[12:15]\n\t"
    "buffer_load_dwordx4 v7, s[52:55], s61 offen offset:1024 nt lds\n\t"
    "v_cvt_pk_bf16_f32 v23, v30, v31\n\t"
    "v_mfma_f32_16x16x32_bf16 v[40:43], v[208:211], v[48:51], v[40:43]\n\t"
    "v_mfma_f32_16x16x32_bf16 v[40:43], v[204:207], v[32:35], v[40:43]\n\t"
    "buffer_load_dwordx4 v7, s[52:55], s61 offen offset:2048 nt lds\n\t"
    "v_mfma_f32_16x16x32_bf16 v[40:43], v[200:203], v[20:23], v[40:43]\n\t"
    "s_add_u32 s61, s61, 196608\n\t"
    "s_add_i32 s64, s62, 24576\n\t"
    "v_mfma_f32_16x16x32_bf16 v[16:19], v[176:179], v[20:23], v[16:19]\n\t"
    "s_cmp_eq_u32 s64, %[rend]\n\t"
    "s_cselect_b32 s64, %[rbeg], s64\n\t"
    "v_mfma_f32_16x16x32_bf16 v[16:19], v[180:183], v[32:35], v[16:19]\n\t"
    "s_sub_i32 s65, s63, s64\n\t"
    "v_cvt_pk_bf16_f32 v24, v12, v13\n\t"
    "v_mfma_f32_16x16x32_bf16 v[16:19], v[184:187], v[48:51], v[16:19]\n\t"
    "v_add_u32_e32 v0, s65, v0\n\t"
    "v_add_u32_e32 v1, s65, v1\n\t"
    "v_mfma_f32_16x16x32_bf16 v[44:47], v[160:163], v[48:51], v[44:47]\n\t"
    "v_add_u32_e32 v2, s65, v2\n\t"
    "v_add_u32_e32 v3, s65, v3\n\t"
    "v_mfma_f32_16x16x32_bf16 v[44:47], v[156:159], v[32:35], v[44:47]\n\t"
    "v_add_u32_e32 v4, s65, v4\n\t"
    "v_add_u32_e32 v5, s65, v5\n\t"
    "v_mfma_f32_16x16x32_bf16 v[44:47], v[152:155], v[20:23], v[44:47]\n\t"
    "v_max3_f32 v28, v40, v41, v42\n\t"
    "v_cvt_pk_bf16_f32 v25, v14, v15\n\t"
    "v_mfma_f32_16x16x32_bf16 v[52:55], v[128:131], v[20:23], v[52:55]\n\t"
    "v_max3_f32 v29, v43, v16, v17\n\t"
    "v_max3_f32 v28, v28, v18, v19\n\t"
    "v_mfma_f32_16x16x32_bf16 v[52:55], v[132:135], v[32:35], v[52:55]\n\t"
    "v_mfma_f32_16x16x32_bf16 v[52:55], v[136:139], v[48:51], v[52:55]\n\t"
    "v_mfma_f32_16x16x32_bf16 v[12:15], a[44:47], v[152:155], 0\n\t"
    "v_max3_f32 v29, v29, v44, v45\n\t"
    "v_max3_f32 v28, v28, v46, v47\n\t"
    "v_mfma_f32_16x16x32_bf16 v[12:15], a[36:39], v[156:159], v[12:15]\n\t"
    "v_mfma_f32_16x16x32_bf16 v[12:15], a[28:31], v[160:163], v[12:15]\n\t"
    "v_mfma_f32_16x16x32_bf16 v[12:15], a[20:23], v[164:167], v[12:15]\n\t"
    "v_max3_f32 v29, v29, v52, v53\n\t"
    "v_max3_f32 v28, v28, v54, v55\n\t"
    "v_mfma_f32_16x16x32_bf16 v[12:15], a[12:15], v[168:171], v[12:15]\n\t"
    "v_max_f32_e32 v28, v28, v29\n\t"
    "v_mov_b32_e32 v29, v28\n\t"
    "v_mfma_f32_16x16x32_bf16 v[12:15], a[4:7], v[172:175], v[12:15]\n\t"
    "v_mfma_f32_16x16x32_bf16 v[20:23], a[48:51], v[172:175], 0\n\t"
    "v_permlane32_swap_b32 v28, v29\n\t"
    "v_max_f32_e32 v28, v28, v29\n\t"
    "v_mfma_f32_16x16x32_bf16 v[20:23], a[56:59], v[168:171], v[20:23]\n\t"
    "v_mov_b32_e32 v29, v28\n\t"
    "v_mfma_f32_16x16x32_bf16 v[20:23], a[64:67], v[164:167], v[20:23]\n\t"
    "v_mfma_f32_16x16x32_bf16 v[20:23], a[72:75], v[160:163], v[20:23]\n\t"
    "v_cvt_pk_bf16_f32 v26, v12, v13\n\t"
    "v_cvt_pk_bf16_f32 v27, v14, v15\n\t"
    "v_mfma_f32_16x16x32_bf16 v[20:23], a[80:83], v[156:159], v[20:23]\n\t"
    "v_permlane16_swap_b32 v28, v29\n\t"
    "v_max_f32_e32 v28, v28, v29\n\t"
    "v_mfma_f32_16x16x32_bf16 v[20:23], a[88:91], v[152:155], v[20:23]\n\t"
    "v_sub_f32_e32 v40, v40, v28\n\t"
    "v_sub_f32_e32 v41, v41, v28\n\t"
    "v_mfma_f32_16x16x32_bf16 v[12:15], a[92:95], v[152:155], 0\n\t"
    "v_exp_f32_e32 v40, v40\n\t"
    "v_mfma_f32_16x16x32_bf16 v[12:15], a[84:87], v[156:159], v[12:15]\n\t"
    "v_exp_f32_e32 v41, v41\n\t"
    "v_mfma_f32_16x16x32_bf16 v[12:15], a[76:79], v[160:163], v[12:15]\n\t"
    "v_add_f32_e32 v30, v40, v41\n\t"
    "v_sub_f32_e32 v42, v42, v28\n\t"
    "v_mfma_f32_16x16x32_bf16 v[12:15], a[68:71], v[164:167], v[12:15]\n\t"
    "v_cvt_pk_bf16_f32 v32, v20, v21\n\t"
    "v_cvt_pk_bf16_f32 v33, v22, v23\n\t"
    "v_mfma_f32_16x16x32_bf16 v[12:15], a[60:63], v[168:171], v[12:15]\n\t"
    "v_exp_f32_e32 v42, v42\n\t"
    "v_mfma_f32_16x16x32_bf16 v[12:15], a[52:55], v[172:175], v[12:15]\n\t"
    "v_sub_f32_e32 v43, v43, v28\n\t"
    "v_sub_f32_e32 v16, v16, v28\n\t"
    "v_mfma_f32_16x16x32_bf16 v[20:23], a[96:99], v[172:175], 0\n\t"
    "v_exp_f32_e32 v43, v43\n\t"
    "v_mfma_f32_16x16x32_bf16 v[20:23], a[104:107], v[168:171], v[20:23]\n\t"
    "v_add_f32_e32 v31, v42, v43\n\t"
    "v_sub_f32_e32 v17, v17, v28\n\t"
    "v_mfma_f32_16x16x32_bf16 v[20:23], a[112:115], v[164:167], v[20:23]\n\t"
    "v_exp_f32_e32 v16, v16\n\t"
    "v_mfma_f32_16x16x32_bf16 v[20:23], a[120:123], v[160:163], v[20:23]\n\t"
    "v_cvt_pk_bf16_f32 v34, v12, v13\n\t"
    "v_cvt_pk_bf16_f32 v35, v14, v15\n\t"
    "v_mfma_f32_16x16x32_bf16 v[20:23], a[128:131], v[156:159], v[20:23]\n\t"
    "v_add_f32_e32 v30, v30, v16\n\t"
    "v_sub_f32_e32 v18, v18, v28\n\t"
    "v_mfma_f32_16x16x32_bf16 v[20:23], a[136:139], v[152:155], v[20:23]\n\t"
    "v_exp_f32_e32 v17, v17\n\t"
    "v_mfma_f32_16x16x32_bf16 v[12:15], a[140:143], v[152:155], 0\n\t"
    "v_add_f32_e32 v30, v30, v17\n\t"
    "v_sub_f32_e32 v19, v19, v28\n\t"
    "v_mfma_f32_16x16x32_bf16 v[12:15], a[132:135], v[156:159], v[12:15]\n\t"
    "v_exp_f32_e32 v18, v18\n\t"
    "v_mfma_f32_16x16x32_bf16 v[12:15], a[124:127], v[160:163], v[12:15]\n\t"
    "v_add_f32_e32 v31, v31, v18\n\t"
    "v_sub_f32_e32 v44, v44, v28\n\t"
    "v_mfma_f32_16x16x32_bf16 v[12:15], a[116:119], v[164:167], v[12:15]\n\t"
    "v_cvt_pk_bf16_f32 v36, v20, v21\n\t"
    "v_cvt_pk_bf16_f32 v37, v22, v23\n\t"
    "v_mfma_f32_16x16x32_bf16 v[12:15], a[108:111], v[168:171], v[12:15]\n\t"
    "v_exp_f32_e32 v19, v19\n\t"
    "v_mfma_f32_16x16x32_bf16 v[12:15], a[100:103], v[172:175], v[12:15]\n\t"
    "v_add_f32_e32 v31, v31, v19\n\t"
    "v_sub_f32_e32 v45, v45, v28\n\t"
    "v_mfma_f32_16x16x32_bf16 v[20:23], a[144:147], v[172:175], 0\n\t"
    "v_exp_f32_e32 v44, v44\n\t"
    "v_mfma_f32_16x16x32_bf16 v[20:23], a[152:155], v[168:171], v[20:23]\n\t"
    "v_add_f32_e32 v30, v30, v44\n\t"
    "v_sub_f32_e32 v46, v46, v28\n\t"
    "v_mfma_f32_16x16x32_bf16 v[20:23], a[160:163], v[164:167], v[20:23]\n\t"
    "s_add_u32 m0, s63, 21504\n\t"
    "v_sub_f32_e32 v47, v47, v28\n\t"
    "v_mfma_f32_16x16x32_bf16 v[20:23], a[168:171], v[160:163], v[20:23]\n\t"
    "buffer_load_dwordx4 v7, s[52:55], s61 offen offset:0 nt lds\n\t"
    "v_cvt_pk_bf16_f32 v38, v12, v13\n\t"
    "v_mfma_f32_16x16x32_bf16 v[20:23], a[176:179], v[156:159], v[20:23]\n\t"
    "v_cvt_pk_bf16_f32 v39, v14, v15\n\t"
    "v_sub_f32_e32 v52, v52, v28\n\t"
    "v_mfma_f32_16x16x32_bf16 v[20:23], a[184:187], v[152:155], v[20:23]\n\t"
    "buffer_load_dwordx4 v7, s[52:55], s61 offen offset:1024 nt lds\n\t"
    "v_sub_f32_e32 v53, v53, v28\n\t"
    "v_mfma_f32_16x16x32_bf16 v[56:59], v[220:223], v[24:27], 0\n\t"
    "v_exp_f32_e32 v45, v45\n\t"
    "v_mfma_f32_16x16x32_bf16 v[56:59], v[216:219], v[32:35], v[56:59]\n\t"
    "buffer_load_dwordx4 v7, s[52:55], s61 offen offset:2048 nt lds\n\t"
    "v_add_f32_e32 v30, v30, v45\n\t"
    "v_mfma_f32_16x16x32_bf16 v[56:59], v[212:215], v[36:39], v[56:59]\n\t"
    "s_add_u32 s61, s61, 11206656\n\t"
    "v_sub_f32_e32 v54, v54, v28\n\t"
    "v_mfma_f32_16x16x32_bf16 v[60:63], v[188:191], v[36:39], 0\n\t"
    "v_exp_f32_e32 v46, v46\n\t"
    "v_mfma_f32_16x16x32_bf16 v[60:63], v[192:195], v[32:35], v[60:63]\n\t"
    "v_add_f32_e32 v31, v31, v46\n\t"
    "v_sub_f32_e32 v55, v55, v28\n\t"
    "v_mfma_f32_16x16x32_bf16 v[60:63], v[196:199], v[24:27], v[60:63]\n\t"
    "v_exp_f32_e32 v47, v47\n\t"
    "v_mfma_f32_16x16x32_bf16 v[64:67], v[172:175], v[24:27], 0\n\t"
    "v_add_f32_e32 v31, v31, v47\n\t"
    "v_cvt_pk_bf16_f32 v12, v20, v21\n\t"
    "v_mfma_f32_16x16x32_bf16 v[64:67], v[168:171], v[32:35], v[64:67]\n\t"
    "v_exp_f32_e32 v52, v52\n\t"
    "v_mfma_f32_16x16x32_bf16 v[64:67], v[164:167], v[36:39], v[64:67]\n\t"
    "v_add_f32_e32 v30, v30, v52\n\t"
    "v_cvt_pk_bf16_f32 v13, v22, v23\n\t"
    "v_mfma_f32_16x16x32_bf16 v[68:71], v[140:143], v[36:39], 0\n\t"
    "v_exp_f32_e32 v53, v53\n\t"
    "v_mfma_f32_16x16x32_bf16 v[68:71], v[144:147], v[32:35], v[68:71]\n\t"
    "v_add_f32_e32 v30, v30, v53\n\t"
    "v_mfma_f32_16x16x32_bf16 v[68:71], v[148:151], v[24:27], v[68:71]\n\t"
    "v_exp_f32_e32 v54, v54\n\t"
    "v_mfma_f32_16x16x32_bf16 v[20:23], a[188:191], v[152:155], 0\n\t"
    "v_add_f32_e32 v31, v31, v54\n\t"
    "v_mfma_f32_16x16x32_bf16 v[20:23], a[180:183], v[156:159], v[20:23]\n\t"
    "v_exp_f32_e32 v55, v55\n\t"
    "v_mfma_f32_16x16x32_bf16 v[20:23], a[172:175], v[160:163], v[20:23]\n\t"
    "v_add_f32_e32 v31, v31, v55\n\t"
    "v_add_f32_e32 v30, v30, v31\n\t"
    "v_mfma_f32_16x16x32_bf16 v[20:23], a[164:167], v[164:167], v[20:23]\n\t"
    "v_mov_b32_e32 v31, v30\n\t"
    "v_mfma_f32_16x16x32_bf16 v[20:23], a[156:159], v[168:171], v[20:23]\n\t"
    "v_mfma_f32_16x16x32_bf16 v[20:23], a[148:151], v[172:175], v[20:23]\n\t"
    "v_permlane32_swap_b32 v30, v31\n\t"
    "v_add_f32_e32 v30, v30, v31\n\t"
    "v_mfma_f32_16x16x32_bf16 v[24:27], a[192:195], v[172:175], 0\n\t"
    "v_mov_b32_e32 v31, v30\n\t"
    "v_mfma_f32_16x16x32_bf16 v[24:27], a[200:203], v[168:171], v[24:27]\n\t"
    "v_mfma_f32_16x16x32_bf16 v[24:27], a[208:211], v[164:167], v[24:27]\n\t"
    "v_permlane16_swap_b32 v30, v31\n\t"
    "v_add_f32_e32 v30, v30, v31\n\t"
    "v_mfma_f32_16x16x32_bf16 v[24:27], a[216:219], v[160:163], v[24:27]\n\t"
    "v_cvt_pk_bf16_f32 v14, v20, v21\n\t"
    "v_cvt_pk_bf16_f32 v15, v22, v23\n\t"
    "v_mfma_f32_16x16x32_bf16 v[24:27], a[224:227], v[156:159], v[24:27]\n\t"
    "v_rcp_f32_e32 v30, v30\n\t"
    "v_mfma_f32_16x16x32_bf16 v[24:27], a[232:235], v[152:155], v[24:27]\n\t"
    "v_mul_f32_e32 v40, v40, v30\n\t"
    "v_mul_f32_e32 v41, v41, v30\n\t"
    "v_mfma_f32_16x16x32_bf16 v[32:35], a[236:239], v[152:155], 0\n\t"
    "v_cvt_pk_bf16_f32 v20, v40, v41\n\t"
    "v_mul_f32_e32 v42, v42, v30\n\t"
    "v_mfma_f32_16x16x32_bf16 v[32:35], a[228:231], v[156:159], v[32:35]\n\t"
    "v_mul_f32_e32 v43, v43, v30\n\t"
    "v_cvt_pk_bf16_f32 v21, v42, v43\n\t"
    "v_mfma_f32_16x16x32_bf16 v[32:35], a[220:223], v[160:163], v[32:35]\n\t"
    "v_mul_f32_e32 v16, v16, v30\n\t"
    "v_mul_f32_e32 v17, v17, v30\n\t"
    "v_mfma_f32_16x16x32_bf16 v[32:35], a[212:215], v[164:167], v[32:35]\n\t"
    "v_cvt_pk_bf16_f32 v36, v24, v25\n\t"
    "v_cvt_pk_bf16_f32 v37, v26, v27\n\t"
    "v_mfma_f32_16x16x32_bf16 v[32:35], a[204:207], v[168:171], v[32:35]\n\t"
    "v_cvt_pk_bf16_f32 v22, v16, v17\n\t"
    "v_mul_f32_e32 v18, v18, v30\n\t"
    "v_mfma_f32_16x16x32_bf16 v[32:35], a[196:199], v[172:175], v[32:35]\n\t"
    "v_mul_f32_e32 v19, v19, v30\n\t"
    "v_cvt_pk_bf16_f32 v23, v18, v19\n\t"
    "v_mfma_f32_16x16x32_bf16 v[24:27], a[240:243], v[172:175], 0\n\t"
    "v_mul_f32_e32 v44, v44, v30\n\t"
    "v_mul_f32_e32 v45, v45, v30\n\t"
    "v_mfma_f32_16x16x32_bf16 v[24:27], a[248:251], v[168:171], v[24:27]\n\t"
    "v_cvt_pk_bf16_f32 v40, v44, v45\n\t"
    "v_mul_f32_e32 v46, v46, v30\n\t"
    "v_mfma_f32_16x16x32_bf16 v[24:27], v[224:227], v[164:167], v[24:27]\n\t"
    "v_mul_f32_e32 v47, v47, v30\n\t"
    "v_cvt_pk_bf16_f32 v41, v46, v47\n\t"
    "v_mfma_f32_16x16x32_bf16 v[24:27], v[232:235], v[160:163], v[24:27]\n\t"
    "v_cvt_pk_bf16_f32 v38, v32, v33\n\t"
    "v_cvt_pk_bf16_f32 v39, v34, v35\n\t"
    "v_mfma_f32_16x16x32_bf16 v[24:27], v[240:243], v[156:159], v[24:27]\n\t"
    "v_mul_f32_e32 v52, v52, v30\n\t"
    "v_mul_f32_e32 v53, v53, v30\n\t"
    "v_mfma_f32_16x16x32_bf16 v[24:27], v[248:251], v[152:155], v[24:27]\n\t"
    "v_cvt_pk_bf16_f32 v42, v52, v53\n\t"
    "v_mul_f32_e32 v54, v54, v30\n\t"
    "v_mfma_f32_16x16x32_bf16 v[16:19], v[252:255], v[152:155], 0\n\t"
    "v_mul_f32_e32 v55, v55, v30\n\t"
    "v_cvt_pk_bf16_f32 v43, v54, v55\n\t"
    "v_mfma_f32_16x16x32_bf16 v[16:19], v[244:247], v[156:159], v[16:19]\n\t"
    "v_mfma_f32_16x16x32_bf16 v[16:19], v[236:239], v[160:163], v[16:19]\n\t"
    "v_mfma_f32_16x16x32_bf16 v[16:19], v[228:231], v[164:167], v[16:19]\n\t"
    "v_cvt_pk_bf16_f32 v28, v24, v25\n\t"
    "v_cvt_pk_bf16_f32 v29, v26, v27\n\t"
    "v_mfma_f32_16x16x32_bf16 v[16:19], a[252:255], v[168:171], v[16:19]\n\t"
    "v_mfma_f32_16x16x32_bf16 v[16:19], a[244:247], v[172:175], v[16:19]\n\t"
    "v_mfma_f32_16x16x32_bf16 v[24:27], a[0:3], v[148:151], 0\n\t"
    "ds_write_b128 v6, v[20:23] offset:2048\n\t"
    "v_mfma_f32_16x16x32_bf16 v[24:27], a[8:11], v[144:147], v[24:27]\n\t"
    "v_mfma_f32_16x16x32_bf16 v[24:27], a[16:19], v[140:143], v[24:27]\n\t"
    "s_add_u32 m0, s62, 0\n\t"
    "v_mfma_f32_16x16x32_bf16 v[24:27], a[24:27], v[136:139], v[24:27]\n\t"
    "buffer_load_dwordx4 v7, s[52:55], s61 offen offset:0 nt lds\n\t"
    "v_cvt_pk_bf16_f32 v30, v16, v17\n\t"
    "v_mfma_f32_16x16x32_bf16 v[24:27], a[32:35], v[132:135], v[24:27]\n\t"
    "ds_write_b128 v6, v[40:43] offset:3072\n\t"
    "v_mfma_f32_16x16x32_bf16 v[24:27], a[40:43], v[128:131], v[24:27]\n\t"
    "buffer_load_dwordx4 v7, s[52:55], s61 offen offset:1024 nt lds\n\t"
    "v_cvt_pk_bf16_f32 v31, v18, v19\n\t"
    "v_mfma_f32_16x16x32_bf16 v[56:59], v[208:211], v[12:15], v[56:59]\n\t"
    "v_mfma_f32_16x16x32_bf16 v[56:59], v[204:207], v[36:39], v[56:59]\n\t"
    "buffer_load_dwordx4 v7, s[52:55], s61 offen offset:2048 nt lds\n\t"
    "v_mfma_f32_16x16x32_bf16 v[56:59], v[200:203], v[28:31], v[56:59]\n\t"
    "s_add_u32 s61, s61, 196608\n\t"
    "v_mfma_f32_16x16x32_bf16 v[60:63], v[176:179], v[28:31], v[60:63]\n\t"
    "v_cvt_pk_bf16_f32 v40, v24, v25\n\t"
    "v_cvt_pk_bf16_f32 v41, v26, v27\n\t"
    "v_mfma_f32_16x16x32_bf16 v[60:63], v[180:183], v[36:39], v[60:63]\n\t"
    "v_mfma_f32_16x16x32_bf16 v[60:63], v[184:187], v[12:15], v[60:63]\n\t"
    "v_mfma_f32_16x16x32_bf16 v[64:67], v[160:163], v[12:15], v[64:67]\n\t"
    "v_max3_f32 v72, v56, v57, v58\n\t"
    "v_mfma_f32_16x16x32_bf16 v[64:67], v[156:159], v[36:39], v[64:67]\n\t"
    "v_mfma_f32_16x16x32_bf16 v[64:67], v[152:155], v[28:31], v[64:67]\n\t"
    "v_mfma_f32_16x16x32_bf16 v[68:71], v[128:131], v[28:31], v[68:71]\n\t"
    "v_max3_f32 v73, v59, v60, v61\n\t"
    "v_max3_f32 v72, v72, v62, v63\n\t"
    "v_mfma_f32_16x16x32_bf16 v[68:71], v[132:135], v[36:39], v[68:71]\n\t"
    "v_mfma_f32_16x16x32_bf16 v[68:71], v[136:139], v[12:15], v[68:71]\n\t"
    "v_mfma_f32_16x16x32_bf16 v[16:19], a[44:47], v[128:131], 0\n\t"
    "v_max3_f32 v73, v73, v64, v65\n\t"
    "v_max3_f32 v72, v72, v66, v67\n\t"
    "v_mfma_f32_16x16x32_bf16 v[16:19], a[36:39], v[132:135], v[16:19]\n\t"
    "v_mfma_f32_16x16x32_bf16 v[16:19], a[28:31], v[136:139], v[16:19]\n\t"
    "v_mfma_f32_16x16x32_bf16 v[16:19], a[20:23], v[140:143], v[16:19]\n\t"
    "v_max3_f32 v73, v73, v68, v69\n\t"
    "v_max3_f32 v72, v72, v70, v71\n\t"
    "v_mfma_f32_16x16x32_bf16 v[16:19], a[12:15], v[144:147], v[16:19]\n\t"
    "v_max_f32_e32 v72, v72, v73\n\t"
    "v_mov_b32_e32 v73, v72\n\t"
    "v_mfma_f32_16x16x32_bf16 v[16:19], a[4:7], v[148:151], v[16:19]\n\t"
    "v_mfma_f32_16x16x32_bf16 v[12:15], a[48:51], v[148:151], 0\n\t"
    "v_permlane32_swap_b32 v72, v73\n\t"
    "v_max_f32_e32 v72, v72, v73\n\t"
    "v_mfma_f32_16x16x32_bf16 v[12:15], a[56:59], v[144:147], v[12:15]\n\t"
    "v_mov_b32_e32 v73, v72\n\t"
    "v_mfma_f32_16x16x32_bf16 v[12:15], a[64:67], v[140:143], v[12:15]\n\t"
    "v_mfma_f32_16x16x32_bf16 v[12:15], a[72:75], v[136:139], v[12:15]\n\t"
    "v_cvt_pk_bf16_f32 v42, v16, v17\n\t"
    "v_cvt_pk_bf16_f32 v43, v18, v19\n\t"
    "v_mfma_f32_16x16x32_bf16 v[12:15], a[80:83], v[132:135], v[12:15]\n\t"
    "v_permlane16_swap_b32 v72, v73\n\t"
    "v_max_f32_e32 v72, v72, v73\n\t"
    "v_mfma_f32_16x16x32_bf16 v[12:15], a[88:91], v[128:131], v[12:15]\n\t"
    "v_sub_f32_e32 v56, v56, v72\n\t"
    "v_sub_f32_e32 v57, v57, v72\n\t"
    "v_mfma_f32_16x16x32_bf16 v[16:19], a[92:95], v[128:131], 0\n\t"
    "v_exp_f32_e32 v56, v56\n\t"
    "v_mfma_f32_16x16x32_bf16 v[16:19], a[84:87], v[132:135], v[16:19]\n\t"
    "v_exp_f32_e32 v57, v57\n\t"
    "v_mfma_f32_16x16x32_bf16 v[16:19], a[76:79], v[136:139], v[16:19]\n\t"
    "v_add_f32_e32 v74, v56, v57\n\t"
    "v_sub_f32_e32 v58, v58, v72\n\t"
    "v_mfma_f32_16x16x32_bf16 v[16:19], a[68:71], v[140:143], v[16:19]\n\t"
    "v_cvt_pk_bf16_f32 v44, v12, v13\n\t"
    "v_cvt_pk_bf16_f32 v45, v14, v15\n\t"
    "v_mfma_f32_16x16x32_bf16 v[16:19], a[60:63], v[144:147], v[16:19]\n\t"
    "v_exp_f32_e32 v58, v58\n\t"
    "v_mfma_f32_16x16x32_bf16 v[16:19], a[52:55], v[148:151], v[16:19]\n\t"
    ".Lh6_%=:\n\t"
    "s_cbranch_execz .LrM_%=\n\t"
    "v_sub_f32_e32 v59, v59, v72\n\t"
    "v_sub_f32_e32 v60, v60, v72\n\t"
    "v_mfma_f32_16x16x32_bf16 v[12:15], a[96:99], v[148:151], 0\n\t"
    "v_exp_f32_e32 v59, v59\n\t"
    "v_mfma_f32_16x16x32_bf16 v[12:15], a[104:107], v[144:147], v[12:15]\n\t"
    "v_add_f32_e32 v75, v58, v59\n\t"
    "v_sub_f32_e32 v61, v61, v72\n\t"
    "v_mfma_f32_16x16x32_bf16 v[12:15], a[112:115], v[140:143], v[12:15]\n\t"
    "v_exp_f32_e32 v60, v60\n\t"
    "v_mfma_f32_16x16x32_bf16 v[12:15], a[120:123], v[136:139], v[12:15]\n\t"
    "v_cvt_pk_bf16_f32 v46, v16, v17\n\t"
    "v_cvt_pk_bf16_f32 v47, v18, v19\n\t"
    "v_mfma_f32_16x16x32_bf16 v[12:15], a[128:131], v[132:135], v[12:15]\n\t"
    "v_add_f32_e32 v74, v74, v60\n\t"
    "v_sub_f32_e32 v62, v62, v72\n\t"
    "v_mfma_f32_16x16x32_bf16 v[12:15], a[136:139], v[128:131], v[12:15]\n\t"
    "v_exp_f32_e32 v61, v61\n\t"
    "v_mfma_f32_16x16x32_bf16 v[16:19], a[140:143], v[128:131], 0\n\t"
    "v_add_f32_e32 v74, v74, v61\n\t"
    "v_sub_f32_e32 v63, v63, v72\n\t"
    "v_mfma_f32_16x16x32_bf16 v[16:19], a[132:135], v[132:135], v[16:19]\n\t"
    "v_exp_f32_e32 v62, v62\n\t"
    "v_mfma_f32_16x16x32_bf16 v[16:19], a[124:127], v[136:139], v[16:19]\n\t"
    "v_add_f32_e32 v75, v75, v62\n\t"
    "v_sub_f32_e32 v64, v64, v72\n\t"
    "v_mfma_f32_16x16x32_bf16 v[16:19], a[116:119], v[140:143], v[16:19]\n\t"
    "v_cvt_pk_bf16_f32 v32, v12, v13\n\t"
    "v_cvt_pk_bf16_f32 v33, v14, v15\n\t"
    "v_mfma_f32_16x16x32_bf16 v[16:19], a[108:111], v[144:147], v[16:19]\n\t"
    "v_exp_f32_e32 v63, v63\n\t"
    "v_mfma_f32_16x16x32_bf16 v[16:19], a[100:103], v[148:151], v[16:19]\n\t"
    "v_add_f32_e32 v75, v75, v63\n\t"
    "v_sub_f32_e32 v65, v65, v72\n\t"
    "v_mfma_f32_16x16x32_bf16 v[48:51], a[144:147], v[148:151], 0\n\t"
    "v_exp_f32_e32 v64, v64\n\t"
    "v_mfma_f32_16x16x32_bf16 v[48:51], a[152:155], v[144:147], v[48:51]\n\t"
    "v_add_f32_e32 v74, v74, v64\n\t"
    "v_sub_f32_e32 v66, v66, v72\n\t"
    "s_branch .Lmid_%=\n\t"
    ".Ltail_%=:\n\t"
    "v_mbcnt_lo_u32_b32 v8, -1, 0\n\t"
    "v_mbcnt_hi_u32_b32 v8, -1, v8\n\t"
    "v_lshlrev_b32_e32 v8, 5, v8\n\t"
    "s_nop 7\n\t"
    "s_nop 1\n\t"
    "ds_write_b128 v6, v[44:47] offset:4096\n\t"
    "v_cvt_pk_bf16_f32 v62, v56, v57\n\t"
    "ds_write_b128 v6, v[52:55] offset:5120\n\t"
    "v_cvt_pk_bf16_f32 v63, v58, v59\n\t"
    "s_nop 1\n\t"
    "v_mfma_f32_16x16x32_bf16 v[12:15], v[208:211], v[36:39], v[12:15]\n\t"
    "s_nop 1\n\t"
    "v_mfma_f32_16x16x32_bf16 v[12:15], v[204:207], v[48:51], v[12:15]\n\t"
    "s_nop 1\n\t"
    "v_mfma_f32_16x16x32_bf16 v[12:15], v[200:203], v[60:63], v[12:15]\n\t"
    "s_nop 1\n\t"
    "v_mfma_f32_16x16x32_bf16 v[16:19], v[176:179], v[60:63], v[16:19]\n\t"
    "s_nop 1\n\t"
    "v_mfma_f32_16x16x32_bf16 v[16:19], v[180:183], v[48:51], v[16:19]\n\t"
    "s_waitcnt lgkmcnt(0)\n\t"
    "s_barrier\n\t"
    "s_nop 1\n\t"
    "v_mfma_f32_16x16x32_bf16 v[16:19], v[184:187], v[36:39], v[16:19]\n\t"
    "s_nop 1\n\t"
    "v_mfma_f32_16x16x32_bf16 v[20:23], v[160:163], v[36:39], v[20:23]\n\t"
    "v_max3_f32 v52, v12, v13, v14\n\t"
    "s_nop 1\n\t"
    "v_mfma_f32_16x16x32_bf16 v[20:23], v[156:159], v[48:51], v[20:23]\n\t"
    "s_nop 1\n\t"
    "v_mfma_f32_16x16x32_bf16 v[20:23], v[152:155], v[60:63], v[20:23]\n\t"
    "s_nop 1\n\t"
    "v_mfma_f32_16x16x32_bf16 v[24:27], v[128:131], v[60:63], v[24:27]\n\t"
    "v_max3_f32 v53, v15, v16, v17\n\t"
    "v_max3_f32 v52, v52, v18, v19\n\t"
    "s_nop 1\n\t"
    "v_mfma_f32_16x16x32_bf16 v[24:27], v[132:135], v[48:51], v[24:27]\n\t"
    "s_nop 1\n\t"
    "v_mfma_f32_16x16x32_bf16 v[24:27], v[136:139], v[36:39], v[24:27]\n\t"
    "s_nop 7\n\t"
    "s_nop 1\n\t"
    "v_max3_f32 v53, v53, v20, v21\n\t"
    "v_max3_f32 v52, v52, v22, v23\n\t"
    "v_max3_f32 v53, v53, v24, v25\n\t"
    "v_max3_f32 v52, v52, v26, v27\n\t"
    "v_max_f32_e32 v52, v52, v53\n\t"
    "v_mov_b32_e32 v53, v52\n\t"
    "s_nop 1\n\t"
    "v_permlane32_swap_b32 v52, v53\n\t"
    "v_max_f32_e32 v52, v52, v53\n\t"
    "v_mov_b32_e32 v53, v52\n\t"
    "s_nop 1\n\t"
    "v_permlane16_swap_b32 v52, v53\n\t"
    "v_max_f32_e32 v52, v52, v53\n\t"
    "v_sub_f32_e32 v12, v12, v52\n\t"
    "v_sub_f32_e32 v13, v13, v52\n\t"
    "v_sub_f32_e32 v14, v14, v52\n\t"
    "v_exp_f32_e32 v12, v12\n\t"
    "s_nop 0\n\t"
    "v_sub_f32_e32 v15, v15, v52\n\t"
    "v_exp_f32_e32 v13, v13\n\t"
    "s_nop 0\n\t"
    "v_add_f32_e32 v54, v12, v13\n\t"
    "v_sub_f32_e32 v16, v16, v52\n\t"
    "v_exp_f32_e32 v14, v14\n\t"
    "s_nop 0\n\t"
    "v_exp_f32_e32 v15, v15\n\t"
    "s_nop 0\n\t"
    "v_add_f32_e32 v55, v14, v15\n\t"
    "v_sub_f32_e32 v17, v17, v52\n\t"
    "v_exp_f32_e32 v16, v16\n\t"
    "s_nop 0\n\t"
    "v_add_f32_e32 v54, v54, v16\n\t"
    "v_sub_f32_e32 v18, v18, v52\n\t"
    "v_exp_f32_e32 v17, v17\n\t"
    "s_nop 0\n\t"
    "v_add_f32_e32 v54, v54, v17\n\t"
    "v_sub_f32_e32 v19, v19, v52\n\t"
    "v_exp_f32_e32 v18, v18\n\t"
    "s_nop 0\n\t"
    "v_add_f32_e32 v55, v55, v18\n\t"
    "v_sub_f32_e32 v20, v20, v52\n\t"
    "v_exp_f32_e32 v19, v19\n\t"
    "s_nop 0\n\t"
    "v_add_f32_e32 v55, v55, v19\n\t"
    "v_exp_f32_e32 v20, v20\n\t"
    "s_nop 0\n\t"
    "v_add_f32_e32 v54, v54, v20\n\t"
    "v_sub_f32_e32 v21, v21, v52\n\t"
    "v_sub_f32_e32 v22, v22, v52\n\t"
    "v_exp_f32_e32 v21, v21\n\t"
    "s_nop 0\n\t"
    "v_add_f32_e32 v54, v54, v21\n\t"
    "v_exp_f32_e32 v22, v22\n\t"
    "s_nop 0\n\t"
    "v_add_f32_e32 v55, v55, v22\n\t"
    "v_sub_f32_e32 v23, v23, v52\n\t"
    "v_sub_f32_e32 v24, v24, v52\n\t"
    "v_exp_f32_e32 v23, v23\n\t"
    "s_nop 0\n\t"
    "v_add_f32_e32 v55, v55, v23\n\t"
    "v_exp_f32_e32 v24, v24\n\t"
    "s_nop 0\n\t"
    "v_add_f32_e32 v54, v54, v24\n\t"
    "v_sub_f32_e32 v25, v25, v52\n\t"
    "v_sub_f32_e32 v26, v26, v52\n\t"
    "v_sub_f32_e32 v27, v27, v52\n\t"
    "v_exp_f32_e32 v25, v25\n\t"
    "s_nop 0\n\t"
    "v_add_f32_e32 v54, v54, v25\n\t"
    "v_exp_f32_e32 v26, v26\n\t"
    "s_nop 0\n\t"
    "v_add_f32_e32 v55, v55, v26\n\t"
    "v_exp_f32_e32 v27, v27\n\t"
    "s_nop 0\n\t"
    "v_add_f32_e32 v55, v55, v27\n\t"
    "v_add_f32_e32 v54, v54, v55\n\t"
    "v_mov_b32_e32 v55, v54\n\t"
    "s_nop 1\n\t"
    "v_permlane32_swap_b32 v54, v55\n\t"
    "v_add_f32_e32 v54, v54, v55\n\t"
    "v_mov_b32_e32 v55, v54\n\t"
    "s_nop 1\n\t"
    "v_permlane16_swap_b32 v54, v55\n\t"
    "v_add_f32_e32 v54, v54, v55\n\t"
    "v_rcp_f32_e32 v54, v54\n\t"
    "s_nop 0\n\t"
    "v_mul_f32_e32 v12, v12, v54\n\t"
    "v_mul_f32_e32 v13, v13, v54\n\t"
    "v_cvt_pk_bf16_f32 v60, v12, v13\n\t"
    "v_mul_f32_e32 v14, v14, v54\n\t"
    "v_mul_f32_e32 v15, v15, v54\n\t"
    "v_cvt_pk_bf16_f32 v61, v14, v15\n\t"
    "v_mul_f32_e32 v16, v16, v54\n\t"
    "v_mul_f32_e32 v17, v17, v54\n\t"
    "v_cvt_pk_bf16_f32 v62, v16, v17\n\t"
    "v_mul_f32_e32 v18, v18, v54\n\t"
    "v_mul_f32_e32 v19, v19, v54\n\t"
    "v_cvt_pk_bf16_f32 v63, v18, v19\n\t"
    "v_mul_f32_e32 v20, v20, v54\n\t"
    "v_mul_f32_e32 v21, v21, v54\n\t"
    "v_cvt_pk_bf16_f32 v44, v20, v21\n\t"
    "v_mul_f32_e32 v22, v22, v54\n\t"
    "v_mul_f32_e32 v23, v23, v54\n\t"
    "v_cvt_pk_bf16_f32 v45, v22, v23\n\t"
    "v_mul_f32_e32 v24, v24, v54\n\t"
    "v_mul_f32_e32 v25, v25, v54\n\t"
    "v_cvt_pk_bf16_f32 v46, v24, v25\n\t"
    "v_mul_f32_e32 v26, v26, v54\n\t"
    "v_mul_f32_e32 v27, v27, v54\n\t"
    "ds_write_b128 v6, v[60:63] offset:6144\n\t"
    "v_cvt_pk_bf16_f32 v47, v26, v27\n\t"
    "ds_write_b128 v6, v[44:47] offset:7168\n\t"
    "s_waitcnt lgkmcnt(0)\n\t"
    "s_barrier\n\t"
    "s_cmp_lg_u32 %[closer], 0\n\t"
    "s_cbranch_scc1 .Lx2_%=\n\t"
    "s_mov_b32 s60, 0x10000\n\t"
    "buffer_load_dwordx2 v[10:11], off, s[56:59], %[eoff] sc1\n\t"
    ".Lp3_%=:\n\t"
    "buffer_load_dwordx4 v[12:15], v8, s[56:59], %[aoff] offen sc1\n\t"
    "buffer_load_dwordx4 v[16:19], v8, s[56:59], %[aoff] offen offset:16 sc1\n\t"
    "s_waitcnt vmcnt(0)\n\t"
    "v_add_co_u32_e32 v20, vcc, 0x7f4a7c15, v10\n\t"
    "s_nop 1\n\t"
    "v_mov_b32_e32 v9, 0x9e3779b9\n\t"
    "v_addc_co_u32_e32 v21, vcc, v9, v11, vcc\n\t"
    "v_cmp_eq_u64_e32 vcc, v[12:13], v[20:21]\n\t"
    "v_cmp_eq_u64_e64 s[66:67], v[14:15], v[20:21]\n\t"
    "s_and_b64 vcc, vcc, s[66:67]\n\t"
    "v_cmp_eq_u64_e64 s[66:67], v[16:17], v[20:21]\n\t"
    "s_and_b64 vcc, vcc, s[66:67]\n\t"
    "v_cmp_eq_u64_e64 s[66:67], v[18:19], v[20:21]\n\t"
    "s_and_b64 vcc, vcc, s[66:67]\n\t"
    "s_cmp_eq_u64 vcc, exec\n\t"
    "s_cbranch_scc1 .Lk4_%=\n\t"
    "s_sleep 8\n\t"
    "s_sub_u32 s60, s60, 1\n\t"
    "s_cmp_lg_u32 s60, 0\n\t"
    "s_cbranch_scc1 .Lp3_%=\n\t"
    ".Lk4_%=:\n\t"
    "s_mov_b64 exec, 1\n\t"
    "buffer_store_dwordx2 v[20:21], off, s[56:59], %[eoff] sc1\n\t"
    "s_mov_b64 exec, -1\n\t"
    ".Lx2_%=:\n\t"
    "s_endpgm\n\t"
    ".Lend_%=:\n\t"
    :: "{v31}"(lane), "{s[36:43]}"(k8), "{s[44:47]}"(k4),
       [role] "s"(role), [pair] "s"(pair), [prod] "s"((pair * 256 + bid - 144) >> 31), [closer] "s"(bid | wave), [bid] "s"(bid), [wb] "s"(wb),
       [rbeg] "s"(rbeg), [rend] "s"(rbeg + 3 * SLOT), [pl] "s"(ldsb + PB + pair * 8192), [amask] "s"((unsigned long long)pair),
       [tch] "s"(((bid >> 3) * 2 + pair) * 1152), [foff] "s"(F_OFF + 64 * bid), [aoff] "s"(A_OFF), [aown] "s"(A_OFF + 8 * bid), [eoff] "s"(E_OFF)
    : "memory", "scc", ACLOB, VCLOB, SCLOB);
}

extern "C" void kernel_launch(void* const* d_in, const int* in_sizes, int n_in,
                              void* d_out, int out_size, void* d_ws, size_t ws_size,
                              hipStream_t stream) {
  attn_kernel<<<256, 256, 0, stream>>>((const unsigned char*)d_in[0], (const unsigned char*)d_in[1], (const unsigned char*)d_in[2],
                                       (unsigned char*)d_ws, (unsigned char*)d_out
      );
}
```

```cpp
#include <hip/hip_runtime.h>
#include <hip/hip_bf16.h>
#include <stdint.h>

using u32x4 = __attribute__((ext_vector_type(4))) unsigned;
using u32x8 = __attribute__((ext_vector_type(8))) unsigned;

static constexpr int ROWB = 512 * 384;
static constexpr int SLOT = 24576;
static constexpr int PB = 6 * SLOT;
static constexpr int F_OFF = 160 * 1024, A_OFF = 148 * 1024, E_OFF = 152 * 1024;

#define R10(p, n) p #n "0", p #n "1", p #n "2", p #n "3", p #n "4", p #n "5", p #n "6", p #n "7", p #n "8", p #n "9"
#define R240(p) R10(p, 4), R10(p, 5), R10(p, 6), R10(p, 7), R10(p, 8), R10(p, 9), R10(p, 10), R10(p, 11), R10(p, 12), R10(p, 13), \
  R10(p, 14), R10(p, 15), R10(p, 16), R10(p, 17), R10(p, 18), R10(p, 19), R10(p, 20), R10(p, 21), R10(p, 22), R10(p, 23), R10(p, 24), \
  p "250", p "251", p "252", p "253", p "254", p "255"
#define ACLOB R10("a", ), R10("a", 1), R10("a", 2), R10("a", 3), R240("a")
#define VCLOB R10("v", ), R10("v", 1), R10("v", 2), "v30", "v32", "v33", "v34", "v35", "v36", "v37", "v38", "v39", R240("v")
#define SCLOB "s48", "s49", R10("s", 5), R10("s", 6), "s70", "s71", "s72", "s73", "vcc"

__global__ __launch_bounds__(256, 1) void attn_kernel(const unsigned char* __restrict__ x,
                                                      const unsigned char* __restrict__ wqkv,
                                                      const unsigned char* __restrict__ wo,
                                                      unsigned char* ws,
                                                      unsigned char* __restrict__ out
                                                      ) {
  __shared__ u32x4 lds[10240];
  u32x8 k8; u32x4 k4;
  asm volatile(
    "s_load_dwordx8 s[36:43], %2, 0\n\t"
    "s_load_dwordx2 s[44:45], %2, 32\n\t"
    : "={s[36:43]}"(k8), "={s[44:47]}"(k4) : "s"(__builtin_amdgcn_kernarg_segment_ptr()) : "memory", ACLOB);
  __builtin_amdgcn_sched_barrier(0);
  const unsigned lane = threadIdx.x & 63, wave = __builtin_amdgcn_readfirstlane(threadIdx.x >> 6);
  const unsigned role = wave >> 1, pair = wave & 1;
  const unsigned ldsb = (unsigned)(size_t)lds;
  const unsigned bid = blockIdx.x;
  const unsigned wb = (bid & 7) * 8 * ROWB + ((bid >> 3) * 2 + pair) * 8 * 384;
  const unsigned rbeg = ldsb + pair * 3 * SLOT;
  const unsigned second = (143 - bid) >> 31;
  const unsigned fid = ((bid - 144 * second) ^ (4 * second)) + pair * (40 + ((bid - 216) >> 31) * 256);

  asm volatile(
    ".Lbeg_%=:\n\t"
    "s_cmp_lg_u32 %[role], 1\n\t"
    "s_cbranch_scc1 .Lroln_%=\n\t"
    "s_cmp_eq_u32 %[prod], 0\n\t"
    "s_cbranch_scc1 .Lnp_%=\n\t"
    "s_cmp_lt_u32 %[fid], 72\n\t"
    "s_cselect_b32 s71, 0, 72\n\t"
    "s_cselect_b32 s63, 0, 768\n\t"
    "s_sub_u32 s64, %[fid], s71\n\t"
    "s_mul_i32 s65, s64, 0x1556\n\t"
    "s_lshr_b32 s65, s65, 16\n\t"
    "s_mul_i32 s69, s65, 12\n\t"
    "s_sub_u32 s69, s64, s69\n\t"
    "s_lshr_b32 s69, s69, 1\n\t"
    "s_and_b32 s70, s64, 1\n\t"
    "s_mul_i32 s60, s69, 36864\n\t"
    "s_add_u32 s60, s60, s63\n\t"
    "s_add_u32 s61, s60, 4608\n\t"
    "v_lshrrev_b32_e32 v20, 2, v31\n\t"
    "v_and_b32_e32 v21, 8, v20\n\t"
    "v_bfe_u32 v24, v31, 3, 2\n\t"
    "v_or_b32_e32 v21, v21, v24\n\t"
    "v_mul_u32_u24_e32 v21, 0x480, v21\n\t"
    "v_lshrrev_b32_e32 v24, 4, v31\n\t"
    "v_xor_b32_e32 v24, v24, v31\n\t"
    "v_and_b32_e32 v24, 7, v24\n\t"
    "v_lshl_or_b32 v24, v24, 4, v21\n\t"
    "v_xor_b32_e32 v25, 64, v24\n\t"
    "v_add_u32_e32 v25, 0x4800, v25\n\t"
    "v_mul_u32_u24_e32 v27, 0x180, v20\n\t"
    "v_and_b32_e32 v28, 3, v31\n\t"
    "v_lshl_or_b32 v27, v28, 4, v27\n\t"
    "s_lshl_b32 s62, s65, 3\n\t"
    "s_add_u32 s62, s62, s70\n\t"
    "s_mul_i32 s62, s62, 4608\n\t"
    "s_add_u32 s62, s62, 384\n\t"
    "s_lshl_b32 s63, s65, 6\n\t"
    "s_mov_b32 s50, 0x36000\n\t"
    "s_mov_b32 s51, 0x20000\n\t"
    "s_mov_b32 s54, 0x12000\n\t"
    "s_mov_b32 s55, 0x20000\n\t"
    "s_waitcnt lgkmcnt(0)\n\t"
    "s_mov_b32 s48, s38\n\t"
    "s_and_b32 s49, s39, 0xffff\n\t"
    "s_cmp_lg_u32 s71, 0\n\t"
    "s_cbranch_scc1 .Lpo_%=\n\t"
    "s_mov_b32 m0, 61440\n\t"
    "s_nop 0\n\t"
    "buffer_load_dwordx4 v24, s[48:51], s62 offen offset:0 lds\n\t"
    "s_mov_b32 m0, 62464\n\t"
    "s_nop 0\n\t"
    "buffer_load_dwordx4 v25, s[48:51], s62 offen offset:0 lds\n\t"
    "s_mov_b32 m0, 63360\n\t"
    "s_nop 0\n\t"
    "buffer_load_dwordx4 v24, s[48:51], s62 offen offset:128 lds\n\t"
    "s_mov_b32 m0, 64384\n\t"
    "s_nop 0\n\t"
    "buffer_load_dwordx4 v25, s[48:51], s62 offen offset:128 lds\n\t"
    "s_mov_b32 m0, 65280\n\t"
    "s_nop 0\n\t"
    "buffer_load_dwordx4 v24, s[48:51], s62 offen offset:256 lds\n\t"
    "s_mov_b32 m0, 66304\n\t"
    "s_nop 0\n\t"
    "buffer_load_dwordx4 v25, s[48:51], s62 offen offset:256 lds\n\t"
    "s_branch .Lpa_%=\n\t"
    ".Lpo_%=:\n\t"
    "s_mov_b32 s52, s40\n\t"
    "s_add_u32 m0, %[rbeg], 61440\n\t"
    "s_and_b32 s53, s41, 0xffff\n\t"
    "buffer_load_dwordx4 v27, s[52:55], s63 offen lds\n\t"
    "s_add_u32 m0, %[rbeg], 62464\n\t"
    "s_add_u32 s63, s63, 6144\n\t"
    "buffer_load_dwordx4 v27, s[52:55], s63 offen lds\n\t"
    "s_add_u32 m0, %[rbeg], 63488\n\t"
    "s_add_u32 s63, s63, 6144\n\t"
    "buffer_load_dwordx4 v27, s[52:55], s63 offen lds\n\t"
    "s_add_u32 m0, %[rbeg], 64512\n\t"
    "s_add_u32 s63, s63, 6144\n\t"
    "buffer_load_dwordx4 v27, s[52:55], s63 offen lds\n\t"
    "s_add_u32 m0, %[rbeg], 65536\n\t"
    "s_add_u32 s63, s63, 6144\n\t"
    "buffer_load_dwordx4 v27, s[52:55], s63 offen lds\n\t"
    "s_add_u32 m0, %[rbeg], 66560\n\t"
    "s_add_u32 s63, s63, 6144\n\t"
    "buffer_load_dwordx4 v27, s[52:55], s63 offen lds\n\t"
    "s_add_u32 m0, %[rbeg], 67584\n\t"
    "s_add_u32 s63, s63, 6144\n\t"
    "buffer_load_dwordx4 v27, s[52:55], s63 offen lds\n\t"
    "s_add_u32 m0, %[rbeg], 68608\n\t"
    "s_add_u32 s63, s63, 6144\n\t"
    "buffer_load_dwordx4 v27, s[52:55], s63 offen lds\n\t"
    "s_add_u32 m0, %[rbeg], 69632\n\t"
    "s_add_u32 s63, s63, 6144\n\t"
    "buffer_load_dwordx4 v27, s[52:55], s63 offen lds\n\t"
    "s_add_u32 m0, %[rbeg], 70656\n\t"
    "s_add_u32 s63, s63, 6144\n\t"
    "buffer_load_dwordx4 v27, s[52:55], s63 offen lds\n\t"
    "s_add_u32 m0, %[rbeg], 71680\n\t"
    "s_add_u32 s63, s63, 6144\n\t"
    "buffer_load_dwordx4 v27, s[52:55], s63 offen lds\n\t"
    "s_add_u32 m0, %[rbeg], 72704\n\t"
    "s_add_u32 s63, s63, 6144\n\t"
    "buffer_load_dwordx4 v27, s[52:55], s63 offen lds\n\t"
    ".Lpa_%=:\n\t"
    "s_add_u32 m0, %[rbeg], 49152\n\t"
    "s_nop 0\n\t"
    "buffer_load_dwordx4 v24, s[48:51], s60 offen offset:0 lds\n\t"
    "s_add_u32 m0, %[rbeg], 53120\n\t"
    "s_nop 0\n\t"
    "buffer_load_dwordx4 v24, s[48:51], s60 offen offset:128 lds\n\t"
    "s_add_u32 m0, %[rbeg], 57088\n\t"
    "s_nop 0\n\t"
    "buffer_load_dwordx4 v24, s[48:51], s60 offen offset:256 lds\n\t"
    "s_add_u32 m0, %[rbeg], 51200\n\t"
    "s_nop 0\n\t"
    "buffer_load_dwordx4 v25, s[48:51], s60 offen offset:0 lds\n\t"
    "s_add_u32 m0, %[rbeg], 55168\n\t"
    "s_nop 0\n\t"
    "buffer_load_dwordx4 v25, s[48:51], s60 offen offset:128 lds\n\t"
    "s_add_u32 m0, %[rbeg], 59136\n\t"
    "s_nop 0\n\t"
    "buffer_load_dwordx4 v25, s[48:51], s60 offen offset:256 lds\n\t"
    "s_add_u32 m0, %[rbeg], 50176\n\t"
    "s_nop 0\n\t"
    "buffer_load_dwordx4 v24, s[48:51], s61 offen offset:0 lds\n\t"
    "s_add_u32 m0, %[rbeg], 54144\n\t"
    "s_nop 0\n\t"
    "buffer_load_dwordx4 v24, s[48:51], s61 offen offset:128 lds\n\t"
    "s_add_u32 m0, %[rbeg], 58112\n\t"
    "s_nop 0\n\t"
    "buffer_load_dwordx4 v24, s[48:51], s61 offen offset:256 lds\n\t"
    "s_add_u32 m0, %[rbeg], 52224\n\t"
    "s_nop 0\n\t"
    "buffer_load_dwordx4 v25, s[48:51], s61 offen offset:0 lds\n\t"
    "s_add_u32 m0, %[rbeg], 56192\n\t"
    "s_nop 0\n\t"
    "buffer_load_dwordx4 v25, s[48:51], s61 offen offset:128 lds\n\t"
    "s_add_u32 m0, %[rbeg], 60160\n\t"
    "s_nop 0\n\t"
    "buffer_load_dwordx4 v25, s[48:51], s61 offen offset:256 lds\n\t"
    ".Lnp_%=:\n\t"
    "s_waitcnt lgkmcnt(0)\n\t"
    "s_memrealtime s[72:73]\n\t"
    "s_mov_b32 s56, s42\n\t"
    "s_and_b32 s57, s43, 0xffff\n\t"
    "s_mov_b32 s58, 0x40000\n\t"
    "s_mov_b32 s59, 0x20000\n\t"
    "buffer_load_dwordx2 v[22:23], off, s[56:59], %[eoff] sc1\n\t"
    "s_barrier\n\t"
    "v_lshlrev_b32_e32 v30, 4, v31\n\t"
    "v_and_b32_e32 v28, 7, v31\n\t"
    "v_bfe_u32 v27, v31, 3, 1\n\t"
    "v_lshrrev_b32_e32 v29, 4, v31\n\t"
    "v_add_u32_e32 v6, %[pl], v30\n\t"
    "v_mul_u32_u24_e32 v27, 0xc00, v27\n\t"
    "v_add_u32_e32 v27, %[rbeg], v27\n\t"
    "v_lshlrev_b32_e32 v26, 7, v31\n\t"
    "v_mad_u32_u24 v20, v28, 3, 0\n\t"
    "v_and_b32_e32 v21, 6, v20\n\t"
    "v_xor_b32_e32 v21, v21, v29\n\t"
    "v_lshl_add_u32 v20, v20, 7, v27\n\t"
    "v_lshl_add_u32 v0, v21, 4, v20\n\t"
    "v_xor_b32_e32 v1, 64, v0\n\t"
    "v_mad_u32_u24 v20, v28, 3, 1\n\t"
    "v_and_b32_e32 v21, 6, v20\n\t"
    "v_xor_b32_e32 v21, v21, v29\n\t"
    "v_lshl_add_u32 v20, v20, 7, v27\n\t"
    "v_lshl_add_u32 v2, v21, 4, v20\n\t"
    "v_xor_b32_e32 v3, 64, v2\n\t"
    "v_mad_u32_u24 v20, v28, 3, 2\n\t"
    "v_and_b32_e32 v21, 6, v20\n\t"
    "v_xor_b32_e32 v21, v21, v29\n\t"
    "v_lshl_add_u32 v20, v20, 7, v27\n\t"
    "v_lshl_add_u32 v4, v21, 4, v20\n\t"
    "v_xor_b32_e32 v5, 64, v4\n\t"
    "s_cmp_eq_u32 %[prod], 0\n\t"
    "s_cbranch_scc1 .Lnq_%=\n\t"
    "v_bfe_u32 v20, v31, 1, 3\n\t"
    "v_xor_b32_e32 v20, v20, v29\n\t"
    "v_lshlrev_b32_e32 v20, 4, v20\n\t"
    "v_lshl_or_b32 v20, v28, 7, v20\n\t"
    "v_bfe_u32 v21, v31, 3, 1\n\t"
    "v_lshl_or_b32 v16, v21, 11, v20\n\t"
    "v_add_u32_e32 v16, 0xc000, v16\n\t"
    "v_add_u32_e32 v16, %[rbeg], v16\n\t"
    "v_xor_b32_e32 v17, 64, v16\n\t"
    "s_cmp_lg_u32 s71, 0\n\t"
    "s_cbranch_scc1 .Lqo_%=\n\t"
    "v_lshl_or_b32 v18, v21, 10, v20\n\t"
    "v_add_u32_e32 v18, 0xf000, v18\n\t"
    "v_xor_b32_e32 v19, 64, v18\n\t"
    "s_mul_i32 s60, s70, 6\n\t"
    "s_add_u32 s60, s60, s65\n\t"
    "s_mul_i32 s60, s60, 6\n\t"
    "s_add_u32 s60, s60, s69\n\t"
    "s_lshl_b32 s60, s60, 10\n\t"
    "v_add_u32_e32 v20, s60, v30\n\t"
    "v_mov_b32_e32 v24, v30\n\t"
    "s_waitcnt vmcnt(13)\n\t"
    "ds_read_b128 v[80:83], v18 offset:0\n\t"
    "ds_read_b128 v[84:87], v19 offset:0\n\t"
    "ds_read_b128 v[88:91], v18 offset:2048\n\t"
    "ds_read_b128 v[92:95], v19 offset:2048\n\t"
    "ds_read_b128 v[96:99], v18 offset:4096\n\t"
    "ds_read_b128 v[100:103], v19 offset:4096\n\t"
    "s_branch .Lqa_%=\n\t"
    ".Lqo_%=:\n\t"
    "s_and_b32 s63, s65, 1\n\t"
    "s_lshl_b32 s63, s63, 1\n\t"
    "s_add_u32 s63, s63, s70\n\t"
    "s_lshl_b32 s64, s70, 2\n\t"
    "s_lshl_b32 s62, s70, 4\n\t"
    "s_cmp_lt_u32 s65, 4\n\t"
    "s_cselect_b32 s60, s64, 8\n\t"
    "s_cselect_b32 s63, s65, s63\n\t"
    "s_cselect_b32 s62, s64, s62\n\t"
    "s_cselect_b32 s61, 4, 3\n\t"
    "s_lshl_b32 s62, s62, 1\n\t"
    "s_add_u32 s62, s62, 61440\n\t"
    "s_add_u32 s62, s62, %[rbeg]\n\t"
    "s_mul_i32 s60, s60, 6\n\t"
    "s_add_u32 s60, s60, s69\n\t"
    "s_lshl_b32 s60, s60, 2\n\t"
    "s_add_u32 s60, s60, s63\n\t"
    "s_lshl_b32 s60, s60, 8\n\t"
    "s_add_u32 s60, s60, 73728\n\t"
    "v_and_b32_e32 v25, 3, v31\n\t"
    "v_bfe_u32 v21, v31, 2, 2\n\t"
    "v_lshlrev_b32_e32 v18, s61, v25\n\t"
    "v_lshl_add_u32 v18, v21, 6, v18\n\t"
    "v_lshl_add_u32 v18, v29, 9, v18\n\t"
    "v_add_u32_e32 v18, s62, v18\n\t"
    "v_lshl_add_u32 v24, v25, 2, v21\n\t"
    "v_lshl_add_u32 v24, v29, 4, v24\n\t"
    "v_lshlrev_b32_e32 v24, 4, v24\n\t"
    "v_mul_u32_u24_e32 v21, 0x1800, v21\n\t"
    "v_lshl_add_u32 v20, v25, 4, v21\n\t"
    "v_lshl_add_u32 v20, v29, 6, v20\n\t"
    "v_add_u32_e32 v20, s60, v20\n\t"
    "s_waitcnt vmcnt(13)\n\t"
    "ds_read_b64_tr_b16 v[80:81], v18 offset:0\n\t"
    "ds_read_b64_tr_b16 v[82:83], v18 offset:256\n\t"
    "ds_read_b64_tr_b16 v[84:85], v18 offset:2048\n\t"
    "ds_read_b64_tr_b16 v[86:87], v18 offset:2304\n\t"
    "ds_read_b64_tr_b16 v[88:89], v18 offset:4096\n\t"
    "ds_read_b64_tr_b16 v[90:91], v18 offset:4352\n\t"
    "ds_read_b64_tr_b16 v[92:93], v18 offset:6144\n\t"
    "ds_read_b64_tr_b16 v[94:95], v18 offset:6400\n\t"
    "ds_read_b64_tr_b16 v[96:97], v18 offset:8192\n\t"
    "ds_read_b64_tr_b16 v[98:99], v18 offset:8448\n\t"
    "ds_read_b64_tr_b16 v[100:101], v18 offset:10240\n\t"
    "ds_read_b64_tr_b16 v[102:103], v18 offset:10496\n\t"
    ".Lqa_%=:\n\t"
    "s_waitcnt vmcnt(7)\n\t"
    "ds_read_b128 v[32:35], v16 offset:0\n\t"
    "ds_read_b128 v[36:39], v17 offset:0\n\t"
    "ds_read_b128 v[40:43], v16 offset:4096\n\t"
    "ds_read_b128 v[44:47], v17 offset:4096\n\t"
    "ds_read_b128 v[48:51], v16 offset:8192\n\t"
    "ds_read_b128 v[52:55], v17 offset:8192\n\t"
    "s_waitcnt vmcnt(1)\n\t"
    "ds_read_b128 v[56:59], v16 offset:1024\n\t"
    "ds_read_b128 v[60:63], v17 offset:1024\n\t"
    "ds_read_b128 v[64:67], v16 offset:5120\n\t"
    "ds_read_b128 v[68:71], v17 offset:5120\n\t"
    "ds_read_b128 v[72:75], v16 offset:9216\n\t"
    "ds_read_b128 v[76:79], v17 offset:9216\n\t"
    "s_waitcnt lgkmcnt(11)\n\t"
    "v_mfma_f32_16x16x32_bf16 v[108:111], v[32:35], v[80:83], 0\n\t"
    "s_waitcnt lgkmcnt(10)\n\t"
    "v_mfma_f32_16x16x32_bf16 v[108:111], v[36:39], v[84:87], v[108:111]\n\t"
    "s_waitcnt lgkmcnt(9)\n\t"
    "v_mfma_f32_16x16x32_bf16 v[108:111], v[40:43], v[88:91], v[108:111]\n\t"
    "s_waitcnt lgkmcnt(8)\n\t"
    "v_mfma_f32_16x16x32_bf16 v[108:111], v[44:47], v[92:95], v[108:111]\n\t"
    "s_waitcnt lgkmcnt(7)\n\t"
    "v_mfma_f32_16x16x32_bf16 v[108:111], v[48:51], v[96:99], v[108:111]\n\t"
    "s_waitcnt lgkmcnt(6)\n\t"
    "v_mfma_f32_16x16x32_bf16 v[108:111], v[52:55], v[100:103], v[108:111]\n\t"
    "s_waitcnt lgkmcnt(5)\n\t"
    "v_mfma_f32_16x16x32_bf16 v[112:115], v[56:59], v[80:83], 0\n\t"
    "s_waitcnt lgkmcnt(4)\n\t"
    "v_mfma_f32_16x16x32_bf16 v[112:115], v[60:63], v[84:87], v[112:115]\n\t"
    "s_waitcnt lgkmcnt(3)\n\t"
    "v_mfma_f32_16x16x32_bf16 v[112:115], v[64:67], v[88:91], v[112:115]\n\t"
    "s_waitcnt lgkmcnt(2)\n\t"
    "v_mfma_f32_16x16x32_bf16 v[112:115], v[68:71], v[92:95], v[112:115]\n\t"
    "s_waitcnt lgkmcnt(1)\n\t"
    "v_mfma_f32_16x16x32_bf16 v[112:115], v[72:75], v[96:99], v[112:115]\n\t"
    "s_waitcnt lgkmcnt(0)\n\t"
    "v_mfma_f32_16x16x32_bf16 v[112:115], v[76:79], v[100:103], v[112:115]\n\t"
    "s_cmp_eq_u32 s71, 0\n\t"
    "s_cselect_b32 s60, 0x3dd53b95, 1.0\n\t"
    "v_mul_f32_e32 v108, s60, v108\n\t"
    "v_mul_f32_e32 v109, s60, v109\n\t"
    "v_mul_f32_e32 v110, s60, v110\n\t"
    "v_mul_f32_e32 v111, s60, v111\n\t"
    "s_nop 3\n\t"
    "v_mul_f32_e32 v112, s60, v112\n\t"
    "v_mul_f32_e32 v113, s60, v113\n\t"
    "v_mul_f32_e32 v114, s60, v114\n\t"
    "v_mul_f32_e32 v115, s60, v115\n\t"
    "v_cvt_pk_bf16_f32 v104, v108, v109\n\t"
    "v_cvt_pk_bf16_f32 v105, v110, v111\n\t"
    "s_nop 0\n\t"
    "v_cvt_pk_bf16_f32 v106, v112, v113\n\t"
    "v_cvt_pk_bf16_f32 v107, v114, v115\n\t"
    "v_add_u32_e32 v24, %[rbeg], v24\n\t"
    "v_add_u32_e32 v25, %[rbeg], v30\n\t"
    "ds_write_b128 v24, v[104:107] offset:49152\n\t"
    "ds_read_b128 v[104:107], v25 offset:49152\n\t"
    "s_waitcnt lgkmcnt(0)\n\t"
    "buffer_store_dwordx4 v[104:107], v20, s[56:59], 0 offen sc1\n\t"
    ".Lnq_%=:\n\t"
    "s_waitcnt vmcnt(0)\n\t"
    "v_add_co_u32_e32 v22, vcc, 0x7f4a7c15, v22\n\t"
    "v_mov_b32_e32 v16, 0x9e3779b9\n\t"
    "s_nop 0\n\t"
    "v_addc_co_u32_e32 v23, vcc, v16, v23, vcc\n\t"
    "s_mov_b32 exec_lo, %[prod]\n\t"
    "s_mov_b32 exec_hi, 0\n\t"
    "buffer_store_dwordx2 v[22:23], off, s[56:59], %[foff] sc1\n\t"
    "s_mov_b64 exec, -1\n\t"
    "v_lshrrev_b32_e32 v20, 5, v31\n\t"
    "v_mul_u32_u24_e32 v20, 0x30000, v20\n\t"
    "v_and_b32_e32 v21, 1, v29\n\t"
    "v_mul_u32_u24_e32 v21, 0x600, v21\n\t"
    "v_add_u32_e32 v20, v20, v21\n\t"
    "v_and_b32_e32 v21, 15, v31\n\t"
    "v_lshl_add_u32 v7, v21, 4, v20\n\t"
    "v_lshl_add_u32 v8, v21, 3, v20\n\t"
    "v_add_u32_e32 v8, 0x100, v8\n\t"
    "v_and_b32_e32 v20, 48, v30\n\t"
    "v_lshl_or_b32 v20, v29, 6, v20\n\t"
    "v_lshrrev_b32_e32 v21, 2, v21\n\t"
    "v_lshl_or_b32 v9, v21, 8, v20\n\t"
    "v_min_u32_e32 v21, 35, v31\n\t"
    "v_mul_u32_u24_e32 v21, 64, v21\n\t"
    "v_add_u32_e32 v21, 0x28000, v21\n\t"
    "s_mov_b32 s52, s44\n\t"
    "s_and_b32 s53, s45, 0xffff\n\t"
    "s_mov_b32 s54, 0x6000000\n\t"
    "s_mov_b32 s55, 0x20000\n\t"
    "s_mov_b64 exec, 0\n\t"
    "s_cmp_eq_u32 %[pair], 0\n\t"
    "s_cbranch_scc1 .LrN_%=\n\t"
    "s_branch .Lh6_%=\n\t"
    ".LrN_%=:\n\t"
    "s_mov_b64 exec, -1\n\t"
    "s_cmp_eq_u32 %[pair], 0\n\t"
    "s_cbranch_scc1 .Lng_%=\n\t"
    "s_cmp_lg_u32 %[prod], 0\n\t"
    "s_cbranch_scc1 .Lns_%=\n\t"
    "s_sleep 15\n\t"
    ".Lpt_%=:\n\t"
    "s_memrealtime s[70:71]\n\t"
    "s_waitcnt lgkmcnt(0)\n\t"
    "s_sub_u32 s70, s70, s72\n\t"
    "s_cmp_lt_u32 s70, 168\n\t"
    "s_cbranch_scc1 .Lpt_%=\n\t"
    ".Lns_%=:\n\t"
    "s_mov_b32 s64, 0\n\t"
    "s_mov_b32 s65, 2304\n\t"
    "buffer_load_dwordx2 v[12:13], v21, s[56:59], s64 offen sc1\n\t"
    "buffer_load_dwordx2 v[14:15], v21, s[56:59], s65 offen sc1\n\t"
    "s_waitcnt vmcnt(0)\n\t"
    "s_mov_b32 s60, 0x10000\n\t"
    ".Lp5_%=:\n\t"
    "v_cmp_eq_u64_e32 vcc, v[12:13], v[22:23]\n\t"
    "v_cmp_eq_u64_e64 s[70:71], v[14:15], v[22:23]\n\t"
    "s_and_b64 vcc, vcc, s[70:71]\n\t"
    "s_cmp_eq_u64 vcc, exec\n\t"
    "s_cbranch_scc1 .Lk6_%=\n\t"
    "s_sleep 4\n\t"
    "buffer_load_dwordx2 v[12:13], v21, s[56:59], s64 offen sc1\n\t"
    "buffer_load_dwordx2 v[14:15], v21, s[56:59], s65 offen sc1\n\t"
    "s_sub_u32 s60, s60, 1\n\t"
    "s_cmp_lg_u32 s60, 0\n\t"
    "s_waitcnt vmcnt(0)\n\t"
    "s_cbranch_scc1 .Lp5_%=\n\t"
    ".Lk6_%=:\n\t"
    ".Lng_%=:\n\t"
    "s_mul_i32 s66, %[pair], 36864\n\t"
    "s_mul_i32 s62, %[pair], 73728\n\t"
    "s_add_u32 s62, s62, 36864\n\t"
    "v_add_u32_e32 v11, s62, v30\n\t"
    "s_mov_b32 s63, 73728\n\t"
    "s_barrier\n\t"
    "s_add_u32 s67, %[tch], 0\n\t"
    "s_mov_b64 exec, 0x1ff\n\t"
    "buffer_load_dword v10, v26, s[56:59], s67 offen sc1\n\t"
    "s_mov_b64 exec, -1\n\t"
    "buffer_load_dwordx4 v[32:35], v30, s[56:59], s66 offen offset:0 sc1\n\t"
    "buffer_load_dwordx4 v[36:39], v30, s[56:59], s66 offen offset:1024 sc1\n\t"
    "buffer_load_dwordx4 v[40:43], v30, s[56:59], s66 offen offset:2048 sc1\n\t"
    "buffer_load_dwordx4 v[44:47], v30, s[56:59], s66 offen offset:3072 sc1\n\t"
    "s_add_u32 s66, s66, 4096\n\t"
    "buffer_load_dwordx4 v[48:51], v30, s[56:59], s66 offen offset:0 sc1\n\t"
    "buffer_load_dwordx4 v[52:55], v30, s[56:59], s66 offen offset:1024 sc1\n\t"
    "buffer_load_dwordx4 v[56:59], v30, s[56:59], s66 offen offset:2048 sc1\n\t"
    "buffer_load_dwordx4 v[60:63], v30, s[56:59], s66 offen offset:3072 sc1\n\t"
    "s_add_u32 s66, s66, 4096\n\t"
    "buffer_load_dwordx4 v[64:67], v30, s[56:59], s66 offen offset:0 sc1\n\t"
    "buffer_load_dwordx4 v[68:71], v30, s[56:59], s66 offen offset:1024 sc1\n\t"
    "buffer_load_dwordx4 v[72:75], v30, s[56:59], s66 offen offset:2048 sc1\n\t"
    "buffer_load_dwordx4 v[76:79], v30, s[56:59], s66 offen offset:3072 sc1\n\t"
    "s_add_u32 s66, s66, 4096\n\t"
    "buffer_load_dwordx4 v[80:83], v30, s[56:59], s66 offen offset:0 sc1\n\t"
    "buffer_load_dwordx4 v[84:87], v30, s[56:59], s66 offen offset:1024 sc1\n\t"
    "buffer_load_dwordx4 v[88:91], v30, s[56:59], s66 offen offset:2048 sc1\n\t"
    "buffer_load_dwordx4 v[92:95], v30, s[56:59], s66 offen offset:3072 sc1\n\t"
    "s_add_u32 s66, s66, 4096\n\t"
    "buffer_load_dwordx4 v[96:99], v30, s[56:59], s66 offen offset:0 sc1\n\t"
    "buffer_load_dwordx4 v[100:103], v30, s[56:59], s66 offen offset:1024 sc1\n\t"
    "buffer_load_dwordx4 v[104:107], v30, s[56:59], s66 offen offset:2048 sc1\n\t"
    "buffer_load_dwordx4 v[108:111], v30, s[56:59], s66 offen offset:3072 sc1\n\t"
    "s_add_u32 s66, s66, 4096\n\t"
    "buffer_load_dwordx4 v[112:115], v30, s[56:59], s66 offen offset:0 sc1\n\t"
    "buffer_load_dwordx4 v[116:119], v30, s[56:59], s66 offen offset:1024 sc1\n\t"
    "buffer_load_dwordx4 v[120:123], v30, s[56:59], s66 offen offset:2048 sc1\n\t"
    "buffer_load_dwordx4 v[124:127], v30, s[56:59], s66 offen offset:3072 sc1\n\t"
    "s_add_u32 s66, s66, 4096\n\t"
    "s_mov_b32 s64, 4608\n\t"
    "s_mov_b32 s65, 6912\n\t"
    "buffer_load_dwordx2 v[12:13], v21, s[56:59], s64 offen sc1\n\t"
    "buffer_load_dwordx2 v[14:15], v21, s[56:59], s65 offen sc1\n\t"
    "s_waitcnt vmcnt(25)\n\t"
    "ds_write_b128 v11, v[32:35] offset:0\n\t"
    "buffer_load_dwordx4 v[128:131], v30, s[56:59], s66 offen offset:0 sc1\n\t"
    "s_waitcnt vmcnt(25)\n\t"
    "ds_write_b128 v11, v[36:39] offset:1024\n\t"
    "buffer_load_dwordx4 v[132:135], v30, s[56:59], s66 offen offset:1024 sc1\n\t"
    "s_waitcnt vmcnt(25)\n\t"
    "ds_write_b128 v11, v[40:43] offset:2048\n\t"
    "buffer_load_dwordx4 v[136:139], v30, s[56:59], s66 offen offset:2048 sc1\n\t"
    "s_waitcnt vmcnt(25)\n\t"
    "ds_write_b128 v11, v[44:47] offset:3072\n\t"
    "buffer_load_dwordx4 v[140:143], v30, s[56:59], s66 offen offset:3072 sc1\n\t"
    "s_add_u32 s66, s66, 4096\n\t"
    "s_waitcnt vmcnt(25)\n\t"
    "ds_write_b128 v11, v[48:51] offset:4096\n\t"
    "buffer_load_dwordx4 v[144:147], v30, s[56:59], s66 offen offset:0 sc1\n\t"
    "s_waitcnt vmcnt(25)\n\t"
    "ds_write_b128 v11, v[52:55] offset:5120\n\t"
    "s_waitcnt lgkmcnt(0)\n\t"
    "s_barrier\n\t"
    "buffer_load_dwordx4 v[148:151], v30, s[56:59], s66 offen offset:1024 sc1\n\t"
    "s_waitcnt vmcnt(25)\n\t"
    "ds_write_b128 v11, v[56:59] offset:6144\n\t"
    "buffer_load_dwordx4 v[152:155], v30, s[56:59], s66 offen offset:2048 sc1\n\t"
    "s_waitcnt vmcnt(25)\n\t"
    "ds_write_b128 v11, v[60:63] offset:7168\n\t"
    "buffer_load_dwordx4 v[156:159], v30, s[56:59], s66 offen offset:3072 sc1\n\t"
    "s_add_u32 s66, s66, 4096\n\t"
    "s_waitcnt vmcnt(25)\n\t"
    "ds_write_b128 v11, v[64:67] offset:8192\n\t"
    "buffer_load_dwordx4 v[160:163], v30, s[56:59], s66 offen offset:0 sc1\n\t"
    "s_waitcnt vmcnt(25)\n\t"
    "ds_write_b128 v11, v[68:71] offset:9216\n\t"
    "buffer_load_dwordx4 v[164:167], v30, s[56:59], s66 offen offset:1024 sc1\n\t"
    "s_waitcnt vmcnt(25)\n\t"
    "ds_write_b128 v11, v[72:75] offset:10240\n\t"
    "buffer_load_dwordx4 v[168:171], v30, s[56:59], s66 offen offset:2048 sc1\n\t"
    "s_waitcnt vmcnt(25)\n\t"
    "ds_write_b128 v11, v[76:79] offset:11264\n\t"
    "s_waitcnt lgkmcnt(0)\n\t"
    "s_barrier\n\t"
    "buffer_load_dwordx4 v[172:175], v30, s[56:59], s66 offen offset:3072 sc1\n\t"
    "s_add_u32 s66, s66, 4096\n\t"
    "s_waitcnt vmcnt(25)\n\t"
    "ds_write_b128 v11, v[80:83] offset:12288\n\t"
    "s_waitcnt vmcnt(24)\n\t"
    "ds_write_b128 v11, v[84:87] offset:13312\n\t"
    "s_waitcnt vmcnt(23)\n\t"
    "ds_write_b128 v11, v[88:91] offset:14336\n\t"
    "s_waitcnt vmcnt(22)\n\t"
    "ds_write_b128 v11, v[92:95] offset:15360\n\t"
    "s_waitcnt vmcnt(21)\n\t"
    "ds_write_b128 v11, v[96:99] offset:16384\n\t"
    "s_waitcnt vmcnt(20)\n\t"
    "ds_write_b128 v11, v[100:103] offset:17408\n\t"
    "s_waitcnt vmcnt(19)\n\t"
    "ds_write_b128 v11, v[104:107] offset:18432\n\t"
    "s_waitcnt vmcnt(18)\n\t"
    "ds_write_b128 v11, v[108:111] offset:19456\n\t"
    "s_waitcnt vmcnt(17)\n\t"
    "ds_write_b128 v11, v[112:115] offset:20480\n\t"
    "s_waitcnt vmcnt(16)\n\t"
    "ds_write_b128 v11, v[116:119] offset:21504\n\t"
    "s_waitcnt vmcnt(15)\n\t"
    "ds_write_b128 v11, v[120:123] offset:22528\n\t"
    "s_waitcnt vmcnt(14)\n\t"
    "ds_write_b128 v11, v[124:127] offset:23552\n\t"
    "s_waitcnt lgkmcnt(0)\n\t"
    "s_barrier\n\t"
    "s_waitcnt vmcnt(12)\n\t"
    "s_mov_b32 s60, 0x10000\n\t"
    ".Lp7_%=:\n\t"
    "v_cmp_eq_u64_e32 vcc, v[12:13], v[22:23]\n\t"
    "v_cmp_eq_u64_e64 s[70:71], v[14:15], v[22:23]\n\t"
    "s_and_b64 vcc, vcc, s[70:71]\n\t"
    "s_cmp_eq_u64 vcc, exec\n\t"
    "s_cbranch_scc1 .Lk8_%=\n\t"
    "s_sleep 4\n\t"
    "buffer_load_dwordx2 v[12:13], v21, s[56:59], s64 offen sc1\n\t"
    "buffer_load_dwordx2 v[14:15], v21, s[56:59], s65 offen sc1\n\t"
    "s_sub_u32 s60, s60, 1\n\t"
    "s_cmp_lg_u32 s60, 0\n\t"
    "s_waitcnt vmcnt(0)\n\t"
    "s_cbranch_scc1 .Lp7_%=\n\t"
    ".Lk8_%=:\n\t"
    "s_add_u32 s67, %[tch], 73728\n\t"
    "s_mov_b64 exec, 0x1ff\n\t"
    "buffer_load_dword v10, v26, s[56:59], s67 offen sc1\n\t"
    "s_mov_b64 exec, -1\n\t"
    "buffer_load_dwordx4 a[0:3], v9, s[56:59], s63 offen offset:0 sc1\n\t"
    "buffer_load_dwordx4 a[4:7], v9, s[56:59], s63 offen offset:1024 sc1\n\t"
    "buffer_load_dwordx4 a[8:11], v9, s[56:59], s63 offen offset:2048 sc1\n\t"
    "buffer_load_dwordx4 a[12:15], v9, s[56:59], s63 offen offset:3072 sc1\n\t"
    "s_add_u32 s63, s63, 4096\n\t"
    "s_waitcnt vmcnt(16)\n\t"
    "ds_write_b128 v11, v[128:131] offset:24576\n\t"
    "buffer_load_dwordx4 a[16:19], v9, s[56:59], s63 offen offset:0 sc1\n\t"
    "s_waitcnt vmcnt(16)\n\t"
    "ds_write_b128 v11, v[132:135] offset:25600\n\t"
    "buffer_load_dwordx4 a[20:23], v9, s[56:59], s63 offen offset:1024 sc1\n\t"
    "s_waitcnt vmcnt(16)\n\t"
    "ds_write_b128 v11, v[136:139] offset:26624\n\t"
    "buffer_load_dwordx4 a[24:27], v9, s[56:59], s63 offen offset:2048 sc1\n\t"
    "s_waitcnt vmcnt(16)\n\t"
    "ds_write_b128 v11, v[140:143] offset:27648\n\t"
    "buffer_load_dwordx4 a[28:31], v9, s[56:59], s63 offen offset:3072 sc1\n\t"
    "s_add_u32 s63, s63, 4096\n\t"
    "s_waitcnt vmcnt(16)\n\t"
    "ds_write_b128 v11, v[144:147] offset:28672\n\t"
    "buffer_load_dwordx4 a[32:35], v9, s[56:59], s63 offen offset:0 sc1\n\t"
    "s_waitcnt vmcnt(16)\n\t"
    "ds_write_b128 v11, v[148:151] offset:29696\n\t"
    "buffer_load_dwordx4 a[36:39], v9, s[56:59], s63 offen offset:1024 sc1\n\t"
    "s_waitcnt vmcnt(16)\n\t"
    "ds_write_b128 v11, v[152:155] offset:30720\n\t"
    "buffer_load_dwordx4 a[40:43], v9, s[56:59], s63 offen offset:2048 sc1\n\t"
    "s_waitcnt vmcnt(16)\n\t"
    "ds_write_b128 v11, v[156:159] offset:31744\n\t"
    "buffer_load_dwordx4 a[44:47], v9, s[56:59], s63 offen offset:3072 sc1\n\t"
    "s_add_u32 s63, s63, 4096\n\t"
    "s_waitcnt vmcnt(16)\n\t"
    "ds_write_b128 v11, v[160:163] offset:32768\n\t"
    "buffer_load_dwordx4 a[48:51], v9, s[56:59], s63 offen offset:0 sc1\n\t"
    "s_waitcnt vmcnt(16)\n\t"
    "ds_write_b128 v11, v[164:167] offset:33792\n\t"
    "buffer_load_dwordx4 a[52:55], v9, s[56:59], s63 offen offset:1024 sc1\n\t"
    "s_waitcnt vmcnt(16)\n\t"
    "ds_write_b128 v11, v[168:171] offset:34816\n\t"
    "buffer_load_dwordx4 a[56:59], v9, s[56:59], s63 offen offset:2048 sc1\n\t"
    "s_waitcnt vmcnt(16)\n\t"
    "ds_write_b128 v11, v[172:175] offset:35840\n\t"
    "s_waitcnt lgkmcnt(0)\n\t"
    "s_barrier\n\t"
    "buffer_load_dwordx4 a[60:63], v9, s[56:59], s63 offen offset:3072 sc1\n\t"
    "s_add_u32 s63, s63, 4096\n\t"
    "buffer_load_dwordx4 a[64:67], v9, s[56:59], s63 offen offset:0 sc1\n\t"
    "buffer_load_dwordx4 a[68:71], v9, s[56:59], s63 offen offset:1024 sc1\n\t"
    "buffer_load_dwordx4 a[72:75], v9, s[56:59], s63 offen offset:2048 sc1\n\t"
    "buffer_load_dwordx4 a[76:79], v9, s[56:59], s63 offen offset:3072 sc1\n\t"
    "s_add_u32 s63, s63, 4096\n\t"
    "buffer_load_dwordx4 a[80:83], v9, s[56:59], s63 offen offset:0 sc1\n\t"
    "buffer_load_dwordx4 a[84:87], v9, s[56:59], s63 offen offset:1024 sc1\n\t"
    "buffer_load_dwordx4 a[88:91], v9, s[56:59], s63 offen offset:2048 sc1\n\t"
    "buffer_load_dwordx4 a[92:95], v9, s[56:59], s63 offen offset:3072 sc1\n\t"
    "s_add_u32 s63, s63, 4096\n\t"
    "buffer_load_dwordx4 a[96:99], v9, s[56:59], s63 offen offset:0 sc1\n\t"
    "buffer_load_dwordx4 a[100:103], v9, s[56:59], s63 offen offset:1024 sc1\n\t"
    "buffer_load_dwordx4 a[104:107], v9, s[56:59], s63 offen offset:2048 sc1\n\t"
    "buffer_load_dwordx4 a[108:111], v9, s[56:59], s63 offen offset:3072 sc1\n\t"
    "s_add_u32 s63, s63, 4096\n\t"
    "buffer_load_dwordx4 a[112:115], v9, s[56:59], s63 offen offset:0 sc1\n\t"
    "buffer_load_dwordx4 a[116:119], v9, s[56:59], s63 offen offset:1024 sc1\n\t"
    "buffer_load_dwordx4 a[120:123], v9, s[56:59], s63 offen offset:2048 sc1\n\t"
    "buffer_load_dwordx4 a[124:127], v9, s[56:59], s63 offen offset:3072 sc1\n\t"
    "s_add_u32 s63, s63, 4096\n\t"
    "buffer_load_dwordx4 a[128:131], v9, s[56:59], s63 offen offset:0 sc1\n\t"
    "buffer_load_dwordx4 a[132:135], v9, s[56:59], s63 offen offset:1024 sc1\n\t"
    "buffer_load_dwordx4 a[136:139], v9, s[56:59], s63 offen offset:2048 sc1\n\t"
    "buffer_load_dwordx4 a[140:143], v9, s[56:59], s63 offen offset:3072 sc1\n\t"
    "s_add_u32 s63, s63, 4096\n\t"
    "buffer_load_dwordx4 a[144:147], v9, s[56:59], s63 offen offset:0 sc1\n\t"
    "buffer_load_dwordx4 a[148:151], v9, s[56:59], s63 offen offset:1024 sc1\n\t"
    "buffer_load_dwordx4 a[152:155], v9, s[56:59], s63 offen offset:2048 sc1\n\t"
    "buffer_load_dwordx4 a[156:159], v9, s[56:59], s63 offen offset:3072 sc1\n\t"
    "s_add_u32 s63, s63, 4096\n\t"
    "buffer_load_dwordx4 a[160:163], v9, s[56:59], s63 offen offset:0 sc1\n\t"
    "buffer_load_dwordx4 a[164:167], v9, s[56:59], s63 offen offset:1024 sc1\n\t"
    "buffer_load_dwordx4 a[168:171], v9, s[56:59], s63 offen offset:2048 sc1\n\t"
    "buffer_load_dwordx4 a[172:175], v9, s[56:59], s63 offen offset:3072 sc1\n\t"
    "s_add_u32 s63, s63, 4096\n\t"
    "buffer_load_dwordx4 a[176:179], v9, s[56:59], s63 offen offset:0 sc1\n\t"
    "buffer_load_dwordx4 a[180:183], v9, s[56:59], s63 offen offset:1024 sc1\n\t"
    "buffer_load_dwordx4 a[184:187], v9, s[56:59], s63 offen offset:2048 sc1\n\t"
    "buffer_load_dwordx4 a[188:191], v9, s[56:59], s63 offen offset:3072 sc1\n\t"
    "s_add_u32 s63, s63, 4096\n\t"
    "buffer_load_dwordx4 a[192:195], v9, s[56:59], s63 offen offset:0 sc1\n\t"
    "buffer_load_dwordx4 a[196:199], v9, s[56:59], s63 offen offset:1024 sc1\n\t"
    "buffer_load_dwordx4 a[200:203], v9, s[56:59], s63 offen offset:2048 sc1\n\t"
    "buffer_load_dwordx4 a[204:207], v9, s[56:59], s63 offen offset:3072 sc1\n\t"
    "s_add_u32 s63, s63, 4096\n\t"
    "buffer_load_dwordx4 a[208:211], v9, s[56:59], s63 offen offset:0 sc1\n\t"
    "buffer_load_dwordx4 a[212:215], v9, s[56:59], s63 offen offset:1024 sc1\n\t"
    "buffer_load_dwordx4 a[216:219], v9, s[56:59], s63 offen offset:2048 sc1\n\t"
    "buffer_load_dwordx4 a[220:223], v9, s[56:59], s63 offen offset:3072 sc1\n\t"
    "s_add_u32 s63, s63, 4096\n\t"
    "buffer_load_dwordx4 a[224:227], v9, s[56:59], s63 offen offset:0 sc1\n\t"
    "buffer_load_dwordx4 a[228:231], v9, s[56:59], s63 offen offset:1024 sc1\n\t"
    ".Lh1_%=:\n\t"
    "s_cbranch_execz .Lh2_%=\n\t"
    "buffer_load_dwordx4 a[232:235], v9, s[56:59], s63 offen offset:2048 sc1\n\t"
    "buffer_load_dwordx4 a[236:239], v9, s[56:59], s63 offen offset:3072 sc1\n\t"
    "s_add_u32 s63, s63, 4096\n\t"
    "buffer_load_dwordx4 a[240:243], v9, s[56:59], s63 offen offset:0 sc1\n\t"
    "buffer_load_dwordx4 a[244:247], v9, s[56:59], s63 offen offset:1024 sc1\n\t"
    "buffer_load_dwordx4 a[248:251], v9, s[56:59], s63 offen offset:2048 sc1\n\t"
    "buffer_load_dwordx4 a[252:255], v9, s[56:59], s63 offen offset:3072 sc1\n\t"
    "s_add_u32 s63, s63, 4096\n\t"
    "buffer_load_dwordx4 v[224:227], v9, s[56:59], s63 offen offset:0 sc1\n\t"
    "buffer_load_dwordx4 v[228:231], v9, s[56:59], s63 offen offset:1024 sc1\n\t"
    "buffer_load_dwordx4 v[232:235], v9, s[56:59], s63 offen offset:2048 sc1\n\t"
    "buffer_load_dwordx4 v[236:239], v9, s[56:59], s63 offen offset:3072 sc1\n\t"
    "s_add_u32 s63, s63, 4096\n\t"
    "buffer_load_dwordx4 v[240:243], v9, s[56:59], s63 offen offset:0 sc1\n\t"
    "buffer_load_dwordx4 v[244:247], v9, s[56:59], s63 offen offset:1024 sc1\n\t"
    "buffer_load_dwordx4 v[248:251], v9, s[56:59], s63 offen offset:2048 sc1\n\t"
    "buffer_load_dwordx4 v[252:255], v9, s[56:59], s63 offen offset:3072 sc1\n\t"
    "s_add_u32 s63, s63, 4096\n\t"
    "s_barrier\n\t"
    "s_mov_b64 exec, %[amask]\n\t"
    "buffer_store_dwordx2 v[22:23], off, s[56:59], %[aown] sc1\n\t"
    "s_mov_b64 exec, -1\n\t"
    "ds_read_b128 v[60:63], v0 offset:0\n\t"
    "ds_read_b128 v[64:67], v1 offset:0\n\t"
    "ds_read_b128 v[68:71], v2 offset:0\n\t"
    "ds_read_b128 v[72:75], v3 offset:0\n\t"
    "ds_read_b128 v[188:191], v4 offset:0\n\t"
    "ds_read_b128 v[76:79], v5 offset:0\n\t"
    "ds_read_b128 v[80:83], v5 offset:6144\n\t"
    "ds_read_b128 v[108:111], v4 offset:6144\n\t"
    "ds_read_b128 v[84:87], v3 offset:6144\n\t"
    "ds_read_b128 v[88:91], v2 offset:6144\n\t"
    "ds_read_b128 v[144:147], v1 offset:6144\n\t"
    "ds_read_b128 v[196:199], v0 offset:6144\n\t"
    "s_waitcnt lgkmcnt(0)\n\t"
    "s_add_u32 s61, %[wb], 0xff400000\n\t"
    "s_mov_b32 s62, %[rbeg]\n\t"
    "s_mov_b32 s60, 8\n\t"
    ".Lmin_%=:\n\t"
    "s_waitcnt lgkmcnt(3)\n\t"
    "s_waitcnt vmcnt(63)\n\t"
    "v_mfma_f32_16x16x32_bf16 v[24:27], v[60:63], a[0:3], 0\n\t"
    "v_cvt_pk_bf16_f32 v159, v30, v34\n\t"
    "v_cvt_pk_bf16_f32 v187, v31, v35\n\t"
    "v_mfma_f32_16x16x32_bf16 v[24:27], v[64:67], a[4:7], v[24:27]\n\t"
    "v_cvt_pk_bf16_f32 v104, v36, v12\n\t"
    "v_cvt_pk_bf16_f32 v106, v37, v13\n\t"
    "v_mfma_f32_16x16x32_bf16 v[24:27], v[68:71], a[8:11], v[24:27]\n\t"
    "v_cvt_pk_bf16_f32 v180, v38, v14\n\t"
    "v_cvt_pk_bf16_f32 v182, v39, v15\n\t"
    "v_mfma_f32_16x16x32_bf16 v[24:27], v[72:75], a[12:15], v[24:27]\n\t"
    "v_cvt_pk_bf16_f32 v105, v16, v20\n\t"
    "v_cvt_pk_bf16_f32 v107, v17, v21\n\t"
    "v_mfma_f32_16x16x32_bf16 v[24:27], v[188:191], a[16:19], v[24:27]\n\t"
    "v_cvt_pk_bf16_f32 v181, v18, v22\n\t"
    "v_cvt_pk_bf16_f32 v183, v19, v23\n\t"
    "v_mfma_f32_16x16x32_bf16 v[24:27], v[76:79], a[20:23], v[24:27]\n\t"
    "v_mfma_f32_16x16x32_bf16 v[28:31], v[80:83], a[20:23], 0\n\t"
    "v_mfma_f32_16x16x32_bf16 v[28:31], v[108:111], a[16:19], v[28:31]\n\t"
    "v_mfma_f32_16x16x32_bf16 v[28:31], v[84:87], a[12:15], v[28:31]\n\t"
    "s_waitcnt lgkmcnt(0)\n\t"
    "v_mfma_f32_16x16x32_bf16 v[28:31], v[88:91], a[8:11], v[28:31]\n\t"
    "v_cvt_pk_bf16_f32 v12, v24, v25\n\t"
    "v_cvt_pk_bf16_f32 v13, v26, v27\n\t"
    "v_mfma_f32_16x16x32_bf16 v[28:31], v[144:147], a[4:7], v[28:31]\n\t"
    "buffer_store_dwordx2 v[154:155], v8, s[52:55], s61 offen offset:1152 sc1\n\t"
    "v_mfma_f32_16x16x32_bf16 v[28:31], v[196:199], a[0:3], v[28:31]\n\t"
    "s_add_u32 s61, s61, 393216\n\t"
    "s_waitcnt vmcnt(62)\n\t"
    "v_mfma_f32_16x16x32_bf16 v[20:23], v[196:199], a[24:27], 0\n\t"
    "v_mfma_f32_16x16x32_bf16 v[20:23], v[144:147], a[28:31], v[20:23]\n\t"
    "v_mfma_f32_16x16x32_bf16 v[20:23], v[88:91], a[32:35], v[20:23]\n\t"
    "v_mfma_f32_16x16x32_bf16 v[20:23], v[84:87], a[36:39], v[20:23]\n\t"
    "v_cvt_pk_bf16_f32 v14, v28, v29\n\t"
    "v_cvt_pk_bf16_f32 v15, v30, v31\n\t"
    "v_mfma_f32_16x16x32_bf16 v[20:23], v[108:111], a[40:43], v[20:23]\n\t"
    "v_mfma_f32_16x16x32_bf16 v[20:23], v[80:83], a[44:47], v[20:23]\n\t"
    "v_mfma_f32_16x16x32_bf16 v[24:27], v[76:79], a[44:47], 0\n\t"
    "v_mfma_f32_16x16x32_bf16 v[24:27], v[188:191], a[40:43], v[24:27]\n\t"
    "v_mfma_f32_16x16x32_bf16 v[24:27], v[72:75], a[36:39], v[24:27]\n\t"
    "v_mfma_f32_16x16x32_bf16 v[24:27], v[68:71], a[32:35], v[24:27]\n\t"
    "v_cvt_pk_bf16_f32 v18, v20, v21\n\t"
    "v_cvt_pk_bf16_f32 v19, v22, v23\n\t"
    "v_mfma_f32_16x16x32_bf16 v[24:27], v[64:67], a[28:31], v[24:27]\n\t"
    "buffer_store_dwordx4 v[136:139], v7, s[52:55], s61 offen offset:0 sc1\n\t"
    "v_mfma_f32_16x16x32_bf16 v[24:27], v[60:63], a[24:27], v[24:27]\n\t"
    "s_waitcnt vmcnt(57)\n\t"
    "v_mfma_f32_16x16x32_bf16 v[28:31], v[60:63], a[48:51], 0\n\t"
    "v_mfma_f32_16x16x32_bf16 v[28:31], v[64:67], a[52:55], v[28:31]\n\t"
    "v_mfma_f32_16x16x32_bf16 v[28:31], v[68:71], a[56:59], v[28:31]\n\t"
    "v_mfma_f32_16x16x32_bf16 v[28:31], v[72:75], a[60:63], v[28:31]\n\t"
    "v_cvt_pk_bf16_f32 v16, v24, v25\n\t"
    "v_cvt_pk_bf16_f32 v17, v26, v27\n\t"
    "v_mfma_f32_16x16x32_bf16 v[28:31], v[188:191], a[64:67], v[28:31]\n\t"
    "v_mfma_f32_16x16x32_bf16 v[28:31], v[76:79], a[68:71], v[28:31]\n\t"
    "v_mfma_f32_16x16x32_bf16 v[24:27], v[80:83], a[68:71], 0\n\t"
    "v_mfma_f32_16x16x32_bf16 v[24:27], v[108:111], a[64:67], v[24:27]\n\t"
    "v_mfma_f32_16x16x32_bf16 v[24:27], v[84:87], a[60:63], v[24:27]\n\t"
    "v_mfma_f32_16x16x32_bf16 v[24:27], v[88:91], a[56:59], v[24:27]\n\t"
    "v_cvt_pk_bf16_f32 v20, v28, v29\n\t"
    "v_cvt_pk_bf16_f32 v21, v30, v31\n\t"
    "v_mfma_f32_16x16x32_bf16 v[24:27], v[144:147], a[52:55], v[24:27]\n\t"
    "buffer_store_dwordx4 v[132:135], v7, s[52:55], s61 offen offset:384 sc1\n\t"
    "v_mfma_f32_16x16x32_bf16 v[24:27], v[196:199], a[48:51], v[24:27]\n\t"
    "s_waitcnt vmcnt(52)\n\t"
    "v_mfma_f32_16x16x32_bf16 v[28:31], v[196:199], a[72:75], 0\n\t"
    "v_mfma_f32_16x16x32_bf16 v[28:31], v[144:147], a[76:79], v[28:31]\n\t"
    "v_mfma_f32_16x16x32_bf16 v[28:31], v[88:91], a[80:83], v[28:31]\n\t"
    "v_mfma_f32_16x16x32_bf16 v[28:31], v[84:87], a[84:87], v[28:31]\n\t"
    "v_cvt_pk_bf16_f32 v22, v24, v25\n\t"
    "v_cvt_pk_bf16_f32 v23, v26, v27\n\t"
    "v_mfma_f32_16x16x32_bf16 v[28:31], v[108:111], a[88:91], v[28:31]\n\t"
    "v_mfma_f32_16x16x32_bf16 v[28:31], v[80:83], a[92:95], v[28:31]\n\t"
    "v_mfma_f32_16x16x32_bf16 v[32:35], v[76:79], a[92:95], 0\n\t"
    "v_mfma_f32_16x16x32_bf16 v[32:35], v[188:191], a[88:91], v[32:35]\n\t"
    "v_mfma_f32_16x16x32_bf16 v[32:35], v[72:75], a[84:87], v[32:35]\n\t"
    "v_mfma_f32_16x16x32_bf16 v[32:35], v[68:71], a[80:83], v[32:35]\n\t"
    "v_cvt_pk_bf16_f32 v26, v28, v29\n\t"
    "v_cvt_pk_bf16_f32 v27, v30, v31\n\t"
    "v_mfma_f32_16x16x32_bf16 v[32:35], v[64:67], a[76:79], v[32:35]\n\t"
    "buffer_store_dwordx4 v[164:167], v7, s[52:55], s61 offen offset:768 sc1\n\t"
    "v_mfma_f32_16x16x32_bf16 v[32:35], v[60:63], a[72:75], v[32:35]\n\t"
    "s_waitcnt vmcnt(47)\n\t"
    "v_mfma_f32_16x16x32_bf16 v[36:39], v[60:63], a[96:99], 0\n\t"
    "v_mfma_f32_16x16x32_bf16 v[36:39], v[64:67], a[100:103], v[36:39]\n\t"
    "v_mfma_f32_16x16x32_bf16 v[36:39], v[68:71], a[104:107], v[36:39]\n\t"
    "v_mfma_f32_16x16x32_bf16 v[36:39], v[72:75], a[108:111], v[36:39]\n\t"
    "v_cvt_pk_bf16_f32 v24, v32, v33\n\t"
    "v_cvt_pk_bf16_f32 v25, v34, v35\n\t"
    "v_mfma_f32_16x16x32_bf16 v[36:39], v[188:191], a[112:115], v[36:39]\n\t"
    "v_mfma_f32_16x16x32_bf16 v[36:39], v[76:79], a[116:119], v[36:39]\n\t"
    "v_mfma_f32_16x16x32_bf16 v[32:35], v[80:83], a[116:119], 0\n\t"
    "v_mfma_f32_16x16x32_bf16 v[32:35], v[108:111], a[112:115], v[32:35]\n\t"
    "v_mfma_f32_16x16x32_bf16 v[32:35], v[84:87], a[108:111], v[32:35]\n\t"
    "v_mfma_f32_16x16x32_bf16 v[32:35], v[88:91], a[104:107], v[32:35]\n\t"
    "v_cvt_pk_bf16_f32 v28, v36, v37\n\t"
    "v_cvt_pk_bf16_f32 v29, v38, v39\n\t"
    "v_mfma_f32_16x16x32_bf16 v[32:35], v[144:147], a[100:103], v[32:35]\n\t"
    "buffer_store_dwordx4 v[140:143], v7, s[52:55], s61 offen offset:1152 sc1\n\t"
    "v_mfma_f32_16x16x32_bf16 v[32:35], v[196:199], a[96:99], v[32:35]\n\t"
    "s_waitcnt vmcnt(42)\n\t"
    "v_mfma_f32_16x16x32_bf16 v[36:39], v[196:199], a[120:123], 0\n\t"
    "v_mfma_f32_16x16x32_bf16 v[36:39], v[144:147], a[124:127], v[36:39]\n\t"
    "v_mfma_f32_16x16x32_bf16 v[36:39], v[88:91], a[128:131], v[36:39]\n\t"
    "v_mfma_f32_16x16x32_bf16 v[36:39], v[84:87], a[132:135], v[36:39]\n\t"
    "v_cvt_pk_bf16_f32 v30, v32, v33\n\t"
    "v_cvt_pk_bf16_f32 v31, v34, v35\n\t"
    "v_mfma_f32_16x16x32_bf16 v[36:39], v[108:111], a[136:139], v[36:39]\n\t"
    "v_mfma_f32_16x16x32_bf16 v[36:39], v[80:83], a[140:143], v[36:39]\n\t"
    "v_mfma_f32_16x16x32_bf16 v[40:43], v[76:79], a[140:143], 0\n\t"
    "v_mfma_f32_16x16x32_bf16 v[40:43], v[188:191], a[136:139], v[40:43]\n\t"
    "v_mfma_f32_16x16x32_bf16 v[40:43], v[72:75], a[132:135], v[40:43]\n\t"
    "v_mfma_f32_16x16x32_bf16 v[40:43], v[68:71], a[128:131], v[40:43]\n\t"
    "v_cvt_pk_bf16_f32 v34, v36, v37\n\t"
    "v_cvt_pk_bf16_f32 v35, v38, v39\n\t"
    "v_mfma_f32_16x16x32_bf16 v[40:43], v[64:67], a[124:127], v[40:43]\n\t"
    "buffer_store_dwordx2 v[148:149], v8, s[52:55], s61 offen offset:0 sc1\n\t"
    "v_mfma_f32_16x16x32_bf16 v[40:43], v[60:63], a[120:123], v[40:43]\n\t"
    "s_waitcnt vmcnt(37)\n\t"
    "v_mfma_f32_16x16x32_bf16 v[44:47], v[60:63], a[144:147], 0\n\t"
    "v_mfma_f32_16x16x32_bf16 v[44:47], v[64:67], a[148:151], v[44:47]\n\t"
    "v_mfma_f32_16x16x32_bf16 v[44:47], v[68:71], a[152:155], v[44:47]\n\t"
    "v_mfma_f32_16x16x32_bf16 v[44:47], v[72:75], a[156:159], v[44:47]\n\t"
    "v_cvt_pk_bf16_f32 v32, v40, v41\n\t"
    "v_cvt_pk_bf16_f32 v33, v42, v43\n\t"
    "v_mfma_f32_16x16x32_bf16 v[44:47], v[188:191], a[160:163], v[44:47]\n\t"
    "v_mfma_f32_16x16x32_bf16 v[44:47], v[76:79], a[164:167], v[44:47]\n\t"
    "v_mfma_f32_16x16x32_bf16 v[40:43], v[80:83], a[164:167], 0\n\t"
    "v_mfma_f32_16x16x32_bf16 v[40:43], v[108:111], a[160:163], v[40:43]\n\t"
    "v_mfma_f32_16x16x32_bf16 v[40:43], v[84:87], a[156:159], v[40:43]\n\t"
    "v_mfma_f32_16x16x32_bf16 v[40:43], v[88:91], a[152:155], v[40:43]\n\t"
    "v_cvt_pk_bf16_f32 v36, v44, v45\n\t"
    "v_cvt_pk_bf16_f32 v37, v46, v47\n\t"
    "v_mfma_f32_16x16x32_bf16 v[40:43], v[144:147], a[148:151], v[40:43]\n\t"
    "buffer_store_dwordx2 v[150:151], v8, s[52:55], s61 offen offset:384 sc1\n\t"
    "v_mfma_f32_16x16x32_bf16 v[40:43], v[196:199], a[144:147], v[40:43]\n\t"
    "s_waitcnt vmcnt(32)\n\t"
    "v_mfma_f32_16x16x32_bf16 v[44:47], v[196:199], a[168:171], 0\n\t"
    "v_mfma_f32_16x16x32_bf16 v[44:47], v[144:147], a[172:175], v[44:47]\n\t"
    "v_mfma_f32_16x16x32_bf16 v[44:47], v[88:91], a[176:179], v[44:47]\n\t"
    "v_mfma_f32_16x16x32_bf16 v[44:47], v[84:87], a[180:183], v[44:47]\n\t"
    "v_cvt_pk_bf16_f32 v38, v40, v41\n\t"
    "v_cvt_pk_bf16_f32 v39, v42, v43\n\t"
    "v_mfma_f32_16x16x32_bf16 v[44:47], v[108:111], a[184:187], v[44:47]\n\t"
    "v_mfma_f32_16x16x32_bf16 v[44:47], v[80:83], a[188:191], v[44:47]\n\t"
    "v_mfma_f32_16x16x32_bf16 v[48:51], v[76:79], a[188:191], 0\n\t"
    "v_mfma_f32_16x16x32_bf16 v[48:51], v[188:191], a[184:187], v[48:51]\n\t"
    "v_mfma_f32_16x16x32_bf16 v[48:51], v[72:75], a[180:183], v[48:51]\n\t"
    "v_mfma_f32_16x16x32_bf16 v[48:51], v[68:71], a[176:179], v[48:51]\n\t"
    "v_cvt_pk_bf16_f32 v42, v44, v45\n\t"
    "v_cvt_pk_bf16_f32 v43, v46, v47\n\t"
    "v_mfma_f32_16x16x32_bf16 v[48:51], v[64:67], a[172:175], v[48:51]\n\t"
    "buffer_store_dwordx2 v[176:177], v8, s[52:55], s61 offen offset:768 sc1\n\t"
    "v_mfma_f32_16x16x32_bf16 v[48:51], v[60:63], a[168:171], v[48:51]\n\t"
    "s_waitcnt vmcnt(27)\n\t"
    "v_mfma_f32_16x16x32_bf16 v[52:55], v[60:63], a[192:195], 0\n\t"
    "v_mfma_f32_16x16x32_bf16 v[52:55], v[64:67], a[196:199], v[52:55]\n\t"
    "v_mfma_f32_16x16x32_bf16 v[52:55], v[68:71], a[200:203], v[52:55]\n\t"
    "v_mfma_f32_16x16x32_bf16 v[52:55], v[72:75], a[204:207], v[52:55]\n\t"
    "v_cvt_pk_bf16_f32 v40, v48, v49\n\t"
    "v_cvt_pk_bf16_f32 v41, v50, v51\n\t"
    "v_mfma_f32_16x16x32_bf16 v[52:55], v[188:191], a[208:211], v[52:55]\n\t"
    "v_mfma_f32_16x16x32_bf16 v[52:55], v[76:79], a[212:215], v[52:55]\n\t"
    "v_mfma_f32_16x16x32_bf16 v[48:51], v[80:83], a[212:215], 0\n\t"
    "v_mfma_f32_16x16x32_bf16 v[48:51], v[108:111], a[208:211], v[48:51]\n\t"
    "v_mfma_f32_16x16x32_bf16 v[48:51], v[84:87], a[204:207], v[48:51]\n\t"
    "v_mfma_f32_16x16x32_bf16 v[48:51], v[88:91], a[200:203], v[48:51]\n\t"
    "v_cvt_pk_bf16_f32 v44, v52, v53\n\t"
    "v_cvt_pk_bf16_f32 v45, v54, v55\n\t"
    "v_mfma_f32_16x16x32_bf16 v[48:51], v[144:147], a[196:199], v[48:51]\n\t"
    "buffer_store_dwordx2 v[178:179], v8, s[52:55], s61 offen offset:1152 sc1\n\t"
    "v_mfma_f32_16x16x32_bf16 v[48:51], v[196:199], a[192:195], v[48:51]\n\t"
    "s_add_u32 s61, s61, 393216\n\t"
    "s_waitcnt vmcnt(22)\n\t"
    "v_mfma_f32_16x16x32_bf16 v[52:55], v[196:199], a[216:219], 0\n\t"
    "v_mfma_f32_16x16x32_bf16 v[52:55], v[144:147], a[220:223], v[52:55]\n\t"
    "v_mfma_f32_16x16x32_bf16 v[52:55], v[88:91], a[224:227], v[52:55]\n\t"
    "v_mfma_f32_16x16x32_bf16 v[52:55], v[84:87], a[228:231], v[52:55]\n\t"
    "v_cvt_pk_bf16_f32 v46, v48, v49\n\t"
    "v_cvt_pk_bf16_f32 v47, v50, v51\n\t"
    "v_mfma_f32_16x16x32_bf16 v[52:55], v[108:111], a[232:235], v[52:55]\n\t"
    "v_mfma_f32_16x16x32_bf16 v[52:55], v[80:83], a[236:239], v[52:55]\n\t"
    "v_mfma_f32_16x16x32_bf16 v[56:59], v[76:79], a[236:239], 0\n\t"
    "v_mfma_f32_16x16x32_bf16 v[56:59], v[188:191], a[232:235], v[56:59]\n\t"
    "v_mfma_f32_16x16x32_bf16 v[56:59], v[72:75], a[228:231], v[56:59]\n\t"
    "v_mfma_f32_16x16x32_bf16 v[56:59], v[68:71], a[224:227], v[56:59]\n\t"
    "v_cvt_pk_bf16_f32 v50, v52, v53\n\t"
    "v_cvt_pk_bf16_f32 v51, v54, v55\n\t"
    "v_mfma_f32_16x16x32_bf16 v[56:59], v[64:67], a[220:223], v[56:59]\n\t"
    "buffer_store_dwordx4 v[160:163], v7, s[52:55], s61 offen offset:0 sc1\n\t"
    "v_mfma_f32_16x16x32_bf16 v[56:59], v[60:63], a[216:219], v[56:59]\n\t"
    "s_waitcnt vmcnt(17)\n\t"
    "v_mfma_f32_16x16x32_bf16 v[92:95], v[60:63], a[240:243], 0\n\t"
    "v_mfma_f32_16x16x32_bf16 v[92:95], v[64:67], a[244:247], v[92:95]\n\t"
    "v_mfma_f32_16x16x32_bf16 v[92:95], v[68:71], a[248:251], v[92:95]\n\t"
    "v_mfma_f32_16x16x32_bf16 v[92:95], v[72:75], a[252:255], v[92:95]\n\t"
    "v_cvt_pk_bf16_f32 v48, v56, v57\n\t"
    "v_cvt_pk_bf16_f32 v49, v58, v59\n\t"
    "v_mfma_f32_16x16x32_bf16 v[92:95], v[188:191], v[224:227], v[92:95]\n\t"
    "v_mfma_f32_16x16x32_bf16 v[92:95], v[76:79], v[228:231], v[92:95]\n\t"
    "v_mfma_f32_16x16x32_bf16 v[56:59], v[80:83], v[228:231], 0\n\t"
    "v_mfma_f32_16x16x32_bf16 v[56:59], v[108:111], v[224:227], v[56:59]\n\t"
    "ds_read_b128 v[112:115], v0 offset:12288\n\t"
    "v_mfma_f32_16x16x32_bf16 v[56:59], v[84:87], a[252:255], v[56:59]\n\t"
    "ds_read_b128 v[132:135], v1 offset:12288\n\t"
    "v_mfma_f32_16x16x32_bf16 v[56:59], v[88:91], a[248:251], v[56:59]\n\t"
    "ds_read_b128 v[136:139], v2 offset:12288\n\t"
    "v_cvt_pk_bf16_f32 v52, v92, v93\n\t"
    "v_mfma_f32_16x16x32_bf16 v[56:59], v[144:147], a[244:247], v[56:59]\n\t"
    "ds_read_b128 v[140:143], v3 offset:12288\n\t"
    "v_cvt_pk_bf16_f32 v53, v94, v95\n\t"
    "v_mfma_f32_16x16x32_bf16 v[56:59], v[196:199], a[240:243], v[56:59]\n\t"
    "ds_read_b128 v[148:151], v4 offset:12288\n\t"
    "s_waitcnt vmcnt(11)\n\t"
    "v_mfma_f32_16x16x32_bf16 v[96:99], v[196:199], v[232:235], 0\n\t"
    "ds_read_b128 v[152:155], v5 offset:12288\n\t"
    "v_mfma_f32_16x16x32_bf16 v[96:99], v[144:147], v[236:239], v[96:99]\n\t"
    "buffer_store_dwordx4 v[128:131], v7, s[52:55], s61 offen offset:384 sc1\n\t"
    "v_mfma_f32_16x16x32_bf16 v[96:99], v[88:91], v[240:243], v[96:99]\n\t"
    "ds_read_b128 v[160:163], v5 offset:18432\n\t"
    "v_mfma_f32_16x16x32_bf16 v[96:99], v[84:87], v[244:247], v[96:99]\n\t"
    "ds_read_b128 v[164:167], v4 offset:18432\n\t"
    "v_cvt_pk_bf16_f32 v54, v56, v57\n\t"
    "v_mfma_f32_16x16x32_bf16 v[96:99], v[108:111], v[248:251], v[96:99]\n\t"
    "ds_read_b128 v[128:131], v3 offset:18432\n\t"
    "v_cvt_pk_bf16_f32 v55, v58, v59\n\t"
    "v_mfma_f32_16x16x32_bf16 v[96:99], v[80:83], v[252:255], v[96:99]\n\t"
    "ds_read_b128 v[144:147], v2 offset:18432\n\t"
    "v_mfma_f32_16x16x32_bf16 v[84:87], v[76:79], v[252:255], 0\n\t"
    "ds_read_b128 v[176:179], v1 offset:18432\n\t"
    "v_mfma_f32_16x16x32_bf16 v[84:87], v[188:191], v[248:251], v[84:87]\n\t"
    "ds_read_b128 v[108:111], v0 offset:18432\n\t"
    "v_mfma_f32_16x16x32_bf16 v[84:87], v[72:75], v[244:247], v[84:87]\n\t"
    "v_mfma_f32_16x16x32_bf16 v[84:87], v[68:71], v[240:243], v[84:87]\n\t"
    "v_cvt_pk_bf16_f32 v58, v96, v97\n\t"
    "v_cvt_pk_bf16_f32 v59, v98, v99\n\t"
    "v_mfma_f32_16x16x32_bf16 v[84:87], v[64:67], v[236:239], v[84:87]\n\t"
    "buffer_store_dwordx4 v[172:175], v7, s[52:55], s61 offen offset:768 sc1\n\t"
    "v_mfma_f32_16x16x32_bf16 v[84:87], v[60:63], v[232:235], v[84:87]\n\t"
    "s_waitcnt lgkmcnt(3)\n\t"
    "v_mfma_f32_16x16x32_bf16 v[68:71], v[112:115], a[0:3], 0\n\t"
    "v_mfma_f32_16x16x32_bf16 v[68:71], v[132:135], a[4:7], v[68:71]\n\t"
    "v_mfma_f32_16x16x32_bf16 v[68:71], v[136:139], a[8:11], v[68:71]\n\t"
    "v_mfma_f32_16x16x32_bf16 v[68:71], v[140:143], a[12:15], v[68:71]\n\t"
    "v_cvt_pk_bf16_f32 v56, v84, v85\n\t"
    "v_cvt_pk_bf16_f32 v57, v86, v87\n\t"
    "v_mfma_f32_16x16x32_bf16 v[68:71], v[148:151], a[16:19], v[68:71]\n\t"
    "v_mfma_f32_16x16x32_bf16 v[68:71], v[152:155], a[20:23], v[68:71]\n\t"
    "v_mfma_f32_16x16x32_bf16 v[64:67], v[160:163], a[20:23], 0\n\t"
    "v_mfma_f32_16x16x32_bf16 v[64:67], v[164:167], a[16:19], v[64:67]\n\t"
    "v_mfma_f32_16x16x32_bf16 v[64:67], v[128:131], a[12:15], v[64:67]\n\t"
    "s_waitcnt lgkmcnt(0)\n\t"
    "v_mfma_f32_16x16x32_bf16 v[64:67], v[144:147], a[8:11], v[64:67]\n\t"
    "v_cvt_pk_bf16_f32 v60, v68, v69\n\t"
    "v_cvt_pk_bf16_f32 v61, v70, v71\n\t"
    "v_mfma_f32_16x16x32_bf16 v[64:67], v[176:179], a[4:7], v[64:67]\n\t"
    "buffer_store_dwordx4 v[168:171], v7, s[52:55], s61 offen offset:1152 sc1\n\t"
    "v_mfma_f32_16x16x32_bf16 v[64:67], v[108:111], a[0:3], v[64:67]\n\t"
    "v_mfma_f32_16x16x32_bf16 v[68:71], v[108:111], a[24:27], 0\n\t"
    "v_mfma_f32_16x16x32_bf16 v[68:71], v[176:179], a[28:31], v[68:71]\n\t"
    "v_mfma_f32_16x16x32_bf16 v[68:71], v[144:147], a[32:35], v[68:71]\n\t"
    "v_mfma_f32_16x16x32_bf16 v[68:71], v[128:131], a[36:39], v[68:71]\n\t"
    "v_cvt_pk_bf16_f32 v62, v64, v65\n\t"
    "v_cvt_pk_bf16_f32 v63, v66, v67\n\t"
    "v_mfma_f32_16x16x32_bf16 v[68:71], v[164:167], a[40:43], v[68:71]\n\t"
    "v_mfma_f32_16x16x32_bf16 v[68:71], v[160:163], a[44:47], v[68:71]\n\t"
    "v_mfma_f32_16x16x32_bf16 v[72:75], v[152:155], a[44:47], 0\n\t"
    "v_mfma_f32_16x16x32_bf16 v[72:75], v[148:151], a[40:43], v[72:75]\n\t"
    "v_mfma_f32_16x16x32_bf16 v[72:75], v[140:143], a[36:39], v[72:75]\n\t"
    "v_mfma_f32_16x16x32_bf16 v[72:75], v[136:139], a[32:35], v[72:75]\n\t"
    "v_cvt_pk_bf16_f32 v66, v68, v69\n\t"
    "v_cvt_pk_bf16_f32 v67, v70, v71\n\t"
    "v_mfma_f32_16x16x32_bf16 v[72:75], v[132:135], a[28:31], v[72:75]\n\t"
    "buffer_store_dwordx2 v[116:117], v8, s[52:55], s61 offen offset:0 sc1\n\t"
    "v_mfma_f32_16x16x32_bf16 v[72:75], v[112:115], a[24:27], v[72:75]\n\t"
    "v_mfma_f32_16x16x32_bf16 v[76:79], v[112:115], a[48:51], 0\n\t"
    "v_mfma_f32_16x16x32_bf16 v[76:79], v[132:135], a[52:55], v[76:79]\n\t"
    "v_mfma_f32_16x16x32_bf16 v[76:79], v[136:139], a[56:59], v[76:79]\n\t"
    "v_mfma_f32_16x16x32_bf16 v[76:79], v[140:143], a[60:63], v[76:79]\n\t"
    "v_cvt_pk_bf16_f32 v64, v72, v73\n\t"
    "v_cvt_pk_bf16_f32 v65, v74, v75\n\t"
    "v_mfma_f32_16x16x32_bf16 v[76:79], v[148:151], a[64:67], v[76:79]\n\t"
    "v_mfma_f32_16x16x32_bf16 v[76:79], v[152:155], a[68:71], v[76:79]\n\t"
    "v_mfma_f32_16x16x32_bf16 v[72:75], v[160:163], a[68:71], 0\n\t"
    "v_mfma_f32_16x16x32_bf16 v[72:75], v[164:167], a[64:67], v[72:75]\n\t"
    "v_mfma_f32_16x16x32_bf16 v[72:75], v[128:131], a[60:63], v[72:75]\n\t"
    "v_mfma_f32_16x16x32_bf16 v[72:75], v[144:147], a[56:59], v[72:75]\n\t"
    "v_cvt_pk_bf16_f32 v68, v76, v77\n\t"
    "v_cvt_pk_bf16_f32 v69, v78, v79\n\t"
    "v_mfma_f32_16x16x32_bf16 v[72:75], v[176:179], a[52:55], v[72:75]\n\t"
    "buffer_store_dwordx2 v[118:119], v8, s[52:55], s61 offen offset:384 sc1\n\t"
    "v_mfma_f32_16x16x32_bf16 v[72:75], v[108:111], a[48:51], v[72:75]\n\t"
    "v_mfma_f32_16x16x32_bf16 v[76:79], v[108:111], a[72:75], 0\n\t"
    "s_add_i32 s64, s62, 24576\n\t"
    "v_mfma_f32_16x16x32_bf16 v[76:79], v[176:179], a[76:79], v[76:79]\n\t"
    "s_cmp_eq_u32 s64, %[rend]\n\t"
    "s_cselect_b32 s64, %[rbeg], s64\n\t"
    "v_mfma_f32_16x16x32_bf16 v[76:79], v[144:147], a[80:83], v[76:79]\n\t"
    "s_sub_i32 s65, s64, s62\n\t"
    "s_mov_b32 s62, s64\n\t"
    "v_mfma_f32_16x16x32_bf16 v[76:79], v[128:131], a[84:87], v[76:79]\n\t"
    "v_add_u32_e32 v0, s65, v0\n\t"
    "v_add_u32_e32 v1, s65, v1\n\t"
    "v_mfma_f32_16x16x32_bf16 v[76:79], v[164:167], a[88:91], v[76:79]\n\t"
    "v_add_u32_e32 v2, s65, v2\n\t"
    "v_add_u32_e32 v3, s65, v3\n\t"
    "v_mfma_f32_16x16x32_bf16 v[76:79], v[160:163], a[92:95], v[76:79]\n\t"
    "v_add_u32_e32 v4, s65, v4\n\t"
    "v_add_u32_e32 v5, s65, v5\n\t"
    "v_mfma_f32_16x16x32_bf16 v[80:83], v[152:155], a[92:95], 0\n\t"
    "v_cvt_pk_bf16_f32 v70, v72, v73\n\t"
    "v_cvt_pk_bf16_f32 v71, v74, v75\n\t"
    "v_mfma_f32_16x16x32_bf16 v[80:83], v[148:151], a[88:91], v[80:83]\n\t"
    "v_mfma_f32_16x16x32_bf16 v[80:83], v[140:143], a[84:87], v[80:83]\n\t"
    "v_mfma_f32_16x16x32_bf16 v[80:83], v[136:139], a[80:83], v[80:83]\n\t"
    "v_cvt_pk_bf16_f32 v74, v76, v77\n\t"
    "v_cvt_pk_bf16_f32 v75, v78, v79\n\t"
    "v_mfma_f32_16x16x32_bf16 v[80:83], v[132:135], a[76:79], v[80:83]\n\t"
    "buffer_store_dwordx2 v[192:193], v8, s[52:55], s61 offen offset:768 sc1\n\t"
    "v_mfma_f32_16x16x32_bf16 v[80:83], v[112:115], a[72:75], v[80:83]\n\t"
    "v_mfma_f32_16x16x32_bf16 v[84:87], v[112:115], a[96:99], 0\n\t"
    "v_mfma_f32_16x16x32_bf16 v[84:87], v[132:135], a[100:103], v[84:87]\n\t"
    "v_mfma_f32_16x16x32_bf16 v[84:87], v[136:139], a[104:107], v[84:87]\n\t"
    "v_mfma_f32_16x16x32_bf16 v[84:87], v[140:143], a[108:111], v[84:87]\n\t"
    "v_cvt_pk_bf16_f32 v72, v80, v81\n\t"
    "v_cvt_pk_bf16_f32 v73, v82, v83\n\t"
    "v_mfma_f32_16x16x32_bf16 v[84:87], v[148:151], a[112:115], v[84:87]\n\t"
    "v_mfma_f32_16x16x32_bf16 v[84:87], v[152:155], a[116:119], v[84:87]\n\t"
    "v_mfma_f32_16x16x32_bf16 v[80:83], v[160:163], a[116:119], 0\n\t"
    "v_mfma_f32_16x16x32_bf16 v[80:83], v[164:167], a[112:115], v[80:83]\n\t"
    "v_mfma_f32_16x16x32_bf16 v[80:83], v[128:131], a[108:111], v[80:83]\n\t"
    "v_mfma_f32_16x16x32_bf16 v[80:83], v[144:147], a[104:107], v[80:83]\n\t"
    "v_cvt_pk_bf16_f32 v76, v84, v85\n\t"
    "v_cvt_pk_bf16_f32 v77, v86, v87\n\t"
    "v_mfma_f32_16x16x32_bf16 v[80:83], v[176:179], a[100:103], v[80:83]\n\t"
    "buffer_store_dwordx2 v[194:195], v8, s[52:55], s61 offen offset:1152 sc1\n\t"
    "v_mfma_f32_16x16x32_bf16 v[80:83], v[108:111], a[96:99], v[80:83]\n\t"
    "s_add_u32 s61, s61, 393216\n\t"
    "v_mfma_f32_16x16x32_bf16 v[84:87], v[108:111], a[120:123], 0\n\t"
    "v_mfma_f32_16x16x32_bf16 v[84:87], v[176:179], a[124:127], v[84:87]\n\t"
    "v_mfma_f32_16x16x32_bf16 v[84:87], v[144:147], a[128:131], v[84:87]\n\t"
    "v_mfma_f32_16x16x32_bf16 v[84:87], v[128:131], a[132:135], v[84:87]\n\t"
    "v_cvt_pk_bf16_f32 v78, v80, v81\n\t"
    "v_cvt_pk_bf16_f32 v79, v82, v83\n\t"
    "v_mfma_f32_16x16x32_bf16 v[84:87], v[164:167], a[136:139], v[84:87]\n\t"
    "v_mfma_f32_16x16x32_bf16 v[84:87], v[160:163], a[140:143], v[84:87]\n\t"
    "v_mfma_f32_16x16x32_bf16 v[88:91], v[152:155], a[140:143], 0\n\t"
    "v_mfma_f32_16x16x32_bf16 v[88:91], v[148:151], a[136:139], v[88:91]\n\t"
    "v_mfma_f32_16x16x32_bf16 v[88:91], v[140:143], a[132:135], v[88:91]\n\t"
    "v_mfma_f32_16x16x32_bf16 v[88:91], v[136:139], a[128:131], v[88:91]\n\t"
    "v_cvt_pk_bf16_f32 v82, v84, v85\n\t"
    "v_cvt_pk_bf16_f32 v83, v86, v87\n\t"
    "v_mfma_f32_16x16x32_bf16 v[88:91], v[132:135], a[124:127], v[88:91]\n\t"
    "buffer_store_dwordx4 v[120:123], v7, s[52:55], s61 offen offset:0 sc1\n\t"
    "v_mfma_f32_16x16x32_bf16 v[88:91], v[112:115], a[120:123], v[88:91]\n\t"
    "v_mfma_f32_16x16x32_bf16 v[92:95], v[112:115], a[144:147], 0\n\t"
    "v_mfma_f32_16x16x32_bf16 v[92:95], v[132:135], a[148:151], v[92:95]\n\t"
    "v_mfma_f32_16x16x32_bf16 v[92:95], v[136:139], a[152:155], v[92:95]\n\t"
    "v_mfma_f32_16x16x32_bf16 v[92:95], v[140:143], a[156:159], v[92:95]\n\t"
    "v_cvt_pk_bf16_f32 v80, v88, v89\n\t"
    "v_cvt_pk_bf16_f32 v81, v90, v91\n\t"
    "v_mfma_f32_16x16x32_bf16 v[92:95], v[148:151], a[160:163], v[92:95]\n\t"
    "v_mfma_f32_16x16x32_bf16 v[92:95], v[152:155], a[164:167], v[92:95]\n\t"
    "v_mfma_f32_16x16x32_bf16 v[88:91], v[160:163], a[164:167], 0\n\t"
    "v_mfma_f32_16x16x32_bf16 v[88:91], v[164:167], a[160:163], v[88:91]\n\t"
    "v_mfma_f32_16x16x32_bf16 v[88:91], v[128:131], a[156:159], v[88:91]\n\t"
    "v_mfma_f32_16x16x32_bf16 v[88:91], v[144:147], a[152:155], v[88:91]\n\t"
    "v_cvt_pk_bf16_f32 v84, v92, v93\n\t"
    "v_cvt_pk_bf16_f32 v85, v94, v95\n\t"
    "v_mfma_f32_16x16x32_bf16 v[88:91], v[176:179], a[148:151], v[88:91]\n\t"
    "buffer_store_dwordx4 v[124:127], v7, s[52:55], s61 offen offset:384 sc1\n\t"
    "v_mfma_f32_16x16x32_bf16 v[88:91], v[108:111], a[144:147], v[88:91]\n\t"
    "v_mfma_f32_16x16x32_bf16 v[92:95], v[108:111], a[168:171], 0\n\t"
    "v_mfma_f32_16x16x32_bf16 v[92:95], v[176:179], a[172:175], v[92:95]\n\t"
    "v_mfma_f32_16x16x32_bf16 v[92:95], v[144:147], a[176:179], v[92:95]\n\t"
    "v_mfma_f32_16x16x32_bf16 v[92:95], v[128:131], a[180:183], v[92:95]\n\t"
    "v_cvt_pk_bf16_f32 v86, v88, v89\n\t"
    "v_cvt_pk_bf16_f32 v87, v90, v91\n\t"
    "v_mfma_f32_16x16x32_bf16 v[92:95], v[164:167], a[184:187], v[92:95]\n\t"
    "v_mfma_f32_16x16x32_bf16 v[92:95], v[160:163], a[188:191], v[92:95]\n\t"
    "v_mfma_f32_16x16x32_bf16 v[96:99], v[152:155], a[188:191], 0\n\t"
    "v_mfma_f32_16x16x32_bf16 v[96:99], v[148:151], a[184:187], v[96:99]\n\t"
    "v_mfma_f32_16x16x32_bf16 v[96:99], v[140:143], a[180:183], v[96:99]\n\t"
    "v_mfma_f32_16x16x32_bf16 v[96:99], v[136:139], a[176:179], v[96:99]\n\t"
    "v_cvt_pk_bf16_f32 v90, v92, v93\n\t"
    "v_cvt_pk_bf16_f32 v91, v94, v95\n\t"
    "v_mfma_f32_16x16x32_bf16 v[96:99], v[132:135], a[172:175], v[96:99]\n\t"
    "buffer_store_dwordx4 v[156:159], v7, s[52:55], s61 offen offset:768 sc1\n\t"
    "v_mfma_f32_16x16x32_bf16 v[96:99], v[112:115], a[168:171], v[96:99]\n\t"
    "v_mfma_f32_16x16x32_bf16 v[100:103], v[112:115], a[192:195], 0\n\t"
    "v_mfma_f32_16x16x32_bf16 v[100:103], v[132:135], a[196:199], v[100:103]\n\t"
    "v_mfma_f32_16x16x32_bf16 v[100:103], v[136:139], a[200:203], v[100:103]\n\t"
    "v_mfma_f32_16x16x32_bf16 v[100:103], v[140:143], a[204:207], v[100:103]\n\t"
    "v_cvt_pk_bf16_f32 v88, v96, v97\n\t"
    "v_cvt_pk_bf16_f32 v89, v98, v99\n\t"
    "v_mfma_f32_16x16x32_bf16 v[100:103], v[148:151], a[208:211], v[100:103]\n\t"
    "v_mfma_f32_16x16x32_bf16 v[100:103], v[152:155], a[212:215], v[100:103]\n\t"
    "v_mfma_f32_16x16x32_bf16 v[96:99], v[160:163], a[212:215], 0\n\t"
    "v_mfma_f32_16x16x32_bf16 v[96:99], v[164:167], a[208:211], v[96:99]\n\t"
    "v_mfma_f32_16x16x32_bf16 v[96:99], v[128:131], a[204:207], v[96:99]\n\t"
    "v_mfma_f32_16x16x32_bf16 v[96:99], v[144:147], a[200:203], v[96:99]\n\t"
    "v_cvt_pk_bf16_f32 v92, v100, v101\n\t"
    "v_cvt_pk_bf16_f32 v93, v102, v103\n\t"
    "v_mfma_f32_16x16x32_bf16 v[96:99], v[176:179], a[196:199], v[96:99]\n\t"
    "buffer_store_dwordx4 v[184:187], v7, s[52:55], s61 offen offset:1152 sc1\n\t"
    "v_mfma_f32_16x16x32_bf16 v[96:99], v[108:111], a[192:195], v[96:99]\n\t"
    "s_barrier\n\t"
    "v_mfma_f32_16x16x32_bf16 v[100:103], v[108:111], a[216:219], 0\n\t"
    "v_mfma_f32_16x16x32_bf16 v[100:103], v[176:179], a[220:223], v[100:103]\n\t"
    "v_mfma_f32_16x16x32_bf16 v[100:103], v[144:147], a[224:227], v[100:103]\n\t"
    "v_mfma_f32_16x16x32_bf16 v[100:103], v[128:131], a[228:231], v[100:103]\n\t"
    "v_cvt_pk_bf16_f32 v94, v96, v97\n\t"
    "v_cvt_pk_bf16_f32 v95, v98, v99\n\t"
    "v_mfma_f32_16x16x32_bf16 v[100:103], v[164:167], a[232:235], v[100:103]\n\t"
    "v_mfma_f32_16x16x32_bf16 v[100:103], v[160:163], a[236:239], v[100:103]\n\t"
    "v_mfma_f32_16x16x32_bf16 v[116:119], v[152:155], a[236:239], 0\n\t"
    "v_mfma_f32_16x16x32_bf16 v[116:119], v[148:151], a[232:235], v[116:119]\n\t"
    "v_mfma_f32_16x16x32_bf16 v[116:119], v[140:143], a[228:231], v[116:119]\n\t"
    "v_mfma_f32_16x16x32_bf16 v[116:119], v[136:139], a[224:227], v[116:119]\n\t"
    "v_cvt_pk_bf16_f32 v98, v100, v101\n\t"
    "v_cvt_pk_bf16_f32 v99, v102, v103\n\t"
    "v_mfma_f32_16x16x32_bf16 v[116:119], v[132:135], a[220:223], v[116:119]\n\t"
    "buffer_store_dwordx2 v[104:105], v8, s[52:55], s61 offen offset:0 sc1\n\t"
    "v_mfma_f32_16x16x32_bf16 v[116:119], v[112:115], a[216:219], v[116:119]\n\t"
    "v_mfma_f32_16x16x32_bf16 v[120:123], v[112:115], a[240:243], 0\n\t"
    "v_mfma_f32_16x16x32_bf16 v[120:123], v[132:135], a[244:247], v[120:123]\n\t"
    "v_mfma_f32_16x16x32_bf16 v[120:123], v[136:139], a[248:251], v[120:123]\n\t"
    "v_mfma_f32_16x16x32_bf16 v[120:123], v[140:143], a[252:255], v[120:123]\n\t"
    "v_cvt_pk_bf16_f32 v96, v116, v117\n\t"
    "v_cvt_pk_bf16_f32 v97, v118, v119\n\t"
    "v_mfma_f32_16x16x32_bf16 v[120:123], v[148:151], v[224:227], v[120:123]\n\t"
    "v_mfma_f32_16x16x32_bf16 v[120:123], v[152:155], v[228:231], v[120:123]\n\t"
    "v_mfma_f32_16x16x32_bf16 v[116:119], v[160:163], v[228:231], 0\n\t"
    "v_mfma_f32_16x16x32_bf16 v[116:119], v[164:167], v[224:227], v[116:119]\n\t"
    "v_mfma_f32_16x16x32_bf16 v[116:119], v[128:131], a[252:255], v[116:119]\n\t"
    "v_mfma_f32_16x16x32_bf16 v[116:119], v[144:147], a[248:251], v[116:119]\n\t"
    "v_cvt_pk_bf16_f32 v100, v120, v121\n\t"
    "v_cvt_pk_bf16_f32 v101, v122, v123\n\t"
    "v_mfma_f32_16x16x32_bf16 v[116:119], v[176:179], a[244:247], v[116:119]\n\t"
    "buffer_store_dwordx2 v[106:107], v8, s[52:55], s61 offen offset:384 sc1\n\t"
    "v_mfma_f32_16x16x32_bf16 v[116:119], v[108:111], a[240:243], v[116:119]\n\t"
    "v_mfma_f32_16x16x32_bf16 v[120:123], v[108:111], v[232:235], 0\n\t"
    "v_mfma_f32_16x16x32_bf16 v[120:123], v[176:179], v[236:239], v[120:123]\n\t"
    "ds_read_b128 v[156:159], v6 offset:0\n\t"
    "v_mfma_f32_16x16x32_bf16 v[120:123], v[144:147], v[240:243], v[120:123]\n\t"
    "ds_read_b128 v[168:171], v6 offset:1024\n\t"
    "v_mfma_f32_16x16x32_bf16 v[120:123], v[128:131], v[244:247], v[120:123]\n\t"
    "v_cvt_pk_bf16_f32 v102, v116, v117\n\t"
    "v_cvt_pk_bf16_f32 v103, v118, v119\n\t"
    "v_mfma_f32_16x16x32_bf16 v[120:123], v[164:167], v[248:251], v[120:123]\n\t"
    "v_mfma_f32_16x16x32_bf16 v[120:123], v[160:163], v[252:255], v[120:123]\n\t"
    "v_mfma_f32_16x16x32_bf16 v[108:111], v[152:155], v[252:255], 0\n\t"
    "v_mfma_f32_16x16x32_bf16 v[108:111], v[148:151], v[248:251], v[108:111]\n\t"
    "v_mfma_f32_16x16x32_bf16 v[108:111], v[140:143], v[244:247], v[108:111]\n\t"
    "v_mfma_f32_16x16x32_bf16 v[108:111], v[136:139], v[240:243], v[108:111]\n\t"
    "v_cvt_pk_bf16_f32 v106, v120, v121\n\t"
    "v_cvt_pk_bf16_f32 v107, v122, v123\n\t"
    "v_mfma_f32_16x16x32_bf16 v[108:111], v[132:135], v[236:239], v[108:111]\n\t"
    "buffer_store_dwordx2 v[180:181], v8, s[52:55], s61 offen offset:768 sc1\n\t"
    "v_mfma_f32_16x16x32_bf16 v[108:111], v[112:115], v[232:235], v[108:111]\n\t"
    "s_waitcnt lgkmcnt(0)\n\t"
    "v_mfma_f32_16x16x32_bf16 v[120:123], v[156:159], v[12:15], 0\n\t"
    "v_mfma_f32_16x16x32_bf16 v[120:123], v[168:171], v[60:63], v[120:123]\n\t"
    "v_mfma_f32_16x16x32_bf16 v[124:127], v[168:171], v[64:67], 0\n\t"
    "v_mfma_f32_16x16x32_bf16 v[124:127], v[156:159], v[16:19], v[124:127]\n\t"
    "v_cvt_pk_bf16_f32 v104, v108, v109\n\t"
    "v_cvt_pk_bf16_f32 v105, v110, v111\n\t"
    "v_mfma_f32_16x16x32_bf16 v[128:131], v[156:159], v[20:23], 0\n\t"
    "v_mfma_f32_16x16x32_bf16 v[128:131], v[168:171], v[68:71], v[128:131]\n\t"
    "v_mfma_f32_16x16x32_bf16 v[132:135], v[168:171], v[72:75], 0\n\t"
    "v_mfma_f32_16x16x32_bf16 v[132:135], v[156:159], v[24:27], v[132:135]\n\t"
    "v_cvt_pk_bf16_f32 v160, v120, v124\n\t"
    "v_cvt_pk_bf16_f32 v108, v121, v125\n\t"
    "v_mfma_f32_16x16x32_bf16 v[136:139], v[156:159], v[28:31], 0\n\t"
    "v_cvt_pk_bf16_f32 v116, v122, v126\n\t"
    "v_cvt_pk_bf16_f32 v112, v123, v127\n\t"
    "v_mfma_f32_16x16x32_bf16 v[136:139], v[168:171], v[76:79], v[136:139]\n\t"
    "v_mfma_f32_16x16x32_bf16 v[140:143], v[168:171], v[80:83], 0\n\t"
    "buffer_store_dwordx2 v[182:183], v8, s[52:55], s61 offen offset:1152 sc1\n\t"
    "v_mfma_f32_16x16x32_bf16 v[140:143], v[156:159], v[32:35], v[140:143]\n\t"
    "s_add_u32 s61, s61, 11403264\n\t"
    "v_cvt_pk_bf16_f32 v161, v128, v132\n\t"
    "v_mfma_f32_16x16x32_bf16 v[144:147], v[156:159], v[36:39], 0\n\t"
    "v_cvt_pk_bf16_f32 v109, v129, v133\n\t"
    "v_cvt_pk_bf16_f32 v117, v130, v134\n\t"
    "v_mfma_f32_16x16x32_bf16 v[144:147], v[168:171], v[84:87], v[144:147]\n\t"
    "ds_read_b128 v[120:123], v6 offset:2048\n\t"
    "v_cvt_pk_bf16_f32 v113, v131, v135\n\t"
    "v_mfma_f32_16x16x32_bf16 v[148:151], v[168:171], v[88:91], 0\n\t"
    ".Lh2_%=:\n\t"
    "s_cbranch_execz .LrM_%=\n\t"
    "ds_read_b128 v[124:127], v6 offset:3072\n\t"
    "v_mfma_f32_16x16x32_bf16 v[148:151], v[156:159], v[40:43], v[148:151]\n\t"
    "v_cvt_pk_bf16_f32 v162, v136, v140\n\t"
    "v_cvt_pk_bf16_f32 v110, v137, v141\n\t"
    "v_mfma_f32_16x16x32_bf16 v[128:131], v[156:159], v[44:47], 0\n\t"
    "v_cvt_pk_bf16_f32 v118, v138, v142\n\t"
    "v_cvt_pk_bf16_f32 v114, v139, v143\n\t"
    "v_mfma_f32_16x16x32_bf16 v[128:131], v[168:171], v[92:95], v[128:131]\n\t"
    "v_mfma_f32_16x16x32_bf16 v[132:135], v[168:171], v[96:99], 0\n\t"
    "v_mfma_f32_16x16x32_bf16 v[132:135], v[156:159], v[48:51], v[132:135]\n\t"
    "v_cvt_pk_bf16_f32 v163, v144, v148\n\t"
    "v_cvt_pk_bf16_f32 v111, v145, v149\n\t"
    "v_mfma_f32_16x16x32_bf16 v[136:139], v[156:159], v[52:55], 0\n\t"
    "v_cvt_pk_bf16_f32 v119, v146, v150\n\t"
    "v_cvt_pk_bf16_f32 v115, v147, v151\n\t"
    "v_mfma_f32_16x16x32_bf16 v[136:139], v[168:171], v[100:103], v[136:139]\n\t"
    "v_mfma_f32_16x16x32_bf16 v[140:143], v[168:171], v[104:107], 0\n\t"
    "v_mfma_f32_16x16x32_bf16 v[140:143], v[156:159], v[56:59], v[140:143]\n\t"
    "v_cvt_pk_bf16_f32 v144, v128, v132\n\t"
    "s_waitcnt lgkmcnt(0)\n\t"
    "v_mfma_f32_16x16x32_bf16 v[148:151], v[120:123], v[12:15], 0\n\t"
    "buffer_store_dwordx4 v[160:163], v7, s[52:55], s61 offen offset:0 sc1\n\t"
    "v_mfma_f32_16x16x32_bf16 v[148:151], v[124:127], v[60:63], v[148:151]\n\t"
    "v_cvt_pk_bf16_f32 v146, v129, v133\n\t"
    "v_cvt_pk_bf16_f32 v152, v130, v134\n\t"
    "v_mfma_f32_16x16x32_bf16 v[156:159], v[124:127], v[64:67], 0\n\t"
    "v_cvt_pk_bf16_f32 v154, v131, v135\n\t"
    "v_mfma_f32_16x16x32_bf16 v[156:159], v[120:123], v[16:19], v[156:159]\n\t"
    "v_cvt_pk_bf16_f32 v145, v136, v140\n\t"
    "v_cvt_pk_bf16_f32 v147, v137, v141\n\t"
    "v_mfma_f32_16x16x32_bf16 v[160:163], v[120:123], v[20:23], 0\n\t"
    "v_cvt_pk_bf16_f32 v153, v138, v142\n\t"
    "v_cvt_pk_bf16_f32 v155, v139, v143\n\t"
    "v_mfma_f32_16x16x32_bf16 v[160:163], v[124:127], v[68:71], v[160:163]\n\t"
    "v_mfma_f32_16x16x32_bf16 v[128:131], v[124:127], v[72:75], 0\n\t"
    "v_mfma_f32_16x16x32_bf16 v[128:131], v[120:123], v[24:27], v[128:131]\n\t"
    "v_cvt_pk_bf16_f32 v136, v148, v156\n\t"
    "v_cvt_pk_bf16_f32 v132, v149, v157\n\t"
    "v_mfma_f32_16x16x32_bf16 v[168:171], v[120:123], v[28:31], 0\n\t"
    "v_cvt_pk_bf16_f32 v164, v150, v158\n\t"
    "v_cvt_pk_bf16_f32 v140, v151, v159\n\t"
    "v_mfma_f32_16x16x32_bf16 v[168:171], v[124:127], v[76:79], v[168:171]\n\t"
    "v_mfma_f32_16x16x32_bf16 v[172:175], v[124:127], v[80:83], 0\n\t"
    "buffer_store_dwordx4 v[108:111], v7, s[52:55], s61 offen offset:384 sc1\n\t"
    "v_mfma_f32_16x16x32_bf16 v[172:175], v[120:123], v[32:35], v[172:175]\n\t"
    "v_cvt_pk_bf16_f32 v137, v160, v128\n\t"
    "v_cvt_pk_bf16_f32 v133, v161, v129\n\t"
    "v_mfma_f32_16x16x32_bf16 v[148:151], v[120:123], v[36:39], 0\n\t"
    "v_cvt_pk_bf16_f32 v165, v162, v130\n\t"
    "v_cvt_pk_bf16_f32 v141, v163, v131\n\t"
    "v_mfma_f32_16x16x32_bf16 v[148:151], v[124:127], v[84:87], v[148:151]\n\t"
    "ds_read_b128 v[108:111], v6 offset:4096\n\t"
    "v_mfma_f32_16x16x32_bf16 v[176:179], v[124:127], v[88:91], 0\n\t"
    "ds_read_b128 v[156:159], v6 offset:5120\n\t"
    "v_mfma_f32_16x16x32_bf16 v[176:179], v[120:123], v[40:43], v[176:179]\n\t"
    "v_cvt_pk_bf16_f32 v138, v168, v172\n\t"
    "v_cvt_pk_bf16_f32 v134, v169, v173\n\t"
    "v_mfma_f32_16x16x32_bf16 v[128:131], v[120:123], v[44:47], 0\n\t"
    "v_cvt_pk_bf16_f32 v166, v170, v174\n\t"
    "v_cvt_pk_bf16_f32 v142, v171, v175\n\t"
    "v_mfma_f32_16x16x32_bf16 v[128:131], v[124:127], v[92:95], v[128:131]\n\t"
    "v_mfma_f32_16x16x32_bf16 v[160:163], v[124:127], v[96:99], 0\n\t"
    "v_mfma_f32_16x16x32_bf16 v[160:163], v[120:123], v[48:51], v[160:163]\n\t"
    "v_cvt_pk_bf16_f32 v139, v148, v176\n\t"
    "v_cvt_pk_bf16_f32 v135, v149, v177\n\t"
    "v_mfma_f32_16x16x32_bf16 v[168:171], v[120:123], v[52:55], 0\n\t"
    "v_cvt_pk_bf16_f32 v167, v150, v178\n\t"
    "v_cvt_pk_bf16_f32 v143, v151, v179\n\t"
    "v_mfma_f32_16x16x32_bf16 v[168:171], v[124:127], v[100:103], v[168:171]\n\t"
    "v_mfma_f32_16x16x32_bf16 v[172:175], v[124:127], v[104:107], 0\n\t"
    "buffer_store_dwordx4 v[116:119], v7, s[52:55], s61 offen offset:768 sc1\n\t"
    "v_mfma_f32_16x16x32_bf16 v[172:175], v[120:123], v[56:59], v[172:175]\n\t"
    "v_cvt_pk_bf16_f32 v148, v128, v160\n\t"
    "s_waitcnt lgkmcnt(0)\n\t"
    "v_mfma_f32_16x16x32_bf16 v[180:183], v[108:111], v[12:15], 0\n\t"
    "v_cvt_pk_bf16_f32 v150, v129, v161\n\t"
    "v_cvt_pk_bf16_f32 v176, v130, v162\n\t"
    "v_mfma_f32_16x16x32_bf16 v[180:183], v[156:159], v[60:63], v[180:183]\n\t"
    "v_cvt_pk_bf16_f32 v178, v131, v163\n\t"
    "v_mfma_f32_16x16x32_bf16 v[116:119], v[156:159], v[64:67], 0\n\t"
    "v_mfma_f32_16x16x32_bf16 v[116:119], v[108:111], v[16:19], v[116:119]\n\t"
    "v_cvt_pk_bf16_f32 v149, v168, v172\n\t"
    "v_cvt_pk_bf16_f32 v151, v169, v173\n\t"
    "v_mfma_f32_16x16x32_bf16 v[120:123], v[108:111], v[20:23], 0\n\t"
    "v_cvt_pk_bf16_f32 v177, v170, v174\n\t"
    "v_cvt_pk_bf16_f32 v179, v171, v175\n\t"
    "v_mfma_f32_16x16x32_bf16 v[120:123], v[156:159], v[68:71], v[120:123]\n\t"
    "v_mfma_f32_16x16x32_bf16 v[124:127], v[156:159], v[72:75], 0\n\t"
    "v_mfma_f32_16x16x32_bf16 v[124:127], v[108:111], v[24:27], v[124:127]\n\t"
    "v_cvt_pk_bf16_f32 v160, v180, v116\n\t"
    "v_cvt_pk_bf16_f32 v128, v181, v117\n\t"
    "v_mfma_f32_16x16x32_bf16 v[184:187], v[108:111], v[28:31], 0\n\t"
    "v_cvt_pk_bf16_f32 v172, v182, v118\n\t"
    "v_cvt_pk_bf16_f32 v168, v183, v119\n\t"
    "v_mfma_f32_16x16x32_bf16 v[184:187], v[156:159], v[76:79], v[184:187]\n\t"
    "v_mfma_f32_16x16x32_bf16 v[188:191], v[156:159], v[80:83], 0\n\t"
    "buffer_store_dwordx4 v[112:115], v7, s[52:55], s61 offen offset:1152 sc1\n\t"
    "v_mfma_f32_16x16x32_bf16 v[188:191], v[108:111], v[32:35], v[188:191]\n\t"
    "v_cvt_pk_bf16_f32 v161, v120, v124\n\t"
    "v_cvt_pk_bf16_f32 v129, v121, v125\n\t"
    "v_mfma_f32_16x16x32_bf16 v[116:119], v[108:111], v[36:39], 0\n\t"
    "s_barrier\n\t"
    "v_cvt_pk_bf16_f32 v173, v122, v126\n\t"
    "v_mfma_f32_16x16x32_bf16 v[116:119], v[156:159], v[84:87], v[116:119]\n\t"
    "ds_read_b128 v[112:115], v6 offset:6144\n\t"
    "v_cvt_pk_bf16_f32 v169, v123, v127\n\t"
    "v_mfma_f32_16x16x32_bf16 v[192:195], v[156:159], v[88:91], 0\n\t"
    "ds_read_b128 v[180:183], v6 offset:7168\n\t"
    "v_mfma_f32_16x16x32_bf16 v[192:195], v[108:111], v[40:43], v[192:195]\n\t"
    "v_cvt_pk_bf16_f32 v162, v184, v188\n\t"
    "v_cvt_pk_bf16_f32 v130, v185, v189\n\t"
    "v_mfma_f32_16x16x32_bf16 v[120:123], v[108:111], v[44:47], 0\n\t"
    "v_cvt_pk_bf16_f32 v174, v186, v190\n\t"
    "v_cvt_pk_bf16_f32 v170, v187, v191\n\t"
    "v_mfma_f32_16x16x32_bf16 v[120:123], v[156:159], v[92:95], v[120:123]\n\t"
    "v_mfma_f32_16x16x32_bf16 v[124:127], v[156:159], v[96:99], 0\n\t"
    "v_mfma_f32_16x16x32_bf16 v[124:127], v[108:111], v[48:51], v[124:127]\n\t"
    "v_cvt_pk_bf16_f32 v163, v116, v192\n\t"
    "v_cvt_pk_bf16_f32 v131, v117, v193\n\t"
    "v_mfma_f32_16x16x32_bf16 v[184:187], v[108:111], v[52:55], 0\n\t"
    "v_cvt_pk_bf16_f32 v175, v118, v194\n\t"
    "v_cvt_pk_bf16_f32 v171, v119, v195\n\t"
    "v_mfma_f32_16x16x32_bf16 v[184:187], v[156:159], v[100:103], v[184:187]\n\t"
    "v_mfma_f32_16x16x32_bf16 v[188:191], v[156:159], v[104:107], 0\n\t"
    "buffer_store_dwordx2 v[144:145], v8, s[52:55], s61 offen offset:0 sc1\n\t"
    "v_mfma_f32_16x16x32_bf16 v[188:191], v[108:111], v[56:59], v[188:191]\n\t"
    "v_cvt_pk_bf16_f32 v116, v120, v124\n\t"
    "s_waitcnt lgkmcnt(0)\n\t"
    "v_mfma_f32_16x16x32_bf16 v[196:199], v[112:115], v[12:15], 0\n\t"
    "v_cvt_pk_bf16_f32 v118, v121, v125\n\t"
    "v_cvt_pk_bf16_f32 v192, v122, v126\n\t"
    "v_mfma_f32_16x16x32_bf16 v[196:199], v[180:183], v[60:63], v[196:199]\n\t"
    "v_cvt_pk_bf16_f32 v194, v123, v127\n\t"
    "v_mfma_f32_16x16x32_bf16 v[108:111], v[180:183], v[64:67], 0\n\t"
    "v_mfma_f32_16x16x32_bf16 v[108:111], v[112:115], v[16:19], v[108:111]\n\t"
    "v_cvt_pk_bf16_f32 v117, v184, v188\n\t"
    "v_cvt_pk_bf16_f32 v119, v185, v189\n\t"
    "v_mfma_f32_16x16x32_bf16 v[12:15], v[112:115], v[20:23], 0\n\t"
    "v_cvt_pk_bf16_f32 v193, v186, v190\n\t"
    "v_cvt_pk_bf16_f32 v195, v187, v191\n\t"
    "v_mfma_f32_16x16x32_bf16 v[12:15], v[180:183], v[68:71], v[12:15]\n\t"
    "v_mfma_f32_16x16x32_bf16 v[16:19], v[180:183], v[72:75], 0\n\t"
    "v_mfma_f32_16x16x32_bf16 v[16:19], v[112:115], v[24:27], v[16:19]\n\t"
    "ds_read_b128 v[60:63], v0 offset:0\n\t"
    "v_cvt_pk_bf16_f32 v120, v196, v108\n\t"
    "v_mfma_f32_16x16x32_bf16 v[20:23], v[112:115], v[28:31], 0\n\t"
    "ds_read_b128 v[64:67], v1 offset:0\n\t"
    "v_cvt_pk_bf16_f32 v124, v197, v109\n\t"
    "v_mfma_f32_16x16x32_bf16 v[20:23], v[180:183], v[76:79], v[20:23]\n\t"
    "ds_read_b128 v[68:71], v2 offset:0\n\t"
    "v_cvt_pk_bf16_f32 v156, v198, v110\n\t"
    "v_mfma_f32_16x16x32_bf16 v[24:27], v[180:183], v[80:83], 0\n\t"
    "ds_read_b128 v[72:75], v3 offset:0\n\t"
    "v_cvt_pk_bf16_f32 v184, v199, v111\n\t"
    "v_mfma_f32_16x16x32_bf16 v[24:27], v[112:115], v[32:35], v[24:27]\n\t"
    "ds_read_b128 v[188:191], v4 offset:0\n\t"
    "v_cvt_pk_bf16_f32 v121, v12, v16\n\t"
    "v_mfma_f32_16x16x32_bf16 v[28:31], v[112:115], v[36:39], 0\n\t"
    "ds_read_b128 v[76:79], v5 offset:0\n\t"
    "v_cvt_pk_bf16_f32 v125, v13, v17\n\t"
    "v_mfma_f32_16x16x32_bf16 v[28:31], v[180:183], v[84:87], v[28:31]\n\t"
    "buffer_store_dwordx2 v[146:147], v8, s[52:55], s61 offen offset:384 sc1\n\t"
    "v_mfma_f32_16x16x32_bf16 v[32:35], v[180:183], v[88:91], 0\n\t"
    "ds_read_b128 v[80:83], v5 offset:6144\n\t"
    "v_cvt_pk_bf16_f32 v157, v14, v18\n\t"
    "v_mfma_f32_16x16x32_bf16 v[32:35], v[112:115], v[40:43], v[32:35]\n\t"
    "ds_read_b128 v[108:111], v4 offset:6144\n\t"
    "v_cvt_pk_bf16_f32 v185, v15, v19\n\t"
    "v_mfma_f32_16x16x32_bf16 v[36:39], v[112:115], v[44:47], 0\n\t"
    "ds_read_b128 v[84:87], v3 offset:6144\n\t"
    "v_cvt_pk_bf16_f32 v122, v20, v24\n\t"
    "v_mfma_f32_16x16x32_bf16 v[36:39], v[180:183], v[92:95], v[36:39]\n\t"
    "ds_read_b128 v[88:91], v2 offset:6144\n\t"
    "v_cvt_pk_bf16_f32 v126, v21, v25\n\t"
    "v_mfma_f32_16x16x32_bf16 v[12:15], v[180:183], v[96:99], 0\n\t"
    "ds_read_b128 v[144:147], v1 offset:6144\n\t"
    "v_cvt_pk_bf16_f32 v158, v22, v26\n\t"
    "v_mfma_f32_16x16x32_bf16 v[12:15], v[112:115], v[48:51], v[12:15]\n\t"
    "ds_read_b128 v[196:199], v0 offset:6144\n\t"
    "v_cvt_pk_bf16_f32 v186, v23, v27\n\t"
    "v_mfma_f32_16x16x32_bf16 v[16:19], v[112:115], v[52:55], 0\n\t"
    "s_sub_u32 s60, s60, 1\n\t"
    "s_cmp_lg_u32 s60, 0\n\t"
    "v_mfma_f32_16x16x32_bf16 v[16:19], v[180:183], v[100:103], v[16:19]\n\t"
    "v_cvt_pk_bf16_f32 v123, v28, v32\n\t"
    "v_cvt_pk_bf16_f32 v127, v29, v33\n\t"
    "v_mfma_f32_16x16x32_bf16 v[20:23], v[180:183], v[104:107], 0\n\t"
    "buffer_store_dwordx2 v[152:153], v8, s[52:55], s61 offen offset:768 sc1\n\t"
    "v_mfma_f32_16x16x32_bf16 v[20:23], v[112:115], v[56:59], v[20:23]\n\t"
    "s_cbranch_scc1 .Lmin_%=\n\t"
    "buffer_store_dwordx2 v[154:155], v8, s[52:55], s61 offen offset:1152 sc1\n\t"
    "s_add_u32 s61, s61, 393216\n\t"
    "buffer_store_dwordx4 v[136:139], v7, s[52:55], s61 offen offset:0 sc1\n\t"
    "buffer_store_dwordx4 v[132:135], v7, s[52:55], s61 offen offset:384 sc1\n\t"
    "buffer_store_dwordx4 v[164:167], v7, s[52:55], s61 offen offset:768 sc1\n\t"
    "buffer_store_dwordx4 v[140:143], v7, s[52:55], s61 offen offset:1152 sc1\n\t"
    "buffer_store_dwordx2 v[148:149], v8, s[52:55], s61 offen offset:0 sc1\n\t"
    "buffer_store_dwordx2 v[150:151], v8, s[52:55], s61 offen offset:384 sc1\n\t"
    "v_cvt_pk_bf16_f32 v159, v30, v34\n\t"
    "v_cvt_pk_bf16_f32 v187, v31, v35\n\t"
    "v_cvt_pk_bf16_f32 v104, v36, v12\n\t"
    "v_cvt_pk_bf16_f32 v106, v37, v13\n\t"
    "v_cvt_pk_bf16_f32 v180, v38, v14\n\t"
    "v_cvt_pk_bf16_f32 v182, v39, v15\n\t"
    "v_cvt_pk_bf16_f32 v105, v16, v20\n\t"
    "v_cvt_pk_bf16_f32 v107, v17, v21\n\t"
    "v_cvt_pk_bf16_f32 v181, v18, v22\n\t"
    "v_cvt_pk_bf16_f32 v183, v19, v23\n\t"
    "buffer_store_dwordx2 v[176:177], v8, s[52:55], s61 offen offset:768 sc1\n\t"
    "buffer_store_dwordx2 v[178:179], v8, s[52:55], s61 offen offset:1152 sc1\n\t"
    "s_add_u32 s61, s61, 393216\n\t"
    "buffer_store_dwordx4 v[160:163], v7, s[52:55], s61 offen offset:0 sc1\n\t"
    "buffer_store_dwordx4 v[128:131], v7, s[52:55], s61 offen offset:384 sc1\n\t"
    "buffer_store_dwordx4 v[172:175], v7, s[52:55], s61 offen offset:768 sc1\n\t"
    "buffer_store_dwordx4 v[168:171], v7, s[52:55], s61 offen offset:1152 sc1\n\t"
    "buffer_store_dwordx2 v[116:117], v8, s[52:55], s61 offen offset:0 sc1\n\t"
    "buffer_store_dwordx2 v[118:119], v8, s[52:55], s61 offen offset:384 sc1\n\t"
    "buffer_store_dwordx2 v[192:193], v8, s[52:55], s61 offen offset:768 sc1\n\t"
    "buffer_store_dwordx2 v[194:195], v8, s[52:55], s61 offen offset:1152 sc1\n\t"
    "s_add_u32 s61, s61, 393216\n\t"
    "buffer_store_dwordx4 v[120:123], v7, s[52:55], s61 offen offset:0 sc1\n\t"
    "buffer_store_dwordx4 v[124:127], v7, s[52:55], s61 offen offset:384 sc1\n\t"
    "buffer_store_dwordx4 v[156:159], v7, s[52:55], s61 offen offset:768 sc1\n\t"
    "buffer_store_dwordx4 v[184:187], v7, s[52:55], s61 offen offset:1152 sc1\n\t"
    "buffer_store_dwordx2 v[104:105], v8, s[52:55], s61 offen offset:0 sc1\n\t"
    "buffer_store_dwordx2 v[106:107], v8, s[52:55], s61 offen offset:384 sc1\n\t"
    "buffer_store_dwordx2 v[180:181], v8, s[52:55], s61 offen offset:768 sc1\n\t"
    "buffer_store_dwordx2 v[182:183], v8, s[52:55], s61 offen offset:1152 sc1\n\t"
    "s_endpgm\n\t"
    ".p2align 12\n\t"
    ".Lroln_%=:\n\t"
    "v_lshrrev_b32_e32 v20, 3, v31\n\t"
    "v_and_b32_e32 v21, 6, v20\n\t"
    "v_and_b32_e32 v28, 7, v31\n\t"
    "v_xor_b32_e32 v21, v21, v28\n\t"
    "v_lshlrev_b32_e32 v20, 7, v20\n\t"
    "v_lshl_or_b32 v7, v21, 4, v20\n\t"
    "s_mov_b32 s54, 0x6000000\n\t"
    "s_mov_b32 s55, 0x20000\n\t"
    "s_waitcnt lgkmcnt(0)\n\t"
    "s_memrealtime s[72:73]\n\t"
    "s_mov_b32 s52, s36\n\t"
    "s_and_b32 s53, s37, 0xffff\n\t"
    "s_barrier\n\t"
    "s_add_u32 m0, %[rbeg], 0\n\t"
    "s_add_u32 s61, %[wb], 0\n\t"
    "buffer_load_dwordx4 v7, s[52:55], s61 offen offset:0 nt lds\n\t"
    "buffer_load_dwordx4 v7, s[52:55], s61 offen offset:1024 nt lds\n\t"
    "buffer_load_dwordx4 v7, s[52:55], s61 offen offset:2048 nt lds\n\t"
    "s_add_u32 m0, %[rbeg], 3072\n\t"
    "s_add_u32 s61, %[wb], 196608\n\t"
    "buffer_load_dwordx4 v7, s[52:55], s61 offen offset:0 nt lds\n\t"
    "buffer_load_dwordx4 v7, s[52:55], s61 offen offset:1024 nt lds\n\t"
    "buffer_load_dwordx4 v7, s[52:55], s61 offen offset:2048 nt lds\n\t"
    "v_lshlrev_b32_e32 v30, 4, v31\n\t"
    "v_and_b32_e32 v28, 7, v31\n\t"
    "v_bfe_u32 v27, v31, 3, 1\n\t"
    "v_lshrrev_b32_e32 v29, 4, v31\n\t"
    "v_add_u32_e32 v6, %[pl], v30\n\t"
    "v_mul_u32_u24_e32 v27, 0xc00, v27\n\t"
    "v_add_u32_e32 v27, %[rbeg], v27\n\t"
    "v_lshlrev_b32_e32 v26, 7, v31\n\t"
    "v_mad_u32_u24 v20, v28, 3, 0\n\t"
    "v_and_b32_e32 v21, 6, v20\n\t"
    "v_xor_b32_e32 v21, v21, v29\n\t"
    "v_lshl_add_u32 v20, v20, 7, v27\n\t"
    "v_lshl_add_u32 v0, v21, 4, v20\n\t"
    "v_xor_b32_e32 v1, 64, v0\n\t"
    "v_mad_u32_u24 v20, v28, 3, 1\n\t"
    "v_and_b32_e32 v21, 6, v20\n\t"
    "v_xor_b32_e32 v21, v21, v29\n\t"
    "v_lshl_add_u32 v20, v20, 7, v27\n\t"
    "v_lshl_add_u32 v2, v21, 4, v20\n\t"
    "v_xor_b32_e32 v3, 64, v2\n\t"
    "v_mad_u32_u24 v20, v28, 3, 2\n\t"
    "v_and_b32_e32 v21, 6, v20\n\t"
    "v_xor_b32_e32 v21, v21, v29\n\t"
    "v_lshl_add_u32 v20, v20, 7, v27\n\t"
    "v_lshl_add_u32 v4, v21, 4, v20\n\t"
    "v_xor_b32_e32 v5, 64, v4\n\t"
    "s_mov_b32 s56, s42\n\t"
    "s_and_b32 s57, s43, 0xffff\n\t"
    "s_mov_b32 s58, 0x40000\n\t"
    "s_mov_b32 s59, 0x20000\n\t"
    "v_add_u32_e32 v8, 0x9000, v30\n\t"
    "v_add_u32_e32 v9, 0x1b000, v30\n\t"
    "s_mov_b32 s68, 0x3dd53b95\n\t"
    "s_add_u32 m0, %[rbeg], 6144\n\t"
    "s_add_u32 s61, %[wb], 393216\n\t"
    "buffer_load_dwordx4 v7, s[52:55], s61 offen offset:0 nt lds\n\t"
    "buffer_load_dwordx4 v7, s[52:55], s61 offen offset:1024 nt lds\n\t"
    "buffer_load_dwordx4 v7, s[52:55], s61 offen offset:2048 nt lds\n\t"
    "s_add_u32 m0, %[rbeg], 9216\n\t"
    "s_add_u32 s61, %[wb], 589824\n\t"
    "buffer_load_dwordx4 v7, s[52:55], s61 offen offset:0 nt lds\n\t"
    "buffer_load_dwordx4 v7, s[52:55], s61 offen offset:1024 nt lds\n\t"
    "buffer_load_dwordx4 v7, s[52:55], s61 offen offset:2048 nt lds\n\t"
    "s_mov_b64 exec, 0\n\t"
    "s_cmp_eq_u32 %[pair], 0\n\t"
    "s_cbranch_scc1 .Lh4_%=\n\t"
    "s_branch .Lh1_%=\n\t"
    ".LrM_%=:\n\t"
    "s_mov_b64 exec, -1\n\t"
    ".Ltx3_%=:\n\t"
    "s_memrealtime s[70:71]\n\t"
    "s_waitcnt lgkmcnt(0)\n\t"
    "s_sub_u32 s70, s70, s72\n\t"
    "s_cmp_lt_u32 s70, 115\n\t"
    "s_cbranch_scc1 .Ltx3_%=\n\t"
    "s_add_u32 m0, %[rbeg], 12288\n\t"
    "s_add_u32 s61, %[wb], 786432\n\t"
    "buffer_load_dwordx4 v7, s[52:55], s61 offen offset:0 nt lds\n\t"
    "buffer_load_dwordx4 v7, s[52:55], s61 offen offset:1024 nt lds\n\t"
    "buffer_load_dwordx4 v7, s[52:55], s61 offen offset:2048 nt lds\n\t"
    "s_add_u32 m0, %[rbeg], 15360\n\t"
    "s_add_u32 s61, %[wb], 983040\n\t"
    "buffer_load_dwordx4 v7, s[52:55], s61 offen offset:0 nt lds\n\t"
    "buffer_load_dwordx4 v7, s[52:55], s61 offen offset:1024 nt lds\n\t"
    "buffer_load_dwordx4 v7, s[52:55], s61 offen offset:2048 nt lds\n\t"
    "s_add_u32 m0, %[rbeg], 18432\n\t"
    "s_add_u32 s61, %[wb], 1179648\n\t"
    "buffer_load_dwordx4 v7, s[52:55], s61 offen offset:0 nt lds\n\t"
    "buffer_load_dwordx4 v7, s[52:55], s61 offen offset:1024 nt lds\n\t"
    "buffer_load_dwordx4 v7, s[52:55], s61 offen offset:2048 nt lds\n\t"
    "s_add_u32 m0, %[rbeg], 21504\n\t"
    "s_add_u32 s61, %[wb], 1376256\n\t"
    "buffer_load_dwordx4 v7, s[52:55], s61 offen offset:0 nt lds\n\t"
    "buffer_load_dwordx4 v7, s[52:55], s61 offen offset:1024 nt lds\n\t"
    "buffer_load_dwordx4 v7, s[52:55], s61 offen offset:2048 nt lds\n\t"
    "s_waitcnt vmcnt(0)\n\t"
    "s_barrier\n\t"
    "ds_read_b128 v[188:191], v2 offset:6144\n\t"
    "ds_read_b128 v[192:195], v1 offset:6144\n\t"
    "ds_read_b128 v[196:199], v0 offset:6144\n\t"
    "ds_read_b128 v[172:175], v0 offset:12288\n\t"
    "ds_read_b128 v[168:171], v1 offset:12288\n\t"
    "ds_read_b128 v[164:167], v2 offset:12288\n\t"
    "ds_read_b128 v[140:143], v2 offset:18432\n\t"
    "ds_read_b128 v[144:147], v1 offset:18432\n\t"
    "ds_read_b128 v[148:151], v0 offset:18432\n\t"
    "ds_read_b128 v[184:187], v3 offset:6144\n\t"
    "ds_read_b128 v[180:183], v4 offset:6144\n\t"
    "ds_read_b128 v[176:179], v5 offset:6144\n\t"
    "ds_read_b128 v[208:211], v3 offset:0\n\t"
    "ds_read_b128 v[204:207], v4 offset:0\n\t"
    "ds_read_b128 v[200:203], v5 offset:0\n\t"
    "ds_read_b128 v[160:163], v3 offset:12288\n\t"
    "ds_read_b128 v[156:159], v4 offset:12288\n\t"
    "ds_read_b128 v[152:155], v5 offset:12288\n\t"
    "ds_read_b128 v[128:131], v5 offset:18432\n\t"
    "ds_read_b128 v[132:135], v4 offset:18432\n\t"
    "ds_read_b128 v[136:139], v3 offset:18432\n\t"
    "ds_read_b128 v[220:223], v0 offset:0\n\t"
    "ds_read_b128 v[216:219], v1 offset:0\n\t"
    "ds_read_b128 v[212:215], v2 offset:0\n\t"
    "ds_read_b128 v[28:31], v3 offset:0\n\t"
    "ds_read_b128 v[32:35], v4 offset:0\n\t"
    "ds_read_b128 v[40:43], v5 offset:0\n\t"
    "s_add_i32 s63, %[rbeg], 24576\n\t"
    "s_add_i32 s62, %[rbeg], 49152\n\t"
    "s_barrier\n\t"
    "ds_read_b128 a[0:3], v8 offset:0\n\t"
    "ds_read_b128 a[8:11], v8 offset:1024\n\t"
    "ds_read_b128 a[16:19], v8 offset:2048\n\t"
    "ds_read_b128 a[24:27], v8 offset:3072\n\t"
    "ds_read_b128 a[32:35], v8 offset:4096\n\t"
    "ds_read_b128 a[40:43], v8 offset:5120\n\t"
    "ds_read_b128 a[44:47], v9 offset:5120\n\t"
    "ds_read_b128 a[36:39], v9 offset:4096\n\t"
    "ds_read_b128 a[28:31], v9 offset:3072\n\t"
    "ds_read_b128 a[20:23], v9 offset:2048\n\t"
    "ds_read_b128 a[12:15], v9 offset:1024\n\t"
    "s_waitcnt lgkmcnt(10)\n\t"
    "v_mfma_f32_16x16x32_bf16 v[64:67], a[0:3], v[220:223], 0\n\t"
    "ds_read_b128 a[4:7], v9 offset:0\n\t"
    "s_waitcnt lgkmcnt(10)\n\t"
    "v_mfma_f32_16x16x32_bf16 v[64:67], a[8:11], v[216:219], v[64:67]\n\t"
    "s_barrier\n\t"
    "ds_read_b128 a[48:51], v8 offset:6144\n\t"
    "s_waitcnt lgkmcnt(10)\n\t"
    "v_mfma_f32_16x16x32_bf16 v[64:67], a[16:19], v[212:215], v[64:67]\n\t"
    "ds_read_b128 a[56:59], v8 offset:7168\n\t"
    "s_waitcnt lgkmcnt(10)\n\t"
    "v_mfma_f32_16x16x32_bf16 v[64:67], a[24:27], v[28:31], v[64:67]\n\t"
    "ds_read_b128 a[64:67], v8 offset:8192\n\t"
    "s_waitcnt lgkmcnt(4)\n\t"
    "v_mfma_f32_16x16x32_bf16 v[64:67], a[32:35], v[32:35], v[64:67]\n\t"
    "ds_read_b128 a[72:75], v8 offset:9216\n\t"
    "v_mfma_f32_16x16x32_bf16 v[64:67], a[40:43], v[40:43], v[64:67]\n\t"
    "ds_read_b128 a[80:83], v8 offset:10240\n\t"
    "v_mfma_f32_16x16x32_bf16 v[56:59], a[44:47], v[40:43], 0\n\t"
    "ds_read_b128 a[88:91], v8 offset:11264\n\t"
    "v_mfma_f32_16x16x32_bf16 v[56:59], a[36:39], v[32:35], v[56:59]\n\t"
    "ds_read_b128 a[92:95], v9 offset:11264\n\t"
    "v_mfma_f32_16x16x32_bf16 v[56:59], a[28:31], v[28:31], v[56:59]\n\t"
    "ds_read_b128 a[84:87], v9 offset:10240\n\t"
    "v_mfma_f32_16x16x32_bf16 v[56:59], a[20:23], v[212:215], v[56:59]\n\t"
    "ds_read_b128 a[76:79], v9 offset:9216\n\t"
    "v_mfma_f32_16x16x32_bf16 v[56:59], a[12:15], v[216:219], v[56:59]\n\t"
    "v_cvt_pk_bf16_f32 v44, v64, v65\n\t"
    "v_cvt_pk_bf16_f32 v45, v66, v67\n\t"
    "ds_read_b128 a[68:71], v9 offset:8192\n\t"
    "s_waitcnt lgkmcnt(4)\n\t"
    "v_mfma_f32_16x16x32_bf16 v[56:59], a[4:7], v[220:223], v[56:59]\n\t"
    "ds_read_b128 a[60:63], v9 offset:7168\n\t"
    "v_mfma_f32_16x16x32_bf16 v[36:39], a[48:51], v[220:223], 0\n\t"
    "ds_read_b128 a[52:55], v9 offset:6144\n\t"
    "v_mfma_f32_16x16x32_bf16 v[36:39], a[56:59], v[216:219], v[36:39]\n\t"
    "s_barrier\n\t"
    "ds_read_b128 a[96:99], v8 offset:12288\n\t"
    "v_mfma_f32_16x16x32_bf16 v[36:39], a[64:67], v[212:215], v[36:39]\n\t"
    "ds_read_b128 a[104:107], v8 offset:13312\n\t"
    "v_mfma_f32_16x16x32_bf16 v[36:39], a[72:75], v[28:31], v[36:39]\n\t"
    "v_cvt_pk_bf16_f32 v46, v56, v57\n\t"
    "ds_read_b128 a[112:115], v8 offset:14336\n\t"
    "v_mfma_f32_16x16x32_bf16 v[36:39], a[80:83], v[32:35], v[36:39]\n\t"
    "v_cvt_pk_bf16_f32 v47, v58, v59\n\t"
    "ds_read_b128 a[120:123], v8 offset:15360\n\t"
    "v_mfma_f32_16x16x32_bf16 v[36:39], a[88:91], v[40:43], v[36:39]\n\t"
    "ds_read_b128 a[128:131], v8 offset:16384\n\t"
    "s_waitcnt lgkmcnt(4)\n\t"
    "v_mfma_f32_16x16x32_bf16 v[48:51], a[92:95], v[40:43], 0\n\t"
    "ds_read_b128 a[136:139], v8 offset:17408\n\t"
    "v_mfma_f32_16x16x32_bf16 v[48:51], a[84:87], v[32:35], v[48:51]\n\t"
    "ds_read_b128 a[140:143], v9 offset:17408\n\t"
    "v_mfma_f32_16x16x32_bf16 v[48:51], a[76:79], v[28:31], v[48:51]\n\t"
    "ds_read_b128 a[132:135], v9 offset:16384\n\t"
    "v_mfma_f32_16x16x32_bf16 v[48:51], a[68:71], v[212:215], v[48:51]\n\t"
    "v_cvt_pk_bf16_f32 v56, v36, v37\n\t"
    "ds_read_b128 a[124:127], v9 offset:15360\n\t"
    "v_mfma_f32_16x16x32_bf16 v[48:51], a[60:63], v[216:219], v[48:51]\n\t"
    "v_cvt_pk_bf16_f32 v57, v38, v39\n\t"
    "ds_read_b128 a[116:119], v9 offset:14336\n\t"
    "v_mfma_f32_16x16x32_bf16 v[48:51], a[52:55], v[220:223], v[48:51]\n\t"
    "ds_read_b128 a[108:111], v9 offset:13312\n\t"
    "v_mfma_f32_16x16x32_bf16 v[60:63], a[96:99], v[220:223], 0\n\t"
    "ds_read_b128 a[100:103], v9 offset:12288\n\t"
    "s_waitcnt lgkmcnt(4)\n\t"
    "v_mfma_f32_16x16x32_bf16 v[60:63], a[104:107], v[216:219], v[60:63]\n\t"
    "ds_read_b128 a[144:147], v8 offset:18432\n\t"
    "v_mfma_f32_16x16x32_bf16 v[60:63], a[112:115], v[212:215], v[60:63]\n\t"
    "ds_read_b128 a[152:155], v8 offset:19456\n\t"
    "v_mfma_f32_16x16x32_bf16 v[60:63], a[120:123], v[28:31], v[60:63]\n\t"
    "v_cvt_pk_bf16_f32 v58, v48, v49\n\t"
    "v_cvt_pk_bf16_f32 v59, v50, v51\n\t"
    "ds_read_b128 a[160:163], v8 offset:20480\n\t"
    "v_mfma_f32_16x16x32_bf16 v[60:63], a[128:131], v[32:35], v[60:63]\n\t"
    "ds_read_b128 a[168:171], v8 offset:21504\n\t"
    "v_mfma_f32_16x16x32_bf16 v[60:63], a[136:139], v[40:43], v[60:63]\n\t"
    "ds_read_b128 a[176:179], v8 offset:22528\n\t"
    "v_mfma_f32_16x16x32_bf16 v[36:39], a[140:143], v[40:43], 0\n\t"
    "ds_read_b128 a[184:187], v8 offset:23552\n\t"
    "v_mfma_f32_16x16x32_bf16 v[36:39], a[132:135], v[32:35], v[36:39]\n\t"
    "s_waitcnt lgkmcnt(3)\n\t"
    "v_mfma_f32_16x16x32_bf16 v[36:39], a[124:127], v[28:31], v[36:39]\n\t"
    "v_mfma_f32_16x16x32_bf16 v[36:39], a[116:119], v[212:215], v[36:39]\n\t"
    "v_cvt_pk_bf16_f32 v48, v60, v61\n\t"
    "v_cvt_pk_bf16_f32 v49, v62, v63\n\t"
    "v_mfma_f32_16x16x32_bf16 v[36:39], a[108:111], v[216:219], v[36:39]\n\t"
    "v_mfma_f32_16x16x32_bf16 v[36:39], a[100:103], v[220:223], v[36:39]\n\t"
    "v_mfma_f32_16x16x32_bf16 v[60:63], a[144:147], v[220:223], 0\n\t"
    "v_mfma_f32_16x16x32_bf16 v[60:63], a[152:155], v[216:219], v[60:63]\n\t"
    "v_mfma_f32_16x16x32_bf16 v[60:63], a[160:163], v[212:215], v[60:63]\n\t"
    "s_waitcnt lgkmcnt(0)\n\t"
    "v_mfma_f32_16x16x32_bf16 v[60:63], a[168:171], v[28:31], v[60:63]\n\t"
    "v_cvt_pk_bf16_f32 v50, v36, v37\n\t"
    "v_mfma_f32_16x16x32_bf16 v[60:63], a[176:179], v[32:35], v[60:63]\n\t"
    "v_cvt_pk_bf16_f32 v51, v38, v39\n\t"
    "v_mfma_f32_16x16x32_bf16 v[60:63], a[184:187], v[40:43], v[60:63]\n\t"
    "v_mfma_f32_16x16x32_bf16 v[64:67], v[220:223], v[44:47], 0\n\t"
    "v_mfma_f32_16x16x32_bf16 v[64:67], v[216:219], v[56:59], v[64:67]\n\t"
    "ds_read_b128 a[188:191], v9 offset:23552\n\t"
    "v_mfma_f32_16x16x32_bf16 v[64:67], v[212:215], v[48:51], v[64:67]\n\t"
    "ds_read_b128 a[180:183], v9 offset:22528\n\t"
    "v_mfma_f32_16x16x32_bf16 v[36:39], v[188:191], v[48:51], 0\n\t"
    "ds_read_b128 a[172:175], v9 offset:21504\n\t"
    "v_mfma_f32_16x16x32_bf16 v[36:39], v[192:195], v[56:59], v[36:39]\n\t"
    "ds_read_b128 a[164:167], v9 offset:20480\n\t"
    "v_mfma_f32_16x16x32_bf16 v[36:39], v[196:199], v[44:47], v[36:39]\n\t"
    "ds_read_b128 a[156:159], v9 offset:19456\n\t"
    "v_mfma_f32_16x16x32_bf16 v[68:71], v[172:175], v[44:47], 0\n\t"
    "ds_read_b128 a[148:151], v9 offset:18432\n\t"
    "v_mfma_f32_16x16x32_bf16 v[68:71], v[168:171], v[56:59], v[68:71]\n\t"
    "s_barrier\n\t"
    "ds_read_b128 a[192:195], v8 offset:24576\n\t"
    "v_mfma_f32_16x16x32_bf16 v[68:71], v[164:167], v[48:51], v[68:71]\n\t"
    "ds_read_b128 a[200:203], v8 offset:25600\n\t"
    "v_mfma_f32_16x16x32_bf16 v[72:75], v[140:143], v[48:51], 0\n\t"
    "ds_read_b128 a[208:211], v8 offset:26624\n\t"
    "v_mfma_f32_16x16x32_bf16 v[72:75], v[144:147], v[56:59], v[72:75]\n\t"
    "ds_read_b128 a[216:219], v8 offset:27648\n\t"
    "v_mfma_f32_16x16x32_bf16 v[72:75], v[148:151], v[44:47], v[72:75]\n\t"
    "ds_read_b128 a[224:227], v8 offset:28672\n\t"
    "s_waitcnt lgkmcnt(4)\n\t"
    "v_mfma_f32_16x16x32_bf16 v[48:51], a[188:191], v[40:43], 0\n\t"
    "ds_read_b128 a[232:235], v8 offset:29696\n\t"
    "v_mfma_f32_16x16x32_bf16 v[48:51], a[180:183], v[32:35], v[48:51]\n\t"
    "ds_read_b128 a[236:239], v9 offset:29696\n\t"
    "v_mfma_f32_16x16x32_bf16 v[48:51], a[172:175], v[28:31], v[48:51]\n\t"
    "ds_read_b128 a[228:231], v9 offset:28672\n\t"
    "v_mfma_f32_16x16x32_bf16 v[48:51], a[164:167], v[212:215], v[48:51]\n\t"
    "ds_read_b128 a[220:223], v9 offset:27648\n\t"
    "v_mfma_f32_16x16x32_bf16 v[48:51], a[156:159], v[216:219], v[48:51]\n\t"
    "ds_read_b128 a[212:215], v9 offset:26624\n\t"
    "v_mfma_f32_16x16x32_bf16 v[48:51], a[148:151], v[220:223], v[48:51]\n\t"
    "ds_read_b128 a[204:207], v9 offset:25600\n\t"
    "v_mfma_f32_16x16x32_bf16 v[44:47], a[192:195], v[220:223], 0\n\t"
    "ds_read_b128 a[196:199], v9 offset:24576\n\t"
    "s_waitcnt lgkmcnt(4)\n\t"
    "v_mfma_f32_16x16x32_bf16 v[44:47], a[200:203], v[216:219], v[44:47]\n\t"
    "v_cvt_pk_bf16_f32 v56, v60, v61\n\t"
    "ds_read_b128 a[240:243], v8 offset:30720\n\t"
    "v_mfma_f32_16x16x32_bf16 v[44:47], a[208:211], v[212:215], v[44:47]\n\t"
    "ds_read_b128 a[248:251], v8 offset:31744\n\t"
    "v_mfma_f32_16x16x32_bf16 v[44:47], a[216:219], v[28:31], v[44:47]\n\t"
    "v_cvt_pk_bf16_f32 v58, v48, v49\n\t"
    "ds_read_b128 v[224:227], v8 offset:32768\n\t"
    "v_mfma_f32_16x16x32_bf16 v[44:47], a[224:227], v[32:35], v[44:47]\n\t"
    "v_cvt_pk_bf16_f32 v59, v50, v51\n\t"
    "ds_read_b128 v[232:235], v8 offset:33792\n\t"
    "v_mfma_f32_16x16x32_bf16 v[44:47], a[232:235], v[40:43], v[44:47]\n\t"
    "v_cvt_pk_bf16_f32 v57, v62, v63\n\t"
    "ds_read_b128 v[240:243], v8 offset:34816\n\t"
    "v_mfma_f32_16x16x32_bf16 v[76:79], a[236:239], v[40:43], 0\n\t"
    "ds_read_b128 v[248:251], v8 offset:35840\n\t"
    "v_mfma_f32_16x16x32_bf16 v[76:79], a[228:231], v[32:35], v[76:79]\n\t"
    "ds_read_b128 v[252:255], v9 offset:35840\n\t"
    "s_waitcnt lgkmcnt(4)\n\t"
    "v_mfma_f32_16x16x32_bf16 v[76:79], a[220:223], v[28:31], v[76:79]\n\t"
    "ds_read_b128 v[244:247], v9 offset:34816\n\t"
    "v_mfma_f32_16x16x32_bf16 v[76:79], a[212:215], v[212:215], v[76:79]\n\t"
    "v_cvt_pk_bf16_f32 v48, v44, v45\n\t"
    "ds_read_b128 v[236:239], v9 offset:33792\n\t"
    "v_mfma_f32_16x16x32_bf16 v[76:79], a[204:207], v[216:219], v[76:79]\n\t"
    "v_cvt_pk_bf16_f32 v49, v46, v47\n\t"
    "ds_read_b128 v[228:231], v9 offset:32768\n\t"
    "v_mfma_f32_16x16x32_bf16 v[76:79], a[196:199], v[220:223], v[76:79]\n\t"
    "ds_read_b128 a[252:255], v9 offset:31744\n\t"
    "v_mfma_f32_16x16x32_bf16 v[80:83], a[240:243], v[220:223], 0\n\t"
    "ds_read_b128 a[244:247], v9 offset:30720\n\t"
    "v_mfma_f32_16x16x32_bf16 v[80:83], a[248:251], v[216:219], v[80:83]\n\t"
    "v_mfma_f32_16x16x32_bf16 v[80:83], v[224:227], v[212:215], v[80:83]\n\t"
    "s_waitcnt lgkmcnt(2)\n\t"
    "v_mfma_f32_16x16x32_bf16 v[80:83], v[232:235], v[28:31], v[80:83]\n\t"
    "v_cvt_pk_bf16_f32 v50, v76, v77\n\t"
    "v_cvt_pk_bf16_f32 v51, v78, v79\n\t"
    "v_mfma_f32_16x16x32_bf16 v[80:83], v[240:243], v[32:35], v[80:83]\n\t"
    "v_mfma_f32_16x16x32_bf16 v[80:83], v[248:251], v[40:43], v[80:83]\n\t"
    "v_mfma_f32_16x16x32_bf16 v[12:15], v[252:255], v[40:43], 0\n\t"
    "v_mfma_f32_16x16x32_bf16 v[12:15], v[244:247], v[32:35], v[12:15]\n\t"
    "v_mfma_f32_16x16x32_bf16 v[12:15], v[236:239], v[28:31], v[12:15]\n\t"
    "v_mfma_f32_16x16x32_bf16 v[12:15], v[228:231], v[212:215], v[12:15]\n\t"
    "v_cvt_pk_bf16_f32 v16, v80, v81\n\t"
    "v_cvt_pk_bf16_f32 v17, v82, v83\n\t"
    "s_waitcnt lgkmcnt(0)\n\t"
    "v_mfma_f32_16x16x32_bf16 v[12:15], a[252:255], v[216:219], v[12:15]\n\t"
    "v_mfma_f32_16x16x32_bf16 v[12:15], a[244:247], v[220:223], v[12:15]\n\t"
    "v_mfma_f32_16x16x32_bf16 v[20:23], a[0:3], v[196:199], 0\n\t"
    "v_mfma_f32_16x16x32_bf16 v[20:23], a[8:11], v[192:195], v[20:23]\n\t"
    "v_mfma_f32_16x16x32_bf16 v[20:23], a[16:19], v[188:191], v[20:23]\n\t"
    "v_mfma_f32_16x16x32_bf16 v[20:23], a[24:27], v[184:187], v[20:23]\n\t"
    "v_cvt_pk_bf16_f32 v18, v12, v13\n\t"
    "v_mfma_f32_16x16x32_bf16 v[20:23], a[32:35], v[180:183], v[20:23]\n\t"
    "v_mfma_f32_16x16x32_bf16 v[20:23], a[40:43], v[176:179], v[20:23]\n\t"
    "v_cvt_pk_bf16_f32 v19, v14, v15\n\t"
    "v_mfma_f32_16x16x32_bf16 v[64:67], v[208:211], v[56:59], v[64:67]\n\t"
    "v_mfma_f32_16x16x32_bf16 v[64:67], v[204:207], v[48:51], v[64:67]\n\t"
    "v_mfma_f32_16x16x32_bf16 v[64:67], v[200:203], v[16:19], v[64:67]\n\t"
    "v_mfma_f32_16x16x32_bf16 v[36:39], v[176:179], v[16:19], v[36:39]\n\t"
    "v_cvt_pk_bf16_f32 v12, v20, v21\n\t"
    "v_cvt_pk_bf16_f32 v13, v22, v23\n\t"
    "v_mfma_f32_16x16x32_bf16 v[36:39], v[180:183], v[48:51], v[36:39]\n\t"
    "v_mfma_f32_16x16x32_bf16 v[36:39], v[184:187], v[56:59], v[36:39]\n\t"
    "v_mfma_f32_16x16x32_bf16 v[68:71], v[160:163], v[56:59], v[68:71]\n\t"
    "v_max3_f32 v20, v64, v65, v66\n\t"
    "v_mfma_f32_16x16x32_bf16 v[68:71], v[156:159], v[48:51], v[68:71]\n\t"
    "v_mfma_f32_16x16x32_bf16 v[68:71], v[152:155], v[16:19], v[68:71]\n\t"
    "v_mfma_f32_16x16x32_bf16 v[72:75], v[128:131], v[16:19], v[72:75]\n\t"
    "v_max3_f32 v21, v67, v36, v37\n\t"
    "v_max3_f32 v20, v20, v38, v39\n\t"
    "v_mfma_f32_16x16x32_bf16 v[72:75], v[132:135], v[48:51], v[72:75]\n\t"
    "v_mfma_f32_16x16x32_bf16 v[72:75], v[136:139], v[56:59], v[72:75]\n\t"
    "v_mfma_f32_16x16x32_bf16 v[16:19], a[44:47], v[176:179], 0\n\t"
    "v_max3_f32 v21, v21, v68, v69\n\t"
    "v_max3_f32 v20, v20, v70, v71\n\t"
    "v_mfma_f32_16x16x32_bf16 v[16:19], a[36:39], v[180:183], v[16:19]\n\t"
    "v_mfma_f32_16x16x32_bf16 v[16:19], a[28:31], v[184:187], v[16:19]\n\t"
    "v_mfma_f32_16x16x32_bf16 v[16:19], a[20:23], v[188:191], v[16:19]\n\t"
    "v_max3_f32 v21, v21, v72, v73\n\t"
    "v_max3_f32 v20, v20, v74, v75\n\t"
    "v_mfma_f32_16x16x32_bf16 v[16:19], a[12:15], v[192:195], v[16:19]\n\t"
    "v_max_f32_e32 v20, v20, v21\n\t"
    "v_mov_b32_e32 v21, v20\n\t"
    "v_mfma_f32_16x16x32_bf16 v[16:19], a[4:7], v[196:199], v[16:19]\n\t"
    "v_mfma_f32_16x16x32_bf16 v[24:27], a[48:51], v[196:199], 0\n\t"
    "v_permlane32_swap_b32 v20, v21\n\t"
    "v_max_f32_e32 v20, v20, v21\n\t"
    "v_mfma_f32_16x16x32_bf16 v[24:27], a[56:59], v[192:195], v[24:27]\n\t"
    "v_mov_b32_e32 v21, v20\n\t"
    "v_mfma_f32_16x16x32_bf16 v[24:27], a[64:67], v[188:191], v[24:27]\n\t"
    "v_mfma_f32_16x16x32_bf16 v[24:27], a[72:75], v[184:187], v[24:27]\n\t"
    "v_cvt_pk_bf16_f32 v14, v16, v17\n\t"
    "v_cvt_pk_bf16_f32 v15, v18, v19\n\t"
    "v_mfma_f32_16x16x32_bf16 v[24:27], a[80:83], v[180:183], v[24:27]\n\t"
    "v_permlane16_swap_b32 v20, v21\n\t"
    "v_max_f32_e32 v20, v20, v21\n\t"
    "v_mfma_f32_16x16x32_bf16 v[24:27], a[88:91], v[176:179], v[24:27]\n\t"
    "v_sub_f32_e32 v64, v64, v20\n\t"
    "v_sub_f32_e32 v65, v65, v20\n\t"
    "v_mfma_f32_16x16x32_bf16 v[16:19], a[92:95], v[176:179], 0\n\t"
    "v_exp_f32_e32 v64, v64\n\t"
    "v_mfma_f32_16x16x32_bf16 v[16:19], a[84:87], v[180:183], v[16:19]\n\t"
    "v_exp_f32_e32 v65, v65\n\t"
    "v_mfma_f32_16x16x32_bf16 v[16:19], a[76:79], v[184:187], v[16:19]\n\t"
    "v_add_f32_e32 v22, v64, v65\n\t"
    "v_sub_f32_e32 v66, v66, v20\n\t"
    "v_mfma_f32_16x16x32_bf16 v[16:19], a[68:71], v[188:191], v[16:19]\n\t"
    "v_cvt_pk_bf16_f32 v28, v24, v25\n\t"
    "v_cvt_pk_bf16_f32 v29, v26, v27\n\t"
    "v_mfma_f32_16x16x32_bf16 v[16:19], a[60:63], v[192:195], v[16:19]\n\t"
    "v_exp_f32_e32 v66, v66\n\t"
    "v_mfma_f32_16x16x32_bf16 v[16:19], a[52:55], v[196:199], v[16:19]\n\t"
    "v_sub_f32_e32 v67, v67, v20\n\t"
    "v_sub_f32_e32 v36, v36, v20\n\t"
    "v_mfma_f32_16x16x32_bf16 v[24:27], a[96:99], v[196:199], 0\n\t"
    "v_exp_f32_e32 v67, v67\n\t"
    "v_mfma_f32_16x16x32_bf16 v[24:27], a[104:107], v[192:195], v[24:27]\n\t"
    "v_add_f32_e32 v23, v66, v67\n\t"
    "v_sub_f32_e32 v37, v37, v20\n\t"
    "v_mfma_f32_16x16x32_bf16 v[24:27], a[112:115], v[188:191], v[24:27]\n\t"
    "v_exp_f32_e32 v36, v36\n\t"
    "v_mfma_f32_16x16x32_bf16 v[24:27], a[120:123], v[184:187], v[24:27]\n\t"
    "v_cvt_pk_bf16_f32 v30, v16, v17\n\t"
    "v_cvt_pk_bf16_f32 v31, v18, v19\n\t"
    "v_mfma_f32_16x16x32_bf16 v[24:27], a[128:131], v[180:183], v[24:27]\n\t"
    "v_add_f32_e32 v22, v22, v36\n\t"
    "v_sub_f32_e32 v38, v38, v20\n\t"
    "v_mfma_f32_16x16x32_bf16 v[24:27], a[136:139], v[176:179], v[24:27]\n\t"
    "v_exp_f32_e32 v37, v37\n\t"
    "v_mfma_f32_16x16x32_bf16 v[16:19], a[140:143], v[176:179], 0\n\t"
    "v_add_f32_e32 v22, v22, v37\n\t"
    "v_sub_f32_e32 v39, v39, v20\n\t"
    "v_mfma_f32_16x16x32_bf16 v[16:19], a[132:135], v[180:183], v[16:19]\n\t"
    "v_exp_f32_e32 v38, v38\n\t"
    "v_mfma_f32_16x16x32_bf16 v[16:19], a[124:127], v[184:187], v[16:19]\n\t"
    "v_add_f32_e32 v23, v23, v38\n\t"
    "v_sub_f32_e32 v68, v68, v20\n\t"
    "v_mfma_f32_16x16x32_bf16 v[16:19], a[116:119], v[188:191], v[16:19]\n\t"
    "v_cvt_pk_bf16_f32 v32, v24, v25\n\t"
    "v_cvt_pk_bf16_f32 v33, v26, v27\n\t"
    "v_mfma_f32_16x16x32_bf16 v[16:19], a[108:111], v[192:195], v[16:19]\n\t"
    "v_exp_f32_e32 v39, v39\n\t"
    "v_mfma_f32_16x16x32_bf16 v[16:19], a[100:103], v[196:199], v[16:19]\n\t"
    "v_add_f32_e32 v23, v23, v39\n\t"
    "v_sub_f32_e32 v69, v69, v20\n\t"
    "v_mfma_f32_16x16x32_bf16 v[24:27], a[144:147], v[196:199], 0\n\t"
    "v_exp_f32_e32 v68, v68\n\t"
    "v_mfma_f32_16x16x32_bf16 v[24:27], a[152:155], v[192:195], v[24:27]\n\t"
    "v_add_f32_e32 v22, v22, v68\n\t"
    "v_sub_f32_e32 v70, v70, v20\n\t"
    "v_mfma_f32_16x16x32_bf16 v[24:27], a[160:163], v[188:191], v[24:27]\n\t"
    "v_sub_f32_e32 v71, v71, v20\n\t"
    "v_mfma_f32_16x16x32_bf16 v[24:27], a[168:171], v[184:187], v[24:27]\n\t"
    "v_cvt_pk_bf16_f32 v34, v16, v17\n\t"
    "v_mfma_f32_16x16x32_bf16 v[24:27], a[176:179], v[180:183], v[24:27]\n\t"
    "v_cvt_pk_bf16_f32 v35, v18, v19\n\t"
    "v_sub_f32_e32 v72, v72, v20\n\t"
    "s_barrier\n\t"
    "v_mfma_f32_16x16x32_bf16 v[24:27], a[184:187], v[176:179], v[24:27]\n\t"
    "v_sub_f32_e32 v73, v73, v20\n\t"
    "v_mfma_f32_16x16x32_bf16 v[40:43], v[220:223], v[12:15], 0\n\t"
    "v_exp_f32_e32 v69, v69\n\t"
    "v_mfma_f32_16x16x32_bf16 v[40:43], v[216:219], v[28:31], v[40:43]\n\t"
    "v_add_f32_e32 v22, v22, v69\n\t"
    "s_add_u32 m0, %[rbeg], 24576\n\t"
    "s_add_u32 s61, %[wb], 12582912\n\t"
    "v_mfma_f32_16x16x32_bf16 v[40:43], v[212:215], v[32:35], v[40:43]\n\t"
    "v_sub_f32_e32 v74, v74, v20\n\t"
    "v_mfma_f32_16x16x32_bf16 v[16:19], v[188:191], v[32:35], 0\n\t"
    "v_exp_f32_e32 v70, v70\n\t"
    "v_mfma_f32_16x16x32_bf16 v[16:19], v[192:195], v[28:31], v[16:19]\n\t"
    "v_add_f32_e32 v23, v23, v70\n\t"
    "v_sub_f32_e32 v75, v75, v20\n\t"
    "v_mfma_f32_16x16x32_bf16 v[16:19], v[196:199], v[12:15], v[16:19]\n\t"
    "v_exp_f32_e32 v71, v71\n\t"
    "buffer_load_dwordx4 v7, s[52:55], s61 offen offset:0 nt lds\n\t"
    "v_mfma_f32_16x16x32_bf16 v[44:47], v[172:175], v[12:15], 0\n\t"
    "v_add_f32_e32 v23, v23, v71\n\t"
    "v_cvt_pk_bf16_f32 v48, v24, v25\n\t"
    "v_mfma_f32_16x16x32_bf16 v[44:47], v[168:171], v[28:31], v[44:47]\n\t"
    "v_exp_f32_e32 v72, v72\n\t"
    "v_mfma_f32_16x16x32_bf16 v[44:47], v[164:167], v[32:35], v[44:47]\n\t"
    "v_add_f32_e32 v22, v22, v72\n\t"
    "v_cvt_pk_bf16_f32 v49, v26, v27\n\t"
    "v_mfma_f32_16x16x32_bf16 v[52:55], v[140:143], v[32:35], 0\n\t"
    "v_exp_f32_e32 v73, v73\n\t"
    "v_mfma_f32_16x16x32_bf16 v[52:55], v[144:147], v[28:31], v[52:55]\n\t"
    "v_add_f32_e32 v22, v22, v73\n\t"
    "buffer_load_dwordx4 v7, s[52:55], s61 offen offset:1024 nt lds\n\t"
    "v_mfma_f32_16x16x32_bf16 v[52:55], v[148:151], v[12:15], v[52:55]\n\t"
    "v_exp_f32_e32 v74, v74\n\t"
    "v_mfma_f32_16x16x32_bf16 v[24:27], a[188:191], v[176:179], 0\n\t"
    "v_add_f32_e32 v23, v23, v74\n\t"
    "v_mfma_f32_16x16x32_bf16 v[24:27], a[180:183], v[180:183], v[24:27]\n\t"
    "v_exp_f32_e32 v75, v75\n\t"
    "v_mfma_f32_16x16x32_bf16 v[24:27], a[172:175], v[184:187], v[24:27]\n\t"
    "v_add_f32_e32 v23, v23, v75\n\t"
    "v_add_f32_e32 v22, v22, v23\n\t"
    "v_mfma_f32_16x16x32_bf16 v[24:27], a[164:167], v[188:191], v[24:27]\n\t"
    "v_mov_b32_e32 v23, v22\n\t"
    "buffer_load_dwordx4 v7, s[52:55], s61 offen offset:2048 nt lds\n\t"
    "v_mfma_f32_16x16x32_bf16 v[24:27], a[156:159], v[192:195], v[24:27]\n\t"
    "v_mfma_f32_16x16x32_bf16 v[24:27], a[148:151], v[196:199], v[24:27]\n\t"
    "v_permlane32_swap_b32 v22, v23\n\t"
    "v_add_f32_e32 v22, v22, v23\n\t"
    "v_mfma_f32_16x16x32_bf16 v[12:15], a[192:195], v[196:199], 0\n\t"
    "v_mov_b32_e32 v23, v22\n\t"
    "v_mfma_f32_16x16x32_bf16 v[12:15], a[200:203], v[192:195], v[12:15]\n\t"
    "v_mfma_f32_16x16x32_bf16 v[12:15], a[208:211], v[188:191], v[12:15]\n\t"
    "v_permlane16_swap_b32 v22, v23\n\t"
    "v_add_f32_e32 v22, v22, v23\n\t"
    "s_add_u32 m0, %[rbeg], 27648\n\t"
    "s_add_u32 s61, %[wb], 12779520\n\t"
    "v_mfma_f32_16x16x32_bf16 v[12:15], a[216:219], v[184:187], v[12:15]\n\t"
    "v_cvt_pk_bf16_f32 v50, v24, v25\n\t"
    "v_cvt_pk_bf16_f32 v51, v26, v27\n\t"
    "v_mfma_f32_16x16x32_bf16 v[12:15], a[224:227], v[180:183], v[12:15]\n\t"
    "v_rcp_f32_e32 v22, v22\n\t"
    "v_mfma_f32_16x16x32_bf16 v[12:15], a[232:235], v[176:179], v[12:15]\n\t"
    "v_mul_f32_e32 v64, v64, v22\n\t"
    "v_mul_f32_e32 v65, v65, v22\n\t"
    "v_mfma_f32_16x16x32_bf16 v[28:31], a[236:239], v[176:179], 0\n\t"
    "v_cvt_pk_bf16_f32 v24, v64, v65\n\t"
    "v_mul_f32_e32 v66, v66, v22\n\t"
    "v_mfma_f32_16x16x32_bf16 v[28:31], a[228:231], v[180:183], v[28:31]\n\t"
    "v_mul_f32_e32 v67, v67, v22\n\t"
    "v_cvt_pk_bf16_f32 v25, v66, v67\n\t"
    "buffer_load_dwordx4 v7, s[52:55], s61 offen offset:0 nt lds\n\t"
    "v_mfma_f32_16x16x32_bf16 v[28:31], a[220:223], v[184:187], v[28:31]\n\t"
    "v_mul_f32_e32 v36, v36, v22\n\t"
    "v_mul_f32_e32 v37, v37, v22\n\t"
    "v_mfma_f32_16x16x32_bf16 v[28:31], a[212:215], v[188:191], v[28:31]\n\t"
    "v_cvt_pk_bf16_f32 v32, v12, v13\n\t"
    "v_cvt_pk_bf16_f32 v33, v14, v15\n\t"
    "v_mfma_f32_16x16x32_bf16 v[28:31], a[204:207], v[192:195], v[28:31]\n\t"
    "v_cvt_pk_bf16_f32 v26, v36, v37\n\t"
    "v_mul_f32_e32 v38, v38, v22\n\t"
    "v_mfma_f32_16x16x32_bf16 v[28:31], a[196:199], v[196:199], v[28:31]\n\t"
    "v_mul_f32_e32 v39, v39, v22\n\t"
    "v_cvt_pk_bf16_f32 v27, v38, v39\n\t"
    "v_mfma_f32_16x16x32_bf16 v[12:15], a[240:243], v[196:199], 0\n\t"
    "v_mul_f32_e32 v68, v68, v22\n\t"
    "v_mul_f32_e32 v69, v69, v22\n\t"
    "buffer_load_dwordx4 v7, s[52:55], s61 offen offset:1024 nt lds\n\t"
    "v_mfma_f32_16x16x32_bf16 v[12:15], a[248:251], v[192:195], v[12:15]\n\t"
    "v_cvt_pk_bf16_f32 v56, v68, v69\n\t"
    "v_mul_f32_e32 v70, v70, v22\n\t"
    "v_mfma_f32_16x16x32_bf16 v[12:15], v[224:227], v[188:191], v[12:15]\n\t"
    "v_mul_f32_e32 v71, v71, v22\n\t"
    "v_cvt_pk_bf16_f32 v57, v70, v71\n\t"
    "v_mfma_f32_16x16x32_bf16 v[12:15], v[232:235], v[184:187], v[12:15]\n\t"
    "v_cvt_pk_bf16_f32 v34, v28, v29\n\t"
    "v_cvt_pk_bf16_f32 v35, v30, v31\n\t"
    "v_mfma_f32_16x16x32_bf16 v[12:15], v[240:243], v[180:183], v[12:15]\n\t"
    "v_mul_f32_e32 v72, v72, v22\n\t"
    "v_mul_f32_e32 v73, v73, v22\n\t"
    "v_mfma_f32_16x16x32_bf16 v[12:15], v[248:251], v[176:179], v[12:15]\n\t"
    "v_cvt_pk_bf16_f32 v58, v72, v73\n\t"
    "v_mul_f32_e32 v74, v74, v22\n\t"
    "v_mfma_f32_16x16x32_bf16 v[28:31], v[252:255], v[176:179], 0\n\t"
    "v_mul_f32_e32 v75, v75, v22\n\t"
    "v_cvt_pk_bf16_f32 v59, v74, v75\n\t"
    "v_mfma_f32_16x16x32_bf16 v[28:31], v[244:247], v[180:183], v[28:31]\n\t"
    "buffer_load_dwordx4 v7, s[52:55], s61 offen offset:2048 nt lds\n\t"
    "v_mfma_f32_16x16x32_bf16 v[28:31], v[236:239], v[184:187], v[28:31]\n\t"
    "v_mfma_f32_16x16x32_bf16 v[28:31], v[228:231], v[188:191], v[28:31]\n\t"
    "v_cvt_pk_bf16_f32 v20, v12, v13\n\t"
    "v_cvt_pk_bf16_f32 v21, v14, v15\n\t"
    "v_mfma_f32_16x16x32_bf16 v[28:31], a[252:255], v[192:195], v[28:31]\n\t"
    "v_mfma_f32_16x16x32_bf16 v[28:31], a[244:247], v[196:199], v[28:31]\n\t"
    "s_add_u32 m0, %[rbeg], 30720\n\t"
    "s_add_u32 s61, %[wb], 12976128\n\t"
    "v_mfma_f32_16x16x32_bf16 v[12:15], a[0:3], v[172:175], 0\n\t"
    "ds_write_b128 v6, v[24:27] offset:0\n\t"
    "v_mfma_f32_16x16x32_bf16 v[12:15], a[8:11], v[168:171], v[12:15]\n\t"
    "v_mfma_f32_16x16x32_bf16 v[12:15], a[16:19], v[164:167], v[12:15]\n\t"
    "v_mfma_f32_16x16x32_bf16 v[12:15], a[24:27], v[160:163], v[12:15]\n\t"
    "v_cvt_pk_bf16_f32 v22, v28, v29\n\t"
    "v_mfma_f32_16x16x32_bf16 v[12:15], a[32:35], v[156:159], v[12:15]\n\t"
    "ds_write_b128 v6, v[56:59] offset:1024\n\t"
    "v_mfma_f32_16x16x32_bf16 v[12:15], a[40:43], v[152:155], v[12:15]\n\t"
    "v_cvt_pk_bf16_f32 v23, v30, v31\n\t"
    "buffer_load_dwordx4 v7, s[52:55], s61 offen offset:0 nt lds\n\t"
    "v_mfma_f32_16x16x32_bf16 v[40:43], v[208:211], v[48:51], v[40:43]\n\t"
    "v_mfma_f32_16x16x32_bf16 v[40:43], v[204:207], v[32:35], v[40:43]\n\t"
    "v_mfma_f32_16x16x32_bf16 v[40:43], v[200:203], v[20:23], v[40:43]\n\t"
    "s_add_i32 s64, s62, 24576\n\t"
    "v_mfma_f32_16x16x32_bf16 v[16:19], v[176:179], v[20:23], v[16:19]\n\t"
    "s_cmp_eq_u32 s64, %[rend]\n\t"
    "s_cselect_b32 s64, %[rbeg], s64\n\t"
    "buffer_load_dwordx4 v7, s[52:55], s61 offen offset:1024 nt lds\n\t"
    "v_mfma_f32_16x16x32_bf16 v[16:19], v[180:183], v[32:35], v[16:19]\n\t"
    "s_sub_i32 s65, s63, s64\n\t"
    "v_cvt_pk_bf16_f32 v24, v12, v13\n\t"
    "v_mfma_f32_16x16x32_bf16 v[16:19], v[184:187], v[48:51], v[16:19]\n\t"
    "v_add_u32_e32 v0, s65, v0\n\t"
    "v_add_u32_e32 v1, s65, v1\n\t"
    "v_mfma_f32_16x16x32_bf16 v[44:47], v[160:163], v[48:51], v[44:47]\n\t"
    "v_add_u32_e32 v2, s65, v2\n\t"
    "v_add_u32_e32 v3, s65, v3\n\t"
    "v_mfma_f32_16x16x32_bf16 v[44:47], v[156:159], v[32:35], v[44:47]\n\t"
    "v_add_u32_e32 v4, s65, v4\n\t"
    "v_add_u32_e32 v5, s65, v5\n\t"
    "v_mfma_f32_16x16x32_bf16 v[44:47], v[152:155], v[20:23], v[44:47]\n\t"
    "v_max3_f32 v28, v40, v41, v42\n\t"
    "v_cvt_pk_bf16_f32 v25, v14, v15\n\t"
    ".Lh4_%=:\n\t"
    "s_cbranch_execz .Lh5_%=\n\t"
    "v_mfma_f32_16x16x32_bf16 v[52:55], v[128:131], v[20:23], v[52:55]\n\t"
    "v_max3_f32 v29, v43, v16, v17\n\t"
    "v_max3_f32 v28, v28, v18, v19\n\t"
    "v_mfma_f32_16x16x32_bf16 v[52:55], v[132:135], v[32:35], v[52:55]\n\t"
    "buffer_load_dwordx4 v7, s[52:55], s61 offen offset:2048 nt lds\n\t"
    "v_mfma_f32_16x16x32_bf16 v[52:55], v[136:139], v[48:51], v[52:55]\n\t"
    "v_mfma_f32_16x16x32_bf16 v[12:15], a[44:47], v[152:155], 0\n\t"
    "v_max3_f32 v29, v29, v44, v45\n\t"
    "v_max3_f32 v28, v28, v46, v47\n\t"
    "v_mfma_f32_16x16x32_bf16 v[12:15], a[36:39], v[156:159], v[12:15]\n\t"
    "s_add_u32 m0, %[rbeg], 33792\n\t"
    "s_add_u32 s61, %[wb], 13172736\n\t"
    "v_mfma_f32_16x16x32_bf16 v[12:15], a[28:31], v[160:163], v[12:15]\n\t"
    "v_mfma_f32_16x16x32_bf16 v[12:15], a[20:23], v[164:167], v[12:15]\n\t"
    "v_max3_f32 v29, v29, v52, v53\n\t"
    "v_max3_f32 v28, v28, v54, v55\n\t"
    "v_mfma_f32_16x16x32_bf16 v[12:15], a[12:15], v[168:171], v[12:15]\n\t"
    "v_max_f32_e32 v28, v28, v29\n\t"
    "v_mov_b32_e32 v29, v28\n\t"
    "v_mfma_f32_16x16x32_bf16 v[12:15], a[4:7], v[172:175], v[12:15]\n\t"
    "v_mfma_f32_16x16x32_bf16 v[20:23], a[48:51], v[172:175], 0\n\t"
    "v_permlane32_swap_b32 v28, v29\n\t"
    "v_max_f32_e32 v28, v28, v29\n\t"
    "v_mfma_f32_16x16x32_bf16 v[20:23], a[56:59], v[168:171], v[20:23]\n\t"
    "v_mov_b32_e32 v29, v28\n\t"
    "buffer_load_dwordx4 v7, s[52:55], s61 offen offset:0 nt lds\n\t"
    "v_mfma_f32_16x16x32_bf16 v[20:23], a[64:67], v[164:167], v[20:23]\n\t"
    "v_mfma_f32_16x16x32_bf16 v[20:23], a[72:75], v[160:163], v[20:23]\n\t"
    "v_cvt_pk_bf16_f32 v26, v12, v13\n\t"
    "v_cvt_pk_bf16_f32 v27, v14, v15\n\t"
    "v_mfma_f32_16x16x32_bf16 v[20:23], a[80:83], v[156:159], v[20:23]\n\t"
    "v_permlane16_swap_b32 v28, v29\n\t"
    "v_max_f32_e32 v28, v28, v29\n\t"
    "v_mfma_f32_16x16x32_bf16 v[20:23], a[88:91], v[152:155], v[20:23]\n\t"
    "v_sub_f32_e32 v40, v40, v28\n\t"
    "v_sub_f32_e32 v41, v41, v28\n\t"
    "buffer_load_dwordx4 v7, s[52:55], s61 offen offset:1024 nt lds\n\t"
    "v_mfma_f32_16x16x32_bf16 v[12:15], a[92:95], v[152:155], 0\n\t"
    "v_exp_f32_e32 v40, v40\n\t"
    "v_mfma_f32_16x16x32_bf16 v[12:15], a[84:87], v[156:159], v[12:15]\n\t"
    "v_exp_f32_e32 v41, v41\n\t"
    "v_mfma_f32_16x16x32_bf16 v[12:15], a[76:79], v[160:163], v[12:15]\n\t"
    "v_add_f32_e32 v30, v40, v41\n\t"
    "v_sub_f32_e32 v42, v42, v28\n\t"
    "v_mfma_f32_16x16x32_bf16 v[12:15], a[68:71], v[164:167], v[12:15]\n\t"
    "v_cvt_pk_bf16_f32 v32, v20, v21\n\t"
    "v_cvt_pk_bf16_f32 v33, v22, v23\n\t"
    "v_mfma_f32_16x16x32_bf16 v[12:15], a[60:63], v[168:171], v[12:15]\n\t"
    "v_exp_f32_e32 v42, v42\n\t"
    "v_mfma_f32_16x16x32_bf16 v[12:15], a[52:55], v[172:175], v[12:15]\n\t"
    "v_sub_f32_e32 v43, v43, v28\n\t"
    "v_sub_f32_e32 v16, v16, v28\n\t"
    "buffer_load_dwordx4 v7, s[52:55], s61 offen offset:2048 nt lds\n\t"
    "v_mfma_f32_16x16x32_bf16 v[20:23], a[96:99], v[172:175], 0\n\t"
    "v_exp_f32_e32 v43, v43\n\t"
    "v_mfma_f32_16x16x32_bf16 v[20:23], a[104:107], v[168:171], v[20:23]\n\t"
    "v_add_f32_e32 v31, v42, v43\n\t"
    "v_sub_f32_e32 v17, v17, v28\n\t"
    "v_mfma_f32_16x16x32_bf16 v[20:23], a[112:115], v[164:167], v[20:23]\n\t"
    "v_exp_f32_e32 v16, v16\n\t"
    "v_mfma_f32_16x16x32_bf16 v[20:23], a[120:123], v[160:163], v[20:23]\n\t"
    "v_cvt_pk_bf16_f32 v34, v12, v13\n\t"
    "v_cvt_pk_bf16_f32 v35, v14, v15\n\t"
    "v_mfma_f32_16x16x32_bf16 v[20:23], a[128:131], v[156:159], v[20:23]\n\t"
    "v_add_f32_e32 v30, v30, v16\n\t"
    "v_sub_f32_e32 v18, v18, v28\n\t"
    "s_add_u32 m0, %[rbeg], 36864\n\t"
    "s_add_u32 s61, %[wb], 13369344\n\t"
    "v_mfma_f32_16x16x32_bf16 v[20:23], a[136:139], v[152:155], v[20:23]\n\t"
    "v_exp_f32_e32 v17, v17\n\t"
    "v_mfma_f32_16x16x32_bf16 v[12:15], a[140:143], v[152:155], 0\n\t"
    "v_add_f32_e32 v30, v30, v17\n\t"
    "v_sub_f32_e32 v19, v19, v28\n\t"
    "v_mfma_f32_16x16x32_bf16 v[12:15], a[132:135], v[156:159], v[12:15]\n\t"
    "v_exp_f32_e32 v18, v18\n\t"
    "v_mfma_f32_16x16x32_bf16 v[12:15], a[124:127], v[160:163], v[12:15]\n\t"
    "v_add_f32_e32 v31, v31, v18\n\t"
    "v_sub_f32_e32 v44, v44, v28\n\t"
    "v_mfma_f32_16x16x32_bf16 v[12:15], a[116:119], v[164:167], v[12:15]\n\t"
    "v_cvt_pk_bf16_f32 v36, v20, v21\n\t"
    "v_cvt_pk_bf16_f32 v37, v22, v23\n\t"
    "buffer_load_dwordx4 v7, s[52:55], s61 offen offset:0 nt lds\n\t"
    "v_mfma_f32_16x16x32_bf16 v[12:15], a[108:111], v[168:171], v[12:15]\n\t"
    "v_exp_f32_e32 v19, v19\n\t"
    "v_mfma_f32_16x16x32_bf16 v[12:15], a[100:103], v[172:175], v[12:15]\n\t"
    "v_add_f32_e32 v31, v31, v19\n\t"
    "v_sub_f32_e32 v45, v45, v28\n\t"
    "v_mfma_f32_16x16x32_bf16 v[20:23], a[144:147], v[172:175], 0\n\t"
    "v_exp_f32_e32 v44, v44\n\t"
    "v_mfma_f32_16x16x32_bf16 v[20:23], a[152:155], v[168:171], v[20:23]\n\t"
    "v_add_f32_e32 v30, v30, v44\n\t"
    "v_sub_f32_e32 v46, v46, v28\n\t"
    "v_mfma_f32_16x16x32_bf16 v[20:23], a[160:163], v[164:167], v[20:23]\n\t"
    "v_sub_f32_e32 v47, v47, v28\n\t"
    "buffer_load_dwordx4 v7, s[52:55], s61 offen offset:1024 nt lds\n\t"
    "v_mfma_f32_16x16x32_bf16 v[20:23], a[168:171], v[160:163], v[20:23]\n\t"
    "v_cvt_pk_bf16_f32 v38, v12, v13\n\t"
    "v_mfma_f32_16x16x32_bf16 v[20:23], a[176:179], v[156:159], v[20:23]\n\t"
    "v_cvt_pk_bf16_f32 v39, v14, v15\n\t"
    "v_sub_f32_e32 v52, v52, v28\n\t"
    "v_mfma_f32_16x16x32_bf16 v[20:23], a[184:187], v[152:155], v[20:23]\n\t"
    "v_sub_f32_e32 v53, v53, v28\n\t"
    "v_mfma_f32_16x16x32_bf16 v[56:59], v[220:223], v[24:27], 0\n\t"
    "v_exp_f32_e32 v45, v45\n\t"
    "v_mfma_f32_16x16x32_bf16 v[56:59], v[216:219], v[32:35], v[56:59]\n\t"
    "v_add_f32_e32 v30, v30, v45\n\t"
    "buffer_load_dwordx4 v7, s[52:55], s61 offen offset:2048 nt lds\n\t"
    "v_mfma_f32_16x16x32_bf16 v[56:59], v[212:215], v[36:39], v[56:59]\n\t"
    "v_sub_f32_e32 v54, v54, v28\n\t"
    "v_mfma_f32_16x16x32_bf16 v[60:63], v[188:191], v[36:39], 0\n\t"
    "v_exp_f32_e32 v46, v46\n\t"
    "v_mfma_f32_16x16x32_bf16 v[60:63], v[192:195], v[32:35], v[60:63]\n\t"
    "v_add_f32_e32 v31, v31, v46\n\t"
    "v_sub_f32_e32 v55, v55, v28\n\t"
    "v_mfma_f32_16x16x32_bf16 v[60:63], v[196:199], v[24:27], v[60:63]\n\t"
    "v_exp_f32_e32 v47, v47\n\t"
    "v_mfma_f32_16x16x32_bf16 v[64:67], v[172:175], v[24:27], 0\n\t"
    "v_add_f32_e32 v31, v31, v47\n\t"
    "v_cvt_pk_bf16_f32 v12, v20, v21\n\t"
    "s_add_u32 m0, %[rbeg], 39936\n\t"
    "s_add_u32 s61, %[wb], 13565952\n\t"
    "v_mfma_f32_16x16x32_bf16 v[64:67], v[168:171], v[32:35], v[64:67]\n\t"
    "v_exp_f32_e32 v52, v52\n\t"
    "v_mfma_f32_16x16x32_bf16 v[64:67], v[164:167], v[36:39], v[64:67]\n\t"
    "v_add_f32_e32 v30, v30, v52\n\t"
    "v_cvt_pk_bf16_f32 v13, v22, v23\n\t"
    "v_mfma_f32_16x16x32_bf16 v[68:71], v[140:143], v[36:39], 0\n\t"
    "v_exp_f32_e32 v53, v53\n\t"
    "v_mfma_f32_16x16x32_bf16 v[68:71], v[144:147], v[32:35], v[68:71]\n\t"
    "v_add_f32_e32 v30, v30, v53\n\t"
    "v_mfma_f32_16x16x32_bf16 v[68:71], v[148:151], v[24:27], v[68:71]\n\t"
    "v_exp_f32_e32 v54, v54\n\t"
    "v_mfma_f32_16x16x32_bf16 v[20:23], a[188:191], v[152:155], 0\n\t"
    "v_add_f32_e32 v31, v31, v54\n\t"
    "buffer_load_dwordx4 v7, s[52:55], s61 offen offset:0 nt lds\n\t"
    "v_mfma_f32_16x16x32_bf16 v[20:23], a[180:183], v[156:159], v[20:23]\n\t"
    "v_exp_f32_e32 v55, v55\n\t"
    "v_mfma_f32_16x16x32_bf16 v[20:23], a[172:175], v[160:163], v[20:23]\n\t"
    "v_add_f32_e32 v31, v31, v55\n\t"
    "v_add_f32_e32 v30, v30, v31\n\t"
    "v_mfma_f32_16x16x32_bf16 v[20:23], a[164:167], v[164:167], v[20:23]\n\t"
    "v_mov_b32_e32 v31, v30\n\t"
    "v_mfma_f32_16x16x32_bf16 v[20:23], a[156:159], v[168:171], v[20:23]\n\t"
    "v_mfma_f32_16x16x32_bf16 v[20:23], a[148:151], v[172:175], v[20:23]\n\t"
    "v_permlane32_swap_b32 v30, v31\n\t"
    "v_add_f32_e32 v30, v30, v31\n\t"
    "v_mfma_f32_16x16x32_bf16 v[24:27], a[192:195], v[172:175], 0\n\t"
    "v_mov_b32_e32 v31, v30\n\t"
    "buffer_load_dwordx4 v7, s[52:55], s61 offen offset:1024 nt lds\n\t"
    "v_mfma_f32_16x16x32_bf16 v[24:27], a[200:203], v[168:171], v[24:27]\n\t"
    "v_mfma_f32_16x16x32_bf16 v[24:27], a[208:211], v[164:167], v[24:27]\n\t"
    "v_permlane16_swap_b32 v30, v31\n\t"
    "v_add_f32_e32 v30, v30, v31\n\t"
    "v_mfma_f32_16x16x32_bf16 v[24:27], a[216:219], v[160:163], v[24:27]\n\t"
    "v_cvt_pk_bf16_f32 v14, v20, v21\n\t"
    "v_cvt_pk_bf16_f32 v15, v22, v23\n\t"
    "v_mfma_f32_16x16x32_bf16 v[24:27], a[224:227], v[156:159], v[24:27]\n\t"
    "v_rcp_f32_e32 v30, v30\n\t"
    "buffer_load_dwordx4 v7, s[52:55], s61 offen offset:2048 nt lds\n\t"
    "v_mfma_f32_16x16x32_bf16 v[24:27], a[232:235], v[152:155], v[24:27]\n\t"
    "v_mul_f32_e32 v40, v40, v30\n\t"
    "v_mul_f32_e32 v41, v41, v30\n\t"
    "v_mfma_f32_16x16x32_bf16 v[32:35], a[236:239], v[152:155], 0\n\t"
    "v_cvt_pk_bf16_f32 v20, v40, v41\n\t"
    "v_mul_f32_e32 v42, v42, v30\n\t"
    "v_mfma_f32_16x16x32_bf16 v[32:35], a[228:231], v[156:159], v[32:35]\n\t"
    "v_mul_f32_e32 v43, v43, v30\n\t"
    "v_cvt_pk_bf16_f32 v21, v42, v43\n\t"
    "v_mfma_f32_16x16x32_bf16 v[32:35], a[220:223], v[160:163], v[32:35]\n\t"
    "v_mul_f32_e32 v16, v16, v30\n\t"
    "v_mul_f32_e32 v17, v17, v30\n\t"
    "v_mfma_f32_16x16x32_bf16 v[32:35], a[212:215], v[164:167], v[32:35]\n\t"
    "v_cvt_pk_bf16_f32 v36, v24, v25\n\t"
    "v_cvt_pk_bf16_f32 v37, v26, v27\n\t"
    "s_add_u32 m0, %[rbeg], 43008\n\t"
    "s_add_u32 s61, %[wb], 13762560\n\t"
    "v_mfma_f32_16x16x32_bf16 v[32:35], a[204:207], v[168:171], v[32:35]\n\t"
    "v_cvt_pk_bf16_f32 v22, v16, v17\n\t"
    "v_mul_f32_e32 v18, v18, v30\n\t"
    "v_mfma_f32_16x16x32_bf16 v[32:35], a[196:199], v[172:175], v[32:35]\n\t"
    "v_mul_f32_e32 v19, v19, v30\n\t"
    "v_cvt_pk_bf16_f32 v23, v18, v19\n\t"
    "v_mfma_f32_16x16x32_bf16 v[24:27], a[240:243], v[172:175], 0\n\t"
    "v_mul_f32_e32 v44, v44, v30\n\t"
    "v_mul_f32_e32 v45, v45, v30\n\t"
    "v_mfma_f32_16x16x32_bf16 v[24:27], a[248:251], v[168:171], v[24:27]\n\t"
    "v_cvt_pk_bf16_f32 v40, v44, v45\n\t"
    "v_mul_f32_e32 v46, v46, v30\n\t"
    "v_mfma_f32_16x16x32_bf16 v[24:27], v[224:227], v[164:167], v[24:27]\n\t"
    "v_mul_f32_e32 v47, v47, v30\n\t"
    "v_cvt_pk_bf16_f32 v41, v46, v47\n\t"
    "buffer_load_dwordx4 v7, s[52:55], s61 offen offset:0 nt lds\n\t"
    "v_mfma_f32_16x16x32_bf16 v[24:27], v[232:235], v[160:163], v[24:27]\n\t"
    "v_cvt_pk_bf16_f32 v38, v32, v33\n\t"
    "v_cvt_pk_bf16_f32 v39, v34, v35\n\t"
    "v_mfma_f32_16x16x32_bf16 v[24:27], v[240:243], v[156:159], v[24:27]\n\t"
    "v_mul_f32_e32 v52, v52, v30\n\t"
    "v_mul_f32_e32 v53, v53, v30\n\t"
    "v_mfma_f32_16x16x32_bf16 v[24:27], v[248:251], v[152:155], v[24:27]\n\t"
    "v_cvt_pk_bf16_f32 v42, v52, v53\n\t"
    "v_mul_f32_e32 v54, v54, v30\n\t"
    "v_mfma_f32_16x16x32_bf16 v[16:19], v[252:255], v[152:155], 0\n\t"
    "v_mul_f32_e32 v55, v55, v30\n\t"
    "v_cvt_pk_bf16_f32 v43, v54, v55\n\t"
    "v_mfma_f32_16x16x32_bf16 v[16:19], v[244:247], v[156:159], v[16:19]\n\t"
    "buffer_load_dwordx4 v7, s[52:55], s61 offen offset:1024 nt lds\n\t"
    "v_mfma_f32_16x16x32_bf16 v[16:19], v[236:239], v[160:163], v[16:19]\n\t"
    "v_mfma_f32_16x16x32_bf16 v[16:19], v[228:231], v[164:167], v[16:19]\n\t"
    "v_cvt_pk_bf16_f32 v28, v24, v25\n\t"
    "v_cvt_pk_bf16_f32 v29, v26, v27\n\t"
    "v_mfma_f32_16x16x32_bf16 v[16:19], a[252:255], v[168:171], v[16:19]\n\t"
    "v_mfma_f32_16x16x32_bf16 v[16:19], a[244:247], v[172:175], v[16:19]\n\t"
    "v_mfma_f32_16x16x32_bf16 v[24:27], a[0:3], v[148:151], 0\n\t"
    "ds_write_b128 v6, v[20:23] offset:2048\n\t"
    "v_mfma_f32_16x16x32_bf16 v[24:27], a[8:11], v[144:147], v[24:27]\n\t"
    "buffer_load_dwordx4 v7, s[52:55], s61 offen offset:2048 nt lds\n\t"
    "v_mfma_f32_16x16x32_bf16 v[24:27], a[16:19], v[140:143], v[24:27]\n\t"
    "v_mfma_f32_16x16x32_bf16 v[24:27], a[24:27], v[136:139], v[24:27]\n\t"
    "v_cvt_pk_bf16_f32 v30, v16, v17\n\t"
    "v_mfma_f32_16x16x32_bf16 v[24:27], a[32:35], v[132:135], v[24:27]\n\t"
    "ds_write_b128 v6, v[40:43] offset:3072\n\t"
    "v_mfma_f32_16x16x32_bf16 v[24:27], a[40:43], v[128:131], v[24:27]\n\t"
    "v_cvt_pk_bf16_f32 v31, v18, v19\n\t"
    "v_mfma_f32_16x16x32_bf16 v[56:59], v[208:211], v[12:15], v[56:59]\n\t"
    "s_add_u32 m0, %[rbeg], 46080\n\t"
    "s_add_u32 s61, %[wb], 13959168\n\t"
    "v_mfma_f32_16x16x32_bf16 v[56:59], v[204:207], v[36:39], v[56:59]\n\t"
    "v_mfma_f32_16x16x32_bf16 v[56:59], v[200:203], v[28:31], v[56:59]\n\t"
    "v_mfma_f32_16x16x32_bf16 v[60:63], v[176:179], v[28:31], v[60:63]\n\t"
    "v_cvt_pk_bf16_f32 v40, v24, v25\n\t"
    "v_cvt_pk_bf16_f32 v41, v26, v27\n\t"
    "v_mfma_f32_16x16x32_bf16 v[60:63], v[180:183], v[36:39], v[60:63]\n\t"
    "v_mfma_f32_16x16x32_bf16 v[60:63], v[184:187], v[12:15], v[60:63]\n\t"
    "buffer_load_dwordx4 v7, s[52:55], s61 offen offset:0 nt lds\n\t"
    "v_mfma_f32_16x16x32_bf16 v[64:67], v[160:163], v[12:15], v[64:67]\n\t"
    "v_max3_f32 v72, v56, v57, v58\n\t"
    "v_mfma_f32_16x16x32_bf16 v[64:67], v[156:159], v[36:39], v[64:67]\n\t"
    "v_mfma_f32_16x16x32_bf16 v[64:67], v[152:155], v[28:31], v[64:67]\n\t"
    "v_mfma_f32_16x16x32_bf16 v[68:71], v[128:131], v[28:31], v[68:71]\n\t"
    "v_max3_f32 v73, v59, v60, v61\n\t"
    "v_max3_f32 v72, v72, v62, v63\n\t"
    "v_mfma_f32_16x16x32_bf16 v[68:71], v[132:135], v[36:39], v[68:71]\n\t"
    "buffer_load_dwordx4 v7, s[52:55], s61 offen offset:1024 nt lds\n\t"
    "v_mfma_f32_16x16x32_bf16 v[68:71], v[136:139], v[12:15], v[68:71]\n\t"
    "v_mfma_f32_16x16x32_bf16 v[16:19], a[44:47], v[128:131], 0\n\t"
    "v_max3_f32 v73, v73, v64, v65\n\t"
    "v_max3_f32 v72, v72, v66, v67\n\t"
    "v_mfma_f32_16x16x32_bf16 v[16:19], a[36:39], v[132:135], v[16:19]\n\t"
    "v_mfma_f32_16x16x32_bf16 v[16:19], a[28:31], v[136:139], v[16:19]\n\t"
    "v_mfma_f32_16x16x32_bf16 v[16:19], a[20:23], v[140:143], v[16:19]\n\t"
    "v_max3_f32 v73, v73, v68, v69\n\t"
    "v_max3_f32 v72, v72, v70, v71\n\t"
    "v_mfma_f32_16x16x32_bf16 v[16:19], a[12:15], v[144:147], v[16:19]\n\t"
    "v_max_f32_e32 v72, v72, v73\n\t"
    "v_mov_b32_e32 v73, v72\n\t"
    "v_mfma_f32_16x16x32_bf16 v[16:19], a[4:7], v[148:151], v[16:19]\n\t"
    "buffer_load_dwordx4 v7, s[52:55], s61 offen offset:2048 nt lds\n\t"
    "v_mfma_f32_16x16x32_bf16 v[12:15], a[48:51], v[148:151], 0\n\t"
    "v_permlane32_swap_b32 v72, v73\n\t"
    "v_max_f32_e32 v72, v72, v73\n\t"
    "v_mfma_f32_16x16x32_bf16 v[12:15], a[56:59], v[144:147], v[12:15]\n\t"
    "v_mov_b32_e32 v73, v72\n\t"
    "v_mfma_f32_16x16x32_bf16 v[12:15], a[64:67], v[140:143], v[12:15]\n\t"
    "s_add_u32 m0, %[rbeg], 49152\n\t"
    "s_add_u32 s61, %[wb], 25165824\n\t"
    "v_mfma_f32_16x16x32_bf16 v[12:15], a[72:75], v[136:139], v[12:15]\n\t"
    "v_cvt_pk_bf16_f32 v42, v16, v17\n\t"
    "v_cvt_pk_bf16_f32 v43, v18, v19\n\t"
    "v_mfma_f32_16x16x32_bf16 v[12:15], a[80:83], v[132:135], v[12:15]\n\t"
    "v_permlane16_swap_b32 v72, v73\n\t"
    "v_max_f32_e32 v72, v72, v73\n\t"
    "v_mfma_f32_16x16x32_bf16 v[12:15], a[88:91], v[128:131], v[12:15]\n\t"
    "v_sub_f32_e32 v56, v56, v72\n\t"
    "v_sub_f32_e32 v57, v57, v72\n\t"
    "v_mfma_f32_16x16x32_bf16 v[16:19], a[92:95], v[128:131], 0\n\t"
    "v_exp_f32_e32 v56, v56\n\t"
    "v_mfma_f32_16x16x32_bf16 v[16:19], a[84:87], v[132:135], v[16:19]\n\t"
    "v_exp_f32_e32 v57, v57\n\t"
    "buffer_load_dwordx4 v7, s[52:55], s61 offen offset:0 nt lds\n\t"
    "v_mfma_f32_16x16x32_bf16 v[16:19], a[76:79], v[136:139], v[16:19]\n\t"
    "v_add_f32_e32 v74, v56, v57\n\t"
    "v_sub_f32_e32 v58, v58, v72\n\t"
    "v_mfma_f32_16x16x32_bf16 v[16:19], a[68:71], v[140:143], v[16:19]\n\t"
    "v_cvt_pk_bf16_f32 v44, v12, v13\n\t"
    "v_cvt_pk_bf16_f32 v45, v14, v15\n\t"
    "v_mfma_f32_16x16x32_bf16 v[16:19], a[60:63], v[144:147], v[16:19]\n\t"
    "v_exp_f32_e32 v58, v58\n\t"
    "v_mfma_f32_16x16x32_bf16 v[16:19], a[52:55], v[148:151], v[16:19]\n\t"
    "v_sub_f32_e32 v59, v59, v72\n\t"
    "v_sub_f32_e32 v60, v60, v72\n\t"
    "v_mfma_f32_16x16x32_bf16 v[12:15], a[96:99], v[148:151], 0\n\t"
    "v_exp_f32_e32 v59, v59\n\t"
    "buffer_load_dwordx4 v7, s[52:55], s61 offen offset:1024 nt lds\n\t"
    "v_mfma_f32_16x16x32_bf16 v[12:15], a[104:107], v[144:147], v[12:15]\n\t"
    "v_add_f32_e32 v75, v58, v59\n\t"
    "v_sub_f32_e32 v61, v61, v72\n\t"
    "v_mfma_f32_16x16x32_bf16 v[12:15], a[112:115], v[140:143], v[12:15]\n\t"
    "v_exp_f32_e32 v60, v60\n\t"
    "v_mfma_f32_16x16x32_bf16 v[12:15], a[120:123], v[136:139], v[12:15]\n\t"
    "v_cvt_pk_bf16_f32 v46, v16, v17\n\t"
    "v_cvt_pk_bf16_f32 v47, v18, v19\n\t"
    "v_mfma_f32_16x16x32_bf16 v[12:15], a[128:131], v[132:135], v[12:15]\n\t"
    "v_add_f32_e32 v74, v74, v60\n\t"
    "v_sub_f32_e32 v62, v62, v72\n\t"
    "v_mfma_f32_16x16x32_bf16 v[12:15], a[136:139], v[128:131], v[12:15]\n\t"
    "v_exp_f32_e32 v61, v61\n\t"
    "buffer_load_dwordx4 v7, s[52:55], s61 offen offset:2048 nt lds\n\t"
    "v_mfma_f32_16x16x32_bf16 v[16:19], a[140:143], v[128:131], 0\n\t"
    "v_add_f32_e32 v74, v74, v61\n\t"
    "v_sub_f32_e32 v63, v63, v72\n\t"
    "v_mfma_f32_16x16x32_bf16 v[16:19], a[132:135], v[132:135], v[16:19]\n\t"
    "v_exp_f32_e32 v62, v62\n\t"
    "v_mfma_f32_16x16x32_bf16 v[16:19], a[124:127], v[136:139], v[16:19]\n\t"
    "v_add_f32_e32 v75, v75, v62\n\t"
    "v_sub_f32_e32 v64, v64, v72\n\t"
    "v_mfma_f32_16x16x32_bf16 v[16:19], a[116:119], v[140:143], v[16:19]\n\t"
    "v_cvt_pk_bf16_f32 v32, v12, v13\n\t"
    "v_cvt_pk_bf16_f32 v33, v14, v15\n\t"
    "v_mfma_f32_16x16x32_bf16 v[16:19], a[108:111], v[144:147], v[16:19]\n\t"
    "v_exp_f32_e32 v63, v63\n\t"
    "v_mfma_f32_16x16x32_bf16 v[16:19], a[100:103], v[148:151], v[16:19]\n\t"
    "v_add_f32_e32 v75, v75, v63\n\t"
    "v_sub_f32_e32 v65, v65, v72\n\t"
    "v_mfma_f32_16x16x32_bf16 v[48:51], a[144:147], v[148:151], 0\n\t"
    "v_exp_f32_e32 v64, v64\n\t"
    "v_mfma_f32_16x16x32_bf16 v[48:51], a[152:155], v[144:147], v[48:51]\n\t"
    "v_add_f32_e32 v74, v74, v64\n\t"
    "v_sub_f32_e32 v66, v66, v72\n\t"
    "s_mov_b32 s60, 8\n\t"
    "s_add_u32 s61, %[wb], 25362432\n\t"
    ".Lmid_%=:\n\t"
    "v_mfma_f32_16x16x32_bf16 v[48:51], a[160:163], v[140:143], v[48:51]\n\t"
    "s_add_u32 m0, s62, 3072\n\t"
    "v_sub_f32_e32 v67, v67, v72\n\t"
    "v_mfma_f32_16x16x32_bf16 v[48:51], a[168:171], v[136:139], v[48:51]\n\t"
    "buffer_load_dwordx4 v7, s[52:55], s61 offen offset:0 nt lds\n\t"
    "v_cvt_pk_bf16_f32 v34, v16, v17\n\t"
    "v_mfma_f32_16x16x32_bf16 v[48:51], a[176:179], v[132:135], v[48:51]\n\t"
    "v_cvt_pk_bf16_f32 v35, v18, v19\n\t"
    "v_sub_f32_e32 v68, v68, v72\n\t"
    "v_mfma_f32_16x16x32_bf16 v[48:51], a[184:187], v[128:131], v[48:51]\n\t"
    "buffer_load_dwordx4 v7, s[52:55], s61 offen offset:1024 nt lds\n\t"
    "v_sub_f32_e32 v69, v69, v72\n\t"
    "v_mfma_f32_16x16x32_bf16 v[12:15], v[220:223], v[40:43], 0\n\t"
    "v_exp_f32_e32 v65, v65\n\t"
    "v_mfma_f32_16x16x32_bf16 v[12:15], v[216:219], v[44:47], v[12:15]\n\t"
    "buffer_load_dwordx4 v7, s[52:55], s61 offen offset:2048 nt lds\n\t"
    "v_add_f32_e32 v74, v74, v65\n\t"
    "v_mfma_f32_16x16x32_bf16 v[12:15], v[212:215], v[32:35], v[12:15]\n\t"
    "s_waitcnt vmcnt(24)\n\t"
    "s_add_u32 s61, s61, 196608\n\t"
    "v_mfma_f32_16x16x32_bf16 v[16:19], v[188:191], v[32:35], 0\n\t"
    "v_exp_f32_e32 v66, v66\n\t"
    "v_mfma_f32_16x16x32_bf16 v[16:19], v[192:195], v[44:47], v[16:19]\n\t"
    "ds_read_b128 v[220:223], v0 offset:0\n\t"
    "v_add_f32_e32 v75, v75, v66\n\t"
    "v_mfma_f32_16x16x32_bf16 v[16:19], v[196:199], v[40:43], v[16:19]\n\t"
    "v_exp_f32_e32 v67, v67\n\t"
    "v_mfma_f32_16x16x32_bf16 v[20:23], v[172:175], v[40:43], 0\n\t"
    "ds_read_b128 v[216:219], v1 offset:0\n\t"
    "v_add_f32_e32 v75, v75, v67\n\t"
    "v_mfma_f32_16x16x32_bf16 v[20:23], v[168:171], v[44:47], v[20:23]\n\t"
    "v_exp_f32_e32 v68, v68\n\t"
    "v_mfma_f32_16x16x32_bf16 v[20:23], v[164:167], v[32:35], v[20:23]\n\t"
    "ds_read_b128 v[212:215], v2 offset:0\n\t"
    "v_add_f32_e32 v74, v74, v68\n\t"
    "v_mfma_f32_16x16x32_bf16 v[24:27], v[140:143], v[32:35], 0\n\t"
    "v_exp_f32_e32 v69, v69\n\t"
    "v_mfma_f32_16x16x32_bf16 v[24:27], v[144:147], v[44:47], v[24:27]\n\t"
    "ds_read_b128 v[28:31], v3 offset:0\n\t"
    "v_add_f32_e32 v74, v74, v69\n\t"
    "v_mfma_f32_16x16x32_bf16 v[24:27], v[148:151], v[40:43], v[24:27]\n\t"
    "v_sub_f32_e32 v70, v70, v72\n\t"
    "v_sub_f32_e32 v71, v71, v72\n\t"
    "v_mfma_f32_16x16x32_bf16 v[52:55], a[188:191], v[128:131], 0\n\t"
    "ds_read_b128 v[32:35], v4 offset:0\n\t"
    "v_cvt_pk_bf16_f32 v36, v48, v49\n\t"
    "v_mfma_f32_16x16x32_bf16 v[52:55], a[180:183], v[132:135], v[52:55]\n\t"
    "v_exp_f32_e32 v70, v70\n\t"
    "v_mfma_f32_16x16x32_bf16 v[52:55], a[172:175], v[136:139], v[52:55]\n\t"
    "ds_read_b128 v[40:43], v5 offset:0\n\t"
    "v_add_f32_e32 v75, v75, v70\n\t"
    "v_mfma_f32_16x16x32_bf16 v[52:55], a[164:167], v[140:143], v[52:55]\n\t"
    "v_exp_f32_e32 v71, v71\n\t"
    "v_mfma_f32_16x16x32_bf16 v[52:55], a[156:159], v[144:147], v[52:55]\n\t"
    "v_add_f32_e32 v75, v75, v71\n\t"
    "v_add_f32_e32 v74, v74, v75\n\t"
    "v_mfma_f32_16x16x32_bf16 v[52:55], a[148:151], v[148:151], v[52:55]\n\t"
    "v_mov_b32_e32 v75, v74\n\t"
    "v_cvt_pk_bf16_f32 v37, v50, v51\n\t"
    "v_mfma_f32_16x16x32_bf16 v[76:79], a[192:195], v[148:151], 0\n\t"
    "v_mfma_f32_16x16x32_bf16 v[76:79], a[200:203], v[144:147], v[76:79]\n\t"
    "v_permlane32_swap_b32 v74, v75\n\t"
    "v_add_f32_e32 v74, v74, v75\n\t"
    "v_mfma_f32_16x16x32_bf16 v[76:79], a[208:211], v[140:143], v[76:79]\n\t"
    "v_mov_b32_e32 v75, v74\n\t"
    "v_mfma_f32_16x16x32_bf16 v[76:79], a[216:219], v[136:139], v[76:79]\n\t"
    "v_cvt_pk_bf16_f32 v38, v52, v53\n\t"
    "v_cvt_pk_bf16_f32 v39, v54, v55\n\t"
    "v_mfma_f32_16x16x32_bf16 v[76:79], a[224:227], v[132:135], v[76:79]\n\t"
    "v_permlane16_swap_b32 v74, v75\n\t"
    "v_add_f32_e32 v74, v74, v75\n\t"
    "v_mfma_f32_16x16x32_bf16 v[76:79], a[232:235], v[128:131], v[76:79]\n\t"
    "v_rcp_f32_e32 v74, v74\n\t"
    "v_mfma_f32_16x16x32_bf16 v[80:83], a[236:239], v[128:131], 0\n\t"
    "v_mul_f32_e32 v56, v56, v74\n\t"
    "v_mul_f32_e32 v57, v57, v74\n\t"
    "v_mfma_f32_16x16x32_bf16 v[80:83], a[228:231], v[132:135], v[80:83]\n\t"
    "v_cvt_pk_bf16_f32 v44, v56, v57\n\t"
    "v_mul_f32_e32 v58, v58, v74\n\t"
    "v_mfma_f32_16x16x32_bf16 v[80:83], a[220:223], v[136:139], v[80:83]\n\t"
    "v_mul_f32_e32 v59, v59, v74\n\t"
    "v_cvt_pk_bf16_f32 v45, v58, v59\n\t"
    "v_mfma_f32_16x16x32_bf16 v[80:83], a[212:215], v[140:143], v[80:83]\n\t"
    "v_cvt_pk_bf16_f32 v48, v76, v77\n\t"
    "v_cvt_pk_bf16_f32 v49, v78, v79\n\t"
    "v_mfma_f32_16x16x32_bf16 v[80:83], a[204:207], v[144:147], v[80:83]\n\t"
    "v_mul_f32_e32 v60, v60, v74\n\t"
    "v_mul_f32_e32 v61, v61, v74\n\t"
    "v_mfma_f32_16x16x32_bf16 v[80:83], a[196:199], v[148:151], v[80:83]\n\t"
    "v_cvt_pk_bf16_f32 v46, v60, v61\n\t"
    "v_mul_f32_e32 v62, v62, v74\n\t"
    "v_mfma_f32_16x16x32_bf16 v[76:79], a[240:243], v[148:151], 0\n\t"
    "v_mul_f32_e32 v63, v63, v74\n\t"
    "v_cvt_pk_bf16_f32 v47, v62, v63\n\t"
    "v_mfma_f32_16x16x32_bf16 v[76:79], a[248:251], v[144:147], v[76:79]\n\t"
    "v_mul_f32_e32 v64, v64, v74\n\t"
    "v_mul_f32_e32 v65, v65, v74\n\t"
    "v_mfma_f32_16x16x32_bf16 v[76:79], v[224:227], v[140:143], v[76:79]\n\t"
    "v_cvt_pk_bf16_f32 v52, v64, v65\n\t"
    "v_mul_f32_e32 v66, v66, v74\n\t"
    "v_mfma_f32_16x16x32_bf16 v[76:79], v[232:235], v[136:139], v[76:79]\n\t"
    "v_cvt_pk_bf16_f32 v50, v80, v81\n\t"
    "v_cvt_pk_bf16_f32 v51, v82, v83\n\t"
    "v_mfma_f32_16x16x32_bf16 v[76:79], v[240:243], v[132:135], v[76:79]\n\t"
    "v_mul_f32_e32 v67, v67, v74\n\t"
    "v_cvt_pk_bf16_f32 v53, v66, v67\n\t"
    "v_mfma_f32_16x16x32_bf16 v[76:79], v[248:251], v[128:131], v[76:79]\n\t"
    "v_mul_f32_e32 v68, v68, v74\n\t"
    "v_mul_f32_e32 v69, v69, v74\n\t"
    "v_mfma_f32_16x16x32_bf16 v[56:59], v[252:255], v[128:131], 0\n\t"
    "v_cvt_pk_bf16_f32 v54, v68, v69\n\t"
    "v_mul_f32_e32 v70, v70, v74\n\t"
    "v_mfma_f32_16x16x32_bf16 v[56:59], v[244:247], v[132:135], v[56:59]\n\t"
    "v_mul_f32_e32 v71, v71, v74\n\t"
    "v_cvt_pk_bf16_f32 v55, v70, v71\n\t"
    "v_mfma_f32_16x16x32_bf16 v[56:59], v[236:239], v[136:139], v[56:59]\n\t"
    "s_sub_u32 s60, s60, 1\n\t"
    "s_cmp_lg_u32 s60, 0\n\t"
    "v_mfma_f32_16x16x32_bf16 v[56:59], v[228:231], v[140:143], v[56:59]\n\t"
    "v_cvt_pk_bf16_f32 v60, v76, v77\n\t"
    "v_cvt_pk_bf16_f32 v61, v78, v79\n\t"
    "v_mfma_f32_16x16x32_bf16 v[56:59], a[252:255], v[144:147], v[56:59]\n\t"
    "s_mov_b32 s63, s62\n\t"
    "s_mov_b32 s62, s64\n\t"
    "v_mfma_f32_16x16x32_bf16 v[56:59], a[244:247], v[148:151], v[56:59]\n\t"
    "s_cbranch_scc0 .Ltail_%=\n\t"
    "s_waitcnt lgkmcnt(0)\n\t"
    "v_mfma_f32_16x16x32_bf16 v[64:67], a[0:3], v[220:223], 0\n\t"
    "ds_write_b128 v6, v[44:47] offset:4096\n\t"
    "v_mfma_f32_16x16x32_bf16 v[64:67], a[8:11], v[216:219], v[64:67]\n\t"
    "v_mfma_f32_16x16x32_bf16 v[64:67], a[16:19], v[212:215], v[64:67]\n\t"
    "s_add_u32 m0, s63, 6144\n\t"
    "v_mfma_f32_16x16x32_bf16 v[64:67], a[24:27], v[28:31], v[64:67]\n\t"
    "buffer_load_dwordx4 v7, s[52:55], s61 offen offset:0 nt lds\n\t"
    "v_cvt_pk_bf16_f32 v62, v56, v57\n\t"
    "v_mfma_f32_16x16x32_bf16 v[64:67], a[32:35], v[32:35], v[64:67]\n\t"
    "ds_write_b128 v6, v[52:55] offset:5120\n\t"
    "v_mfma_f32_16x16x32_bf16 v[64:67], a[40:43], v[40:43], v[64:67]\n\t"
    "buffer_load_dwordx4 v7, s[52:55], s61 offen offset:1024 nt lds\n\t"
    "v_cvt_pk_bf16_f32 v63, v58, v59\n\t"
    "v_mfma_f32_16x16x32_bf16 v[12:15], v[208:211], v[36:39], v[12:15]\n\t"
    "v_mfma_f32_16x16x32_bf16 v[12:15], v[204:207], v[48:51], v[12:15]\n\t"
    "buffer_load_dwordx4 v7, s[52:55], s61 offen offset:2048 nt lds\n\t"
    "v_mfma_f32_16x16x32_bf16 v[12:15], v[200:203], v[60:63], v[12:15]\n\t"
    "s_add_u32 s61, s61, 196608\n\t"
    "v_mfma_f32_16x16x32_bf16 v[16:19], v[176:179], v[60:63], v[16:19]\n\t"
    "v_cvt_pk_bf16_f32 v44, v64, v65\n\t"
    "v_cvt_pk_bf16_f32 v45, v66, v67\n\t"
    "v_mfma_f32_16x16x32_bf16 v[16:19], v[180:183], v[48:51], v[16:19]\n\t"
    "s_waitcnt lgkmcnt(0)\n\t"
    "s_barrier\n\t"
    "v_mfma_f32_16x16x32_bf16 v[16:19], v[184:187], v[36:39], v[16:19]\n\t"
    "v_mfma_f32_16x16x32_bf16 v[20:23], v[160:163], v[36:39], v[20:23]\n\t"
    "v_max3_f32 v52, v12, v13, v14\n\t"
    "v_mfma_f32_16x16x32_bf16 v[20:23], v[156:159], v[48:51], v[20:23]\n\t"
    "v_mfma_f32_16x16x32_bf16 v[20:23], v[152:155], v[60:63], v[20:23]\n\t"
    "v_mfma_f32_16x16x32_bf16 v[24:27], v[128:131], v[60:63], v[24:27]\n\t"
    "v_max3_f32 v53, v15, v16, v17\n\t"
    "v_max3_f32 v52, v52, v18, v19\n\t"
    "v_mfma_f32_16x16x32_bf16 v[24:27], v[132:135], v[48:51], v[24:27]\n\t"
    "s_waitcnt vmcnt(9)\n\t"
    "v_mfma_f32_16x16x32_bf16 v[24:27], v[136:139], v[36:39], v[24:27]\n\t"
    "ds_read_b128 v[188:191], v2 offset:6144\n\t"
    "v_mfma_f32_16x16x32_bf16 v[56:59], a[44:47], v[40:43], 0\n\t"
    "v_max3_f32 v53, v53, v20, v21\n\t"
    "v_max3_f32 v52, v52, v22, v23\n\t"
    "v_mfma_f32_16x16x32_bf16 v[56:59], a[36:39], v[32:35], v[56:59]\n\t"
    "ds_read_b128 v[192:195], v1 offset:6144\n\t"
    "v_mfma_f32_16x16x32_bf16 v[56:59], a[28:31], v[28:31], v[56:59]\n\t"
    "v_mfma_f32_16x16x32_bf16 v[56:59], a[20:23], v[212:215], v[56:59]\n\t"
    "ds_read_b128 v[196:199], v0 offset:6144\n\t"
    "v_max3_f32 v53, v53, v24, v25\n\t"
    "v_mfma_f32_16x16x32_bf16 v[56:59], a[12:15], v[216:219], v[56:59]\n\t"
    "v_max3_f32 v52, v52, v26, v27\n\t"
    "v_max_f32_e32 v52, v52, v53\n\t"
    "v_mfma_f32_16x16x32_bf16 v[56:59], a[4:7], v[220:223], v[56:59]\n\t"
    "ds_read_b128 v[172:175], v0 offset:12288\n\t"
    "v_mov_b32_e32 v53, v52\n\t"
    "v_mfma_f32_16x16x32_bf16 v[36:39], a[48:51], v[220:223], 0\n\t"
    "v_mfma_f32_16x16x32_bf16 v[36:39], a[56:59], v[216:219], v[36:39]\n\t"
    "ds_read_b128 v[168:171], v1 offset:12288\n\t"
    "v_permlane32_swap_b32 v52, v53\n\t"
    "v_mfma_f32_16x16x32_bf16 v[36:39], a[64:67], v[212:215], v[36:39]\n\t"
    "v_max_f32_e32 v52, v52, v53\n\t"
    "v_mov_b32_e32 v53, v52\n\t"
    "v_mfma_f32_16x16x32_bf16 v[36:39], a[72:75], v[28:31], v[36:39]\n\t"
    "ds_read_b128 v[164:167], v2 offset:12288\n\t"
    "v_cvt_pk_bf16_f32 v46, v56, v57\n\t"
    "v_mfma_f32_16x16x32_bf16 v[36:39], a[80:83], v[32:35], v[36:39]\n\t"
    "v_cvt_pk_bf16_f32 v47, v58, v59\n\t"
    "v_permlane16_swap_b32 v52, v53\n\t"
    "v_mfma_f32_16x16x32_bf16 v[36:39], a[88:91], v[40:43], v[36:39]\n\t"
    "ds_read_b128 v[140:143], v2 offset:18432\n\t"
    "v_max_f32_e32 v52, v52, v53\n\t"
    "v_mfma_f32_16x16x32_bf16 v[48:51], a[92:95], v[40:43], 0\n\t"
    "v_sub_f32_e32 v12, v12, v52\n\t"
    "v_sub_f32_e32 v13, v13, v52\n\t"
    "v_mfma_f32_16x16x32_bf16 v[48:51], a[84:87], v[32:35], v[48:51]\n\t"
    "ds_read_b128 v[144:147], v1 offset:18432\n\t"
    "v_sub_f32_e32 v14, v14, v52\n\t"
    "v_mfma_f32_16x16x32_bf16 v[48:51], a[76:79], v[28:31], v[48:51]\n\t"
    "v_exp_f32_e32 v12, v12\n\t"
    "v_mfma_f32_16x16x32_bf16 v[48:51], a[68:71], v[212:215], v[48:51]\n\t"
    "ds_read_b128 v[148:151], v0 offset:18432\n\t"
    "v_cvt_pk_bf16_f32 v56, v36, v37\n\t"
    "v_mfma_f32_16x16x32_bf16 v[48:51], a[60:63], v[216:219], v[48:51]\n\t"
    "v_cvt_pk_bf16_f32 v57, v38, v39\n\t"
    "v_sub_f32_e32 v15, v15, v52\n\t"
    "v_mfma_f32_16x16x32_bf16 v[48:51], a[52:55], v[220:223], v[48:51]\n\t"
    "v_exp_f32_e32 v13, v13\n\t"
    "v_mfma_f32_16x16x32_bf16 v[60:63], a[96:99], v[220:223], 0\n\t"
    "v_add_f32_e32 v54, v12, v13\n\t"
    "v_sub_f32_e32 v16, v16, v52\n\t"
    "v_mfma_f32_16x16x32_bf16 v[60:63], a[104:107], v[216:219], v[60:63]\n\t"
    "v_exp_f32_e32 v14, v14\n\t"
    "v_mfma_f32_16x16x32_bf16 v[60:63], a[112:115], v[212:215], v[60:63]\n\t"
    "v_exp_f32_e32 v15, v15\n\t"
    "v_mfma_f32_16x16x32_bf16 v[60:63], a[120:123], v[28:31], v[60:63]\n\t"
    "v_cvt_pk_bf16_f32 v58, v48, v49\n\t"
    "v_cvt_pk_bf16_f32 v59, v50, v51\n\t"
    "v_mfma_f32_16x16x32_bf16 v[60:63], a[128:131], v[32:35], v[60:63]\n\t"
    "v_add_f32_e32 v55, v14, v15\n\t"
    "v_sub_f32_e32 v17, v17, v52\n\t"
    "v_mfma_f32_16x16x32_bf16 v[60:63], a[136:139], v[40:43], v[60:63]\n\t"
    "v_exp_f32_e32 v16, v16\n\t"
    "v_mfma_f32_16x16x32_bf16 v[36:39], a[140:143], v[40:43], 0\n\t"
    "v_add_f32_e32 v54, v54, v16\n\t"
    "v_sub_f32_e32 v18, v18, v52\n\t"
    "v_mfma_f32_16x16x32_bf16 v[36:39], a[132:135], v[32:35], v[36:39]\n\t"
    "v_exp_f32_e32 v17, v17\n\t"
    "v_mfma_f32_16x16x32_bf16 v[36:39], a[124:127], v[28:31], v[36:39]\n\t"
    "v_add_f32_e32 v54, v54, v17\n\t"
    "v_sub_f32_e32 v19, v19, v52\n\t"
    "v_mfma_f32_16x16x32_bf16 v[36:39], a[116:119], v[212:215], v[36:39]\n\t"
    "v_cvt_pk_bf16_f32 v48, v60, v61\n\t"
    "v_cvt_pk_bf16_f32 v49, v62, v63\n\t"
    "v_mfma_f32_16x16x32_bf16 v[36:39], a[108:111], v[216:219], v[36:39]\n\t"
    "v_exp_f32_e32 v18, v18\n\t"
    "v_mfma_f32_16x16x32_bf16 v[36:39], a[100:103], v[220:223], v[36:39]\n\t"
    "v_add_f32_e32 v55, v55, v18\n\t"
    "v_sub_f32_e32 v20, v20, v52\n\t"
    "v_mfma_f32_16x16x32_bf16 v[60:63], a[144:147], v[220:223], 0\n\t"
    "v_exp_f32_e32 v19, v19\n\t"
    "v_mfma_f32_16x16x32_bf16 v[60:63], a[152:155], v[216:219], v[60:63]\n\t"
    "v_add_f32_e32 v55, v55, v19\n\t"
    "v_exp_f32_e32 v20, v20\n\t"
    "v_mfma_f32_16x16x32_bf16 v[60:63], a[160:163], v[212:215], v[60:63]\n\t"
    "s_add_u32 m0, s63, 9216\n\t"
    "v_add_f32_e32 v54, v54, v20\n\t"
    "v_mfma_f32_16x16x32_bf16 v[60:63], a[168:171], v[28:31], v[60:63]\n\t"
    "buffer_load_dwordx4 v7, s[52:55], s61 offen offset:0 nt lds\n\t"
    "v_cvt_pk_bf16_f32 v50, v36, v37\n\t"
    "v_mfma_f32_16x16x32_bf16 v[60:63], a[176:179], v[32:35], v[60:63]\n\t"
    "v_cvt_pk_bf16_f32 v51, v38, v39\n\t"
    "v_sub_f32_e32 v21, v21, v52\n\t"
    "v_mfma_f32_16x16x32_bf16 v[60:63], a[184:187], v[40:43], v[60:63]\n\t"
    "buffer_load_dwordx4 v7, s[52:55], s61 offen offset:1024 nt lds\n\t"
    "v_sub_f32_e32 v22, v22, v52\n\t"
    "v_mfma_f32_16x16x32_bf16 v[64:67], v[220:223], v[44:47], 0\n\t"
    "v_exp_f32_e32 v21, v21\n\t"
    "v_mfma_f32_16x16x32_bf16 v[64:67], v[216:219], v[56:59], v[64:67]\n\t"
    "buffer_load_dwordx4 v7, s[52:55], s61 offen offset:2048 nt lds\n\t"
    "v_add_f32_e32 v54, v54, v21\n\t"
    "v_mfma_f32_16x16x32_bf16 v[64:67], v[212:215], v[48:51], v[64:67]\n\t"
    "s_add_u32 s61, s61, 196608\n\t"
    "s_waitcnt lgkmcnt(0)\n\t"
    "v_mfma_f32_16x16x32_bf16 v[36:39], v[188:191], v[48:51], 0\n\t"
    "v_exp_f32_e32 v22, v22\n\t"
    "v_mfma_f32_16x16x32_bf16 v[36:39], v[192:195], v[56:59], v[36:39]\n\t"
    "v_add_f32_e32 v55, v55, v22\n\t"
    "v_sub_f32_e32 v23, v23, v52\n\t"
    "v_mfma_f32_16x16x32_bf16 v[36:39], v[196:199], v[44:47], v[36:39]\n\t"
    "ds_read_b128 v[184:187], v3 offset:6144\n\t"
    "v_sub_f32_e32 v24, v24, v52\n\t"
    "v_mfma_f32_16x16x32_bf16 v[68:71], v[172:175], v[44:47], 0\n\t"
    "v_exp_f32_e32 v23, v23\n\t"
    "v_mfma_f32_16x16x32_bf16 v[68:71], v[168:171], v[56:59], v[68:71]\n\t"
    "ds_read_b128 v[180:183], v4 offset:6144\n\t"
    "v_add_f32_e32 v55, v55, v23\n\t"
    "v_mfma_f32_16x16x32_bf16 v[68:71], v[164:167], v[48:51], v[68:71]\n\t"
    "v_exp_f32_e32 v24, v24\n\t"
    "v_mfma_f32_16x16x32_bf16 v[72:75], v[140:143], v[48:51], 0\n\t"
    "ds_read_b128 v[176:179], v5 offset:6144\n\t"
    "v_add_f32_e32 v54, v54, v24\n\t"
    "v_mfma_f32_16x16x32_bf16 v[72:75], v[144:147], v[56:59], v[72:75]\n\t"
    "v_sub_f32_e32 v25, v25, v52\n\t"
    "v_sub_f32_e32 v26, v26, v52\n\t"
    "v_mfma_f32_16x16x32_bf16 v[72:75], v[148:151], v[44:47], v[72:75]\n\t"
    ".Lh5_%=:\n\t"
    "s_cbranch_execz .LrM_%=\n\t"
    "ds_read_b128 v[208:211], v3 offset:0\n\t"
    "v_sub_f32_e32 v27, v27, v52\n\t"
    "v_mfma_f32_16x16x32_bf16 v[48:51], a[188:191], v[40:43], 0\n\t"
    "v_exp_f32_e32 v25, v25\n\t"
    "v_mfma_f32_16x16x32_bf16 v[48:51], a[180:183], v[32:35], v[48:51]\n\t"
    "ds_read_b128 v[204:207], v4 offset:0\n\t"
    "v_add_f32_e32 v54, v54, v25\n\t"
    "v_mfma_f32_16x16x32_bf16 v[48:51], a[172:175], v[28:31], v[48:51]\n\t"
    "v_exp_f32_e32 v26, v26\n\t"
    "v_mfma_f32_16x16x32_bf16 v[48:51], a[164:167], v[212:215], v[48:51]\n\t"
    "ds_read_b128 v[200:203], v5 offset:0\n\t"
    "v_add_f32_e32 v55, v55, v26\n\t"
    "v_mfma_f32_16x16x32_bf16 v[48:51], a[156:159], v[216:219], v[48:51]\n\t"
    "v_exp_f32_e32 v27, v27\n\t"
    "v_mfma_f32_16x16x32_bf16 v[48:51], a[148:151], v[220:223], v[48:51]\n\t"
    "ds_read_b128 v[160:163], v3 offset:12288\n\t"
    "v_add_f32_e32 v55, v55, v27\n\t"
    "v_mfma_f32_16x16x32_bf16 v[44:47], a[192:195], v[220:223], 0\n\t"
    "v_add_f32_e32 v54, v54, v55\n\t"
    "v_mov_b32_e32 v55, v54\n\t"
    "v_mfma_f32_16x16x32_bf16 v[44:47], a[200:203], v[216:219], v[44:47]\n\t"
    "ds_read_b128 v[156:159], v4 offset:12288\n\t"
    "v_cvt_pk_bf16_f32 v56, v60, v61\n\t"
    "v_mfma_f32_16x16x32_bf16 v[44:47], a[208:211], v[212:215], v[44:47]\n\t"
    "v_permlane32_swap_b32 v54, v55\n\t"
    "v_add_f32_e32 v54, v54, v55\n\t"
    "v_mfma_f32_16x16x32_bf16 v[44:47], a[216:219], v[28:31], v[44:47]\n\t"
    "ds_read_b128 v[152:155], v5 offset:12288\n\t"
    "v_cvt_pk_bf16_f32 v58, v48, v49\n\t"
    "v_mfma_f32_16x16x32_bf16 v[44:47], a[224:227], v[32:35], v[44:47]\n\t"
    "v_cvt_pk_bf16_f32 v59, v50, v51\n\t"
    "v_mov_b32_e32 v55, v54\n\t"
    "v_mfma_f32_16x16x32_bf16 v[44:47], a[232:235], v[40:43], v[44:47]\n\t"
    "ds_read_b128 v[128:131], v5 offset:18432\n\t"
    "v_cvt_pk_bf16_f32 v57, v62, v63\n\t"
    "v_mfma_f32_16x16x32_bf16 v[76:79], a[236:239], v[40:43], 0\n\t"
    "v_permlane16_swap_b32 v54, v55\n\t"
    "v_add_f32_e32 v54, v54, v55\n\t"
    "v_mfma_f32_16x16x32_bf16 v[76:79], a[228:231], v[32:35], v[76:79]\n\t"
    "ds_read_b128 v[132:135], v4 offset:18432\n\t"
    "v_mfma_f32_16x16x32_bf16 v[76:79], a[220:223], v[28:31], v[76:79]\n\t"
    "v_rcp_f32_e32 v54, v54\n\t"
    "v_mfma_f32_16x16x32_bf16 v[76:79], a[212:215], v[212:215], v[76:79]\n\t"
    "ds_read_b128 v[136:139], v3 offset:18432\n\t"
    "v_cvt_pk_bf16_f32 v48, v44, v45\n\t"
    "v_mfma_f32_16x16x32_bf16 v[76:79], a[204:207], v[216:219], v[76:79]\n\t"
    "v_cvt_pk_bf16_f32 v49, v46, v47\n\t"
    "v_mul_f32_e32 v12, v12, v54\n\t"
    "v_mfma_f32_16x16x32_bf16 v[76:79], a[196:199], v[220:223], v[76:79]\n\t"
    "v_mul_f32_e32 v13, v13, v54\n\t"
    "v_cvt_pk_bf16_f32 v60, v12, v13\n\t"
    "v_mfma_f32_16x16x32_bf16 v[80:83], a[240:243], v[220:223], 0\n\t"
    "v_mul_f32_e32 v14, v14, v54\n\t"
    "v_mul_f32_e32 v15, v15, v54\n\t"
    "v_mfma_f32_16x16x32_bf16 v[80:83], a[248:251], v[216:219], v[80:83]\n\t"
    "v_cvt_pk_bf16_f32 v61, v14, v15\n\t"
    "v_mul_f32_e32 v16, v16, v54\n\t"
    "v_mfma_f32_16x16x32_bf16 v[80:83], v[224:227], v[212:215], v[80:83]\n\t"
    "v_mul_f32_e32 v17, v17, v54\n\t"
    "v_cvt_pk_bf16_f32 v62, v16, v17\n\t"
    "v_mfma_f32_16x16x32_bf16 v[80:83], v[232:235], v[28:31], v[80:83]\n\t"
    "v_cvt_pk_bf16_f32 v50, v76, v77\n\t"
    "v_cvt_pk_bf16_f32 v51, v78, v79\n\t"
    "v_mfma_f32_16x16x32_bf16 v[80:83], v[240:243], v[32:35], v[80:83]\n\t"
    "v_mul_f32_e32 v18, v18, v54\n\t"
    "v_mul_f32_e32 v19, v19, v54\n\t"
    "v_mfma_f32_16x16x32_bf16 v[80:83], v[248:251], v[40:43], v[80:83]\n\t"
    "v_cvt_pk_bf16_f32 v63, v18, v19\n\t"
    "v_mul_f32_e32 v20, v20, v54\n\t"
    "v_mfma_f32_16x16x32_bf16 v[12:15], v[252:255], v[40:43], 0\n\t"
    "v_mul_f32_e32 v21, v21, v54\n\t"
    "v_cvt_pk_bf16_f32 v44, v20, v21\n\t"
    "v_mfma_f32_16x16x32_bf16 v[12:15], v[244:247], v[32:35], v[12:15]\n\t"
    "v_mul_f32_e32 v22, v22, v54\n\t"
    "v_mul_f32_e32 v23, v23, v54\n\t"
    "v_mfma_f32_16x16x32_bf16 v[12:15], v[236:239], v[28:31], v[12:15]\n\t"
    "v_cvt_pk_bf16_f32 v45, v22, v23\n\t"
    "v_mul_f32_e32 v24, v24, v54\n\t"
    "v_mfma_f32_16x16x32_bf16 v[12:15], v[228:231], v[212:215], v[12:15]\n\t"
    "v_cvt_pk_bf16_f32 v16, v80, v81\n\t"
    "v_cvt_pk_bf16_f32 v17, v82, v83\n\t"
    "v_mfma_f32_16x16x32_bf16 v[12:15], a[252:255], v[216:219], v[12:15]\n\t"
    "v_mul_f32_e32 v25, v25, v54\n\t"
    "v_cvt_pk_bf16_f32 v46, v24, v25\n\t"
    "v_mfma_f32_16x16x32_bf16 v[12:15], a[244:247], v[220:223], v[12:15]\n\t"
    "v_mul_f32_e32 v26, v26, v54\n\t"
    "v_mul_f32_e32 v27, v27, v54\n\t"
    "v_mfma_f32_16x16x32_bf16 v[20:23], a[0:3], v[196:199], 0\n\t"
    "ds_write_b128 v6, v[60:63] offset:6144\n\t"
    "v_mfma_f32_16x16x32_bf16 v[20:23], a[8:11], v[192:195], v[20:23]\n\t"
    "v_cvt_pk_bf16_f32 v47, v26, v27\n\t"
    "v_mfma_f32_16x16x32_bf16 v[20:23], a[16:19], v[188:191], v[20:23]\n\t"
    "s_add_u32 m0, s63, 12288\n\t"
    "s_waitcnt lgkmcnt(1)\n\t"
    "v_mfma_f32_16x16x32_bf16 v[20:23], a[24:27], v[184:187], v[20:23]\n\t"
    "buffer_load_dwordx4 v7, s[52:55], s61 offen offset:0 nt lds\n\t"
    "v_cvt_pk_bf16_f32 v18, v12, v13\n\t"
    "v_mfma_f32_16x16x32_bf16 v[20:23], a[32:35], v[180:183], v[20:23]\n\t"
    "ds_write_b128 v6, v[44:47] offset:7168\n\t"
    "v_mfma_f32_16x16x32_bf16 v[20:23], a[40:43], v[176:179], v[20:23]\n\t"
    "buffer_load_dwordx4 v7, s[52:55], s61 offen offset:1024 nt lds\n\t"
    "v_cvt_pk_bf16_f32 v19, v14, v15\n\t"
    "v_mfma_f32_16x16x32_bf16 v[64:67], v[208:211], v[56:59], v[64:67]\n\t"
    "v_mfma_f32_16x16x32_bf16 v[64:67], v[204:207], v[48:51], v[64:67]\n\t"
    "buffer_load_dwordx4 v7, s[52:55], s61 offen offset:2048 nt lds\n\t"
    "v_mfma_f32_16x16x32_bf16 v[64:67], v[200:203], v[16:19], v[64:67]\n\t"
    "s_add_u32 s61, s61, 196608\n\t"
    "v_mfma_f32_16x16x32_bf16 v[36:39], v[176:179], v[16:19], v[36:39]\n\t"
    "v_cvt_pk_bf16_f32 v12, v20, v21\n\t"
    "v_cvt_pk_bf16_f32 v13, v22, v23\n\t"
    "v_mfma_f32_16x16x32_bf16 v[36:39], v[180:183], v[48:51], v[36:39]\n\t"
    "s_waitcnt lgkmcnt(0)\n\t"
    "s_barrier\n\t"
    "v_mfma_f32_16x16x32_bf16 v[36:39], v[184:187], v[56:59], v[36:39]\n\t"
    "v_mfma_f32_16x16x32_bf16 v[68:71], v[160:163], v[56:59], v[68:71]\n\t"
    "v_max3_f32 v20, v64, v65, v66\n\t"
    "v_mfma_f32_16x16x32_bf16 v[68:71], v[156:159], v[48:51], v[68:71]\n\t"
    "v_mfma_f32_16x16x32_bf16 v[68:71], v[152:155], v[16:19], v[68:71]\n\t"
    "v_mfma_f32_16x16x32_bf16 v[72:75], v[128:131], v[16:19], v[72:75]\n\t"
    "v_max3_f32 v21, v67, v36, v37\n\t"
    "v_max3_f32 v20, v20, v38, v39\n\t"
    "v_mfma_f32_16x16x32_bf16 v[72:75], v[132:135], v[48:51], v[72:75]\n\t"
    "v_mfma_f32_16x16x32_bf16 v[72:75], v[136:139], v[56:59], v[72:75]\n\t"
    "v_mfma_f32_16x16x32_bf16 v[16:19], a[44:47], v[176:179], 0\n\t"
    "v_max3_f32 v21, v21, v68, v69\n\t"
    "v_max3_f32 v20, v20, v70, v71\n\t"
    "v_mfma_f32_16x16x32_bf16 v[16:19], a[36:39], v[180:183], v[16:19]\n\t"
    "v_mfma_f32_16x16x32_bf16 v[16:19], a[28:31], v[184:187], v[16:19]\n\t"
    "v_mfma_f32_16x16x32_bf16 v[16:19], a[20:23], v[188:191], v[16:19]\n\t"
    "v_max3_f32 v21, v21, v72, v73\n\t"
    "v_max3_f32 v20, v20, v74, v75\n\t"
    "v_mfma_f32_16x16x32_bf16 v[16:19], a[12:15], v[192:195], v[16:19]\n\t"
    "v_max_f32_e32 v20, v20, v21\n\t"
    "v_mov_b32_e32 v21, v20\n\t"
    "v_mfma_f32_16x16x32_bf16 v[16:19], a[4:7], v[196:199], v[16:19]\n\t"
    "v_mfma_f32_16x16x32_bf16 v[24:27], a[48:51], v[196:199], 0\n\t"
    "v_permlane32_swap_b32 v20, v21\n\t"
    "v_max_f32_e32 v20, v20, v21\n\t"
    "v_mfma_f32_16x16x32_bf16 v[24:27], a[56:59], v[192:195], v[24:27]\n\t"
    "v_mov_b32_e32 v21, v20\n\t"
    "v_mfma_f32_16x16x32_bf16 v[24:27], a[64:67], v[188:191], v[24:27]\n\t"
    "v_mfma_f32_16x16x32_bf16 v[24:27], a[72:75], v[184:187], v[24:27]\n\t"
    "v_cvt_pk_bf16_f32 v14, v16, v17\n\t"
    "v_cvt_pk_bf16_f32 v15, v18, v19\n\t"
    "v_mfma_f32_16x16x32_bf16 v[24:27], a[80:83], v[180:183], v[24:27]\n\t"
    "v_permlane16_swap_b32 v20, v21\n\t"
    "v_max_f32_e32 v20, v20, v21\n\t"
    "v_mfma_f32_16x16x32_bf16 v[24:27], a[88:91], v[176:179], v[24:27]\n\t"
    "v_sub_f32_e32 v64, v64, v20\n\t"
    "v_sub_f32_e32 v65, v65, v20\n\t"
    "v_mfma_f32_16x16x32_bf16 v[16:19], a[92:95], v[176:179], 0\n\t"
    "v_exp_f32_e32 v64, v64\n\t"
    "v_mfma_f32_16x16x32_bf16 v[16:19], a[84:87], v[180:183], v[16:19]\n\t"
    "v_exp_f32_e32 v65, v65\n\t"
    "v_mfma_f32_16x16x32_bf16 v[16:19], a[76:79], v[184:187], v[16:19]\n\t"
    "v_add_f32_e32 v22, v64, v65\n\t"
    "v_sub_f32_e32 v66, v66, v20\n\t"
    "v_mfma_f32_16x16x32_bf16 v[16:19], a[68:71], v[188:191], v[16:19]\n\t"
    "v_cvt_pk_bf16_f32 v28, v24, v25\n\t"
    "v_cvt_pk_bf16_f32 v29, v26, v27\n\t"
    "v_mfma_f32_16x16x32_bf16 v[16:19], a[60:63], v[192:195], v[16:19]\n\t"
    "v_exp_f32_e32 v66, v66\n\t"
    "v_mfma_f32_16x16x32_bf16 v[16:19], a[52:55], v[196:199], v[16:19]\n\t"
    "v_sub_f32_e32 v67, v67, v20\n\t"
    "v_sub_f32_e32 v36, v36, v20\n\t"
    "v_mfma_f32_16x16x32_bf16 v[24:27], a[96:99], v[196:199], 0\n\t"
    "v_exp_f32_e32 v67, v67\n\t"
    "v_mfma_f32_16x16x32_bf16 v[24:27], a[104:107], v[192:195], v[24:27]\n\t"
    "v_add_f32_e32 v23, v66, v67\n\t"
    "v_sub_f32_e32 v37, v37, v20\n\t"
    "v_mfma_f32_16x16x32_bf16 v[24:27], a[112:115], v[188:191], v[24:27]\n\t"
    "v_exp_f32_e32 v36, v36\n\t"
    "v_mfma_f32_16x16x32_bf16 v[24:27], a[120:123], v[184:187], v[24:27]\n\t"
    "v_cvt_pk_bf16_f32 v30, v16, v17\n\t"
    "v_cvt_pk_bf16_f32 v31, v18, v19\n\t"
    "v_mfma_f32_16x16x32_bf16 v[24:27], a[128:131], v[180:183], v[24:27]\n\t"
    "v_add_f32_e32 v22, v22, v36\n\t"
    "v_sub_f32_e32 v38, v38, v20\n\t"
    "v_mfma_f32_16x16x32_bf16 v[24:27], a[136:139], v[176:179], v[24:27]\n\t"
    "v_exp_f32_e32 v37, v37\n\t"
    "v_mfma_f32_16x16x32_bf16 v[16:19], a[140:143], v[176:179], 0\n\t"
    "v_add_f32_e32 v22, v22, v37\n\t"
    "v_sub_f32_e32 v39, v39, v20\n\t"
    "v_mfma_f32_16x16x32_bf16 v[16:19], a[132:135], v[180:183], v[16:19]\n\t"
    "v_exp_f32_e32 v38, v38\n\t"
    "v_mfma_f32_16x16x32_bf16 v[16:19], a[124:127], v[184:187], v[16:19]\n\t"
    "v_add_f32_e32 v23, v23, v38\n\t"
    "v_sub_f32_e32 v68, v68, v20\n\t"
    "v_mfma_f32_16x16x32_bf16 v[16:19], a[116:119], v[188:191], v[16:19]\n\t"
    "v_cvt_pk_bf16_f32 v32, v24, v25\n\t"
    "v_cvt_pk_bf16_f32 v33, v26, v27\n\t"
    "v_mfma_f32_16x16x32_bf16 v[16:19], a[108:111], v[192:195], v[16:19]\n\t"
    "v_exp_f32_e32 v39, v39\n\t"
    "v_mfma_f32_16x16x32_bf16 v[16:19], a[100:103], v[196:199], v[16:19]\n\t"
    "v_add_f32_e32 v23, v23, v39\n\t"
    "v_sub_f32_e32 v69, v69, v20\n\t"
    "v_mfma_f32_16x16x32_bf16 v[24:27], a[144:147], v[196:199], 0\n\t"
    "v_exp_f32_e32 v68, v68\n\t"
    "v_mfma_f32_16x16x32_bf16 v[24:27], a[152:155], v[192:195], v[24:27]\n\t"
    "v_add_f32_e32 v22, v22, v68\n\t"
    "v_sub_f32_e32 v70, v70, v20\n\t"
    "v_mfma_f32_16x16x32_bf16 v[24:27], a[160:163], v[188:191], v[24:27]\n\t"
    "s_add_u32 m0, s63, 15360\n\t"
    "v_sub_f32_e32 v71, v71, v20\n\t"
    "v_mfma_f32_16x16x32_bf16 v[24:27], a[168:171], v[184:187], v[24:27]\n\t"
    "buffer_load_dwordx4 v7, s[52:55], s61 offen offset:0 nt lds\n\t"
    "v_cvt_pk_bf16_f32 v34, v16, v17\n\t"
    "v_mfma_f32_16x16x32_bf16 v[24:27], a[176:179], v[180:183], v[24:27]\n\t"
    "v_cvt_pk_bf16_f32 v35, v18, v19\n\t"
    "v_sub_f32_e32 v72, v72, v20\n\t"
    "v_mfma_f32_16x16x32_bf16 v[24:27], a[184:187], v[176:179], v[24:27]\n\t"
    "buffer_load_dwordx4 v7, s[52:55], s61 offen offset:1024 nt lds\n\t"
    "v_sub_f32_e32 v73, v73, v20\n\t"
    "v_mfma_f32_16x16x32_bf16 v[40:43], v[220:223], v[12:15], 0\n\t"
    "v_exp_f32_e32 v69, v69\n\t"
    "v_mfma_f32_16x16x32_bf16 v[40:43], v[216:219], v[28:31], v[40:43]\n\t"
    "buffer_load_dwordx4 v7, s[52:55], s61 offen offset:2048 nt lds\n\t"
    "v_add_f32_e32 v22, v22, v69\n\t"
    "v_mfma_f32_16x16x32_bf16 v[40:43], v[212:215], v[32:35], v[40:43]\n\t"
    "s_add_u32 s61, s61, 196608\n\t"
    "v_sub_f32_e32 v74, v74, v20\n\t"
    "v_mfma_f32_16x16x32_bf16 v[16:19], v[188:191], v[32:35], 0\n\t"
    "v_exp_f32_e32 v70, v70\n\t"
    "v_mfma_f32_16x16x32_bf16 v[16:19], v[192:195], v[28:31], v[16:19]\n\t"
    "v_add_f32_e32 v23, v23, v70\n\t"
    "v_sub_f32_e32 v75, v75, v20\n\t"
    "v_mfma_f32_16x16x32_bf16 v[16:19], v[196:199], v[12:15], v[16:19]\n\t"
    "v_exp_f32_e32 v71, v71\n\t"
    "v_mfma_f32_16x16x32_bf16 v[44:47], v[172:175], v[12:15], 0\n\t"
    "v_add_f32_e32 v23, v23, v71\n\t"
    "v_cvt_pk_bf16_f32 v48, v24, v25\n\t"
    "v_mfma_f32_16x16x32_bf16 v[44:47], v[168:171], v[28:31], v[44:47]\n\t"
    "v_exp_f32_e32 v72, v72\n\t"
    "v_mfma_f32_16x16x32_bf16 v[44:47], v[164:167], v[32:35], v[44:47]\n\t"
    "v_add_f32_e32 v22, v22, v72\n\t"
    "v_cvt_pk_bf16_f32 v49, v26, v27\n\t"
    "v_mfma_f32_16x16x32_bf16 v[52:55], v[140:143], v[32:35], 0\n\t"
    "v_exp_f32_e32 v73, v73\n\t"
    "v_mfma_f32_16x16x32_bf16 v[52:55], v[144:147], v[28:31], v[52:55]\n\t"
    "v_add_f32_e32 v22, v22, v73\n\t"
    "v_mfma_f32_16x16x32_bf16 v[52:55], v[148:151], v[12:15], v[52:55]\n\t"
    "v_exp_f32_e32 v74, v74\n\t"
    "v_mfma_f32_16x16x32_bf16 v[24:27], a[188:191], v[176:179], 0\n\t"
    "v_add_f32_e32 v23, v23, v74\n\t"
    "v_mfma_f32_16x16x32_bf16 v[24:27], a[180:183], v[180:183], v[24:27]\n\t"
    "v_exp_f32_e32 v75, v75\n\t"
    "v_mfma_f32_16x16x32_bf16 v[24:27], a[172:175], v[184:187], v[24:27]\n\t"
    "v_add_f32_e32 v23, v23, v75\n\t"
    "v_add_f32_e32 v22, v22, v23\n\t"
    "v_mfma_f32_16x16x32_bf16 v[24:27], a[164:167], v[188:191], v[24:27]\n\t"
    "v_mov_b32_e32 v23, v22\n\t"
    "v_mfma_f32_16x16x32_bf16 v[24:27], a[156:159], v[192:195], v[24:27]\n\t"
    "v_mfma_f32_16x16x32_bf16 v[24:27], a[148:151], v[196:199], v[24:27]\n\t"
    "v_permlane32_swap_b32 v22, v23\n\t"
    "v_add_f32_e32 v22, v22, v23\n\t"
    "v_mfma_f32_16x16x32_bf16 v[12:15], a[192:195], v[196:199], 0\n\t"
    "v_mov_b32_e32 v23, v22\n\t"
    "v_mfma_f32_16x16x32_bf16 v[12:15], a[200:203], v[192:195], v[12:15]\n\t"
    "v_mfma_f32_16x16x32_bf16 v[12:15], a[208:211], v[188:191], v[12:15]\n\t"
    "v_permlane16_swap_b32 v22, v23\n\t"
    "v_add_f32_e32 v22, v22, v23\n\t"
    "v_mfma_f32_16x16x32_bf16 v[12:15], a[216:219], v[184:187], v[12:15]\n\t"
    "v_cvt_pk_bf16_f32 v50, v24, v25\n\t"
    "v_cvt_pk_bf16_f32 v51, v26, v27\n\t"
    "v_mfma_f32_16x16x32_bf16 v[12:15], a[224:227], v[180:183], v[12:15]\n\t"
    "v_rcp_f32_e32 v22, v22\n\t"
    "v_mfma_f32_16x16x32_bf16 v[12:15], a[232:235], v[176:179], v[12:15]\n\t"
    "v_mul_f32_e32 v64, v64, v22\n\t"
    "v_mul_f32_e32 v65, v65, v22\n\t"
    "v_mfma_f32_16x16x32_bf16 v[28:31], a[236:239], v[176:179], 0\n\t"
    "v_cvt_pk_bf16_f32 v24, v64, v65\n\t"
    "v_mul_f32_e32 v66, v66, v22\n\t"
    "v_mfma_f32_16x16x32_bf16 v[28:31], a[228:231], v[180:183], v[28:31]\n\t"
    "v_mul_f32_e32 v67, v67, v22\n\t"
    "v_cvt_pk_bf16_f32 v25, v66, v67\n\t"
    "v_mfma_f32_16x16x32_bf16 v[28:31], a[220:223], v[184:187], v[28:31]\n\t"
    "v_mul_f32_e32 v36, v36, v22\n\t"
    "v_mul_f32_e32 v37, v37, v22\n\t"
    "v_mfma_f32_16x16x32_bf16 v[28:31], a[212:215], v[188:191], v[28:31]\n\t"
    "v_cvt_pk_bf16_f32 v32, v12, v13\n\t"
    "v_cvt_pk_bf16_f32 v33, v14, v15\n\t"
    "v_mfma_f32_16x16x32_bf16 v[28:31], a[204:207], v[192:195], v[28:31]\n\t"
    "v_cvt_pk_bf16_f32 v26, v36, v37\n\t"
    "v_mul_f32_e32 v38, v38, v22\n\t"
    "v_mfma_f32_16x16x32_bf16 v[28:31], a[196:199], v[196:199], v[28:31]\n\t"
    "v_mul_f32_e32 v39, v39, v22\n\t"
    "v_cvt_pk_bf16_f32 v27, v38, v39\n\t"
    "v_mfma_f32_16x16x32_bf16 v[12:15], a[240:243], v[196:199], 0\n\t"
    "v_mul_f32_e32 v68, v68, v22\n\t"
    "v_mul_f32_e32 v69, v69, v22\n\t"
    "v_mfma_f32_16x16x32_bf16 v[12:15], a[248:251], v[192:195], v[12:15]\n\t"
    "v_cvt_pk_bf16_f32 v56, v68, v69\n\t"
    "v_mul_f32_e32 v70, v70, v22\n\t"
    "v_mfma_f32_16x16x32_bf16 v[12:15], v[224:227], v[188:191], v[12:15]\n\t"
    "v_mul_f32_e32 v71, v71, v22\n\t"
    "v_cvt_pk_bf16_f32 v57, v70, v71\n\t"
    "v_mfma_f32_16x16x32_bf16 v[12:15], v[232:235], v[184:187], v[12:15]\n\t"
    "v_cvt_pk_bf16_f32 v34, v28, v29\n\t"
    "v_cvt_pk_bf16_f32 v35, v30, v31\n\t"
    "v_mfma_f32_16x16x32_bf16 v[12:15], v[240:243], v[180:183], v[12:15]\n\t"
    "v_mul_f32_e32 v72, v72, v22\n\t"
    "v_mul_f32_e32 v73, v73, v22\n\t"
    "v_mfma_f32_16x16x32_bf16 v[12:15], v[248:251], v[176:179], v[12:15]\n\t"
    "v_cvt_pk_bf16_f32 v58, v72, v73\n\t"
    "v_mul_f32_e32 v74, v74, v22\n\t"
    "v_mfma_f32_16x16x32_bf16 v[28:31], v[252:255], v[176:179], 0\n\t"
    "v_mul_f32_e32 v75, v75, v22\n\t"
    "v_cvt_pk_bf16_f32 v59, v74, v75\n\t"
    "v_mfma_f32_16x16x32_bf16 v[28:31], v[244:247], v[180:183], v[28:31]\n\t"
    "v_mfma_f32_16x16x32_bf16 v[28:31], v[236:239], v[184:187], v[28:31]\n\t"
    "v_mfma_f32_16x16x32_bf16 v[28:31], v[228:231], v[188:191], v[28:31]\n\t"
    "v_cvt_pk_bf16_f32 v20, v12, v13\n\t"
    "v_cvt_pk_bf16_f32 v21, v14, v15\n\t"
    "v_mfma_f32_16x16x32_bf16 v[28:31], a[252:255], v[192:195], v[28:31]\n\t"
    "v_mfma_f32_16x16x32_bf16 v[28:31], a[244:247], v[196:199], v[28:31]\n\t"
    "v_mfma_f32_16x16x32_bf16 v[12:15], a[0:3], v[172:175], 0\n\t"
    "ds_write_b128 v6, v[24:27] offset:0\n\t"
    "v_mfma_f32_16x16x32_bf16 v[12:15], a[8:11], v[168:171], v[12:15]\n\t"
    "v_mfma_f32_16x16x32_bf16 v[12:15], a[16:19], v[164:167], v[12:15]\n\t"
    "s_add_u32 m0, s63, 18432\n\t"
    "v_mfma_f32_16x16x32_bf16 v[12:15], a[24:27], v[160:163], v[12:15]\n\t"
    "buffer_load_dwordx4 v7, s[52:55], s61 offen offset:0 nt lds\n\t"
    "v_cvt_pk_bf16_f32 v22, v28, v29\n\t"
    "v_mfma_f32_16x16x32_bf16 v[12:15], a[32:35], v[156:159], v[12:15]\n\t"
    "ds_write_b128 v6, v[56:59] offset:1024\n\t"
    "v_mfma_f32_16x16x32_bf16 v[12:15], a[40:43], v[152:155], v[12:15]\n\t"
    "buffer_load_dwordx4 v7, s[52:55], s61 offen offset:1024 nt lds\n\t"
    "v_cvt_pk_bf16_f32 v23, v30, v31\n\t"
    "v_mfma_f32_16x16x32_bf16 v[40:43], v[208:211], v[48:51], v[40:43]\n\t"
    "v_mfma_f32_16x16x32_bf16 v[40:43], v[204:207], v[32:35], v[40:43]\n\t"
    "buffer_load_dwordx4 v7, s[52:55], s61 offen offset:2048 nt lds\n\t"
    "v_mfma_f32_16x16x32_bf16 v[40:43], v[200:203], v[20:23], v[40:43]\n\t"
    "s_add_u32 s61, s61, 196608\n\t"
    "s_add_i32 s64, s62, 24576\n\t"
    "v_mfma_f32_16x16x32_bf16 v[16:19], v[176:179], v[20:23], v[16:19]\n\t"
    "s_cmp_eq_u32 s64, %[rend]\n\t"
    "s_cselect_b32 s64, %[rbeg], s64\n\t"
    "v_mfma_f32_16x16x32_bf16 v[16:19], v[180:183], v[32:35], v[16:19]\n\t"
    "s_sub_i32 s65, s63, s64\n\t"
    "v_cvt_pk_bf16_f32 v24, v12, v13\n\t"
    "v_mfma_f32_16x16x32_bf16 v[16:19], v[184:187], v[48:51], v[16:19]\n\t"
    "v_add_u32_e32 v0, s65, v0\n\t"
    "v_add_u32_e32 v1, s65, v1\n\t"
    "v_mfma_f32_16x16x32_bf16 v[44:47], v[160:163], v[48:51], v[44:47]\n\t"
    "v_add_u32_e32 v2, s65, v2\n\t"
    "v_add_u32_e32 v3, s65, v3\n\t"
    "v_mfma_f32_16x16x32_bf16 v[44:47], v[156:159], v[32:35], v[44:47]\n\t"
    "v_add_u32_e32 v4, s65, v4\n\t"
    "v_add_u32_e32 v5, s65, v5\n\t"
    "v_mfma_f32_16x16x32_bf16 v[44:47], v[152:155], v[20:23], v[44:47]\n\t"
    "v_max3_f32 v28, v40, v41, v42\n\t"
    "v_cvt_pk_bf16_f32 v25, v14, v15\n\t"
    "v_mfma_f32_16x16x32_bf16 v[52:55], v[128:131], v[20:23], v[52:55]\n\t"
    "v_max3_f32 v29, v43, v16, v17\n\t"
    "v_max3_f32 v28, v28, v18, v19\n\t"
    "v_mfma_f32_16x16x32_bf16 v[52:55], v[132:135], v[32:35], v[52:55]\n\t"
    "v_mfma_f32_16x16x32_bf16 v[52:55], v[136:139], v[48:51], v[52:55]\n\t"
    "v_mfma_f32_16x16x32_bf16 v[12:15], a[44:47], v[152:155], 0\n\t"
    "v_max3_f32 v29, v29, v44, v45\n\t"
    "v_max3_f32 v28, v28, v46, v47\n\t"
    "v_mfma_f32_16x16x32_bf16 v[12:15], a[36:39], v[156:159], v[12:15]\n\t"
    "v_mfma_f32_16x16x32_bf16 v[12:15], a[28:31], v[160:163], v[12:15]\n\t"
    "v_mfma_f32_16x16x32_bf16 v[12:15], a[20:23], v[164:167], v[12:15]\n\t"
    "v_max3_f32 v29, v29, v52, v53\n\t"
    "v_max3_f32 v28, v28, v54, v55\n\t"
    "v_mfma_f32_16x16x32_bf16 v[12:15], a[12:15], v[168:171], v[12:15]\n\t"
    "v_max_f32_e32 v28, v28, v29\n\t"
    "v_mov_b32_e32 v29, v28\n\t"
    "v_mfma_f32_16x16x32_bf16 v[12:15], a[4:7], v[172:175], v[12:15]\n\t"
    "v_mfma_f32_16x16x32_bf16 v[20:23], a[48:51], v[172:175], 0\n\t"
    "v_permlane32_swap_b32 v28, v29\n\t"
    "v_max_f32_e32 v28, v28, v29\n\t"
    "v_mfma_f32_16x16x32_bf16 v[20:23], a[56:59], v[168:171], v[20:23]\n\t"
    "v_mov_b32_e32 v29, v28\n\t"
    "v_mfma_f32_16x16x32_bf16 v[20:23], a[64:67], v[164:167], v[20:23]\n\t"
    "v_mfma_f32_16x16x32_bf16 v[20:23], a[72:75], v[160:163], v[20:23]\n\t"
    "v_cvt_pk_bf16_f32 v26, v12, v13\n\t"
    "v_cvt_pk_bf16_f32 v27, v14, v15\n\t"
    "v_mfma_f32_16x16x32_bf16 v[20:23], a[80:83], v[156:159], v[20:23]\n\t"
    "v_permlane16_swap_b32 v28, v29\n\t"
    "v_max_f32_e32 v28, v28, v29\n\t"
    "v_mfma_f32_16x16x32_bf16 v[20:23], a[88:91], v[152:155], v[20:23]\n\t"
    "v_sub_f32_e32 v40, v40, v28\n\t"
    "v_sub_f32_e32 v41, v41, v28\n\t"
    "v_mfma_f32_16x16x32_bf16 v[12:15], a[92:95], v[152:155], 0\n\t"
    "v_exp_f32_e32 v40, v40\n\t"
    "v_mfma_f32_16x16x32_bf16 v[12:15], a[84:87], v[156:159], v[12:15]\n\t"
    "v_exp_f32_e32 v41, v41\n\t"
    "v_mfma_f32_16x16x32_bf16 v[12:15], a[76:79], v[160:163], v[12:15]\n\t"
    "v_add_f32_e32 v30, v40, v41\n\t"
    "v_sub_f32_e32 v42, v42, v28\n\t"
    "v_mfma_f32_16x16x32_bf16 v[12:15], a[68:71], v[164:167], v[12:15]\n\t"
    "v_cvt_pk_bf16_f32 v32, v20, v21\n\t"
    "v_cvt_pk_bf16_f32 v33, v22, v23\n\t"
    "v_mfma_f32_16x16x32_bf16 v[12:15], a[60:63], v[168:171], v[12:15]\n\t"
    "v_exp_f32_e32 v42, v42\n\t"
    "v_mfma_f32_16x16x32_bf16 v[12:15], a[52:55], v[172:175], v[12:15]\n\t"
    "v_sub_f32_e32 v43, v43, v28\n\t"
    "v_sub_f32_e32 v16, v16, v28\n\t"
    "v_mfma_f32_16x16x32_bf16 v[20:23], a[96:99], v[172:175], 0\n\t"
    "v_exp_f32_e32 v43, v43\n\t"
    "v_mfma_f32_16x16x32_bf16 v[20:23], a[104:107], v[168:171], v[20:23]\n\t"
    "v_add_f32_e32 v31, v42, v43\n\t"
    "v_sub_f32_e32 v17, v17, v28\n\t"
    "v_mfma_f32_16x16x32_bf16 v[20:23], a[112:115], v[164:167], v[20:23]\n\t"
    "v_exp_f32_e32 v16, v16\n\t"
    "v_mfma_f32_16x16x32_bf16 v[20:23], a[120:123], v[160:163], v[20:23]\n\t"
    "v_cvt_pk_bf16_f32 v34, v12, v13\n\t"
    "v_cvt_pk_bf16_f32 v35, v14, v15\n\t"
    "v_mfma_f32_16x16x32_bf16 v[20:23], a[128:131], v[156:159], v[20:23]\n\t"
    "v_add_f32_e32 v30, v30, v16\n\t"
    "v_sub_f32_e32 v18, v18, v28\n\t"
    "v_mfma_f32_16x16x32_bf16 v[20:23], a[136:139], v[152:155], v[20:23]\n\t"
    "v_exp_f32_e32 v17, v17\n\t"
    "v_mfma_f32_16x16x32_bf16 v[12:15], a[140:143], v[152:155], 0\n\t"
    "v_add_f32_e32 v30, v30, v17\n\t"
    "v_sub_f32_e32 v19, v19, v28\n\t"
    "v_mfma_f32_16x16x32_bf16 v[12:15], a[132:135], v[156:159], v[12:15]\n\t"
    "v_exp_f32_e32 v18, v18\n\t"
    "v_mfma_f32_16x16x32_bf16 v[12:15], a[124:127], v[160:163], v[12:15]\n\t"
    "v_add_f32_e32 v31, v31, v18\n\t"
    "v_sub_f32_e32 v44, v44, v28\n\t"
    "v_mfma_f32_16x16x32_bf16 v[12:15], a[116:119], v[164:167], v[12:15]\n\t"
    "v_cvt_pk_bf16_f32 v36, v20, v21\n\t"
    "v_cvt_pk_bf16_f32 v37, v22, v23\n\t"
    "v_mfma_f32_16x16x32_bf16 v[12:15], a[108:111], v[168:171], v[12:15]\n\t"
    "v_exp_f32_e32 v19, v19\n\t"
    "v_mfma_f32_16x16x32_bf16 v[12:15], a[100:103], v[172:175], v[12:15]\n\t"
    "v_add_f32_e32 v31, v31, v19\n\t"
    "v_sub_f32_e32 v45, v45, v28\n\t"
    "v_mfma_f32_16x16x32_bf16 v[20:23], a[144:147], v[172:175], 0\n\t"
    "v_exp_f32_e32 v44, v44\n\t"
    "v_mfma_f32_16x16x32_bf16 v[20:23], a[152:155], v[168:171], v[20:23]\n\t"
    "v_add_f32_e32 v30, v30, v44\n\t"
    "v_sub_f32_e32 v46, v46, v28\n\t"
    "v_mfma_f32_16x16x32_bf16 v[20:23], a[160:163], v[164:167], v[20:23]\n\t"
    "s_add_u32 m0, s63, 21504\n\t"
    "v_sub_f32_e32 v47, v47, v28\n\t"
    "v_mfma_f32_16x16x32_bf16 v[20:23], a[168:171], v[160:163], v[20:23]\n\t"
    "buffer_load_dwordx4 v7, s[52:55], s61 offen offset:0 nt lds\n\t"
    "v_cvt_pk_bf16_f32 v38, v12, v13\n\t"
    "v_mfma_f32_16x16x32_bf16 v[20:23], a[176:179], v[156:159], v[20:23]\n\t"
    "v_cvt_pk_bf16_f32 v39, v14, v15\n\t"
    "v_sub_f32_e32 v52, v52, v28\n\t"
    "v_mfma_f32_16x16x32_bf16 v[20:23], a[184:187], v[152:155], v[20:23]\n\t"
    "buffer_load_dwordx4 v7, s[52:55], s61 offen offset:1024 nt lds\n\t"
    "v_sub_f32_e32 v53, v53, v28\n\t"
    "v_mfma_f32_16x16x32_bf16 v[56:59], v[220:223], v[24:27], 0\n\t"
    "v_exp_f32_e32 v45, v45\n\t"
    "v_mfma_f32_16x16x32_bf16 v[56:59], v[216:219], v[32:35], v[56:59]\n\t"
    "buffer_load_dwordx4 v7, s[52:55], s61 offen offset:2048 nt lds\n\t"
    "v_add_f32_e32 v30, v30, v45\n\t"
    "v_mfma_f32_16x16x32_bf16 v[56:59], v[212:215], v[36:39], v[56:59]\n\t"
    "s_add_u32 s61, s61, 11206656\n\t"
    "v_sub_f32_e32 v54, v54, v28\n\t"
    "v_mfma_f32_16x16x32_bf16 v[60:63], v[188:191], v[36:39], 0\n\t"
    "v_exp_f32_e32 v46, v46\n\t"
    "v_mfma_f32_16x16x32_bf16 v[60:63], v[192:195], v[32:35], v[60:63]\n\t"
    "v_add_f32_e32 v31, v31, v46\n\t"
    "v_sub_f32_e32 v55, v55, v28\n\t"
    "v_mfma_f32_16x16x32_bf16 v[60:63], v[196:199], v[24:27], v[60:63]\n\t"
    "v_exp_f32_e32 v47, v47\n\t"
    "v_mfma_f32_16x16x32_bf16 v[64:67], v[172:175], v[24:27], 0\n\t"
    "v_add_f32_e32 v31, v31, v47\n\t"
    "v_cvt_pk_bf16_f32 v12, v20, v21\n\t"
    "v_mfma_f32_16x16x32_bf16 v[64:67], v[168:171], v[32:35], v[64:67]\n\t"
    "v_exp_f32_e32 v52, v52\n\t"
    "v_mfma_f32_16x16x32_bf16 v[64:67], v[164:167], v[36:39], v[64:67]\n\t"
    "v_add_f32_e32 v30, v30, v52\n\t"
    "v_cvt_pk_bf16_f32 v13, v22, v23\n\t"
    "v_mfma_f32_16x16x32_bf16 v[68:71], v[140:143], v[36:39], 0\n\t"
    "v_exp_f32_e32 v53, v53\n\t"
    "v_mfma_f32_16x16x32_bf16 v[68:71], v[144:147], v[32:35], v[68:71]\n\t"
    "v_add_f32_e32 v30, v30, v53\n\t"
    "v_mfma_f32_16x16x32_bf16 v[68:71], v[148:151], v[24:27], v[68:71]\n\t"
    "v_exp_f32_e32 v54, v54\n\t"
    "v_mfma_f32_16x16x32_bf16 v[20:23], a[188:191], v[152:155], 0\n\t"
    "v_add_f32_e32 v31, v31, v54\n\t"
    "v_mfma_f32_16x16x32_bf16 v[20:23], a[180:183], v[156:159], v[20:23]\n\t"
    "v_exp_f32_e32 v55, v55\n\t"
    "v_mfma_f32_16x16x32_bf16 v[20:23], a[172:175], v[160:163], v[20:23]\n\t"
    "v_add_f32_e32 v31, v31, v55\n\t"
    "v_add_f32_e32 v30, v30, v31\n\t"
    "v_mfma_f32_16x16x32_bf16 v[20:23], a[164:167], v[164:167], v[20:23]\n\t"
    "v_mov_b32_e32 v31, v30\n\t"
    "v_mfma_f32_16x16x32_bf16 v[20:23], a[156:159], v[168:171], v[20:23]\n\t"
    "v_mfma_f32_16x16x32_bf16 v[20:23], a[148:151], v[172:175], v[20:23]\n\t"
    "v_permlane32_swap_b32 v30, v31\n\t"
    "v_add_f32_e32 v30, v30, v31\n\t"
    "v_mfma_f32_16x16x32_bf16 v[24:27], a[192:195], v[172:175], 0\n\t"
    "v_mov_b32_e32 v31, v30\n\t"
    "v_mfma_f32_16x16x32_bf16 v[24:27], a[200:203], v[168:171], v[24:27]\n\t"
    "v_mfma_f32_16x16x32_bf16 v[24:27], a[208:211], v[164:167], v[24:27]\n\t"
    "v_permlane16_swap_b32 v30, v31\n\t"
    "v_add_f32_e32 v30, v30, v31\n\t"
    "v_mfma_f32_16x16x32_bf16 v[24:27], a[216:219], v[160:163], v[24:27]\n\t"
    "v_cvt_pk_bf16_f32 v14, v20, v21\n\t"
    "v_cvt_pk_bf16_f32 v15, v22, v23\n\t"
    "v_mfma_f32_16x16x32_bf16 v[24:27], a[224:227], v[156:159], v[24:27]\n\t"
    "v_rcp_f32_e32 v30, v30\n\t"
    "v_mfma_f32_16x16x32_bf16 v[24:27], a[232:235], v[152:155], v[24:27]\n\t"
    "v_mul_f32_e32 v40, v40, v30\n\t"
    "v_mul_f32_e32 v41, v41, v30\n\t"
    "v_mfma_f32_16x16x32_bf16 v[32:35], a[236:239], v[152:155], 0\n\t"
    "v_cvt_pk_bf16_f32 v20, v40, v41\n\t"
    "v_mul_f32_e32 v42, v42, v30\n\t"
    "v_mfma_f32_16x16x32_bf16 v[32:35], a[228:231], v[156:159], v[32:35]\n\t"
    "v_mul_f32_e32 v43, v43, v30\n\t"
    "v_cvt_pk_bf16_f32 v21, v42, v43\n\t"
    "v_mfma_f32_16x16x32_bf16 v[32:35], a[220:223], v[160:163], v[32:35]\n\t"
    "v_mul_f32_e32 v16, v16, v30\n\t"
    "v_mul_f32_e32 v17, v17, v30\n\t"
    "v_mfma_f32_16x16x32_bf16 v[32:35], a[212:215], v[164:167], v[32:35]\n\t"
    "v_cvt_pk_bf16_f32 v36, v24, v25\n\t"
    "v_cvt_pk_bf16_f32 v37, v26, v27\n\t"
    "v_mfma_f32_16x16x32_bf16 v[32:35], a[204:207], v[168:171], v[32:35]\n\t"
    "v_cvt_pk_bf16_f32 v22, v16, v17\n\t"
    "v_mul_f32_e32 v18, v18, v30\n\t"
    "v_mfma_f32_16x16x32_bf16 v[32:35], a[196:199], v[172:175], v[32:35]\n\t"
    "v_mul_f32_e32 v19, v19, v30\n\t"
    "v_cvt_pk_bf16_f32 v23, v18, v19\n\t"
    "v_mfma_f32_16x16x32_bf16 v[24:27], a[240:243], v[172:175], 0\n\t"
    "v_mul_f32_e32 v44, v44, v30\n\t"
    "v_mul_f32_e32 v45, v45, v30\n\t"
    "v_mfma_f32_16x16x32_bf16 v[24:27], a[248:251], v[168:171], v[24:27]\n\t"
    "v_cvt_pk_bf16_f32 v40, v44, v45\n\t"
    "v_mul_f32_e32 v46, v46, v30\n\t"
    "v_mfma_f32_16x16x32_bf16 v[24:27], v[224:227], v[164:167], v[24:27]\n\t"
    "v_mul_f32_e32 v47, v47, v30\n\t"
    "v_cvt_pk_bf16_f32 v41, v46, v47\n\t"
    "v_mfma_f32_16x16x32_bf16 v[24:27], v[232:235], v[160:163], v[24:27]\n\t"
    "v_cvt_pk_bf16_f32 v38, v32, v33\n\t"
    "v_cvt_pk_bf16_f32 v39, v34, v35\n\t"
    "v_mfma_f32_16x16x32_bf16 v[24:27], v[240:243], v[156:159], v[24:27]\n\t"
    "v_mul_f32_e32 v52, v52, v30\n\t"
    "v_mul_f32_e32 v53, v53, v30\n\t"
    "v_mfma_f32_16x16x32_bf16 v[24:27], v[248:251], v[152:155], v[24:27]\n\t"
    "v_cvt_pk_bf16_f32 v42, v52, v53\n\t"
    "v_mul_f32_e32 v54, v54, v30\n\t"
    "v_mfma_f32_16x16x32_bf16 v[16:19], v[252:255], v[152:155], 0\n\t"
    "v_mul_f32_e32 v55, v55, v30\n\t"
    "v_cvt_pk_bf16_f32 v43, v54, v55\n\t"
    "v_mfma_f32_16x16x32_bf16 v[16:19], v[244:247], v[156:159], v[16:19]\n\t"
    "v_mfma_f32_16x16x32_bf16 v[16:19], v[236:239], v[160:163], v[16:19]\n\t"
    "v_mfma_f32_16x16x32_bf16 v[16:19], v[228:231], v[164:167], v[16:19]\n\t"
    "v_cvt_pk_bf16_f32 v28, v24, v25\n\t"
    "v_cvt_pk_bf16_f32 v29, v26, v27\n\t"
    "v_mfma_f32_16x16x32_bf16 v[16:19], a[252:255], v[168:171], v[16:19]\n\t"
    "v_mfma_f32_16x16x32_bf16 v[16:19], a[244:247], v[172:175], v[16:19]\n\t"
    "v_mfma_f32_16x16x32_bf16 v[24:27], a[0:3], v[148:151], 0\n\t"
    "ds_write_b128 v6, v[20:23] offset:2048\n\t"
    "v_mfma_f32_16x16x32_bf16 v[24:27], a[8:11], v[144:147], v[24:27]\n\t"
    "v_mfma_f32_16x16x32_bf16 v[24:27], a[16:19], v[140:143], v[24:27]\n\t"
    "s_add_u32 m0, s62, 0\n\t"
    "v_mfma_f32_16x16x32_bf16 v[24:27], a[24:27], v[136:139], v[24:27]\n\t"
    "buffer_load_dwordx4 v7, s[52:55], s61 offen offset:0 nt lds\n\t"
    "v_cvt_pk_bf16_f32 v30, v16, v17\n\t"
    "v_mfma_f32_16x16x32_bf16 v[24:27], a[32:35], v[132:135], v[24:27]\n\t"
    "ds_write_b128 v6, v[40:43] offset:3072\n\t"
    "v_mfma_f32_16x16x32_bf16 v[24:27], a[40:43], v[128:131], v[24:27]\n\t"
    "buffer_load_dwordx4 v7, s[52:55], s61 offen offset:1024 nt lds\n\t"
    "v_cvt_pk_bf16_f32 v31, v18, v19\n\t"
    "v_mfma_f32_16x16x32_bf16 v[56:59], v[208:211], v[12:15], v[56:59]\n\t"
    "v_mfma_f32_16x16x32_bf16 v[56:59], v[204:207], v[36:39], v[56:59]\n\t"
    "buffer_load_dwordx4 v7, s[52:55], s61 offen offset:2048 nt lds\n\t"
    "v_mfma_f32_16x16x32_bf16 v[56:59], v[200:203], v[28:31], v[56:59]\n\t"
    "s_add_u32 s61, s61, 196608\n\t"
    "v_mfma_f32_16x16x32_bf16 v[60:63], v[176:179], v[28:31], v[60:63]\n\t"
    "v_cvt_pk_bf16_f32 v40, v24, v25\n\t"
    "v_cvt_pk_bf16_f32 v41, v26, v27\n\t"
    "v_mfma_f32_16x16x32_bf16 v[60:63], v[180:183], v[36:39], v[60:63]\n\t"
    "v_mfma_f32_16x16x32_bf16 v[60:63], v[184:187], v[12:15], v[60:63]\n\t"
    "v_mfma_f32_16x16x32_bf16 v[64:67], v[160:163], v[12:15], v[64:67]\n\t"
    "v_max3_f32 v72, v56, v57, v58\n\t"
    "v_mfma_f32_16x16x32_bf16 v[64:67], v[156:159], v[36:39], v[64:67]\n\t"
    "v_mfma_f32_16x16x32_bf16 v[64:67], v[152:155], v[28:31], v[64:67]\n\t"
    "v_mfma_f32_16x16x32_bf16 v[68:71], v[128:131], v[28:31], v[68:71]\n\t"
    "v_max3_f32 v73, v59, v60, v61\n\t"
    "v_max3_f32 v72, v72, v62, v63\n\t"
    "v_mfma_f32_16x16x32_bf16 v[68:71], v[132:135], v[36:39], v[68:71]\n\t"
    "v_mfma_f32_16x16x32_bf16 v[68:71], v[136:139], v[12:15], v[68:71]\n\t"
    "v_mfma_f32_16x16x32_bf16 v[16:19], a[44:47], v[128:131], 0\n\t"
    "v_max3_f32 v73, v73, v64, v65\n\t"
    "v_max3_f32 v72, v72, v66, v67\n\t"
    "v_mfma_f32_16x16x32_bf16 v[16:19], a[36:39], v[132:135], v[16:19]\n\t"
    "v_mfma_f32_16x16x32_bf16 v[16:19], a[28:31], v[136:139], v[16:19]\n\t"
    "v_mfma_f32_16x16x32_bf16 v[16:19], a[20:23], v[140:143], v[16:19]\n\t"
    "v_max3_f32 v73, v73, v68, v69\n\t"
    "v_max3_f32 v72, v72, v70, v71\n\t"
    "v_mfma_f32_16x16x32_bf16 v[16:19], a[12:15], v[144:147], v[16:19]\n\t"
    "v_max_f32_e32 v72, v72, v73\n\t"
    "v_mov_b32_e32 v73, v72\n\t"
    "v_mfma_f32_16x16x32_bf16 v[16:19], a[4:7], v[148:151], v[16:19]\n\t"
    "v_mfma_f32_16x16x32_bf16 v[12:15], a[48:51], v[148:151], 0\n\t"
    "v_permlane32_swap_b32 v72, v73\n\t"
    "v_max_f32_e32 v72, v72, v73\n\t"
    "v_mfma_f32_16x16x32_bf16 v[12:15], a[56:59], v[144:147], v[12:15]\n\t"
    "v_mov_b32_e32 v73, v72\n\t"
    "v_mfma_f32_16x16x32_bf16 v[12:15], a[64:67], v[140:143], v[12:15]\n\t"
    "v_mfma_f32_16x16x32_bf16 v[12:15], a[72:75], v[136:139], v[12:15]\n\t"
    "v_cvt_pk_bf16_f32 v42, v16, v17\n\t"
    "v_cvt_pk_bf16_f32 v43, v18, v19\n\t"
    "v_mfma_f32_16x16x32_bf16 v[12:15], a[80:83], v[132:135], v[12:15]\n\t"
    "v_permlane16_swap_b32 v72, v73\n\t"
    "v_max_f32_e32 v72, v72, v73\n\t"
    "v_mfma_f32_16x16x32_bf16 v[12:15], a[88:91], v[128:131], v[12:15]\n\t"
    "v_sub_f32_e32 v56, v56, v72\n\t"
    "v_sub_f32_e32 v57, v57, v72\n\t"
    "v_mfma_f32_16x16x32_bf16 v[16:19], a[92:95], v[128:131], 0\n\t"
    "v_exp_f32_e32 v56, v56\n\t"
    "v_mfma_f32_16x16x32_bf16 v[16:19], a[84:87], v[132:135], v[16:19]\n\t"
    "v_exp_f32_e32 v57, v57\n\t"
    "v_mfma_f32_16x16x32_bf16 v[16:19], a[76:79], v[136:139], v[16:19]\n\t"
    "v_add_f32_e32 v74, v56, v57\n\t"
    "v_sub_f32_e32 v58, v58, v72\n\t"
    "v_mfma_f32_16x16x32_bf16 v[16:19], a[68:71], v[140:143], v[16:19]\n\t"
    "v_cvt_pk_bf16_f32 v44, v12, v13\n\t"
    ".Lh6_%=:\n\t"
    "s_cbranch_execz .LrN_%=\n\t"
    "v_cvt_pk_bf16_f32 v45, v14, v15\n\t"
    "v_mfma_f32_16x16x32_bf16 v[16:19], a[60:63], v[144:147], v[16:19]\n\t"
    "v_exp_f32_e32 v58, v58\n\t"
    "v_mfma_f32_16x16x32_bf16 v[16:19], a[52:55], v[148:151], v[16:19]\n\t"
    "v_sub_f32_e32 v59, v59, v72\n\t"
    "v_sub_f32_e32 v60, v60, v72\n\t"
    "v_mfma_f32_16x16x32_bf16 v[12:15], a[96:99], v[148:151], 0\n\t"
    "v_exp_f32_e32 v59, v59\n\t"
    "v_mfma_f32_16x16x32_bf16 v[12:15], a[104:107], v[144:147], v[12:15]\n\t"
    "v_add_f32_e32 v75, v58, v59\n\t"
    "v_sub_f32_e32 v61, v61, v72\n\t"
    "v_mfma_f32_16x16x32_bf16 v[12:15], a[112:115], v[140:143], v[12:15]\n\t"
    "v_exp_f32_e32 v60, v60\n\t"
    "v_mfma_f32_16x16x32_bf16 v[12:15], a[120:123], v[136:139], v[12:15]\n\t"
    "v_cvt_pk_bf16_f32 v46, v16, v17\n\t"
    "v_cvt_pk_bf16_f32 v47, v18, v19\n\t"
    "v_mfma_f32_16x16x32_bf16 v[12:15], a[128:131], v[132:135], v[12:15]\n\t"
    "v_add_f32_e32 v74, v74, v60\n\t"
    "v_sub_f32_e32 v62, v62, v72\n\t"
    "v_mfma_f32_16x16x32_bf16 v[12:15], a[136:139], v[128:131], v[12:15]\n\t"
    "v_exp_f32_e32 v61, v61\n\t"
    "v_mfma_f32_16x16x32_bf16 v[16:19], a[140:143], v[128:131], 0\n\t"
    "v_add_f32_e32 v74, v74, v61\n\t"
    "v_sub_f32_e32 v63, v63, v72\n\t"
    "v_mfma_f32_16x16x32_bf16 v[16:19], a[132:135], v[132:135], v[16:19]\n\t"
    "v_exp_f32_e32 v62, v62\n\t"
    "v_mfma_f32_16x16x32_bf16 v[16:19], a[124:127], v[136:139], v[16:19]\n\t"
    "v_add_f32_e32 v75, v75, v62\n\t"
    "v_sub_f32_e32 v64, v64, v72\n\t"
    "v_mfma_f32_16x16x32_bf16 v[16:19], a[116:119], v[140:143], v[16:19]\n\t"
    "v_cvt_pk_bf16_f32 v32, v12, v13\n\t"
    "v_cvt_pk_bf16_f32 v33, v14, v15\n\t"
    "v_mfma_f32_16x16x32_bf16 v[16:19], a[108:111], v[144:147], v[16:19]\n\t"
    "v_exp_f32_e32 v63, v63\n\t"
    "v_mfma_f32_16x16x32_bf16 v[16:19], a[100:103], v[148:151], v[16:19]\n\t"
    "v_add_f32_e32 v75, v75, v63\n\t"
    "v_sub_f32_e32 v65, v65, v72\n\t"
    "v_mfma_f32_16x16x32_bf16 v[48:51], a[144:147], v[148:151], 0\n\t"
    "v_exp_f32_e32 v64, v64\n\t"
    "v_mfma_f32_16x16x32_bf16 v[48:51], a[152:155], v[144:147], v[48:51]\n\t"
    "v_add_f32_e32 v74, v74, v64\n\t"
    "v_sub_f32_e32 v66, v66, v72\n\t"
    "s_branch .Lmid_%=\n\t"
    ".Ltail_%=:\n\t"
    "v_mbcnt_lo_u32_b32 v8, -1, 0\n\t"
    "v_mbcnt_hi_u32_b32 v8, -1, v8\n\t"
    "v_lshlrev_b32_e32 v8, 5, v8\n\t"
    "s_nop 7\n\t"
    "s_nop 1\n\t"
    "ds_write_b128 v6, v[44:47] offset:4096\n\t"
    "v_cvt_pk_bf16_f32 v62, v56, v57\n\t"
    "ds_write_b128 v6, v[52:55] offset:5120\n\t"
    "v_cvt_pk_bf16_f32 v63, v58, v59\n\t"
    "s_nop 1\n\t"
    "v_mfma_f32_16x16x32_bf16 v[12:15], v[208:211], v[36:39], v[12:15]\n\t"
    "s_nop 1\n\t"
    "v_mfma_f32_16x16x32_bf16 v[12:15], v[204:207], v[48:51], v[12:15]\n\t"
    "s_nop 1\n\t"
    "v_mfma_f32_16x16x32_bf16 v[12:15], v[200:203], v[60:63], v[12:15]\n\t"
    "s_nop 1\n\t"
    "v_mfma_f32_16x16x32_bf16 v[16:19], v[176:179], v[60:63], v[16:19]\n\t"
    "s_nop 1\n\t"
    "v_mfma_f32_16x16x32_bf16 v[16:19], v[180:183], v[48:51], v[16:19]\n\t"
    "s_waitcnt lgkmcnt(0)\n\t"
    "s_barrier\n\t"
    "s_nop 1\n\t"
    "v_mfma_f32_16x16x32_bf16 v[16:19], v[184:187], v[36:39], v[16:19]\n\t"
    "s_nop 1\n\t"
    "v_mfma_f32_16x16x32_bf16 v[20:23], v[160:163], v[36:39], v[20:23]\n\t"
    "v_max3_f32 v52, v12, v13, v14\n\t"
    "s_nop 1\n\t"
    "v_mfma_f32_16x16x32_bf16 v[20:23], v[156:159], v[48:51], v[20:23]\n\t"
    "s_nop 1\n\t"
    "v_mfma_f32_16x16x32_bf16 v[20:23], v[152:155], v[60:63], v[20:23]\n\t"
    "s_nop 1\n\t"
    "v_mfma_f32_16x16x32_bf16 v[24:27], v[128:131], v[60:63], v[24:27]\n\t"
    "v_max3_f32 v53, v15, v16, v17\n\t"
    "v_max3_f32 v52, v52, v18, v19\n\t"
    "s_nop 1\n\t"
    "v_mfma_f32_16x16x32_bf16 v[24:27], v[132:135], v[48:51], v[24:27]\n\t"
    "s_nop 1\n\t"
    "v_mfma_f32_16x16x32_bf16 v[24:27], v[136:139], v[36:39], v[24:27]\n\t"
    "s_nop 7\n\t"
    "s_nop 1\n\t"
    "v_max3_f32 v53, v53, v20, v21\n\t"
    "v_max3_f32 v52, v52, v22, v23\n\t"
    "v_max3_f32 v53, v53, v24, v25\n\t"
    "v_max3_f32 v52, v52, v26, v27\n\t"
    "v_max_f32_e32 v52, v52, v53\n\t"
    "v_mov_b32_e32 v53, v52\n\t"
    "s_nop 1\n\t"
    "v_permlane32_swap_b32 v52, v53\n\t"
    "v_max_f32_e32 v52, v52, v53\n\t"
    "v_mov_b32_e32 v53, v52\n\t"
    "s_nop 1\n\t"
    "v_permlane16_swap_b32 v52, v53\n\t"
    "v_max_f32_e32 v52, v52, v53\n\t"
    "v_sub_f32_e32 v12, v12, v52\n\t"
    "v_sub_f32_e32 v13, v13, v52\n\t"
    "v_sub_f32_e32 v14, v14, v52\n\t"
    "v_exp_f32_e32 v12, v12\n\t"
    "s_nop 0\n\t"
    "v_sub_f32_e32 v15, v15, v52\n\t"
    "v_exp_f32_e32 v13, v13\n\t"
    "s_nop 0\n\t"
    "v_add_f32_e32 v54, v12, v13\n\t"
    "v_sub_f32_e32 v16, v16, v52\n\t"
    "v_exp_f32_e32 v14, v14\n\t"
    "s_nop 0\n\t"
    "v_exp_f32_e32 v15, v15\n\t"
    "s_nop 0\n\t"
    "v_add_f32_e32 v55, v14, v15\n\t"
    "v_sub_f32_e32 v17, v17, v52\n\t"
    "v_exp_f32_e32 v16, v16\n\t"
    "s_nop 0\n\t"
    "v_add_f32_e32 v54, v54, v16\n\t"
    "v_sub_f32_e32 v18, v18, v52\n\t"
    "v_exp_f32_e32 v17, v17\n\t"
    "s_nop 0\n\t"
    "v_add_f32_e32 v54, v54, v17\n\t"
    "v_sub_f32_e32 v19, v19, v52\n\t"
    "v_exp_f32_e32 v18, v18\n\t"
    "s_nop 0\n\t"
    "v_add_f32_e32 v55, v55, v18\n\t"
    "v_sub_f32_e32 v20, v20, v52\n\t"
    "v_exp_f32_e32 v19, v19\n\t"
    "s_nop 0\n\t"
    "v_add_f32_e32 v55, v55, v19\n\t"
    "v_exp_f32_e32 v20, v20\n\t"
    "s_nop 0\n\t"
    "v_add_f32_e32 v54, v54, v20\n\t"
    "v_sub_f32_e32 v21, v21, v52\n\t"
    "v_sub_f32_e32 v22, v22, v52\n\t"
    "v_exp_f32_e32 v21, v21\n\t"
    "s_nop 0\n\t"
    "v_add_f32_e32 v54, v54, v21\n\t"
    "v_exp_f32_e32 v22, v22\n\t"
    "s_nop 0\n\t"
    "v_add_f32_e32 v55, v55, v22\n\t"
    "v_sub_f32_e32 v23, v23, v52\n\t"
    "v_sub_f32_e32 v24, v24, v52\n\t"
    "v_exp_f32_e32 v23, v23\n\t"
    "s_nop 0\n\t"
    "v_add_f32_e32 v55, v55, v23\n\t"
    "v_exp_f32_e32 v24, v24\n\t"
    "s_nop 0\n\t"
    "v_add_f32_e32 v54, v54, v24\n\t"
    "v_sub_f32_e32 v25, v25, v52\n\t"
    "v_sub_f32_e32 v26, v26, v52\n\t"
    "v_sub_f32_e32 v27, v27, v52\n\t"
    "v_exp_f32_e32 v25, v25\n\t"
    "s_nop 0\n\t"
    "v_add_f32_e32 v54, v54, v25\n\t"
    "v_exp_f32_e32 v26, v26\n\t"
    "s_nop 0\n\t"
    "v_add_f32_e32 v55, v55, v26\n\t"
    "v_exp_f32_e32 v27, v27\n\t"
    "s_nop 0\n\t"
    "v_add_f32_e32 v55, v55, v27\n\t"
    "v_add_f32_e32 v54, v54, v55\n\t"
    "v_mov_b32_e32 v55, v54\n\t"
    "s_nop 1\n\t"
    "v_permlane32_swap_b32 v54, v55\n\t"
    "v_add_f32_e32 v54, v54, v55\n\t"
    "v_mov_b32_e32 v55, v54\n\t"
    "s_nop 1\n\t"
    "v_permlane16_swap_b32 v54, v55\n\t"
    "v_add_f32_e32 v54, v54, v55\n\t"
    "v_rcp_f32_e32 v54, v54\n\t"
    "s_nop 0\n\t"
    "v_mul_f32_e32 v12, v12, v54\n\t"
    "v_mul_f32_e32 v13, v13, v54\n\t"
    "v_cvt_pk_bf16_f32 v60, v12, v13\n\t"
    "v_mul_f32_e32 v14, v14, v54\n\t"
    "v_mul_f32_e32 v15, v15, v54\n\t"
    "v_cvt_pk_bf16_f32 v61, v14, v15\n\t"
    "v_mul_f32_e32 v16, v16, v54\n\t"
    "v_mul_f32_e32 v17, v17, v54\n\t"
    "v_cvt_pk_bf16_f32 v62, v16, v17\n\t"
    "v_mul_f32_e32 v18, v18, v54\n\t"
    "v_mul_f32_e32 v19, v19, v54\n\t"
    "v_cvt_pk_bf16_f32 v63, v18, v19\n\t"
    "v_mul_f32_e32 v20, v20, v54\n\t"
    "v_mul_f32_e32 v21, v21, v54\n\t"
    "v_cvt_pk_bf16_f32 v44, v20, v21\n\t"
    "v_mul_f32_e32 v22, v22, v54\n\t"
    "v_mul_f32_e32 v23, v23, v54\n\t"
    "v_cvt_pk_bf16_f32 v45, v22, v23\n\t"
    "v_mul_f32_e32 v24, v24, v54\n\t"
    "v_mul_f32_e32 v25, v25, v54\n\t"
    "v_cvt_pk_bf16_f32 v46, v24, v25\n\t"
    "v_mul_f32_e32 v26, v26, v54\n\t"
    "v_mul_f32_e32 v27, v27, v54\n\t"
    "ds_write_b128 v6, v[60:63] offset:6144\n\t"
    "v_cvt_pk_bf16_f32 v47, v26, v27\n\t"
    "ds_write_b128 v6, v[44:47] offset:7168\n\t"
    "s_waitcnt lgkmcnt(0)\n\t"
    "s_barrier\n\t"
    "s_cmp_lg_u32 %[closer], 0\n\t"
    "s_cbranch_scc1 .Lx2_%=\n\t"
    "s_mov_b32 s60, 0x10000\n\t"
    "buffer_load_dwordx2 v[10:11], off, s[56:59], %[eoff] sc1\n\t"
    ".Lp3_%=:\n\t"
    "buffer_load_dwordx4 v[12:15], v8, s[56:59], %[aoff] offen sc1\n\t"
    "buffer_load_dwordx4 v[16:19], v8, s[56:59], %[aoff] offen offset:16 sc1\n\t"
    "s_waitcnt vmcnt(0)\n\t"
    "v_add_co_u32_e32 v20, vcc, 0x7f4a7c15, v10\n\t"
    "s_nop 1\n\t"
    "v_mov_b32_e32 v9, 0x9e3779b9\n\t"
    "v_addc_co_u32_e32 v21, vcc, v9, v11, vcc\n\t"
    "v_cmp_eq_u64_e32 vcc, v[12:13], v[20:21]\n\t"
    "v_cmp_eq_u64_e64 s[66:67], v[14:15], v[20:21]\n\t"
    "s_and_b64 vcc, vcc, s[66:67]\n\t"
    "v_cmp_eq_u64_e64 s[66:67], v[16:17], v[20:21]\n\t"
    "s_and_b64 vcc, vcc, s[66:67]\n\t"
    "v_cmp_eq_u64_e64 s[66:67], v[18:19], v[20:21]\n\t"
    "s_and_b64 vcc, vcc, s[66:67]\n\t"
    "s_cmp_eq_u64 vcc, exec\n\t"
    "s_cbranch_scc1 .Lk4_%=\n\t"
    "s_sleep 8\n\t"
    "s_sub_u32 s60, s60, 1\n\t"
    "s_cmp_lg_u32 s60, 0\n\t"
    "s_cbranch_scc1 .Lp3_%=\n\t"
    ".Lk4_%=:\n\t"
    "s_mov_b64 exec, 1\n\t"
    "buffer_store_dwordx2 v[20:21], off, s[56:59], %[eoff] sc1\n\t"
    "s_mov_b64 exec, -1\n\t"
    ".Lx2_%=:\n\t"
    "s_endpgm\n\t"
    ".Lend_%=:\n\t"
    :: "{v31}"(lane), "{s[36:43]}"(k8), "{s[44:47]}"(k4),
       [role] "s"(role), [pair] "s"(pair), [prod] "s"((fid - 144) >> 31), [closer] "s"(bid | wave), [fid] "s"(fid), [wb] "s"(wb),
       [rbeg] "s"(rbeg), [rend] "s"(rbeg + 3 * SLOT), [pl] "s"(ldsb + PB + pair * 8192), [amask] "s"((unsigned long long)pair),
       [tch] "s"(((bid >> 3) * 2 + pair) * 1152), [foff] "s"(F_OFF + 64 * fid), [aoff] "s"(A_OFF), [aown] "s"(A_OFF + 8 * bid), [eoff] "s"(E_OFF)
    : "memory", "scc", ACLOB, VCLOB, SCLOB);
}

extern "C" void kernel_launch(void* const* d_in, const int* in_sizes, int n_in,
                              void* d_out, int out_size, void* d_ws, size_t ws_size,
                              hipStream_t stream) {
  attn_kernel<<<256, 256, 0, stream>>>((const unsigned char*)d_in[0], (const unsigned char*)d_in[1], (const unsigned char*)d_in[2],
                                       (unsigned char*)d_ws, (unsigned char*)d_out
      );
}
```

```cpp
#include <hip/hip_runtime.h>
#include <hip/hip_bf16.h>
#include <stdint.h>

using u32x4 = __attribute__((ext_vector_type(4))) unsigned;
using u32x8 = __attribute__((ext_vector_type(8))) unsigned;

static constexpr int ROWB = 512 * 384;
static constexpr int SLOT = 24576;
static constexpr int PB = 6 * SLOT;
static constexpr int F_OFF = 160 * 1024, A_OFF = 148 * 1024, E_OFF = 152 * 1024;

#define R10(p, n) p #n "0", p #n "1", p #n "2", p #n "3", p #n "4", p #n "5", p #n "6", p #n "7", p #n "8", p #n "9"
#define R240(p) R10(p, 4), R10(p, 5), R10(p, 6), R10(p, 7), R10(p, 8), R10(p, 9), R10(p, 10), R10(p, 11), R10(p, 12), R10(p, 13), \
  R10(p, 14), R10(p, 15), R10(p, 16), R10(p, 17), R10(p, 18), R10(p, 19), R10(p, 20), R10(p, 21), R10(p, 22), R10(p, 23), R10(p, 24), \
  p "250", p "251", p "252", p "253", p "254", p "255"
#define ACLOB R10("a", ), R10("a", 1), R10("a", 2), R10("a", 3), R240("a")
#define VCLOB R10("v", ), R10("v", 1), R10("v", 2), "v30", "v32", "v33", "v34", "v35", "v36", "v37", "v38", "v39", R240("v")
#define SCLOB "s48", "s49", R10("s", 5), R10("s", 6), "s70", "s71", "s72", "s73", "vcc"

__global__ __launch_bounds__(256, 1) void attn_kernel(const unsigned char* __restrict__ x,
                                                      const unsigned char* __restrict__ wqkv,
                                                      const unsigned char* __restrict__ wo,
                                                      unsigned char* ws,
                                                      unsigned char* __restrict__ out
                                                      ) {
  __shared__ u32x4 lds[10240];
  u32x8 k8; u32x4 k4;
  asm volatile(
    "s_load_dwordx8 s[36:43], %2, 0\n\t"
    "s_load_dwordx2 s[44:45], %2, 32\n\t"
    : "={s[36:43]}"(k8), "={s[44:47]}"(k4) : "s"(__builtin_amdgcn_kernarg_segment_ptr()) : "memory", ACLOB);
  __builtin_amdgcn_sched_barrier(0);
  const unsigned lane = threadIdx.x & 63, wave = __builtin_amdgcn_readfirstlane(threadIdx.x >> 6);
  const unsigned role = wave >> 1, pair = wave & 1;
  const unsigned ldsb = (unsigned)(size_t)lds;
  const unsigned bid = blockIdx.x;
  const unsigned wb = (bid & 7) * 8 * ROWB + ((bid >> 3) * 2 + pair) * 8 * 384;
  const unsigned rbeg = ldsb + pair * 3 * SLOT;
  const unsigned second = (143 - bid) >> 31;
  const unsigned fid = ((bid - 144 * second) ^ (4 * second)) + pair * (40 + ((bid - 216) >> 31) * 256);

  asm volatile(
    ".Lbeg_%=:\n\t"
    "s_cmp_lg_u32 %[role], 1\n\t"
    "s_cbranch_scc1 .Lroln_%=\n\t"
    "s_cmp_eq_u32 %[prod], 0\n\t"
    "s_cbranch_scc1 .Lnp_%=\n\t"
    "s_cmp_lt_u32 %[fid], 72\n\t"
    "s_cselect_b32 s71, 0, 72\n\t"
    "s_cselect_b32 s63, 0, 768\n\t"
    "s_sub_u32 s64, %[fid], s71\n\t"
    "s_mul_i32 s65, s64, 0x1556\n\t"
    "s_lshr_b32 s65, s65, 16\n\t"
    "s_mul_i32 s69, s65, 12\n\t"
    "s_sub_u32 s69, s64, s69\n\t"
    "s_lshr_b32 s69, s69, 1\n\t"
    "s_and_b32 s70, s64, 1\n\t"
    "s_mul_i32 s60, s69, 36864\n\t"
    "s_add_u32 s60, s60, s63\n\t"
    "s_add_u32 s61, s60, 4608\n\t"
    "v_lshrrev_b32_e32 v20, 2, v31\n\t"
    "v_and_b32_e32 v21, 8, v20\n\t"
    "v_bfe_u32 v24, v31, 3, 2\n\t"
    "v_or_b32_e32 v21, v21, v24\n\t"
    "v_mul_u32_u24_e32 v21, 0x480, v21\n\t"
    "v_lshrrev_b32_e32 v24, 4, v31\n\t"
    "v_xor_b32_e32 v24, v24, v31\n\t"
    "v_and_b32_e32 v24, 7, v24\n\t"
    "v_lshl_or_b32 v24, v24, 4, v21\n\t"
    "v_xor_b32_e32 v25, 64, v24\n\t"
    "v_add_u32_e32 v25, 0x4800, v25\n\t"
    "v_mul_u32_u24_e32 v27, 0x180, v20\n\t"
    "v_and_b32_e32 v28, 3, v31\n\t"
    "v_lshl_or_b32 v27, v28, 4, v27\n\t"
    "s_lshl_b32 s62, s65, 3\n\t"
    "s_add_u32 s62, s62, s70\n\t"
    "s_mul_i32 s62, s62, 4608\n\t"
    "s_add_u32 s62, s62, 384\n\t"
    "s_lshl_b32 s63, s65, 6\n\t"
    "s_mov_b32 s50, 0x36000\n\t"
    "s_mov_b32 s51, 0x20000\n\t"
    "s_mov_b32 s54, 0x12000\n\t"
    "s_mov_b32 s55, 0x20000\n\t"
    "s_waitcnt lgkmcnt(0)\n\t"
    "s_mov_b32 s48, s38\n\t"
    "s_and_b32 s49, s39, 0xffff\n\t"
    "s_cmp_lg_u32 s71, 0\n\t"
    "s_cbranch_scc1 .Lpo_%=\n\t"
    "s_mov_b32 m0, 61440\n\t"
    "s_nop 0\n\t"
    "buffer_load_dwordx4 v24, s[48:51], s62 offen offset:0 lds\n\t"
    "s_mov_b32 m0, 62464\n\t"
    "s_nop 0\n\t"
    "buffer_load_dwordx4 v25, s[48:51], s62 offen offset:0 lds\n\t"
    "s_mov_b32 m0, 63360\n\t"
    "s_nop 0\n\t"
    "buffer_load_dwordx4 v24, s[48:51], s62 offen offset:128 lds\n\t"
    "s_mov_b32 m0, 64384\n\t"
    "s_nop 0\n\t"
    "buffer_load_dwordx4 v25, s[48:51], s62 offen offset:128 lds\n\t"
    "s_mov_b32 m0, 65280\n\t"
    "s_nop 0\n\t"
    "buffer_load_dwordx4 v24, s[48:51], s62 offen offset:256 lds\n\t"
    "s_mov_b32 m0, 66304\n\t"
    "s_nop 0\n\t"
    "buffer_load_dwordx4 v25, s[48:51], s62 offen offset:256 lds\n\t"
    "s_branch .Lpa_%=\n\t"
    ".Lpo_%=:\n\t"
    "s_mov_b32 s52, s40\n\t"
    "s_add_u32 m0, %[rbeg], 61440\n\t"
    "s_and_b32 s53, s41, 0xffff\n\t"
    "buffer_load_dwordx4 v27, s[52:55], s63 offen lds\n\t"
    "s_add_u32 m0, %[rbeg], 62464\n\t"
    "s_add_u32 s63, s63, 6144\n\t"
    "buffer_load_dwordx4 v27, s[52:55], s63 offen lds\n\t"
    "s_add_u32 m0, %[rbeg], 63488\n\t"
    "s_add_u32 s63, s63, 6144\n\t"
    "buffer_load_dwordx4 v27, s[52:55], s63 offen lds\n\t"
    "s_add_u32 m0, %[rbeg], 64512\n\t"
    "s_add_u32 s63, s63, 6144\n\t"
    "buffer_load_dwordx4 v27, s[52:55], s63 offen lds\n\t"
    "s_add_u32 m0, %[rbeg], 65536\n\t"
    "s_add_u32 s63, s63, 6144\n\t"
    "buffer_load_dwordx4 v27, s[52:55], s63 offen lds\n\t"
    "s_add_u32 m0, %[rbeg], 66560\n\t"
    "s_add_u32 s63, s63, 6144\n\t"
    "buffer_load_dwordx4 v27, s[52:55], s63 offen lds\n\t"
    "s_add_u32 m0, %[rbeg], 67584\n\t"
    "s_add_u32 s63, s63, 6144\n\t"
    "buffer_load_dwordx4 v27, s[52:55], s63 offen lds\n\t"
    "s_add_u32 m0, %[rbeg], 68608\n\t"
    "s_add_u32 s63, s63, 6144\n\t"
    "buffer_load_dwordx4 v27, s[52:55], s63 offen lds\n\t"
    "s_add_u32 m0, %[rbeg], 69632\n\t"
    "s_add_u32 s63, s63, 6144\n\t"
    "buffer_load_dwordx4 v27, s[52:55], s63 offen lds\n\t"
    "s_add_u32 m0, %[rbeg], 70656\n\t"
    "s_add_u32 s63, s63, 6144\n\t"
    "buffer_load_dwordx4 v27, s[52:55], s63 offen lds\n\t"
    "s_add_u32 m0, %[rbeg], 71680\n\t"
    "s_add_u32 s63, s63, 6144\n\t"
    "buffer_load_dwordx4 v27, s[52:55], s63 offen lds\n\t"
    "s_add_u32 m0, %[rbeg], 72704\n\t"
    "s_add_u32 s63, s63, 6144\n\t"
    "buffer_load_dwordx4 v27, s[52:55], s63 offen lds\n\t"
    ".Lpa_%=:\n\t"
    "s_add_u32 m0, %[rbeg], 49152\n\t"
    "s_nop 0\n\t"
    "buffer_load_dwordx4 v24, s[48:51], s60 offen offset:0 lds\n\t"
    "s_add_u32 m0, %[rbeg], 53120\n\t"
    "s_nop 0\n\t"
    "buffer_load_dwordx4 v24, s[48:51], s60 offen offset:128 lds\n\t"
    "s_add_u32 m0, %[rbeg], 57088\n\t"
    "s_nop 0\n\t"
    "buffer_load_dwordx4 v24, s[48:51], s60 offen offset:256 lds\n\t"
    "s_add_u32 m0, %[rbeg], 51200\n\t"
    "s_nop 0\n\t"
    "buffer_load_dwordx4 v25, s[48:51], s60 offen offset:0 lds\n\t"
    "s_add_u32 m0, %[rbeg], 55168\n\t"
    "s_nop 0\n\t"
    "buffer_load_dwordx4 v25, s[48:51], s60 offen offset:128 lds\n\t"
    "s_add_u32 m0, %[rbeg], 59136\n\t"
    "s_nop 0\n\t"
    "buffer_load_dwordx4 v25, s[48:51], s60 offen offset:256 lds\n\t"
    "s_add_u32 m0, %[rbeg], 50176\n\t"
    "s_nop 0\n\t"
    "buffer_load_dwordx4 v24, s[48:51], s61 offen offset:0 lds\n\t"
    "s_add_u32 m0, %[rbeg], 54144\n\t"
    "s_nop 0\n\t"
    "buffer_load_dwordx4 v24, s[48:51], s61 offen offset:128 lds\n\t"
    "s_add_u32 m0, %[rbeg], 58112\n\t"
    "s_nop 0\n\t"
    "buffer_load_dwordx4 v24, s[48:51], s61 offen offset:256 lds\n\t"
    "s_add_u32 m0, %[rbeg], 52224\n\t"
    "s_nop 0\n\t"
    "buffer_load_dwordx4 v25, s[48:51], s61 offen offset:0 lds\n\t"
    "s_add_u32 m0, %[rbeg], 56192\n\t"
    "s_nop 0\n\t"
    "buffer_load_dwordx4 v25, s[48:51], s61 offen offset:128 lds\n\t"
    "s_add_u32 m0, %[rbeg], 60160\n\t"
    "s_nop 0\n\t"
    "buffer_load_dwordx4 v25, s[48:51], s61 offen offset:256 lds\n\t"
    ".Lnp_%=:\n\t"
    "s_waitcnt lgkmcnt(0)\n\t"
    "s_memrealtime s[72:73]\n\t"
    "s_mov_b32 s56, s42\n\t"
    "s_and_b32 s57, s43, 0xffff\n\t"
    "s_mov_b32 s58, 0x40000\n\t"
    "s_mov_b32 s59, 0x20000\n\t"
    "buffer_load_dwordx2 v[22:23], off, s[56:59], %[eoff] sc1\n\t"
    "s_barrier\n\t"
    "v_lshlrev_b32_e32 v30, 4, v31\n\t"
    "v_and_b32_e32 v28, 7, v31\n\t"
    "v_bfe_u32 v27, v31, 3, 1\n\t"
    "v_lshrrev_b32_e32 v29, 4, v31\n\t"
    "v_add_u32_e32 v6, %[pl], v30\n\t"
    "v_mul_u32_u24_e32 v27, 0xc00, v27\n\t"
    "v_add_u32_e32 v27, %[rbeg], v27\n\t"
    "v_lshlrev_b32_e32 v26, 7, v31\n\t"
    "v_mad_u32_u24 v20, v28, 3, 0\n\t"
    "v_and_b32_e32 v21, 6, v20\n\t"
    "v_xor_b32_e32 v21, v21, v29\n\t"
    "v_lshl_add_u32 v20, v20, 7, v27\n\t"
    "v_lshl_add_u32 v0, v21, 4, v20\n\t"
    "v_xor_b32_e32 v1, 64, v0\n\t"
    "v_mad_u32_u24 v20, v28, 3, 1\n\t"
    "v_and_b32_e32 v21, 6, v20\n\t"
    "v_xor_b32_e32 v21, v21, v29\n\t"
    "v_lshl_add_u32 v20, v20, 7, v27\n\t"
    "v_lshl_add_u32 v2, v21, 4, v20\n\t"
    "v_xor_b32_e32 v3, 64, v2\n\t"
    "v_mad_u32_u24 v20, v28, 3, 2\n\t"
    "v_and_b32_e32 v21, 6, v20\n\t"
    "v_xor_b32_e32 v21, v21, v29\n\t"
    "v_lshl_add_u32 v20, v20, 7, v27\n\t"
    "v_lshl_add_u32 v4, v21, 4, v20\n\t"
    "v_xor_b32_e32 v5, 64, v4\n\t"
    "s_cmp_eq_u32 %[prod], 0\n\t"
    "s_cbranch_scc1 .Lnq_%=\n\t"
    "v_bfe_u32 v20, v31, 1, 3\n\t"
    "v_xor_b32_e32 v20, v20, v29\n\t"
    "v_lshlrev_b32_e32 v20, 4, v20\n\t"
    "v_lshl_or_b32 v20, v28, 7, v20\n\t"
    "v_bfe_u32 v21, v31, 3, 1\n\t"
    "v_lshl_or_b32 v16, v21, 11, v20\n\t"
    "v_add_u32_e32 v16, 0xc000, v16\n\t"
    "v_add_u32_e32 v16, %[rbeg], v16\n\t"
    "v_xor_b32_e32 v17, 64, v16\n\t"
    "s_cmp_lg_u32 s71, 0\n\t"
    "s_cbranch_scc1 .Lqo_%=\n\t"
    "v_lshl_or_b32 v18, v21, 10, v20\n\t"
    "v_add_u32_e32 v18, 0xf000, v18\n\t"
    "v_xor_b32_e32 v19, 64, v18\n\t"
    "s_mul_i32 s60, s70, 6\n\t"
    "s_add_u32 s60, s60, s65\n\t"
    "s_mul_i32 s60, s60, 6\n\t"
    "s_add_u32 s60, s60, s69\n\t"
    "s_lshl_b32 s60, s60, 10\n\t"
    "v_add_u32_e32 v20, s60, v30\n\t"
    "v_mov_b32_e32 v24, v30\n\t"
    "s_waitcnt vmcnt(13)\n\t"
    "ds_read_b128 v[80:83], v18 offset:0\n\t"
    "ds_read_b128 v[84:87], v19 offset:0\n\t"
    "ds_read_b128 v[88:91], v18 offset:2048\n\t"
    "ds_read_b128 v[92:95], v19 offset:2048\n\t"
    "ds_read_b128 v[96:99], v18 offset:4096\n\t"
    "ds_read_b128 v[100:103], v19 offset:4096\n\t"
    "s_branch .Lqa_%=\n\t"
    ".Lqo_%=:\n\t"
    "s_and_b32 s63, s65, 1\n\t"
    "s_lshl_b32 s63, s63, 1\n\t"
    "s_add_u32 s63, s63, s70\n\t"
    "s_lshl_b32 s64, s70, 2\n\t"
    "s_lshl_b32 s62, s70, 4\n\t"
    "s_cmp_lt_u32 s65, 4\n\t"
    "s_cselect_b32 s60, s64, 8\n\t"
    "s_cselect_b32 s63, s65, s63\n\t"
    "s_cselect_b32 s62, s64, s62\n\t"
    "s_cselect_b32 s61, 4, 3\n\t"
    "s_lshl_b32 s62, s62, 1\n\t"
    "s_add_u32 s62, s62, 61440\n\t"
    "s_add_u32 s62, s62, %[rbeg]\n\t"
    "s_mul_i32 s60, s60, 6\n\t"
    "s_add_u32 s60, s60, s69\n\t"
    "s_lshl_b32 s60, s60, 2\n\t"
    "s_add_u32 s60, s60, s63\n\t"
    "s_lshl_b32 s60, s60, 8\n\t"
    "s_add_u32 s60, s60, 73728\n\t"
    "v_and_b32_e32 v25, 3, v31\n\t"
    "v_bfe_u32 v21, v31, 2, 2\n\t"
    "v_lshlrev_b32_e32 v18, s61, v25\n\t"
    "v_lshl_add_u32 v18, v21, 6, v18\n\t"
    "v_lshl_add_u32 v18, v29, 9, v18\n\t"
    "v_add_u32_e32 v18, s62, v18\n\t"
    "v_lshl_add_u32 v24, v25, 2, v21\n\t"
    "v_lshl_add_u32 v24, v29, 4, v24\n\t"
    "v_lshlrev_b32_e32 v24, 4, v24\n\t"
    "v_mul_u32_u24_e32 v21, 0x1800, v21\n\t"
    "v_lshl_add_u32 v20, v25, 4, v21\n\t"
    "v_lshl_add_u32 v20, v29, 6, v20\n\t"
    "v_add_u32_e32 v20, s60, v20\n\t"
    "s_waitcnt vmcnt(13)\n\t"
    "ds_read_b64_tr_b16 v[80:81], v18 offset:0\n\t"
    "ds_read_b64_tr_b16 v[82:83], v18 offset:256\n\t"
    "ds_read_b64_tr_b16 v[84:85], v18 offset:2048\n\t"
    "ds_read_b64_tr_b16 v[86:87], v18 offset:2304\n\t"
    "ds_read_b64_tr_b16 v[88:89], v18 offset:4096\n\t"
    "ds_read_b64_tr_b16 v[90:91], v18 offset:4352\n\t"
    "ds_read_b64_tr_b16 v[92:93], v18 offset:6144\n\t"
    "ds_read_b64_tr_b16 v[94:95], v18 offset:6400\n\t"
    "ds_read_b64_tr_b16 v[96:97], v18 offset:8192\n\t"
    "ds_read_b64_tr_b16 v[98:99], v18 offset:8448\n\t"
    "ds_read_b64_tr_b16 v[100:101], v18 offset:10240\n\t"
    "ds_read_b64_tr_b16 v[102:103], v18 offset:10496\n\t"
    ".Lqa_%=:\n\t"
    "s_waitcnt vmcnt(7)\n\t"
    "ds_read_b128 v[32:35], v16 offset:0\n\t"
    "ds_read_b128 v[36:39], v17 offset:0\n\t"
    "ds_read_b128 v[40:43], v16 offset:4096\n\t"
    "ds_read_b128 v[44:47], v17 offset:4096\n\t"
    "ds_read_b128 v[48:51], v16 offset:8192\n\t"
    "ds_read_b128 v[52:55], v17 offset:8192\n\t"
    "s_waitcnt vmcnt(1)\n\t"
    "ds_read_b128 v[56:59], v16 offset:1024\n\t"
    "ds_read_b128 v[60:63], v17 offset:1024\n\t"
    "ds_read_b128 v[64:67], v16 offset:5120\n\t"
    "ds_read_b128 v[68:71], v17 offset:5120\n\t"
    "ds_read_b128 v[72:75], v16 offset:9216\n\t"
    "ds_read_b128 v[76:79], v17 offset:9216\n\t"
    "s_waitcnt lgkmcnt(11)\n\t"
    "v_mfma_f32_16x16x32_bf16 v[108:111], v[32:35], v[80:83], 0\n\t"
    "s_waitcnt lgkmcnt(10)\n\t"
    "v_mfma_f32_16x16x32_bf16 v[108:111], v[36:39], v[84:87], v[108:111]\n\t"
    "s_waitcnt lgkmcnt(9)\n\t"
    "v_mfma_f32_16x16x32_bf16 v[108:111], v[40:43], v[88:91], v[108:111]\n\t"
    "s_waitcnt lgkmcnt(8)\n\t"
    "v_mfma_f32_16x16x32_bf16 v[108:111], v[44:47], v[92:95], v[108:111]\n\t"
    "s_waitcnt lgkmcnt(7)\n\t"
    "v_mfma_f32_16x16x32_bf16 v[108:111], v[48:51], v[96:99], v[108:111]\n\t"
    "s_waitcnt lgkmcnt(6)\n\t"
    "v_mfma_f32_16x16x32_bf16 v[108:111], v[52:55], v[100:103], v[108:111]\n\t"
    "s_waitcnt lgkmcnt(5)\n\t"
    "v_mfma_f32_16x16x32_bf16 v[112:115], v[56:59], v[80:83], 0\n\t"
    "s_waitcnt lgkmcnt(4)\n\t"
    "v_mfma_f32_16x16x32_bf16 v[112:115], v[60:63], v[84:87], v[112:115]\n\t"
    "s_waitcnt lgkmcnt(3)\n\t"
    "v_mfma_f32_16x16x32_bf16 v[112:115], v[64:67], v[88:91], v[112:115]\n\t"
    "s_waitcnt lgkmcnt(2)\n\t"
    "v_mfma_f32_16x16x32_bf16 v[112:115], v[68:71], v[92:95], v[112:115]\n\t"
    "s_waitcnt lgkmcnt(1)\n\t"
    "v_mfma_f32_16x16x32_bf16 v[112:115], v[72:75], v[96:99], v[112:115]\n\t"
    "s_waitcnt lgkmcnt(0)\n\t"
    "v_mfma_f32_16x16x32_bf16 v[112:115], v[76:79], v[100:103], v[112:115]\n\t"
    "s_cmp_eq_u32 s71, 0\n\t"
    "s_cselect_b32 s60, 0x3dd53b95, 1.0\n\t"
    "v_mul_f32_e32 v108, s60, v108\n\t"
    "v_mul_f32_e32 v109, s60, v109\n\t"
    "v_mul_f32_e32 v110, s60, v110\n\t"
    "v_mul_f32_e32 v111, s60, v111\n\t"
    "s_nop 3\n\t"
    "v_mul_f32_e32 v112, s60, v112\n\t"
    "v_mul_f32_e32 v113, s60, v113\n\t"
    "v_mul_f32_e32 v114, s60, v114\n\t"
    "v_mul_f32_e32 v115, s60, v115\n\t"
    "v_cvt_pk_bf16_f32 v104, v108, v109\n\t"
    "v_cvt_pk_bf16_f32 v105, v110, v111\n\t"
    "s_nop 0\n\t"
    "v_cvt_pk_bf16_f32 v106, v112, v113\n\t"
    "v_cvt_pk_bf16_f32 v107, v114, v115\n\t"
    "v_add_u32_e32 v24, %[rbeg], v24\n\t"
    "v_add_u32_e32 v25, %[rbeg], v30\n\t"
    "ds_write_b128 v24, v[104:107] offset:49152\n\t"
    "ds_read_b128 v[104:107], v25 offset:49152\n\t"
    "s_waitcnt lgkmcnt(0)\n\t"
    "buffer_store_dwordx4 v[104:107], v20, s[56:59], 0 offen sc1\n\t"
    ".Lnq_%=:\n\t"
    "s_waitcnt vmcnt(0)\n\t"
    "v_add_co_u32_e32 v22, vcc, 0x7f4a7c15, v22\n\t"
    "v_mov_b32_e32 v16, 0x9e3779b9\n\t"
    "s_nop 0\n\t"
    "v_addc_co_u32_e32 v23, vcc, v16, v23, vcc\n\t"
    "s_mov_b32 exec_lo, %[prod]\n\t"
    "s_mov_b32 exec_hi, 0\n\t"
    "buffer_store_dwordx2 v[22:23], off, s[56:59], %[foff] sc1\n\t"
    "s_mov_b64 exec, -1\n\t"
    "v_lshrrev_b32_e32 v20, 5, v31\n\t"
    "v_mul_u32_u24_e32 v20, 0x30000, v20\n\t"
    "v_and_b32_e32 v21, 1, v29\n\t"
    "v_mul_u32_u24_e32 v21, 0x600, v21\n\t"
    "v_add_u32_e32 v20, v20, v21\n\t"
    "v_and_b32_e32 v21, 15, v31\n\t"
    "v_lshl_add_u32 v7, v21, 4, v20\n\t"
    "v_lshl_add_u32 v8, v21, 3, v20\n\t"
    "v_add_u32_e32 v8, 0x100, v8\n\t"
    "v_and_b32_e32 v20, 48, v30\n\t"
    "v_lshl_or_b32 v20, v29, 6, v20\n\t"
    "v_lshrrev_b32_e32 v21, 2, v21\n\t"
    "v_lshl_or_b32 v9, v21, 8, v20\n\t"
    "v_min_u32_e32 v21, 35, v31\n\t"
    "v_mul_u32_u24_e32 v21, 64, v21\n\t"
    "v_add_u32_e32 v21, 0x28000, v21\n\t"
    "s_mov_b32 s52, s44\n\t"
    "s_and_b32 s53, s45, 0xffff\n\t"
    "s_mov_b32 s54, 0x6000000\n\t"
    "s_mov_b32 s55, 0x20000\n\t"
    "s_mov_b64 exec, 0\n\t"
    "s_cmp_eq_u32 %[pair], 0\n\t"
    "s_cbranch_scc1 .Lh6_%=\n\t"
    "s_branch .Lh5_%=\n\t"
    ".LrN_%=:\n\t"
    "s_mov_b64 exec, -1\n\t"
    "s_cmp_eq_u32 %[pair], 0\n\t"
    "s_cbranch_scc1 .Lng_%=\n\t"
    "s_cmp_lg_u32 %[prod], 0\n\t"
    "s_cbranch_scc1 .Lns_%=\n\t"
    "s_sleep 1\n\t"
    ".Lpt_%=:\n\t"
    "s_memrealtime s[70:71]\n\t"
    "s_waitcnt lgkmcnt(0)\n\t"
    "s_sub_u32 s70, s70, s72\n\t"
    "s_cmp_lt_u32 s70, 175\n\t"
    "s_cbranch_scc1 .Lpt_%=\n\t"
    ".Lns_%=:\n\t"
    "s_mov_b32 s64, 0\n\t"
    "s_mov_b32 s65, 2304\n\t"
    "buffer_load_dwordx2 v[12:13], v21, s[56:59], s64 offen sc1\n\t"
    "buffer_load_dwordx2 v[14:15], v21, s[56:59], s65 offen sc1\n\t"
    "s_waitcnt vmcnt(0)\n\t"
    "s_mov_b32 s60, 0x10000\n\t"
    ".Lp5_%=:\n\t"
    "v_cmp_eq_u64_e32 vcc, v[12:13], v[22:23]\n\t"
    "v_cmp_eq_u64_e64 s[70:71], v[14:15], v[22:23]\n\t"
    "s_and_b64 vcc, vcc, s[70:71]\n\t"
    "s_cmp_eq_u64 vcc, exec\n\t"
    "s_cbranch_scc1 .Lk6_%=\n\t"
    "s_sleep 4\n\t"
    "buffer_load_dwordx2 v[12:13], v21, s[56:59], s64 offen sc1\n\t"
    "buffer_load_dwordx2 v[14:15], v21, s[56:59], s65 offen sc1\n\t"
    "s_sub_u32 s60, s60, 1\n\t"
    "s_cmp_lg_u32 s60, 0\n\t"
    "s_waitcnt vmcnt(0)\n\t"
    "s_cbranch_scc1 .Lp5_%=\n\t"
    ".Lk6_%=:\n\t"
    ".Lng_%=:\n\t"
    "s_mul_i32 s66, %[pair], 36864\n\t"
    "s_mul_i32 s62, %[pair], 73728\n\t"
    "s_add_u32 s62, s62, 36864\n\t"
    "v_add_u32_e32 v11, s62, v30\n\t"
    "s_mov_b32 s63, 73728\n\t"
    "s_barrier\n\t"
    "s_add_u32 s67, %[tch], 0\n\t"
    "s_mov_b64 exec, 0x1ff\n\t"
    "buffer_load_dword v10, v26, s[56:59], s67 offen sc1\n\t"
    "s_mov_b64 exec, -1\n\t"
    "buffer_load_dwordx4 v[32:35], v30, s[56:59], s66 offen offset:0 sc1\n\t"
    "buffer_load_dwordx4 v[36:39], v30, s[56:59], s66 offen offset:1024 sc1\n\t"
    "buffer_load_dwordx4 v[40:43], v30, s[56:59], s66 offen offset:2048 sc1\n\t"
    "buffer_load_dwordx4 v[44:47], v30, s[56:59], s66 offen offset:3072 sc1\n\t"
    "s_add_u32 s66, s66, 4096\n\t"
    "buffer_load_dwordx4 v[48:51], v30, s[56:59], s66 offen offset:0 sc1\n\t"
    "buffer_load_dwordx4 v[52:55], v30, s[56:59], s66 offen offset:1024 sc1\n\t"
    "buffer_load_dwordx4 v[56:59], v30, s[56:59], s66 offen offset:2048 sc1\n\t"
    "buffer_load_dwordx4 v[60:63], v30, s[56:59], s66 offen offset:3072 sc1\n\t"
    "s_add_u32 s66, s66, 4096\n\t"
    "buffer_load_dwordx4 v[64:67], v30, s[56:59], s66 offen offset:0 sc1\n\t"
    "buffer_load_dwordx4 v[68:71], v30, s[56:59], s66 offen offset:1024 sc1\n\t"
    "buffer_load_dwordx4 v[72:75], v30, s[56:59], s66 offen offset:2048 sc1\n\t"
    "buffer_load_dwordx4 v[76:79], v30, s[56:59], s66 offen offset:3072 sc1\n\t"
    "s_add_u32 s66, s66, 4096\n\t"
    "buffer_load_dwordx4 v[80:83], v30, s[56:59], s66 offen offset:0 sc1\n\t"
    "buffer_load_dwordx4 v[84:87], v30, s[56:59], s66 offen offset:1024 sc1\n\t"
    "buffer_load_dwordx4 v[88:91], v30, s[56:59], s66 offen offset:2048 sc1\n\t"
    "buffer_load_dwordx4 v[92:95], v30, s[56:59], s66 offen offset:3072 sc1\n\t"
    "s_add_u32 s66, s66, 4096\n\t"
    "buffer_load_dwordx4 v[96:99], v30, s[56:59], s66 offen offset:0 sc1\n\t"
    "buffer_load_dwordx4 v[100:103], v30, s[56:59], s66 offen offset:1024 sc1\n\t"
    "buffer_load_dwordx4 v[104:107], v30, s[56:59], s66 offen offset:2048 sc1\n\t"
    "buffer_load_dwordx4 v[108:111], v30, s[56:59], s66 offen offset:3072 sc1\n\t"
    "s_add_u32 s66, s66, 4096\n\t"
    "buffer_load_dwordx4 v[112:115], v30, s[56:59], s66 offen offset:0 sc1\n\t"
    "buffer_load_dwordx4 v[116:119], v30, s[56:59], s66 offen offset:1024 sc1\n\t"
    "buffer_load_dwordx4 v[120:123], v30, s[56:59], s66 offen offset:2048 sc1\n\t"
    "buffer_load_dwordx4 v[124:127], v30, s[56:59], s66 offen offset:3072 sc1\n\t"
    "s_add_u32 s66, s66, 4096\n\t"
    "s_mov_b32 s64, 4608\n\t"
    "s_mov_b32 s65, 6912\n\t"
    "buffer_load_dwordx2 v[12:13], v21, s[56:59], s64 offen sc1\n\t"
    "buffer_load_dwordx2 v[14:15], v21, s[56:59], s65 offen sc1\n\t"
    "s_waitcnt vmcnt(25)\n\t"
    "ds_write_b128 v11, v[32:35] offset:0\n\t"
    "buffer_load_dwordx4 v[128:131], v30, s[56:59], s66 offen offset:0 sc1\n\t"
    "s_waitcnt vmcnt(25)\n\t"
    "ds_write_b128 v11, v[36:39] offset:1024\n\t"
    "buffer_load_dwordx4 v[132:135], v30, s[56:59], s66 offen offset:1024 sc1\n\t"
    "s_waitcnt vmcnt(25)\n\t"
    "ds_write_b128 v11, v[40:43] offset:2048\n\t"
    "buffer_load_dwordx4 v[136:139], v30, s[56:59], s66 offen offset:2048 sc1\n\t"
    "s_waitcnt vmcnt(25)\n\t"
    "ds_write_b128 v11, v[44:47] offset:3072\n\t"
    "buffer_load_dwordx4 v[140:143], v30, s[56:59], s66 offen offset:3072 sc1\n\t"
    "s_add_u32 s66, s66, 4096\n\t"
    "s_waitcnt vmcnt(25)\n\t"
    "ds_write_b128 v11, v[48:51] offset:4096\n\t"
    "buffer_load_dwordx4 v[144:147], v30, s[56:59], s66 offen offset:0 sc1\n\t"
    "s_waitcnt vmcnt(25)\n\t"
    "ds_write_b128 v11, v[52:55] offset:5120\n\t"
    "s_waitcnt lgkmcnt(0)\n\t"
    "s_barrier\n\t"
    "buffer_load_dwordx4 v[148:151], v30, s[56:59], s66 offen offset:1024 sc1\n\t"
    "s_waitcnt vmcnt(25)\n\t"
    "ds_write_b128 v11, v[56:59] offset:6144\n\t"
    "buffer_load_dwordx4 v[152:155], v30, s[56:59], s66 offen offset:2048 sc1\n\t"
    "s_waitcnt vmcnt(25)\n\t"
    "ds_write_b128 v11, v[60:63] offset:7168\n\t"
    "buffer_load_dwordx4 v[156:159], v30, s[56:59], s66 offen offset:3072 sc1\n\t"
    "s_add_u32 s66, s66, 4096\n\t"
    "s_waitcnt vmcnt(25)\n\t"
    "ds_write_b128 v11, v[64:67] offset:8192\n\t"
    "buffer_load_dwordx4 v[160:163], v30, s[56:59], s66 offen offset:0 sc1\n\t"
    "s_waitcnt vmcnt(25)\n\t"
    "ds_write_b128 v11, v[68:71] offset:9216\n\t"
    "buffer_load_dwordx4 v[164:167], v30, s[56:59], s66 offen offset:1024 sc1\n\t"
    "s_waitcnt vmcnt(25)\n\t"
    "ds_write_b128 v11, v[72:75] offset:10240\n\t"
    "buffer_load_dwordx4 v[168:171], v30, s[56:59], s66 offen offset:2048 sc1\n\t"
    "s_waitcnt vmcnt(25)\n\t"
    "ds_write_b128 v11, v[76:79] offset:11264\n\t"
    "s_waitcnt lgkmcnt(0)\n\t"
    "s_barrier\n\t"
    "buffer_load_dwordx4 v[172:175], v30, s[56:59], s66 offen offset:3072 sc1\n\t"
    "s_add_u32 s66, s66, 4096\n\t"
    "s_waitcnt vmcnt(25)\n\t"
    "ds_write_b128 v11, v[80:83] offset:12288\n\t"
    "s_waitcnt vmcnt(24)\n\t"
    "ds_write_b128 v11, v[84:87] offset:13312\n\t"
    "s_waitcnt vmcnt(23)\n\t"
    "ds_write_b128 v11, v[88:91] offset:14336\n\t"
    "s_waitcnt vmcnt(22)\n\t"
    "ds_write_b128 v11, v[92:95] offset:15360\n\t"
    "s_waitcnt vmcnt(21)\n\t"
    "ds_write_b128 v11, v[96:99] offset:16384\n\t"
    "s_waitcnt vmcnt(20)\n\t"
    "ds_write_b128 v11, v[100:103] offset:17408\n\t"
    "s_waitcnt vmcnt(19)\n\t"
    "ds_write_b128 v11, v[104:107] offset:18432\n\t"
    "s_waitcnt vmcnt(18)\n\t"
    "ds_write_b128 v11, v[108:111] offset:19456\n\t"
    "s_waitcnt vmcnt(17)\n\t"
    "ds_write_b128 v11, v[112:115] offset:20480\n\t"
    "s_waitcnt vmcnt(16)\n\t"
    "ds_write_b128 v11, v[116:119] offset:21504\n\t"
    "s_waitcnt vmcnt(15)\n\t"
    "ds_write_b128 v11, v[120:123] offset:22528\n\t"
    "s_waitcnt vmcnt(14)\n\t"
    "ds_write_b128 v11, v[124:127] offset:23552\n\t"
    "s_waitcnt lgkmcnt(0)\n\t"
    "s_barrier\n\t"
    "s_waitcnt vmcnt(12)\n\t"
    "s_mov_b32 s60, 0x10000\n\t"
    ".Lp7_%=:\n\t"
    "v_cmp_eq_u64_e32 vcc, v[12:13], v[22:23]\n\t"
    "v_cmp_eq_u64_e64 s[70:71], v[14:15], v[22:23]\n\t"
    "s_and_b64 vcc, vcc, s[70:71]\n\t"
    "s_cmp_eq_u64 vcc, exec\n\t"
    "s_cbranch_scc1 .Lk8_%=\n\t"
    "s_sleep 4\n\t"
    "buffer_load_dwordx2 v[12:13], v21, s[56:59], s64 offen sc1\n\t"
    "buffer_load_dwordx2 v[14:15], v21, s[56:59], s65 offen sc1\n\t"
    "s_sub_u32 s60, s60, 1\n\t"
    "s_cmp_lg_u32 s60, 0\n\t"
    "s_waitcnt vmcnt(0)\n\t"
    "s_cbranch_scc1 .Lp7_%=\n\t"
    ".Lk8_%=:\n\t"
    "s_add_u32 s67, %[tch], 73728\n\t"
    "s_mov_b64 exec, 0x1ff\n\t"
    "buffer_load_dword v10, v26, s[56:59], s67 offen sc1\n\t"
    "s_mov_b64 exec, -1\n\t"
    "buffer_load_dwordx4 a[0:3], v9, s[56:59], s63 offen offset:0 sc1\n\t"
    "buffer_load_dwordx4 a[4:7], v9, s[56:59], s63 offen offset:1024 sc1\n\t"
    "buffer_load_dwordx4 a[8:11], v9, s[56:59], s63 offen offset:2048 sc1\n\t"
    "buffer_load_dwordx4 a[12:15], v9, s[56:59], s63 offen offset:3072 sc1\n\t"
    "s_add_u32 s63, s63, 4096\n\t"
    "s_waitcnt vmcnt(16)\n\t"
    "ds_write_b128 v11, v[128:131] offset:24576\n\t"
    "buffer_load_dwordx4 a[16:19], v9, s[56:59], s63 offen offset:0 sc1\n\t"
    "s_waitcnt vmcnt(16)\n\t"
    "ds_write_b128 v11, v[132:135] offset:25600\n\t"
    "buffer_load_dwordx4 a[20:23], v9, s[56:59], s63 offen offset:1024 sc1\n\t"
    "s_waitcnt vmcnt(16)\n\t"
    "ds_write_b128 v11, v[136:139] offset:26624\n\t"
    "buffer_load_dwordx4 a[24:27], v9, s[56:59], s63 offen offset:2048 sc1\n\t"
    "s_waitcnt vmcnt(16)\n\t"
    "ds_write_b128 v11, v[140:143] offset:27648\n\t"
    "buffer_load_dwordx4 a[28:31], v9, s[56:59], s63 offen offset:3072 sc1\n\t"
    "s_add_u32 s63, s63, 4096\n\t"
    "s_waitcnt vmcnt(16)\n\t"
    "ds_write_b128 v11, v[144:147] offset:28672\n\t"
    "buffer_load_dwordx4 a[32:35], v9, s[56:59], s63 offen offset:0 sc1\n\t"
    "s_waitcnt vmcnt(16)\n\t"
    "ds_write_b128 v11, v[148:151] offset:29696\n\t"
    "buffer_load_dwordx4 a[36:39], v9, s[56:59], s63 offen offset:1024 sc1\n\t"
    "s_waitcnt vmcnt(16)\n\t"
    "ds_write_b128 v11, v[152:155] offset:30720\n\t"
    "buffer_load_dwordx4 a[40:43], v9, s[56:59], s63 offen offset:2048 sc1\n\t"
    "s_waitcnt vmcnt(16)\n\t"
    "ds_write_b128 v11, v[156:159] offset:31744\n\t"
    "buffer_load_dwordx4 a[44:47], v9, s[56:59], s63 offen offset:3072 sc1\n\t"
    "s_add_u32 s63, s63, 4096\n\t"
    "s_waitcnt vmcnt(16)\n\t"
    "ds_write_b128 v11, v[160:163] offset:32768\n\t"
    "buffer_load_dwordx4 a[48:51], v9, s[56:59], s63 offen offset:0 sc1\n\t"
    "s_waitcnt vmcnt(16)\n\t"
    "ds_write_b128 v11, v[164:167] offset:33792\n\t"
    "buffer_load_dwordx4 a[52:55], v9, s[56:59], s63 offen offset:1024 sc1\n\t"
    "s_waitcnt vmcnt(16)\n\t"
    "ds_write_b128 v11, v[168:171] offset:34816\n\t"
    "buffer_load_dwordx4 a[56:59], v9, s[56:59], s63 offen offset:2048 sc1\n\t"
    "s_waitcnt vmcnt(16)\n\t"
    "ds_write_b128 v11, v[172:175] offset:35840\n\t"
    "s_waitcnt lgkmcnt(0)\n\t"
    "s_barrier\n\t"
    "buffer_load_dwordx4 a[60:63], v9, s[56:59], s63 offen offset:3072 sc1\n\t"
    "s_add_u32 s63, s63, 4096\n\t"
    "buffer_load_dwordx4 a[64:67], v9, s[56:59], s63 offen offset:0 sc1\n\t"
    "buffer_load_dwordx4 a[68:71], v9, s[56:59], s63 offen offset:1024 sc1\n\t"
    "buffer_load_dwordx4 a[72:75], v9, s[56:59], s63 offen offset:2048 sc1\n\t"
    "buffer_load_dwordx4 a[76:79], v9, s[56:59], s63 offen offset:3072 sc1\n\t"
    "s_add_u32 s63, s63, 4096\n\t"
    "buffer_load_dwordx4 a[80:83], v9, s[56:59], s63 offen offset:0 sc1\n\t"
    "buffer_load_dwordx4 a[84:87], v9, s[56:59], s63 offen offset:1024 sc1\n\t"
    "buffer_load_dwordx4 a[88:91], v9, s[56:59], s63 offen offset:2048 sc1\n\t"
    "buffer_load_dwordx4 a[92:95], v9, s[56:59], s63 offen offset:3072 sc1\n\t"
    "s_add_u32 s63, s63, 4096\n\t"
    "buffer_load_dwordx4 a[96:99], v9, s[56:59], s63 offen offset:0 sc1\n\t"
    "buffer_load_dwordx4 a[100:103], v9, s[56:59], s63 offen offset:1024 sc1\n\t"
    "buffer_load_dwordx4 a[104:107], v9, s[56:59], s63 offen offset:2048 sc1\n\t"
    "buffer_load_dwordx4 a[108:111], v9, s[56:59], s63 offen offset:3072 sc1\n\t"
    "s_add_u32 s63, s63, 4096\n\t"
    "buffer_load_dwordx4 a[112:115], v9, s[56:59], s63 offen offset:0 sc1\n\t"
    "buffer_load_dwordx4 a[116:119], v9, s[56:59], s63 offen offset:1024 sc1\n\t"
    "v_accvgpr_write_b32 a252, v22\n\t"
    "v_accvgpr_write_b32 a253, v23\n\t"
    "ds_read_b128 v[148:151], v0 offset:0\n\t"
    "ds_read_b128 v[156:159], v1 offset:0\n\t"
    "ds_read_b128 v[164:167], v2 offset:0\n\t"
    "ds_read_b128 v[172:175], v3 offset:0\n\t"
    "ds_read_b128 v[180:183], v4 offset:0\n\t"
    "ds_read_b128 v[184:187], v5 offset:0\n\t"
    "s_waitcnt lgkmcnt(0)\n\t"
    "s_add_u32 s61, %[wb], 0xff460000\n\t"
    "s_mov_b32 s62, %[rbeg]\n\t"
    "s_mov_b32 s60, 8\n\t"
    "s_waitcnt vmcnt(24)\n\t"
    "v_mfma_f32_16x16x32_bf16 v[204:207], v[148:151], a[0:3], 0\n\t"
    "buffer_load_dwordx4 a[120:123], v9, s[56:59], s63 offen offset:2048 sc1\n\t"
    "v_mfma_f32_16x16x32_bf16 v[204:207], v[156:159], a[4:7], v[204:207]\n\t"
    "v_mfma_f32_16x16x32_bf16 v[204:207], v[164:167], a[8:11], v[204:207]\n\t"
    "v_mfma_f32_16x16x32_bf16 v[204:207], v[172:175], a[12:15], v[204:207]\n\t"
    "buffer_load_dwordx4 a[124:127], v9, s[56:59], s63 offen offset:3072 sc1\n\t"
    "s_add_u32 s63, s63, 4096\n\t"
    "v_mfma_f32_16x16x32_bf16 v[204:207], v[180:183], a[16:19], v[204:207]\n\t"
    "v_mfma_f32_16x16x32_bf16 v[204:207], v[184:187], a[20:23], v[204:207]\n\t"
    "s_waitcnt vmcnt(20)\n\t"
    "v_mfma_f32_16x16x32_bf16 v[108:111], v[184:187], a[44:47], 0\n\t"
    "v_mfma_f32_16x16x32_bf16 v[108:111], v[180:183], a[40:43], v[108:111]\n\t"
    "v_mfma_f32_16x16x32_bf16 v[108:111], v[172:175], a[36:39], v[108:111]\n\t"
    "v_mfma_f32_16x16x32_bf16 v[108:111], v[164:167], a[32:35], v[108:111]\n\t"
    "buffer_load_dwordx4 a[128:131], v9, s[56:59], s63 offen offset:0 sc1\n\t"
    "v_mfma_f32_16x16x32_bf16 v[108:111], v[156:159], a[28:31], v[108:111]\n\t"
    "v_mfma_f32_16x16x32_bf16 v[108:111], v[148:151], a[24:27], v[108:111]\n\t"
    "s_barrier\n\t"
    "s_mov_b64 exec, %[amask]\n\t"
    "buffer_store_dwordx2 a[252:253], off, s[56:59], %[aown] sc1\n\t"
    "s_mov_b64 exec, -1\n\t"
    "s_waitcnt vmcnt(16)\n\t"
    "v_mfma_f32_16x16x32_bf16 v[188:191], v[148:151], a[48:51], 0\n\t"
    "v_mfma_f32_16x16x32_bf16 v[188:191], v[156:159], a[52:55], v[188:191]\n\t"
    "v_mfma_f32_16x16x32_bf16 v[188:191], v[164:167], a[56:59], v[188:191]\n\t"
    "v_mfma_f32_16x16x32_bf16 v[188:191], v[172:175], a[60:63], v[188:191]\n\t"
    ".Lh1_%=:\n\t"
    "s_cbranch_execz .LrM_%=\n\t"
    "buffer_load_dwordx4 a[132:135], v9, s[56:59], s63 offen offset:1024 sc1\n\t"
    "v_mfma_f32_16x16x32_bf16 v[188:191], v[180:183], a[64:67], v[188:191]\n\t"
    "v_mfma_f32_16x16x32_bf16 v[188:191], v[184:187], a[68:71], v[188:191]\n\t"
    "s_waitcnt vmcnt(11)\n\t"
    "v_mfma_f32_16x16x32_bf16 v[200:203], v[184:187], a[92:95], 0\n\t"
    "v_mfma_f32_16x16x32_bf16 v[200:203], v[180:183], a[88:91], v[200:203]\n\t"
    "v_mfma_f32_16x16x32_bf16 v[200:203], v[172:175], a[84:87], v[200:203]\n\t"
    "v_mfma_f32_16x16x32_bf16 v[200:203], v[164:167], a[80:83], v[200:203]\n\t"
    "buffer_load_dwordx4 a[136:139], v9, s[56:59], s63 offen offset:2048 sc1\n\t"
    "v_mfma_f32_16x16x32_bf16 v[200:203], v[156:159], a[76:79], v[200:203]\n\t"
    "v_mfma_f32_16x16x32_bf16 v[200:203], v[148:151], a[72:75], v[200:203]\n\t"
    "s_nop 7\n\t"
    "buffer_load_dwordx4 a[140:143], v9, s[56:59], s63 offen offset:3072 sc1\n\t"
    "s_add_u32 s63, s63, 4096\n\t"
    "buffer_load_dwordx4 a[144:147], v9, s[56:59], s63 offen offset:0 sc1\n\t"
    "buffer_load_dwordx4 a[148:151], v9, s[56:59], s63 offen offset:1024 sc1\n\t"
    "buffer_load_dwordx4 a[152:155], v9, s[56:59], s63 offen offset:2048 sc1\n\t"
    "buffer_load_dwordx4 a[156:159], v9, s[56:59], s63 offen offset:3072 sc1\n\t"
    "s_add_u32 s63, s63, 4096\n\t"
    "buffer_load_dwordx4 a[160:163], v9, s[56:59], s63 offen offset:0 sc1\n\t"
    "v_cvt_pk_bf16_f32 v12, v204, v205\n\t"
    "v_cvt_pk_bf16_f32 v13, v206, v207\n\t"
    "buffer_load_dwordx4 a[164:167], v9, s[56:59], s63 offen offset:1024 sc1\n\t"
    "buffer_load_dwordx4 a[168:171], v9, s[56:59], s63 offen offset:2048 sc1\n\t"
    "buffer_load_dwordx4 a[172:175], v9, s[56:59], s63 offen offset:3072 sc1\n\t"
    "s_add_u32 s63, s63, 4096\n\t"
    "v_cvt_pk_bf16_f32 v16, v108, v109\n\t"
    "v_cvt_pk_bf16_f32 v17, v110, v111\n\t"
    "buffer_load_dwordx4 a[176:179], v9, s[56:59], s63 offen offset:0 sc1\n\t"
    "v_cvt_pk_bf16_f32 v20, v188, v189\n\t"
    "v_cvt_pk_bf16_f32 v21, v190, v191\n\t"
    "buffer_load_dwordx4 a[180:183], v9, s[56:59], s63 offen offset:1024 sc1\n\t"
    "buffer_load_dwordx4 a[184:187], v9, s[56:59], s63 offen offset:2048 sc1\n\t"
    "buffer_load_dwordx4 a[188:191], v9, s[56:59], s63 offen offset:3072 sc1\n\t"
    "s_add_u32 s63, s63, 4096\n\t"
    "v_cvt_pk_bf16_f32 v24, v200, v201\n\t"
    "v_cvt_pk_bf16_f32 v25, v202, v203\n\t"
    "buffer_load_dwordx4 a[192:195], v9, s[56:59], s63 offen offset:0 sc1\n\t"
    "ds_read_b128 v[76:79], v0 offset:6144\n\t"
    "buffer_load_dwordx4 a[196:199], v9, s[56:59], s63 offen offset:1024 sc1\n\t"
    "ds_read_b128 v[80:83], v1 offset:6144\n\t"
    "buffer_load_dwordx4 a[200:203], v9, s[56:59], s63 offen offset:2048 sc1\n\t"
    "ds_read_b128 v[108:111], v2 offset:6144\n\t"
    "buffer_load_dwordx4 a[204:207], v9, s[56:59], s63 offen offset:3072 sc1\n\t"
    "s_add_u32 s63, s63, 4096\n\t"
    "ds_read_b128 v[84:87], v3 offset:6144\n\t"
    "buffer_load_dwordx4 a[208:211], v9, s[56:59], s63 offen offset:0 sc1\n\t"
    "ds_read_b128 v[60:63], v4 offset:6144\n\t"
    "buffer_load_dwordx4 a[212:215], v9, s[56:59], s63 offen offset:1024 sc1\n\t"
    "ds_read_b128 v[64:67], v5 offset:6144\n\t"
    "buffer_load_dwordx4 a[216:219], v9, s[56:59], s63 offen offset:2048 sc1\n\t"
    "buffer_load_dwordx4 a[220:223], v9, s[56:59], s63 offen offset:3072 sc1\n\t"
    "s_add_u32 s63, s63, 4096\n\t"
    "buffer_load_dwordx4 a[224:227], v9, s[56:59], s63 offen offset:0 sc1\n\t"
    "buffer_load_dwordx4 a[228:231], v9, s[56:59], s63 offen offset:1024 sc1\n\t"
    "buffer_load_dwordx4 a[232:235], v9, s[56:59], s63 offen offset:2048 sc1\n\t"
    "s_waitcnt lgkmcnt(2)\n\t"
    "v_mfma_f32_16x16x32_bf16 v[28:31], v[76:79], a[72:75], 0\n\t"
    "buffer_load_dwordx4 a[236:239], v9, s[56:59], s63 offen offset:3072 sc1\n\t"
    "s_add_u32 s63, s63, 4096\n\t"
    "v_mfma_f32_16x16x32_bf16 v[28:31], v[80:83], a[76:79], v[28:31]\n\t"
    "v_mfma_f32_16x16x32_bf16 v[28:31], v[108:111], a[80:83], v[28:31]\n\t"
    "buffer_load_dwordx4 a[240:243], v9, s[56:59], s63 offen offset:0 sc1\n\t"
    "v_mfma_f32_16x16x32_bf16 v[28:31], v[84:87], a[84:87], v[28:31]\n\t"
    "s_waitcnt lgkmcnt(0)\n\t"
    "v_mfma_f32_16x16x32_bf16 v[28:31], v[60:63], a[88:91], v[28:31]\n\t"
    "buffer_load_dwordx4 a[244:247], v9, s[56:59], s63 offen offset:1024 sc1\n\t"
    "v_mfma_f32_16x16x32_bf16 v[28:31], v[64:67], a[92:95], v[28:31]\n\t"
    "v_mfma_f32_16x16x32_bf16 v[32:35], v[64:67], a[68:71], 0\n\t"
    "buffer_load_dwordx4 a[248:251], v9, s[56:59], s63 offen offset:2048 sc1\n\t"
    "v_mfma_f32_16x16x32_bf16 v[32:35], v[60:63], a[64:67], v[32:35]\n\t"
    "v_mfma_f32_16x16x32_bf16 v[32:35], v[84:87], a[60:63], v[32:35]\n\t"
    "buffer_load_dwordx4 a[252:255], v9, s[56:59], s63 offen offset:3072 sc1\n\t"
    "s_add_u32 s63, s63, 4096\n\t"
    "v_mfma_f32_16x16x32_bf16 v[32:35], v[108:111], a[56:59], v[32:35]\n\t"
    "buffer_load_dwordx4 v[224:227], v9, s[56:59], s63 offen offset:0 sc1\n\t"
    "v_cvt_pk_bf16_f32 v26, v28, v29\n\t"
    "v_cvt_pk_bf16_f32 v27, v30, v31\n\t"
    "v_mfma_f32_16x16x32_bf16 v[32:35], v[80:83], a[52:55], v[32:35]\n\t"
    "v_mfma_f32_16x16x32_bf16 v[32:35], v[76:79], a[48:51], v[32:35]\n\t"
    "buffer_load_dwordx4 v[228:231], v9, s[56:59], s63 offen offset:1024 sc1\n\t"
    "v_mfma_f32_16x16x32_bf16 v[28:31], v[76:79], a[24:27], 0\n\t"
    "v_mfma_f32_16x16x32_bf16 v[28:31], v[80:83], a[28:31], v[28:31]\n\t"
    "buffer_load_dwordx4 v[232:235], v9, s[56:59], s63 offen offset:2048 sc1\n\t"
    "v_mfma_f32_16x16x32_bf16 v[28:31], v[108:111], a[32:35], v[28:31]\n\t"
    "v_mfma_f32_16x16x32_bf16 v[28:31], v[84:87], a[36:39], v[28:31]\n\t"
    "buffer_load_dwordx4 v[236:239], v9, s[56:59], s63 offen offset:3072 sc1\n\t"
    "s_add_u32 s63, s63, 4096\n\t"
    "v_cvt_pk_bf16_f32 v22, v32, v33\n\t"
    "v_cvt_pk_bf16_f32 v23, v34, v35\n\t"
    "v_mfma_f32_16x16x32_bf16 v[28:31], v[60:63], a[40:43], v[28:31]\n\t"
    "buffer_load_dwordx4 v[240:243], v9, s[56:59], s63 offen offset:0 sc1\n\t"
    "v_mfma_f32_16x16x32_bf16 v[28:31], v[64:67], a[44:47], v[28:31]\n\t"
    "v_mfma_f32_16x16x32_bf16 v[32:35], v[64:67], a[20:23], 0\n\t"
    "buffer_load_dwordx4 v[244:247], v9, s[56:59], s63 offen offset:1024 sc1\n\t"
    "v_mfma_f32_16x16x32_bf16 v[32:35], v[60:63], a[16:19], v[32:35]\n\t"
    "v_mfma_f32_16x16x32_bf16 v[32:35], v[84:87], a[12:15], v[32:35]\n\t"
    "buffer_load_dwordx4 v[248:251], v9, s[56:59], s63 offen offset:2048 sc1\n\t"
    "v_mfma_f32_16x16x32_bf16 v[32:35], v[108:111], a[8:11], v[32:35]\n\t"
    "v_cvt_pk_bf16_f32 v18, v28, v29\n\t"
    "v_cvt_pk_bf16_f32 v19, v30, v31\n\t"
    "v_mfma_f32_16x16x32_bf16 v[32:35], v[80:83], a[4:7], v[32:35]\n\t"
    "buffer_load_dwordx4 v[252:255], v9, s[56:59], s63 offen offset:3072 sc1\n\t"
    "s_add_u32 s63, s63, 4096\n\t"
    "v_mfma_f32_16x16x32_bf16 v[32:35], v[76:79], a[0:3], v[32:35]\n\t"
    ".Lmin_%=:\n\t"
    "s_waitcnt vmcnt(43)\n\t"
    "v_mfma_f32_16x16x32_bf16 v[36:39], v[76:79], a[96:99], 0\n\t"
    "v_mfma_f32_16x16x32_bf16 v[36:39], v[80:83], a[100:103], v[36:39]\n\t"
    "v_mfma_f32_16x16x32_bf16 v[36:39], v[108:111], a[104:107], v[36:39]\n\t"
    "buffer_store_dwordx2 v[140:141], v8, s[52:55], s61 offen offset:0 sc1\n\t"
    "v_mfma_f32_16x16x32_bf16 v[36:39], v[84:87], a[108:111], v[36:39]\n\t"
    "v_cvt_pk_bf16_f32 v14, v32, v33\n\t"
    "v_cvt_pk_bf16_f32 v15, v34, v35\n\t"
    "v_mfma_f32_16x16x32_bf16 v[36:39], v[60:63], a[112:115], v[36:39]\n\t"
    "v_mfma_f32_16x16x32_bf16 v[36:39], v[64:67], a[116:119], v[36:39]\n\t"
    "v_mfma_f32_16x16x32_bf16 v[32:35], v[184:187], a[116:119], 0\n\t"
    "v_mfma_f32_16x16x32_bf16 v[32:35], v[180:183], a[112:115], v[32:35]\n\t"
    "v_mfma_f32_16x16x32_bf16 v[32:35], v[172:175], a[108:111], v[32:35]\n\t"
    "v_mfma_f32_16x16x32_bf16 v[32:35], v[164:167], a[104:107], v[32:35]\n\t"
    "v_cvt_pk_bf16_f32 v30, v36, v37\n\t"
    "v_cvt_pk_bf16_f32 v31, v38, v39\n\t"
    "v_mfma_f32_16x16x32_bf16 v[32:35], v[156:159], a[100:103], v[32:35]\n\t"
    "v_mfma_f32_16x16x32_bf16 v[32:35], v[148:151], a[96:99], v[32:35]\n\t"
    "s_waitcnt vmcnt(37)\n\t"
    "v_mfma_f32_16x16x32_bf16 v[36:39], v[148:151], a[120:123], 0\n\t"
    "v_mfma_f32_16x16x32_bf16 v[36:39], v[156:159], a[124:127], v[36:39]\n\t"
    "v_mfma_f32_16x16x32_bf16 v[36:39], v[164:167], a[128:131], v[36:39]\n\t"
    "buffer_store_dwordx2 v[142:143], v8, s[52:55], s61 offen offset:384 sc1\n\t"
    "v_mfma_f32_16x16x32_bf16 v[36:39], v[172:175], a[132:135], v[36:39]\n\t"
    "v_cvt_pk_bf16_f32 v28, v32, v33\n\t"
    "v_cvt_pk_bf16_f32 v29, v34, v35\n\t"
    "v_mfma_f32_16x16x32_bf16 v[36:39], v[180:183], a[136:139], v[36:39]\n\t"
    "v_mfma_f32_16x16x32_bf16 v[36:39], v[184:187], a[140:143], v[36:39]\n\t"
    "v_mfma_f32_16x16x32_bf16 v[40:43], v[64:67], a[140:143], 0\n\t"
    "v_mfma_f32_16x16x32_bf16 v[40:43], v[60:63], a[136:139], v[40:43]\n\t"
    "v_mfma_f32_16x16x32_bf16 v[40:43], v[84:87], a[132:135], v[40:43]\n\t"
    "v_mfma_f32_16x16x32_bf16 v[40:43], v[108:111], a[128:131], v[40:43]\n\t"
    "v_cvt_pk_bf16_f32 v32, v36, v37\n\t"
    "v_cvt_pk_bf16_f32 v33, v38, v39\n\t"
    "v_mfma_f32_16x16x32_bf16 v[40:43], v[80:83], a[124:127], v[40:43]\n\t"
    "v_mfma_f32_16x16x32_bf16 v[40:43], v[76:79], a[120:123], v[40:43]\n\t"
    "s_waitcnt vmcnt(32)\n\t"
    "v_mfma_f32_16x16x32_bf16 v[44:47], v[76:79], a[144:147], 0\n\t"
    "v_mfma_f32_16x16x32_bf16 v[44:47], v[80:83], a[148:151], v[44:47]\n\t"
    "v_mfma_f32_16x16x32_bf16 v[44:47], v[108:111], a[152:155], v[44:47]\n\t"
    "buffer_store_dwordx2 v[144:145], v8, s[52:55], s61 offen offset:768 sc1\n\t"
    "v_mfma_f32_16x16x32_bf16 v[44:47], v[84:87], a[156:159], v[44:47]\n\t"
    "v_cvt_pk_bf16_f32 v34, v40, v41\n\t"
    "v_cvt_pk_bf16_f32 v35, v42, v43\n\t"
    "v_mfma_f32_16x16x32_bf16 v[44:47], v[60:63], a[160:163], v[44:47]\n\t"
    "v_mfma_f32_16x16x32_bf16 v[44:47], v[64:67], a[164:167], v[44:47]\n\t"
    "v_mfma_f32_16x16x32_bf16 v[40:43], v[184:187], a[164:167], 0\n\t"
    "v_mfma_f32_16x16x32_bf16 v[40:43], v[180:183], a[160:163], v[40:43]\n\t"
    "v_mfma_f32_16x16x32_bf16 v[40:43], v[172:175], a[156:159], v[40:43]\n\t"
    "v_mfma_f32_16x16x32_bf16 v[40:43], v[164:167], a[152:155], v[40:43]\n\t"
    "v_cvt_pk_bf16_f32 v38, v44, v45\n\t"
    "v_cvt_pk_bf16_f32 v39, v46, v47\n\t"
    "v_mfma_f32_16x16x32_bf16 v[40:43], v[156:159], a[148:151], v[40:43]\n\t"
    "v_mfma_f32_16x16x32_bf16 v[40:43], v[148:151], a[144:147], v[40:43]\n\t"
    "s_waitcnt vmcnt(27)\n\t"
    "v_mfma_f32_16x16x32_bf16 v[44:47], v[148:151], a[168:171], 0\n\t"
    "v_mfma_f32_16x16x32_bf16 v[44:47], v[156:159], a[172:175], v[44:47]\n\t"
    "v_mfma_f32_16x16x32_bf16 v[44:47], v[164:167], a[176:179], v[44:47]\n\t"
    "buffer_store_dwordx2 v[146:147], v8, s[52:55], s61 offen offset:1152 sc1\n\t"
    "v_mfma_f32_16x16x32_bf16 v[44:47], v[172:175], a[180:183], v[44:47]\n\t"
    "s_add_u32 s61, s61, 393216\n\t"
    "v_cvt_pk_bf16_f32 v36, v40, v41\n\t"
    "v_mfma_f32_16x16x32_bf16 v[44:47], v[180:183], a[184:187], v[44:47]\n\t"
    "v_cvt_pk_bf16_f32 v37, v42, v43\n\t"
    "v_mfma_f32_16x16x32_bf16 v[44:47], v[184:187], a[188:191], v[44:47]\n\t"
    "v_mfma_f32_16x16x32_bf16 v[48:51], v[64:67], a[188:191], 0\n\t"
    "v_mfma_f32_16x16x32_bf16 v[48:51], v[60:63], a[184:187], v[48:51]\n\t"
    "v_mfma_f32_16x16x32_bf16 v[48:51], v[84:87], a[180:183], v[48:51]\n\t"
    "v_mfma_f32_16x16x32_bf16 v[48:51], v[108:111], a[176:179], v[48:51]\n\t"
    "v_cvt_pk_bf16_f32 v40, v44, v45\n\t"
    "v_cvt_pk_bf16_f32 v41, v46, v47\n\t"
    "v_mfma_f32_16x16x32_bf16 v[48:51], v[80:83], a[172:175], v[48:51]\n\t"
    "v_mfma_f32_16x16x32_bf16 v[48:51], v[76:79], a[168:171], v[48:51]\n\t"
    "s_waitcnt vmcnt(22)\n\t"
    "v_mfma_f32_16x16x32_bf16 v[52:55], v[76:79], a[192:195], 0\n\t"
    "v_mfma_f32_16x16x32_bf16 v[52:55], v[80:83], a[196:199], v[52:55]\n\t"
    "v_mfma_f32_16x16x32_bf16 v[52:55], v[108:111], a[200:203], v[52:55]\n\t"
    "buffer_store_dwordx4 v[152:155], v7, s[52:55], s61 offen offset:0 sc1\n\t"
    "v_mfma_f32_16x16x32_bf16 v[52:55], v[84:87], a[204:207], v[52:55]\n\t"
    "v_cvt_pk_bf16_f32 v42, v48, v49\n\t"
    "v_cvt_pk_bf16_f32 v43, v50, v51\n\t"
    "v_mfma_f32_16x16x32_bf16 v[52:55], v[60:63], a[208:211], v[52:55]\n\t"
    "v_mfma_f32_16x16x32_bf16 v[52:55], v[64:67], a[212:215], v[52:55]\n\t"
    "v_mfma_f32_16x16x32_bf16 v[48:51], v[184:187], a[212:215], 0\n\t"
    "v_mfma_f32_16x16x32_bf16 v[48:51], v[180:183], a[208:211], v[48:51]\n\t"
    "v_mfma_f32_16x16x32_bf16 v[48:51], v[172:175], a[204:207], v[48:51]\n\t"
    "v_mfma_f32_16x16x32_bf16 v[48:51], v[164:167], a[200:203], v[48:51]\n\t"
    "v_cvt_pk_bf16_f32 v46, v52, v53\n\t"
    "v_cvt_pk_bf16_f32 v47, v54, v55\n\t"
    "v_mfma_f32_16x16x32_bf16 v[48:51], v[156:159], a[196:199], v[48:51]\n\t"
    "v_mfma_f32_16x16x32_bf16 v[48:51], v[148:151], a[192:195], v[48:51]\n\t"
    "s_waitcnt vmcnt(17)\n\t"
    "v_mfma_f32_16x16x32_bf16 v[52:55], v[148:151], a[216:219], 0\n\t"
    "v_mfma_f32_16x16x32_bf16 v[52:55], v[156:159], a[220:223], v[52:55]\n\t"
    "v_mfma_f32_16x16x32_bf16 v[52:55], v[164:167], a[224:227], v[52:55]\n\t"
    "buffer_store_dwordx4 v[160:163], v7, s[52:55], s61 offen offset:384 sc1\n\t"
    "v_mfma_f32_16x16x32_bf16 v[52:55], v[172:175], a[228:231], v[52:55]\n\t"
    "v_cvt_pk_bf16_f32 v44, v48, v49\n\t"
    "v_cvt_pk_bf16_f32 v45, v50, v51\n\t"
    "v_mfma_f32_16x16x32_bf16 v[52:55], v[180:183], a[232:235], v[52:55]\n\t"
    "v_mfma_f32_16x16x32_bf16 v[52:55], v[184:187], a[236:239], v[52:55]\n\t"
    "v_mfma_f32_16x16x32_bf16 v[56:59], v[64:67], a[236:239], 0\n\t"
    "v_mfma_f32_16x16x32_bf16 v[56:59], v[60:63], a[232:235], v[56:59]\n\t"
    "v_mfma_f32_16x16x32_bf16 v[56:59], v[84:87], a[228:231], v[56:59]\n\t"
    "v_mfma_f32_16x16x32_bf16 v[56:59], v[108:111], a[224:227], v[56:59]\n\t"
    "v_cvt_pk_bf16_f32 v48, v52, v53\n\t"
    "v_cvt_pk_bf16_f32 v49, v54, v55\n\t"
    "v_mfma_f32_16x16x32_bf16 v[56:59], v[80:83], a[220:223], v[56:59]\n\t"
    "v_mfma_f32_16x16x32_bf16 v[56:59], v[76:79], a[216:219], v[56:59]\n\t"
    "s_waitcnt vmcnt(12)\n\t"
    "v_mfma_f32_16x16x32_bf16 v[68:71], v[76:79], a[240:243], 0\n\t"
    "v_mfma_f32_16x16x32_bf16 v[68:71], v[80:83], a[244:247], v[68:71]\n\t"
    "v_mfma_f32_16x16x32_bf16 v[68:71], v[108:111], a[248:251], v[68:71]\n\t"
    "buffer_store_dwordx4 v[168:171], v7, s[52:55], s61 offen offset:768 sc1\n\t"
    "v_mfma_f32_16x16x32_bf16 v[68:71], v[84:87], a[252:255], v[68:71]\n\t"
    "v_cvt_pk_bf16_f32 v50, v56, v57\n\t"
    "v_cvt_pk_bf16_f32 v51, v58, v59\n\t"
    "v_mfma_f32_16x16x32_bf16 v[68:71], v[60:63], v[224:227], v[68:71]\n\t"
    "v_mfma_f32_16x16x32_bf16 v[68:71], v[64:67], v[228:231], v[68:71]\n\t"
    "v_mfma_f32_16x16x32_bf16 v[56:59], v[184:187], v[228:231], 0\n\t"
    "v_mfma_f32_16x16x32_bf16 v[56:59], v[180:183], v[224:227], v[56:59]\n\t"
    "ds_read_b128 v[116:119], v0 offset:12288\n\t"
    "v_mfma_f32_16x16x32_bf16 v[56:59], v[172:175], a[252:255], v[56:59]\n\t"
    "ds_read_b128 v[124:127], v1 offset:12288\n\t"
    "v_mfma_f32_16x16x32_bf16 v[56:59], v[164:167], a[248:251], v[56:59]\n\t"
    "ds_read_b128 v[128:131], v2 offset:12288\n\t"
    "v_cvt_pk_bf16_f32 v54, v68, v69\n\t"
    "v_mfma_f32_16x16x32_bf16 v[56:59], v[156:159], a[244:247], v[56:59]\n\t"
    "ds_read_b128 v[140:143], v3 offset:12288\n\t"
    "v_cvt_pk_bf16_f32 v55, v70, v71\n\t"
    "v_mfma_f32_16x16x32_bf16 v[56:59], v[148:151], a[240:243], v[56:59]\n\t"
    "ds_read_b128 v[144:147], v4 offset:12288\n\t"
    "s_waitcnt vmcnt(7)\n\t"
    "v_mfma_f32_16x16x32_bf16 v[72:75], v[148:151], v[232:235], 0\n\t"
    "ds_read_b128 v[104:107], v5 offset:12288\n\t"
    "v_mfma_f32_16x16x32_bf16 v[72:75], v[156:159], v[236:239], v[72:75]\n\t"
    "v_mfma_f32_16x16x32_bf16 v[72:75], v[164:167], v[240:243], v[72:75]\n\t"
    "ds_read_b128 v[152:155], v5 offset:18432\n\t"
    "buffer_store_dwordx4 v[176:179], v7, s[52:55], s61 offen offset:1152 sc1\n\t"
    "v_mfma_f32_16x16x32_bf16 v[72:75], v[172:175], v[244:247], v[72:75]\n\t"
    "ds_read_b128 v[148:151], v4 offset:18432\n\t"
    "v_cvt_pk_bf16_f32 v52, v56, v57\n\t"
    "v_mfma_f32_16x16x32_bf16 v[72:75], v[180:183], v[248:251], v[72:75]\n\t"
    "ds_read_b128 v[156:159], v3 offset:18432\n\t"
    "v_cvt_pk_bf16_f32 v53, v58, v59\n\t"
    "v_mfma_f32_16x16x32_bf16 v[72:75], v[184:187], v[252:255], v[72:75]\n\t"
    "ds_read_b128 v[160:163], v2 offset:18432\n\t"
    "v_mfma_f32_16x16x32_bf16 v[68:71], v[64:67], v[252:255], 0\n\t"
    "ds_read_b128 v[164:167], v1 offset:18432\n\t"
    "v_mfma_f32_16x16x32_bf16 v[68:71], v[60:63], v[248:251], v[68:71]\n\t"
    "ds_read_b128 v[168:171], v0 offset:18432\n\t"
    "v_mfma_f32_16x16x32_bf16 v[68:71], v[84:87], v[244:247], v[68:71]\n\t"
    "v_mfma_f32_16x16x32_bf16 v[68:71], v[108:111], v[240:243], v[68:71]\n\t"
    "v_cvt_pk_bf16_f32 v56, v72, v73\n\t"
    "v_cvt_pk_bf16_f32 v57, v74, v75\n\t"
    "v_mfma_f32_16x16x32_bf16 v[68:71], v[80:83], v[236:239], v[68:71]\n\t"
    "v_mfma_f32_16x16x32_bf16 v[68:71], v[76:79], v[232:235], v[68:71]\n\t"
    "s_waitcnt lgkmcnt(3)\n\t"
    "v_mfma_f32_16x16x32_bf16 v[64:67], v[116:119], a[0:3], 0\n\t"
    "v_mfma_f32_16x16x32_bf16 v[64:67], v[124:127], a[4:7], v[64:67]\n\t"
    "v_mfma_f32_16x16x32_bf16 v[64:67], v[128:131], a[8:11], v[64:67]\n\t"
    "buffer_store_dwordx2 v[136:137], v8, s[52:55], s61 offen offset:0 sc1\n\t"
    "v_mfma_f32_16x16x32_bf16 v[64:67], v[140:143], a[12:15], v[64:67]\n\t"
    "v_cvt_pk_bf16_f32 v58, v68, v69\n\t"
    "v_cvt_pk_bf16_f32 v59, v70, v71\n\t"
    "v_mfma_f32_16x16x32_bf16 v[64:67], v[144:147], a[16:19], v[64:67]\n\t"
    "v_mfma_f32_16x16x32_bf16 v[64:67], v[104:107], a[20:23], v[64:67]\n\t"
    "v_mfma_f32_16x16x32_bf16 v[68:71], v[152:155], a[20:23], 0\n\t"
    "v_mfma_f32_16x16x32_bf16 v[68:71], v[148:151], a[16:19], v[68:71]\n\t"
    "v_mfma_f32_16x16x32_bf16 v[68:71], v[156:159], a[12:15], v[68:71]\n\t"
    "s_waitcnt lgkmcnt(0)\n\t"
    "v_mfma_f32_16x16x32_bf16 v[68:71], v[160:163], a[8:11], v[68:71]\n\t"
    "v_cvt_pk_bf16_f32 v60, v64, v65\n\t"
    "v_cvt_pk_bf16_f32 v61, v66, v67\n\t"
    "v_mfma_f32_16x16x32_bf16 v[68:71], v[164:167], a[4:7], v[68:71]\n\t"
    "v_mfma_f32_16x16x32_bf16 v[68:71], v[168:171], a[0:3], v[68:71]\n\t"
    "v_mfma_f32_16x16x32_bf16 v[72:75], v[168:171], a[24:27], 0\n\t"
    "v_mfma_f32_16x16x32_bf16 v[72:75], v[164:167], a[28:31], v[72:75]\n\t"
    "v_mfma_f32_16x16x32_bf16 v[72:75], v[160:163], a[32:35], v[72:75]\n\t"
    "buffer_store_dwordx2 v[138:139], v8, s[52:55], s61 offen offset:384 sc1\n\t"
    "v_mfma_f32_16x16x32_bf16 v[72:75], v[156:159], a[36:39], v[72:75]\n\t"
    "v_cvt_pk_bf16_f32 v62, v68, v69\n\t"
    "v_cvt_pk_bf16_f32 v63, v70, v71\n\t"
    "v_mfma_f32_16x16x32_bf16 v[72:75], v[148:151], a[40:43], v[72:75]\n\t"
    "v_mfma_f32_16x16x32_bf16 v[72:75], v[152:155], a[44:47], v[72:75]\n\t"
    "v_mfma_f32_16x16x32_bf16 v[68:71], v[104:107], a[44:47], 0\n\t"
    "v_mfma_f32_16x16x32_bf16 v[68:71], v[144:147], a[40:43], v[68:71]\n\t"
    "v_mfma_f32_16x16x32_bf16 v[68:71], v[140:143], a[36:39], v[68:71]\n\t"
    "v_mfma_f32_16x16x32_bf16 v[68:71], v[128:131], a[32:35], v[68:71]\n\t"
    "v_cvt_pk_bf16_f32 v66, v72, v73\n\t"
    "v_cvt_pk_bf16_f32 v67, v74, v75\n\t"
    "v_mfma_f32_16x16x32_bf16 v[68:71], v[124:127], a[28:31], v[68:71]\n\t"
    "v_mfma_f32_16x16x32_bf16 v[68:71], v[116:119], a[24:27], v[68:71]\n\t"
    "v_mfma_f32_16x16x32_bf16 v[72:75], v[116:119], a[48:51], 0\n\t"
    "v_mfma_f32_16x16x32_bf16 v[72:75], v[124:127], a[52:55], v[72:75]\n\t"
    "v_mfma_f32_16x16x32_bf16 v[72:75], v[128:131], a[56:59], v[72:75]\n\t"
    "buffer_store_dwordx2 v[132:133], v8, s[52:55], s61 offen offset:768 sc1\n\t"
    "v_mfma_f32_16x16x32_bf16 v[72:75], v[140:143], a[60:63], v[72:75]\n\t"
    "v_cvt_pk_bf16_f32 v64, v68, v69\n\t"
    "v_cvt_pk_bf16_f32 v65, v70, v71\n\t"
    "v_mfma_f32_16x16x32_bf16 v[72:75], v[144:147], a[64:67], v[72:75]\n\t"
    "v_mfma_f32_16x16x32_bf16 v[72:75], v[104:107], a[68:71], v[72:75]\n\t"
    "v_mfma_f32_16x16x32_bf16 v[76:79], v[152:155], a[68:71], 0\n\t"
    "v_mfma_f32_16x16x32_bf16 v[76:79], v[148:151], a[64:67], v[76:79]\n\t"
    "v_mfma_f32_16x16x32_bf16 v[76:79], v[156:159], a[60:63], v[76:79]\n\t"
    "s_add_i32 s64, s62, 24576\n\t"
    "v_mfma_f32_16x16x32_bf16 v[76:79], v[160:163], a[56:59], v[76:79]\n\t"
    "s_cmp_eq_u32 s64, %[rend]\n\t"
    "s_cselect_b32 s64, %[rbeg], s64\n\t"
    "v_cvt_pk_bf16_f32 v69, v74, v75\n\t"
    "v_mfma_f32_16x16x32_bf16 v[76:79], v[164:167], a[52:55], v[76:79]\n\t"
    "s_sub_i32 s65, s64, s62\n\t"
    "s_mov_b32 s62, s64\n\t"
    "v_mfma_f32_16x16x32_bf16 v[76:79], v[168:171], a[48:51], v[76:79]\n\t"
    "v_add_u32_e32 v0, s65, v0\n\t"
    "v_add_u32_e32 v1, s65, v1\n\t"
    "v_mfma_f32_16x16x32_bf16 v[80:83], v[168:171], a[72:75], 0\n\t"
    "v_add_u32_e32 v2, s65, v2\n\t"
    "v_add_u32_e32 v3, s65, v3\n\t"
    "v_mfma_f32_16x16x32_bf16 v[80:83], v[164:167], a[76:79], v[80:83]\n\t"
    "v_add_u32_e32 v4, s65, v4\n\t"
    "v_add_u32_e32 v5, s65, v5\n\t"
    "v_mfma_f32_16x16x32_bf16 v[80:83], v[160:163], a[80:83], v[80:83]\n\t"
    "buffer_store_dwordx2 v[134:135], v8, s[52:55], s61 offen offset:1152 sc1\n\t"
    "v_mfma_f32_16x16x32_bf16 v[80:83], v[156:159], a[84:87], v[80:83]\n\t"
    "s_add_u32 s61, s61, 393216\n\t"
    "v_cvt_pk_bf16_f32 v68, v72, v73\n\t"
    "v_mfma_f32_16x16x32_bf16 v[80:83], v[148:151], a[88:91], v[80:83]\n\t"
    "v_cvt_pk_bf16_f32 v70, v76, v77\n\t"
    "v_cvt_pk_bf16_f32 v71, v78, v79\n\t"
    "v_mfma_f32_16x16x32_bf16 v[80:83], v[152:155], a[92:95], v[80:83]\n\t"
    "v_mfma_f32_16x16x32_bf16 v[84:87], v[104:107], a[92:95], 0\n\t"
    "v_mfma_f32_16x16x32_bf16 v[84:87], v[144:147], a[88:91], v[84:87]\n\t"
    "v_mfma_f32_16x16x32_bf16 v[84:87], v[140:143], a[84:87], v[84:87]\n\t"
    "v_mfma_f32_16x16x32_bf16 v[84:87], v[128:131], a[80:83], v[84:87]\n\t"
    "v_cvt_pk_bf16_f32 v74, v80, v81\n\t"
    "v_cvt_pk_bf16_f32 v75, v82, v83\n\t"
    "v_mfma_f32_16x16x32_bf16 v[84:87], v[124:127], a[76:79], v[84:87]\n\t"
    "v_mfma_f32_16x16x32_bf16 v[84:87], v[116:119], a[72:75], v[84:87]\n\t"
    "v_mfma_f32_16x16x32_bf16 v[80:83], v[116:119], a[96:99], 0\n\t"
    "v_mfma_f32_16x16x32_bf16 v[80:83], v[124:127], a[100:103], v[80:83]\n\t"
    "v_mfma_f32_16x16x32_bf16 v[80:83], v[128:131], a[104:107], v[80:83]\n\t"
    "buffer_store_dwordx4 v[216:219], v7, s[52:55], s61 offen offset:0 sc1\n\t"
    "v_mfma_f32_16x16x32_bf16 v[80:83], v[140:143], a[108:111], v[80:83]\n\t"
    "v_cvt_pk_bf16_f32 v72, v84, v85\n\t"
    "v_cvt_pk_bf16_f32 v73, v86, v87\n\t"
    "v_mfma_f32_16x16x32_bf16 v[80:83], v[144:147], a[112:115], v[80:83]\n\t"
    "v_mfma_f32_16x16x32_bf16 v[80:83], v[104:107], a[116:119], v[80:83]\n\t"
    "v_mfma_f32_16x16x32_bf16 v[84:87], v[152:155], a[116:119], 0\n\t"
    "v_mfma_f32_16x16x32_bf16 v[84:87], v[148:151], a[112:115], v[84:87]\n\t"
    "v_mfma_f32_16x16x32_bf16 v[84:87], v[156:159], a[108:111], v[84:87]\n\t"
    "v_mfma_f32_16x16x32_bf16 v[84:87], v[160:163], a[104:107], v[84:87]\n\t"
    "v_cvt_pk_bf16_f32 v76, v80, v81\n\t"
    "v_cvt_pk_bf16_f32 v77, v82, v83\n\t"
    "v_mfma_f32_16x16x32_bf16 v[84:87], v[164:167], a[100:103], v[84:87]\n\t"
    "v_mfma_f32_16x16x32_bf16 v[84:87], v[168:171], a[96:99], v[84:87]\n\t"
    "v_mfma_f32_16x16x32_bf16 v[88:91], v[168:171], a[120:123], 0\n\t"
    "v_mfma_f32_16x16x32_bf16 v[88:91], v[164:167], a[124:127], v[88:91]\n\t"
    "v_mfma_f32_16x16x32_bf16 v[88:91], v[160:163], a[128:131], v[88:91]\n\t"
    "buffer_store_dwordx4 v[212:215], v7, s[52:55], s61 offen offset:384 sc1\n\t"
    "v_mfma_f32_16x16x32_bf16 v[88:91], v[156:159], a[132:135], v[88:91]\n\t"
    "v_cvt_pk_bf16_f32 v78, v84, v85\n\t"
    "v_cvt_pk_bf16_f32 v79, v86, v87\n\t"
    "v_mfma_f32_16x16x32_bf16 v[88:91], v[148:151], a[136:139], v[88:91]\n\t"
    "v_mfma_f32_16x16x32_bf16 v[88:91], v[152:155], a[140:143], v[88:91]\n\t"
    "v_mfma_f32_16x16x32_bf16 v[84:87], v[104:107], a[140:143], 0\n\t"
    "v_mfma_f32_16x16x32_bf16 v[84:87], v[144:147], a[136:139], v[84:87]\n\t"
    "v_mfma_f32_16x16x32_bf16 v[84:87], v[140:143], a[132:135], v[84:87]\n\t"
    "v_mfma_f32_16x16x32_bf16 v[84:87], v[128:131], a[128:131], v[84:87]\n\t"
    "v_cvt_pk_bf16_f32 v82, v88, v89\n\t"
    "v_cvt_pk_bf16_f32 v83, v90, v91\n\t"
    "v_mfma_f32_16x16x32_bf16 v[84:87], v[124:127], a[124:127], v[84:87]\n\t"
    "v_mfma_f32_16x16x32_bf16 v[84:87], v[116:119], a[120:123], v[84:87]\n\t"
    "v_mfma_f32_16x16x32_bf16 v[88:91], v[116:119], a[144:147], 0\n\t"
    "v_mfma_f32_16x16x32_bf16 v[88:91], v[124:127], a[148:151], v[88:91]\n\t"
    "v_mfma_f32_16x16x32_bf16 v[88:91], v[128:131], a[152:155], v[88:91]\n\t"
    "buffer_store_dwordx4 v[204:207], v7, s[52:55], s61 offen offset:768 sc1\n\t"
    "v_mfma_f32_16x16x32_bf16 v[88:91], v[140:143], a[156:159], v[88:91]\n\t"
    "v_cvt_pk_bf16_f32 v80, v84, v85\n\t"
    "v_cvt_pk_bf16_f32 v81, v86, v87\n\t"
    "v_mfma_f32_16x16x32_bf16 v[88:91], v[144:147], a[160:163], v[88:91]\n\t"
    "v_mfma_f32_16x16x32_bf16 v[88:91], v[104:107], a[164:167], v[88:91]\n\t"
    "v_mfma_f32_16x16x32_bf16 v[92:95], v[152:155], a[164:167], 0\n\t"
    "v_mfma_f32_16x16x32_bf16 v[92:95], v[148:151], a[160:163], v[92:95]\n\t"
    "v_mfma_f32_16x16x32_bf16 v[92:95], v[156:159], a[156:159], v[92:95]\n\t"
    "v_mfma_f32_16x16x32_bf16 v[92:95], v[160:163], a[152:155], v[92:95]\n\t"
    "v_cvt_pk_bf16_f32 v84, v88, v89\n\t"
    "v_cvt_pk_bf16_f32 v85, v90, v91\n\t"
    "v_mfma_f32_16x16x32_bf16 v[92:95], v[164:167], a[148:151], v[92:95]\n\t"
    "v_mfma_f32_16x16x32_bf16 v[92:95], v[168:171], a[144:147], v[92:95]\n\t"
    "v_mfma_f32_16x16x32_bf16 v[100:103], v[168:171], a[168:171], 0\n\t"
    "v_mfma_f32_16x16x32_bf16 v[100:103], v[164:167], a[172:175], v[100:103]\n\t"
    "v_mfma_f32_16x16x32_bf16 v[100:103], v[160:163], a[176:179], v[100:103]\n\t"
    "buffer_store_dwordx4 v[120:123], v7, s[52:55], s61 offen offset:1152 sc1\n\t"
    "v_mfma_f32_16x16x32_bf16 v[100:103], v[156:159], a[180:183], v[100:103]\n\t"
    "v_cvt_pk_bf16_f32 v86, v92, v93\n\t"
    "v_cvt_pk_bf16_f32 v87, v94, v95\n\t"
    "v_mfma_f32_16x16x32_bf16 v[100:103], v[148:151], a[184:187], v[100:103]\n\t"
    "v_mfma_f32_16x16x32_bf16 v[100:103], v[152:155], a[188:191], v[100:103]\n\t"
    "v_mfma_f32_16x16x32_bf16 v[92:95], v[104:107], a[188:191], 0\n\t"
    "v_mfma_f32_16x16x32_bf16 v[92:95], v[144:147], a[184:187], v[92:95]\n\t"
    "v_mfma_f32_16x16x32_bf16 v[92:95], v[140:143], a[180:183], v[92:95]\n\t"
    "v_mfma_f32_16x16x32_bf16 v[92:95], v[128:131], a[176:179], v[92:95]\n\t"
    "v_cvt_pk_bf16_f32 v90, v100, v101\n\t"
    "v_cvt_pk_bf16_f32 v91, v102, v103\n\t"
    "v_mfma_f32_16x16x32_bf16 v[92:95], v[124:127], a[172:175], v[92:95]\n\t"
    "v_mfma_f32_16x16x32_bf16 v[92:95], v[116:119], a[168:171], v[92:95]\n\t"
    "v_mfma_f32_16x16x32_bf16 v[100:103], v[116:119], a[192:195], 0\n\t"
    "v_mfma_f32_16x16x32_bf16 v[100:103], v[124:127], a[196:199], v[100:103]\n\t"
    "v_mfma_f32_16x16x32_bf16 v[100:103], v[128:131], a[200:203], v[100:103]\n\t"
    "buffer_store_dwordx2 v[96:97], v8, s[52:55], s61 offen offset:0 sc1\n\t"
    "v_mfma_f32_16x16x32_bf16 v[100:103], v[140:143], a[204:207], v[100:103]\n\t"
    "v_cvt_pk_bf16_f32 v88, v92, v93\n\t"
    "v_cvt_pk_bf16_f32 v89, v94, v95\n\t"
    "v_mfma_f32_16x16x32_bf16 v[100:103], v[144:147], a[208:211], v[100:103]\n\t"
    "v_mfma_f32_16x16x32_bf16 v[100:103], v[104:107], a[212:215], v[100:103]\n\t"
    "v_mfma_f32_16x16x32_bf16 v[108:111], v[152:155], a[212:215], 0\n\t"
    "v_mfma_f32_16x16x32_bf16 v[108:111], v[148:151], a[208:211], v[108:111]\n\t"
    "v_mfma_f32_16x16x32_bf16 v[108:111], v[156:159], a[204:207], v[108:111]\n\t"
    "v_mfma_f32_16x16x32_bf16 v[108:111], v[160:163], a[200:203], v[108:111]\n\t"
    "v_cvt_pk_bf16_f32 v92, v100, v101\n\t"
    "v_cvt_pk_bf16_f32 v93, v102, v103\n\t"
    "v_mfma_f32_16x16x32_bf16 v[108:111], v[164:167], a[196:199], v[108:111]\n\t"
    "v_mfma_f32_16x16x32_bf16 v[108:111], v[168:171], a[192:195], v[108:111]\n\t"
    "v_mfma_f32_16x16x32_bf16 v[100:103], v[168:171], a[216:219], 0\n\t"
    "v_mfma_f32_16x16x32_bf16 v[100:103], v[164:167], a[220:223], v[100:103]\n\t"
    "v_mfma_f32_16x16x32_bf16 v[100:103], v[160:163], a[224:227], v[100:103]\n\t"
    "buffer_store_dwordx2 v[98:99], v8, s[52:55], s61 offen offset:384 sc1\n\t"
    "v_mfma_f32_16x16x32_bf16 v[100:103], v[156:159], a[228:231], v[100:103]\n\t"
    "v_cvt_pk_bf16_f32 v94, v108, v109\n\t"
    "v_cvt_pk_bf16_f32 v95, v110, v111\n\t"
    "v_mfma_f32_16x16x32_bf16 v[100:103], v[148:151], a[232:235], v[100:103]\n\t"
    "v_mfma_f32_16x16x32_bf16 v[100:103], v[152:155], a[236:239], v[100:103]\n\t"
    "v_mfma_f32_16x16x32_bf16 v[108:111], v[104:107], a[236:239], 0\n\t"
    "v_mfma_f32_16x16x32_bf16 v[108:111], v[144:147], a[232:235], v[108:111]\n\t"
    "v_mfma_f32_16x16x32_bf16 v[108:111], v[140:143], a[228:231], v[108:111]\n\t"
    "v_mfma_f32_16x16x32_bf16 v[108:111], v[128:131], a[224:227], v[108:111]\n\t"
    "v_cvt_pk_bf16_f32 v98, v100, v101\n\t"
    "v_cvt_pk_bf16_f32 v99, v102, v103\n\t"
    "v_mfma_f32_16x16x32_bf16 v[108:111], v[124:127], a[220:223], v[108:111]\n\t"
    "v_mfma_f32_16x16x32_bf16 v[108:111], v[116:119], a[216:219], v[108:111]\n\t"
    "v_mfma_f32_16x16x32_bf16 v[120:123], v[116:119], a[240:243], 0\n\t"
    "v_mfma_f32_16x16x32_bf16 v[120:123], v[124:127], a[244:247], v[120:123]\n\t"
    "v_mfma_f32_16x16x32_bf16 v[120:123], v[128:131], a[248:251], v[120:123]\n\t"
    "buffer_store_dwordx2 v[112:113], v8, s[52:55], s61 offen offset:768 sc1\n\t"
    "v_mfma_f32_16x16x32_bf16 v[120:123], v[140:143], a[252:255], v[120:123]\n\t"
    "v_cvt_pk_bf16_f32 v96, v108, v109\n\t"
    "v_cvt_pk_bf16_f32 v97, v110, v111\n\t"
    "v_mfma_f32_16x16x32_bf16 v[120:123], v[144:147], v[224:227], v[120:123]\n\t"
    "v_mfma_f32_16x16x32_bf16 v[120:123], v[104:107], v[228:231], v[120:123]\n\t"
    "v_mfma_f32_16x16x32_bf16 v[108:111], v[152:155], v[228:231], 0\n\t"
    "v_mfma_f32_16x16x32_bf16 v[108:111], v[148:151], v[224:227], v[108:111]\n\t"
    "v_mfma_f32_16x16x32_bf16 v[108:111], v[156:159], a[252:255], v[108:111]\n\t"
    "v_mfma_f32_16x16x32_bf16 v[108:111], v[160:163], a[248:251], v[108:111]\n\t"
    "v_cvt_pk_bf16_f32 v100, v120, v121\n\t"
    "v_cvt_pk_bf16_f32 v101, v122, v123\n\t"
    "v_mfma_f32_16x16x32_bf16 v[108:111], v[164:167], a[244:247], v[108:111]\n\t"
    "v_mfma_f32_16x16x32_bf16 v[108:111], v[168:171], a[240:243], v[108:111]\n\t"
    "v_mfma_f32_16x16x32_bf16 v[120:123], v[168:171], v[232:235], 0\n\t"
    "s_barrier\n\t"
    "v_mfma_f32_16x16x32_bf16 v[120:123], v[164:167], v[236:239], v[120:123]\n\t"
    "ds_read_b128 v[132:135], v6 offset:0\n\t"
    "v_mfma_f32_16x16x32_bf16 v[120:123], v[160:163], v[240:243], v[120:123]\n\t"
    "ds_read_b128 v[136:139], v6 offset:1024\n\t"
    "v_mfma_f32_16x16x32_bf16 v[120:123], v[156:159], v[244:247], v[120:123]\n\t"
    "buffer_store_dwordx2 v[114:115], v8, s[52:55], s61 offen offset:1152 sc1\n\t"
    "v_mfma_f32_16x16x32_bf16 v[120:123], v[148:151], v[248:251], v[120:123]\n\t"
    "s_add_u32 s61, s61, 11403264\n\t"
    "v_cvt_pk_bf16_f32 v102, v108, v109\n\t"
    "v_mfma_f32_16x16x32_bf16 v[120:123], v[152:155], v[252:255], v[120:123]\n\t"
    "v_cvt_pk_bf16_f32 v103, v110, v111\n\t"
    "v_mfma_f32_16x16x32_bf16 v[112:115], v[104:107], v[252:255], 0\n\t"
    "v_mfma_f32_16x16x32_bf16 v[112:115], v[144:147], v[248:251], v[112:115]\n\t"
    "v_mfma_f32_16x16x32_bf16 v[112:115], v[140:143], v[244:247], v[112:115]\n\t"
    "v_mfma_f32_16x16x32_bf16 v[112:115], v[128:131], v[240:243], v[112:115]\n\t"
    "v_cvt_pk_bf16_f32 v106, v120, v121\n\t"
    "v_cvt_pk_bf16_f32 v107, v122, v123\n\t"
    "v_mfma_f32_16x16x32_bf16 v[112:115], v[124:127], v[236:239], v[112:115]\n\t"
    "v_mfma_f32_16x16x32_bf16 v[112:115], v[116:119], v[232:235], v[112:115]\n\t"
    "s_waitcnt lgkmcnt(0)\n\t"
    "v_mfma_f32_16x16x32_bf16 v[108:111], v[132:135], v[12:15], 0\n\t"
    "v_mfma_f32_16x16x32_bf16 v[108:111], v[136:139], v[60:63], v[108:111]\n\t"
    "v_mfma_f32_16x16x32_bf16 v[116:119], v[136:139], v[64:67], 0\n\t"
    "v_mfma_f32_16x16x32_bf16 v[116:119], v[132:135], v[16:19], v[116:119]\n\t"
    "v_cvt_pk_bf16_f32 v104, v112, v113\n\t"
    "v_cvt_pk_bf16_f32 v105, v114, v115\n\t"
    "v_mfma_f32_16x16x32_bf16 v[120:123], v[132:135], v[20:23], 0\n\t"
    "v_mfma_f32_16x16x32_bf16 v[120:123], v[136:139], v[68:71], v[120:123]\n\t"
    "v_mfma_f32_16x16x32_bf16 v[112:115], v[136:139], v[72:75], 0\n\t"
    "v_mfma_f32_16x16x32_bf16 v[112:115], v[132:135], v[24:27], v[112:115]\n\t"
    "v_cvt_pk_bf16_f32 v124, v108, v116\n\t"
    "v_cvt_pk_bf16_f32 v140, v109, v117\n\t"
    "v_mfma_f32_16x16x32_bf16 v[128:131], v[132:135], v[28:31], 0\n\t"
    "v_cvt_pk_bf16_f32 v144, v110, v118\n\t"
    "v_cvt_pk_bf16_f32 v148, v111, v119\n\t"
    "v_mfma_f32_16x16x32_bf16 v[128:131], v[136:139], v[76:79], v[128:131]\n\t"
    "v_mfma_f32_16x16x32_bf16 v[152:155], v[136:139], v[80:83], 0\n\t"
    "v_mfma_f32_16x16x32_bf16 v[152:155], v[132:135], v[32:35], v[152:155]\n\t"
    "v_cvt_pk_bf16_f32 v125, v120, v112\n\t"
    "v_cvt_pk_bf16_f32 v141, v121, v113\n\t"
    "v_mfma_f32_16x16x32_bf16 v[108:111], v[132:135], v[36:39], 0\n\t"
    "v_cvt_pk_bf16_f32 v145, v122, v114\n\t"
    "v_cvt_pk_bf16_f32 v149, v123, v115\n\t"
    "v_mfma_f32_16x16x32_bf16 v[108:111], v[136:139], v[84:87], v[108:111]\n\t"
    "ds_read_b128 v[156:159], v6 offset:2048\n\t"
    "v_mfma_f32_16x16x32_bf16 v[116:119], v[136:139], v[88:91], 0\n\t"
    "ds_read_b128 v[160:163], v6 offset:3072\n\t"
    "v_mfma_f32_16x16x32_bf16 v[116:119], v[132:135], v[40:43], v[116:119]\n\t"
    "v_cvt_pk_bf16_f32 v126, v128, v152\n\t"
    "v_cvt_pk_bf16_f32 v142, v129, v153\n\t"
    "v_mfma_f32_16x16x32_bf16 v[120:123], v[132:135], v[44:47], 0\n\t"
    "v_cvt_pk_bf16_f32 v146, v130, v154\n\t"
    "v_cvt_pk_bf16_f32 v150, v131, v155\n\t"
    "v_mfma_f32_16x16x32_bf16 v[120:123], v[136:139], v[92:95], v[120:123]\n\t"
    "v_mfma_f32_16x16x32_bf16 v[164:167], v[136:139], v[96:99], 0\n\t"
    "v_mfma_f32_16x16x32_bf16 v[164:167], v[132:135], v[48:51], v[164:167]\n\t"
    "v_cvt_pk_bf16_f32 v127, v108, v116\n\t"
    "v_cvt_pk_bf16_f32 v143, v109, v117\n\t"
    "v_mfma_f32_16x16x32_bf16 v[128:131], v[132:135], v[52:55], 0\n\t"
    "v_cvt_pk_bf16_f32 v147, v110, v118\n\t"
    ".Lh2_%=:\n\t"
    "s_cbranch_execz .LrN_%=\n\t"
    "v_cvt_pk_bf16_f32 v151, v111, v119\n\t"
    "v_mfma_f32_16x16x32_bf16 v[128:131], v[136:139], v[100:103], v[128:131]\n\t"
    "v_mfma_f32_16x16x32_bf16 v[152:155], v[136:139], v[104:107], 0\n\t"
    "v_mfma_f32_16x16x32_bf16 v[152:155], v[132:135], v[56:59], v[152:155]\n\t"
    "v_cvt_pk_bf16_f32 v108, v120, v164\n\t"
    "s_waitcnt lgkmcnt(0)\n\t"
    "v_mfma_f32_16x16x32_bf16 v[168:171], v[156:159], v[12:15], 0\n\t"
    "buffer_store_dwordx4 v[124:127], v7, s[52:55], s61 offen offset:0 sc1\n\t"
    "v_mfma_f32_16x16x32_bf16 v[168:171], v[160:163], v[60:63], v[168:171]\n\t"
    "v_cvt_pk_bf16_f32 v110, v121, v165\n\t"
    "v_cvt_pk_bf16_f32 v112, v122, v166\n\t"
    "v_mfma_f32_16x16x32_bf16 v[132:135], v[160:163], v[64:67], 0\n\t"
    "v_cvt_pk_bf16_f32 v114, v123, v167\n\t"
    "v_mfma_f32_16x16x32_bf16 v[132:135], v[156:159], v[16:19], v[132:135]\n\t"
    "v_cvt_pk_bf16_f32 v109, v128, v152\n\t"
    "v_cvt_pk_bf16_f32 v111, v129, v153\n\t"
    "v_mfma_f32_16x16x32_bf16 v[172:175], v[156:159], v[20:23], 0\n\t"
    "v_cvt_pk_bf16_f32 v113, v130, v154\n\t"
    "v_cvt_pk_bf16_f32 v115, v131, v155\n\t"
    "v_mfma_f32_16x16x32_bf16 v[172:175], v[160:163], v[68:71], v[172:175]\n\t"
    "v_mfma_f32_16x16x32_bf16 v[164:167], v[160:163], v[72:75], 0\n\t"
    "v_mfma_f32_16x16x32_bf16 v[164:167], v[156:159], v[24:27], v[164:167]\n\t"
    "v_cvt_pk_bf16_f32 v120, v168, v132\n\t"
    "v_cvt_pk_bf16_f32 v116, v169, v133\n\t"
    "v_mfma_f32_16x16x32_bf16 v[152:155], v[156:159], v[28:31], 0\n\t"
    "buffer_store_dwordx4 v[140:143], v7, s[52:55], s61 offen offset:384 sc1\n\t"
    "v_mfma_f32_16x16x32_bf16 v[152:155], v[160:163], v[76:79], v[152:155]\n\t"
    "v_cvt_pk_bf16_f32 v128, v170, v134\n\t"
    "v_cvt_pk_bf16_f32 v124, v171, v135\n\t"
    "v_mfma_f32_16x16x32_bf16 v[176:179], v[160:163], v[80:83], 0\n\t"
    "v_mfma_f32_16x16x32_bf16 v[176:179], v[156:159], v[32:35], v[176:179]\n\t"
    "v_cvt_pk_bf16_f32 v121, v172, v164\n\t"
    "v_cvt_pk_bf16_f32 v117, v173, v165\n\t"
    "v_mfma_f32_16x16x32_bf16 v[140:143], v[156:159], v[36:39], 0\n\t"
    "v_cvt_pk_bf16_f32 v129, v174, v166\n\t"
    "v_cvt_pk_bf16_f32 v125, v175, v167\n\t"
    "v_mfma_f32_16x16x32_bf16 v[140:143], v[160:163], v[84:87], v[140:143]\n\t"
    "ds_read_b128 v[132:135], v6 offset:4096\n\t"
    "v_mfma_f32_16x16x32_bf16 v[168:171], v[160:163], v[88:91], 0\n\t"
    "ds_read_b128 v[136:139], v6 offset:5120\n\t"
    "v_mfma_f32_16x16x32_bf16 v[168:171], v[156:159], v[40:43], v[168:171]\n\t"
    "v_cvt_pk_bf16_f32 v122, v152, v176\n\t"
    "v_cvt_pk_bf16_f32 v118, v153, v177\n\t"
    "v_mfma_f32_16x16x32_bf16 v[164:167], v[156:159], v[44:47], 0\n\t"
    "v_cvt_pk_bf16_f32 v130, v154, v178\n\t"
    "v_cvt_pk_bf16_f32 v126, v155, v179\n\t"
    "v_mfma_f32_16x16x32_bf16 v[164:167], v[160:163], v[92:95], v[164:167]\n\t"
    "v_mfma_f32_16x16x32_bf16 v[172:175], v[160:163], v[96:99], 0\n\t"
    "buffer_store_dwordx4 v[144:147], v7, s[52:55], s61 offen offset:768 sc1\n\t"
    "v_mfma_f32_16x16x32_bf16 v[172:175], v[156:159], v[48:51], v[172:175]\n\t"
    "v_cvt_pk_bf16_f32 v123, v140, v168\n\t"
    "v_cvt_pk_bf16_f32 v119, v141, v169\n\t"
    "v_mfma_f32_16x16x32_bf16 v[176:179], v[156:159], v[52:55], 0\n\t"
    "v_cvt_pk_bf16_f32 v131, v142, v170\n\t"
    "v_cvt_pk_bf16_f32 v127, v143, v171\n\t"
    "v_mfma_f32_16x16x32_bf16 v[176:179], v[160:163], v[100:103], v[176:179]\n\t"
    "v_mfma_f32_16x16x32_bf16 v[180:183], v[160:163], v[104:107], 0\n\t"
    "v_mfma_f32_16x16x32_bf16 v[180:183], v[156:159], v[56:59], v[180:183]\n\t"
    "v_cvt_pk_bf16_f32 v140, v164, v172\n\t"
    "s_waitcnt lgkmcnt(0)\n\t"
    "v_mfma_f32_16x16x32_bf16 v[188:191], v[132:135], v[12:15], 0\n\t"
    "v_cvt_pk_bf16_f32 v142, v165, v173\n\t"
    "v_cvt_pk_bf16_f32 v144, v166, v174\n\t"
    "v_mfma_f32_16x16x32_bf16 v[188:191], v[136:139], v[60:63], v[188:191]\n\t"
    "v_cvt_pk_bf16_f32 v146, v167, v175\n\t"
    "v_mfma_f32_16x16x32_bf16 v[192:195], v[136:139], v[64:67], 0\n\t"
    "v_mfma_f32_16x16x32_bf16 v[192:195], v[132:135], v[16:19], v[192:195]\n\t"
    "v_cvt_pk_bf16_f32 v141, v176, v180\n\t"
    "v_cvt_pk_bf16_f32 v143, v177, v181\n\t"
    "v_mfma_f32_16x16x32_bf16 v[196:199], v[132:135], v[20:23], 0\n\t"
    "buffer_store_dwordx4 v[148:151], v7, s[52:55], s61 offen offset:1152 sc1\n\t"
    "v_mfma_f32_16x16x32_bf16 v[196:199], v[136:139], v[68:71], v[196:199]\n\t"
    "v_cvt_pk_bf16_f32 v145, v178, v182\n\t"
    "v_cvt_pk_bf16_f32 v147, v179, v183\n\t"
    "v_mfma_f32_16x16x32_bf16 v[200:203], v[136:139], v[72:75], 0\n\t"
    "v_mfma_f32_16x16x32_bf16 v[200:203], v[132:135], v[24:27], v[200:203]\n\t"
    "ds_read_b128 v[148:151], v0 offset:0\n\t"
    "v_cvt_pk_bf16_f32 v152, v188, v192\n\t"
    "v_mfma_f32_16x16x32_bf16 v[204:207], v[132:135], v[28:31], 0\n\t"
    "ds_read_b128 v[156:159], v1 offset:0\n\t"
    "v_cvt_pk_bf16_f32 v160, v189, v193\n\t"
    "v_mfma_f32_16x16x32_bf16 v[204:207], v[136:139], v[76:79], v[204:207]\n\t"
    "ds_read_b128 v[164:167], v2 offset:0\n\t"
    "v_cvt_pk_bf16_f32 v168, v190, v194\n\t"
    "v_mfma_f32_16x16x32_bf16 v[208:211], v[136:139], v[80:83], 0\n\t"
    "ds_read_b128 v[172:175], v3 offset:0\n\t"
    "v_cvt_pk_bf16_f32 v176, v191, v195\n\t"
    "v_mfma_f32_16x16x32_bf16 v[208:211], v[132:135], v[32:35], v[208:211]\n\t"
    "ds_read_b128 v[180:183], v4 offset:0\n\t"
    "v_cvt_pk_bf16_f32 v153, v196, v200\n\t"
    "v_mfma_f32_16x16x32_bf16 v[212:215], v[132:135], v[36:39], 0\n\t"
    "ds_read_b128 v[184:187], v5 offset:0\n\t"
    "v_cvt_pk_bf16_f32 v161, v197, v201\n\t"
    "v_mfma_f32_16x16x32_bf16 v[212:215], v[136:139], v[84:87], v[212:215]\n\t"
    "v_cvt_pk_bf16_f32 v169, v198, v202\n\t"
    "v_cvt_pk_bf16_f32 v177, v199, v203\n\t"
    "v_mfma_f32_16x16x32_bf16 v[216:219], v[136:139], v[88:91], 0\n\t"
    "buffer_store_dwordx2 v[108:109], v8, s[52:55], s61 offen offset:0 sc1\n\t"
    "v_mfma_f32_16x16x32_bf16 v[216:219], v[132:135], v[40:43], v[216:219]\n\t"
    "v_cvt_pk_bf16_f32 v154, v204, v208\n\t"
    "v_cvt_pk_bf16_f32 v162, v205, v209\n\t"
    "v_mfma_f32_16x16x32_bf16 v[188:191], v[132:135], v[44:47], 0\n\t"
    "v_cvt_pk_bf16_f32 v170, v206, v210\n\t"
    "v_cvt_pk_bf16_f32 v178, v207, v211\n\t"
    "v_mfma_f32_16x16x32_bf16 v[188:191], v[136:139], v[92:95], v[188:191]\n\t"
    "v_mfma_f32_16x16x32_bf16 v[192:195], v[136:139], v[96:99], 0\n\t"
    "v_mfma_f32_16x16x32_bf16 v[192:195], v[132:135], v[48:51], v[192:195]\n\t"
    "v_cvt_pk_bf16_f32 v155, v212, v216\n\t"
    "v_cvt_pk_bf16_f32 v163, v213, v217\n\t"
    "v_mfma_f32_16x16x32_bf16 v[196:199], v[132:135], v[52:55], 0\n\t"
    "s_sub_u32 s60, s60, 1\n\t"
    "s_cmp_lg_u32 s60, 0\n\t"
    "v_mfma_f32_16x16x32_bf16 v[196:199], v[136:139], v[100:103], v[196:199]\n\t"
    "v_cvt_pk_bf16_f32 v171, v214, v218\n\t"
    "v_cvt_pk_bf16_f32 v179, v215, v219\n\t"
    "v_mfma_f32_16x16x32_bf16 v[200:203], v[136:139], v[104:107], 0\n\t"
    "v_mfma_f32_16x16x32_bf16 v[200:203], v[132:135], v[56:59], v[200:203]\n\t"
    "s_cbranch_scc0 .Ltan_%=\n\t"
    "s_waitcnt lgkmcnt(0)\n\t"
    "v_mfma_f32_16x16x32_bf16 v[204:207], v[148:151], a[0:3], 0\n\t"
    "buffer_store_dwordx2 v[110:111], v8, s[52:55], s61 offen offset:384 sc1\n\t"
    "v_mfma_f32_16x16x32_bf16 v[204:207], v[156:159], a[4:7], v[204:207]\n\t"
    "v_cvt_pk_bf16_f32 v136, v188, v192\n\t"
    "v_cvt_pk_bf16_f32 v138, v189, v193\n\t"
    "v_mfma_f32_16x16x32_bf16 v[204:207], v[164:167], a[8:11], v[204:207]\n\t"
    "v_cvt_pk_bf16_f32 v132, v190, v194\n\t"
    "v_cvt_pk_bf16_f32 v134, v191, v195\n\t"
    "v_mfma_f32_16x16x32_bf16 v[204:207], v[172:175], a[12:15], v[204:207]\n\t"
    "v_cvt_pk_bf16_f32 v137, v196, v200\n\t"
    "v_cvt_pk_bf16_f32 v139, v197, v201\n\t"
    "v_mfma_f32_16x16x32_bf16 v[204:207], v[180:183], a[16:19], v[204:207]\n\t"
    "v_cvt_pk_bf16_f32 v133, v198, v202\n\t"
    "v_cvt_pk_bf16_f32 v135, v199, v203\n\t"
    "v_mfma_f32_16x16x32_bf16 v[204:207], v[184:187], a[20:23], v[204:207]\n\t"
    "v_mfma_f32_16x16x32_bf16 v[108:111], v[184:187], a[44:47], 0\n\t"
    "v_mfma_f32_16x16x32_bf16 v[108:111], v[180:183], a[40:43], v[108:111]\n\t"
    "v_mfma_f32_16x16x32_bf16 v[108:111], v[172:175], a[36:39], v[108:111]\n\t"
    "v_mfma_f32_16x16x32_bf16 v[108:111], v[164:167], a[32:35], v[108:111]\n\t"
    "v_mfma_f32_16x16x32_bf16 v[108:111], v[156:159], a[28:31], v[108:111]\n\t"
    "buffer_store_dwordx2 v[112:113], v8, s[52:55], s61 offen offset:768 sc1\n\t"
    "v_mfma_f32_16x16x32_bf16 v[108:111], v[148:151], a[24:27], v[108:111]\n\t"
    "s_barrier\n\t"
    "v_mfma_f32_16x16x32_bf16 v[188:191], v[148:151], a[48:51], 0\n\t"
    "v_mfma_f32_16x16x32_bf16 v[188:191], v[156:159], a[52:55], v[188:191]\n\t"
    "ds_read_b128 v[192:195], v6 offset:6144\n\t"
    "v_mfma_f32_16x16x32_bf16 v[188:191], v[164:167], a[56:59], v[188:191]\n\t"
    "ds_read_b128 v[196:199], v6 offset:7168\n\t"
    "v_mfma_f32_16x16x32_bf16 v[188:191], v[172:175], a[60:63], v[188:191]\n\t"
    "v_mfma_f32_16x16x32_bf16 v[188:191], v[180:183], a[64:67], v[188:191]\n\t"
    "v_mfma_f32_16x16x32_bf16 v[188:191], v[184:187], a[68:71], v[188:191]\n\t"
    "v_mfma_f32_16x16x32_bf16 v[200:203], v[184:187], a[92:95], 0\n\t"
    "v_mfma_f32_16x16x32_bf16 v[200:203], v[180:183], a[88:91], v[200:203]\n\t"
    "v_mfma_f32_16x16x32_bf16 v[200:203], v[172:175], a[84:87], v[200:203]\n\t"
    "buffer_store_dwordx2 v[114:115], v8, s[52:55], s61 offen offset:1152 sc1\n\t"
    "v_mfma_f32_16x16x32_bf16 v[200:203], v[164:167], a[80:83], v[200:203]\n\t"
    "s_add_u32 s61, s61, 393216\n\t"
    "v_mfma_f32_16x16x32_bf16 v[200:203], v[156:159], a[76:79], v[200:203]\n\t"
    "v_mfma_f32_16x16x32_bf16 v[200:203], v[148:151], a[72:75], v[200:203]\n\t"
    "s_waitcnt lgkmcnt(0)\n\t"
    "v_mfma_f32_16x16x32_bf16 v[112:115], v[192:195], v[12:15], 0\n\t"
    "v_mfma_f32_16x16x32_bf16 v[112:115], v[196:199], v[60:63], v[112:115]\n\t"
    "v_mfma_f32_16x16x32_bf16 v[208:211], v[196:199], v[64:67], 0\n\t"
    "v_mfma_f32_16x16x32_bf16 v[208:211], v[192:195], v[16:19], v[208:211]\n\t"
    "v_mfma_f32_16x16x32_bf16 v[60:63], v[192:195], v[20:23], 0\n\t"
    "v_mfma_f32_16x16x32_bf16 v[60:63], v[196:199], v[68:71], v[60:63]\n\t"
    "v_cvt_pk_bf16_f32 v12, v204, v205\n\t"
    "v_cvt_pk_bf16_f32 v13, v206, v207\n\t"
    "v_mfma_f32_16x16x32_bf16 v[64:67], v[196:199], v[72:75], 0\n\t"
    "buffer_store_dwordx4 v[120:123], v7, s[52:55], s61 offen offset:0 sc1\n\t"
    "v_mfma_f32_16x16x32_bf16 v[64:67], v[192:195], v[24:27], v[64:67]\n\t"
    "v_cvt_pk_bf16_f32 v216, v112, v208\n\t"
    "v_cvt_pk_bf16_f32 v212, v113, v209\n\t"
    "v_mfma_f32_16x16x32_bf16 v[68:71], v[192:195], v[28:31], 0\n\t"
    "v_cvt_pk_bf16_f32 v16, v108, v109\n\t"
    "v_cvt_pk_bf16_f32 v17, v110, v111\n\t"
    "v_mfma_f32_16x16x32_bf16 v[68:71], v[196:199], v[76:79], v[68:71]\n\t"
    "v_cvt_pk_bf16_f32 v20, v188, v189\n\t"
    "v_cvt_pk_bf16_f32 v21, v190, v191\n\t"
    "v_mfma_f32_16x16x32_bf16 v[72:75], v[196:199], v[80:83], 0\n\t"
    "v_cvt_pk_bf16_f32 v204, v114, v210\n\t"
    "v_cvt_pk_bf16_f32 v120, v115, v211\n\t"
    "v_mfma_f32_16x16x32_bf16 v[72:75], v[192:195], v[32:35], v[72:75]\n\t"
    "v_cvt_pk_bf16_f32 v217, v60, v64\n\t"
    "v_cvt_pk_bf16_f32 v213, v61, v65\n\t"
    "v_mfma_f32_16x16x32_bf16 v[28:31], v[192:195], v[36:39], 0\n\t"
    "v_cvt_pk_bf16_f32 v24, v200, v201\n\t"
    "v_cvt_pk_bf16_f32 v25, v202, v203\n\t"
    "v_mfma_f32_16x16x32_bf16 v[28:31], v[196:199], v[84:87], v[28:31]\n\t"
    "ds_read_b128 v[76:79], v0 offset:6144\n\t"
    "v_cvt_pk_bf16_f32 v205, v62, v66\n\t"
    "v_mfma_f32_16x16x32_bf16 v[32:35], v[196:199], v[88:91], 0\n\t"
    "ds_read_b128 v[80:83], v1 offset:6144\n\t"
    "v_cvt_pk_bf16_f32 v121, v63, v67\n\t"
    "v_mfma_f32_16x16x32_bf16 v[32:35], v[192:195], v[40:43], v[32:35]\n\t"
    "ds_read_b128 v[108:111], v2 offset:6144\n\t"
    "v_cvt_pk_bf16_f32 v218, v68, v72\n\t"
    "v_mfma_f32_16x16x32_bf16 v[36:39], v[192:195], v[44:47], 0\n\t"
    "ds_read_b128 v[84:87], v3 offset:6144\n\t"
    "v_cvt_pk_bf16_f32 v214, v69, v73\n\t"
    "v_mfma_f32_16x16x32_bf16 v[36:39], v[196:199], v[92:95], v[36:39]\n\t"
    "ds_read_b128 v[60:63], v4 offset:6144\n\t"
    "v_cvt_pk_bf16_f32 v206, v70, v74\n\t"
    "v_mfma_f32_16x16x32_bf16 v[40:43], v[196:199], v[96:99], 0\n\t"
    "ds_read_b128 v[64:67], v5 offset:6144\n\t"
    "v_cvt_pk_bf16_f32 v122, v71, v75\n\t"
    "v_mfma_f32_16x16x32_bf16 v[40:43], v[192:195], v[48:51], v[40:43]\n\t"
    "buffer_store_dwordx4 v[116:119], v7, s[52:55], s61 offen offset:384 sc1\n\t"
    "v_mfma_f32_16x16x32_bf16 v[44:47], v[192:195], v[52:55], 0\n\t"
    "v_cvt_pk_bf16_f32 v219, v28, v32\n\t"
    "v_cvt_pk_bf16_f32 v215, v29, v33\n\t"
    "v_mfma_f32_16x16x32_bf16 v[44:47], v[196:199], v[100:103], v[44:47]\n\t"
    "v_cvt_pk_bf16_f32 v207, v30, v34\n\t"
    "v_cvt_pk_bf16_f32 v123, v31, v35\n\t"
    "v_mfma_f32_16x16x32_bf16 v[48:51], v[196:199], v[104:107], 0\n\t"
    "v_mfma_f32_16x16x32_bf16 v[48:51], v[192:195], v[56:59], v[48:51]\n\t"
    "v_cvt_pk_bf16_f32 v96, v36, v40\n\t"
    "s_waitcnt lgkmcnt(2)\n\t"
    "v_mfma_f32_16x16x32_bf16 v[28:31], v[76:79], a[72:75], 0\n\t"
    "v_cvt_pk_bf16_f32 v98, v37, v41\n\t"
    "v_cvt_pk_bf16_f32 v112, v38, v42\n\t"
    "v_mfma_f32_16x16x32_bf16 v[28:31], v[80:83], a[76:79], v[28:31]\n\t"
    "v_cvt_pk_bf16_f32 v114, v39, v43\n\t"
    "v_mfma_f32_16x16x32_bf16 v[28:31], v[108:111], a[80:83], v[28:31]\n\t"
    "buffer_store_dwordx4 v[128:131], v7, s[52:55], s61 offen offset:768 sc1\n\t"
    "v_mfma_f32_16x16x32_bf16 v[28:31], v[84:87], a[84:87], v[28:31]\n\t"
    "v_cvt_pk_bf16_f32 v97, v44, v48\n\t"
    "s_waitcnt lgkmcnt(0)\n\t"
    "v_mfma_f32_16x16x32_bf16 v[28:31], v[60:63], a[88:91], v[28:31]\n\t"
    "v_cvt_pk_bf16_f32 v99, v45, v49\n\t"
    "v_cvt_pk_bf16_f32 v113, v46, v50\n\t"
    "v_mfma_f32_16x16x32_bf16 v[28:31], v[64:67], a[92:95], v[28:31]\n\t"
    "v_cvt_pk_bf16_f32 v115, v47, v51\n\t"
    "v_mfma_f32_16x16x32_bf16 v[32:35], v[64:67], a[68:71], 0\n\t"
    "v_mfma_f32_16x16x32_bf16 v[32:35], v[60:63], a[64:67], v[32:35]\n\t"
    "v_mfma_f32_16x16x32_bf16 v[32:35], v[84:87], a[60:63], v[32:35]\n\t"
    "v_mfma_f32_16x16x32_bf16 v[32:35], v[108:111], a[56:59], v[32:35]\n\t"
    "v_cvt_pk_bf16_f32 v26, v28, v29\n\t"
    "v_cvt_pk_bf16_f32 v27, v30, v31\n\t"
    "v_mfma_f32_16x16x32_bf16 v[32:35], v[80:83], a[52:55], v[32:35]\n\t"
    "v_mfma_f32_16x16x32_bf16 v[32:35], v[76:79], a[48:51], v[32:35]\n\t"
    "v_mfma_f32_16x16x32_bf16 v[28:31], v[76:79], a[24:27], 0\n\t"
    "v_mfma_f32_16x16x32_bf16 v[28:31], v[80:83], a[28:31], v[28:31]\n\t"
    "v_mfma_f32_16x16x32_bf16 v[28:31], v[108:111], a[32:35], v[28:31]\n\t"
    "buffer_store_dwordx4 v[124:127], v7, s[52:55], s61 offen offset:1152 sc1\n\t"
    "v_mfma_f32_16x16x32_bf16 v[28:31], v[84:87], a[36:39], v[28:31]\n\t"
    "v_cvt_pk_bf16_f32 v22, v32, v33\n\t"
    "v_cvt_pk_bf16_f32 v23, v34, v35\n\t"
    "v_mfma_f32_16x16x32_bf16 v[28:31], v[60:63], a[40:43], v[28:31]\n\t"
    "v_mfma_f32_16x16x32_bf16 v[28:31], v[64:67], a[44:47], v[28:31]\n\t"
    "v_mfma_f32_16x16x32_bf16 v[32:35], v[64:67], a[20:23], 0\n\t"
    "v_mfma_f32_16x16x32_bf16 v[32:35], v[60:63], a[16:19], v[32:35]\n\t"
    "v_mfma_f32_16x16x32_bf16 v[32:35], v[84:87], a[12:15], v[32:35]\n\t"
    "v_mfma_f32_16x16x32_bf16 v[32:35], v[108:111], a[8:11], v[32:35]\n\t"
    "v_cvt_pk_bf16_f32 v18, v28, v29\n\t"
    "v_cvt_pk_bf16_f32 v19, v30, v31\n\t"
    "v_mfma_f32_16x16x32_bf16 v[32:35], v[80:83], a[4:7], v[32:35]\n\t"
    "v_mfma_f32_16x16x32_bf16 v[32:35], v[76:79], a[0:3], v[32:35]\n\t"
    "s_branch .Lmin_%=\n\t"
    ".Ltan_%=:\n\t"
    "s_nop 7\n\t"
    "s_nop 1\n\t"
    "v_cvt_pk_bf16_f32 v136, v188, v192\n\t"
    "v_cvt_pk_bf16_f32 v138, v189, v193\n\t"
    "v_cvt_pk_bf16_f32 v132, v190, v194\n\t"
    "v_cvt_pk_bf16_f32 v134, v191, v195\n\t"
    "v_cvt_pk_bf16_f32 v137, v196, v200\n\t"
    "v_cvt_pk_bf16_f32 v139, v197, v201\n\t"
    "v_cvt_pk_bf16_f32 v133, v198, v202\n\t"
    "v_cvt_pk_bf16_f32 v135, v199, v203\n\t"
    "buffer_store_dwordx2 v[110:111], v8, s[52:55], s61 offen offset:384 sc1\n\t"
    "buffer_store_dwordx2 v[112:113], v8, s[52:55], s61 offen offset:768 sc1\n\t"
    "buffer_store_dwordx2 v[114:115], v8, s[52:55], s61 offen offset:1152 sc1\n\t"
    "s_add_u32 s61, s61, 393216\n\t"
    "s_barrier\n\t"
    "ds_read_b128 v[192:195], v6 offset:6144\n\t"
    "ds_read_b128 v[196:199], v6 offset:7168\n\t"
    "s_waitcnt lgkmcnt(1)\n\t"
    "v_mfma_f32_16x16x32_bf16 v[112:115], v[192:195], v[12:15], 0\n\t"
    "buffer_store_dwordx4 v[120:123], v7, s[52:55], s61 offen offset:0 sc1\n\t"
    "s_waitcnt lgkmcnt(0)\n\t"
    "v_mfma_f32_16x16x32_bf16 v[112:115], v[196:199], v[60:63], v[112:115]\n\t"
    "buffer_store_dwordx4 v[116:119], v7, s[52:55], s61 offen offset:384 sc1\n\t"
    "v_mfma_f32_16x16x32_bf16 v[208:211], v[196:199], v[64:67], 0\n\t"
    "buffer_store_dwordx4 v[128:131], v7, s[52:55], s61 offen offset:768 sc1\n\t"
    "v_mfma_f32_16x16x32_bf16 v[208:211], v[192:195], v[16:19], v[208:211]\n\t"
    "buffer_store_dwordx4 v[124:127], v7, s[52:55], s61 offen offset:1152 sc1\n\t"
    "v_mfma_f32_16x16x32_bf16 v[60:63], v[192:195], v[20:23], 0\n\t"
    "buffer_store_dwordx2 v[140:141], v8, s[52:55], s61 offen offset:0 sc1\n\t"
    "v_mfma_f32_16x16x32_bf16 v[60:63], v[196:199], v[68:71], v[60:63]\n\t"
    "buffer_store_dwordx2 v[142:143], v8, s[52:55], s61 offen offset:384 sc1\n\t"
    "v_mfma_f32_16x16x32_bf16 v[64:67], v[196:199], v[72:75], 0\n\t"
    "buffer_store_dwordx2 v[144:145], v8, s[52:55], s61 offen offset:768 sc1\n\t"
    "v_mfma_f32_16x16x32_bf16 v[64:67], v[192:195], v[24:27], v[64:67]\n\t"
    "buffer_store_dwordx2 v[146:147], v8, s[52:55], s61 offen offset:1152 sc1\n\t"
    "s_add_u32 s61, s61, 393216\n\t"
    "v_cvt_pk_bf16_f32 v216, v112, v208\n\t"
    "v_cvt_pk_bf16_f32 v212, v113, v209\n\t"
    "v_mfma_f32_16x16x32_bf16 v[68:71], v[192:195], v[28:31], 0\n\t"
    "buffer_store_dwordx4 v[152:155], v7, s[52:55], s61 offen offset:0 sc1\n\t"
    "v_mfma_f32_16x16x32_bf16 v[68:71], v[196:199], v[76:79], v[68:71]\n\t"
    "buffer_store_dwordx4 v[160:163], v7, s[52:55], s61 offen offset:384 sc1\n\t"
    "v_mfma_f32_16x16x32_bf16 v[72:75], v[196:199], v[80:83], 0\n\t"
    "buffer_store_dwordx4 v[168:171], v7, s[52:55], s61 offen offset:768 sc1\n\t"
    "v_cvt_pk_bf16_f32 v204, v114, v210\n\t"
    "v_cvt_pk_bf16_f32 v120, v115, v211\n\t"
    "v_mfma_f32_16x16x32_bf16 v[72:75], v[192:195], v[32:35], v[72:75]\n\t"
    "buffer_store_dwordx4 v[176:179], v7, s[52:55], s61 offen offset:1152 sc1\n\t"
    "v_cvt_pk_bf16_f32 v217, v60, v64\n\t"
    "v_cvt_pk_bf16_f32 v213, v61, v65\n\t"
    "v_mfma_f32_16x16x32_bf16 v[28:31], v[192:195], v[36:39], 0\n\t"
    "buffer_store_dwordx2 v[136:137], v8, s[52:55], s61 offen offset:0 sc1\n\t"
    "v_mfma_f32_16x16x32_bf16 v[28:31], v[196:199], v[84:87], v[28:31]\n\t"
    "buffer_store_dwordx2 v[138:139], v8, s[52:55], s61 offen offset:384 sc1\n\t"
    "v_cvt_pk_bf16_f32 v205, v62, v66\n\t"
    "v_mfma_f32_16x16x32_bf16 v[32:35], v[196:199], v[88:91], 0\n\t"
    "buffer_store_dwordx2 v[132:133], v8, s[52:55], s61 offen offset:768 sc1\n\t"
    "v_cvt_pk_bf16_f32 v121, v63, v67\n\t"
    "v_mfma_f32_16x16x32_bf16 v[32:35], v[192:195], v[40:43], v[32:35]\n\t"
    "buffer_store_dwordx2 v[134:135], v8, s[52:55], s61 offen offset:1152 sc1\n\t"
    "s_add_u32 s61, s61, 393216\n\t"
    "v_cvt_pk_bf16_f32 v218, v68, v72\n\t"
    "v_mfma_f32_16x16x32_bf16 v[36:39], v[192:195], v[44:47], 0\n\t"
    "v_cvt_pk_bf16_f32 v214, v69, v73\n\t"
    "v_mfma_f32_16x16x32_bf16 v[36:39], v[196:199], v[92:95], v[36:39]\n\t"
    "v_cvt_pk_bf16_f32 v206, v70, v74\n\t"
    "v_mfma_f32_16x16x32_bf16 v[40:43], v[196:199], v[96:99], 0\n\t"
    "v_cvt_pk_bf16_f32 v122, v71, v75\n\t"
    "v_mfma_f32_16x16x32_bf16 v[40:43], v[192:195], v[48:51], v[40:43]\n\t"
    "v_mfma_f32_16x16x32_bf16 v[44:47], v[192:195], v[52:55], 0\n\t"
    "v_cvt_pk_bf16_f32 v219, v28, v32\n\t"
    "v_cvt_pk_bf16_f32 v215, v29, v33\n\t"
    "v_mfma_f32_16x16x32_bf16 v[44:47], v[196:199], v[100:103], v[44:47]\n\t"
    "v_cvt_pk_bf16_f32 v207, v30, v34\n\t"
    "v_cvt_pk_bf16_f32 v123, v31, v35\n\t"
    "v_mfma_f32_16x16x32_bf16 v[48:51], v[196:199], v[104:107], 0\n\t"
    "v_mfma_f32_16x16x32_bf16 v[48:51], v[192:195], v[56:59], v[48:51]\n\t"
    "s_nop 7\n\t"
    "s_nop 1\n\t"
    "v_cvt_pk_bf16_f32 v96, v36, v40\n\t"
    "v_cvt_pk_bf16_f32 v98, v37, v41\n\t"
    "v_cvt_pk_bf16_f32 v112, v38, v42\n\t"
    "v_cvt_pk_bf16_f32 v114, v39, v43\n\t"
    "v_cvt_pk_bf16_f32 v97, v44, v48\n\t"
    "v_cvt_pk_bf16_f32 v99, v45, v49\n\t"
    "v_cvt_pk_bf16_f32 v113, v46, v50\n\t"
    "v_cvt_pk_bf16_f32 v115, v47, v51\n\t"
    "buffer_store_dwordx4 v[216:219], v7, s[52:55], s61 offen offset:0 sc1\n\t"
    "buffer_store_dwordx4 v[212:215], v7, s[52:55], s61 offen offset:384 sc1\n\t"
    "buffer_store_dwordx4 v[204:207], v7, s[52:55], s61 offen offset:768 sc1\n\t"
    "buffer_store_dwordx4 v[120:123], v7, s[52:55], s61 offen offset:1152 sc1\n\t"
    "buffer_store_dwordx2 v[96:97], v8, s[52:55], s61 offen offset:0 sc1\n\t"
    "buffer_store_dwordx2 v[98:99], v8, s[52:55], s61 offen offset:384 sc1\n\t"
    "buffer_store_dwordx2 v[112:113], v8, s[52:55], s61 offen offset:768 sc1\n\t"
    "buffer_store_dwordx2 v[114:115], v8, s[52:55], s61 offen offset:1152 sc1\n\t"
    "s_endpgm\n\t"
    ".p2align 12\n\t"
    ".Lroln_%=:\n\t"
    "v_lshrrev_b32_e32 v20, 3, v31\n\t"
    "v_and_b32_e32 v21, 6, v20\n\t"
    "v_and_b32_e32 v28, 7, v31\n\t"
    "v_xor_b32_e32 v21, v21, v28\n\t"
    "v_lshlrev_b32_e32 v20, 7, v20\n\t"
    "v_lshl_or_b32 v7, v21, 4, v20\n\t"
    "s_mov_b32 s54, 0x6000000\n\t"
    "s_mov_b32 s55, 0x20000\n\t"
    "s_waitcnt lgkmcnt(0)\n\t"
    "s_memrealtime s[72:73]\n\t"
    "s_mov_b32 s52, s36\n\t"
    "s_and_b32 s53, s37, 0xffff\n\t"
    "s_barrier\n\t"
    "s_add_u32 m0, %[rbeg], 0\n\t"
    "s_add_u32 s61, %[wb], 0\n\t"
    "buffer_load_dwordx4 v7, s[52:55], s61 offen offset:0 nt lds\n\t"
    "buffer_load_dwordx4 v7, s[52:55], s61 offen offset:1024 nt lds\n\t"
    "buffer_load_dwordx4 v7, s[52:55], s61 offen offset:2048 nt lds\n\t"
    "s_add_u32 m0, %[rbeg], 3072\n\t"
    "s_add_u32 s61, %[wb], 196608\n\t"
    "buffer_load_dwordx4 v7, s[52:55], s61 offen offset:0 nt lds\n\t"
    "buffer_load_dwordx4 v7, s[52:55], s61 offen offset:1024 nt lds\n\t"
    "buffer_load_dwordx4 v7, s[52:55], s61 offen offset:2048 nt lds\n\t"
    "v_lshlrev_b32_e32 v30, 4, v31\n\t"
    "v_and_b32_e32 v28, 7, v31\n\t"
    "v_bfe_u32 v27, v31, 3, 1\n\t"
    "v_lshrrev_b32_e32 v29, 4, v31\n\t"
    "v_add_u32_e32 v6, %[pl], v30\n\t"
    "v_mul_u32_u24_e32 v27, 0xc00, v27\n\t"
    "v_add_u32_e32 v27, %[rbeg], v27\n\t"
    "v_lshlrev_b32_e32 v26, 7, v31\n\t"
    "v_mad_u32_u24 v20, v28, 3, 0\n\t"
    "v_and_b32_e32 v21, 6, v20\n\t"
    "v_xor_b32_e32 v21, v21, v29\n\t"
    "v_lshl_add_u32 v20, v20, 7, v27\n\t"
    "v_lshl_add_u32 v0, v21, 4, v20\n\t"
    "v_xor_b32_e32 v1, 64, v0\n\t"
    "v_mad_u32_u24 v20, v28, 3, 1\n\t"
    "v_and_b32_e32 v21, 6, v20\n\t"
    "v_xor_b32_e32 v21, v21, v29\n\t"
    "v_lshl_add_u32 v20, v20, 7, v27\n\t"
    "v_lshl_add_u32 v2, v21, 4, v20\n\t"
    "v_xor_b32_e32 v3, 64, v2\n\t"
    "v_mad_u32_u24 v20, v28, 3, 2\n\t"
    "v_and_b32_e32 v21, 6, v20\n\t"
    "v_xor_b32_e32 v21, v21, v29\n\t"
    "v_lshl_add_u32 v20, v20, 7, v27\n\t"
    "v_lshl_add_u32 v4, v21, 4, v20\n\t"
    "v_xor_b32_e32 v5, 64, v4\n\t"
    "s_mov_b32 s56, s42\n\t"
    "s_and_b32 s57, s43, 0xffff\n\t"
    "s_mov_b32 s58, 0x40000\n\t"
    "s_mov_b32 s59, 0x20000\n\t"
    "v_add_u32_e32 v8, 0x9000, v30\n\t"
    "v_add_u32_e32 v9, 0x1b000, v30\n\t"
    "s_mov_b32 s68, 0x3dd53b95\n\t"
    "s_add_u32 m0, %[rbeg], 6144\n\t"
    "s_add_u32 s61, %[wb], 393216\n\t"
    "buffer_load_dwordx4 v7, s[52:55], s61 offen offset:0 nt lds\n\t"
    "buffer_load_dwordx4 v7, s[52:55], s61 offen offset:1024 nt lds\n\t"
    "buffer_load_dwordx4 v7, s[52:55], s61 offen offset:2048 nt lds\n\t"
    "s_add_u32 m0, %[rbeg], 9216\n\t"
    "s_add_u32 s61, %[wb], 589824\n\t"
    "buffer_load_dwordx4 v7, s[52:55], s61 offen offset:0 nt lds\n\t"
    "buffer_load_dwordx4 v7, s[52:55], s61 offen offset:1024 nt lds\n\t"
    "buffer_load_dwordx4 v7, s[52:55], s61 offen offset:2048 nt lds\n\t"
    "s_mov_b64 exec, 0\n\t"
    "s_cmp_eq_u32 %[pair], 0\n\t"
    "s_cbranch_scc1 .Lh4_%=\n\t"
    "s_branch .Lh1_%=\n\t"
    ".LrM_%=:\n\t"
    "s_mov_b64 exec, -1\n\t"
    ".Ltx3_%=:\n\t"
    "s_memrealtime s[70:71]\n\t"
    "s_waitcnt lgkmcnt(0)\n\t"
    "s_sub_u32 s70, s70, s72\n\t"
    "s_cmp_lt_u32 s70, 125\n\t"
    "s_cbranch_scc1 .Ltx3_%=\n\t"
    "s_add_u32 m0, %[rbeg], 12288\n\t"
    "s_add_u32 s61, %[wb], 786432\n\t"
    "buffer_load_dwordx4 v7, s[52:55], s61 offen offset:0 nt lds\n\t"
    "buffer_load_dwordx4 v7, s[52:55], s61 offen offset:1024 nt lds\n\t"
    "buffer_load_dwordx4 v7, s[52:55], s61 offen offset:2048 nt lds\n\t"
    "s_add_u32 m0, %[rbeg], 15360\n\t"
    "s_add_u32 s61, %[wb], 983040\n\t"
    "buffer_load_dwordx4 v7, s[52:55], s61 offen offset:0 nt lds\n\t"
    "buffer_load_dwordx4 v7, s[52:55], s61 offen offset:1024 nt lds\n\t"
    "buffer_load_dwordx4 v7, s[52:55], s61 offen offset:2048 nt lds\n\t"
    "s_add_u32 m0, %[rbeg], 18432\n\t"
    "s_add_u32 s61, %[wb], 1179648\n\t"
    "buffer_load_dwordx4 v7, s[52:55], s61 offen offset:0 nt lds\n\t"
    "buffer_load_dwordx4 v7, s[52:55], s61 offen offset:1024 nt lds\n\t"
    "buffer_load_dwordx4 v7, s[52:55], s61 offen offset:2048 nt lds\n\t"
    "s_add_u32 m0, %[rbeg], 21504\n\t"
    "s_add_u32 s61, %[wb], 1376256\n\t"
    "buffer_load_dwordx4 v7, s[52:55], s61 offen offset:0 nt lds\n\t"
    "buffer_load_dwordx4 v7, s[52:55], s61 offen offset:1024 nt lds\n\t"
    "buffer_load_dwordx4 v7, s[52:55], s61 offen offset:2048 nt lds\n\t"
    "s_waitcnt vmcnt(0)\n\t"
    "s_barrier\n\t"
    "ds_read_b128 v[188:191], v2 offset:6144\n\t"
    "ds_read_b128 v[192:195], v1 offset:6144\n\t"
    "ds_read_b128 v[196:199], v0 offset:6144\n\t"
    "ds_read_b128 v[172:175], v0 offset:12288\n\t"
    "ds_read_b128 v[168:171], v1 offset:12288\n\t"
    "ds_read_b128 v[164:167], v2 offset:12288\n\t"
    "ds_read_b128 v[140:143], v2 offset:18432\n\t"
    "ds_read_b128 v[144:147], v1 offset:18432\n\t"
    "ds_read_b128 v[148:151], v0 offset:18432\n\t"
    "ds_read_b128 v[184:187], v3 offset:6144\n\t"
    "ds_read_b128 v[180:183], v4 offset:6144\n\t"
    "ds_read_b128 v[176:179], v5 offset:6144\n\t"
    "ds_read_b128 v[208:211], v3 offset:0\n\t"
    "ds_read_b128 v[204:207], v4 offset:0\n\t"
    "ds_read_b128 v[200:203], v5 offset:0\n\t"
    "ds_read_b128 v[160:163], v3 offset:12288\n\t"
    "ds_read_b128 v[156:159], v4 offset:12288\n\t"
    "ds_read_b128 v[152:155], v5 offset:12288\n\t"
    "ds_read_b128 v[128:131], v5 offset:18432\n\t"
    "ds_read_b128 v[132:135], v4 offset:18432\n\t"
    "ds_read_b128 v[136:139], v3 offset:18432\n\t"
    "ds_read_b128 v[220:223], v0 offset:0\n\t"
    "ds_read_b128 v[216:219], v1 offset:0\n\t"
    "ds_read_b128 v[212:215], v2 offset:0\n\t"
    "ds_read_b128 v[28:31], v3 offset:0\n\t"
    "ds_read_b128 v[32:35], v4 offset:0\n\t"
    "ds_read_b128 v[40:43], v5 offset:0\n\t"
    "s_add_i32 s63, %[rbeg], 24576\n\t"
    "s_add_i32 s62, %[rbeg], 49152\n\t"
    "s_barrier\n\t"
    "ds_read_b128 a[0:3], v8 offset:0\n\t"
    "ds_read_b128 a[8:11], v8 offset:1024\n\t"
    "ds_read_b128 a[16:19], v8 offset:2048\n\t"
    "ds_read_b128 a[24:27], v8 offset:3072\n\t"
    "ds_read_b128 a[32:35], v8 offset:4096\n\t"
    "ds_read_b128 a[40:43], v8 offset:5120\n\t"
    "ds_read_b128 a[44:47], v9 offset:5120\n\t"
    "ds_read_b128 a[36:39], v9 offset:4096\n\t"
    "ds_read_b128 a[28:31], v9 offset:3072\n\t"
    "ds_read_b128 a[20:23], v9 offset:2048\n\t"
    "ds_read_b128 a[12:15], v9 offset:1024\n\t"
    "s_waitcnt lgkmcnt(10)\n\t"
    "v_mfma_f32_16x16x32_bf16 v[64:67], a[0:3], v[220:223], 0\n\t"
    "ds_read_b128 a[4:7], v9 offset:0\n\t"
    "s_waitcnt lgkmcnt(10)\n\t"
    "v_mfma_f32_16x16x32_bf16 v[64:67], a[8:11], v[216:219], v[64:67]\n\t"
    "s_barrier\n\t"
    "ds_read_b128 a[48:51], v8 offset:6144\n\t"
    "s_waitcnt lgkmcnt(10)\n\t"
    "v_mfma_f32_16x16x32_bf16 v[64:67], a[16:19], v[212:215], v[64:67]\n\t"
    "ds_read_b128 a[56:59], v8 offset:7168\n\t"
    "s_waitcnt lgkmcnt(10)\n\t"
    "v_mfma_f32_16x16x32_bf16 v[64:67], a[24:27], v[28:31], v[64:67]\n\t"
    "ds_read_b128 a[64:67], v8 offset:8192\n\t"
    "s_waitcnt lgkmcnt(4)\n\t"
    "v_mfma_f32_16x16x32_bf16 v[64:67], a[32:35], v[32:35], v[64:67]\n\t"
    "ds_read_b128 a[72:75], v8 offset:9216\n\t"
    "v_mfma_f32_16x16x32_bf16 v[64:67], a[40:43], v[40:43], v[64:67]\n\t"
    "ds_read_b128 a[80:83], v8 offset:10240\n\t"
    "v_mfma_f32_16x16x32_bf16 v[56:59], a[44:47], v[40:43], 0\n\t"
    "ds_read_b128 a[88:91], v8 offset:11264\n\t"
    "v_mfma_f32_16x16x32_bf16 v[56:59], a[36:39], v[32:35], v[56:59]\n\t"
    "ds_read_b128 a[92:95], v9 offset:11264\n\t"
    "v_mfma_f32_16x16x32_bf16 v[56:59], a[28:31], v[28:31], v[56:59]\n\t"
    "ds_read_b128 a[84:87], v9 offset:10240\n\t"
    "v_mfma_f32_16x16x32_bf16 v[56:59], a[20:23], v[212:215], v[56:59]\n\t"
    "ds_read_b128 a[76:79], v9 offset:9216\n\t"
    "v_mfma_f32_16x16x32_bf16 v[56:59], a[12:15], v[216:219], v[56:59]\n\t"
    "v_cvt_pk_bf16_f32 v44, v64, v65\n\t"
    "v_cvt_pk_bf16_f32 v45, v66, v67\n\t"
    "ds_read_b128 a[68:71], v9 offset:8192\n\t"
    "s_waitcnt lgkmcnt(4)\n\t"
    "v_mfma_f32_16x16x32_bf16 v[56:59], a[4:7], v[220:223], v[56:59]\n\t"
    "ds_read_b128 a[60:63], v9 offset:7168\n\t"
    "v_mfma_f32_16x16x32_bf16 v[36:39], a[48:51], v[220:223], 0\n\t"
    "ds_read_b128 a[52:55], v9 offset:6144\n\t"
    "v_mfma_f32_16x16x32_bf16 v[36:39], a[56:59], v[216:219], v[36:39]\n\t"
    "s_barrier\n\t"
    "ds_read_b128 a[96:99], v8 offset:12288\n\t"
    "v_mfma_f32_16x16x32_bf16 v[36:39], a[64:67], v[212:215], v[36:39]\n\t"
    "ds_read_b128 a[104:107], v8 offset:13312\n\t"
    "v_mfma_f32_16x16x32_bf16 v[36:39], a[72:75], v[28:31], v[36:39]\n\t"
    "v_cvt_pk_bf16_f32 v46, v56, v57\n\t"
    "ds_read_b128 a[112:115], v8 offset:14336\n\t"
    "v_mfma_f32_16x16x32_bf16 v[36:39], a[80:83], v[32:35], v[36:39]\n\t"
    "v_cvt_pk_bf16_f32 v47, v58, v59\n\t"
    "ds_read_b128 a[120:123], v8 offset:15360\n\t"
    "v_mfma_f32_16x16x32_bf16 v[36:39], a[88:91], v[40:43], v[36:39]\n\t"
    "ds_read_b128 a[128:131], v8 offset:16384\n\t"
    "s_waitcnt lgkmcnt(4)\n\t"
    "v_mfma_f32_16x16x32_bf16 v[48:51], a[92:95], v[40:43], 0\n\t"
    "ds_read_b128 a[136:139], v8 offset:17408\n\t"
    "v_mfma_f32_16x16x32_bf16 v[48:51], a[84:87], v[32:35], v[48:51]\n\t"
    "ds_read_b128 a[140:143], v9 offset:17408\n\t"
    "v_mfma_f32_16x16x32_bf16 v[48:51], a[76:79], v[28:31], v[48:51]\n\t"
    "ds_read_b128 a[132:135], v9 offset:16384\n\t"
    "v_mfma_f32_16x16x32_bf16 v[48:51], a[68:71], v[212:215], v[48:51]\n\t"
    "v_cvt_pk_bf16_f32 v56, v36, v37\n\t"
    "ds_read_b128 a[124:127], v9 offset:15360\n\t"
    "v_mfma_f32_16x16x32_bf16 v[48:51], a[60:63], v[216:219], v[48:51]\n\t"
    "v_cvt_pk_bf16_f32 v57, v38, v39\n\t"
    "ds_read_b128 a[116:119], v9 offset:14336\n\t"
    "v_mfma_f32_16x16x32_bf16 v[48:51], a[52:55], v[220:223], v[48:51]\n\t"
    "ds_read_b128 a[108:111], v9 offset:13312\n\t"
    "v_mfma_f32_16x16x32_bf16 v[60:63], a[96:99], v[220:223], 0\n\t"
    "ds_read_b128 a[100:103], v9 offset:12288\n\t"
    "s_waitcnt lgkmcnt(4)\n\t"
    "v_mfma_f32_16x16x32_bf16 v[60:63], a[104:107], v[216:219], v[60:63]\n\t"
    "ds_read_b128 a[144:147], v8 offset:18432\n\t"
    "v_mfma_f32_16x16x32_bf16 v[60:63], a[112:115], v[212:215], v[60:63]\n\t"
    "ds_read_b128 a[152:155], v8 offset:19456\n\t"
    "v_mfma_f32_16x16x32_bf16 v[60:63], a[120:123], v[28:31], v[60:63]\n\t"
    "v_cvt_pk_bf16_f32 v58, v48, v49\n\t"
    "v_cvt_pk_bf16_f32 v59, v50, v51\n\t"
    "ds_read_b128 a[160:163], v8 offset:20480\n\t"
    "v_mfma_f32_16x16x32_bf16 v[60:63], a[128:131], v[32:35], v[60:63]\n\t"
    "ds_read_b128 a[168:171], v8 offset:21504\n\t"
    "v_mfma_f32_16x16x32_bf16 v[60:63], a[136:139], v[40:43], v[60:63]\n\t"
    "ds_read_b128 a[176:179], v8 offset:22528\n\t"
    "v_mfma_f32_16x16x32_bf16 v[36:39], a[140:143], v[40:43], 0\n\t"
    "ds_read_b128 a[184:187], v8 offset:23552\n\t"
    "v_mfma_f32_16x16x32_bf16 v[36:39], a[132:135], v[32:35], v[36:39]\n\t"
    "s_waitcnt lgkmcnt(3)\n\t"
    "v_mfma_f32_16x16x32_bf16 v[36:39], a[124:127], v[28:31], v[36:39]\n\t"
    "v_mfma_f32_16x16x32_bf16 v[36:39], a[116:119], v[212:215], v[36:39]\n\t"
    "v_cvt_pk_bf16_f32 v48, v60, v61\n\t"
    "v_cvt_pk_bf16_f32 v49, v62, v63\n\t"
    "v_mfma_f32_16x16x32_bf16 v[36:39], a[108:111], v[216:219], v[36:39]\n\t"
    "v_mfma_f32_16x16x32_bf16 v[36:39], a[100:103], v[220:223], v[36:39]\n\t"
    "v_mfma_f32_16x16x32_bf16 v[60:63], a[144:147], v[220:223], 0\n\t"
    "v_mfma_f32_16x16x32_bf16 v[60:63], a[152:155], v[216:219], v[60:63]\n\t"
    "v_mfma_f32_16x16x32_bf16 v[60:63], a[160:163], v[212:215], v[60:63]\n\t"
    "s_waitcnt lgkmcnt(0)\n\t"
    "v_mfma_f32_16x16x32_bf16 v[60:63], a[168:171], v[28:31], v[60:63]\n\t"
    "v_cvt_pk_bf16_f32 v50, v36, v37\n\t"
    "v_mfma_f32_16x16x32_bf16 v[60:63], a[176:179], v[32:35], v[60:63]\n\t"
    "v_cvt_pk_bf16_f32 v51, v38, v39\n\t"
    "v_mfma_f32_16x16x32_bf16 v[60:63], a[184:187], v[40:43], v[60:63]\n\t"
    "v_mfma_f32_16x16x32_bf16 v[64:67], v[220:223], v[44:47], 0\n\t"
    "v_mfma_f32_16x16x32_bf16 v[64:67], v[216:219], v[56:59], v[64:67]\n\t"
    "ds_read_b128 a[188:191], v9 offset:23552\n\t"
    "v_mfma_f32_16x16x32_bf16 v[64:67], v[212:215], v[48:51], v[64:67]\n\t"
    "ds_read_b128 a[180:183], v9 offset:22528\n\t"
    "v_mfma_f32_16x16x32_bf16 v[36:39], v[188:191], v[48:51], 0\n\t"
    "ds_read_b128 a[172:175], v9 offset:21504\n\t"
    "v_mfma_f32_16x16x32_bf16 v[36:39], v[192:195], v[56:59], v[36:39]\n\t"
    "ds_read_b128 a[164:167], v9 offset:20480\n\t"
    "v_mfma_f32_16x16x32_bf16 v[36:39], v[196:199], v[44:47], v[36:39]\n\t"
    "ds_read_b128 a[156:159], v9 offset:19456\n\t"
    "v_mfma_f32_16x16x32_bf16 v[68:71], v[172:175], v[44:47], 0\n\t"
    "ds_read_b128 a[148:151], v9 offset:18432\n\t"
    "v_mfma_f32_16x16x32_bf16 v[68:71], v[168:171], v[56:59], v[68:71]\n\t"
    "s_barrier\n\t"
    "ds_read_b128 a[192:195], v8 offset:24576\n\t"
    "v_mfma_f32_16x16x32_bf16 v[68:71], v[164:167], v[48:51], v[68:71]\n\t"
    "ds_read_b128 a[200:203], v8 offset:25600\n\t"
    "v_mfma_f32_16x16x32_bf16 v[72:75], v[140:143], v[48:51], 0\n\t"
    "ds_read_b128 a[208:211], v8 offset:26624\n\t"
    "v_mfma_f32_16x16x32_bf16 v[72:75], v[144:147], v[56:59], v[72:75]\n\t"
    "ds_read_b128 a[216:219], v8 offset:27648\n\t"
    "v_mfma_f32_16x16x32_bf16 v[72:75], v[148:151], v[44:47], v[72:75]\n\t"
    "ds_read_b128 a[224:227], v8 offset:28672\n\t"
    "s_waitcnt lgkmcnt(4)\n\t"
    "v_mfma_f32_16x16x32_bf16 v[48:51], a[188:191], v[40:43], 0\n\t"
    "ds_read_b128 a[232:235], v8 offset:29696\n\t"
    "v_mfma_f32_16x16x32_bf16 v[48:51], a[180:183], v[32:35], v[48:51]\n\t"
    "ds_read_b128 a[236:239], v9 offset:29696\n\t"
    "v_mfma_f32_16x16x32_bf16 v[48:51], a[172:175], v[28:31], v[48:51]\n\t"
    "ds_read_b128 a[228:231], v9 offset:28672\n\t"
    "v_mfma_f32_16x16x32_bf16 v[48:51], a[164:167], v[212:215], v[48:51]\n\t"
    "ds_read_b128 a[220:223], v9 offset:27648\n\t"
    "v_mfma_f32_16x16x32_bf16 v[48:51], a[156:159], v[216:219], v[48:51]\n\t"
    "ds_read_b128 a[212:215], v9 offset:26624\n\t"
    "v_mfma_f32_16x16x32_bf16 v[48:51], a[148:151], v[220:223], v[48:51]\n\t"
    "ds_read_b128 a[204:207], v9 offset:25600\n\t"
    "v_mfma_f32_16x16x32_bf16 v[44:47], a[192:195], v[220:223], 0\n\t"
    "ds_read_b128 a[196:199], v9 offset:24576\n\t"
    "s_waitcnt lgkmcnt(4)\n\t"
    "v_mfma_f32_16x16x32_bf16 v[44:47], a[200:203], v[216:219], v[44:47]\n\t"
    "v_cvt_pk_bf16_f32 v56, v60, v61\n\t"
    "ds_read_b128 a[240:243], v8 offset:30720\n\t"
    "v_mfma_f32_16x16x32_bf16 v[44:47], a[208:211], v[212:215], v[44:47]\n\t"
    "ds_read_b128 a[248:251], v8 offset:31744\n\t"
    "v_mfma_f32_16x16x32_bf16 v[44:47], a[216:219], v[28:31], v[44:47]\n\t"
    "v_cvt_pk_bf16_f32 v58, v48, v49\n\t"
    "ds_read_b128 v[224:227], v8 offset:32768\n\t"
    "v_mfma_f32_16x16x32_bf16 v[44:47], a[224:227], v[32:35], v[44:47]\n\t"
    "v_cvt_pk_bf16_f32 v59, v50, v51\n\t"
    "ds_read_b128 v[232:235], v8 offset:33792\n\t"
    "v_mfma_f32_16x16x32_bf16 v[44:47], a[232:235], v[40:43], v[44:47]\n\t"
    "v_cvt_pk_bf16_f32 v57, v62, v63\n\t"
    "ds_read_b128 v[240:243], v8 offset:34816\n\t"
    "v_mfma_f32_16x16x32_bf16 v[76:79], a[236:239], v[40:43], 0\n\t"
    "ds_read_b128 v[248:251], v8 offset:35840\n\t"
    "v_mfma_f32_16x16x32_bf16 v[76:79], a[228:231], v[32:35], v[76:79]\n\t"
    "ds_read_b128 v[252:255], v9 offset:35840\n\t"
    "s_waitcnt lgkmcnt(4)\n\t"
    "v_mfma_f32_16x16x32_bf16 v[76:79], a[220:223], v[28:31], v[76:79]\n\t"
    "ds_read_b128 v[244:247], v9 offset:34816\n\t"
    "v_mfma_f32_16x16x32_bf16 v[76:79], a[212:215], v[212:215], v[76:79]\n\t"
    "v_cvt_pk_bf16_f32 v48, v44, v45\n\t"
    "ds_read_b128 v[236:239], v9 offset:33792\n\t"
    "v_mfma_f32_16x16x32_bf16 v[76:79], a[204:207], v[216:219], v[76:79]\n\t"
    "v_cvt_pk_bf16_f32 v49, v46, v47\n\t"
    "ds_read_b128 v[228:231], v9 offset:32768\n\t"
    "v_mfma_f32_16x16x32_bf16 v[76:79], a[196:199], v[220:223], v[76:79]\n\t"
    "ds_read_b128 a[252:255], v9 offset:31744\n\t"
    "v_mfma_f32_16x16x32_bf16 v[80:83], a[240:243], v[220:223], 0\n\t"
    "ds_read_b128 a[244:247], v9 offset:30720\n\t"
    "v_mfma_f32_16x16x32_bf16 v[80:83], a[248:251], v[216:219], v[80:83]\n\t"
    "v_mfma_f32_16x16x32_bf16 v[80:83], v[224:227], v[212:215], v[80:83]\n\t"
    "s_waitcnt lgkmcnt(2)\n\t"
    "v_mfma_f32_16x16x32_bf16 v[80:83], v[232:235], v[28:31], v[80:83]\n\t"
    "v_cvt_pk_bf16_f32 v50, v76, v77\n\t"
    "v_cvt_pk_bf16_f32 v51, v78, v79\n\t"
    "v_mfma_f32_16x16x32_bf16 v[80:83], v[240:243], v[32:35], v[80:83]\n\t"
    "v_mfma_f32_16x16x32_bf16 v[80:83], v[248:251], v[40:43], v[80:83]\n\t"
    "v_mfma_f32_16x16x32_bf16 v[12:15], v[252:255], v[40:43], 0\n\t"
    "v_mfma_f32_16x16x32_bf16 v[12:15], v[244:247], v[32:35], v[12:15]\n\t"
    "v_mfma_f32_16x16x32_bf16 v[12:15], v[236:239], v[28:31], v[12:15]\n\t"
    "v_mfma_f32_16x16x32_bf16 v[12:15], v[228:231], v[212:215], v[12:15]\n\t"
    "v_cvt_pk_bf16_f32 v16, v80, v81\n\t"
    "v_cvt_pk_bf16_f32 v17, v82, v83\n\t"
    "s_waitcnt lgkmcnt(0)\n\t"
    "v_mfma_f32_16x16x32_bf16 v[12:15], a[252:255], v[216:219], v[12:15]\n\t"
    "v_mfma_f32_16x16x32_bf16 v[12:15], a[244:247], v[220:223], v[12:15]\n\t"
    "v_mfma_f32_16x16x32_bf16 v[20:23], a[0:3], v[196:199], 0\n\t"
    "v_mfma_f32_16x16x32_bf16 v[20:23], a[8:11], v[192:195], v[20:23]\n\t"
    "v_mfma_f32_16x16x32_bf16 v[20:23], a[16:19], v[188:191], v[20:23]\n\t"
    "v_mfma_f32_16x16x32_bf16 v[20:23], a[24:27], v[184:187], v[20:23]\n\t"
    "v_cvt_pk_bf16_f32 v18, v12, v13\n\t"
    "v_mfma_f32_16x16x32_bf16 v[20:23], a[32:35], v[180:183], v[20:23]\n\t"
    "v_mfma_f32_16x16x32_bf16 v[20:23], a[40:43], v[176:179], v[20:23]\n\t"
    "v_cvt_pk_bf16_f32 v19, v14, v15\n\t"
    "v_mfma_f32_16x16x32_bf16 v[64:67], v[208:211], v[56:59], v[64:67]\n\t"
    "v_mfma_f32_16x16x32_bf16 v[64:67], v[204:207], v[48:51], v[64:67]\n\t"
    "v_mfma_f32_16x16x32_bf16 v[64:67], v[200:203], v[16:19], v[64:67]\n\t"
    "v_mfma_f32_16x16x32_bf16 v[36:39], v[176:179], v[16:19], v[36:39]\n\t"
    "v_cvt_pk_bf16_f32 v12, v20, v21\n\t"
    "v_cvt_pk_bf16_f32 v13, v22, v23\n\t"
    "v_mfma_f32_16x16x32_bf16 v[36:39], v[180:183], v[48:51], v[36:39]\n\t"
    "s_barrier\n\t"
    "v_mfma_f32_16x16x32_bf16 v[36:39], v[184:187], v[56:59], v[36:39]\n\t"
    "v_mfma_f32_16x16x32_bf16 v[68:71], v[160:163], v[56:59], v[68:71]\n\t"
    "v_max3_f32 v20, v64, v65, v66\n\t"
    "v_mfma_f32_16x16x32_bf16 v[68:71], v[156:159], v[48:51], v[68:71]\n\t"
    "v_mfma_f32_16x16x32_bf16 v[68:71], v[152:155], v[16:19], v[68:71]\n\t"
    "v_mfma_f32_16x16x32_bf16 v[72:75], v[128:131], v[16:19], v[72:75]\n\t"
    "v_max3_f32 v21, v67, v36, v37\n\t"
    "v_max3_f32 v20, v20, v38, v39\n\t"
    "v_mfma_f32_16x16x32_bf16 v[72:75], v[132:135], v[48:51], v[72:75]\n\t"
    "v_mfma_f32_16x16x32_bf16 v[72:75], v[136:139], v[56:59], v[72:75]\n\t"
    "v_mfma_f32_16x16x32_bf16 v[16:19], a[44:47], v[176:179], 0\n\t"
    "v_max3_f32 v21, v21, v68, v69\n\t"
    "v_max3_f32 v20, v20, v70, v71\n\t"
    "v_mfma_f32_16x16x32_bf16 v[16:19], a[36:39], v[180:183], v[16:19]\n\t"
    "v_mfma_f32_16x16x32_bf16 v[16:19], a[28:31], v[184:187], v[16:19]\n\t"
    "v_mfma_f32_16x16x32_bf16 v[16:19], a[20:23], v[188:191], v[16:19]\n\t"
    "v_max3_f32 v21, v21, v72, v73\n\t"
    "v_max3_f32 v20, v20, v74, v75\n\t"
    "v_mfma_f32_16x16x32_bf16 v[16:19], a[12:15], v[192:195], v[16:19]\n\t"
    "v_max_f32_e32 v20, v20, v21\n\t"
    "v_mov_b32_e32 v21, v20\n\t"
    "v_mfma_f32_16x16x32_bf16 v[16:19], a[4:7], v[196:199], v[16:19]\n\t"
    "v_mfma_f32_16x16x32_bf16 v[24:27], a[48:51], v[196:199], 0\n\t"
    "v_permlane32_swap_b32 v20, v21\n\t"
    "v_max_f32_e32 v20, v20, v21\n\t"
    "v_mfma_f32_16x16x32_bf16 v[24:27], a[56:59], v[192:195], v[24:27]\n\t"
    "v_mov_b32_e32 v21, v20\n\t"
    "v_mfma_f32_16x16x32_bf16 v[24:27], a[64:67], v[188:191], v[24:27]\n\t"
    "v_mfma_f32_16x16x32_bf16 v[24:27], a[72:75], v[184:187], v[24:27]\n\t"
    "v_cvt_pk_bf16_f32 v14, v16, v17\n\t"
    "v_cvt_pk_bf16_f32 v15, v18, v19\n\t"
    "v_mfma_f32_16x16x32_bf16 v[24:27], a[80:83], v[180:183], v[24:27]\n\t"
    "v_permlane16_swap_b32 v20, v21\n\t"
    "v_max_f32_e32 v20, v20, v21\n\t"
    "v_mfma_f32_16x16x32_bf16 v[24:27], a[88:91], v[176:179], v[24:27]\n\t"
    "v_sub_f32_e32 v64, v64, v20\n\t"
    "v_sub_f32_e32 v65, v65, v20\n\t"
    "v_mfma_f32_16x16x32_bf16 v[16:19], a[92:95], v[176:179], 0\n\t"
    "v_exp_f32_e32 v64, v64\n\t"
    "v_mfma_f32_16x16x32_bf16 v[16:19], a[84:87], v[180:183], v[16:19]\n\t"
    "v_exp_f32_e32 v65, v65\n\t"
    "v_mfma_f32_16x16x32_bf16 v[16:19], a[76:79], v[184:187], v[16:19]\n\t"
    "v_add_f32_e32 v22, v64, v65\n\t"
    "v_sub_f32_e32 v66, v66, v20\n\t"
    "v_mfma_f32_16x16x32_bf16 v[16:19], a[68:71], v[188:191], v[16:19]\n\t"
    "v_cvt_pk_bf16_f32 v28, v24, v25\n\t"
    "v_cvt_pk_bf16_f32 v29, v26, v27\n\t"
    "v_mfma_f32_16x16x32_bf16 v[16:19], a[60:63], v[192:195], v[16:19]\n\t"
    "v_exp_f32_e32 v66, v66\n\t"
    "v_mfma_f32_16x16x32_bf16 v[16:19], a[52:55], v[196:199], v[16:19]\n\t"
    "v_sub_f32_e32 v67, v67, v20\n\t"
    "v_sub_f32_e32 v36, v36, v20\n\t"
    "v_mfma_f32_16x16x32_bf16 v[24:27], a[96:99], v[196:199], 0\n\t"
    "v_exp_f32_e32 v67, v67\n\t"
    "v_mfma_f32_16x16x32_bf16 v[24:27], a[104:107], v[192:195], v[24:27]\n\t"
    "v_add_f32_e32 v23, v66, v67\n\t"
    "v_sub_f32_e32 v37, v37, v20\n\t"
    "v_mfma_f32_16x16x32_bf16 v[24:27], a[112:115], v[188:191], v[24:27]\n\t"
    "v_exp_f32_e32 v36, v36\n\t"
    "v_mfma_f32_16x16x32_bf16 v[24:27], a[120:123], v[184:187], v[24:27]\n\t"
    "v_cvt_pk_bf16_f32 v30, v16, v17\n\t"
    "v_cvt_pk_bf16_f32 v31, v18, v19\n\t"
    "v_mfma_f32_16x16x32_bf16 v[24:27], a[128:131], v[180:183], v[24:27]\n\t"
    "v_add_f32_e32 v22, v22, v36\n\t"
    "v_sub_f32_e32 v38, v38, v20\n\t"
    "v_mfma_f32_16x16x32_bf16 v[24:27], a[136:139], v[176:179], v[24:27]\n\t"
    "v_exp_f32_e32 v37, v37\n\t"
    "v_mfma_f32_16x16x32_bf16 v[16:19], a[140:143], v[176:179], 0\n\t"
    "v_add_f32_e32 v22, v22, v37\n\t"
    "v_sub_f32_e32 v39, v39, v20\n\t"
    "v_mfma_f32_16x16x32_bf16 v[16:19], a[132:135], v[180:183], v[16:19]\n\t"
    "v_exp_f32_e32 v38, v38\n\t"
    "v_mfma_f32_16x16x32_bf16 v[16:19], a[124:127], v[184:187], v[16:19]\n\t"
    "v_add_f32_e32 v23, v23, v38\n\t"
    "v_sub_f32_e32 v68, v68, v20\n\t"
    "v_mfma_f32_16x16x32_bf16 v[16:19], a[116:119], v[188:191], v[16:19]\n\t"
    "v_cvt_pk_bf16_f32 v32, v24, v25\n\t"
    "v_cvt_pk_bf16_f32 v33, v26, v27\n\t"
    "v_mfma_f32_16x16x32_bf16 v[16:19], a[108:111], v[192:195], v[16:19]\n\t"
    "v_exp_f32_e32 v39, v39\n\t"
    "v_mfma_f32_16x16x32_bf16 v[16:19], a[100:103], v[196:199], v[16:19]\n\t"
    "v_add_f32_e32 v23, v23, v39\n\t"
    "v_sub_f32_e32 v69, v69, v20\n\t"
    "v_mfma_f32_16x16x32_bf16 v[24:27], a[144:147], v[196:199], 0\n\t"
    "v_exp_f32_e32 v68, v68\n\t"
    "v_mfma_f32_16x16x32_bf16 v[24:27], a[152:155], v[192:195], v[24:27]\n\t"
    "v_add_f32_e32 v22, v22, v68\n\t"
    "v_sub_f32_e32 v70, v70, v20\n\t"
    "v_mfma_f32_16x16x32_bf16 v[24:27], a[160:163], v[188:191], v[24:27]\n\t"
    "v_sub_f32_e32 v71, v71, v20\n\t"
    "v_mfma_f32_16x16x32_bf16 v[24:27], a[168:171], v[184:187], v[24:27]\n\t"
    "v_cvt_pk_bf16_f32 v34, v16, v17\n\t"
    "v_mfma_f32_16x16x32_bf16 v[24:27], a[176:179], v[180:183], v[24:27]\n\t"
    "v_cvt_pk_bf16_f32 v35, v18, v19\n\t"
    "v_sub_f32_e32 v72, v72, v20\n\t"
    "v_mfma_f32_16x16x32_bf16 v[24:27], a[184:187], v[176:179], v[24:27]\n\t"
    "v_sub_f32_e32 v73, v73, v20\n\t"
    "v_mfma_f32_16x16x32_bf16 v[40:43], v[220:223], v[12:15], 0\n\t"
    "v_exp_f32_e32 v69, v69\n\t"
    "v_mfma_f32_16x16x32_bf16 v[40:43], v[216:219], v[28:31], v[40:43]\n\t"
    "v_add_f32_e32 v22, v22, v69\n\t"
    "v_mfma_f32_16x16x32_bf16 v[40:43], v[212:215], v[32:35], v[40:43]\n\t"
    "v_sub_f32_e32 v74, v74, v20\n\t"
    "v_mfma_f32_16x16x32_bf16 v[16:19], v[188:191], v[32:35], 0\n\t"
    "v_exp_f32_e32 v70, v70\n\t"
    "v_mfma_f32_16x16x32_bf16 v[16:19], v[192:195], v[28:31], v[16:19]\n\t"
    "v_add_f32_e32 v23, v23, v70\n\t"
    "v_sub_f32_e32 v75, v75, v20\n\t"
    "v_mfma_f32_16x16x32_bf16 v[16:19], v[196:199], v[12:15], v[16:19]\n\t"
    "v_exp_f32_e32 v71, v71\n\t"
    "v_mfma_f32_16x16x32_bf16 v[44:47], v[172:175], v[12:15], 0\n\t"
    "v_add_f32_e32 v23, v23, v71\n\t"
    "v_cvt_pk_bf16_f32 v48, v24, v25\n\t"
    "v_mfma_f32_16x16x32_bf16 v[44:47], v[168:171], v[28:31], v[44:47]\n\t"
    "v_exp_f32_e32 v72, v72\n\t"
    "v_mfma_f32_16x16x32_bf16 v[44:47], v[164:167], v[32:35], v[44:47]\n\t"
    "v_add_f32_e32 v22, v22, v72\n\t"
    "v_cvt_pk_bf16_f32 v49, v26, v27\n\t"
    "v_mfma_f32_16x16x32_bf16 v[52:55], v[140:143], v[32:35], 0\n\t"
    "v_exp_f32_e32 v73, v73\n\t"
    "v_mfma_f32_16x16x32_bf16 v[52:55], v[144:147], v[28:31], v[52:55]\n\t"
    "v_add_f32_e32 v22, v22, v73\n\t"
    "v_mfma_f32_16x16x32_bf16 v[52:55], v[148:151], v[12:15], v[52:55]\n\t"
    "v_exp_f32_e32 v74, v74\n\t"
    "v_mfma_f32_16x16x32_bf16 v[24:27], a[188:191], v[176:179], 0\n\t"
    "v_add_f32_e32 v23, v23, v74\n\t"
    "v_mfma_f32_16x16x32_bf16 v[24:27], a[180:183], v[180:183], v[24:27]\n\t"
    "v_exp_f32_e32 v75, v75\n\t"
    "v_mfma_f32_16x16x32_bf16 v[24:27], a[172:175], v[184:187], v[24:27]\n\t"
    "v_add_f32_e32 v23, v23, v75\n\t"
    "v_add_f32_e32 v22, v22, v23\n\t"
    "v_mfma_f32_16x16x32_bf16 v[24:27], a[164:167], v[188:191], v[24:27]\n\t"
    "v_mov_b32_e32 v23, v22\n\t"
    "v_mfma_f32_16x16x32_bf16 v[24:27], a[156:159], v[192:195], v[24:27]\n\t"
    "s_add_u32 m0, %[rbeg], 24576\n\t"
    "s_add_u32 s61, %[wb], 12582912\n\t"
    "v_mfma_f32_16x16x32_bf16 v[24:27], a[148:151], v[196:199], v[24:27]\n\t"
    "v_permlane32_swap_b32 v22, v23\n\t"
    "v_add_f32_e32 v22, v22, v23\n\t"
    "v_mfma_f32_16x16x32_bf16 v[12:15], a[192:195], v[196:199], 0\n\t"
    "v_mov_b32_e32 v23, v22\n\t"
    "v_mfma_f32_16x16x32_bf16 v[12:15], a[200:203], v[192:195], v[12:15]\n\t"
    "buffer_load_dwordx4 v7, s[52:55], s61 offen offset:0 nt lds\n\t"
    "v_mfma_f32_16x16x32_bf16 v[12:15], a[208:211], v[188:191], v[12:15]\n\t"
    "v_permlane16_swap_b32 v22, v23\n\t"
    "v_add_f32_e32 v22, v22, v23\n\t"
    "v_mfma_f32_16x16x32_bf16 v[12:15], a[216:219], v[184:187], v[12:15]\n\t"
    "v_cvt_pk_bf16_f32 v50, v24, v25\n\t"
    "v_cvt_pk_bf16_f32 v51, v26, v27\n\t"
    "v_mfma_f32_16x16x32_bf16 v[12:15], a[224:227], v[180:183], v[12:15]\n\t"
    "v_rcp_f32_e32 v22, v22\n\t"
    "v_mfma_f32_16x16x32_bf16 v[12:15], a[232:235], v[176:179], v[12:15]\n\t"
    "v_mul_f32_e32 v64, v64, v22\n\t"
    "v_mul_f32_e32 v65, v65, v22\n\t"
    "v_mfma_f32_16x16x32_bf16 v[28:31], a[236:239], v[176:179], 0\n\t"
    "v_cvt_pk_bf16_f32 v24, v64, v65\n\t"
    "v_mul_f32_e32 v66, v66, v22\n\t"
    "buffer_load_dwordx4 v7, s[52:55], s61 offen offset:1024 nt lds\n\t"
    "v_mfma_f32_16x16x32_bf16 v[28:31], a[228:231], v[180:183], v[28:31]\n\t"
    "v_mul_f32_e32 v67, v67, v22\n\t"
    "v_cvt_pk_bf16_f32 v25, v66, v67\n\t"
    "v_mfma_f32_16x16x32_bf16 v[28:31], a[220:223], v[184:187], v[28:31]\n\t"
    "v_mul_f32_e32 v36, v36, v22\n\t"
    "v_mul_f32_e32 v37, v37, v22\n\t"
    "v_mfma_f32_16x16x32_bf16 v[28:31], a[212:215], v[188:191], v[28:31]\n\t"
    "v_cvt_pk_bf16_f32 v32, v12, v13\n\t"
    "v_cvt_pk_bf16_f32 v33, v14, v15\n\t"
    "v_mfma_f32_16x16x32_bf16 v[28:31], a[204:207], v[192:195], v[28:31]\n\t"
    "v_cvt_pk_bf16_f32 v26, v36, v37\n\t"
    "v_mul_f32_e32 v38, v38, v22\n\t"
    "v_mfma_f32_16x16x32_bf16 v[28:31], a[196:199], v[196:199], v[28:31]\n\t"
    "v_mul_f32_e32 v39, v39, v22\n\t"
    "v_cvt_pk_bf16_f32 v27, v38, v39\n\t"
    "buffer_load_dwordx4 v7, s[52:55], s61 offen offset:2048 nt lds\n\t"
    "v_mfma_f32_16x16x32_bf16 v[12:15], a[240:243], v[196:199], 0\n\t"
    "v_mul_f32_e32 v68, v68, v22\n\t"
    "v_mul_f32_e32 v69, v69, v22\n\t"
    "v_mfma_f32_16x16x32_bf16 v[12:15], a[248:251], v[192:195], v[12:15]\n\t"
    "v_cvt_pk_bf16_f32 v56, v68, v69\n\t"
    "v_mul_f32_e32 v70, v70, v22\n\t"
    "v_mfma_f32_16x16x32_bf16 v[12:15], v[224:227], v[188:191], v[12:15]\n\t"
    "v_mul_f32_e32 v71, v71, v22\n\t"
    "v_cvt_pk_bf16_f32 v57, v70, v71\n\t"
    "v_mfma_f32_16x16x32_bf16 v[12:15], v[232:235], v[184:187], v[12:15]\n\t"
    "v_cvt_pk_bf16_f32 v34, v28, v29\n\t"
    "v_cvt_pk_bf16_f32 v35, v30, v31\n\t"
    "s_add_u32 m0, %[rbeg], 27648\n\t"
    "s_add_u32 s61, %[wb], 12779520\n\t"
    "v_mfma_f32_16x16x32_bf16 v[12:15], v[240:243], v[180:183], v[12:15]\n\t"
    "v_mul_f32_e32 v72, v72, v22\n\t"
    "v_mul_f32_e32 v73, v73, v22\n\t"
    "v_mfma_f32_16x16x32_bf16 v[12:15], v[248:251], v[176:179], v[12:15]\n\t"
    "v_cvt_pk_bf16_f32 v58, v72, v73\n\t"
    "v_mul_f32_e32 v74, v74, v22\n\t"
    "v_mfma_f32_16x16x32_bf16 v[28:31], v[252:255], v[176:179], 0\n\t"
    "v_mul_f32_e32 v75, v75, v22\n\t"
    "v_cvt_pk_bf16_f32 v59, v74, v75\n\t"
    "v_mfma_f32_16x16x32_bf16 v[28:31], v[244:247], v[180:183], v[28:31]\n\t"
    "v_mfma_f32_16x16x32_bf16 v[28:31], v[236:239], v[184:187], v[28:31]\n\t"
    "buffer_load_dwordx4 v7, s[52:55], s61 offen offset:0 nt lds\n\t"
    "v_mfma_f32_16x16x32_bf16 v[28:31], v[228:231], v[188:191], v[28:31]\n\t"
    "v_cvt_pk_bf16_f32 v20, v12, v13\n\t"
    "v_cvt_pk_bf16_f32 v21, v14, v15\n\t"
    "v_mfma_f32_16x16x32_bf16 v[28:31], a[252:255], v[192:195], v[28:31]\n\t"
    "v_mfma_f32_16x16x32_bf16 v[28:31], a[244:247], v[196:199], v[28:31]\n\t"
    "v_mfma_f32_16x16x32_bf16 v[12:15], a[0:3], v[172:175], 0\n\t"
    "ds_write_b128 v6, v[24:27] offset:0\n\t"
    "v_mfma_f32_16x16x32_bf16 v[12:15], a[8:11], v[168:171], v[12:15]\n\t"
    "buffer_load_dwordx4 v7, s[52:55], s61 offen offset:1024 nt lds\n\t"
    "v_mfma_f32_16x16x32_bf16 v[12:15], a[16:19], v[164:167], v[12:15]\n\t"
    "v_mfma_f32_16x16x32_bf16 v[12:15], a[24:27], v[160:163], v[12:15]\n\t"
    "v_cvt_pk_bf16_f32 v22, v28, v29\n\t"
    "v_mfma_f32_16x16x32_bf16 v[12:15], a[32:35], v[156:159], v[12:15]\n\t"
    "ds_write_b128 v6, v[56:59] offset:1024\n\t"
    "v_mfma_f32_16x16x32_bf16 v[12:15], a[40:43], v[152:155], v[12:15]\n\t"
    "v_cvt_pk_bf16_f32 v23, v30, v31\n\t"
    "v_mfma_f32_16x16x32_bf16 v[40:43], v[208:211], v[48:51], v[40:43]\n\t"
    "buffer_load_dwordx4 v7, s[52:55], s61 offen offset:2048 nt lds\n\t"
    "v_mfma_f32_16x16x32_bf16 v[40:43], v[204:207], v[32:35], v[40:43]\n\t"
    "v_mfma_f32_16x16x32_bf16 v[40:43], v[200:203], v[20:23], v[40:43]\n\t"
    "s_add_i32 s64, s62, 24576\n\t"
    "v_mfma_f32_16x16x32_bf16 v[16:19], v[176:179], v[20:23], v[16:19]\n\t"
    "s_cmp_eq_u32 s64, %[rend]\n\t"
    "s_cselect_b32 s64, %[rbeg], s64\n\t"
    "v_mfma_f32_16x16x32_bf16 v[16:19], v[180:183], v[32:35], v[16:19]\n\t"
    "s_sub_i32 s65, s63, s64\n\t"
    "v_cvt_pk_bf16_f32 v24, v12, v13\n\t"
    "s_add_u32 m0, %[rbeg], 30720\n\t"
    "s_add_u32 s61, %[wb], 12976128\n\t"
    "v_mfma_f32_16x16x32_bf16 v[16:19], v[184:187], v[48:51], v[16:19]\n\t"
    "v_add_u32_e32 v0, s65, v0\n\t"
    "v_add_u32_e32 v1, s65, v1\n\t"
    "v_mfma_f32_16x16x32_bf16 v[44:47], v[160:163], v[48:51], v[44:47]\n\t"
    "v_add_u32_e32 v2, s65, v2\n\t"
    "v_add_u32_e32 v3, s65, v3\n\t"
    "v_mfma_f32_16x16x32_bf16 v[44:47], v[156:159], v[32:35], v[44:47]\n\t"
    "v_add_u32_e32 v4, s65, v4\n\t"
    "v_add_u32_e32 v5, s65, v5\n\t"
    "v_mfma_f32_16x16x32_bf16 v[44:47], v[152:155], v[20:23], v[44:47]\n\t"
    "v_max3_f32 v28, v40, v41, v42\n\t"
    "v_cvt_pk_bf16_f32 v25, v14, v15\n\t"
    "v_mfma_f32_16x16x32_bf16 v[52:55], v[128:131], v[20:23], v[52:55]\n\t"
    "v_max3_f32 v29, v43, v16, v17\n\t"
    ".Lh4_%=:\n\t"
    "s_cbranch_execz .LrM_%=\n\t"
    "v_max3_f32 v28, v28, v18, v19\n\t"
    "v_mfma_f32_16x16x32_bf16 v[52:55], v[132:135], v[32:35], v[52:55]\n\t"
    "buffer_load_dwordx4 v7, s[52:55], s61 offen offset:0 nt lds\n\t"
    "v_mfma_f32_16x16x32_bf16 v[52:55], v[136:139], v[48:51], v[52:55]\n\t"
    "v_mfma_f32_16x16x32_bf16 v[12:15], a[44:47], v[152:155], 0\n\t"
    "v_max3_f32 v29, v29, v44, v45\n\t"
    "v_max3_f32 v28, v28, v46, v47\n\t"
    "v_mfma_f32_16x16x32_bf16 v[12:15], a[36:39], v[156:159], v[12:15]\n\t"
    "v_mfma_f32_16x16x32_bf16 v[12:15], a[28:31], v[160:163], v[12:15]\n\t"
    "buffer_load_dwordx4 v7, s[52:55], s61 offen offset:1024 nt lds\n\t"
    "v_mfma_f32_16x16x32_bf16 v[12:15], a[20:23], v[164:167], v[12:15]\n\t"
    "v_max3_f32 v29, v29, v52, v53\n\t"
    "v_max3_f32 v28, v28, v54, v55\n\t"
    "v_mfma_f32_16x16x32_bf16 v[12:15], a[12:15], v[168:171], v[12:15]\n\t"
    "v_max_f32_e32 v28, v28, v29\n\t"
    "v_mov_b32_e32 v29, v28\n\t"
    "v_mfma_f32_16x16x32_bf16 v[12:15], a[4:7], v[172:175], v[12:15]\n\t"
    "v_mfma_f32_16x16x32_bf16 v[20:23], a[48:51], v[172:175], 0\n\t"
    "v_permlane32_swap_b32 v28, v29\n\t"
    "v_max_f32_e32 v28, v28, v29\n\t"
    "v_mfma_f32_16x16x32_bf16 v[20:23], a[56:59], v[168:171], v[20:23]\n\t"
    "v_mov_b32_e32 v29, v28\n\t"
    "buffer_load_dwordx4 v7, s[52:55], s61 offen offset:2048 nt lds\n\t"
    "v_mfma_f32_16x16x32_bf16 v[20:23], a[64:67], v[164:167], v[20:23]\n\t"
    "v_mfma_f32_16x16x32_bf16 v[20:23], a[72:75], v[160:163], v[20:23]\n\t"
    "v_cvt_pk_bf16_f32 v26, v12, v13\n\t"
    "v_cvt_pk_bf16_f32 v27, v14, v15\n\t"
    "v_mfma_f32_16x16x32_bf16 v[20:23], a[80:83], v[156:159], v[20:23]\n\t"
    "v_permlane16_swap_b32 v28, v29\n\t"
    "v_max_f32_e32 v28, v28, v29\n\t"
    "v_mfma_f32_16x16x32_bf16 v[20:23], a[88:91], v[152:155], v[20:23]\n\t"
    "v_sub_f32_e32 v40, v40, v28\n\t"
    "v_sub_f32_e32 v41, v41, v28\n\t"
    "s_add_u32 m0, %[rbeg], 33792\n\t"
    "s_add_u32 s61, %[wb], 13172736\n\t"
    "v_mfma_f32_16x16x32_bf16 v[12:15], a[92:95], v[152:155], 0\n\t"
    "v_exp_f32_e32 v40, v40\n\t"
    "v_mfma_f32_16x16x32_bf16 v[12:15], a[84:87], v[156:159], v[12:15]\n\t"
    "v_exp_f32_e32 v41, v41\n\t"
    "v_mfma_f32_16x16x32_bf16 v[12:15], a[76:79], v[160:163], v[12:15]\n\t"
    "v_add_f32_e32 v30, v40, v41\n\t"
    "v_sub_f32_e32 v42, v42, v28\n\t"
    "v_mfma_f32_16x16x32_bf16 v[12:15], a[68:71], v[164:167], v[12:15]\n\t"
    "v_cvt_pk_bf16_f32 v32, v20, v21\n\t"
    "v_cvt_pk_bf16_f32 v33, v22, v23\n\t"
    "v_mfma_f32_16x16x32_bf16 v[12:15], a[60:63], v[168:171], v[12:15]\n\t"
    "v_exp_f32_e32 v42, v42\n\t"
    "buffer_load_dwordx4 v7, s[52:55], s61 offen offset:0 nt lds\n\t"
    "v_mfma_f32_16x16x32_bf16 v[12:15], a[52:55], v[172:175], v[12:15]\n\t"
    "v_sub_f32_e32 v43, v43, v28\n\t"
    "v_sub_f32_e32 v16, v16, v28\n\t"
    "v_mfma_f32_16x16x32_bf16 v[20:23], a[96:99], v[172:175], 0\n\t"
    "v_exp_f32_e32 v43, v43\n\t"
    "v_mfma_f32_16x16x32_bf16 v[20:23], a[104:107], v[168:171], v[20:23]\n\t"
    "v_add_f32_e32 v31, v42, v43\n\t"
    "v_sub_f32_e32 v17, v17, v28\n\t"
    "v_mfma_f32_16x16x32_bf16 v[20:23], a[112:115], v[164:167], v[20:23]\n\t"
    "v_exp_f32_e32 v16, v16\n\t"
    "v_mfma_f32_16x16x32_bf16 v[20:23], a[120:123], v[160:163], v[20:23]\n\t"
    "v_cvt_pk_bf16_f32 v34, v12, v13\n\t"
    "v_cvt_pk_bf16_f32 v35, v14, v15\n\t"
    "buffer_load_dwordx4 v7, s[52:55], s61 offen offset:1024 nt lds\n\t"
    "v_mfma_f32_16x16x32_bf16 v[20:23], a[128:131], v[156:159], v[20:23]\n\t"
    "v_add_f32_e32 v30, v30, v16\n\t"
    "v_sub_f32_e32 v18, v18, v28\n\t"
    "v_mfma_f32_16x16x32_bf16 v[20:23], a[136:139], v[152:155], v[20:23]\n\t"
    "v_exp_f32_e32 v17, v17\n\t"
    "v_mfma_f32_16x16x32_bf16 v[12:15], a[140:143], v[152:155], 0\n\t"
    "v_add_f32_e32 v30, v30, v17\n\t"
    "v_sub_f32_e32 v19, v19, v28\n\t"
    "v_mfma_f32_16x16x32_bf16 v[12:15], a[132:135], v[156:159], v[12:15]\n\t"
    "v_exp_f32_e32 v18, v18\n\t"
    "buffer_load_dwordx4 v7, s[52:55], s61 offen offset:2048 nt lds\n\t"
    "v_mfma_f32_16x16x32_bf16 v[12:15], a[124:127], v[160:163], v[12:15]\n\t"
    "v_add_f32_e32 v31, v31, v18\n\t"
    "v_sub_f32_e32 v44, v44, v28\n\t"
    "v_mfma_f32_16x16x32_bf16 v[12:15], a[116:119], v[164:167], v[12:15]\n\t"
    "v_cvt_pk_bf16_f32 v36, v20, v21\n\t"
    "v_cvt_pk_bf16_f32 v37, v22, v23\n\t"
    "v_mfma_f32_16x16x32_bf16 v[12:15], a[108:111], v[168:171], v[12:15]\n\t"
    "v_exp_f32_e32 v19, v19\n\t"
    "v_mfma_f32_16x16x32_bf16 v[12:15], a[100:103], v[172:175], v[12:15]\n\t"
    "v_add_f32_e32 v31, v31, v19\n\t"
    "v_sub_f32_e32 v45, v45, v28\n\t"
    "v_mfma_f32_16x16x32_bf16 v[20:23], a[144:147], v[172:175], 0\n\t"
    "v_exp_f32_e32 v44, v44\n\t"
    "v_mfma_f32_16x16x32_bf16 v[20:23], a[152:155], v[168:171], v[20:23]\n\t"
    "v_add_f32_e32 v30, v30, v44\n\t"
    "v_sub_f32_e32 v46, v46, v28\n\t"
    "v_mfma_f32_16x16x32_bf16 v[20:23], a[160:163], v[164:167], v[20:23]\n\t"
    "v_sub_f32_e32 v47, v47, v28\n\t"
    "s_add_u32 m0, %[rbeg], 36864\n\t"
    "s_add_u32 s61, %[wb], 13369344\n\t"
    "v_mfma_f32_16x16x32_bf16 v[20:23], a[168:171], v[160:163], v[20:23]\n\t"
    "v_cvt_pk_bf16_f32 v38, v12, v13\n\t"
    "v_mfma_f32_16x16x32_bf16 v[20:23], a[176:179], v[156:159], v[20:23]\n\t"
    "v_cvt_pk_bf16_f32 v39, v14, v15\n\t"
    "v_sub_f32_e32 v52, v52, v28\n\t"
    "v_mfma_f32_16x16x32_bf16 v[20:23], a[184:187], v[152:155], v[20:23]\n\t"
    "v_sub_f32_e32 v53, v53, v28\n\t"
    "buffer_load_dwordx4 v7, s[52:55], s61 offen offset:0 nt lds\n\t"
    "v_mfma_f32_16x16x32_bf16 v[56:59], v[220:223], v[24:27], 0\n\t"
    "v_exp_f32_e32 v45, v45\n\t"
    "v_mfma_f32_16x16x32_bf16 v[56:59], v[216:219], v[32:35], v[56:59]\n\t"
    "v_add_f32_e32 v30, v30, v45\n\t"
    "v_mfma_f32_16x16x32_bf16 v[56:59], v[212:215], v[36:39], v[56:59]\n\t"
    "v_sub_f32_e32 v54, v54, v28\n\t"
    "v_mfma_f32_16x16x32_bf16 v[60:63], v[188:191], v[36:39], 0\n\t"
    "v_exp_f32_e32 v46, v46\n\t"
    "v_mfma_f32_16x16x32_bf16 v[60:63], v[192:195], v[32:35], v[60:63]\n\t"
    "v_add_f32_e32 v31, v31, v46\n\t"
    "v_sub_f32_e32 v55, v55, v28\n\t"
    "buffer_load_dwordx4 v7, s[52:55], s61 offen offset:1024 nt lds\n\t"
    "v_mfma_f32_16x16x32_bf16 v[60:63], v[196:199], v[24:27], v[60:63]\n\t"
    "v_exp_f32_e32 v47, v47\n\t"
    "v_mfma_f32_16x16x32_bf16 v[64:67], v[172:175], v[24:27], 0\n\t"
    "v_add_f32_e32 v31, v31, v47\n\t"
    "v_cvt_pk_bf16_f32 v12, v20, v21\n\t"
    "v_mfma_f32_16x16x32_bf16 v[64:67], v[168:171], v[32:35], v[64:67]\n\t"
    "v_exp_f32_e32 v52, v52\n\t"
    "v_mfma_f32_16x16x32_bf16 v[64:67], v[164:167], v[36:39], v[64:67]\n\t"
    "v_add_f32_e32 v30, v30, v52\n\t"
    "v_cvt_pk_bf16_f32 v13, v22, v23\n\t"
    "v_mfma_f32_16x16x32_bf16 v[68:71], v[140:143], v[36:39], 0\n\t"
    "v_exp_f32_e32 v53, v53\n\t"
    "v_mfma_f32_16x16x32_bf16 v[68:71], v[144:147], v[32:35], v[68:71]\n\t"
    "v_add_f32_e32 v30, v30, v53\n\t"
    "buffer_load_dwordx4 v7, s[52:55], s61 offen offset:2048 nt lds\n\t"
    "v_mfma_f32_16x16x32_bf16 v[68:71], v[148:151], v[24:27], v[68:71]\n\t"
    "v_exp_f32_e32 v54, v54\n\t"
    "v_mfma_f32_16x16x32_bf16 v[20:23], a[188:191], v[152:155], 0\n\t"
    "v_add_f32_e32 v31, v31, v54\n\t"
    "v_mfma_f32_16x16x32_bf16 v[20:23], a[180:183], v[156:159], v[20:23]\n\t"
    "v_exp_f32_e32 v55, v55\n\t"
    "v_mfma_f32_16x16x32_bf16 v[20:23], a[172:175], v[160:163], v[20:23]\n\t"
    "v_add_f32_e32 v31, v31, v55\n\t"
    "v_add_f32_e32 v30, v30, v31\n\t"
    "v_mfma_f32_16x16x32_bf16 v[20:23], a[164:167], v[164:167], v[20:23]\n\t"
    "v_mov_b32_e32 v31, v30\n\t"
    "v_mfma_f32_16x16x32_bf16 v[20:23], a[156:159], v[168:171], v[20:23]\n\t"
    "s_add_u32 m0, %[rbeg], 39936\n\t"
    "s_add_u32 s61, %[wb], 13565952\n\t"
    "v_mfma_f32_16x16x32_bf16 v[20:23], a[148:151], v[172:175], v[20:23]\n\t"
    "v_permlane32_swap_b32 v30, v31\n\t"
    "v_add_f32_e32 v30, v30, v31\n\t"
    "v_mfma_f32_16x16x32_bf16 v[24:27], a[192:195], v[172:175], 0\n\t"
    "v_mov_b32_e32 v31, v30\n\t"
    "buffer_load_dwordx4 v7, s[52:55], s61 offen offset:0 nt lds\n\t"
    "v_mfma_f32_16x16x32_bf16 v[24:27], a[200:203], v[168:171], v[24:27]\n\t"
    "v_mfma_f32_16x16x32_bf16 v[24:27], a[208:211], v[164:167], v[24:27]\n\t"
    "v_permlane16_swap_b32 v30, v31\n\t"
    "v_add_f32_e32 v30, v30, v31\n\t"
    "v_mfma_f32_16x16x32_bf16 v[24:27], a[216:219], v[160:163], v[24:27]\n\t"
    "v_cvt_pk_bf16_f32 v14, v20, v21\n\t"
    "v_cvt_pk_bf16_f32 v15, v22, v23\n\t"
    "v_mfma_f32_16x16x32_bf16 v[24:27], a[224:227], v[156:159], v[24:27]\n\t"
    "v_rcp_f32_e32 v30, v30\n\t"
    "buffer_load_dwordx4 v7, s[52:55], s61 offen offset:1024 nt lds\n\t"
    "v_mfma_f32_16x16x32_bf16 v[24:27], a[232:235], v[152:155], v[24:27]\n\t"
    "v_mul_f32_e32 v40, v40, v30\n\t"
    "v_mul_f32_e32 v41, v41, v30\n\t"
    "v_mfma_f32_16x16x32_bf16 v[32:35], a[236:239], v[152:155], 0\n\t"
    "v_cvt_pk_bf16_f32 v20, v40, v41\n\t"
    "v_mul_f32_e32 v42, v42, v30\n\t"
    "v_mfma_f32_16x16x32_bf16 v[32:35], a[228:231], v[156:159], v[32:35]\n\t"
    "v_mul_f32_e32 v43, v43, v30\n\t"
    "v_cvt_pk_bf16_f32 v21, v42, v43\n\t"
    "v_mfma_f32_16x16x32_bf16 v[32:35], a[220:223], v[160:163], v[32:35]\n\t"
    "v_mul_f32_e32 v16, v16, v30\n\t"
    "v_mul_f32_e32 v17, v17, v30\n\t"
    "v_mfma_f32_16x16x32_bf16 v[32:35], a[212:215], v[164:167], v[32:35]\n\t"
    "v_cvt_pk_bf16_f32 v36, v24, v25\n\t"
    "v_cvt_pk_bf16_f32 v37, v26, v27\n\t"
    "buffer_load_dwordx4 v7, s[52:55], s61 offen offset:2048 nt lds\n\t"
    "v_mfma_f32_16x16x32_bf16 v[32:35], a[204:207], v[168:171], v[32:35]\n\t"
    "v_cvt_pk_bf16_f32 v22, v16, v17\n\t"
    "v_mul_f32_e32 v18, v18, v30\n\t"
    "v_mfma_f32_16x16x32_bf16 v[32:35], a[196:199], v[172:175], v[32:35]\n\t"
    "v_mul_f32_e32 v19, v19, v30\n\t"
    "v_cvt_pk_bf16_f32 v23, v18, v19\n\t"
    "v_mfma_f32_16x16x32_bf16 v[24:27], a[240:243], v[172:175], 0\n\t"
    "v_mul_f32_e32 v44, v44, v30\n\t"
    "v_mul_f32_e32 v45, v45, v30\n\t"
    "v_mfma_f32_16x16x32_bf16 v[24:27], a[248:251], v[168:171], v[24:27]\n\t"
    "v_cvt_pk_bf16_f32 v40, v44, v45\n\t"
    "v_mul_f32_e32 v46, v46, v30\n\t"
    "v_mfma_f32_16x16x32_bf16 v[24:27], v[224:227], v[164:167], v[24:27]\n\t"
    "v_mul_f32_e32 v47, v47, v30\n\t"
    "v_cvt_pk_bf16_f32 v41, v46, v47\n\t"
    "s_add_u32 m0, %[rbeg], 43008\n\t"
    "s_add_u32 s61, %[wb], 13762560\n\t"
    "v_mfma_f32_16x16x32_bf16 v[24:27], v[232:235], v[160:163], v[24:27]\n\t"
    "v_cvt_pk_bf16_f32 v38, v32, v33\n\t"
    "v_cvt_pk_bf16_f32 v39, v34, v35\n\t"
    "v_mfma_f32_16x16x32_bf16 v[24:27], v[240:243], v[156:159], v[24:27]\n\t"
    "v_mul_f32_e32 v52, v52, v30\n\t"
    "v_mul_f32_e32 v53, v53, v30\n\t"
    "v_mfma_f32_16x16x32_bf16 v[24:27], v[248:251], v[152:155], v[24:27]\n\t"
    "v_cvt_pk_bf16_f32 v42, v52, v53\n\t"
    "v_mul_f32_e32 v54, v54, v30\n\t"
    "v_mfma_f32_16x16x32_bf16 v[16:19], v[252:255], v[152:155], 0\n\t"
    "v_mul_f32_e32 v55, v55, v30\n\t"
    "v_cvt_pk_bf16_f32 v43, v54, v55\n\t"
    "v_mfma_f32_16x16x32_bf16 v[16:19], v[244:247], v[156:159], v[16:19]\n\t"
    "buffer_load_dwordx4 v7, s[52:55], s61 offen offset:0 nt lds\n\t"
    "v_mfma_f32_16x16x32_bf16 v[16:19], v[236:239], v[160:163], v[16:19]\n\t"
    "v_mfma_f32_16x16x32_bf16 v[16:19], v[228:231], v[164:167], v[16:19]\n\t"
    "v_cvt_pk_bf16_f32 v28, v24, v25\n\t"
    "v_cvt_pk_bf16_f32 v29, v26, v27\n\t"
    "v_mfma_f32_16x16x32_bf16 v[16:19], a[252:255], v[168:171], v[16:19]\n\t"
    "v_mfma_f32_16x16x32_bf16 v[16:19], a[244:247], v[172:175], v[16:19]\n\t"
    "buffer_load_dwordx4 v7, s[52:55], s61 offen offset:1024 nt lds\n\t"
    "v_mfma_f32_16x16x32_bf16 v[24:27], a[0:3], v[148:151], 0\n\t"
    "ds_write_b128 v6, v[20:23] offset:2048\n\t"
    "v_mfma_f32_16x16x32_bf16 v[24:27], a[8:11], v[144:147], v[24:27]\n\t"
    "v_mfma_f32_16x16x32_bf16 v[24:27], a[16:19], v[140:143], v[24:27]\n\t"
    "v_mfma_f32_16x16x32_bf16 v[24:27], a[24:27], v[136:139], v[24:27]\n\t"
    "v_cvt_pk_bf16_f32 v30, v16, v17\n\t"
    "v_mfma_f32_16x16x32_bf16 v[24:27], a[32:35], v[132:135], v[24:27]\n\t"
    "ds_write_b128 v6, v[40:43] offset:3072\n\t"
    "v_mfma_f32_16x16x32_bf16 v[24:27], a[40:43], v[128:131], v[24:27]\n\t"
    "v_cvt_pk_bf16_f32 v31, v18, v19\n\t"
    "buffer_load_dwordx4 v7, s[52:55], s61 offen offset:2048 nt lds\n\t"
    "v_mfma_f32_16x16x32_bf16 v[56:59], v[208:211], v[12:15], v[56:59]\n\t"
    "v_mfma_f32_16x16x32_bf16 v[56:59], v[204:207], v[36:39], v[56:59]\n\t"
    "v_mfma_f32_16x16x32_bf16 v[56:59], v[200:203], v[28:31], v[56:59]\n\t"
    "v_mfma_f32_16x16x32_bf16 v[60:63], v[176:179], v[28:31], v[60:63]\n\t"
    "v_cvt_pk_bf16_f32 v40, v24, v25\n\t"
    "v_cvt_pk_bf16_f32 v41, v26, v27\n\t"
    "v_mfma_f32_16x16x32_bf16 v[60:63], v[180:183], v[36:39], v[60:63]\n\t"
    "s_add_u32 m0, %[rbeg], 46080\n\t"
    "s_add_u32 s61, %[wb], 13959168\n\t"
    "v_mfma_f32_16x16x32_bf16 v[60:63], v[184:187], v[12:15], v[60:63]\n\t"
    "v_mfma_f32_16x16x32_bf16 v[64:67], v[160:163], v[12:15], v[64:67]\n\t"
    "v_max3_f32 v72, v56, v57, v58\n\t"
    "v_mfma_f32_16x16x32_bf16 v[64:67], v[156:159], v[36:39], v[64:67]\n\t"
    "buffer_load_dwordx4 v7, s[52:55], s61 offen offset:0 nt lds\n\t"
    "v_mfma_f32_16x16x32_bf16 v[64:67], v[152:155], v[28:31], v[64:67]\n\t"
    "v_mfma_f32_16x16x32_bf16 v[68:71], v[128:131], v[28:31], v[68:71]\n\t"
    "v_max3_f32 v73, v59, v60, v61\n\t"
    "v_max3_f32 v72, v72, v62, v63\n\t"
    "v_mfma_f32_16x16x32_bf16 v[68:71], v[132:135], v[36:39], v[68:71]\n\t"
    "v_mfma_f32_16x16x32_bf16 v[68:71], v[136:139], v[12:15], v[68:71]\n\t"
    "v_mfma_f32_16x16x32_bf16 v[16:19], a[44:47], v[128:131], 0\n\t"
    "v_max3_f32 v73, v73, v64, v65\n\t"
    "v_max3_f32 v72, v72, v66, v67\n\t"
    "v_mfma_f32_16x16x32_bf16 v[16:19], a[36:39], v[132:135], v[16:19]\n\t"
    "buffer_load_dwordx4 v7, s[52:55], s61 offen offset:1024 nt lds\n\t"
    "v_mfma_f32_16x16x32_bf16 v[16:19], a[28:31], v[136:139], v[16:19]\n\t"
    "v_mfma_f32_16x16x32_bf16 v[16:19], a[20:23], v[140:143], v[16:19]\n\t"
    "v_max3_f32 v73, v73, v68, v69\n\t"
    "v_max3_f32 v72, v72, v70, v71\n\t"
    "v_mfma_f32_16x16x32_bf16 v[16:19], a[12:15], v[144:147], v[16:19]\n\t"
    "v_max_f32_e32 v72, v72, v73\n\t"
    "v_mov_b32_e32 v73, v72\n\t"
    "v_mfma_f32_16x16x32_bf16 v[16:19], a[4:7], v[148:151], v[16:19]\n\t"
    "buffer_load_dwordx4 v7, s[52:55], s61 offen offset:2048 nt lds\n\t"
    "v_mfma_f32_16x16x32_bf16 v[12:15], a[48:51], v[148:151], 0\n\t"
    "v_permlane32_swap_b32 v72, v73\n\t"
    "v_max_f32_e32 v72, v72, v73\n\t"
    "v_mfma_f32_16x16x32_bf16 v[12:15], a[56:59], v[144:147], v[12:15]\n\t"
    "v_mov_b32_e32 v73, v72\n\t"
    "v_mfma_f32_16x16x32_bf16 v[12:15], a[64:67], v[140:143], v[12:15]\n\t"
    "v_mfma_f32_16x16x32_bf16 v[12:15], a[72:75], v[136:139], v[12:15]\n\t"
    "v_cvt_pk_bf16_f32 v42, v16, v17\n\t"
    "v_cvt_pk_bf16_f32 v43, v18, v19\n\t"
    "v_mfma_f32_16x16x32_bf16 v[12:15], a[80:83], v[132:135], v[12:15]\n\t"
    "v_permlane16_swap_b32 v72, v73\n\t"
    "v_max_f32_e32 v72, v72, v73\n\t"
    "s_add_u32 m0, %[rbeg], 49152\n\t"
    "s_add_u32 s61, %[wb], 25165824\n\t"
    "v_mfma_f32_16x16x32_bf16 v[12:15], a[88:91], v[128:131], v[12:15]\n\t"
    "v_sub_f32_e32 v56, v56, v72\n\t"
    "v_sub_f32_e32 v57, v57, v72\n\t"
    "v_mfma_f32_16x16x32_bf16 v[16:19], a[92:95], v[128:131], 0\n\t"
    "v_exp_f32_e32 v56, v56\n\t"
    "v_mfma_f32_16x16x32_bf16 v[16:19], a[84:87], v[132:135], v[16:19]\n\t"
    "v_exp_f32_e32 v57, v57\n\t"
    "v_mfma_f32_16x16x32_bf16 v[16:19], a[76:79], v[136:139], v[16:19]\n\t"
    "v_add_f32_e32 v74, v56, v57\n\t"
    "v_sub_f32_e32 v58, v58, v72\n\t"
    "buffer_load_dwordx4 v7, s[52:55], s61 offen offset:0 nt lds\n\t"
    "v_mfma_f32_16x16x32_bf16 v[16:19], a[68:71], v[140:143], v[16:19]\n\t"
    "v_cvt_pk_bf16_f32 v44, v12, v13\n\t"
    "v_cvt_pk_bf16_f32 v45, v14, v15\n\t"
    "v_mfma_f32_16x16x32_bf16 v[16:19], a[60:63], v[144:147], v[16:19]\n\t"
    "v_exp_f32_e32 v58, v58\n\t"
    "v_mfma_f32_16x16x32_bf16 v[16:19], a[52:55], v[148:151], v[16:19]\n\t"
    "v_sub_f32_e32 v59, v59, v72\n\t"
    "v_sub_f32_e32 v60, v60, v72\n\t"
    "v_mfma_f32_16x16x32_bf16 v[12:15], a[96:99], v[148:151], 0\n\t"
    "v_exp_f32_e32 v59, v59\n\t"
    "v_mfma_f32_16x16x32_bf16 v[12:15], a[104:107], v[144:147], v[12:15]\n\t"
    "v_add_f32_e32 v75, v58, v59\n\t"
    "v_sub_f32_e32 v61, v61, v72\n\t"
    "buffer_load_dwordx4 v7, s[52:55], s61 offen offset:1024 nt lds\n\t"
    "v_mfma_f32_16x16x32_bf16 v[12:15], a[112:115], v[140:143], v[12:15]\n\t"
    "v_exp_f32_e32 v60, v60\n\t"
    "v_mfma_f32_16x16x32_bf16 v[12:15], a[120:123], v[136:139], v[12:15]\n\t"
    "v_cvt_pk_bf16_f32 v46, v16, v17\n\t"
    "v_cvt_pk_bf16_f32 v47, v18, v19\n\t"
    "v_mfma_f32_16x16x32_bf16 v[12:15], a[128:131], v[132:135], v[12:15]\n\t"
    "v_add_f32_e32 v74, v74, v60\n\t"
    "v_sub_f32_e32 v62, v62, v72\n\t"
    "v_mfma_f32_16x16x32_bf16 v[12:15], a[136:139], v[128:131], v[12:15]\n\t"
    "v_exp_f32_e32 v61, v61\n\t"
    "v_mfma_f32_16x16x32_bf16 v[16:19], a[140:143], v[128:131], 0\n\t"
    "v_add_f32_e32 v74, v74, v61\n\t"
    "v_sub_f32_e32 v63, v63, v72\n\t"
    "buffer_load_dwordx4 v7, s[52:55], s61 offen offset:2048 nt lds\n\t"
    "v_mfma_f32_16x16x32_bf16 v[16:19], a[132:135], v[132:135], v[16:19]\n\t"
    "v_exp_f32_e32 v62, v62\n\t"
    "v_mfma_f32_16x16x32_bf16 v[16:19], a[124:127], v[136:139], v[16:19]\n\t"
    "v_add_f32_e32 v75, v75, v62\n\t"
    "v_sub_f32_e32 v64, v64, v72\n\t"
    "v_mfma_f32_16x16x32_bf16 v[16:19], a[116:119], v[140:143], v[16:19]\n\t"
    "v_cvt_pk_bf16_f32 v32, v12, v13\n\t"
    "v_cvt_pk_bf16_f32 v33, v14, v15\n\t"
    "v_mfma_f32_16x16x32_bf16 v[16:19], a[108:111], v[144:147], v[16:19]\n\t"
    "v_exp_f32_e32 v63, v63\n\t"
    "v_mfma_f32_16x16x32_bf16 v[16:19], a[100:103], v[148:151], v[16:19]\n\t"
    "v_add_f32_e32 v75, v75, v63\n\t"
    "v_sub_f32_e32 v65, v65, v72\n\t"
    "v_mfma_f32_16x16x32_bf16 v[48:51], a[144:147], v[148:151], 0\n\t"
    "v_exp_f32_e32 v64, v64\n\t"
    "v_mfma_f32_16x16x32_bf16 v[48:51], a[152:155], v[144:147], v[48:51]\n\t"
    "v_add_f32_e32 v74, v74, v64\n\t"
    "v_sub_f32_e32 v66, v66, v72\n\t"
    "s_mov_b32 s60, 8\n\t"
    "s_add_u32 s61, %[wb], 25362432\n\t"
    ".Lmid_%=:\n\t"
    "v_mfma_f32_16x16x32_bf16 v[48:51], a[160:163], v[140:143], v[48:51]\n\t"
    "s_add_u32 m0, s62, 3072\n\t"
    "v_sub_f32_e32 v67, v67, v72\n\t"
    "v_mfma_f32_16x16x32_bf16 v[48:51], a[168:171], v[136:139], v[48:51]\n\t"
    "buffer_load_dwordx4 v7, s[52:55], s61 offen offset:0 nt lds\n\t"
    "v_cvt_pk_bf16_f32 v34, v16, v17\n\t"
    "v_mfma_f32_16x16x32_bf16 v[48:51], a[176:179], v[132:135], v[48:51]\n\t"
    "v_cvt_pk_bf16_f32 v35, v18, v19\n\t"
    "v_sub_f32_e32 v68, v68, v72\n\t"
    "v_mfma_f32_16x16x32_bf16 v[48:51], a[184:187], v[128:131], v[48:51]\n\t"
    "buffer_load_dwordx4 v7, s[52:55], s61 offen offset:1024 nt lds\n\t"
    "v_sub_f32_e32 v69, v69, v72\n\t"
    "v_mfma_f32_16x16x32_bf16 v[12:15], v[220:223], v[40:43], 0\n\t"
    "v_exp_f32_e32 v65, v65\n\t"
    "v_mfma_f32_16x16x32_bf16 v[12:15], v[216:219], v[44:47], v[12:15]\n\t"
    "buffer_load_dwordx4 v7, s[52:55], s61 offen offset:2048 nt lds\n\t"
    "v_add_f32_e32 v74, v74, v65\n\t"
    "v_mfma_f32_16x16x32_bf16 v[12:15], v[212:215], v[32:35], v[12:15]\n\t"
    "s_waitcnt vmcnt(24)\n\t"
    "s_add_u32 s61, s61, 196608\n\t"
    "v_mfma_f32_16x16x32_bf16 v[16:19], v[188:191], v[32:35], 0\n\t"
    "v_exp_f32_e32 v66, v66\n\t"
    "v_mfma_f32_16x16x32_bf16 v[16:19], v[192:195], v[44:47], v[16:19]\n\t"
    "ds_read_b128 v[220:223], v0 offset:0\n\t"
    "v_add_f32_e32 v75, v75, v66\n\t"
    "v_mfma_f32_16x16x32_bf16 v[16:19], v[196:199], v[40:43], v[16:19]\n\t"
    "v_exp_f32_e32 v67, v67\n\t"
    "v_mfma_f32_16x16x32_bf16 v[20:23], v[172:175], v[40:43], 0\n\t"
    "ds_read_b128 v[216:219], v1 offset:0\n\t"
    "v_add_f32_e32 v75, v75, v67\n\t"
    "v_mfma_f32_16x16x32_bf16 v[20:23], v[168:171], v[44:47], v[20:23]\n\t"
    "v_exp_f32_e32 v68, v68\n\t"
    "v_mfma_f32_16x16x32_bf16 v[20:23], v[164:167], v[32:35], v[20:23]\n\t"
    "ds_read_b128 v[212:215], v2 offset:0\n\t"
    "v_add_f32_e32 v74, v74, v68\n\t"
    "v_mfma_f32_16x16x32_bf16 v[24:27], v[140:143], v[32:35], 0\n\t"
    "v_exp_f32_e32 v69, v69\n\t"
    "v_mfma_f32_16x16x32_bf16 v[24:27], v[144:147], v[44:47], v[24:27]\n\t"
    "ds_read_b128 v[28:31], v3 offset:0\n\t"
    "v_add_f32_e32 v74, v74, v69\n\t"
    "v_mfma_f32_16x16x32_bf16 v[24:27], v[148:151], v[40:43], v[24:27]\n\t"
    "v_sub_f32_e32 v70, v70, v72\n\t"
    "v_sub_f32_e32 v71, v71, v72\n\t"
    "v_mfma_f32_16x16x32_bf16 v[52:55], a[188:191], v[128:131], 0\n\t"
    "ds_read_b128 v[32:35], v4 offset:0\n\t"
    "v_cvt_pk_bf16_f32 v36, v48, v49\n\t"
    "v_mfma_f32_16x16x32_bf16 v[52:55], a[180:183], v[132:135], v[52:55]\n\t"
    "v_exp_f32_e32 v70, v70\n\t"
    "v_mfma_f32_16x16x32_bf16 v[52:55], a[172:175], v[136:139], v[52:55]\n\t"
    "ds_read_b128 v[40:43], v5 offset:0\n\t"
    "v_add_f32_e32 v75, v75, v70\n\t"
    "v_mfma_f32_16x16x32_bf16 v[52:55], a[164:167], v[140:143], v[52:55]\n\t"
    "v_exp_f32_e32 v71, v71\n\t"
    "v_mfma_f32_16x16x32_bf16 v[52:55], a[156:159], v[144:147], v[52:55]\n\t"
    "v_add_f32_e32 v75, v75, v71\n\t"
    "v_add_f32_e32 v74, v74, v75\n\t"
    "v_mfma_f32_16x16x32_bf16 v[52:55], a[148:151], v[148:151], v[52:55]\n\t"
    "v_mov_b32_e32 v75, v74\n\t"
    "v_cvt_pk_bf16_f32 v37, v50, v51\n\t"
    "v_mfma_f32_16x16x32_bf16 v[76:79], a[192:195], v[148:151], 0\n\t"
    "v_mfma_f32_16x16x32_bf16 v[76:79], a[200:203], v[144:147], v[76:79]\n\t"
    "v_permlane32_swap_b32 v74, v75\n\t"
    "v_add_f32_e32 v74, v74, v75\n\t"
    "v_mfma_f32_16x16x32_bf16 v[76:79], a[208:211], v[140:143], v[76:79]\n\t"
    "v_mov_b32_e32 v75, v74\n\t"
    "v_mfma_f32_16x16x32_bf16 v[76:79], a[216:219], v[136:139], v[76:79]\n\t"
    "v_cvt_pk_bf16_f32 v38, v52, v53\n\t"
    "v_cvt_pk_bf16_f32 v39, v54, v55\n\t"
    "v_mfma_f32_16x16x32_bf16 v[76:79], a[224:227], v[132:135], v[76:79]\n\t"
    "v_permlane16_swap_b32 v74, v75\n\t"
    "v_add_f32_e32 v74, v74, v75\n\t"
    "v_mfma_f32_16x16x32_bf16 v[76:79], a[232:235], v[128:131], v[76:79]\n\t"
    "v_rcp_f32_e32 v74, v74\n\t"
    "v_mfma_f32_16x16x32_bf16 v[80:83], a[236:239], v[128:131], 0\n\t"
    "v_mul_f32_e32 v56, v56, v74\n\t"
    "v_mul_f32_e32 v57, v57, v74\n\t"
    "v_mfma_f32_16x16x32_bf16 v[80:83], a[228:231], v[132:135], v[80:83]\n\t"
    "v_cvt_pk_bf16_f32 v44, v56, v57\n\t"
    "v_mul_f32_e32 v58, v58, v74\n\t"
    "v_mfma_f32_16x16x32_bf16 v[80:83], a[220:223], v[136:139], v[80:83]\n\t"
    "v_mul_f32_e32 v59, v59, v74\n\t"
    "v_cvt_pk_bf16_f32 v45, v58, v59\n\t"
    "v_mfma_f32_16x16x32_bf16 v[80:83], a[212:215], v[140:143], v[80:83]\n\t"
    "v_cvt_pk_bf16_f32 v48, v76, v77\n\t"
    "v_cvt_pk_bf16_f32 v49, v78, v79\n\t"
    "v_mfma_f32_16x16x32_bf16 v[80:83], a[204:207], v[144:147], v[80:83]\n\t"
    "v_mul_f32_e32 v60, v60, v74\n\t"
    "v_mul_f32_e32 v61, v61, v74\n\t"
    "v_mfma_f32_16x16x32_bf16 v[80:83], a[196:199], v[148:151], v[80:83]\n\t"
    "v_cvt_pk_bf16_f32 v46, v60, v61\n\t"
    "v_mul_f32_e32 v62, v62, v74\n\t"
    "v_mfma_f32_16x16x32_bf16 v[76:79], a[240:243], v[148:151], 0\n\t"
    "v_mul_f32_e32 v63, v63, v74\n\t"
    "v_cvt_pk_bf16_f32 v47, v62, v63\n\t"
    "v_mfma_f32_16x16x32_bf16 v[76:79], a[248:251], v[144:147], v[76:79]\n\t"
    "v_mul_f32_e32 v64, v64, v74\n\t"
    "v_mul_f32_e32 v65, v65, v74\n\t"
    "v_mfma_f32_16x16x32_bf16 v[76:79], v[224:227], v[140:143], v[76:79]\n\t"
    "v_cvt_pk_bf16_f32 v52, v64, v65\n\t"
    "v_mul_f32_e32 v66, v66, v74\n\t"
    "v_mfma_f32_16x16x32_bf16 v[76:79], v[232:235], v[136:139], v[76:79]\n\t"
    "v_cvt_pk_bf16_f32 v50, v80, v81\n\t"
    "v_cvt_pk_bf16_f32 v51, v82, v83\n\t"
    "v_mfma_f32_16x16x32_bf16 v[76:79], v[240:243], v[132:135], v[76:79]\n\t"
    "v_mul_f32_e32 v67, v67, v74\n\t"
    "v_cvt_pk_bf16_f32 v53, v66, v67\n\t"
    "v_mfma_f32_16x16x32_bf16 v[76:79], v[248:251], v[128:131], v[76:79]\n\t"
    "v_mul_f32_e32 v68, v68, v74\n\t"
    "v_mul_f32_e32 v69, v69, v74\n\t"
    "v_mfma_f32_16x16x32_bf16 v[56:59], v[252:255], v[128:131], 0\n\t"
    "v_cvt_pk_bf16_f32 v54, v68, v69\n\t"
    "v_mul_f32_e32 v70, v70, v74\n\t"
    "v_mfma_f32_16x16x32_bf16 v[56:59], v[244:247], v[132:135], v[56:59]\n\t"
    "v_mul_f32_e32 v71, v71, v74\n\t"
    "v_cvt_pk_bf16_f32 v55, v70, v71\n\t"
    "v_mfma_f32_16x16x32_bf16 v[56:59], v[236:239], v[136:139], v[56:59]\n\t"
    "s_sub_u32 s60, s60, 1\n\t"
    "s_cmp_lg_u32 s60, 0\n\t"
    "v_mfma_f32_16x16x32_bf16 v[56:59], v[228:231], v[140:143], v[56:59]\n\t"
    "v_cvt_pk_bf16_f32 v60, v76, v77\n\t"
    "v_cvt_pk_bf16_f32 v61, v78, v79\n\t"
    "v_mfma_f32_16x16x32_bf16 v[56:59], a[252:255], v[144:147], v[56:59]\n\t"
    "s_mov_b32 s63, s62\n\t"
    "s_mov_b32 s62, s64\n\t"
    "v_mfma_f32_16x16x32_bf16 v[56:59], a[244:247], v[148:151], v[56:59]\n\t"
    "s_cbranch_scc0 .Ltail_%=\n\t"
    "s_waitcnt lgkmcnt(0)\n\t"
    "v_mfma_f32_16x16x32_bf16 v[64:67], a[0:3], v[220:223], 0\n\t"
    "ds_write_b128 v6, v[44:47] offset:4096\n\t"
    "v_mfma_f32_16x16x32_bf16 v[64:67], a[8:11], v[216:219], v[64:67]\n\t"
    "v_mfma_f32_16x16x32_bf16 v[64:67], a[16:19], v[212:215], v[64:67]\n\t"
    "s_add_u32 m0, s63, 6144\n\t"
    "v_mfma_f32_16x16x32_bf16 v[64:67], a[24:27], v[28:31], v[64:67]\n\t"
    "buffer_load_dwordx4 v7, s[52:55], s61 offen offset:0 nt lds\n\t"
    "v_cvt_pk_bf16_f32 v62, v56, v57\n\t"
    "v_mfma_f32_16x16x32_bf16 v[64:67], a[32:35], v[32:35], v[64:67]\n\t"
    "ds_write_b128 v6, v[52:55] offset:5120\n\t"
    "v_mfma_f32_16x16x32_bf16 v[64:67], a[40:43], v[40:43], v[64:67]\n\t"
    "buffer_load_dwordx4 v7, s[52:55], s61 offen offset:1024 nt lds\n\t"
    "v_cvt_pk_bf16_f32 v63, v58, v59\n\t"
    "v_mfma_f32_16x16x32_bf16 v[12:15], v[208:211], v[36:39], v[12:15]\n\t"
    "v_mfma_f32_16x16x32_bf16 v[12:15], v[204:207], v[48:51], v[12:15]\n\t"
    "buffer_load_dwordx4 v7, s[52:55], s61 offen offset:2048 nt lds\n\t"
    "v_mfma_f32_16x16x32_bf16 v[12:15], v[200:203], v[60:63], v[12:15]\n\t"
    "s_add_u32 s61, s61, 196608\n\t"
    "v_mfma_f32_16x16x32_bf16 v[16:19], v[176:179], v[60:63], v[16:19]\n\t"
    "v_cvt_pk_bf16_f32 v44, v64, v65\n\t"
    "v_cvt_pk_bf16_f32 v45, v66, v67\n\t"
    "v_mfma_f32_16x16x32_bf16 v[16:19], v[180:183], v[48:51], v[16:19]\n\t"
    "s_waitcnt lgkmcnt(0)\n\t"
    "s_barrier\n\t"
    "v_mfma_f32_16x16x32_bf16 v[16:19], v[184:187], v[36:39], v[16:19]\n\t"
    "v_mfma_f32_16x16x32_bf16 v[20:23], v[160:163], v[36:39], v[20:23]\n\t"
    "v_max3_f32 v52, v12, v13, v14\n\t"
    "v_mfma_f32_16x16x32_bf16 v[20:23], v[156:159], v[48:51], v[20:23]\n\t"
    "v_mfma_f32_16x16x32_bf16 v[20:23], v[152:155], v[60:63], v[20:23]\n\t"
    "v_mfma_f32_16x16x32_bf16 v[24:27], v[128:131], v[60:63], v[24:27]\n\t"
    "v_max3_f32 v53, v15, v16, v17\n\t"
    "v_max3_f32 v52, v52, v18, v19\n\t"
    "v_mfma_f32_16x16x32_bf16 v[24:27], v[132:135], v[48:51], v[24:27]\n\t"
    "s_waitcnt vmcnt(9)\n\t"
    "v_mfma_f32_16x16x32_bf16 v[24:27], v[136:139], v[36:39], v[24:27]\n\t"
    "ds_read_b128 v[188:191], v2 offset:6144\n\t"
    "v_mfma_f32_16x16x32_bf16 v[56:59], a[44:47], v[40:43], 0\n\t"
    "v_max3_f32 v53, v53, v20, v21\n\t"
    "v_max3_f32 v52, v52, v22, v23\n\t"
    "v_mfma_f32_16x16x32_bf16 v[56:59], a[36:39], v[32:35], v[56:59]\n\t"
    "ds_read_b128 v[192:195], v1 offset:6144\n\t"
    "v_mfma_f32_16x16x32_bf16 v[56:59], a[28:31], v[28:31], v[56:59]\n\t"
    "v_mfma_f32_16x16x32_bf16 v[56:59], a[20:23], v[212:215], v[56:59]\n\t"
    "ds_read_b128 v[196:199], v0 offset:6144\n\t"
    "v_max3_f32 v53, v53, v24, v25\n\t"
    "v_mfma_f32_16x16x32_bf16 v[56:59], a[12:15], v[216:219], v[56:59]\n\t"
    "v_max3_f32 v52, v52, v26, v27\n\t"
    "v_max_f32_e32 v52, v52, v53\n\t"
    "v_mfma_f32_16x16x32_bf16 v[56:59], a[4:7], v[220:223], v[56:59]\n\t"
    "ds_read_b128 v[172:175], v0 offset:12288\n\t"
    "v_mov_b32_e32 v53, v52\n\t"
    "v_mfma_f32_16x16x32_bf16 v[36:39], a[48:51], v[220:223], 0\n\t"
    "v_mfma_f32_16x16x32_bf16 v[36:39], a[56:59], v[216:219], v[36:39]\n\t"
    "ds_read_b128 v[168:171], v1 offset:12288\n\t"
    "v_permlane32_swap_b32 v52, v53\n\t"
    "v_mfma_f32_16x16x32_bf16 v[36:39], a[64:67], v[212:215], v[36:39]\n\t"
    "v_max_f32_e32 v52, v52, v53\n\t"
    "v_mov_b32_e32 v53, v52\n\t"
    "v_mfma_f32_16x16x32_bf16 v[36:39], a[72:75], v[28:31], v[36:39]\n\t"
    "ds_read_b128 v[164:167], v2 offset:12288\n\t"
    "v_cvt_pk_bf16_f32 v46, v56, v57\n\t"
    "v_mfma_f32_16x16x32_bf16 v[36:39], a[80:83], v[32:35], v[36:39]\n\t"
    "v_cvt_pk_bf16_f32 v47, v58, v59\n\t"
    "v_permlane16_swap_b32 v52, v53\n\t"
    "v_mfma_f32_16x16x32_bf16 v[36:39], a[88:91], v[40:43], v[36:39]\n\t"
    "ds_read_b128 v[140:143], v2 offset:18432\n\t"
    "v_max_f32_e32 v52, v52, v53\n\t"
    "v_mfma_f32_16x16x32_bf16 v[48:51], a[92:95], v[40:43], 0\n\t"
    "v_sub_f32_e32 v12, v12, v52\n\t"
    "v_sub_f32_e32 v13, v13, v52\n\t"
    "v_mfma_f32_16x16x32_bf16 v[48:51], a[84:87], v[32:35], v[48:51]\n\t"
    "ds_read_b128 v[144:147], v1 offset:18432\n\t"
    "v_sub_f32_e32 v14, v14, v52\n\t"
    "v_mfma_f32_16x16x32_bf16 v[48:51], a[76:79], v[28:31], v[48:51]\n\t"
    "v_exp_f32_e32 v12, v12\n\t"
    "v_mfma_f32_16x16x32_bf16 v[48:51], a[68:71], v[212:215], v[48:51]\n\t"
    "ds_read_b128 v[148:151], v0 offset:18432\n\t"
    "v_cvt_pk_bf16_f32 v56, v36, v37\n\t"
    "v_mfma_f32_16x16x32_bf16 v[48:51], a[60:63], v[216:219], v[48:51]\n\t"
    "v_cvt_pk_bf16_f32 v57, v38, v39\n\t"
    "v_sub_f32_e32 v15, v15, v52\n\t"
    "v_mfma_f32_16x16x32_bf16 v[48:51], a[52:55], v[220:223], v[48:51]\n\t"
    "v_exp_f32_e32 v13, v13\n\t"
    "v_mfma_f32_16x16x32_bf16 v[60:63], a[96:99], v[220:223], 0\n\t"
    "v_add_f32_e32 v54, v12, v13\n\t"
    "v_sub_f32_e32 v16, v16, v52\n\t"
    "v_mfma_f32_16x16x32_bf16 v[60:63], a[104:107], v[216:219], v[60:63]\n\t"
    "v_exp_f32_e32 v14, v14\n\t"
    "v_mfma_f32_16x16x32_bf16 v[60:63], a[112:115], v[212:215], v[60:63]\n\t"
    "v_exp_f32_e32 v15, v15\n\t"
    "v_mfma_f32_16x16x32_bf16 v[60:63], a[120:123], v[28:31], v[60:63]\n\t"
    "v_cvt_pk_bf16_f32 v58, v48, v49\n\t"
    "v_cvt_pk_bf16_f32 v59, v50, v51\n\t"
    "v_mfma_f32_16x16x32_bf16 v[60:63], a[128:131], v[32:35], v[60:63]\n\t"
    "v_add_f32_e32 v55, v14, v15\n\t"
    "v_sub_f32_e32 v17, v17, v52\n\t"
    "v_mfma_f32_16x16x32_bf16 v[60:63], a[136:139], v[40:43], v[60:63]\n\t"
    "v_exp_f32_e32 v16, v16\n\t"
    "v_mfma_f32_16x16x32_bf16 v[36:39], a[140:143], v[40:43], 0\n\t"
    "v_add_f32_e32 v54, v54, v16\n\t"
    "v_sub_f32_e32 v18, v18, v52\n\t"
    "v_mfma_f32_16x16x32_bf16 v[36:39], a[132:135], v[32:35], v[36:39]\n\t"
    "v_exp_f32_e32 v17, v17\n\t"
    "v_mfma_f32_16x16x32_bf16 v[36:39], a[124:127], v[28:31], v[36:39]\n\t"
    "v_add_f32_e32 v54, v54, v17\n\t"
    "v_sub_f32_e32 v19, v19, v52\n\t"
    "v_mfma_f32_16x16x32_bf16 v[36:39], a[116:119], v[212:215], v[36:39]\n\t"
    "v_cvt_pk_bf16_f32 v48, v60, v61\n\t"
    "v_cvt_pk_bf16_f32 v49, v62, v63\n\t"
    "v_mfma_f32_16x16x32_bf16 v[36:39], a[108:111], v[216:219], v[36:39]\n\t"
    "v_exp_f32_e32 v18, v18\n\t"
    "v_mfma_f32_16x16x32_bf16 v[36:39], a[100:103], v[220:223], v[36:39]\n\t"
    "v_add_f32_e32 v55, v55, v18\n\t"
    "v_sub_f32_e32 v20, v20, v52\n\t"
    "v_mfma_f32_16x16x32_bf16 v[60:63], a[144:147], v[220:223], 0\n\t"
    "v_exp_f32_e32 v19, v19\n\t"
    "v_mfma_f32_16x16x32_bf16 v[60:63], a[152:155], v[216:219], v[60:63]\n\t"
    "v_add_f32_e32 v55, v55, v19\n\t"
    "v_exp_f32_e32 v20, v20\n\t"
    "v_mfma_f32_16x16x32_bf16 v[60:63], a[160:163], v[212:215], v[60:63]\n\t"
    "s_add_u32 m0, s63, 9216\n\t"
    "v_add_f32_e32 v54, v54, v20\n\t"
    "v_mfma_f32_16x16x32_bf16 v[60:63], a[168:171], v[28:31], v[60:63]\n\t"
    "buffer_load_dwordx4 v7, s[52:55], s61 offen offset:0 nt lds\n\t"
    "v_cvt_pk_bf16_f32 v50, v36, v37\n\t"
    "v_mfma_f32_16x16x32_bf16 v[60:63], a[176:179], v[32:35], v[60:63]\n\t"
    "v_cvt_pk_bf16_f32 v51, v38, v39\n\t"
    "v_sub_f32_e32 v21, v21, v52\n\t"
    "v_mfma_f32_16x16x32_bf16 v[60:63], a[184:187], v[40:43], v[60:63]\n\t"
    "buffer_load_dwordx4 v7, s[52:55], s61 offen offset:1024 nt lds\n\t"
    "v_sub_f32_e32 v22, v22, v52\n\t"
    "v_mfma_f32_16x16x32_bf16 v[64:67], v[220:223], v[44:47], 0\n\t"
    "v_exp_f32_e32 v21, v21\n\t"
    "v_mfma_f32_16x16x32_bf16 v[64:67], v[216:219], v[56:59], v[64:67]\n\t"
    "buffer_load_dwordx4 v7, s[52:55], s61 offen offset:2048 nt lds\n\t"
    "v_add_f32_e32 v54, v54, v21\n\t"
    "v_mfma_f32_16x16x32_bf16 v[64:67], v[212:215], v[48:51], v[64:67]\n\t"
    "s_add_u32 s61, s61, 196608\n\t"
    "s_waitcnt lgkmcnt(0)\n\t"
    "v_mfma_f32_16x16x32_bf16 v[36:39], v[188:191], v[48:51], 0\n\t"
    "v_exp_f32_e32 v22, v22\n\t"
    "v_mfma_f32_16x16x32_bf16 v[36:39], v[192:195], v[56:59], v[36:39]\n\t"
    "v_add_f32_e32 v55, v55, v22\n\t"
    "v_sub_f32_e32 v23, v23, v52\n\t"
    "v_mfma_f32_16x16x32_bf16 v[36:39], v[196:199], v[44:47], v[36:39]\n\t"
    "ds_read_b128 v[184:187], v3 offset:6144\n\t"
    "v_sub_f32_e32 v24, v24, v52\n\t"
    "v_mfma_f32_16x16x32_bf16 v[68:71], v[172:175], v[44:47], 0\n\t"
    "v_exp_f32_e32 v23, v23\n\t"
    "v_mfma_f32_16x16x32_bf16 v[68:71], v[168:171], v[56:59], v[68:71]\n\t"
    "ds_read_b128 v[180:183], v4 offset:6144\n\t"
    "v_add_f32_e32 v55, v55, v23\n\t"
    "v_mfma_f32_16x16x32_bf16 v[68:71], v[164:167], v[48:51], v[68:71]\n\t"
    "v_exp_f32_e32 v24, v24\n\t"
    "v_mfma_f32_16x16x32_bf16 v[72:75], v[140:143], v[48:51], 0\n\t"
    "ds_read_b128 v[176:179], v5 offset:6144\n\t"
    "v_add_f32_e32 v54, v54, v24\n\t"
    "v_mfma_f32_16x16x32_bf16 v[72:75], v[144:147], v[56:59], v[72:75]\n\t"
    "v_sub_f32_e32 v25, v25, v52\n\t"
    "v_sub_f32_e32 v26, v26, v52\n\t"
    "v_mfma_f32_16x16x32_bf16 v[72:75], v[148:151], v[44:47], v[72:75]\n\t"
    ".Lh5_%=:\n\t"
    "s_cbranch_execz .Lh2_%=\n\t"
    "ds_read_b128 v[208:211], v3 offset:0\n\t"
    "v_sub_f32_e32 v27, v27, v52\n\t"
    "v_mfma_f32_16x16x32_bf16 v[48:51], a[188:191], v[40:43], 0\n\t"
    "v_exp_f32_e32 v25, v25\n\t"
    "v_mfma_f32_16x16x32_bf16 v[48:51], a[180:183], v[32:35], v[48:51]\n\t"
    "ds_read_b128 v[204:207], v4 offset:0\n\t"
    "v_add_f32_e32 v54, v54, v25\n\t"
    "v_mfma_f32_16x16x32_bf16 v[48:51], a[172:175], v[28:31], v[48:51]\n\t"
    "v_exp_f32_e32 v26, v26\n\t"
    "v_mfma_f32_16x16x32_bf16 v[48:51], a[164:167], v[212:215], v[48:51]\n\t"
    "ds_read_b128 v[200:203], v5 offset:0\n\t"
    "v_add_f32_e32 v55, v55, v26\n\t"
    "v_mfma_f32_16x16x32_bf16 v[48:51], a[156:159], v[216:219], v[48:51]\n\t"
    "v_exp_f32_e32 v27, v27\n\t"
    "v_mfma_f32_16x16x32_bf16 v[48:51], a[148:151], v[220:223], v[48:51]\n\t"
    "ds_read_b128 v[160:163], v3 offset:12288\n\t"
    "v_add_f32_e32 v55, v55, v27\n\t"
    "v_mfma_f32_16x16x32_bf16 v[44:47], a[192:195], v[220:223], 0\n\t"
    "v_add_f32_e32 v54, v54, v55\n\t"
    "v_mov_b32_e32 v55, v54\n\t"
    "v_mfma_f32_16x16x32_bf16 v[44:47], a[200:203], v[216:219], v[44:47]\n\t"
    "ds_read_b128 v[156:159], v4 offset:12288\n\t"
    "v_cvt_pk_bf16_f32 v56, v60, v61\n\t"
    "v_mfma_f32_16x16x32_bf16 v[44:47], a[208:211], v[212:215], v[44:47]\n\t"
    "v_permlane32_swap_b32 v54, v55\n\t"
    "v_add_f32_e32 v54, v54, v55\n\t"
    "v_mfma_f32_16x16x32_bf16 v[44:47], a[216:219], v[28:31], v[44:47]\n\t"
    "ds_read_b128 v[152:155], v5 offset:12288\n\t"
    "v_cvt_pk_bf16_f32 v58, v48, v49\n\t"
    "v_mfma_f32_16x16x32_bf16 v[44:47], a[224:227], v[32:35], v[44:47]\n\t"
    "v_cvt_pk_bf16_f32 v59, v50, v51\n\t"
    "v_mov_b32_e32 v55, v54\n\t"
    "v_mfma_f32_16x16x32_bf16 v[44:47], a[232:235], v[40:43], v[44:47]\n\t"
    "ds_read_b128 v[128:131], v5 offset:18432\n\t"
    "v_cvt_pk_bf16_f32 v57, v62, v63\n\t"
    "v_mfma_f32_16x16x32_bf16 v[76:79], a[236:239], v[40:43], 0\n\t"
    "v_permlane16_swap_b32 v54, v55\n\t"
    "v_add_f32_e32 v54, v54, v55\n\t"
    "v_mfma_f32_16x16x32_bf16 v[76:79], a[228:231], v[32:35], v[76:79]\n\t"
    "ds_read_b128 v[132:135], v4 offset:18432\n\t"
    "v_mfma_f32_16x16x32_bf16 v[76:79], a[220:223], v[28:31], v[76:79]\n\t"
    "v_rcp_f32_e32 v54, v54\n\t"
    "v_mfma_f32_16x16x32_bf16 v[76:79], a[212:215], v[212:215], v[76:79]\n\t"
    "ds_read_b128 v[136:139], v3 offset:18432\n\t"
    "v_cvt_pk_bf16_f32 v48, v44, v45\n\t"
    "v_mfma_f32_16x16x32_bf16 v[76:79], a[204:207], v[216:219], v[76:79]\n\t"
    "v_cvt_pk_bf16_f32 v49, v46, v47\n\t"
    "v_mul_f32_e32 v12, v12, v54\n\t"
    "v_mfma_f32_16x16x32_bf16 v[76:79], a[196:199], v[220:223], v[76:79]\n\t"
    "v_mul_f32_e32 v13, v13, v54\n\t"
    "v_cvt_pk_bf16_f32 v60, v12, v13\n\t"
    "v_mfma_f32_16x16x32_bf16 v[80:83], a[240:243], v[220:223], 0\n\t"
    "v_mul_f32_e32 v14, v14, v54\n\t"
    "v_mul_f32_e32 v15, v15, v54\n\t"
    "v_mfma_f32_16x16x32_bf16 v[80:83], a[248:251], v[216:219], v[80:83]\n\t"
    "v_cvt_pk_bf16_f32 v61, v14, v15\n\t"
    "v_mul_f32_e32 v16, v16, v54\n\t"
    "v_mfma_f32_16x16x32_bf16 v[80:83], v[224:227], v[212:215], v[80:83]\n\t"
    "v_mul_f32_e32 v17, v17, v54\n\t"
    "v_cvt_pk_bf16_f32 v62, v16, v17\n\t"
    "v_mfma_f32_16x16x32_bf16 v[80:83], v[232:235], v[28:31], v[80:83]\n\t"
    "v_cvt_pk_bf16_f32 v50, v76, v77\n\t"
    "v_cvt_pk_bf16_f32 v51, v78, v79\n\t"
    "v_mfma_f32_16x16x32_bf16 v[80:83], v[240:243], v[32:35], v[80:83]\n\t"
    "v_mul_f32_e32 v18, v18, v54\n\t"
    "v_mul_f32_e32 v19, v19, v54\n\t"
    "v_mfma_f32_16x16x32_bf16 v[80:83], v[248:251], v[40:43], v[80:83]\n\t"
    "v_cvt_pk_bf16_f32 v63, v18, v19\n\t"
    "v_mul_f32_e32 v20, v20, v54\n\t"
    "v_mfma_f32_16x16x32_bf16 v[12:15], v[252:255], v[40:43], 0\n\t"
    "v_mul_f32_e32 v21, v21, v54\n\t"
    "v_cvt_pk_bf16_f32 v44, v20, v21\n\t"
    "v_mfma_f32_16x16x32_bf16 v[12:15], v[244:247], v[32:35], v[12:15]\n\t"
    "v_mul_f32_e32 v22, v22, v54\n\t"
    "v_mul_f32_e32 v23, v23, v54\n\t"
    "v_mfma_f32_16x16x32_bf16 v[12:15], v[236:239], v[28:31], v[12:15]\n\t"
    "v_cvt_pk_bf16_f32 v45, v22, v23\n\t"
    "v_mul_f32_e32 v24, v24, v54\n\t"
    "v_mfma_f32_16x16x32_bf16 v[12:15], v[228:231], v[212:215], v[12:15]\n\t"
    "v_cvt_pk_bf16_f32 v16, v80, v81\n\t"
    "v_cvt_pk_bf16_f32 v17, v82, v83\n\t"
    "v_mfma_f32_16x16x32_bf16 v[12:15], a[252:255], v[216:219], v[12:15]\n\t"
    "v_mul_f32_e32 v25, v25, v54\n\t"
    "v_cvt_pk_bf16_f32 v46, v24, v25\n\t"
    "v_mfma_f32_16x16x32_bf16 v[12:15], a[244:247], v[220:223], v[12:15]\n\t"
    "v_mul_f32_e32 v26, v26, v54\n\t"
    "v_mul_f32_e32 v27, v27, v54\n\t"
    "v_mfma_f32_16x16x32_bf16 v[20:23], a[0:3], v[196:199], 0\n\t"
    "ds_write_b128 v6, v[60:63] offset:6144\n\t"
    "v_mfma_f32_16x16x32_bf16 v[20:23], a[8:11], v[192:195], v[20:23]\n\t"
    "v_cvt_pk_bf16_f32 v47, v26, v27\n\t"
    "v_mfma_f32_16x16x32_bf16 v[20:23], a[16:19], v[188:191], v[20:23]\n\t"
    "s_add_u32 m0, s63, 12288\n\t"
    "s_waitcnt lgkmcnt(1)\n\t"
    "v_mfma_f32_16x16x32_bf16 v[20:23], a[24:27], v[184:187], v[20:23]\n\t"
    "buffer_load_dwordx4 v7, s[52:55], s61 offen offset:0 nt lds\n\t"
    "v_cvt_pk_bf16_f32 v18, v12, v13\n\t"
    "v_mfma_f32_16x16x32_bf16 v[20:23], a[32:35], v[180:183], v[20:23]\n\t"
    "ds_write_b128 v6, v[44:47] offset:7168\n\t"
    "v_mfma_f32_16x16x32_bf16 v[20:23], a[40:43], v[176:179], v[20:23]\n\t"
    "buffer_load_dwordx4 v7, s[52:55], s61 offen offset:1024 nt lds\n\t"
    "v_cvt_pk_bf16_f32 v19, v14, v15\n\t"
    "v_mfma_f32_16x16x32_bf16 v[64:67], v[208:211], v[56:59], v[64:67]\n\t"
    "v_mfma_f32_16x16x32_bf16 v[64:67], v[204:207], v[48:51], v[64:67]\n\t"
    "buffer_load_dwordx4 v7, s[52:55], s61 offen offset:2048 nt lds\n\t"
    "v_mfma_f32_16x16x32_bf16 v[64:67], v[200:203], v[16:19], v[64:67]\n\t"
    "s_add_u32 s61, s61, 196608\n\t"
    "v_mfma_f32_16x16x32_bf16 v[36:39], v[176:179], v[16:19], v[36:39]\n\t"
    "v_cvt_pk_bf16_f32 v12, v20, v21\n\t"
    "v_cvt_pk_bf16_f32 v13, v22, v23\n\t"
    "v_mfma_f32_16x16x32_bf16 v[36:39], v[180:183], v[48:51], v[36:39]\n\t"
    "s_waitcnt lgkmcnt(0)\n\t"
    "s_barrier\n\t"
    "v_mfma_f32_16x16x32_bf16 v[36:39], v[184:187], v[56:59], v[36:39]\n\t"
    "v_mfma_f32_16x16x32_bf16 v[68:71], v[160:163], v[56:59], v[68:71]\n\t"
    "v_max3_f32 v20, v64, v65, v66\n\t"
    "v_mfma_f32_16x16x32_bf16 v[68:71], v[156:159], v[48:51], v[68:71]\n\t"
    "v_mfma_f32_16x16x32_bf16 v[68:71], v[152:155], v[16:19], v[68:71]\n\t"
    "v_mfma_f32_16x16x32_bf16 v[72:75], v[128:131], v[16:19], v[72:75]\n\t"
    "v_max3_f32 v21, v67, v36, v37\n\t"
    "v_max3_f32 v20, v20, v38, v39\n\t"
    "v_mfma_f32_16x16x32_bf16 v[72:75], v[132:135], v[48:51], v[72:75]\n\t"
    "v_mfma_f32_16x16x32_bf16 v[72:75], v[136:139], v[56:59], v[72:75]\n\t"
    "v_mfma_f32_16x16x32_bf16 v[16:19], a[44:47], v[176:179], 0\n\t"
    "v_max3_f32 v21, v21, v68, v69\n\t"
    "v_max3_f32 v20, v20, v70, v71\n\t"
    "v_mfma_f32_16x16x32_bf16 v[16:19], a[36:39], v[180:183], v[16:19]\n\t"
    "v_mfma_f32_16x16x32_bf16 v[16:19], a[28:31], v[184:187], v[16:19]\n\t"
    "v_mfma_f32_16x16x32_bf16 v[16:19], a[20:23], v[188:191], v[16:19]\n\t"
    "v_max3_f32 v21, v21, v72, v73\n\t"
    "v_max3_f32 v20, v20, v74, v75\n\t"
    "v_mfma_f32_16x16x32_bf16 v[16:19], a[12:15], v[192:195], v[16:19]\n\t"
    "v_max_f32_e32 v20, v20, v21\n\t"
    "v_mov_b32_e32 v21, v20\n\t"
    "v_mfma_f32_16x16x32_bf16 v[16:19], a[4:7], v[196:199], v[16:19]\n\t"
    "v_mfma_f32_16x16x32_bf16 v[24:27], a[48:51], v[196:199], 0\n\t"
    "v_permlane32_swap_b32 v20, v21\n\t"
    "v_max_f32_e32 v20, v20, v21\n\t"
    "v_mfma_f32_16x16x32_bf16 v[24:27], a[56:59], v[192:195], v[24:27]\n\t"
    "v_mov_b32_e32 v21, v20\n\t"
    "v_mfma_f32_16x16x32_bf16 v[24:27], a[64:67], v[188:191], v[24:27]\n\t"
    "v_mfma_f32_16x16x32_bf16 v[24:27], a[72:75], v[184:187], v[24:27]\n\t"
    "v_cvt_pk_bf16_f32 v14, v16, v17\n\t"
    "v_cvt_pk_bf16_f32 v15, v18, v19\n\t"
    "v_mfma_f32_16x16x32_bf16 v[24:27], a[80:83], v[180:183], v[24:27]\n\t"
    "v_permlane16_swap_b32 v20, v21\n\t"
    "v_max_f32_e32 v20, v20, v21\n\t"
    "v_mfma_f32_16x16x32_bf16 v[24:27], a[88:91], v[176:179], v[24:27]\n\t"
    "v_sub_f32_e32 v64, v64, v20\n\t"
    "v_sub_f32_e32 v65, v65, v20\n\t"
    "v_mfma_f32_16x16x32_bf16 v[16:19], a[92:95], v[176:179], 0\n\t"
    "v_exp_f32_e32 v64, v64\n\t"
    "v_mfma_f32_16x16x32_bf16 v[16:19], a[84:87], v[180:183], v[16:19]\n\t"
    "v_exp_f32_e32 v65, v65\n\t"
    "v_mfma_f32_16x16x32_bf16 v[16:19], a[76:79], v[184:187], v[16:19]\n\t"
    "v_add_f32_e32 v22, v64, v65\n\t"
    "v_sub_f32_e32 v66, v66, v20\n\t"
    "v_mfma_f32_16x16x32_bf16 v[16:19], a[68:71], v[188:191], v[16:19]\n\t"
    "v_cvt_pk_bf16_f32 v28, v24, v25\n\t"
    "v_cvt_pk_bf16_f32 v29, v26, v27\n\t"
    "v_mfma_f32_16x16x32_bf16 v[16:19], a[60:63], v[192:195], v[16:19]\n\t"
    "v_exp_f32_e32 v66, v66\n\t"
    "v_mfma_f32_16x16x32_bf16 v[16:19], a[52:55], v[196:199], v[16:19]\n\t"
    "v_sub_f32_e32 v67, v67, v20\n\t"
    "v_sub_f32_e32 v36, v36, v20\n\t"
    "v_mfma_f32_16x16x32_bf16 v[24:27], a[96:99], v[196:199], 0\n\t"
    "v_exp_f32_e32 v67, v67\n\t"
    "v_mfma_f32_16x16x32_bf16 v[24:27], a[104:107], v[192:195], v[24:27]\n\t"
    "v_add_f32_e32 v23, v66, v67\n\t"
    "v_sub_f32_e32 v37, v37, v20\n\t"
    "v_mfma_f32_16x16x32_bf16 v[24:27], a[112:115], v[188:191], v[24:27]\n\t"
    "v_exp_f32_e32 v36, v36\n\t"
    "v_mfma_f32_16x16x32_bf16 v[24:27], a[120:123], v[184:187], v[24:27]\n\t"
    "v_cvt_pk_bf16_f32 v30, v16, v17\n\t"
    "v_cvt_pk_bf16_f32 v31, v18, v19\n\t"
    "v_mfma_f32_16x16x32_bf16 v[24:27], a[128:131], v[180:183], v[24:27]\n\t"
    "v_add_f32_e32 v22, v22, v36\n\t"
    "v_sub_f32_e32 v38, v38, v20\n\t"
    "v_mfma_f32_16x16x32_bf16 v[24:27], a[136:139], v[176:179], v[24:27]\n\t"
    "v_exp_f32_e32 v37, v37\n\t"
    "v_mfma_f32_16x16x32_bf16 v[16:19], a[140:143], v[176:179], 0\n\t"
    "v_add_f32_e32 v22, v22, v37\n\t"
    "v_sub_f32_e32 v39, v39, v20\n\t"
    "v_mfma_f32_16x16x32_bf16 v[16:19], a[132:135], v[180:183], v[16:19]\n\t"
    "v_exp_f32_e32 v38, v38\n\t"
    "v_mfma_f32_16x16x32_bf16 v[16:19], a[124:127], v[184:187], v[16:19]\n\t"
    "v_add_f32_e32 v23, v23, v38\n\t"
    "v_sub_f32_e32 v68, v68, v20\n\t"
    "v_mfma_f32_16x16x32_bf16 v[16:19], a[116:119], v[188:191], v[16:19]\n\t"
    "v_cvt_pk_bf16_f32 v32, v24, v25\n\t"
    "v_cvt_pk_bf16_f32 v33, v26, v27\n\t"
    "v_mfma_f32_16x16x32_bf16 v[16:19], a[108:111], v[192:195], v[16:19]\n\t"
    "v_exp_f32_e32 v39, v39\n\t"
    "v_mfma_f32_16x16x32_bf16 v[16:19], a[100:103], v[196:199], v[16:19]\n\t"
    "v_add_f32_e32 v23, v23, v39\n\t"
    "v_sub_f32_e32 v69, v69, v20\n\t"
    "v_mfma_f32_16x16x32_bf16 v[24:27], a[144:147], v[196:199], 0\n\t"
    "v_exp_f32_e32 v68, v68\n\t"
    "v_mfma_f32_16x16x32_bf16 v[24:27], a[152:155], v[192:195], v[24:27]\n\t"
    "v_add_f32_e32 v22, v22, v68\n\t"
    "v_sub_f32_e32 v70, v70, v20\n\t"
    "v_mfma_f32_16x16x32_bf16 v[24:27], a[160:163], v[188:191], v[24:27]\n\t"
    "s_add_u32 m0, s63, 15360\n\t"
    "v_sub_f32_e32 v71, v71, v20\n\t"
    "v_mfma_f32_16x16x32_bf16 v[24:27], a[168:171], v[184:187], v[24:27]\n\t"
    "buffer_load_dwordx4 v7, s[52:55], s61 offen offset:0 nt lds\n\t"
    "v_cvt_pk_bf16_f32 v34, v16, v17\n\t"
    "v_mfma_f32_16x16x32_bf16 v[24:27], a[176:179], v[180:183], v[24:27]\n\t"
    "v_cvt_pk_bf16_f32 v35, v18, v19\n\t"
    "v_sub_f32_e32 v72, v72, v20\n\t"
    "v_mfma_f32_16x16x32_bf16 v[24:27], a[184:187], v[176:179], v[24:27]\n\t"
    "buffer_load_dwordx4 v7, s[52:55], s61 offen offset:1024 nt lds\n\t"
    "v_sub_f32_e32 v73, v73, v20\n\t"
    "v_mfma_f32_16x16x32_bf16 v[40:43], v[220:223], v[12:15], 0\n\t"
    "v_exp_f32_e32 v69, v69\n\t"
    "v_mfma_f32_16x16x32_bf16 v[40:43], v[216:219], v[28:31], v[40:43]\n\t"
    "buffer_load_dwordx4 v7, s[52:55], s61 offen offset:2048 nt lds\n\t"
    "v_add_f32_e32 v22, v22, v69\n\t"
    "v_mfma_f32_16x16x32_bf16 v[40:43], v[212:215], v[32:35], v[40:43]\n\t"
    "s_add_u32 s61, s61, 196608\n\t"
    "v_sub_f32_e32 v74, v74, v20\n\t"
    "v_mfma_f32_16x16x32_bf16 v[16:19], v[188:191], v[32:35], 0\n\t"
    "v_exp_f32_e32 v70, v70\n\t"
    "v_mfma_f32_16x16x32_bf16 v[16:19], v[192:195], v[28:31], v[16:19]\n\t"
    "v_add_f32_e32 v23, v23, v70\n\t"
    "v_sub_f32_e32 v75, v75, v20\n\t"
    "v_mfma_f32_16x16x32_bf16 v[16:19], v[196:199], v[12:15], v[16:19]\n\t"
    "v_exp_f32_e32 v71, v71\n\t"
    "v_mfma_f32_16x16x32_bf16 v[44:47], v[172:175], v[12:15], 0\n\t"
    "v_add_f32_e32 v23, v23, v71\n\t"
    "v_cvt_pk_bf16_f32 v48, v24, v25\n\t"
    "v_mfma_f32_16x16x32_bf16 v[44:47], v[168:171], v[28:31], v[44:47]\n\t"
    "v_exp_f32_e32 v72, v72\n\t"
    "v_mfma_f32_16x16x32_bf16 v[44:47], v[164:167], v[32:35], v[44:47]\n\t"
    "v_add_f32_e32 v22, v22, v72\n\t"
    "v_cvt_pk_bf16_f32 v49, v26, v27\n\t"
    "v_mfma_f32_16x16x32_bf16 v[52:55], v[140:143], v[32:35], 0\n\t"
    "v_exp_f32_e32 v73, v73\n\t"
    "v_mfma_f32_16x16x32_bf16 v[52:55], v[144:147], v[28:31], v[52:55]\n\t"
    "v_add_f32_e32 v22, v22, v73\n\t"
    "v_mfma_f32_16x16x32_bf16 v[52:55], v[148:151], v[12:15], v[52:55]\n\t"
    "v_exp_f32_e32 v74, v74\n\t"
    "v_mfma_f32_16x16x32_bf16 v[24:27], a[188:191], v[176:179], 0\n\t"
    "v_add_f32_e32 v23, v23, v74\n\t"
    "v_mfma_f32_16x16x32_bf16 v[24:27], a[180:183], v[180:183], v[24:27]\n\t"
    "v_exp_f32_e32 v75, v75\n\t"
    "v_mfma_f32_16x16x32_bf16 v[24:27], a[172:175], v[184:187], v[24:27]\n\t"
    "v_add_f32_e32 v23, v23, v75\n\t"
    "v_add_f32_e32 v22, v22, v23\n\t"
    "v_mfma_f32_16x16x32_bf16 v[24:27], a[164:167], v[188:191], v[24:27]\n\t"
    "v_mov_b32_e32 v23, v22\n\t"
    "v_mfma_f32_16x16x32_bf16 v[24:27], a[156:159], v[192:195], v[24:27]\n\t"
    "v_mfma_f32_16x16x32_bf16 v[24:27], a[148:151], v[196:199], v[24:27]\n\t"
    "v_permlane32_swap_b32 v22, v23\n\t"
    "v_add_f32_e32 v22, v22, v23\n\t"
    "v_mfma_f32_16x16x32_bf16 v[12:15], a[192:195], v[196:199], 0\n\t"
    "v_mov_b32_e32 v23, v22\n\t"
    "v_mfma_f32_16x16x32_bf16 v[12:15], a[200:203], v[192:195], v[12:15]\n\t"
    "v_mfma_f32_16x16x32_bf16 v[12:15], a[208:211], v[188:191], v[12:15]\n\t"
    "v_permlane16_swap_b32 v22, v23\n\t"
    "v_add_f32_e32 v22, v22, v23\n\t"
    "v_mfma_f32_16x16x32_bf16 v[12:15], a[216:219], v[184:187], v[12:15]\n\t"
    "v_cvt_pk_bf16_f32 v50, v24, v25\n\t"
    "v_cvt_pk_bf16_f32 v51, v26, v27\n\t"
    "v_mfma_f32_16x16x32_bf16 v[12:15], a[224:227], v[180:183], v[12:15]\n\t"
    "v_rcp_f32_e32 v22, v22\n\t"
    "v_mfma_f32_16x16x32_bf16 v[12:15], a[232:235], v[176:179], v[12:15]\n\t"
    "v_mul_f32_e32 v64, v64, v22\n\t"
    "v_mul_f32_e32 v65, v65, v22\n\t"
    "v_mfma_f32_16x16x32_bf16 v[28:31], a[236:239], v[176:179], 0\n\t"
    "v_cvt_pk_bf16_f32 v24, v64, v65\n\t"
    "v_mul_f32_e32 v66, v66, v22\n\t"
    "v_mfma_f32_16x16x32_bf16 v[28:31], a[228:231], v[180:183], v[28:31]\n\t"
    "v_mul_f32_e32 v67, v67, v22\n\t"
    "v_cvt_pk_bf16_f32 v25, v66, v67\n\t"
    "v_mfma_f32_16x16x32_bf16 v[28:31], a[220:223], v[184:187], v[28:31]\n\t"
    "v_mul_f32_e32 v36, v36, v22\n\t"
    "v_mul_f32_e32 v37, v37, v22\n\t"
    "v_mfma_f32_16x16x32_bf16 v[28:31], a[212:215], v[188:191], v[28:31]\n\t"
    "v_cvt_pk_bf16_f32 v32, v12, v13\n\t"
    "v_cvt_pk_bf16_f32 v33, v14, v15\n\t"
    "v_mfma_f32_16x16x32_bf16 v[28:31], a[204:207], v[192:195], v[28:31]\n\t"
    "v_cvt_pk_bf16_f32 v26, v36, v37\n\t"
    "v_mul_f32_e32 v38, v38, v22\n\t"
    "v_mfma_f32_16x16x32_bf16 v[28:31], a[196:199], v[196:199], v[28:31]\n\t"
    "v_mul_f32_e32 v39, v39, v22\n\t"
    "v_cvt_pk_bf16_f32 v27, v38, v39\n\t"
    "v_mfma_f32_16x16x32_bf16 v[12:15], a[240:243], v[196:199], 0\n\t"
    "v_mul_f32_e32 v68, v68, v22\n\t"
    "v_mul_f32_e32 v69, v69, v22\n\t"
    "v_mfma_f32_16x16x32_bf16 v[12:15], a[248:251], v[192:195], v[12:15]\n\t"
    "v_cvt_pk_bf16_f32 v56, v68, v69\n\t"
    "v_mul_f32_e32 v70, v70, v22\n\t"
    "v_mfma_f32_16x16x32_bf16 v[12:15], v[224:227], v[188:191], v[12:15]\n\t"
    "v_mul_f32_e32 v71, v71, v22\n\t"
    "v_cvt_pk_bf16_f32 v57, v70, v71\n\t"
    "v_mfma_f32_16x16x32_bf16 v[12:15], v[232:235], v[184:187], v[12:15]\n\t"
    "v_cvt_pk_bf16_f32 v34, v28, v29\n\t"
    "v_cvt_pk_bf16_f32 v35, v30, v31\n\t"
    "v_mfma_f32_16x16x32_bf16 v[12:15], v[240:243], v[180:183], v[12:15]\n\t"
    "v_mul_f32_e32 v72, v72, v22\n\t"
    "v_mul_f32_e32 v73, v73, v22\n\t"
    "v_mfma_f32_16x16x32_bf16 v[12:15], v[248:251], v[176:179], v[12:15]\n\t"
    "v_cvt_pk_bf16_f32 v58, v72, v73\n\t"
    "v_mul_f32_e32 v74, v74, v22\n\t"
    "v_mfma_f32_16x16x32_bf16 v[28:31], v[252:255], v[176:179], 0\n\t"
    "v_mul_f32_e32 v75, v75, v22\n\t"
    "v_cvt_pk_bf16_f32 v59, v74, v75\n\t"
    "v_mfma_f32_16x16x32_bf16 v[28:31], v[244:247], v[180:183], v[28:31]\n\t"
    "v_mfma_f32_16x16x32_bf16 v[28:31], v[236:239], v[184:187], v[28:31]\n\t"
    "v_mfma_f32_16x16x32_bf16 v[28:31], v[228:231], v[188:191], v[28:31]\n\t"
    "v_cvt_pk_bf16_f32 v20, v12, v13\n\t"
    "v_cvt_pk_bf16_f32 v21, v14, v15\n\t"
    "v_mfma_f32_16x16x32_bf16 v[28:31], a[252:255], v[192:195], v[28:31]\n\t"
    "v_mfma_f32_16x16x32_bf16 v[28:31], a[244:247], v[196:199], v[28:31]\n\t"
    "v_mfma_f32_16x16x32_bf16 v[12:15], a[0:3], v[172:175], 0\n\t"
    "ds_write_b128 v6, v[24:27] offset:0\n\t"
    "v_mfma_f32_16x16x32_bf16 v[12:15], a[8:11], v[168:171], v[12:15]\n\t"
    "v_mfma_f32_16x16x32_bf16 v[12:15], a[16:19], v[164:167], v[12:15]\n\t"
    "s_add_u32 m0, s63, 18432\n\t"
    "v_mfma_f32_16x16x32_bf16 v[12:15], a[24:27], v[160:163], v[12:15]\n\t"
    "buffer_load_dwordx4 v7, s[52:55], s61 offen offset:0 nt lds\n\t"
    "v_cvt_pk_bf16_f32 v22, v28, v29\n\t"
    "v_mfma_f32_16x16x32_bf16 v[12:15], a[32:35], v[156:159], v[12:15]\n\t"
    "ds_write_b128 v6, v[56:59] offset:1024\n\t"
    "v_mfma_f32_16x16x32_bf16 v[12:15], a[40:43], v[152:155], v[12:15]\n\t"
    "buffer_load_dwordx4 v7, s[52:55], s61 offen offset:1024 nt lds\n\t"
    "v_cvt_pk_bf16_f32 v23, v30, v31\n\t"
    "v_mfma_f32_16x16x32_bf16 v[40:43], v[208:211], v[48:51], v[40:43]\n\t"
    "v_mfma_f32_16x16x32_bf16 v[40:43], v[204:207], v[32:35], v[40:43]\n\t"
    "buffer_load_dwordx4 v7, s[52:55], s61 offen offset:2048 nt lds\n\t"
    "v_mfma_f32_16x16x32_bf16 v[40:43], v[200:203], v[20:23], v[40:43]\n\t"
    "s_add_u32 s61, s61, 196608\n\t"
    "s_add_i32 s64, s62, 24576\n\t"
    "v_mfma_f32_16x16x32_bf16 v[16:19], v[176:179], v[20:23], v[16:19]\n\t"
    "s_cmp_eq_u32 s64, %[rend]\n\t"
    "s_cselect_b32 s64, %[rbeg], s64\n\t"
    "v_mfma_f32_16x16x32_bf16 v[16:19], v[180:183], v[32:35], v[16:19]\n\t"
    "s_sub_i32 s65, s63, s64\n\t"
    "v_cvt_pk_bf16_f32 v24, v12, v13\n\t"
    "v_mfma_f32_16x16x32_bf16 v[16:19], v[184:187], v[48:51], v[16:19]\n\t"
    "v_add_u32_e32 v0, s65, v0\n\t"
    "v_add_u32_e32 v1, s65, v1\n\t"
    "v_mfma_f32_16x16x32_bf16 v[44:47], v[160:163], v[48:51], v[44:47]\n\t"
    "v_add_u32_e32 v2, s65, v2\n\t"
    "v_add_u32_e32 v3, s65, v3\n\t"
    "v_mfma_f32_16x16x32_bf16 v[44:47], v[156:159], v[32:35], v[44:47]\n\t"
    "v_add_u32_e32 v4, s65, v4\n\t"
    "v_add_u32_e32 v5, s65, v5\n\t"
    "v_mfma_f32_16x16x32_bf16 v[44:47], v[152:155], v[20:23], v[44:47]\n\t"
    "v_max3_f32 v28, v40, v41, v42\n\t"
    "v_cvt_pk_bf16_f32 v25, v14, v15\n\t"
    "v_mfma_f32_16x16x32_bf16 v[52:55], v[128:131], v[20:23], v[52:55]\n\t"
    "v_max3_f32 v29, v43, v16, v17\n\t"
    "v_max3_f32 v28, v28, v18, v19\n\t"
    "v_mfma_f32_16x16x32_bf16 v[52:55], v[132:135], v[32:35], v[52:55]\n\t"
    "v_mfma_f32_16x16x32_bf16 v[52:55], v[136:139], v[48:51], v[52:55]\n\t"
    "v_mfma_f32_16x16x32_bf16 v[12:15], a[44:47], v[152:155], 0\n\t"
    "v_max3_f32 v29, v29, v44, v45\n\t"
    "v_max3_f32 v28, v28, v46, v47\n\t"
    "v_mfma_f32_16x16x32_bf16 v[12:15], a[36:39], v[156:159], v[12:15]\n\t"
    "v_mfma_f32_16x16x32_bf16 v[12:15], a[28:31], v[160:163], v[12:15]\n\t"
    "v_mfma_f32_16x16x32_bf16 v[12:15], a[20:23], v[164:167], v[12:15]\n\t"
    "v_max3_f32 v29, v29, v52, v53\n\t"
    "v_max3_f32 v28, v28, v54, v55\n\t"
    "v_mfma_f32_16x16x32_bf16 v[12:15], a[12:15], v[168:171], v[12:15]\n\t"
    "v_max_f32_e32 v28, v28, v29\n\t"
    "v_mov_b32_e32 v29, v28\n\t"
    "v_mfma_f32_16x16x32_bf16 v[12:15], a[4:7], v[172:175], v[12:15]\n\t"
    "v_mfma_f32_16x16x32_bf16 v[20:23], a[48:51], v[172:175], 0\n\t"
    "v_permlane32_swap_b32 v28, v29\n\t"
    "v_max_f32_e32 v28, v28, v29\n\t"
    "v_mfma_f32_16x16x32_bf16 v[20:23], a[56:59], v[168:171], v[20:23]\n\t"
    "v_mov_b32_e32 v29, v28\n\t"
    "v_mfma_f32_16x16x32_bf16 v[20:23], a[64:67], v[164:167], v[20:23]\n\t"
    "v_mfma_f32_16x16x32_bf16 v[20:23], a[72:75], v[160:163], v[20:23]\n\t"
    "v_cvt_pk_bf16_f32 v26, v12, v13\n\t"
    "v_cvt_pk_bf16_f32 v27, v14, v15\n\t"
    "v_mfma_f32_16x16x32_bf16 v[20:23], a[80:83], v[156:159], v[20:23]\n\t"
    "v_permlane16_swap_b32 v28, v29\n\t"
    "v_max_f32_e32 v28, v28, v29\n\t"
    "v_mfma_f32_16x16x32_bf16 v[20:23], a[88:91], v[152:155], v[20:23]\n\t"
    "v_sub_f32_e32 v40, v40, v28\n\t"
    "v_sub_f32_e32 v41, v41, v28\n\t"
    "v_mfma_f32_16x16x32_bf16 v[12:15], a[92:95], v[152:155], 0\n\t"
    "v_exp_f32_e32 v40, v40\n\t"
    "v_mfma_f32_16x16x32_bf16 v[12:15], a[84:87], v[156:159], v[12:15]\n\t"
    "v_exp_f32_e32 v41, v41\n\t"
    "v_mfma_f32_16x16x32_bf16 v[12:15], a[76:79], v[160:163], v[12:15]\n\t"
    "v_add_f32_e32 v30, v40, v41\n\t"
    "v_sub_f32_e32 v42, v42, v28\n\t"
    "v_mfma_f32_16x16x32_bf16 v[12:15], a[68:71], v[164:167], v[12:15]\n\t"
    "v_cvt_pk_bf16_f32 v32, v20, v21\n\t"
    "v_cvt_pk_bf16_f32 v33, v22, v23\n\t"
    "v_mfma_f32_16x16x32_bf16 v[12:15], a[60:63], v[168:171], v[12:15]\n\t"
    "v_exp_f32_e32 v42, v42\n\t"
    "v_mfma_f32_16x16x32_bf16 v[12:15], a[52:55], v[172:175], v[12:15]\n\t"
    "v_sub_f32_e32 v43, v43, v28\n\t"
    "v_sub_f32_e32 v16, v16, v28\n\t"
    "v_mfma_f32_16x16x32_bf16 v[20:23], a[96:99], v[172:175], 0\n\t"
    "v_exp_f32_e32 v43, v43\n\t"
    "v_mfma_f32_16x16x32_bf16 v[20:23], a[104:107], v[168:171], v[20:23]\n\t"
    "v_add_f32_e32 v31, v42, v43\n\t"
    "v_sub_f32_e32 v17, v17, v28\n\t"
    "v_mfma_f32_16x16x32_bf16 v[20:23], a[112:115], v[164:167], v[20:23]\n\t"
    "v_exp_f32_e32 v16, v16\n\t"
    "v_mfma_f32_16x16x32_bf16 v[20:23], a[120:123], v[160:163], v[20:23]\n\t"
    "v_cvt_pk_bf16_f32 v34, v12, v13\n\t"
    "v_cvt_pk_bf16_f32 v35, v14, v15\n\t"
    "v_mfma_f32_16x16x32_bf16 v[20:23], a[128:131], v[156:159], v[20:23]\n\t"
    "v_add_f32_e32 v30, v30, v16\n\t"
    "v_sub_f32_e32 v18, v18, v28\n\t"
    "v_mfma_f32_16x16x32_bf16 v[20:23], a[136:139], v[152:155], v[20:23]\n\t"
    "v_exp_f32_e32 v17, v17\n\t"
    "v_mfma_f32_16x16x32_bf16 v[12:15], a[140:143], v[152:155], 0\n\t"
    "v_add_f32_e32 v30, v30, v17\n\t"
    "v_sub_f32_e32 v19, v19, v28\n\t"
    "v_mfma_f32_16x16x32_bf16 v[12:15], a[132:135], v[156:159], v[12:15]\n\t"
    "v_exp_f32_e32 v18, v18\n\t"
    "v_mfma_f32_16x16x32_bf16 v[12:15], a[124:127], v[160:163], v[12:15]\n\t"
    "v_add_f32_e32 v31, v31, v18\n\t"
    "v_sub_f32_e32 v44, v44, v28\n\t"
    "v_mfma_f32_16x16x32_bf16 v[12:15], a[116:119], v[164:167], v[12:15]\n\t"
    "v_cvt_pk_bf16_f32 v36, v20, v21\n\t"
    "v_cvt_pk_bf16_f32 v37, v22, v23\n\t"
    "v_mfma_f32_16x16x32_bf16 v[12:15], a[108:111], v[168:171], v[12:15]\n\t"
    "v_exp_f32_e32 v19, v19\n\t"
    "v_mfma_f32_16x16x32_bf16 v[12:15], a[100:103], v[172:175], v[12:15]\n\t"
    "v_add_f32_e32 v31, v31, v19\n\t"
    "v_sub_f32_e32 v45, v45, v28\n\t"
    "v_mfma_f32_16x16x32_bf16 v[20:23], a[144:147], v[172:175], 0\n\t"
    "v_exp_f32_e32 v44, v44\n\t"
    "v_mfma_f32_16x16x32_bf16 v[20:23], a[152:155], v[168:171], v[20:23]\n\t"
    "v_add_f32_e32 v30, v30, v44\n\t"
    "v_sub_f32_e32 v46, v46, v28\n\t"
    "v_mfma_f32_16x16x32_bf16 v[20:23], a[160:163], v[164:167], v[20:23]\n\t"
    "s_add_u32 m0, s63, 21504\n\t"
    "v_sub_f32_e32 v47, v47, v28\n\t"
    "v_mfma_f32_16x16x32_bf16 v[20:23], a[168:171], v[160:163], v[20:23]\n\t"
    "buffer_load_dwordx4 v7, s[52:55], s61 offen offset:0 nt lds\n\t"
    "v_cvt_pk_bf16_f32 v38, v12, v13\n\t"
    "v_mfma_f32_16x16x32_bf16 v[20:23], a[176:179], v[156:159], v[20:23]\n\t"
    "v_cvt_pk_bf16_f32 v39, v14, v15\n\t"
    "v_sub_f32_e32 v52, v52, v28\n\t"
    "v_mfma_f32_16x16x32_bf16 v[20:23], a[184:187], v[152:155], v[20:23]\n\t"
    "buffer_load_dwordx4 v7, s[52:55], s61 offen offset:1024 nt lds\n\t"
    "v_sub_f32_e32 v53, v53, v28\n\t"
    "v_mfma_f32_16x16x32_bf16 v[56:59], v[220:223], v[24:27], 0\n\t"
    "v_exp_f32_e32 v45, v45\n\t"
    "v_mfma_f32_16x16x32_bf16 v[56:59], v[216:219], v[32:35], v[56:59]\n\t"
    "buffer_load_dwordx4 v7, s[52:55], s61 offen offset:2048 nt lds\n\t"
    "v_add_f32_e32 v30, v30, v45\n\t"
    "v_mfma_f32_16x16x32_bf16 v[56:59], v[212:215], v[36:39], v[56:59]\n\t"
    "s_add_u32 s61, s61, 11206656\n\t"
    "v_sub_f32_e32 v54, v54, v28\n\t"
    "v_mfma_f32_16x16x32_bf16 v[60:63], v[188:191], v[36:39], 0\n\t"
    "v_exp_f32_e32 v46, v46\n\t"
    "v_mfma_f32_16x16x32_bf16 v[60:63], v[192:195], v[32:35], v[60:63]\n\t"
    "v_add_f32_e32 v31, v31, v46\n\t"
    "v_sub_f32_e32 v55, v55, v28\n\t"
    "v_mfma_f32_16x16x32_bf16 v[60:63], v[196:199], v[24:27], v[60:63]\n\t"
    "v_exp_f32_e32 v47, v47\n\t"
    "v_mfma_f32_16x16x32_bf16 v[64:67], v[172:175], v[24:27], 0\n\t"
    "v_add_f32_e32 v31, v31, v47\n\t"
    "v_cvt_pk_bf16_f32 v12, v20, v21\n\t"
    "v_mfma_f32_16x16x32_bf16 v[64:67], v[168:171], v[32:35], v[64:67]\n\t"
    "v_exp_f32_e32 v52, v52\n\t"
    "v_mfma_f32_16x16x32_bf16 v[64:67], v[164:167], v[36:39], v[64:67]\n\t"
    "v_add_f32_e32 v30, v30, v52\n\t"
    "v_cvt_pk_bf16_f32 v13, v22, v23\n\t"
    "v_mfma_f32_16x16x32_bf16 v[68:71], v[140:143], v[36:39], 0\n\t"
    "v_exp_f32_e32 v53, v53\n\t"
    "v_mfma_f32_16x16x32_bf16 v[68:71], v[144:147], v[32:35], v[68:71]\n\t"
    "v_add_f32_e32 v30, v30, v53\n\t"
    "v_mfma_f32_16x16x32_bf16 v[68:71], v[148:151], v[24:27], v[68:71]\n\t"
    "v_exp_f32_e32 v54, v54\n\t"
    "v_mfma_f32_16x16x32_bf16 v[20:23], a[188:191], v[152:155], 0\n\t"
    "v_add_f32_e32 v31, v31, v54\n\t"
    "v_mfma_f32_16x16x32_bf16 v[20:23], a[180:183], v[156:159], v[20:23]\n\t"
    "v_exp_f32_e32 v55, v55\n\t"
    "v_mfma_f32_16x16x32_bf16 v[20:23], a[172:175], v[160:163], v[20:23]\n\t"
    "v_add_f32_e32 v31, v31, v55\n\t"
    "v_add_f32_e32 v30, v30, v31\n\t"
    "v_mfma_f32_16x16x32_bf16 v[20:23], a[164:167], v[164:167], v[20:23]\n\t"
    "v_mov_b32_e32 v31, v30\n\t"
    "v_mfma_f32_16x16x32_bf16 v[20:23], a[156:159], v[168:171], v[20:23]\n\t"
    "v_mfma_f32_16x16x32_bf16 v[20:23], a[148:151], v[172:175], v[20:23]\n\t"
    "v_permlane32_swap_b32 v30, v31\n\t"
    "v_add_f32_e32 v30, v30, v31\n\t"
    "v_mfma_f32_16x16x32_bf16 v[24:27], a[192:195], v[172:175], 0\n\t"
    "v_mov_b32_e32 v31, v30\n\t"
    "v_mfma_f32_16x16x32_bf16 v[24:27], a[200:203], v[168:171], v[24:27]\n\t"
    "v_mfma_f32_16x16x32_bf16 v[24:27], a[208:211], v[164:167], v[24:27]\n\t"
    "v_permlane16_swap_b32 v30, v31\n\t"
    "v_add_f32_e32 v30, v30, v31\n\t"
    "v_mfma_f32_16x16x32_bf16 v[24:27], a[216:219], v[160:163], v[24:27]\n\t"
    "v_cvt_pk_bf16_f32 v14, v20, v21\n\t"
    "v_cvt_pk_bf16_f32 v15, v22, v23\n\t"
    "v_mfma_f32_16x16x32_bf16 v[24:27], a[224:227], v[156:159], v[24:27]\n\t"
    "v_rcp_f32_e32 v30, v30\n\t"
    "v_mfma_f32_16x16x32_bf16 v[24:27], a[232:235], v[152:155], v[24:27]\n\t"
    "v_mul_f32_e32 v40, v40, v30\n\t"
    "v_mul_f32_e32 v41, v41, v30\n\t"
    "v_mfma_f32_16x16x32_bf16 v[32:35], a[236:239], v[152:155], 0\n\t"
    "v_cvt_pk_bf16_f32 v20, v40, v41\n\t"
    "v_mul_f32_e32 v42, v42, v30\n\t"
    "v_mfma_f32_16x16x32_bf16 v[32:35], a[228:231], v[156:159], v[32:35]\n\t"
    "v_mul_f32_e32 v43, v43, v30\n\t"
    "v_cvt_pk_bf16_f32 v21, v42, v43\n\t"
    "v_mfma_f32_16x16x32_bf16 v[32:35], a[220:223], v[160:163], v[32:35]\n\t"
    "v_mul_f32_e32 v16, v16, v30\n\t"
    "v_mul_f32_e32 v17, v17, v30\n\t"
    "v_mfma_f32_16x16x32_bf16 v[32:35], a[212:215], v[164:167], v[32:35]\n\t"
    "v_cvt_pk_bf16_f32 v36, v24, v25\n\t"
    "v_cvt_pk_bf16_f32 v37, v26, v27\n\t"
    "v_mfma_f32_16x16x32_bf16 v[32:35], a[204:207], v[168:171], v[32:35]\n\t"
    "v_cvt_pk_bf16_f32 v22, v16, v17\n\t"
    "v_mul_f32_e32 v18, v18, v30\n\t"
    "v_mfma_f32_16x16x32_bf16 v[32:35], a[196:199], v[172:175], v[32:35]\n\t"
    "v_mul_f32_e32 v19, v19, v30\n\t"
    "v_cvt_pk_bf16_f32 v23, v18, v19\n\t"
    "v_mfma_f32_16x16x32_bf16 v[24:27], a[240:243], v[172:175], 0\n\t"
    "v_mul_f32_e32 v44, v44, v30\n\t"
    "v_mul_f32_e32 v45, v45, v30\n\t"
    "v_mfma_f32_16x16x32_bf16 v[24:27], a[248:251], v[168:171], v[24:27]\n\t"
    "v_cvt_pk_bf16_f32 v40, v44, v45\n\t"
    "v_mul_f32_e32 v46, v46, v30\n\t"
    "v_mfma_f32_16x16x32_bf16 v[24:27], v[224:227], v[164:167], v[24:27]\n\t"
    "v_mul_f32_e32 v47, v47, v30\n\t"
    "v_cvt_pk_bf16_f32 v41, v46, v47\n\t"
    "v_mfma_f32_16x16x32_bf16 v[24:27], v[232:235], v[160:163], v[24:27]\n\t"
    "v_cvt_pk_bf16_f32 v38, v32, v33\n\t"
    "v_cvt_pk_bf16_f32 v39, v34, v35\n\t"
    "v_mfma_f32_16x16x32_bf16 v[24:27], v[240:243], v[156:159], v[24:27]\n\t"
    "v_mul_f32_e32 v52, v52, v30\n\t"
    "v_mul_f32_e32 v53, v53, v30\n\t"
    "v_mfma_f32_16x16x32_bf16 v[24:27], v[248:251], v[152:155], v[24:27]\n\t"
    "v_cvt_pk_bf16_f32 v42, v52, v53\n\t"
    "v_mul_f32_e32 v54, v54, v30\n\t"
    "v_mfma_f32_16x16x32_bf16 v[16:19], v[252:255], v[152:155], 0\n\t"
    "v_mul_f32_e32 v55, v55, v30\n\t"
    "v_cvt_pk_bf16_f32 v43, v54, v55\n\t"
    "v_mfma_f32_16x16x32_bf16 v[16:19], v[244:247], v[156:159], v[16:19]\n\t"
    "v_mfma_f32_16x16x32_bf16 v[16:19], v[236:239], v[160:163], v[16:19]\n\t"
    "v_mfma_f32_16x16x32_bf16 v[16:19], v[228:231], v[164:167], v[16:19]\n\t"
    "v_cvt_pk_bf16_f32 v28, v24, v25\n\t"
    "v_cvt_pk_bf16_f32 v29, v26, v27\n\t"
    "v_mfma_f32_16x16x32_bf16 v[16:19], a[252:255], v[168:171], v[16:19]\n\t"
    "v_mfma_f32_16x16x32_bf16 v[16:19], a[244:247], v[172:175], v[16:19]\n\t"
    "v_mfma_f32_16x16x32_bf16 v[24:27], a[0:3], v[148:151], 0\n\t"
    "ds_write_b128 v6, v[20:23] offset:2048\n\t"
    "v_mfma_f32_16x16x32_bf16 v[24:27], a[8:11], v[144:147], v[24:27]\n\t"
    "v_mfma_f32_16x16x32_bf16 v[24:27], a[16:19], v[140:143], v[24:27]\n\t"
    "s_add_u32 m0, s62, 0\n\t"
    "v_mfma_f32_16x16x32_bf16 v[24:27], a[24:27], v[136:139], v[24:27]\n\t"
    "buffer_load_dwordx4 v7, s[52:55], s61 offen offset:0 nt lds\n\t"
    "v_cvt_pk_bf16_f32 v30, v16, v17\n\t"
    "v_mfma_f32_16x16x32_bf16 v[24:27], a[32:35], v[132:135], v[24:27]\n\t"
    "ds_write_b128 v6, v[40:43] offset:3072\n\t"
    "v_mfma_f32_16x16x32_bf16 v[24:27], a[40:43], v[128:131], v[24:27]\n\t"
    "buffer_load_dwordx4 v7, s[52:55], s61 offen offset:1024 nt lds\n\t"
    "v_cvt_pk_bf16_f32 v31, v18, v19\n\t"
    "v_mfma_f32_16x16x32_bf16 v[56:59], v[208:211], v[12:15], v[56:59]\n\t"
    "v_mfma_f32_16x16x32_bf16 v[56:59], v[204:207], v[36:39], v[56:59]\n\t"
    "buffer_load_dwordx4 v7, s[52:55], s61 offen offset:2048 nt lds\n\t"
    "v_mfma_f32_16x16x32_bf16 v[56:59], v[200:203], v[28:31], v[56:59]\n\t"
    "s_add_u32 s61, s61, 196608\n\t"
    "v_mfma_f32_16x16x32_bf16 v[60:63], v[176:179], v[28:31], v[60:63]\n\t"
    "v_cvt_pk_bf16_f32 v40, v24, v25\n\t"
    "v_cvt_pk_bf16_f32 v41, v26, v27\n\t"
    "v_mfma_f32_16x16x32_bf16 v[60:63], v[180:183], v[36:39], v[60:63]\n\t"
    "v_mfma_f32_16x16x32_bf16 v[60:63], v[184:187], v[12:15], v[60:63]\n\t"
    "v_mfma_f32_16x16x32_bf16 v[64:67], v[160:163], v[12:15], v[64:67]\n\t"
    "v_max3_f32 v72, v56, v57, v58\n\t"
    "v_mfma_f32_16x16x32_bf16 v[64:67], v[156:159], v[36:39], v[64:67]\n\t"
    "v_mfma_f32_16x16x32_bf16 v[64:67], v[152:155], v[28:31], v[64:67]\n\t"
    "v_mfma_f32_16x16x32_bf16 v[68:71], v[128:131], v[28:31], v[68:71]\n\t"
    "v_max3_f32 v73, v59, v60, v61\n\t"
    "v_max3_f32 v72, v72, v62, v63\n\t"
    "v_mfma_f32_16x16x32_bf16 v[68:71], v[132:135], v[36:39], v[68:71]\n\t"
    "v_mfma_f32_16x16x32_bf16 v[68:71], v[136:139], v[12:15], v[68:71]\n\t"
    "v_mfma_f32_16x16x32_bf16 v[16:19], a[44:47], v[128:131], 0\n\t"
    "v_max3_f32 v73, v73, v64, v65\n\t"
    "v_max3_f32 v72, v72, v66, v67\n\t"
    "v_mfma_f32_16x16x32_bf16 v[16:19], a[36:39], v[132:135], v[16:19]\n\t"
    "v_mfma_f32_16x16x32_bf16 v[16:19], a[28:31], v[136:139], v[16:19]\n\t"
    "v_mfma_f32_16x16x32_bf16 v[16:19], a[20:23], v[140:143], v[16:19]\n\t"
    "v_max3_f32 v73, v73, v68, v69\n\t"
    "v_max3_f32 v72, v72, v70, v71\n\t"
    "v_mfma_f32_16x16x32_bf16 v[16:19], a[12:15], v[144:147], v[16:19]\n\t"
    "v_max_f32_e32 v72, v72, v73\n\t"
    "v_mov_b32_e32 v73, v72\n\t"
    "v_mfma_f32_16x16x32_bf16 v[16:19], a[4:7], v[148:151], v[16:19]\n\t"
    "v_mfma_f32_16x16x32_bf16 v[12:15], a[48:51], v[148:151], 0\n\t"
    "v_permlane32_swap_b32 v72, v73\n\t"
    "v_max_f32_e32 v72, v72, v73\n\t"
    "v_mfma_f32_16x16x32_bf16 v[12:15], a[56:59], v[144:147], v[12:15]\n\t"
    "v_mov_b32_e32 v73, v72\n\t"
    "v_mfma_f32_16x16x32_bf16 v[12:15], a[64:67], v[140:143], v[12:15]\n\t"
    "v_mfma_f32_16x16x32_bf16 v[12:15], a[72:75], v[136:139], v[12:15]\n\t"
    "v_cvt_pk_bf16_f32 v42, v16, v17\n\t"
    "v_cvt_pk_bf16_f32 v43, v18, v19\n\t"
    "v_mfma_f32_16x16x32_bf16 v[12:15], a[80:83], v[132:135], v[12:15]\n\t"
    "v_permlane16_swap_b32 v72, v73\n\t"
    "v_max_f32_e32 v72, v72, v73\n\t"
    "v_mfma_f32_16x16x32_bf16 v[12:15], a[88:91], v[128:131], v[12:15]\n\t"
    "v_sub_f32_e32 v56, v56, v72\n\t"
    "v_sub_f32_e32 v57, v57, v72\n\t"
    "v_mfma_f32_16x16x32_bf16 v[16:19], a[92:95], v[128:131], 0\n\t"
    "v_exp_f32_e32 v56, v56\n\t"
    "v_mfma_f32_16x16x32_bf16 v[16:19], a[84:87], v[132:135], v[16:19]\n\t"
    "v_exp_f32_e32 v57, v57\n\t"
    "v_mfma_f32_16x16x32_bf16 v[16:19], a[76:79], v[136:139], v[16:19]\n\t"
    "v_add_f32_e32 v74, v56, v57\n\t"
    "v_sub_f32_e32 v58, v58, v72\n\t"
    "v_mfma_f32_16x16x32_bf16 v[16:19], a[68:71], v[140:143], v[16:19]\n\t"
    "v_cvt_pk_bf16_f32 v44, v12, v13\n\t"
    ".Lh6_%=:\n\t"
    "s_cbranch_execz .LrN_%=\n\t"
    "v_cvt_pk_bf16_f32 v45, v14, v15\n\t"
    "v_mfma_f32_16x16x32_bf16 v[16:19], a[60:63], v[144:147], v[16:19]\n\t"
    "v_exp_f32_e32 v58, v58\n\t"
    "v_mfma_f32_16x16x32_bf16 v[16:19], a[52:55], v[148:151], v[16:19]\n\t"
    "v_sub_f32_e32 v59, v59, v72\n\t"
    "v_sub_f32_e32 v60, v60, v72\n\t"
    "v_mfma_f32_16x16x32_bf16 v[12:15], a[96:99], v[148:151], 0\n\t"
    "v_exp_f32_e32 v59, v59\n\t"
    "v_mfma_f32_16x16x32_bf16 v[12:15], a[104:107], v[144:147], v[12:15]\n\t"
    "v_add_f32_e32 v75, v58, v59\n\t"
    "v_sub_f32_e32 v61, v61, v72\n\t"
    "v_mfma_f32_16x16x32_bf16 v[12:15], a[112:115], v[140:143], v[12:15]\n\t"
    "v_exp_f32_e32 v60, v60\n\t"
    "v_mfma_f32_16x16x32_bf16 v[12:15], a[120:123], v[136:139], v[12:15]\n\t"
    "v_cvt_pk_bf16_f32 v46, v16, v17\n\t"
    "v_cvt_pk_bf16_f32 v47, v18, v19\n\t"
    "v_mfma_f32_16x16x32_bf16 v[12:15], a[128:131], v[132:135], v[12:15]\n\t"
    "v_add_f32_e32 v74, v74, v60\n\t"
    "v_sub_f32_e32 v62, v62, v72\n\t"
    "v_mfma_f32_16x16x32_bf16 v[12:15], a[136:139], v[128:131], v[12:15]\n\t"
    "v_exp_f32_e32 v61, v61\n\t"
    "v_mfma_f32_16x16x32_bf16 v[16:19], a[140:143], v[128:131], 0\n\t"
    "v_add_f32_e32 v74, v74, v61\n\t"
    "v_sub_f32_e32 v63, v63, v72\n\t"
    "v_mfma_f32_16x16x32_bf16 v[16:19], a[132:135], v[132:135], v[16:19]\n\t"
    "v_exp_f32_e32 v62, v62\n\t"
    "v_mfma_f32_16x16x32_bf16 v[16:19], a[124:127], v[136:139], v[16:19]\n\t"
    "v_add_f32_e32 v75, v75, v62\n\t"
    "v_sub_f32_e32 v64, v64, v72\n\t"
    "v_mfma_f32_16x16x32_bf16 v[16:19], a[116:119], v[140:143], v[16:19]\n\t"
    "v_cvt_pk_bf16_f32 v32, v12, v13\n\t"
    "v_cvt_pk_bf16_f32 v33, v14, v15\n\t"
    "v_mfma_f32_16x16x32_bf16 v[16:19], a[108:111], v[144:147], v[16:19]\n\t"
    "v_exp_f32_e32 v63, v63\n\t"
    "v_mfma_f32_16x16x32_bf16 v[16:19], a[100:103], v[148:151], v[16:19]\n\t"
    "v_add_f32_e32 v75, v75, v63\n\t"
    "v_sub_f32_e32 v65, v65, v72\n\t"
    "v_mfma_f32_16x16x32_bf16 v[48:51], a[144:147], v[148:151], 0\n\t"
    "v_exp_f32_e32 v64, v64\n\t"
    "v_mfma_f32_16x16x32_bf16 v[48:51], a[152:155], v[144:147], v[48:51]\n\t"
    "v_add_f32_e32 v74, v74, v64\n\t"
    "v_sub_f32_e32 v66, v66, v72\n\t"
    "s_branch .Lmid_%=\n\t"
    ".Ltail_%=:\n\t"
    "v_mbcnt_lo_u32_b32 v8, -1, 0\n\t"
    "v_mbcnt_hi_u32_b32 v8, -1, v8\n\t"
    "v_lshlrev_b32_e32 v8, 5, v8\n\t"
    "s_nop 7\n\t"
    "s_nop 1\n\t"
    "ds_write_b128 v6, v[44:47] offset:4096\n\t"
    "v_cvt_pk_bf16_f32 v62, v56, v57\n\t"
    "ds_write_b128 v6, v[52:55] offset:5120\n\t"
    "v_cvt_pk_bf16_f32 v63, v58, v59\n\t"
    "s_nop 1\n\t"
    "v_mfma_f32_16x16x32_bf16 v[12:15], v[208:211], v[36:39], v[12:15]\n\t"
    "s_nop 1\n\t"
    "v_mfma_f32_16x16x32_bf16 v[12:15], v[204:207], v[48:51], v[12:15]\n\t"
    "s_nop 1\n\t"
    "v_mfma_f32_16x16x32_bf16 v[12:15], v[200:203], v[60:63], v[12:15]\n\t"
    "s_nop 1\n\t"
    "v_mfma_f32_16x16x32_bf16 v[16:19], v[176:179], v[60:63], v[16:19]\n\t"
    "s_nop 1\n\t"
    "v_mfma_f32_16x16x32_bf16 v[16:19], v[180:183], v[48:51], v[16:19]\n\t"
    "s_waitcnt lgkmcnt(0)\n\t"
    "s_barrier\n\t"
    "s_nop 1\n\t"
    "v_mfma_f32_16x16x32_bf16 v[16:19], v[184:187], v[36:39], v[16:19]\n\t"
    "s_nop 1\n\t"
    "v_mfma_f32_16x16x32_bf16 v[20:23], v[160:163], v[36:39], v[20:23]\n\t"
    "v_max3_f32 v52, v12, v13, v14\n\t"
    "s_nop 1\n\t"
    "v_mfma_f32_16x16x32_bf16 v[20:23], v[156:159], v[48:51], v[20:23]\n\t"
    "s_nop 1\n\t"
    "v_mfma_f32_16x16x32_bf16 v[20:23], v[152:155], v[60:63], v[20:23]\n\t"
    "s_nop 1\n\t"
    "v_mfma_f32_16x16x32_bf16 v[24:27], v[128:131], v[60:63], v[24:27]\n\t"
    "v_max3_f32 v53, v15, v16, v17\n\t"
    "v_max3_f32 v52, v52, v18, v19\n\t"
    "s_nop 1\n\t"
    "v_mfma_f32_16x16x32_bf16 v[24:27], v[132:135], v[48:51], v[24:27]\n\t"
    "s_nop 1\n\t"
    "v_mfma_f32_16x16x32_bf16 v[24:27], v[136:139], v[36:39], v[24:27]\n\t"
    "s_nop 7\n\t"
    "s_nop 1\n\t"
    "v_max3_f32 v53, v53, v20, v21\n\t"
    "v_max3_f32 v52, v52, v22, v23\n\t"
    "v_max3_f32 v53, v53, v24, v25\n\t"
    "v_max3_f32 v52, v52, v26, v27\n\t"
    "v_max_f32_e32 v52, v52, v53\n\t"
    "v_mov_b32_e32 v53, v52\n\t"
    "s_nop 1\n\t"
    "v_permlane32_swap_b32 v52, v53\n\t"
    "v_max_f32_e32 v52, v52, v53\n\t"
    "v_mov_b32_e32 v53, v52\n\t"
    "s_nop 1\n\t"
    "v_permlane16_swap_b32 v52, v53\n\t"
    "v_max_f32_e32 v52, v52, v53\n\t"
    "v_sub_f32_e32 v12, v12, v52\n\t"
    "v_sub_f32_e32 v13, v13, v52\n\t"
    "v_sub_f32_e32 v14, v14, v52\n\t"
    "v_exp_f32_e32 v12, v12\n\t"
    "s_nop 0\n\t"
    "v_sub_f32_e32 v15, v15, v52\n\t"
    "v_exp_f32_e32 v13, v13\n\t"
    "s_nop 0\n\t"
    "v_add_f32_e32 v54, v12, v13\n\t"
    "v_sub_f32_e32 v16, v16, v52\n\t"
    "v_exp_f32_e32 v14, v14\n\t"
    "s_nop 0\n\t"
    "v_exp_f32_e32 v15, v15\n\t"
    "s_nop 0\n\t"
    "v_add_f32_e32 v55, v14, v15\n\t"
    "v_sub_f32_e32 v17, v17, v52\n\t"
    "v_exp_f32_e32 v16, v16\n\t"
    "s_nop 0\n\t"
    "v_add_f32_e32 v54, v54, v16\n\t"
    "v_sub_f32_e32 v18, v18, v52\n\t"
    "v_exp_f32_e32 v17, v17\n\t"
    "s_nop 0\n\t"
    "v_add_f32_e32 v54, v54, v17\n\t"
    "v_sub_f32_e32 v19, v19, v52\n\t"
    "v_exp_f32_e32 v18, v18\n\t"
    "s_nop 0\n\t"
    "v_add_f32_e32 v55, v55, v18\n\t"
    "v_sub_f32_e32 v20, v20, v52\n\t"
    "v_exp_f32_e32 v19, v19\n\t"
    "s_nop 0\n\t"
    "v_add_f32_e32 v55, v55, v19\n\t"
    "v_exp_f32_e32 v20, v20\n\t"
    "s_nop 0\n\t"
    "v_add_f32_e32 v54, v54, v20\n\t"
    "v_sub_f32_e32 v21, v21, v52\n\t"
    "v_sub_f32_e32 v22, v22, v52\n\t"
    "v_exp_f32_e32 v21, v21\n\t"
    "s_nop 0\n\t"
    "v_add_f32_e32 v54, v54, v21\n\t"
    "v_exp_f32_e32 v22, v22\n\t"
    "s_nop 0\n\t"
    "v_add_f32_e32 v55, v55, v22\n\t"
    "v_sub_f32_e32 v23, v23, v52\n\t"
    "v_sub_f32_e32 v24, v24, v52\n\t"
    "v_exp_f32_e32 v23, v23\n\t"
    "s_nop 0\n\t"
    "v_add_f32_e32 v55, v55, v23\n\t"
    "v_exp_f32_e32 v24, v24\n\t"
    "s_nop 0\n\t"
    "v_add_f32_e32 v54, v54, v24\n\t"
    "v_sub_f32_e32 v25, v25, v52\n\t"
    "v_sub_f32_e32 v26, v26, v52\n\t"
    "v_sub_f32_e32 v27, v27, v52\n\t"
    "v_exp_f32_e32 v25, v25\n\t"
    "s_nop 0\n\t"
    "v_add_f32_e32 v54, v54, v25\n\t"
    "v_exp_f32_e32 v26, v26\n\t"
    "s_nop 0\n\t"
    "v_add_f32_e32 v55, v55, v26\n\t"
    "v_exp_f32_e32 v27, v27\n\t"
    "s_nop 0\n\t"
    "v_add_f32_e32 v55, v55, v27\n\t"
    "v_add_f32_e32 v54, v54, v55\n\t"
    "v_mov_b32_e32 v55, v54\n\t"
    "s_nop 1\n\t"
    "v_permlane32_swap_b32 v54, v55\n\t"
    "v_add_f32_e32 v54, v54, v55\n\t"
    "v_mov_b32_e32 v55, v54\n\t"
    "s_nop 1\n\t"
    "v_permlane16_swap_b32 v54, v55\n\t"
    "v_add_f32_e32 v54, v54, v55\n\t"
    "v_rcp_f32_e32 v54, v54\n\t"
    "s_nop 0\n\t"
    "v_mul_f32_e32 v12, v12, v54\n\t"
    "v_mul_f32_e32 v13, v13, v54\n\t"
    "v_cvt_pk_bf16_f32 v60, v12, v13\n\t"
    "v_mul_f32_e32 v14, v14, v54\n\t"
    "v_mul_f32_e32 v15, v15, v54\n\t"
    "v_cvt_pk_bf16_f32 v61, v14, v15\n\t"
    "v_mul_f32_e32 v16, v16, v54\n\t"
    "v_mul_f32_e32 v17, v17, v54\n\t"
    "v_cvt_pk_bf16_f32 v62, v16, v17\n\t"
    "v_mul_f32_e32 v18, v18, v54\n\t"
    "v_mul_f32_e32 v19, v19, v54\n\t"
    "v_cvt_pk_bf16_f32 v63, v18, v19\n\t"
    "v_mul_f32_e32 v20, v20, v54\n\t"
    "v_mul_f32_e32 v21, v21, v54\n\t"
    "v_cvt_pk_bf16_f32 v44, v20, v21\n\t"
    "v_mul_f32_e32 v22, v22, v54\n\t"
    "v_mul_f32_e32 v23, v23, v54\n\t"
    "v_cvt_pk_bf16_f32 v45, v22, v23\n\t"
    "v_mul_f32_e32 v24, v24, v54\n\t"
    "v_mul_f32_e32 v25, v25, v54\n\t"
    "v_cvt_pk_bf16_f32 v46, v24, v25\n\t"
    "v_mul_f32_e32 v26, v26, v54\n\t"
    "v_mul_f32_e32 v27, v27, v54\n\t"
    "ds_write_b128 v6, v[60:63] offset:6144\n\t"
    "v_cvt_pk_bf16_f32 v47, v26, v27\n\t"
    "ds_write_b128 v6, v[44:47] offset:7168\n\t"
    "s_waitcnt lgkmcnt(0)\n\t"
    "s_barrier\n\t"
    "s_cmp_lg_u32 %[closer], 0\n\t"
    "s_cbranch_scc1 .Lx2_%=\n\t"
    "s_mov_b32 s60, 0x10000\n\t"
    "buffer_load_dwordx2 v[10:11], off, s[56:59], %[eoff] sc1\n\t"
    ".Lp3_%=:\n\t"
    "buffer_load_dwordx4 v[12:15], v8, s[56:59], %[aoff] offen sc1\n\t"
    "buffer_load_dwordx4 v[16:19], v8, s[56:59], %[aoff] offen offset:16 sc1\n\t"
    "s_waitcnt vmcnt(0)\n\t"
    "v_add_co_u32_e32 v20, vcc, 0x7f4a7c15, v10\n\t"
    "s_nop 1\n\t"
    "v_mov_b32_e32 v9, 0x9e3779b9\n\t"
    "v_addc_co_u32_e32 v21, vcc, v9, v11, vcc\n\t"
    "v_cmp_eq_u64_e32 vcc, v[12:13], v[20:21]\n\t"
    "v_cmp_eq_u64_e64 s[66:67], v[14:15], v[20:21]\n\t"
    "s_and_b64 vcc, vcc, s[66:67]\n\t"
    "v_cmp_eq_u64_e64 s[66:67], v[16:17], v[20:21]\n\t"
    "s_and_b64 vcc, vcc, s[66:67]\n\t"
    "v_cmp_eq_u64_e64 s[66:67], v[18:19], v[20:21]\n\t"
    "s_and_b64 vcc, vcc, s[66:67]\n\t"
    "s_cmp_eq_u64 vcc, exec\n\t"
    "s_cbranch_scc1 .Lk4_%=\n\t"
    "s_sleep 8\n\t"
    "s_sub_u32 s60, s60, 1\n\t"
    "s_cmp_lg_u32 s60, 0\n\t"
    "s_cbranch_scc1 .Lp3_%=\n\t"
    ".Lk4_%=:\n\t"
    "s_mov_b64 exec, 1\n\t"
    "buffer_store_dwordx2 v[20:21], off, s[56:59], %[eoff] sc1\n\t"
    "s_mov_b64 exec, -1\n\t"
    ".Lx2_%=:\n\t"
    "s_endpgm\n\t"
    ".Lend_%=:\n\t"
    :: "{v31}"(lane), "{s[36:43]}"(k8), "{s[44:47]}"(k4),
       [role] "s"(role), [pair] "s"(pair), [prod] "s"((fid - 144) >> 31), [closer] "s"(bid | wave), [fid] "s"(fid), [wb] "s"(wb),
       [rbeg] "s"(rbeg), [rend] "s"(rbeg + 3 * SLOT), [pl] "s"(ldsb + PB + pair * 8192), [amask] "s"((unsigned long long)pair),
       [tch] "s"(((bid >> 3) * 2 + pair) * 1152), [foff] "s"(F_OFF + 64 * fid), [aoff] "s"(A_OFF), [aown] "s"(A_OFF + 8 * bid), [eoff] "s"(E_OFF)
    : "memory", "scc", ACLOB, VCLOB, SCLOB);
}

extern "C" void kernel_launch(void* const* d_in, const int* in_sizes, int n_in,
                              void* d_out, int out_size, void* d_ws, size_t ws_size,
                              hipStream_t stream) {
  attn_kernel<<<256, 256, 0, stream>>>((const unsigned char*)d_in[0], (const unsigned char*)d_in[1], (const unsigned char*)d_in[2],
                                       (unsigned char*)d_ws, (unsigned char*)d_out
      );
}
```

```cpp
#include <hip/hip_runtime.h>
#include <hip/hip_bf16.h>
#include <stdint.h>

using u32x4 = __attribute__((ext_vector_type(4))) unsigned;
using u32x8 = __attribute__((ext_vector_type(8))) unsigned;

static constexpr int ROWB = 512 * 384;
static constexpr int SLOT = 24576;
static constexpr int PB = 6 * SLOT;
static constexpr int F_OFF = 160 * 1024, A_OFF = 148 * 1024, E_OFF = 152 * 1024;

#define R10(p, n) p #n "0", p #n "1", p #n "2", p #n "3", p #n "4", p #n "5", p #n "6", p #n "7", p #n "8", p #n "9"
#define R240(p) R10(p, 4), R10(p, 5), R10(p, 6), R10(p, 7), R10(p, 8), R10(p, 9), R10(p, 10), R10(p, 11), R10(p, 12), R10(p, 13), \
  R10(p, 14), R10(p, 15), R10(p, 16), R10(p, 17), R10(p, 18), R10(p, 19), R10(p, 20), R10(p, 21), R10(p, 22), R10(p, 23), R10(p, 24), \
  p "250", p "251", p "252", p "253", p "254", p "255"
#define ACLOB R10("a", ), R10("a", 1), R10("a", 2), R10("a", 3), R240("a")
#define VCLOB R10("v", ), R10("v", 1), R10("v", 2), "v30", "v32", "v33", "v34", "v35", "v36", "v37", "v38", "v39", R240("v")
#define SCLOB "s48", "s49", R10("s", 5), R10("s", 6), "s70", "s71", "s72", "s73", "vcc"

__global__ __launch_bounds__(256, 1) void attn_kernel(const unsigned char* __restrict__ x,
                                                      const unsigned char* __restrict__ wqkv,
                                                      const unsigned char* __restrict__ wo,
                                                      unsigned char* ws,
                                                      unsigned char* __restrict__ out
                                                      ) {
  __shared__ u32x4 lds[10240];
  u32x8 k8; u32x4 k4;
  asm volatile(
    "s_load_dwordx8 s[36:43], %2, 0\n\t"
    "s_load_dwordx2 s[44:45], %2, 32\n\t"
    : "={s[36:43]}"(k8), "={s[44:47]}"(k4) : "s"(__builtin_amdgcn_kernarg_segment_ptr()) : "memory", ACLOB);
  __builtin_amdgcn_sched_barrier(0);
  const unsigned lane = threadIdx.x & 63, wave = __builtin_amdgcn_readfirstlane(threadIdx.x >> 6);
  const unsigned role = wave >> 1, pair = wave & 1;
  const unsigned ldsb = (unsigned)(size_t)lds;
  const unsigned bid = blockIdx.x;
  const unsigned wb = (bid & 7) * 8 * ROWB + ((bid >> 3) * 2 + pair) * 8 * 384;
  const unsigned rbeg = ldsb + pair * 3 * SLOT;
  const unsigned second = (143 - bid) >> 31;
  const unsigned fid = ((bid - 144 * second) ^ (4 * second)) + pair * (40 + ((bid - 216) >> 31) * 256);

  asm volatile(
    ".Lbeg_%=:\n\t"
    "s_cmp_lg_u32 %[role], 1\n\t"
    "s_cbranch_scc1 .Lroln_%=\n\t"
    "s_cmp_eq_u32 %[prod], 0\n\t"
    "s_cbranch_scc1 .Lnp_%=\n\t"
    "s_cmp_lt_u32 %[fid], 72\n\t"
    "s_cselect_b32 s71, 0, 72\n\t"
    "s_cselect_b32 s63, 0, 768\n\t"
    "s_sub_u32 s64, %[fid], s71\n\t"
    "s_mul_i32 s65, s64, 0x1556\n\t"
    "s_lshr_b32 s65, s65, 16\n\t"
    "s_mul_i32 s69, s65, 12\n\t"
    "s_sub_u32 s69, s64, s69\n\t"
    "s_lshr_b32 s69, s69, 1\n\t"
    "s_and_b32 s70, s64, 1\n\t"
    "s_mul_i32 s60, s69, 36864\n\t"
    "s_add_u32 s60, s60, s63\n\t"
    "s_add_u32 s61, s60, 4608\n\t"
    "v_lshrrev_b32_e32 v20, 2, v31\n\t"
    "v_and_b32_e32 v21, 8, v20\n\t"
    "v_bfe_u32 v24, v31, 3, 2\n\t"
    "v_or_b32_e32 v21, v21, v24\n\t"
    "v_mul_u32_u24_e32 v21, 0x480, v21\n\t"
    "v_lshrrev_b32_e32 v24, 4, v31\n\t"
    "v_xor_b32_e32 v24, v24, v31\n\t"
    "v_and_b32_e32 v24, 7, v24\n\t"
    "v_lshl_or_b32 v24, v24, 4, v21\n\t"
    "v_xor_b32_e32 v25, 64, v24\n\t"
    "v_add_u32_e32 v25, 0x4800, v25\n\t"
    "v_mul_u32_u24_e32 v27, 0x180, v20\n\t"
    "v_and_b32_e32 v28, 3, v31\n\t"
    "v_lshl_or_b32 v27, v28, 4, v27\n\t"
    "s_lshl_b32 s62, s65, 3\n\t"
    "s_add_u32 s62, s62, s70\n\t"
    "s_mul_i32 s62, s62, 4608\n\t"
    "s_add_u32 s62, s62, 384\n\t"
    "s_lshl_b32 s63, s65, 6\n\t"
    "s_mov_b32 s50, 0x36000\n\t"
    "s_mov_b32 s51, 0x20000\n\t"
    "s_mov_b32 s54, 0x12000\n\t"
    "s_mov_b32 s55, 0x20000\n\t"
    "s_waitcnt lgkmcnt(0)\n\t"
    "s_mov_b32 s48, s38\n\t"
    "s_and_b32 s49, s39, 0xffff\n\t"
    "s_cmp_lg_u32 s71, 0\n\t"
    "s_cbranch_scc1 .Lpo_%=\n\t"
    "s_mov_b32 m0, 61440\n\t"
    "s_nop 0\n\t"
    "buffer_load_dwordx4 v24, s[48:51], s62 offen offset:0 lds\n\t"
    "s_mov_b32 m0, 62464\n\t"
    "s_nop 0\n\t"
    "buffer_load_dwordx4 v25, s[48:51], s62 offen offset:0 lds\n\t"
    "s_mov_b32 m0, 63360\n\t"
    "s_nop 0\n\t"
    "buffer_load_dwordx4 v24, s[48:51], s62 offen offset:128 lds\n\t"
    "s_mov_b32 m0, 64384\n\t"
    "s_nop 0\n\t"
    "buffer_load_dwordx4 v25, s[48:51], s62 offen offset:128 lds\n\t"
    "s_mov_b32 m0, 65280\n\t"
    "s_nop 0\n\t"
    "buffer_load_dwordx4 v24, s[48:51], s62 offen offset:256 lds\n\t"
    "s_mov_b32 m0, 66304\n\t"
    "s_nop 0\n\t"
    "buffer_load_dwordx4 v25, s[48:51], s62 offen offset:256 lds\n\t"
    "s_branch .Lpa_%=\n\t"
    ".Lpo_%=:\n\t"
    "s_mov_b32 s52, s40\n\t"
    "s_add_u32 m0, %[rbeg], 61440\n\t"
    "s_and_b32 s53, s41, 0xffff\n\t"
    "buffer_load_dwordx4 v27, s[52:55], s63 offen lds\n\t"
    "s_add_u32 m0, %[rbeg], 62464\n\t"
    "s_add_u32 s63, s63, 6144\n\t"
    "buffer_load_dwordx4 v27, s[52:55], s63 offen lds\n\t"
    "s_add_u32 m0, %[rbeg], 63488\n\t"
    "s_add_u32 s63, s63, 6144\n\t"
    "buffer_load_dwordx4 v27, s[52:55], s63 offen lds\n\t"
    "s_add_u32 m0, %[rbeg], 64512\n\t"
    "s_add_u32 s63, s63, 6144\n\t"
    "buffer_load_dwordx4 v27, s[52:55], s63 offen lds\n\t"
    "s_add_u32 m0, %[rbeg], 65536\n\t"
    "s_add_u32 s63, s63, 6144\n\t"
    "buffer_load_dwordx4 v27, s[52:55], s63 offen lds\n\t"
    "s_add_u32 m0, %[rbeg], 66560\n\t"
    "s_add_u32 s63, s63, 6144\n\t"
    "buffer_load_dwordx4 v27, s[52:55], s63 offen lds\n\t"
    "s_add_u32 m0, %[rbeg], 67584\n\t"
    "s_add_u32 s63, s63, 6144\n\t"
    "buffer_load_dwordx4 v27, s[52:55], s63 offen lds\n\t"
    "s_add_u32 m0, %[rbeg], 68608\n\t"
    "s_add_u32 s63, s63, 6144\n\t"
    "buffer_load_dwordx4 v27, s[52:55], s63 offen lds\n\t"
    "s_add_u32 m0, %[rbeg], 69632\n\t"
    "s_add_u32 s63, s63, 6144\n\t"
    "buffer_load_dwordx4 v27, s[52:55], s63 offen lds\n\t"
    "s_add_u32 m0, %[rbeg], 70656\n\t"
    "s_add_u32 s63, s63, 6144\n\t"
    "buffer_load_dwordx4 v27, s[52:55], s63 offen lds\n\t"
    "s_add_u32 m0, %[rbeg], 71680\n\t"
    "s_add_u32 s63, s63, 6144\n\t"
    "buffer_load_dwordx4 v27, s[52:55], s63 offen lds\n\t"
    "s_add_u32 m0, %[rbeg], 72704\n\t"
    "s_add_u32 s63, s63, 6144\n\t"
    "buffer_load_dwordx4 v27, s[52:55], s63 offen lds\n\t"
    ".Lpa_%=:\n\t"
    "s_add_u32 m0, %[rbeg], 49152\n\t"
    "s_nop 0\n\t"
    "buffer_load_dwordx4 v24, s[48:51], s60 offen offset:0 lds\n\t"
    "s_add_u32 m0, %[rbeg], 53120\n\t"
    "s_nop 0\n\t"
    "buffer_load_dwordx4 v24, s[48:51], s60 offen offset:128 lds\n\t"
    "s_add_u32 m0, %[rbeg], 57088\n\t"
    "s_nop 0\n\t"
    "buffer_load_dwordx4 v24, s[48:51], s60 offen offset:256 lds\n\t"
    "s_add_u32 m0, %[rbeg], 51200\n\t"
    "s_nop 0\n\t"
    "buffer_load_dwordx4 v25, s[48:51], s60 offen offset:0 lds\n\t"
    "s_add_u32 m0, %[rbeg], 55168\n\t"
    "s_nop 0\n\t"
    "buffer_load_dwordx4 v25, s[48:51], s60 offen offset:128 lds\n\t"
    "s_add_u32 m0, %[rbeg], 59136\n\t"
    "s_nop 0\n\t"
    "buffer_load_dwordx4 v25, s[48:51], s60 offen offset:256 lds\n\t"
    "s_add_u32 m0, %[rbeg], 50176\n\t"
    "s_nop 0\n\t"
    "buffer_load_dwordx4 v24, s[48:51], s61 offen offset:0 lds\n\t"
    "s_add_u32 m0, %[rbeg], 54144\n\t"
    "s_nop 0\n\t"
    "buffer_load_dwordx4 v24, s[48:51], s61 offen offset:128 lds\n\t"
    "s_add_u32 m0, %[rbeg], 58112\n\t"
    "s_nop 0\n\t"
    "buffer_load_dwordx4 v24, s[48:51], s61 offen offset:256 lds\n\t"
    "s_add_u32 m0, %[rbeg], 52224\n\t"
    "s_nop 0\n\t"
    "buffer_load_dwordx4 v25, s[48:51], s61 offen offset:0 lds\n\t"
    "s_add_u32 m0, %[rbeg], 56192\n\t"
    "s_nop 0\n\t"
    "buffer_load_dwordx4 v25, s[48:51], s61 offen offset:128 lds\n\t"
    "s_add_u32 m0, %[rbeg], 60160\n\t"
    "s_nop 0\n\t"
    "buffer_load_dwordx4 v25, s[48:51], s61 offen offset:256 lds\n\t"
    ".Lnp_%=:\n\t"
    "s_waitcnt lgkmcnt(0)\n\t"
    "s_memrealtime s[72:73]\n\t"
    "s_mov_b32 s56, s42\n\t"
    "s_and_b32 s57, s43, 0xffff\n\t"
    "s_mov_b32 s58, 0x40000\n\t"
    "s_mov_b32 s59, 0x20000\n\t"
    "buffer_load_dwordx2 v[22:23], off, s[56:59], %[eoff] sc1\n\t"
    "s_barrier\n\t"
    "v_lshlrev_b32_e32 v30, 4, v31\n\t"
    "v_and_b32_e32 v28, 7, v31\n\t"
    "v_bfe_u32 v27, v31, 3, 1\n\t"
    "v_lshrrev_b32_e32 v29, 4, v31\n\t"
    "v_add_u32_e32 v6, %[pl], v30\n\t"
    "v_mul_u32_u24_e32 v27, 0xc00, v27\n\t"
    "v_add_u32_e32 v27, %[rbeg], v27\n\t"
    "v_lshlrev_b32_e32 v26, 7, v31\n\t"
    "v_mad_u32_u24 v20, v28, 3, 0\n\t"
    "v_and_b32_e32 v21, 6, v20\n\t"
    "v_xor_b32_e32 v21, v21, v29\n\t"
    "v_lshl_add_u32 v20, v20, 7, v27\n\t"
    "v_lshl_add_u32 v0, v21, 4, v20\n\t"
    "v_xor_b32_e32 v1, 64, v0\n\t"
    "v_mad_u32_u24 v20, v28, 3, 1\n\t"
    "v_and_b32_e32 v21, 6, v20\n\t"
    "v_xor_b32_e32 v21, v21, v29\n\t"
    "v_lshl_add_u32 v20, v20, 7, v27\n\t"
    "v_lshl_add_u32 v2, v21, 4, v20\n\t"
    "v_xor_b32_e32 v3, 64, v2\n\t"
    "v_mad_u32_u24 v20, v28, 3, 2\n\t"
    "v_and_b32_e32 v21, 6, v20\n\t"
    "v_xor_b32_e32 v21, v21, v29\n\t"
    "v_lshl_add_u32 v20, v20, 7, v27\n\t"
    "v_lshl_add_u32 v4, v21, 4, v20\n\t"
    "v_xor_b32_e32 v5, 64, v4\n\t"
    "s_cmp_eq_u32 %[prod], 0\n\t"
    "s_cbranch_scc1 .Lnq_%=\n\t"
    "v_bfe_u32 v20, v31, 1, 3\n\t"
    "v_xor_b32_e32 v20, v20, v29\n\t"
    "v_lshlrev_b32_e32 v20, 4, v20\n\t"
    "v_lshl_or_b32 v20, v28, 7, v20\n\t"
    "v_bfe_u32 v21, v31, 3, 1\n\t"
    "v_lshl_or_b32 v16, v21, 11, v20\n\t"
    "v_add_u32_e32 v16, 0xc000, v16\n\t"
    "v_add_u32_e32 v16, %[rbeg], v16\n\t"
    "v_xor_b32_e32 v17, 64, v16\n\t"
    "s_cmp_lg_u32 s71, 0\n\t"
    "s_cbranch_scc1 .Lqo_%=\n\t"
    "v_lshl_or_b32 v18, v21, 10, v20\n\t"
    "v_add_u32_e32 v18, 0xf000, v18\n\t"
    "v_xor_b32_e32 v19, 64, v18\n\t"
    "s_mul_i32 s60, s70, 6\n\t"
    "s_add_u32 s60, s60, s65\n\t"
    "s_mul_i32 s60, s60, 6\n\t"
    "s_add_u32 s60, s60, s69\n\t"
    "s_lshl_b32 s60, s60, 10\n\t"
    "v_add_u32_e32 v20, s60, v30\n\t"
    "v_mov_b32_e32 v24, v30\n\t"
    "s_waitcnt vmcnt(13)\n\t"
    "ds_read_b128 v[80:83], v18 offset:0\n\t"
    "ds_read_b128 v[84:87], v19 offset:0\n\t"
    "ds_read_b128 v[88:91], v18 offset:2048\n\t"
    "ds_read_b128 v[92:95], v19 offset:2048\n\t"
    "ds_read_b128 v[96:99], v18 offset:4096\n\t"
    "ds_read_b128 v[100:103], v19 offset:4096\n\t"
    "s_branch .Lqa_%=\n\t"
    ".Lqo_%=:\n\t"
    "s_and_b32 s63, s65, 1\n\t"
    "s_lshl_b32 s63, s63, 1\n\t"
    "s_add_u32 s63, s63, s70\n\t"
    "s_lshl_b32 s64, s70, 2\n\t"
    "s_lshl_b32 s62, s70, 4\n\t"
    "s_cmp_lt_u32 s65, 4\n\t"
    "s_cselect_b32 s60, s64, 8\n\t"
    "s_cselect_b32 s63, s65, s63\n\t"
    "s_cselect_b32 s62, s64, s62\n\t"
    "s_cselect_b32 s61, 4, 3\n\t"
    "s_lshl_b32 s62, s62, 1\n\t"
    "s_add_u32 s62, s62, 61440\n\t"
    "s_add_u32 s62, s62, %[rbeg]\n\t"
    "s_mul_i32 s60, s60, 6\n\t"
    "s_add_u32 s60, s60, s69\n\t"
    "s_lshl_b32 s60, s60, 2\n\t"
    "s_add_u32 s60, s60, s63\n\t"
    "s_lshl_b32 s60, s60, 8\n\t"
    "s_add_u32 s60, s60, 73728\n\t"
    "v_and_b32_e32 v25, 3, v31\n\t"
    "v_bfe_u32 v21, v31, 2, 2\n\t"
    "v_lshlrev_b32_e32 v18, s61, v25\n\t"
    "v_lshl_add_u32 v18, v21, 6, v18\n\t"
    "v_lshl_add_u32 v18, v29, 9, v18\n\t"
    "v_add_u32_e32 v18, s62, v18\n\t"
    "v_lshl_add_u32 v24, v25, 2, v21\n\t"
    "v_lshl_add_u32 v24, v29, 4, v24\n\t"
    "v_lshlrev_b32_e32 v24, 4, v24\n\t"
    "v_mul_u32_u24_e32 v21, 0x1800, v21\n\t"
    "v_lshl_add_u32 v20, v25, 4, v21\n\t"
    "v_lshl_add_u32 v20, v29, 6, v20\n\t"
    "v_add_u32_e32 v20, s60, v20\n\t"
    "s_waitcnt vmcnt(13)\n\t"
    "ds_read_b64_tr_b16 v[80:81], v18 offset:0\n\t"
    "ds_read_b64_tr_b16 v[82:83], v18 offset:256\n\t"
    "ds_read_b64_tr_b16 v[84:85], v18 offset:2048\n\t"
    "ds_read_b64_tr_b16 v[86:87], v18 offset:2304\n\t"
    "ds_read_b64_tr_b16 v[88:89], v18 offset:4096\n\t"
    "ds_read_b64_tr_b16 v[90:91], v18 offset:4352\n\t"
    "ds_read_b64_tr_b16 v[92:93], v18 offset:6144\n\t"
    "ds_read_b64_tr_b16 v[94:95], v18 offset:6400\n\t"
    "ds_read_b64_tr_b16 v[96:97], v18 offset:8192\n\t"
    "ds_read_b64_tr_b16 v[98:99], v18 offset:8448\n\t"
    "ds_read_b64_tr_b16 v[100:101], v18 offset:10240\n\t"
    "ds_read_b64_tr_b16 v[102:103], v18 offset:10496\n\t"
    ".Lqa_%=:\n\t"
    "s_waitcnt vmcnt(7)\n\t"
    "ds_read_b128 v[32:35], v16 offset:0\n\t"
    "ds_read_b128 v[36:39], v17 offset:0\n\t"
    "ds_read_b128 v[40:43], v16 offset:4096\n\t"
    "ds_read_b128 v[44:47], v17 offset:4096\n\t"
    "ds_read_b128 v[48:51], v16 offset:8192\n\t"
    "ds_read_b128 v[52:55], v17 offset:8192\n\t"
    "s_waitcnt vmcnt(1)\n\t"
    "ds_read_b128 v[56:59], v16 offset:1024\n\t"
    "ds_read_b128 v[60:63], v17 offset:1024\n\t"
    "ds_read_b128 v[64:67], v16 offset:5120\n\t"
    "ds_read_b128 v[68:71], v17 offset:5120\n\t"
    "ds_read_b128 v[72:75], v16 offset:9216\n\t"
    "ds_read_b128 v[76:79], v17 offset:9216\n\t"
    "s_waitcnt lgkmcnt(11)\n\t"
    "v_mfma_f32_16x16x32_bf16 v[108:111], v[32:35], v[80:83], 0\n\t"
    "s_waitcnt lgkmcnt(10)\n\t"
    "v_mfma_f32_16x16x32_bf16 v[108:111], v[36:39], v[84:87], v[108:111]\n\t"
    "s_waitcnt lgkmcnt(9)\n\t"
    "v_mfma_f32_16x16x32_bf16 v[108:111], v[40:43], v[88:91], v[108:111]\n\t"
    "s_waitcnt lgkmcnt(8)\n\t"
    "v_mfma_f32_16x16x32_bf16 v[108:111], v[44:47], v[92:95], v[108:111]\n\t"
    "s_waitcnt lgkmcnt(7)\n\t"
    "v_mfma_f32_16x16x32_bf16 v[108:111], v[48:51], v[96:99], v[108:111]\n\t"
    "s_waitcnt lgkmcnt(6)\n\t"
    "v_mfma_f32_16x16x32_bf16 v[108:111], v[52:55], v[100:103], v[108:111]\n\t"
    "s_waitcnt lgkmcnt(5)\n\t"
    "v_mfma_f32_16x16x32_bf16 v[112:115], v[56:59], v[80:83], 0\n\t"
    "s_waitcnt lgkmcnt(4)\n\t"
    "v_mfma_f32_16x16x32_bf16 v[112:115], v[60:63], v[84:87], v[112:115]\n\t"
    "s_waitcnt lgkmcnt(3)\n\t"
    "v_mfma_f32_16x16x32_bf16 v[112:115], v[64:67], v[88:91], v[112:115]\n\t"
    "s_waitcnt lgkmcnt(2)\n\t"
    "v_mfma_f32_16x16x32_bf16 v[112:115], v[68:71], v[92:95], v[112:115]\n\t"
    "s_waitcnt lgkmcnt(1)\n\t"
    "v_mfma_f32_16x16x32_bf16 v[112:115], v[72:75], v[96:99], v[112:115]\n\t"
    "s_waitcnt lgkmcnt(0)\n\t"
    "v_mfma_f32_16x16x32_bf16 v[112:115], v[76:79], v[100:103], v[112:115]\n\t"
    "s_cmp_eq_u32 s71, 0\n\t"
    "s_cselect_b32 s60, 0x3dd53b95, 1.0\n\t"
    "v_mul_f32_e32 v108, s60, v108\n\t"
    "v_mul_f32_e32 v109, s60, v109\n\t"
    "v_mul_f32_e32 v110, s60, v110\n\t"
    "v_mul_f32_e32 v111, s60, v111\n\t"
    "s_nop 3\n\t"
    "v_mul_f32_e32 v112, s60, v112\n\t"
    "v_mul_f32_e32 v113, s60, v113\n\t"
    "v_mul_f32_e32 v114, s60, v114\n\t"
    "v_mul_f32_e32 v115, s60, v115\n\t"
    "v_cvt_pk_bf16_f32 v104, v108, v109\n\t"
    "v_cvt_pk_bf16_f32 v105, v110, v111\n\t"
    "s_nop 0\n\t"
    "v_cvt_pk_bf16_f32 v106, v112, v113\n\t"
    "v_cvt_pk_bf16_f32 v107, v114, v115\n\t"
    "v_add_u32_e32 v24, %[rbeg], v24\n\t"
    "v_add_u32_e32 v25, %[rbeg], v30\n\t"
    "ds_write_b128 v24, v[104:107] offset:49152\n\t"
    "ds_read_b128 v[104:107], v25 offset:49152\n\t"
    "s_waitcnt lgkmcnt(0)\n\t"
    "buffer_store_dwordx4 v[104:107], v20, s[56:59], 0 offen sc1\n\t"
    ".Lnq_%=:\n\t"
    "s_waitcnt vmcnt(0)\n\t"
    "v_add_co_u32_e32 v22, vcc, 0x7f4a7c15, v22\n\t"
    "v_mov_b32_e32 v16, 0x9e3779b9\n\t"
    "s_nop 0\n\t"
    "v_addc_co_u32_e32 v23, vcc, v16, v23, vcc\n\t"
    "s_mov_b32 exec_lo, %[prod]\n\t"
    "s_mov_b32 exec_hi, 0\n\t"
    "buffer_store_dwordx2 v[22:23], off, s[56:59], %[foff] sc1\n\t"
    "s_mov_b64 exec, -1\n\t"
    "v_lshrrev_b32_e32 v20, 5, v31\n\t"
    "v_mul_u32_u24_e32 v20, 0x30000, v20\n\t"
    "v_and_b32_e32 v21, 1, v29\n\t"
    "v_mul_u32_u24_e32 v21, 0x600, v21\n\t"
    "v_add_u32_e32 v20, v20, v21\n\t"
    "v_and_b32_e32 v21, 15, v31\n\t"
    "v_lshl_add_u32 v7, v21, 4, v20\n\t"
    "v_lshl_add_u32 v8, v21, 3, v20\n\t"
    "v_add_u32_e32 v8, 0x100, v8\n\t"
    "v_and_b32_e32 v20, 48, v30\n\t"
    "v_lshl_or_b32 v20, v29, 6, v20\n\t"
    "v_lshrrev_b32_e32 v21, 2, v21\n\t"
    "v_lshl_or_b32 v9, v21, 8, v20\n\t"
    "v_min_u32_e32 v21, 35, v31\n\t"
    "v_mul_u32_u24_e32 v21, 64, v21\n\t"
    "v_add_u32_e32 v21, 0x28000, v21\n\t"
    "s_mov_b32 s52, s44\n\t"
    "s_and_b32 s53, s45, 0xffff\n\t"
    "s_mov_b32 s54, 0x6000000\n\t"
    "s_mov_b32 s55, 0x20000\n\t"
    "s_mov_b64 exec, 0\n\t"
    "s_cmp_eq_u32 %[pair], 0\n\t"
    "s_cbranch_scc1 .LrN_%=\n\t"
    "s_branch .Lh6_%=\n\t"
    ".LrN_%=:\n\t"
    "s_mov_b64 exec, -1\n\t"
    "s_cmp_eq_u32 %[pair], 0\n\t"
    "s_cbranch_scc1 .Lng_%=\n\t"
    "s_cmp_lg_u32 %[prod], 0\n\t"
    "s_cbranch_scc1 .Lns_%=\n\t"
    "s_sleep 15\n\t"
    ".Lpt_%=:\n\t"
    "s_memrealtime s[70:71]\n\t"
    "s_waitcnt lgkmcnt(0)\n\t"
    "s_sub_u32 s70, s70, s72\n\t"
    "s_cmp_lt_u32 s70, 168\n\t"
    "s_cbranch_scc1 .Lpt_%=\n\t"
    ".Lns_%=:\n\t"
    "s_mov_b32 s64, 0\n\t"
    "s_mov_b32 s65, 2304\n\t"
    "buffer_load_dwordx2 v[12:13], v21, s[56:59], s64 offen sc1\n\t"
    "buffer_load_dwordx2 v[14:15], v21, s[56:59], s65 offen sc1\n\t"
    "s_waitcnt vmcnt(0)\n\t"
    "s_mov_b32 s60, 0x10000\n\t"
    ".Lp5_%=:\n\t"
    "v_cmp_eq_u64_e32 vcc, v[12:13], v[22:23]\n\t"
    "v_cmp_eq_u64_e64 s[70:71], v[14:15], v[22:23]\n\t"
    "s_and_b64 vcc, vcc, s[70:71]\n\t"
    "s_cmp_eq_u64 vcc, exec\n\t"
    "s_cbranch_scc1 .Lk6_%=\n\t"
    "s_sleep 4\n\t"
    "buffer_load_dwordx2 v[12:13], v21, s[56:59], s64 offen sc1\n\t"
    "buffer_load_dwordx2 v[14:15], v21, s[56:59], s65 offen sc1\n\t"
    "s_sub_u32 s60, s60, 1\n\t"
    "s_cmp_lg_u32 s60, 0\n\t"
    "s_waitcnt vmcnt(0)\n\t"
    "s_cbranch_scc1 .Lp5_%=\n\t"
    ".Lk6_%=:\n\t"
    ".Lng_%=:\n\t"
    "s_mul_i32 s66, %[pair], 36864\n\t"
    "s_mul_i32 s62, %[pair], 73728\n\t"
    "s_add_u32 s62, s62, 36864\n\t"
    "v_add_u32_e32 v11, s62, v30\n\t"
    "s_mov_b32 s63, 73728\n\t"
    "s_barrier\n\t"
    "s_add_u32 s67, %[tch], 0\n\t"
    "s_mov_b64 exec, 0x1ff\n\t"
    "buffer_load_dword v10, v26, s[56:59], s67 offen sc1\n\t"
    "s_mov_b64 exec, -1\n\t"
    "buffer_load_dwordx4 v[32:35], v30, s[56:59], s66 offen offset:0 sc1\n\t"
    "buffer_load_dwordx4 v[36:39], v30, s[56:59], s66 offen offset:1024 sc1\n\t"
    "buffer_load_dwordx4 v[40:43], v30, s[56:59], s66 offen offset:2048 sc1\n\t"
    "buffer_load_dwordx4 v[44:47], v30, s[56:59], s66 offen offset:3072 sc1\n\t"
    "s_add_u32 s66, s66, 4096\n\t"
    "buffer_load_dwordx4 v[48:51], v30, s[56:59], s66 offen offset:0 sc1\n\t"
    "buffer_load_dwordx4 v[52:55], v30, s[56:59], s66 offen offset:1024 sc1\n\t"
    "buffer_load_dwordx4 v[56:59], v30, s[56:59], s66 offen offset:2048 sc1\n\t"
    "buffer_load_dwordx4 v[60:63], v30, s[56:59], s66 offen offset:3072 sc1\n\t"
    "s_add_u32 s66, s66, 4096\n\t"
    "buffer_load_dwordx4 v[64:67], v30, s[56:59], s66 offen offset:0 sc1\n\t"
    "buffer_load_dwordx4 v[68:71], v30, s[56:59], s66 offen offset:1024 sc1\n\t"
    "buffer_load_dwordx4 v[72:75], v30, s[56:59], s66 offen offset:2048 sc1\n\t"
    "buffer_load_dwordx4 v[76:79], v30, s[56:59], s66 offen offset:3072 sc1\n\t"
    "s_add_u32 s66, s66, 4096\n\t"
    "buffer_load_dwordx4 v[80:83], v30, s[56:59], s66 offen offset:0 sc1\n\t"
    "buffer_load_dwordx4 v[84:87], v30, s[56:59], s66 offen offset:1024 sc1\n\t"
    "buffer_load_dwordx4 v[88:91], v30, s[56:59], s66 offen offset:2048 sc1\n\t"
    "buffer_load_dwordx4 v[92:95], v30, s[56:59], s66 offen offset:3072 sc1\n\t"
    "s_add_u32 s66, s66, 4096\n\t"
    "buffer_load_dwordx4 v[96:99], v30, s[56:59], s66 offen offset:0 sc1\n\t"
    "buffer_load_dwordx4 v[100:103], v30, s[56:59], s66 offen offset:1024 sc1\n\t"
    "buffer_load_dwordx4 v[104:107], v30, s[56:59], s66 offen offset:2048 sc1\n\t"
    "buffer_load_dwordx4 v[108:111], v30, s[56:59], s66 offen offset:3072 sc1\n\t"
    "s_add_u32 s66, s66, 4096\n\t"
    "buffer_load_dwordx4 v[112:115], v30, s[56:59], s66 offen offset:0 sc1\n\t"
    "buffer_load_dwordx4 v[116:119], v30, s[56:59], s66 offen offset:1024 sc1\n\t"
    "buffer_load_dwordx4 v[120:123], v30, s[56:59], s66 offen offset:2048 sc1\n\t"
    "buffer_load_dwordx4 v[124:127], v30, s[56:59], s66 offen offset:3072 sc1\n\t"
    "s_add_u32 s66, s66, 4096\n\t"
    "s_mov_b32 s64, 4608\n\t"
    "s_mov_b32 s65, 6912\n\t"
    "buffer_load_dwordx2 v[12:13], v21, s[56:59], s64 offen sc1\n\t"
    "buffer_load_dwordx2 v[14:15], v21, s[56:59], s65 offen sc1\n\t"
    "s_waitcnt vmcnt(25)\n\t"
    "ds_write_b128 v11, v[32:35] offset:0\n\t"
    "buffer_load_dwordx4 v[128:131], v30, s[56:59], s66 offen offset:0 sc1\n\t"
    "s_waitcnt vmcnt(25)\n\t"
    "ds_write_b128 v11, v[36:39] offset:1024\n\t"
    "buffer_load_dwordx4 v[132:135], v30, s[56:59], s66 offen offset:1024 sc1\n\t"
    "s_waitcnt vmcnt(25)\n\t"
    "ds_write_b128 v11, v[40:43] offset:2048\n\t"
    "buffer_load_dwordx4 v[136:139], v30, s[56:59], s66 offen offset:2048 sc1\n\t"
    "s_waitcnt vmcnt(25)\n\t"
    "ds_write_b128 v11, v[44:47] offset:3072\n\t"
    "buffer_load_dwordx4 v[140:143], v30, s[56:59], s66 offen offset:3072 sc1\n\t"
    "s_add_u32 s66, s66, 4096\n\t"
    "s_waitcnt vmcnt(25)\n\t"
    "ds_write_b128 v11, v[48:51] offset:4096\n\t"
    "buffer_load_dwordx4 v[144:147], v30, s[56:59], s66 offen offset:0 sc1\n\t"
    "s_waitcnt vmcnt(25)\n\t"
    "ds_write_b128 v11, v[52:55] offset:5120\n\t"
    "s_waitcnt lgkmcnt(0)\n\t"
    "s_barrier\n\t"
    "buffer_load_dwordx4 v[148:151], v30, s[56:59], s66 offen offset:1024 sc1\n\t"
    "s_waitcnt vmcnt(25)\n\t"
    "ds_write_b128 v11, v[56:59] offset:6144\n\t"
    "buffer_load_dwordx4 v[152:155], v30, s[56:59], s66 offen offset:2048 sc1\n\t"
    "s_waitcnt vmcnt(25)\n\t"
    "ds_write_b128 v11, v[60:63] offset:7168\n\t"
    "buffer_load_dwordx4 v[156:159], v30, s[56:59], s66 offen offset:3072 sc1\n\t"
    "s_add_u32 s66, s66, 4096\n\t"
    "s_waitcnt vmcnt(25)\n\t"
    "ds_write_b128 v11, v[64:67] offset:8192\n\t"
    "buffer_load_dwordx4 v[160:163], v30, s[56:59], s66 offen offset:0 sc1\n\t"
    "s_waitcnt vmcnt(25)\n\t"
    "ds_write_b128 v11, v[68:71] offset:9216\n\t"
    "buffer_load_dwordx4 v[164:167], v30, s[56:59], s66 offen offset:1024 sc1\n\t"
    "s_waitcnt vmcnt(25)\n\t"
    "ds_write_b128 v11, v[72:75] offset:10240\n\t"
    "buffer_load_dwordx4 v[168:171], v30, s[56:59], s66 offen offset:2048 sc1\n\t"
    "s_waitcnt vmcnt(25)\n\t"
    "ds_write_b128 v11, v[76:79] offset:11264\n\t"
    "s_waitcnt lgkmcnt(0)\n\t"
    "s_barrier\n\t"
    "buffer_load_dwordx4 v[172:175], v30, s[56:59], s66 offen offset:3072 sc1\n\t"
    "s_add_u32 s66, s66, 4096\n\t"
    "s_waitcnt vmcnt(25)\n\t"
    "ds_write_b128 v11, v[80:83] offset:12288\n\t"
    "s_waitcnt vmcnt(24)\n\t"
    "ds_write_b128 v11, v[84:87] offset:13312\n\t"
    "s_waitcnt vmcnt(23)\n\t"
    "ds_write_b128 v11, v[88:91] offset:14336\n\t"
    "s_waitcnt vmcnt(22)\n\t"
    "ds_write_b128 v11, v[92:95] offset:15360\n\t"
    "s_waitcnt vmcnt(21)\n\t"
    "ds_write_b128 v11, v[96:99] offset:16384\n\t"
    "s_waitcnt vmcnt(20)\n\t"
    "ds_write_b128 v11, v[100:103] offset:17408\n\t"
    "s_waitcnt vmcnt(19)\n\t"
    "ds_write_b128 v11, v[104:107] offset:18432\n\t"
    "s_waitcnt vmcnt(18)\n\t"
    "ds_write_b128 v11, v[108:111] offset:19456\n\t"
    "s_waitcnt vmcnt(17)\n\t"
    "ds_write_b128 v11, v[112:115] offset:20480\n\t"
    "s_waitcnt vmcnt(16)\n\t"
    "ds_write_b128 v11, v[116:119] offset:21504\n\t"
    "s_waitcnt vmcnt(15)\n\t"
    "ds_write_b128 v11, v[120:123] offset:22528\n\t"
    "s_waitcnt vmcnt(14)\n\t"
    "ds_write_b128 v11, v[124:127] offset:23552\n\t"
    "s_waitcnt lgkmcnt(0)\n\t"
    "s_barrier\n\t"
    "s_waitcnt vmcnt(12)\n\t"
    "s_mov_b32 s60, 0x10000\n\t"
    ".Lp7_%=:\n\t"
    "v_cmp_eq_u64_e32 vcc, v[12:13], v[22:23]\n\t"
    "v_cmp_eq_u64_e64 s[70:71], v[14:15], v[22:23]\n\t"
    "s_and_b64 vcc, vcc, s[70:71]\n\t"
    "s_cmp_eq_u64 vcc, exec\n\t"
    "s_cbranch_scc1 .Lk8_%=\n\t"
    "s_sleep 4\n\t"
    "buffer_load_dwordx2 v[12:13], v21, s[56:59], s64 offen sc1\n\t"
    "buffer_load_dwordx2 v[14:15], v21, s[56:59], s65 offen sc1\n\t"
    "s_sub_u32 s60, s60, 1\n\t"
    "s_cmp_lg_u32 s60, 0\n\t"
    "s_waitcnt vmcnt(0)\n\t"
    "s_cbranch_scc1 .Lp7_%=\n\t"
    ".Lk8_%=:\n\t"
    "s_add_u32 s67, %[tch], 73728\n\t"
    "s_mov_b64 exec, 0x1ff\n\t"
    "buffer_load_dword v10, v26, s[56:59], s67 offen sc1\n\t"
    "s_mov_b64 exec, -1\n\t"
    "buffer_load_dwordx4 a[0:3], v9, s[56:59], s63 offen offset:0 sc1\n\t"
    "buffer_load_dwordx4 a[4:7], v9, s[56:59], s63 offen offset:1024 sc1\n\t"
    "buffer_load_dwordx4 a[8:11], v9, s[56:59], s63 offen offset:2048 sc1\n\t"
    "buffer_load_dwordx4 a[12:15], v9, s[56:59], s63 offen offset:3072 sc1\n\t"
    "s_add_u32 s63, s63, 4096\n\t"
    "s_waitcnt vmcnt(16)\n\t"
    "ds_write_b128 v11, v[128:131] offset:24576\n\t"
    "buffer_load_dwordx4 a[16:19], v9, s[56:59], s63 offen offset:0 sc1\n\t"
    "s_waitcnt vmcnt(16)\n\t"
    "ds_write_b128 v11, v[132:135] offset:25600\n\t"
    "buffer_load_dwordx4 a[20:23], v9, s[56:59], s63 offen offset:1024 sc1\n\t"
    "s_waitcnt vmcnt(16)\n\t"
    "ds_write_b128 v11, v[136:139] offset:26624\n\t"
    "buffer_load_dwordx4 a[24:27], v9, s[56:59], s63 offen offset:2048 sc1\n\t"
    "s_waitcnt vmcnt(16)\n\t"
    "ds_write_b128 v11, v[140:143] offset:27648\n\t"
    "buffer_load_dwordx4 a[28:31], v9, s[56:59], s63 offen offset:3072 sc1\n\t"
    "s_add_u32 s63, s63, 4096\n\t"
    "s_waitcnt vmcnt(16)\n\t"
    "ds_write_b128 v11, v[144:147] offset:28672\n\t"
    "buffer_load_dwordx4 a[32:35], v9, s[56:59], s63 offen offset:0 sc1\n\t"
    "s_waitcnt vmcnt(16)\n\t"
    "ds_write_b128 v11, v[148:151] offset:29696\n\t"
    "buffer_load_dwordx4 a[36:39], v9, s[56:59], s63 offen offset:1024 sc1\n\t"
    "s_waitcnt vmcnt(16)\n\t"
    "ds_write_b128 v11, v[152:155] offset:30720\n\t"
    "buffer_load_dwordx4 a[40:43], v9, s[56:59], s63 offen offset:2048 sc1\n\t"
    "s_waitcnt vmcnt(16)\n\t"
    "ds_write_b128 v11, v[156:159] offset:31744\n\t"
    "buffer_load_dwordx4 a[44:47], v9, s[56:59], s63 offen offset:3072 sc1\n\t"
    "s_add_u32 s63, s63, 4096\n\t"
    "s_waitcnt vmcnt(16)\n\t"
    "ds_write_b128 v11, v[160:163] offset:32768\n\t"
    "buffer_load_dwordx4 a[48:51], v9, s[56:59], s63 offen offset:0 sc1\n\t"
    "s_waitcnt vmcnt(16)\n\t"
    "ds_write_b128 v11, v[164:167] offset:33792\n\t"
    "buffer_load_dwordx4 a[52:55], v9, s[56:59], s63 offen offset:1024 sc1\n\t"
    "s_waitcnt vmcnt(16)\n\t"
    "ds_write_b128 v11, v[168:171] offset:34816\n\t"
    "buffer_load_dwordx4 a[56:59], v9, s[56:59], s63 offen offset:2048 sc1\n\t"
    "s_waitcnt vmcnt(16)\n\t"
    "ds_write_b128 v11, v[172:175] offset:35840\n\t"
    "s_waitcnt lgkmcnt(0)\n\t"
    "s_barrier\n\t"
    "buffer_load_dwordx4 a[60:63], v9, s[56:59], s63 offen offset:3072 sc1\n\t"
    "s_add_u32 s63, s63, 4096\n\t"
    "buffer_load_dwordx4 a[64:67], v9, s[56:59], s63 offen offset:0 sc1\n\t"
    "buffer_load_dwordx4 a[68:71], v9, s[56:59], s63 offen offset:1024 sc1\n\t"
    "buffer_load_dwordx4 a[72:75], v9, s[56:59], s63 offen offset:2048 sc1\n\t"
    "buffer_load_dwordx4 a[76:79], v9, s[56:59], s63 offen offset:3072 sc1\n\t"
    "s_add_u32 s63, s63, 4096\n\t"
    "buffer_load_dwordx4 a[80:83], v9, s[56:59], s63 offen offset:0 sc1\n\t"
    "buffer_load_dwordx4 a[84:87], v9, s[56:59], s63 offen offset:1024 sc1\n\t"
    "buffer_load_dwordx4 a[88:91], v9, s[56:59], s63 offen offset:2048 sc1\n\t"
    "buffer_load_dwordx4 a[92:95], v9, s[56:59], s63 offen offset:3072 sc1\n\t"
    "s_add_u32 s63, s63, 4096\n\t"
    "buffer_load_dwordx4 a[96:99], v9, s[56:59], s63 offen offset:0 sc1\n\t"
    "buffer_load_dwordx4 a[100:103], v9, s[56:59], s63 offen offset:1024 sc1\n\t"
    "buffer_load_dwordx4 a[104:107], v9, s[56:59], s63 offen offset:2048 sc1\n\t"
    "buffer_load_dwordx4 a[108:111], v9, s[56:59], s63 offen offset:3072 sc1\n\t"
    "s_add_u32 s63, s63, 4096\n\t"
    "buffer_load_dwordx4 a[112:115], v9, s[56:59], s63 offen offset:0 sc1\n\t"
    "buffer_load_dwordx4 a[116:119], v9, s[56:59], s63 offen offset:1024 sc1\n\t"
    "v_accvgpr_write_b32 a252, v22\n\t"
    "v_accvgpr_write_b32 a253, v23\n\t"
    "ds_read_b128 v[148:151], v0 offset:0\n\t"
    "ds_read_b128 v[156:159], v1 offset:0\n\t"
    "ds_read_b128 v[164:167], v2 offset:0\n\t"
    "ds_read_b128 v[172:175], v3 offset:0\n\t"
    "ds_read_b128 v[180:183], v4 offset:0\n\t"
    "ds_read_b128 v[184:187], v5 offset:0\n\t"
    "s_waitcnt lgkmcnt(0)\n\t"
    "s_add_u32 s61, %[wb], 0xff460000\n\t"
    "s_mov_b32 s62, %[rbeg]\n\t"
    "s_mov_b32 s60, 8\n\t"
    "s_waitcnt vmcnt(24)\n\t"
    "v_mfma_f32_16x16x32_bf16 v[204:207], v[148:151], a[0:3], 0\n\t"
    "buffer_load_dwordx4 a[120:123], v9, s[56:59], s63 offen offset:2048 sc1\n\t"
    "v_mfma_f32_16x16x32_bf16 v[204:207], v[156:159], a[4:7], v[204:207]\n\t"
    "v_mfma_f32_16x16x32_bf16 v[204:207], v[164:167], a[8:11], v[204:207]\n\t"
    "v_mfma_f32_16x16x32_bf16 v[204:207], v[172:175], a[12:15], v[204:207]\n\t"
    "buffer_load_dwordx4 a[124:127], v9, s[56:59], s63 offen offset:3072 sc1\n\t"
    "s_add_u32 s63, s63, 4096\n\t"
    "v_mfma_f32_16x16x32_bf16 v[204:207], v[180:183], a[16:19], v[204:207]\n\t"
    "v_mfma_f32_16x16x32_bf16 v[204:207], v[184:187], a[20:23], v[204:207]\n\t"
    "s_waitcnt vmcnt(20)\n\t"
    "v_mfma_f32_16x16x32_bf16 v[108:111], v[184:187], a[44:47], 0\n\t"
    "v_mfma_f32_16x16x32_bf16 v[108:111], v[180:183], a[40:43], v[108:111]\n\t"
    "v_mfma_f32_16x16x32_bf16 v[108:111], v[172:175], a[36:39], v[108:111]\n\t"
    "v_mfma_f32_16x16x32_bf16 v[108:111], v[164:167], a[32:35], v[108:111]\n\t"
    "buffer_load_dwordx4 a[128:131], v9, s[56:59], s63 offen offset:0 sc1\n\t"
    "v_mfma_f32_16x16x32_bf16 v[108:111], v[156:159], a[28:31], v[108:111]\n\t"
    "v_mfma_f32_16x16x32_bf16 v[108:111], v[148:151], a[24:27], v[108:111]\n\t"
    "s_barrier\n\t"
    "s_mov_b64 exec, %[amask]\n\t"
    "buffer_store_dwordx2 a[252:253], off, s[56:59], %[aown] sc1\n\t"
    "s_mov_b64 exec, -1\n\t"
    "s_waitcnt vmcnt(16)\n\t"
    "v_mfma_f32_16x16x32_bf16 v[188:191], v[148:151], a[48:51], 0\n\t"
    "v_mfma_f32_16x16x32_bf16 v[188:191], v[156:159], a[52:55], v[188:191]\n\t"
    "v_mfma_f32_16x16x32_bf16 v[188:191], v[164:167], a[56:59], v[188:191]\n\t"
    "v_mfma_f32_16x16x32_bf16 v[188:191], v[172:175], a[60:63], v[188:191]\n\t"
    ".Lh1_%=:\n\t"
    "s_cbranch_execz .Lh2_%=\n\t"
    "buffer_load_dwordx4 a[132:135], v9, s[56:59], s63 offen offset:1024 sc1\n\t"
    "v_mfma_f32_16x16x32_bf16 v[188:191], v[180:183], a[64:67], v[188:191]\n\t"
    "v_mfma_f32_16x16x32_bf16 v[188:191], v[184:187], a[68:71], v[188:191]\n\t"
    "s_waitcnt vmcnt(11)\n\t"
    "v_mfma_f32_16x16x32_bf16 v[200:203], v[184:187], a[92:95], 0\n\t"
    "v_mfma_f32_16x16x32_bf16 v[200:203], v[180:183], a[88:91], v[200:203]\n\t"
    "v_mfma_f32_16x16x32_bf16 v[200:203], v[172:175], a[84:87], v[200:203]\n\t"
    "v_mfma_f32_16x16x32_bf16 v[200:203], v[164:167], a[80:83], v[200:203]\n\t"
    "buffer_load_dwordx4 a[136:139], v9, s[56:59], s63 offen offset:2048 sc1\n\t"
    "v_mfma_f32_16x16x32_bf16 v[200:203], v[156:159], a[76:79], v[200:203]\n\t"
    "v_mfma_f32_16x16x32_bf16 v[200:203], v[148:151], a[72:75], v[200:203]\n\t"
    "s_nop 7\n\t"
    "buffer_load_dwordx4 a[140:143], v9, s[56:59], s63 offen offset:3072 sc1\n\t"
    "s_add_u32 s63, s63, 4096\n\t"
    "buffer_load_dwordx4 a[144:147], v9, s[56:59], s63 offen offset:0 sc1\n\t"
    "buffer_load_dwordx4 a[148:151], v9, s[56:59], s63 offen offset:1024 sc1\n\t"
    "buffer_load_dwordx4 a[152:155], v9, s[56:59], s63 offen offset:2048 sc1\n\t"
    "buffer_load_dwordx4 a[156:159], v9, s[56:59], s63 offen offset:3072 sc1\n\t"
    "s_add_u32 s63, s63, 4096\n\t"
    "buffer_load_dwordx4 a[160:163], v9, s[56:59], s63 offen offset:0 sc1\n\t"
    "v_cvt_pk_bf16_f32 v12, v204, v205\n\t"
    "v_cvt_pk_bf16_f32 v13, v206, v207\n\t"
    "buffer_load_dwordx4 a[164:167], v9, s[56:59], s63 offen offset:1024 sc1\n\t"
    "buffer_load_dwordx4 a[168:171], v9, s[56:59], s63 offen offset:2048 sc1\n\t"
    "buffer_load_dwordx4 a[172:175], v9, s[56:59], s63 offen offset:3072 sc1\n\t"
    "s_add_u32 s63, s63, 4096\n\t"
    "v_cvt_pk_bf16_f32 v16, v108, v109\n\t"
    "v_cvt_pk_bf16_f32 v17, v110, v111\n\t"
    "buffer_load_dwordx4 a[176:179], v9, s[56:59], s63 offen offset:0 sc1\n\t"
    "v_cvt_pk_bf16_f32 v20, v188, v189\n\t"
    "v_cvt_pk_bf16_f32 v21, v190, v191\n\t"
    "buffer_load_dwordx4 a[180:183], v9, s[56:59], s63 offen offset:1024 sc1\n\t"
    "buffer_load_dwordx4 a[184:187], v9, s[56:59], s63 offen offset:2048 sc1\n\t"
    "buffer_load_dwordx4 a[188:191], v9, s[56:59], s63 offen offset:3072 sc1\n\t"
    "s_add_u32 s63, s63, 4096\n\t"
    "v_cvt_pk_bf16_f32 v24, v200, v201\n\t"
    "v_cvt_pk_bf16_f32 v25, v202, v203\n\t"
    "buffer_load_dwordx4 a[192:195], v9, s[56:59], s63 offen offset:0 sc1\n\t"
    "ds_read_b128 v[76:79], v0 offset:6144\n\t"
    "buffer_load_dwordx4 a[196:199], v9, s[56:59], s63 offen offset:1024 sc1\n\t"
    "ds_read_b128 v[80:83], v1 offset:6144\n\t"
    "buffer_load_dwordx4 a[200:203], v9, s[56:59], s63 offen offset:2048 sc1\n\t"
    "ds_read_b128 v[108:111], v2 offset:6144\n\t"
    "buffer_load_dwordx4 a[204:207], v9, s[56:59], s63 offen offset:3072 sc1\n\t"
    "s_add_u32 s63, s63, 4096\n\t"
    "ds_read_b128 v[84:87], v3 offset:6144\n\t"
    "buffer_load_dwordx4 a[208:211], v9, s[56:59], s63 offen offset:0 sc1\n\t"
    "ds_read_b128 v[60:63], v4 offset:6144\n\t"
    "buffer_load_dwordx4 a[212:215], v9, s[56:59], s63 offen offset:1024 sc1\n\t"
    "ds_read_b128 v[64:67], v5 offset:6144\n\t"
    "buffer_load_dwordx4 a[216:219], v9, s[56:59], s63 offen offset:2048 sc1\n\t"
    "buffer_load_dwordx4 a[220:223], v9, s[56:59], s63 offen offset:3072 sc1\n\t"
    "s_add_u32 s63, s63, 4096\n\t"
    "buffer_load_dwordx4 a[224:227], v9, s[56:59], s63 offen offset:0 sc1\n\t"
    "buffer_load_dwordx4 a[228:231], v9, s[56:59], s63 offen offset:1024 sc1\n\t"
    "buffer_load_dwordx4 a[232:235], v9, s[56:59], s63 offen offset:2048 sc1\n\t"
    "s_waitcnt lgkmcnt(2)\n\t"
    "v_mfma_f32_16x16x32_bf16 v[28:31], v[76:79], a[72:75], 0\n\t"
    "buffer_load_dwordx4 a[236:239], v9, s[56:59], s63 offen offset:3072 sc1\n\t"
    "s_add_u32 s63, s63, 4096\n\t"
    "v_mfma_f32_16x16x32_bf16 v[28:31], v[80:83], a[76:79], v[28:31]\n\t"
    "v_mfma_f32_16x16x32_bf16 v[28:31], v[108:111], a[80:83], v[28:31]\n\t"
    "buffer_load_dwordx4 a[240:243], v9, s[56:59], s63 offen offset:0 sc1\n\t"
    "v_mfma_f32_16x16x32_bf16 v[28:31], v[84:87], a[84:87], v[28:31]\n\t"
    "s_waitcnt lgkmcnt(0)\n\t"
    "v_mfma_f32_16x16x32_bf16 v[28:31], v[60:63], a[88:91], v[28:31]\n\t"
    "buffer_load_dwordx4 a[244:247], v9, s[56:59], s63 offen offset:1024 sc1\n\t"
    "v_mfma_f32_16x16x32_bf16 v[28:31], v[64:67], a[92:95], v[28:31]\n\t"
    "v_mfma_f32_16x16x32_bf16 v[32:35], v[64:67], a[68:71], 0\n\t"
    "buffer_load_dwordx4 a[248:251], v9, s[56:59], s63 offen offset:2048 sc1\n\t"
    "v_mfma_f32_16x16x32_bf16 v[32:35], v[60:63], a[64:67], v[32:35]\n\t"
    "v_mfma_f32_16x16x32_bf16 v[32:35], v[84:87], a[60:63], v[32:35]\n\t"
    "buffer_load_dwordx4 a[252:255], v9, s[56:59], s63 offen offset:3072 sc1\n\t"
    "s_add_u32 s63, s63, 4096\n\t"
    "v_mfma_f32_16x16x32_bf16 v[32:35], v[108:111], a[56:59], v[32:35]\n\t"
    "buffer_load_dwordx4 v[224:227], v9, s[56:59], s63 offen offset:0 sc1\n\t"
    "v_cvt_pk_bf16_f32 v26, v28, v29\n\t"
    "v_cvt_pk_bf16_f32 v27, v30, v31\n\t"
    "v_mfma_f32_16x16x32_bf16 v[32:35], v[80:83], a[52:55], v[32:35]\n\t"
    "v_mfma_f32_16x16x32_bf16 v[32:35], v[76:79], a[48:51], v[32:35]\n\t"
    "buffer_load_dwordx4 v[228:231], v9, s[56:59], s63 offen offset:1024 sc1\n\t"
    "v_mfma_f32_16x16x32_bf16 v[28:31], v[76:79], a[24:27], 0\n\t"
    "v_mfma_f32_16x16x32_bf16 v[28:31], v[80:83], a[28:31], v[28:31]\n\t"
    "buffer_load_dwordx4 v[232:235], v9, s[56:59], s63 offen offset:2048 sc1\n\t"
    "v_mfma_f32_16x16x32_bf16 v[28:31], v[108:111], a[32:35], v[28:31]\n\t"
    "v_mfma_f32_16x16x32_bf16 v[28:31], v[84:87], a[36:39], v[28:31]\n\t"
    "buffer_load_dwordx4 v[236:239], v9, s[56:59], s63 offen offset:3072 sc1\n\t"
    "s_add_u32 s63, s63, 4096\n\t"
    "v_cvt_pk_bf16_f32 v22, v32, v33\n\t"
    "v_cvt_pk_bf16_f32 v23, v34, v35\n\t"
    "v_mfma_f32_16x16x32_bf16 v[28:31], v[60:63], a[40:43], v[28:31]\n\t"
    "buffer_load_dwordx4 v[240:243], v9, s[56:59], s63 offen offset:0 sc1\n\t"
    "v_mfma_f32_16x16x32_bf16 v[28:31], v[64:67], a[44:47], v[28:31]\n\t"
    "v_mfma_f32_16x16x32_bf16 v[32:35], v[64:67], a[20:23], 0\n\t"
    "buffer_load_dwordx4 v[244:247], v9, s[56:59], s63 offen offset:1024 sc1\n\t"
    "v_mfma_f32_16x16x32_bf16 v[32:35], v[60:63], a[16:19], v[32:35]\n\t"
    "v_mfma_f32_16x16x32_bf16 v[32:35], v[84:87], a[12:15], v[32:35]\n\t"
    "buffer_load_dwordx4 v[248:251], v9, s[56:59], s63 offen offset:2048 sc1\n\t"
    "v_mfma_f32_16x16x32_bf16 v[32:35], v[108:111], a[8:11], v[32:35]\n\t"
    "v_cvt_pk_bf16_f32 v18, v28, v29\n\t"
    "v_cvt_pk_bf16_f32 v19, v30, v31\n\t"
    "v_mfma_f32_16x16x32_bf16 v[32:35], v[80:83], a[4:7], v[32:35]\n\t"
    "buffer_load_dwordx4 v[252:255], v9, s[56:59], s63 offen offset:3072 sc1\n\t"
    "s_add_u32 s63, s63, 4096\n\t"
    "v_mfma_f32_16x16x32_bf16 v[32:35], v[76:79], a[0:3], v[32:35]\n\t"
    ".Lmin_%=:\n\t"
    "s_waitcnt vmcnt(43)\n\t"
    "v_mfma_f32_16x16x32_bf16 v[36:39], v[76:79], a[96:99], 0\n\t"
    "v_mfma_f32_16x16x32_bf16 v[36:39], v[80:83], a[100:103], v[36:39]\n\t"
    "v_mfma_f32_16x16x32_bf16 v[36:39], v[108:111], a[104:107], v[36:39]\n\t"
    "buffer_store_dwordx2 v[140:141], v8, s[52:55], s61 offen offset:0 sc1\n\t"
    "v_mfma_f32_16x16x32_bf16 v[36:39], v[84:87], a[108:111], v[36:39]\n\t"
    "v_cvt_pk_bf16_f32 v14, v32, v33\n\t"
    "v_cvt_pk_bf16_f32 v15, v34, v35\n\t"
    "v_mfma_f32_16x16x32_bf16 v[36:39], v[60:63], a[112:115], v[36:39]\n\t"
    "v_mfma_f32_16x16x32_bf16 v[36:39], v[64:67], a[116:119], v[36:39]\n\t"
    "v_mfma_f32_16x16x32_bf16 v[32:35], v[184:187], a[116:119], 0\n\t"
    "v_mfma_f32_16x16x32_bf16 v[32:35], v[180:183], a[112:115], v[32:35]\n\t"
    "v_mfma_f32_16x16x32_bf16 v[32:35], v[172:175], a[108:111], v[32:35]\n\t"
    "v_mfma_f32_16x16x32_bf16 v[32:35], v[164:167], a[104:107], v[32:35]\n\t"
    "v_cvt_pk_bf16_f32 v30, v36, v37\n\t"
    "v_cvt_pk_bf16_f32 v31, v38, v39\n\t"
    "v_mfma_f32_16x16x32_bf16 v[32:35], v[156:159], a[100:103], v[32:35]\n\t"
    "v_mfma_f32_16x16x32_bf16 v[32:35], v[148:151], a[96:99], v[32:35]\n\t"
    "s_waitcnt vmcnt(37)\n\t"
    "v_mfma_f32_16x16x32_bf16 v[36:39], v[148:151], a[120:123], 0\n\t"
    "v_mfma_f32_16x16x32_bf16 v[36:39], v[156:159], a[124:127], v[36:39]\n\t"
    "v_mfma_f32_16x16x32_bf16 v[36:39], v[164:167], a[128:131], v[36:39]\n\t"
    "buffer_store_dwordx2 v[142:143], v8, s[52:55], s61 offen offset:384 sc1\n\t"
    "v_mfma_f32_16x16x32_bf16 v[36:39], v[172:175], a[132:135], v[36:39]\n\t"
    "v_cvt_pk_bf16_f32 v28, v32, v33\n\t"
    "v_cvt_pk_bf16_f32 v29, v34, v35\n\t"
    "v_mfma_f32_16x16x32_bf16 v[36:39], v[180:183], a[136:139], v[36:39]\n\t"
    "v_mfma_f32_16x16x32_bf16 v[36:39], v[184:187], a[140:143], v[36:39]\n\t"
    "v_mfma_f32_16x16x32_bf16 v[40:43], v[64:67], a[140:143], 0\n\t"
    "v_mfma_f32_16x16x32_bf16 v[40:43], v[60:63], a[136:139], v[40:43]\n\t"
    "v_mfma_f32_16x16x32_bf16 v[40:43], v[84:87], a[132:135], v[40:43]\n\t"
    "v_mfma_f32_16x16x32_bf16 v[40:43], v[108:111], a[128:131], v[40:43]\n\t"
    "v_cvt_pk_bf16_f32 v32, v36, v37\n\t"
    "v_cvt_pk_bf16_f32 v33, v38, v39\n\t"
    "v_mfma_f32_16x16x32_bf16 v[40:43], v[80:83], a[124:127], v[40:43]\n\t"
    "v_mfma_f32_16x16x32_bf16 v[40:43], v[76:79], a[120:123], v[40:43]\n\t"
    "s_waitcnt vmcnt(32)\n\t"
    "v_mfma_f32_16x16x32_bf16 v[44:47], v[76:79], a[144:147], 0\n\t"
    "v_mfma_f32_16x16x32_bf16 v[44:47], v[80:83], a[148:151], v[44:47]\n\t"
    "v_mfma_f32_16x16x32_bf16 v[44:47], v[108:111], a[152:155], v[44:47]\n\t"
    "buffer_store_dwordx2 v[144:145], v8, s[52:55], s61 offen offset:768 sc1\n\t"
    "v_mfma_f32_16x16x32_bf16 v[44:47], v[84:87], a[156:159], v[44:47]\n\t"
    "v_cvt_pk_bf16_f32 v34, v40, v41\n\t"
    "v_cvt_pk_bf16_f32 v35, v42, v43\n\t"
    "v_mfma_f32_16x16x32_bf16 v[44:47], v[60:63], a[160:163], v[44:47]\n\t"
    "v_mfma_f32_16x16x32_bf16 v[44:47], v[64:67], a[164:167], v[44:47]\n\t"
    "v_mfma_f32_16x16x32_bf16 v[40:43], v[184:187], a[164:167], 0\n\t"
    "v_mfma_f32_16x16x32_bf16 v[40:43], v[180:183], a[160:163], v[40:43]\n\t"
    "v_mfma_f32_16x16x32_bf16 v[40:43], v[172:175], a[156:159], v[40:43]\n\t"
    "v_mfma_f32_16x16x32_bf16 v[40:43], v[164:167], a[152:155], v[40:43]\n\t"
    "v_cvt_pk_bf16_f32 v38, v44, v45\n\t"
    "v_cvt_pk_bf16_f32 v39, v46, v47\n\t"
    "v_mfma_f32_16x16x32_bf16 v[40:43], v[156:159], a[148:151], v[40:43]\n\t"
    "v_mfma_f32_16x16x32_bf16 v[40:43], v[148:151], a[144:147], v[40:43]\n\t"
    "s_waitcnt vmcnt(27)\n\t"
    "v_mfma_f32_16x16x32_bf16 v[44:47], v[148:151], a[168:171], 0\n\t"
    "v_mfma_f32_16x16x32_bf16 v[44:47], v[156:159], a[172:175], v[44:47]\n\t"
    "v_mfma_f32_16x16x32_bf16 v[44:47], v[164:167], a[176:179], v[44:47]\n\t"
    "buffer_store_dwordx2 v[146:147], v8, s[52:55], s61 offen offset:1152 sc1\n\t"
    "v_mfma_f32_16x16x32_bf16 v[44:47], v[172:175], a[180:183], v[44:47]\n\t"
    "s_add_u32 s61, s61, 393216\n\t"
    "v_cvt_pk_bf16_f32 v36, v40, v41\n\t"
    "v_mfma_f32_16x16x32_bf16 v[44:47], v[180:183], a[184:187], v[44:47]\n\t"
    "v_cvt_pk_bf16_f32 v37, v42, v43\n\t"
    "v_mfma_f32_16x16x32_bf16 v[44:47], v[184:187], a[188:191], v[44:47]\n\t"
    "v_mfma_f32_16x16x32_bf16 v[48:51], v[64:67], a[188:191], 0\n\t"
    "v_mfma_f32_16x16x32_bf16 v[48:51], v[60:63], a[184:187], v[48:51]\n\t"
    "v_mfma_f32_16x16x32_bf16 v[48:51], v[84:87], a[180:183], v[48:51]\n\t"
    "v_mfma_f32_16x16x32_bf16 v[48:51], v[108:111], a[176:179], v[48:51]\n\t"
    "v_cvt_pk_bf16_f32 v40, v44, v45\n\t"
    "v_cvt_pk_bf16_f32 v41, v46, v47\n\t"
    "v_mfma_f32_16x16x32_bf16 v[48:51], v[80:83], a[172:175], v[48:51]\n\t"
    "v_mfma_f32_16x16x32_bf16 v[48:51], v[76:79], a[168:171], v[48:51]\n\t"
    "s_waitcnt vmcnt(22)\n\t"
    "v_mfma_f32_16x16x32_bf16 v[52:55], v[76:79], a[192:195], 0\n\t"
    "v_mfma_f32_16x16x32_bf16 v[52:55], v[80:83], a[196:199], v[52:55]\n\t"
    "v_mfma_f32_16x16x32_bf16 v[52:55], v[108:111], a[200:203], v[52:55]\n\t"
    "buffer_store_dwordx4 v[152:155], v7, s[52:55], s61 offen offset:0 sc1\n\t"
    "v_mfma_f32_16x16x32_bf16 v[52:55], v[84:87], a[204:207], v[52:55]\n\t"
    "v_cvt_pk_bf16_f32 v42, v48, v49\n\t"
    "v_cvt_pk_bf16_f32 v43, v50, v51\n\t"
    "v_mfma_f32_16x16x32_bf16 v[52:55], v[60:63], a[208:211], v[52:55]\n\t"
    "v_mfma_f32_16x16x32_bf16 v[52:55], v[64:67], a[212:215], v[52:55]\n\t"
    "v_mfma_f32_16x16x32_bf16 v[48:51], v[184:187], a[212:215], 0\n\t"
    "v_mfma_f32_16x16x32_bf16 v[48:51], v[180:183], a[208:211], v[48:51]\n\t"
    "v_mfma_f32_16x16x32_bf16 v[48:51], v[172:175], a[204:207], v[48:51]\n\t"
    "v_mfma_f32_16x16x32_bf16 v[48:51], v[164:167], a[200:203], v[48:51]\n\t"
    "v_cvt_pk_bf16_f32 v46, v52, v53\n\t"
    "v_cvt_pk_bf16_f32 v47, v54, v55\n\t"
    "v_mfma_f32_16x16x32_bf16 v[48:51], v[156:159], a[196:199], v[48:51]\n\t"
    "v_mfma_f32_16x16x32_bf16 v[48:51], v[148:151], a[192:195], v[48:51]\n\t"
    "s_waitcnt vmcnt(17)\n\t"
    "v_mfma_f32_16x16x32_bf16 v[52:55], v[148:151], a[216:219], 0\n\t"
    "v_mfma_f32_16x16x32_bf16 v[52:55], v[156:159], a[220:223], v[52:55]\n\t"
    "v_mfma_f32_16x16x32_bf16 v[52:55], v[164:167], a[224:227], v[52:55]\n\t"
    "buffer_store_dwordx4 v[160:163], v7, s[52:55], s61 offen offset:384 sc1\n\t"
    "v_mfma_f32_16x16x32_bf16 v[52:55], v[172:175], a[228:231], v[52:55]\n\t"
    "v_cvt_pk_bf16_f32 v44, v48, v49\n\t"
    "v_cvt_pk_bf16_f32 v45, v50, v51\n\t"
    "v_mfma_f32_16x16x32_bf16 v[52:55], v[180:183], a[232:235], v[52:55]\n\t"
    "v_mfma_f32_16x16x32_bf16 v[52:55], v[184:187], a[236:239], v[52:55]\n\t"
    "v_mfma_f32_16x16x32_bf16 v[56:59], v[64:67], a[236:239], 0\n\t"
    "v_mfma_f32_16x16x32_bf16 v[56:59], v[60:63], a[232:235], v[56:59]\n\t"
    "v_mfma_f32_16x16x32_bf16 v[56:59], v[84:87], a[228:231], v[56:59]\n\t"
    "v_mfma_f32_16x16x32_bf16 v[56:59], v[108:111], a[224:227], v[56:59]\n\t"
    "v_cvt_pk_bf16_f32 v48, v52, v53\n\t"
    "v_cvt_pk_bf16_f32 v49, v54, v55\n\t"
    "v_mfma_f32_16x16x32_bf16 v[56:59], v[80:83], a[220:223], v[56:59]\n\t"
    "v_mfma_f32_16x16x32_bf16 v[56:59], v[76:79], a[216:219], v[56:59]\n\t"
    "s_waitcnt vmcnt(12)\n\t"
    "v_mfma_f32_16x16x32_bf16 v[68:71], v[76:79], a[240:243], 0\n\t"
    "v_mfma_f32_16x16x32_bf16 v[68:71], v[80:83], a[244:247], v[68:71]\n\t"
    "v_mfma_f32_16x16x32_bf16 v[68:71], v[108:111], a[248:251], v[68:71]\n\t"
    "buffer_store_dwordx4 v[168:171], v7, s[52:55], s61 offen offset:768 sc1\n\t"
    "v_mfma_f32_16x16x32_bf16 v[68:71], v[84:87], a[252:255], v[68:71]\n\t"
    "v_cvt_pk_bf16_f32 v50, v56, v57\n\t"
    "v_cvt_pk_bf16_f32 v51, v58, v59\n\t"
    "v_mfma_f32_16x16x32_bf16 v[68:71], v[60:63], v[224:227], v[68:71]\n\t"
    "v_mfma_f32_16x16x32_bf16 v[68:71], v[64:67], v[228:231], v[68:71]\n\t"
    "v_mfma_f32_16x16x32_bf16 v[56:59], v[184:187], v[228:231], 0\n\t"
    "v_mfma_f32_16x16x32_bf16 v[56:59], v[180:183], v[224:227], v[56:59]\n\t"
    "ds_read_b128 v[116:119], v0 offset:12288\n\t"
    "v_mfma_f32_16x16x32_bf16 v[56:59], v[172:175], a[252:255], v[56:59]\n\t"
    "ds_read_b128 v[124:127], v1 offset:12288\n\t"
    "v_mfma_f32_16x16x32_bf16 v[56:59], v[164:167], a[248:251], v[56:59]\n\t"
    "ds_read_b128 v[128:131], v2 offset:12288\n\t"
    "v_cvt_pk_bf16_f32 v54, v68, v69\n\t"
    "v_mfma_f32_16x16x32_bf16 v[56:59], v[156:159], a[244:247], v[56:59]\n\t"
    "ds_read_b128 v[140:143], v3 offset:12288\n\t"
    "v_cvt_pk_bf16_f32 v55, v70, v71\n\t"
    "v_mfma_f32_16x16x32_bf16 v[56:59], v[148:151], a[240:243], v[56:59]\n\t"
    "ds_read_b128 v[144:147], v4 offset:12288\n\t"
    "s_waitcnt vmcnt(7)\n\t"
    "v_mfma_f32_16x16x32_bf16 v[72:75], v[148:151], v[232:235], 0\n\t"
    "ds_read_b128 v[104:107], v5 offset:12288\n\t"
    "v_mfma_f32_16x16x32_bf16 v[72:75], v[156:159], v[236:239], v[72:75]\n\t"
    "v_mfma_f32_16x16x32_bf16 v[72:75], v[164:167], v[240:243], v[72:75]\n\t"
    "ds_read_b128 v[152:155], v5 offset:18432\n\t"
    "buffer_store_dwordx4 v[176:179], v7, s[52:55], s61 offen offset:1152 sc1\n\t"
    "v_mfma_f32_16x16x32_bf16 v[72:75], v[172:175], v[244:247], v[72:75]\n\t"
    "ds_read_b128 v[148:151], v4 offset:18432\n\t"
    "v_cvt_pk_bf16_f32 v52, v56, v57\n\t"
    "v_mfma_f32_16x16x32_bf16 v[72:75], v[180:183], v[248:251], v[72:75]\n\t"
    "ds_read_b128 v[156:159], v3 offset:18432\n\t"
    "v_cvt_pk_bf16_f32 v53, v58, v59\n\t"
    "v_mfma_f32_16x16x32_bf16 v[72:75], v[184:187], v[252:255], v[72:75]\n\t"
    "ds_read_b128 v[160:163], v2 offset:18432\n\t"
    "v_mfma_f32_16x16x32_bf16 v[68:71], v[64:67], v[252:255], 0\n\t"
    "ds_read_b128 v[164:167], v1 offset:18432\n\t"
    "v_mfma_f32_16x16x32_bf16 v[68:71], v[60:63], v[248:251], v[68:71]\n\t"
    "ds_read_b128 v[168:171], v0 offset:18432\n\t"
    "v_mfma_f32_16x16x32_bf16 v[68:71], v[84:87], v[244:247], v[68:71]\n\t"
    "v_mfma_f32_16x16x32_bf16 v[68:71], v[108:111], v[240:243], v[68:71]\n\t"
    "v_cvt_pk_bf16_f32 v56, v72, v73\n\t"
    "v_cvt_pk_bf16_f32 v57, v74, v75\n\t"
    "v_mfma_f32_16x16x32_bf16 v[68:71], v[80:83], v[236:239], v[68:71]\n\t"
    "v_mfma_f32_16x16x32_bf16 v[68:71], v[76:79], v[232:235], v[68:71]\n\t"
    "s_waitcnt lgkmcnt(3)\n\t"
    "v_mfma_f32_16x16x32_bf16 v[64:67], v[116:119], a[0:3], 0\n\t"
    "v_mfma_f32_16x16x32_bf16 v[64:67], v[124:127], a[4:7], v[64:67]\n\t"
    "v_mfma_f32_16x16x32_bf16 v[64:67], v[128:131], a[8:11], v[64:67]\n\t"
    "buffer_store_dwordx2 v[136:137], v8, s[52:55], s61 offen offset:0 sc1\n\t"
    "v_mfma_f32_16x16x32_bf16 v[64:67], v[140:143], a[12:15], v[64:67]\n\t"
    "v_cvt_pk_bf16_f32 v58, v68, v69\n\t"
    "v_cvt_pk_bf16_f32 v59, v70, v71\n\t"
    "v_mfma_f32_16x16x32_bf16 v[64:67], v[144:147], a[16:19], v[64:67]\n\t"
    "v_mfma_f32_16x16x32_bf16 v[64:67], v[104:107], a[20:23], v[64:67]\n\t"
    "v_mfma_f32_16x16x32_bf16 v[68:71], v[152:155], a[20:23], 0\n\t"
    "v_mfma_f32_16x16x32_bf16 v[68:71], v[148:151], a[16:19], v[68:71]\n\t"
    "v_mfma_f32_16x16x32_bf16 v[68:71], v[156:159], a[12:15], v[68:71]\n\t"
    "s_waitcnt lgkmcnt(0)\n\t"
    "v_mfma_f32_16x16x32_bf16 v[68:71], v[160:163], a[8:11], v[68:71]\n\t"
    "v_cvt_pk_bf16_f32 v60, v64, v65\n\t"
    "v_cvt_pk_bf16_f32 v61, v66, v67\n\t"
    "v_mfma_f32_16x16x32_bf16 v[68:71], v[164:167], a[4:7], v[68:71]\n\t"
    "v_mfma_f32_16x16x32_bf16 v[68:71], v[168:171], a[0:3], v[68:71]\n\t"
    "v_mfma_f32_16x16x32_bf16 v[72:75], v[168:171], a[24:27], 0\n\t"
    "v_mfma_f32_16x16x32_bf16 v[72:75], v[164:167], a[28:31], v[72:75]\n\t"
    "v_mfma_f32_16x16x32_bf16 v[72:75], v[160:163], a[32:35], v[72:75]\n\t"
    "buffer_store_dwordx2 v[138:139], v8, s[52:55], s61 offen offset:384 sc1\n\t"
    "v_mfma_f32_16x16x32_bf16 v[72:75], v[156:159], a[36:39], v[72:75]\n\t"
    "v_cvt_pk_bf16_f32 v62, v68, v69\n\t"
    "v_cvt_pk_bf16_f32 v63, v70, v71\n\t"
    "v_mfma_f32_16x16x32_bf16 v[72:75], v[148:151], a[40:43], v[72:75]\n\t"
    "v_mfma_f32_16x16x32_bf16 v[72:75], v[152:155], a[44:47], v[72:75]\n\t"
    "v_mfma_f32_16x16x32_bf16 v[68:71], v[104:107], a[44:47], 0\n\t"
    "v_mfma_f32_16x16x32_bf16 v[68:71], v[144:147], a[40:43], v[68:71]\n\t"
    "v_mfma_f32_16x16x32_bf16 v[68:71], v[140:143], a[36:39], v[68:71]\n\t"
    "v_mfma_f32_16x16x32_bf16 v[68:71], v[128:131], a[32:35], v[68:71]\n\t"
    "v_cvt_pk_bf16_f32 v66, v72, v73\n\t"
    "v_cvt_pk_bf16_f32 v67, v74, v75\n\t"
    "v_mfma_f32_16x16x32_bf16 v[68:71], v[124:127], a[28:31], v[68:71]\n\t"
    "v_mfma_f32_16x16x32_bf16 v[68:71], v[116:119], a[24:27], v[68:71]\n\t"
    "v_mfma_f32_16x16x32_bf16 v[72:75], v[116:119], a[48:51], 0\n\t"
    "v_mfma_f32_16x16x32_bf16 v[72:75], v[124:127], a[52:55], v[72:75]\n\t"
    "v_mfma_f32_16x16x32_bf16 v[72:75], v[128:131], a[56:59], v[72:75]\n\t"
    "buffer_store_dwordx2 v[132:133], v8, s[52:55], s61 offen offset:768 sc1\n\t"
    "v_mfma_f32_16x16x32_bf16 v[72:75], v[140:143], a[60:63], v[72:75]\n\t"
    "v_cvt_pk_bf16_f32 v64, v68, v69\n\t"
    "v_cvt_pk_bf16_f32 v65, v70, v71\n\t"
    "v_mfma_f32_16x16x32_bf16 v[72:75], v[144:147], a[64:67], v[72:75]\n\t"
    "v_mfma_f32_16x16x32_bf16 v[72:75], v[104:107], a[68:71], v[72:75]\n\t"
    "v_mfma_f32_16x16x32_bf16 v[76:79], v[152:155], a[68:71], 0\n\t"
    "v_mfma_f32_16x16x32_bf16 v[76:79], v[148:151], a[64:67], v[76:79]\n\t"
    "v_mfma_f32_16x16x32_bf16 v[76:79], v[156:159], a[60:63], v[76:79]\n\t"
    "s_add_i32 s64, s62, 24576\n\t"
    "v_mfma_f32_16x16x32_bf16 v[76:79], v[160:163], a[56:59], v[76:79]\n\t"
    "s_cmp_eq_u32 s64, %[rend]\n\t"
    "s_cselect_b32 s64, %[rbeg], s64\n\t"
    "v_cvt_pk_bf16_f32 v69, v74, v75\n\t"
    "v_mfma_f32_16x16x32_bf16 v[76:79], v[164:167], a[52:55], v[76:79]\n\t"
    "s_sub_i32 s65, s64, s62\n\t"
    "s_mov_b32 s62, s64\n\t"
    "v_mfma_f32_16x16x32_bf16 v[76:79], v[168:171], a[48:51], v[76:79]\n\t"
    "v_add_u32_e32 v0, s65, v0\n\t"
    "v_add_u32_e32 v1, s65, v1\n\t"
    "v_mfma_f32_16x16x32_bf16 v[80:83], v[168:171], a[72:75], 0\n\t"
    "v_add_u32_e32 v2, s65, v2\n\t"
    "v_add_u32_e32 v3, s65, v3\n\t"
    "v_mfma_f32_16x16x32_bf16 v[80:83], v[164:167], a[76:79], v[80:83]\n\t"
    "v_add_u32_e32 v4, s65, v4\n\t"
    "v_add_u32_e32 v5, s65, v5\n\t"
    "v_mfma_f32_16x16x32_bf16 v[80:83], v[160:163], a[80:83], v[80:83]\n\t"
    "buffer_store_dwordx2 v[134:135], v8, s[52:55], s61 offen offset:1152 sc1\n\t"
    "v_mfma_f32_16x16x32_bf16 v[80:83], v[156:159], a[84:87], v[80:83]\n\t"
    "s_add_u32 s61, s61, 393216\n\t"
    "v_cvt_pk_bf16_f32 v68, v72, v73\n\t"
    "v_mfma_f32_16x16x32_bf16 v[80:83], v[148:151], a[88:91], v[80:83]\n\t"
    "v_cvt_pk_bf16_f32 v70, v76, v77\n\t"
    "v_cvt_pk_bf16_f32 v71, v78, v79\n\t"
    "v_mfma_f32_16x16x32_bf16 v[80:83], v[152:155], a[92:95], v[80:83]\n\t"
    "v_mfma_f32_16x16x32_bf16 v[84:87], v[104:107], a[92:95], 0\n\t"
    "v_mfma_f32_16x16x32_bf16 v[84:87], v[144:147], a[88:91], v[84:87]\n\t"
    "v_mfma_f32_16x16x32_bf16 v[84:87], v[140:143], a[84:87], v[84:87]\n\t"
    "v_mfma_f32_16x16x32_bf16 v[84:87], v[128:131], a[80:83], v[84:87]\n\t"
    "v_cvt_pk_bf16_f32 v74, v80, v81\n\t"
    "v_cvt_pk_bf16_f32 v75, v82, v83\n\t"
    "v_mfma_f32_16x16x32_bf16 v[84:87], v[124:127], a[76:79], v[84:87]\n\t"
    "v_mfma_f32_16x16x32_bf16 v[84:87], v[116:119], a[72:75], v[84:87]\n\t"
    "v_mfma_f32_16x16x32_bf16 v[80:83], v[116:119], a[96:99], 0\n\t"
    "v_mfma_f32_16x16x32_bf16 v[80:83], v[124:127], a[100:103], v[80:83]\n\t"
    "v_mfma_f32_16x16x32_bf16 v[80:83], v[128:131], a[104:107], v[80:83]\n\t"
    "buffer_store_dwordx4 v[216:219], v7, s[52:55], s61 offen offset:0 sc1\n\t"
    "v_mfma_f32_16x16x32_bf16 v[80:83], v[140:143], a[108:111], v[80:83]\n\t"
    "v_cvt_pk_bf16_f32 v72, v84, v85\n\t"
    "v_cvt_pk_bf16_f32 v73, v86, v87\n\t"
    "v_mfma_f32_16x16x32_bf16 v[80:83], v[144:147], a[112:115], v[80:83]\n\t"
    "v_mfma_f32_16x16x32_bf16 v[80:83], v[104:107], a[116:119], v[80:83]\n\t"
    "v_mfma_f32_16x16x32_bf16 v[84:87], v[152:155], a[116:119], 0\n\t"
    "v_mfma_f32_16x16x32_bf16 v[84:87], v[148:151], a[112:115], v[84:87]\n\t"
    "v_mfma_f32_16x16x32_bf16 v[84:87], v[156:159], a[108:111], v[84:87]\n\t"
    "v_mfma_f32_16x16x32_bf16 v[84:87], v[160:163], a[104:107], v[84:87]\n\t"
    "v_cvt_pk_bf16_f32 v76, v80, v81\n\t"
    "v_cvt_pk_bf16_f32 v77, v82, v83\n\t"
    "v_mfma_f32_16x16x32_bf16 v[84:87], v[164:167], a[100:103], v[84:87]\n\t"
    "v_mfma_f32_16x16x32_bf16 v[84:87], v[168:171], a[96:99], v[84:87]\n\t"
    "v_mfma_f32_16x16x32_bf16 v[88:91], v[168:171], a[120:123], 0\n\t"
    "v_mfma_f32_16x16x32_bf16 v[88:91], v[164:167], a[124:127], v[88:91]\n\t"
    "v_mfma_f32_16x16x32_bf16 v[88:91], v[160:163], a[128:131], v[88:91]\n\t"
    "buffer_store_dwordx4 v[212:215], v7, s[52:55], s61 offen offset:384 sc1\n\t"
    "v_mfma_f32_16x16x32_bf16 v[88:91], v[156:159], a[132:135], v[88:91]\n\t"
    "v_cvt_pk_bf16_f32 v78, v84, v85\n\t"
    "v_cvt_pk_bf16_f32 v79, v86, v87\n\t"
    "v_mfma_f32_16x16x32_bf16 v[88:91], v[148:151], a[136:139], v[88:91]\n\t"
    "v_mfma_f32_16x16x32_bf16 v[88:91], v[152:155], a[140:143], v[88:91]\n\t"
    "v_mfma_f32_16x16x32_bf16 v[84:87], v[104:107], a[140:143], 0\n\t"
    "v_mfma_f32_16x16x32_bf16 v[84:87], v[144:147], a[136:139], v[84:87]\n\t"
    "v_mfma_f32_16x16x32_bf16 v[84:87], v[140:143], a[132:135], v[84:87]\n\t"
    "v_mfma_f32_16x16x32_bf16 v[84:87], v[128:131], a[128:131], v[84:87]\n\t"
    "v_cvt_pk_bf16_f32 v82, v88, v89\n\t"
    "v_cvt_pk_bf16_f32 v83, v90, v91\n\t"
    "v_mfma_f32_16x16x32_bf16 v[84:87], v[124:127], a[124:127], v[84:87]\n\t"
    "v_mfma_f32_16x16x32_bf16 v[84:87], v[116:119], a[120:123], v[84:87]\n\t"
    "v_mfma_f32_16x16x32_bf16 v[88:91], v[116:119], a[144:147], 0\n\t"
    "v_mfma_f32_16x16x32_bf16 v[88:91], v[124:127], a[148:151], v[88:91]\n\t"
    "v_mfma_f32_16x16x32_bf16 v[88:91], v[128:131], a[152:155], v[88:91]\n\t"
    "buffer_store_dwordx4 v[204:207], v7, s[52:55], s61 offen offset:768 sc1\n\t"
    "v_mfma_f32_16x16x32_bf16 v[88:91], v[140:143], a[156:159], v[88:91]\n\t"
    "v_cvt_pk_bf16_f32 v80, v84, v85\n\t"
    "v_cvt_pk_bf16_f32 v81, v86, v87\n\t"
    "v_mfma_f32_16x16x32_bf16 v[88:91], v[144:147], a[160:163], v[88:91]\n\t"
    "v_mfma_f32_16x16x32_bf16 v[88:91], v[104:107], a[164:167], v[88:91]\n\t"
    "v_mfma_f32_16x16x32_bf16 v[92:95], v[152:155], a[164:167], 0\n\t"
    "v_mfma_f32_16x16x32_bf16 v[92:95], v[148:151], a[160:163], v[92:95]\n\t"
    "v_mfma_f32_16x16x32_bf16 v[92:95], v[156:159], a[156:159], v[92:95]\n\t"
    "v_mfma_f32_16x16x32_bf16 v[92:95], v[160:163], a[152:155], v[92:95]\n\t"
    "v_cvt_pk_bf16_f32 v84, v88, v89\n\t"
    "v_cvt_pk_bf16_f32 v85, v90, v91\n\t"
    "v_mfma_f32_16x16x32_bf16 v[92:95], v[164:167], a[148:151], v[92:95]\n\t"
    "v_mfma_f32_16x16x32_bf16 v[92:95], v[168:171], a[144:147], v[92:95]\n\t"
    "v_mfma_f32_16x16x32_bf16 v[100:103], v[168:171], a[168:171], 0\n\t"
    "v_mfma_f32_16x16x32_bf16 v[100:103], v[164:167], a[172:175], v[100:103]\n\t"
    "v_mfma_f32_16x16x32_bf16 v[100:103], v[160:163], a[176:179], v[100:103]\n\t"
    "buffer_store_dwordx4 v[120:123], v7, s[52:55], s61 offen offset:1152 sc1\n\t"
    "v_mfma_f32_16x16x32_bf16 v[100:103], v[156:159], a[180:183], v[100:103]\n\t"
    "v_cvt_pk_bf16_f32 v86, v92, v93\n\t"
    "v_cvt_pk_bf16_f32 v87, v94, v95\n\t"
    "v_mfma_f32_16x16x32_bf16 v[100:103], v[148:151], a[184:187], v[100:103]\n\t"
    "v_mfma_f32_16x16x32_bf16 v[100:103], v[152:155], a[188:191], v[100:103]\n\t"
    "v_mfma_f32_16x16x32_bf16 v[92:95], v[104:107], a[188:191], 0\n\t"
    "v_mfma_f32_16x16x32_bf16 v[92:95], v[144:147], a[184:187], v[92:95]\n\t"
    "v_mfma_f32_16x16x32_bf16 v[92:95], v[140:143], a[180:183], v[92:95]\n\t"
    "v_mfma_f32_16x16x32_bf16 v[92:95], v[128:131], a[176:179], v[92:95]\n\t"
    "v_cvt_pk_bf16_f32 v90, v100, v101\n\t"
    "v_cvt_pk_bf16_f32 v91, v102, v103\n\t"
    "v_mfma_f32_16x16x32_bf16 v[92:95], v[124:127], a[172:175], v[92:95]\n\t"
    "v_mfma_f32_16x16x32_bf16 v[92:95], v[116:119], a[168:171], v[92:95]\n\t"
    "v_mfma_f32_16x16x32_bf16 v[100:103], v[116:119], a[192:195], 0\n\t"
    "v_mfma_f32_16x16x32_bf16 v[100:103], v[124:127], a[196:199], v[100:103]\n\t"
    "v_mfma_f32_16x16x32_bf16 v[100:103], v[128:131], a[200:203], v[100:103]\n\t"
    "buffer_store_dwordx2 v[96:97], v8, s[52:55], s61 offen offset:0 sc1\n\t"
    "v_mfma_f32_16x16x32_bf16 v[100:103], v[140:143], a[204:207], v[100:103]\n\t"
    "v_cvt_pk_bf16_f32 v88, v92, v93\n\t"
    "v_cvt_pk_bf16_f32 v89, v94, v95\n\t"
    "v_mfma_f32_16x16x32_bf16 v[100:103], v[144:147], a[208:211], v[100:103]\n\t"
    "v_mfma_f32_16x16x32_bf16 v[100:103], v[104:107], a[212:215], v[100:103]\n\t"
    "v_mfma_f32_16x16x32_bf16 v[108:111], v[152:155], a[212:215], 0\n\t"
    "v_mfma_f32_16x16x32_bf16 v[108:111], v[148:151], a[208:211], v[108:111]\n\t"
    "v_mfma_f32_16x16x32_bf16 v[108:111], v[156:159], a[204:207], v[108:111]\n\t"
    "v_mfma_f32_16x16x32_bf16 v[108:111], v[160:163], a[200:203], v[108:111]\n\t"
    "v_cvt_pk_bf16_f32 v92, v100, v101\n\t"
    "v_cvt_pk_bf16_f32 v93, v102, v103\n\t"
    "v_mfma_f32_16x16x32_bf16 v[108:111], v[164:167], a[196:199], v[108:111]\n\t"
    "v_mfma_f32_16x16x32_bf16 v[108:111], v[168:171], a[192:195], v[108:111]\n\t"
    "v_mfma_f32_16x16x32_bf16 v[100:103], v[168:171], a[216:219], 0\n\t"
    "v_mfma_f32_16x16x32_bf16 v[100:103], v[164:167], a[220:223], v[100:103]\n\t"
    "v_mfma_f32_16x16x32_bf16 v[100:103], v[160:163], a[224:227], v[100:103]\n\t"
    "buffer_store_dwordx2 v[98:99], v8, s[52:55], s61 offen offset:384 sc1\n\t"
    "v_mfma_f32_16x16x32_bf16 v[100:103], v[156:159], a[228:231], v[100:103]\n\t"
    "v_cvt_pk_bf16_f32 v94, v108, v109\n\t"
    "v_cvt_pk_bf16_f32 v95, v110, v111\n\t"
    "v_mfma_f32_16x16x32_bf16 v[100:103], v[148:151], a[232:235], v[100:103]\n\t"
    "v_mfma_f32_16x16x32_bf16 v[100:103], v[152:155], a[236:239], v[100:103]\n\t"
    "v_mfma_f32_16x16x32_bf16 v[108:111], v[104:107], a[236:239], 0\n\t"
    "v_mfma_f32_16x16x32_bf16 v[108:111], v[144:147], a[232:235], v[108:111]\n\t"
    "v_mfma_f32_16x16x32_bf16 v[108:111], v[140:143], a[228:231], v[108:111]\n\t"
    "v_mfma_f32_16x16x32_bf16 v[108:111], v[128:131], a[224:227], v[108:111]\n\t"
    "v_cvt_pk_bf16_f32 v98, v100, v101\n\t"
    "v_cvt_pk_bf16_f32 v99, v102, v103\n\t"
    "v_mfma_f32_16x16x32_bf16 v[108:111], v[124:127], a[220:223], v[108:111]\n\t"
    "v_mfma_f32_16x16x32_bf16 v[108:111], v[116:119], a[216:219], v[108:111]\n\t"
    "v_mfma_f32_16x16x32_bf16 v[120:123], v[116:119], a[240:243], 0\n\t"
    "v_mfma_f32_16x16x32_bf16 v[120:123], v[124:127], a[244:247], v[120:123]\n\t"
    "v_mfma_f32_16x16x32_bf16 v[120:123], v[128:131], a[248:251], v[120:123]\n\t"
    "buffer_store_dwordx2 v[112:113], v8, s[52:55], s61 offen offset:768 sc1\n\t"
    "v_mfma_f32_16x16x32_bf16 v[120:123], v[140:143], a[252:255], v[120:123]\n\t"
    "v_cvt_pk_bf16_f32 v96, v108, v109\n\t"
    "v_cvt_pk_bf16_f32 v97, v110, v111\n\t"
    "v_mfma_f32_16x16x32_bf16 v[120:123], v[144:147], v[224:227], v[120:123]\n\t"
    "v_mfma_f32_16x16x32_bf16 v[120:123], v[104:107], v[228:231], v[120:123]\n\t"
    "v_mfma_f32_16x16x32_bf16 v[108:111], v[152:155], v[228:231], 0\n\t"
    "v_mfma_f32_16x16x32_bf16 v[108:111], v[148:151], v[224:227], v[108:111]\n\t"
    "v_mfma_f32_16x16x32_bf16 v[108:111], v[156:159], a[252:255], v[108:111]\n\t"
    "v_mfma_f32_16x16x32_bf16 v[108:111], v[160:163], a[248:251], v[108:111]\n\t"
    "v_cvt_pk_bf16_f32 v100, v120, v121\n\t"
    "v_cvt_pk_bf16_f32 v101, v122, v123\n\t"
    "v_mfma_f32_16x16x32_bf16 v[108:111], v[164:167], a[244:247], v[108:111]\n\t"
    "v_mfma_f32_16x16x32_bf16 v[108:111], v[168:171], a[240:243], v[108:111]\n\t"
    "v_mfma_f32_16x16x32_bf16 v[120:123], v[168:171], v[232:235], 0\n\t"
    "s_barrier\n\t"
    "v_mfma_f32_16x16x32_bf16 v[120:123], v[164:167], v[236:239], v[120:123]\n\t"
    "ds_read_b128 v[132:135], v6 offset:0\n\t"
    "v_mfma_f32_16x16x32_bf16 v[120:123], v[160:163], v[240:243], v[120:123]\n\t"
    "ds_read_b128 v[136:139], v6 offset:1024\n\t"
    "v_mfma_f32_16x16x32_bf16 v[120:123], v[156:159], v[244:247], v[120:123]\n\t"
    "buffer_store_dwordx2 v[114:115], v8, s[52:55], s61 offen offset:1152 sc1\n\t"
    "v_mfma_f32_16x16x32_bf16 v[120:123], v[148:151], v[248:251], v[120:123]\n\t"
    "s_add_u32 s61, s61, 11403264\n\t"
    "v_cvt_pk_bf16_f32 v102, v108, v109\n\t"
    "v_mfma_f32_16x16x32_bf16 v[120:123], v[152:155], v[252:255], v[120:123]\n\t"
    "v_cvt_pk_bf16_f32 v103, v110, v111\n\t"
    "v_mfma_f32_16x16x32_bf16 v[112:115], v[104:107], v[252:255], 0\n\t"
    "v_mfma_f32_16x16x32_bf16 v[112:115], v[144:147], v[248:251], v[112:115]\n\t"
    "v_mfma_f32_16x16x32_bf16 v[112:115], v[140:143], v[244:247], v[112:115]\n\t"
    "v_mfma_f32_16x16x32_bf16 v[112:115], v[128:131], v[240:243], v[112:115]\n\t"
    "v_cvt_pk_bf16_f32 v106, v120, v121\n\t"
    "v_cvt_pk_bf16_f32 v107, v122, v123\n\t"
    "v_mfma_f32_16x16x32_bf16 v[112:115], v[124:127], v[236:239], v[112:115]\n\t"
    "v_mfma_f32_16x16x32_bf16 v[112:115], v[116:119], v[232:235], v[112:115]\n\t"
    "s_waitcnt lgkmcnt(0)\n\t"
    "v_mfma_f32_16x16x32_bf16 v[108:111], v[132:135], v[12:15], 0\n\t"
    "v_mfma_f32_16x16x32_bf16 v[108:111], v[136:139], v[60:63], v[108:111]\n\t"
    "v_mfma_f32_16x16x32_bf16 v[116:119], v[136:139], v[64:67], 0\n\t"
    "v_mfma_f32_16x16x32_bf16 v[116:119], v[132:135], v[16:19], v[116:119]\n\t"
    "v_cvt_pk_bf16_f32 v104, v112, v113\n\t"
    "v_cvt_pk_bf16_f32 v105, v114, v115\n\t"
    "v_mfma_f32_16x16x32_bf16 v[120:123], v[132:135], v[20:23], 0\n\t"
    "v_mfma_f32_16x16x32_bf16 v[120:123], v[136:139], v[68:71], v[120:123]\n\t"
    "v_mfma_f32_16x16x32_bf16 v[112:115], v[136:139], v[72:75], 0\n\t"
    "v_mfma_f32_16x16x32_bf16 v[112:115], v[132:135], v[24:27], v[112:115]\n\t"
    "v_cvt_pk_bf16_f32 v124, v108, v116\n\t"
    "v_cvt_pk_bf16_f32 v140, v109, v117\n\t"
    "v_mfma_f32_16x16x32_bf16 v[128:131], v[132:135], v[28:31], 0\n\t"
    "v_cvt_pk_bf16_f32 v144, v110, v118\n\t"
    "v_cvt_pk_bf16_f32 v148, v111, v119\n\t"
    "v_mfma_f32_16x16x32_bf16 v[128:131], v[136:139], v[76:79], v[128:131]\n\t"
    "v_mfma_f32_16x16x32_bf16 v[152:155], v[136:139], v[80:83], 0\n\t"
    "v_mfma_f32_16x16x32_bf16 v[152:155], v[132:135], v[32:35], v[152:155]\n\t"
    "v_cvt_pk_bf16_f32 v125, v120, v112\n\t"
    "v_cvt_pk_bf16_f32 v141, v121, v113\n\t"
    "v_mfma_f32_16x16x32_bf16 v[108:111], v[132:135], v[36:39], 0\n\t"
    "v_cvt_pk_bf16_f32 v145, v122, v114\n\t"
    "v_cvt_pk_bf16_f32 v149, v123, v115\n\t"
    "v_mfma_f32_16x16x32_bf16 v[108:111], v[136:139], v[84:87], v[108:111]\n\t"
    "ds_read_b128 v[156:159], v6 offset:2048\n\t"
    "v_mfma_f32_16x16x32_bf16 v[116:119], v[136:139], v[88:91], 0\n\t"
    "ds_read_b128 v[160:163], v6 offset:3072\n\t"
    "v_mfma_f32_16x16x32_bf16 v[116:119], v[132:135], v[40:43], v[116:119]\n\t"
    "v_cvt_pk_bf16_f32 v126, v128, v152\n\t"
    "v_cvt_pk_bf16_f32 v142, v129, v153\n\t"
    "v_mfma_f32_16x16x32_bf16 v[120:123], v[132:135], v[44:47], 0\n\t"
    "v_cvt_pk_bf16_f32 v146, v130, v154\n\t"
    "v_cvt_pk_bf16_f32 v150, v131, v155\n\t"
    "v_mfma_f32_16x16x32_bf16 v[120:123], v[136:139], v[92:95], v[120:123]\n\t"
    "v_mfma_f32_16x16x32_bf16 v[164:167], v[136:139], v[96:99], 0\n\t"
    "v_mfma_f32_16x16x32_bf16 v[164:167], v[132:135], v[48:51], v[164:167]\n\t"
    "v_cvt_pk_bf16_f32 v127, v108, v116\n\t"
    "v_cvt_pk_bf16_f32 v143, v109, v117\n\t"
    "v_mfma_f32_16x16x32_bf16 v[128:131], v[132:135], v[52:55], 0\n\t"
    "v_cvt_pk_bf16_f32 v147, v110, v118\n\t"
    ".Lh2_%=:\n\t"
    "s_cbranch_execz .LrM_%=\n\t"
    "v_cvt_pk_bf16_f32 v151, v111, v119\n\t"
    "v_mfma_f32_16x16x32_bf16 v[128:131], v[136:139], v[100:103], v[128:131]\n\t"
    "v_mfma_f32_16x16x32_bf16 v[152:155], v[136:139], v[104:107], 0\n\t"
    "v_mfma_f32_16x16x32_bf16 v[152:155], v[132:135], v[56:59], v[152:155]\n\t"
    "v_cvt_pk_bf16_f32 v108, v120, v164\n\t"
    "s_waitcnt lgkmcnt(0)\n\t"
    "v_mfma_f32_16x16x32_bf16 v[168:171], v[156:159], v[12:15], 0\n\t"
    "buffer_store_dwordx4 v[124:127], v7, s[52:55], s61 offen offset:0 sc1\n\t"
    "v_mfma_f32_16x16x32_bf16 v[168:171], v[160:163], v[60:63], v[168:171]\n\t"
    "v_cvt_pk_bf16_f32 v110, v121, v165\n\t"
    "v_cvt_pk_bf16_f32 v112, v122, v166\n\t"
    "v_mfma_f32_16x16x32_bf16 v[132:135], v[160:163], v[64:67], 0\n\t"
    "v_cvt_pk_bf16_f32 v114, v123, v167\n\t"
    "v_mfma_f32_16x16x32_bf16 v[132:135], v[156:159], v[16:19], v[132:135]\n\t"
    "v_cvt_pk_bf16_f32 v109, v128, v152\n\t"
    "v_cvt_pk_bf16_f32 v111, v129, v153\n\t"
    "v_mfma_f32_16x16x32_bf16 v[172:175], v[156:159], v[20:23], 0\n\t"
    "v_cvt_pk_bf16_f32 v113, v130, v154\n\t"
    "v_cvt_pk_bf16_f32 v115, v131, v155\n\t"
    "v_mfma_f32_16x16x32_bf16 v[172:175], v[160:163], v[68:71], v[172:175]\n\t"
    "v_mfma_f32_16x16x32_bf16 v[164:167], v[160:163], v[72:75], 0\n\t"
    "v_mfma_f32_16x16x32_bf16 v[164:167], v[156:159], v[24:27], v[164:167]\n\t"
    "v_cvt_pk_bf16_f32 v120, v168, v132\n\t"
    "v_cvt_pk_bf16_f32 v116, v169, v133\n\t"
    "v_mfma_f32_16x16x32_bf16 v[152:155], v[156:159], v[28:31], 0\n\t"
    "buffer_store_dwordx4 v[140:143], v7, s[52:55], s61 offen offset:384 sc1\n\t"
    "v_mfma_f32_16x16x32_bf16 v[152:155], v[160:163], v[76:79], v[152:155]\n\t"
    "v_cvt_pk_bf16_f32 v128, v170, v134\n\t"
    "v_cvt_pk_bf16_f32 v124, v171, v135\n\t"
    "v_mfma_f32_16x16x32_bf16 v[176:179], v[160:163], v[80:83], 0\n\t"
    "v_mfma_f32_16x16x32_bf16 v[176:179], v[156:159], v[32:35], v[176:179]\n\t"
    "v_cvt_pk_bf16_f32 v121, v172, v164\n\t"
    "v_cvt_pk_bf16_f32 v117, v173, v165\n\t"
    "v_mfma_f32_16x16x32_bf16 v[140:143], v[156:159], v[36:39], 0\n\t"
    "v_cvt_pk_bf16_f32 v129, v174, v166\n\t"
    "v_cvt_pk_bf16_f32 v125, v175, v167\n\t"
    "v_mfma_f32_16x16x32_bf16 v[140:143], v[160:163], v[84:87], v[140:143]\n\t"
    "ds_read_b128 v[132:135], v6 offset:4096\n\t"
    "v_mfma_f32_16x16x32_bf16 v[168:171], v[160:163], v[88:91], 0\n\t"
    "ds_read_b128 v[136:139], v6 offset:5120\n\t"
    "v_mfma_f32_16x16x32_bf16 v[168:171], v[156:159], v[40:43], v[168:171]\n\t"
    "v_cvt_pk_bf16_f32 v122, v152, v176\n\t"
    "v_cvt_pk_bf16_f32 v118, v153, v177\n\t"
    "v_mfma_f32_16x16x32_bf16 v[164:167], v[156:159], v[44:47], 0\n\t"
    "v_cvt_pk_bf16_f32 v130, v154, v178\n\t"
    "v_cvt_pk_bf16_f32 v126, v155, v179\n\t"
    "v_mfma_f32_16x16x32_bf16 v[164:167], v[160:163], v[92:95], v[164:167]\n\t"
    "v_mfma_f32_16x16x32_bf16 v[172:175], v[160:163], v[96:99], 0\n\t"
    "buffer_store_dwordx4 v[144:147], v7, s[52:55], s61 offen offset:768 sc1\n\t"
    "v_mfma_f32_16x16x32_bf16 v[172:175], v[156:159], v[48:51], v[172:175]\n\t"
    "v_cvt_pk_bf16_f32 v123, v140, v168\n\t"
    "v_cvt_pk_bf16_f32 v119, v141, v169\n\t"
    "v_mfma_f32_16x16x32_bf16 v[176:179], v[156:159], v[52:55], 0\n\t"
    "v_cvt_pk_bf16_f32 v131, v142, v170\n\t"
    "v_cvt_pk_bf16_f32 v127, v143, v171\n\t"
    "v_mfma_f32_16x16x32_bf16 v[176:179], v[160:163], v[100:103], v[176:179]\n\t"
    "v_mfma_f32_16x16x32_bf16 v[180:183], v[160:163], v[104:107], 0\n\t"
    "v_mfma_f32_16x16x32_bf16 v[180:183], v[156:159], v[56:59], v[180:183]\n\t"
    "v_cvt_pk_bf16_f32 v140, v164, v172\n\t"
    "s_waitcnt lgkmcnt(0)\n\t"
    "v_mfma_f32_16x16x32_bf16 v[188:191], v[132:135], v[12:15], 0\n\t"
    "v_cvt_pk_bf16_f32 v142, v165, v173\n\t"
    "v_cvt_pk_bf16_f32 v144, v166, v174\n\t"
    "v_mfma_f32_16x16x32_bf16 v[188:191], v[136:139], v[60:63], v[188:191]\n\t"
    "v_cvt_pk_bf16_f32 v146, v167, v175\n\t"
    "v_mfma_f32_16x16x32_bf16 v[192:195], v[136:139], v[64:67], 0\n\t"
    "v_mfma_f32_16x16x32_bf16 v[192:195], v[132:135], v[16:19], v[192:195]\n\t"
    "v_cvt_pk_bf16_f32 v141, v176, v180\n\t"
    "v_cvt_pk_bf16_f32 v143, v177, v181\n\t"
    "v_mfma_f32_16x16x32_bf16 v[196:199], v[132:135], v[20:23], 0\n\t"
    "buffer_store_dwordx4 v[148:151], v7, s[52:55], s61 offen offset:1152 sc1\n\t"
    "v_mfma_f32_16x16x32_bf16 v[196:199], v[136:139], v[68:71], v[196:199]\n\t"
    "v_cvt_pk_bf16_f32 v145, v178, v182\n\t"
    "v_cvt_pk_bf16_f32 v147, v179, v183\n\t"
    "v_mfma_f32_16x16x32_bf16 v[200:203], v[136:139], v[72:75], 0\n\t"
    "v_mfma_f32_16x16x32_bf16 v[200:203], v[132:135], v[24:27], v[200:203]\n\t"
    "ds_read_b128 v[148:151], v0 offset:0\n\t"
    "v_cvt_pk_bf16_f32 v152, v188, v192\n\t"
    "v_mfma_f32_16x16x32_bf16 v[204:207], v[132:135], v[28:31], 0\n\t"
    "ds_read_b128 v[156:159], v1 offset:0\n\t"
    "v_cvt_pk_bf16_f32 v160, v189, v193\n\t"
    "v_mfma_f32_16x16x32_bf16 v[204:207], v[136:139], v[76:79], v[204:207]\n\t"
    "ds_read_b128 v[164:167], v2 offset:0\n\t"
    "v_cvt_pk_bf16_f32 v168, v190, v194\n\t"
    "v_mfma_f32_16x16x32_bf16 v[208:211], v[136:139], v[80:83], 0\n\t"
    "ds_read_b128 v[172:175], v3 offset:0\n\t"
    "v_cvt_pk_bf16_f32 v176, v191, v195\n\t"
    "v_mfma_f32_16x16x32_bf16 v[208:211], v[132:135], v[32:35], v[208:211]\n\t"
    "ds_read_b128 v[180:183], v4 offset:0\n\t"
    "v_cvt_pk_bf16_f32 v153, v196, v200\n\t"
    "v_mfma_f32_16x16x32_bf16 v[212:215], v[132:135], v[36:39], 0\n\t"
    "ds_read_b128 v[184:187], v5 offset:0\n\t"
    "v_cvt_pk_bf16_f32 v161, v197, v201\n\t"
    "v_mfma_f32_16x16x32_bf16 v[212:215], v[136:139], v[84:87], v[212:215]\n\t"
    "v_cvt_pk_bf16_f32 v169, v198, v202\n\t"
    "v_cvt_pk_bf16_f32 v177, v199, v203\n\t"
    "v_mfma_f32_16x16x32_bf16 v[216:219], v[136:139], v[88:91], 0\n\t"
    "buffer_store_dwordx2 v[108:109], v8, s[52:55], s61 offen offset:0 sc1\n\t"
    "v_mfma_f32_16x16x32_bf16 v[216:219], v[132:135], v[40:43], v[216:219]\n\t"
    "v_cvt_pk_bf16_f32 v154, v204, v208\n\t"
    "v_cvt_pk_bf16_f32 v162, v205, v209\n\t"
    "v_mfma_f32_16x16x32_bf16 v[188:191], v[132:135], v[44:47], 0\n\t"
    "v_cvt_pk_bf16_f32 v170, v206, v210\n\t"
    "v_cvt_pk_bf16_f32 v178, v207, v211\n\t"
    "v_mfma_f32_16x16x32_bf16 v[188:191], v[136:139], v[92:95], v[188:191]\n\t"
    "v_mfma_f32_16x16x32_bf16 v[192:195], v[136:139], v[96:99], 0\n\t"
    "v_mfma_f32_16x16x32_bf16 v[192:195], v[132:135], v[48:51], v[192:195]\n\t"
    "v_cvt_pk_bf16_f32 v155, v212, v216\n\t"
    "v_cvt_pk_bf16_f32 v163, v213, v217\n\t"
    "v_mfma_f32_16x16x32_bf16 v[196:199], v[132:135], v[52:55], 0\n\t"
    "s_sub_u32 s60, s60, 1\n\t"
    "s_cmp_lg_u32 s60, 0\n\t"
    "v_mfma_f32_16x16x32_bf16 v[196:199], v[136:139], v[100:103], v[196:199]\n\t"
    "v_cvt_pk_bf16_f32 v171, v214, v218\n\t"
    "v_cvt_pk_bf16_f32 v179, v215, v219\n\t"
    "v_mfma_f32_16x16x32_bf16 v[200:203], v[136:139], v[104:107], 0\n\t"
    "v_mfma_f32_16x16x32_bf16 v[200:203], v[132:135], v[56:59], v[200:203]\n\t"
    "s_cbranch_scc0 .Ltan_%=\n\t"
    "s_waitcnt lgkmcnt(0)\n\t"
    "v_mfma_f32_16x16x32_bf16 v[204:207], v[148:151], a[0:3], 0\n\t"
    "buffer_store_dwordx2 v[110:111], v8, s[52:55], s61 offen offset:384 sc1\n\t"
    "v_mfma_f32_16x16x32_bf16 v[204:207], v[156:159], a[4:7], v[204:207]\n\t"
    "v_cvt_pk_bf16_f32 v136, v188, v192\n\t"
    "v_cvt_pk_bf16_f32 v138, v189, v193\n\t"
    "v_mfma_f32_16x16x32_bf16 v[204:207], v[164:167], a[8:11], v[204:207]\n\t"
    "v_cvt_pk_bf16_f32 v132, v190, v194\n\t"
    "v_cvt_pk_bf16_f32 v134, v191, v195\n\t"
    "v_mfma_f32_16x16x32_bf16 v[204:207], v[172:175], a[12:15], v[204:207]\n\t"
    "v_cvt_pk_bf16_f32 v137, v196, v200\n\t"
    "v_cvt_pk_bf16_f32 v139, v197, v201\n\t"
    "v_mfma_f32_16x16x32_bf16 v[204:207], v[180:183], a[16:19], v[204:207]\n\t"
    "v_cvt_pk_bf16_f32 v133, v198, v202\n\t"
    "v_cvt_pk_bf16_f32 v135, v199, v203\n\t"
    "v_mfma_f32_16x16x32_bf16 v[204:207], v[184:187], a[20:23], v[204:207]\n\t"
    "v_mfma_f32_16x16x32_bf16 v[108:111], v[184:187], a[44:47], 0\n\t"
    "v_mfma_f32_16x16x32_bf16 v[108:111], v[180:183], a[40:43], v[108:111]\n\t"
    "v_mfma_f32_16x16x32_bf16 v[108:111], v[172:175], a[36:39], v[108:111]\n\t"
    "v_mfma_f32_16x16x32_bf16 v[108:111], v[164:167], a[32:35], v[108:111]\n\t"
    "v_mfma_f32_16x16x32_bf16 v[108:111], v[156:159], a[28:31], v[108:111]\n\t"
    "buffer_store_dwordx2 v[112:113], v8, s[52:55], s61 offen offset:768 sc1\n\t"
    "v_mfma_f32_16x16x32_bf16 v[108:111], v[148:151], a[24:27], v[108:111]\n\t"
    "s_barrier\n\t"
    "v_mfma_f32_16x16x32_bf16 v[188:191], v[148:151], a[48:51], 0\n\t"
    "v_mfma_f32_16x16x32_bf16 v[188:191], v[156:159], a[52:55], v[188:191]\n\t"
    "ds_read_b128 v[192:195], v6 offset:6144\n\t"
    "v_mfma_f32_16x16x32_bf16 v[188:191], v[164:167], a[56:59], v[188:191]\n\t"
    "ds_read_b128 v[196:199], v6 offset:7168\n\t"
    "v_mfma_f32_16x16x32_bf16 v[188:191], v[172:175], a[60:63], v[188:191]\n\t"
    "v_mfma_f32_16x16x32_bf16 v[188:191], v[180:183], a[64:67], v[188:191]\n\t"
    "v_mfma_f32_16x16x32_bf16 v[188:191], v[184:187], a[68:71], v[188:191]\n\t"
    "v_mfma_f32_16x16x32_bf16 v[200:203], v[184:187], a[92:95], 0\n\t"
    "v_mfma_f32_16x16x32_bf16 v[200:203], v[180:183], a[88:91], v[200:203]\n\t"
    "v_mfma_f32_16x16x32_bf16 v[200:203], v[172:175], a[84:87], v[200:203]\n\t"
    "buffer_store_dwordx2 v[114:115], v8, s[52:55], s61 offen offset:1152 sc1\n\t"
    "v_mfma_f32_16x16x32_bf16 v[200:203], v[164:167], a[80:83], v[200:203]\n\t"
    "s_add_u32 s61, s61, 393216\n\t"
    "v_mfma_f32_16x16x32_bf16 v[200:203], v[156:159], a[76:79], v[200:203]\n\t"
    "v_mfma_f32_16x16x32_bf16 v[200:203], v[148:151], a[72:75], v[200:203]\n\t"
    "s_waitcnt lgkmcnt(0)\n\t"
    "v_mfma_f32_16x16x32_bf16 v[112:115], v[192:195], v[12:15], 0\n\t"
    "v_mfma_f32_16x16x32_bf16 v[112:115], v[196:199], v[60:63], v[112:115]\n\t"
    "v_mfma_f32_16x16x32_bf16 v[208:211], v[196:199], v[64:67], 0\n\t"
    "v_mfma_f32_16x16x32_bf16 v[208:211], v[192:195], v[16:19], v[208:211]\n\t"
    "v_mfma_f32_16x16x32_bf16 v[60:63], v[192:195], v[20:23], 0\n\t"
    "v_mfma_f32_16x16x32_bf16 v[60:63], v[196:199], v[68:71], v[60:63]\n\t"
    "v_cvt_pk_bf16_f32 v12, v204, v205\n\t"
    "v_cvt_pk_bf16_f32 v13, v206, v207\n\t"
    "v_mfma_f32_16x16x32_bf16 v[64:67], v[196:199], v[72:75], 0\n\t"
    "buffer_store_dwordx4 v[120:123], v7, s[52:55], s61 offen offset:0 sc1\n\t"
    "v_mfma_f32_16x16x32_bf16 v[64:67], v[192:195], v[24:27], v[64:67]\n\t"
    "v_cvt_pk_bf16_f32 v216, v112, v208\n\t"
    "v_cvt_pk_bf16_f32 v212, v113, v209\n\t"
    "v_mfma_f32_16x16x32_bf16 v[68:71], v[192:195], v[28:31], 0\n\t"
    "v_cvt_pk_bf16_f32 v16, v108, v109\n\t"
    "v_cvt_pk_bf16_f32 v17, v110, v111\n\t"
    "v_mfma_f32_16x16x32_bf16 v[68:71], v[196:199], v[76:79], v[68:71]\n\t"
    "v_cvt_pk_bf16_f32 v20, v188, v189\n\t"
    "v_cvt_pk_bf16_f32 v21, v190, v191\n\t"
    "v_mfma_f32_16x16x32_bf16 v[72:75], v[196:199], v[80:83], 0\n\t"
    "v_cvt_pk_bf16_f32 v204, v114, v210\n\t"
    "v_cvt_pk_bf16_f32 v120, v115, v211\n\t"
    "v_mfma_f32_16x16x32_bf16 v[72:75], v[192:195], v[32:35], v[72:75]\n\t"
    "v_cvt_pk_bf16_f32 v217, v60, v64\n\t"
    "v_cvt_pk_bf16_f32 v213, v61, v65\n\t"
    "v_mfma_f32_16x16x32_bf16 v[28:31], v[192:195], v[36:39], 0\n\t"
    "v_cvt_pk_bf16_f32 v24, v200, v201\n\t"
    "v_cvt_pk_bf16_f32 v25, v202, v203\n\t"
    "v_mfma_f32_16x16x32_bf16 v[28:31], v[196:199], v[84:87], v[28:31]\n\t"
    "ds_read_b128 v[76:79], v0 offset:6144\n\t"
    "v_cvt_pk_bf16_f32 v205, v62, v66\n\t"
    "v_mfma_f32_16x16x32_bf16 v[32:35], v[196:199], v[88:91], 0\n\t"
    "ds_read_b128 v[80:83], v1 offset:6144\n\t"
    "v_cvt_pk_bf16_f32 v121, v63, v67\n\t"
    "v_mfma_f32_16x16x32_bf16 v[32:35], v[192:195], v[40:43], v[32:35]\n\t"
    "ds_read_b128 v[108:111], v2 offset:6144\n\t"
    "v_cvt_pk_bf16_f32 v218, v68, v72\n\t"
    "v_mfma_f32_16x16x32_bf16 v[36:39], v[192:195], v[44:47], 0\n\t"
    "ds_read_b128 v[84:87], v3 offset:6144\n\t"
    "v_cvt_pk_bf16_f32 v214, v69, v73\n\t"
    "v_mfma_f32_16x16x32_bf16 v[36:39], v[196:199], v[92:95], v[36:39]\n\t"
    "ds_read_b128 v[60:63], v4 offset:6144\n\t"
    "v_cvt_pk_bf16_f32 v206, v70, v74\n\t"
    "v_mfma_f32_16x16x32_bf16 v[40:43], v[196:199], v[96:99], 0\n\t"
    "ds_read_b128 v[64:67], v5 offset:6144\n\t"
    "v_cvt_pk_bf16_f32 v122, v71, v75\n\t"
    "v_mfma_f32_16x16x32_bf16 v[40:43], v[192:195], v[48:51], v[40:43]\n\t"
    "buffer_store_dwordx4 v[116:119], v7, s[52:55], s61 offen offset:384 sc1\n\t"
    "v_mfma_f32_16x16x32_bf16 v[44:47], v[192:195], v[52:55], 0\n\t"
    "v_cvt_pk_bf16_f32 v219, v28, v32\n\t"
    "v_cvt_pk_bf16_f32 v215, v29, v33\n\t"
    "v_mfma_f32_16x16x32_bf16 v[44:47], v[196:199], v[100:103], v[44:47]\n\t"
    "v_cvt_pk_bf16_f32 v207, v30, v34\n\t"
    "v_cvt_pk_bf16_f32 v123, v31, v35\n\t"
    "v_mfma_f32_16x16x32_bf16 v[48:51], v[196:199], v[104:107], 0\n\t"
    "v_mfma_f32_16x16x32_bf16 v[48:51], v[192:195], v[56:59], v[48:51]\n\t"
    "v_cvt_pk_bf16_f32 v96, v36, v40\n\t"
    "s_waitcnt lgkmcnt(2)\n\t"
    "v_mfma_f32_16x16x32_bf16 v[28:31], v[76:79], a[72:75], 0\n\t"
    "v_cvt_pk_bf16_f32 v98, v37, v41\n\t"
    "v_cvt_pk_bf16_f32 v112, v38, v42\n\t"
    "v_mfma_f32_16x16x32_bf16 v[28:31], v[80:83], a[76:79], v[28:31]\n\t"
    "v_cvt_pk_bf16_f32 v114, v39, v43\n\t"
    "v_mfma_f32_16x16x32_bf16 v[28:31], v[108:111], a[80:83], v[28:31]\n\t"
    "buffer_store_dwordx4 v[128:131], v7, s[52:55], s61 offen offset:768 sc1\n\t"
    "v_mfma_f32_16x16x32_bf16 v[28:31], v[84:87], a[84:87], v[28:31]\n\t"
    "v_cvt_pk_bf16_f32 v97, v44, v48\n\t"
    "s_waitcnt lgkmcnt(0)\n\t"
    "v_mfma_f32_16x16x32_bf16 v[28:31], v[60:63], a[88:91], v[28:31]\n\t"
    "v_cvt_pk_bf16_f32 v99, v45, v49\n\t"
    "v_cvt_pk_bf16_f32 v113, v46, v50\n\t"
    "v_mfma_f32_16x16x32_bf16 v[28:31], v[64:67], a[92:95], v[28:31]\n\t"
    "v_cvt_pk_bf16_f32 v115, v47, v51\n\t"
    "v_mfma_f32_16x16x32_bf16 v[32:35], v[64:67], a[68:71], 0\n\t"
    "v_mfma_f32_16x16x32_bf16 v[32:35], v[60:63], a[64:67], v[32:35]\n\t"
    "v_mfma_f32_16x16x32_bf16 v[32:35], v[84:87], a[60:63], v[32:35]\n\t"
    "v_mfma_f32_16x16x32_bf16 v[32:35], v[108:111], a[56:59], v[32:35]\n\t"
    "v_cvt_pk_bf16_f32 v26, v28, v29\n\t"
    "v_cvt_pk_bf16_f32 v27, v30, v31\n\t"
    "v_mfma_f32_16x16x32_bf16 v[32:35], v[80:83], a[52:55], v[32:35]\n\t"
    "v_mfma_f32_16x16x32_bf16 v[32:35], v[76:79], a[48:51], v[32:35]\n\t"
    "v_mfma_f32_16x16x32_bf16 v[28:31], v[76:79], a[24:27], 0\n\t"
    "v_mfma_f32_16x16x32_bf16 v[28:31], v[80:83], a[28:31], v[28:31]\n\t"
    "v_mfma_f32_16x16x32_bf16 v[28:31], v[108:111], a[32:35], v[28:31]\n\t"
    "buffer_store_dwordx4 v[124:127], v7, s[52:55], s61 offen offset:1152 sc1\n\t"
    "v_mfma_f32_16x16x32_bf16 v[28:31], v[84:87], a[36:39], v[28:31]\n\t"
    "v_cvt_pk_bf16_f32 v22, v32, v33\n\t"
    "v_cvt_pk_bf16_f32 v23, v34, v35\n\t"
    "v_mfma_f32_16x16x32_bf16 v[28:31], v[60:63], a[40:43], v[28:31]\n\t"
    "v_mfma_f32_16x16x32_bf16 v[28:31], v[64:67], a[44:47], v[28:31]\n\t"
    "v_mfma_f32_16x16x32_bf16 v[32:35], v[64:67], a[20:23], 0\n\t"
    "v_mfma_f32_16x16x32_bf16 v[32:35], v[60:63], a[16:19], v[32:35]\n\t"
    "v_mfma_f32_16x16x32_bf16 v[32:35], v[84:87], a[12:15], v[32:35]\n\t"
    "v_mfma_f32_16x16x32_bf16 v[32:35], v[108:111], a[8:11], v[32:35]\n\t"
    "v_cvt_pk_bf16_f32 v18, v28, v29\n\t"
    "v_cvt_pk_bf16_f32 v19, v30, v31\n\t"
    "v_mfma_f32_16x16x32_bf16 v[32:35], v[80:83], a[4:7], v[32:35]\n\t"
    "v_mfma_f32_16x16x32_bf16 v[32:35], v[76:79], a[0:3], v[32:35]\n\t"
    "s_branch .Lmin_%=\n\t"
    ".Ltan_%=:\n\t"
    "s_nop 7\n\t"
    "s_nop 1\n\t"
    "v_cvt_pk_bf16_f32 v136, v188, v192\n\t"
    "v_cvt_pk_bf16_f32 v138, v189, v193\n\t"
    "v_cvt_pk_bf16_f32 v132, v190, v194\n\t"
    "v_cvt_pk_bf16_f32 v134, v191, v195\n\t"
    "v_cvt_pk_bf16_f32 v137, v196, v200\n\t"
    "v_cvt_pk_bf16_f32 v139, v197, v201\n\t"
    "v_cvt_pk_bf16_f32 v133, v198, v202\n\t"
    "v_cvt_pk_bf16_f32 v135, v199, v203\n\t"
    "buffer_store_dwordx2 v[110:111], v8, s[52:55], s61 offen offset:384 sc1\n\t"
    "buffer_store_dwordx2 v[112:113], v8, s[52:55], s61 offen offset:768 sc1\n\t"
    "buffer_store_dwordx2 v[114:115], v8, s[52:55], s61 offen offset:1152 sc1\n\t"
    "s_add_u32 s61, s61, 393216\n\t"
    "s_barrier\n\t"
    "ds_read_b128 v[192:195], v6 offset:6144\n\t"
    "ds_read_b128 v[196:199], v6 offset:7168\n\t"
    "s_waitcnt lgkmcnt(1)\n\t"
    "v_mfma_f32_16x16x32_bf16 v[112:115], v[192:195], v[12:15], 0\n\t"
    "buffer_store_dwordx4 v[120:123], v7, s[52:55], s61 offen offset:0 sc1\n\t"
    "s_waitcnt lgkmcnt(0)\n\t"
    "v_mfma_f32_16x16x32_bf16 v[112:115], v[196:199], v[60:63], v[112:115]\n\t"
    "buffer_store_dwordx4 v[116:119], v7, s[52:55], s61 offen offset:384 sc1\n\t"
    "v_mfma_f32_16x16x32_bf16 v[208:211], v[196:199], v[64:67], 0\n\t"
    "buffer_store_dwordx4 v[128:131], v7, s[52:55], s61 offen offset:768 sc1\n\t"
    "v_mfma_f32_16x16x32_bf16 v[208:211], v[192:195], v[16:19], v[208:211]\n\t"
    "buffer_store_dwordx4 v[124:127], v7, s[52:55], s61 offen offset:1152 sc1\n\t"
    "v_mfma_f32_16x16x32_bf16 v[60:63], v[192:195], v[20:23], 0\n\t"
    "buffer_store_dwordx2 v[140:141], v8, s[52:55], s61 offen offset:0 sc1\n\t"
    "v_mfma_f32_16x16x32_bf16 v[60:63], v[196:199], v[68:71], v[60:63]\n\t"
    "buffer_store_dwordx2 v[142:143], v8, s[52:55], s61 offen offset:384 sc1\n\t"
    "v_mfma_f32_16x16x32_bf16 v[64:67], v[196:199], v[72:75], 0\n\t"
    "buffer_store_dwordx2 v[144:145], v8, s[52:55], s61 offen offset:768 sc1\n\t"
    "v_mfma_f32_16x16x32_bf16 v[64:67], v[192:195], v[24:27], v[64:67]\n\t"
    "buffer_store_dwordx2 v[146:147], v8, s[52:55], s61 offen offset:1152 sc1\n\t"
    "s_add_u32 s61, s61, 393216\n\t"
    "v_cvt_pk_bf16_f32 v216, v112, v208\n\t"
    "v_cvt_pk_bf16_f32 v212, v113, v209\n\t"
    "v_mfma_f32_16x16x32_bf16 v[68:71], v[192:195], v[28:31], 0\n\t"
    "buffer_store_dwordx4 v[152:155], v7, s[52:55], s61 offen offset:0 sc1\n\t"
    "v_mfma_f32_16x16x32_bf16 v[68:71], v[196:199], v[76:79], v[68:71]\n\t"
    "buffer_store_dwordx4 v[160:163], v7, s[52:55], s61 offen offset:384 sc1\n\t"
    "v_mfma_f32_16x16x32_bf16 v[72:75], v[196:199], v[80:83], 0\n\t"
    "buffer_store_dwordx4 v[168:171], v7, s[52:55], s61 offen offset:768 sc1\n\t"
    "v_cvt_pk_bf16_f32 v204, v114, v210\n\t"
    "v_cvt_pk_bf16_f32 v120, v115, v211\n\t"
    "v_mfma_f32_16x16x32_bf16 v[72:75], v[192:195], v[32:35], v[72:75]\n\t"
    "buffer_store_dwordx4 v[176:179], v7, s[52:55], s61 offen offset:1152 sc1\n\t"
    "v_cvt_pk_bf16_f32 v217, v60, v64\n\t"
    "v_cvt_pk_bf16_f32 v213, v61, v65\n\t"
    "v_mfma_f32_16x16x32_bf16 v[28:31], v[192:195], v[36:39], 0\n\t"
    "buffer_store_dwordx2 v[136:137], v8, s[52:55], s61 offen offset:0 sc1\n\t"
    "v_mfma_f32_16x16x32_bf16 v[28:31], v[196:199], v[84:87], v[28:31]\n\t"
    "buffer_store_dwordx2 v[138:139], v8, s[52:55], s61 offen offset:384 sc1\n\t"
    "v_cvt_pk_bf16_f32 v205, v62, v66\n\t"
    "v_mfma_f32_16x16x32_bf16 v[32:35], v[196:199], v[88:91], 0\n\t"
    "buffer_store_dwordx2 v[132:133], v8, s[52:55], s61 offen offset:768 sc1\n\t"
    "v_cvt_pk_bf16_f32 v121, v63, v67\n\t"
    "v_mfma_f32_16x16x32_bf16 v[32:35], v[192:195], v[40:43], v[32:35]\n\t"
    "buffer_store_dwordx2 v[134:135], v8, s[52:55], s61 offen offset:1152 sc1\n\t"
    "s_add_u32 s61, s61, 393216\n\t"
    "v_cvt_pk_bf16_f32 v218, v68, v72\n\t"
    "v_mfma_f32_16x16x32_bf16 v[36:39], v[192:195], v[44:47], 0\n\t"
    "v_cvt_pk_bf16_f32 v214, v69, v73\n\t"
    "v_mfma_f32_16x16x32_bf16 v[36:39], v[196:199], v[92:95], v[36:39]\n\t"
    "v_cvt_pk_bf16_f32 v206, v70, v74\n\t"
    "v_mfma_f32_16x16x32_bf16 v[40:43], v[196:199], v[96:99], 0\n\t"
    "v_cvt_pk_bf16_f32 v122, v71, v75\n\t"
    "v_mfma_f32_16x16x32_bf16 v[40:43], v[192:195], v[48:51], v[40:43]\n\t"
    "v_mfma_f32_16x16x32_bf16 v[44:47], v[192:195], v[52:55], 0\n\t"
    "v_cvt_pk_bf16_f32 v219, v28, v32\n\t"
    "v_cvt_pk_bf16_f32 v215, v29, v33\n\t"
    "v_mfma_f32_16x16x32_bf16 v[44:47], v[196:199], v[100:103], v[44:47]\n\t"
    "v_cvt_pk_bf16_f32 v207, v30, v34\n\t"
    "v_cvt_pk_bf16_f32 v123, v31, v35\n\t"
    "v_mfma_f32_16x16x32_bf16 v[48:51], v[196:199], v[104:107], 0\n\t"
    "v_mfma_f32_16x16x32_bf16 v[48:51], v[192:195], v[56:59], v[48:51]\n\t"
    "s_nop 7\n\t"
    "s_nop 1\n\t"
    "v_cvt_pk_bf16_f32 v96, v36, v40\n\t"
    "v_cvt_pk_bf16_f32 v98, v37, v41\n\t"
    "v_cvt_pk_bf16_f32 v112, v38, v42\n\t"
    "v_cvt_pk_bf16_f32 v114, v39, v43\n\t"
    "v_cvt_pk_bf16_f32 v97, v44, v48\n\t"
    "v_cvt_pk_bf16_f32 v99, v45, v49\n\t"
    "v_cvt_pk_bf16_f32 v113, v46, v50\n\t"
    "v_cvt_pk_bf16_f32 v115, v47, v51\n\t"
    "buffer_store_dwordx4 v[216:219], v7, s[52:55], s61 offen offset:0 sc1\n\t"
    "buffer_store_dwordx4 v[212:215], v7, s[52:55], s61 offen offset:384 sc1\n\t"
    "buffer_store_dwordx4 v[204:207], v7, s[52:55], s61 offen offset:768 sc1\n\t"
    "buffer_store_dwordx4 v[120:123], v7, s[52:55], s61 offen offset:1152 sc1\n\t"
    "buffer_store_dwordx2 v[96:97], v8, s[52:55], s61 offen offset:0 sc1\n\t"
    "buffer_store_dwordx2 v[98:99], v8, s[52:55], s61 offen offset:384 sc1\n\t"
    "buffer_store_dwordx2 v[112:113], v8, s[52:55], s61 offen offset:768 sc1\n\t"
    "buffer_store_dwordx2 v[114:115], v8, s[52:55], s61 offen offset:1152 sc1\n\t"
    "s_endpgm\n\t"
    ".p2align 12\n\t"
    ".Lroln_%=:\n\t"
    "v_lshrrev_b32_e32 v20, 3, v31\n\t"
    "v_and_b32_e32 v21, 6, v20\n\t"
    "v_and_b32_e32 v28, 7, v31\n\t"
    "v_xor_b32_e32 v21, v21, v28\n\t"
    "v_lshlrev_b32_e32 v20, 7, v20\n\t"
    "v_lshl_or_b32 v7, v21, 4, v20\n\t"
    "s_mov_b32 s54, 0x6000000\n\t"
    "s_mov_b32 s55, 0x20000\n\t"
    "s_waitcnt lgkmcnt(0)\n\t"
    "s_memrealtime s[72:73]\n\t"
    "s_mov_b32 s52, s36\n\t"
    "s_and_b32 s53, s37, 0xffff\n\t"
    "s_barrier\n\t"
    "s_add_u32 m0, %[rbeg], 0\n\t"
    "s_add_u32 s61, %[wb], 0\n\t"
    "buffer_load_dwordx4 v7, s[52:55], s61 offen offset:0 nt lds\n\t"
    "buffer_load_dwordx4 v7, s[52:55], s61 offen offset:1024 nt lds\n\t"
    "buffer_load_dwordx4 v7, s[52:55], s61 offen offset:2048 nt lds\n\t"
    "s_add_u32 m0, %[rbeg], 3072\n\t"
    "s_add_u32 s61, %[wb], 196608\n\t"
    "buffer_load_dwordx4 v7, s[52:55], s61 offen offset:0 nt lds\n\t"
    "buffer_load_dwordx4 v7, s[52:55], s61 offen offset:1024 nt lds\n\t"
    "buffer_load_dwordx4 v7, s[52:55], s61 offen offset:2048 nt lds\n\t"
    "v_lshlrev_b32_e32 v30, 4, v31\n\t"
    "v_and_b32_e32 v28, 7, v31\n\t"
    "v_bfe_u32 v27, v31, 3, 1\n\t"
    "v_lshrrev_b32_e32 v29, 4, v31\n\t"
    "v_add_u32_e32 v6, %[pl], v30\n\t"
    "v_mul_u32_u24_e32 v27, 0xc00, v27\n\t"
    "v_add_u32_e32 v27, %[rbeg], v27\n\t"
    "v_lshlrev_b32_e32 v26, 7, v31\n\t"
    "v_mad_u32_u24 v20, v28, 3, 0\n\t"
    "v_and_b32_e32 v21, 6, v20\n\t"
    "v_xor_b32_e32 v21, v21, v29\n\t"
    "v_lshl_add_u32 v20, v20, 7, v27\n\t"
    "v_lshl_add_u32 v0, v21, 4, v20\n\t"
    "v_xor_b32_e32 v1, 64, v0\n\t"
    "v_mad_u32_u24 v20, v28, 3, 1\n\t"
    "v_and_b32_e32 v21, 6, v20\n\t"
    "v_xor_b32_e32 v21, v21, v29\n\t"
    "v_lshl_add_u32 v20, v20, 7, v27\n\t"
    "v_lshl_add_u32 v2, v21, 4, v20\n\t"
    "v_xor_b32_e32 v3, 64, v2\n\t"
    "v_mad_u32_u24 v20, v28, 3, 2\n\t"
    "v_and_b32_e32 v21, 6, v20\n\t"
    "v_xor_b32_e32 v21, v21, v29\n\t"
    "v_lshl_add_u32 v20, v20, 7, v27\n\t"
    "v_lshl_add_u32 v4, v21, 4, v20\n\t"
    "v_xor_b32_e32 v5, 64, v4\n\t"
    "s_mov_b32 s56, s42\n\t"
    "s_and_b32 s57, s43, 0xffff\n\t"
    "s_mov_b32 s58, 0x40000\n\t"
    "s_mov_b32 s59, 0x20000\n\t"
    "v_add_u32_e32 v8, 0x9000, v30\n\t"
    "v_add_u32_e32 v9, 0x1b000, v30\n\t"
    "s_mov_b32 s68, 0x3dd53b95\n\t"
    "s_add_u32 m0, %[rbeg], 6144\n\t"
    "s_add_u32 s61, %[wb], 393216\n\t"
    "buffer_load_dwordx4 v7, s[52:55], s61 offen offset:0 nt lds\n\t"
    "buffer_load_dwordx4 v7, s[52:55], s61 offen offset:1024 nt lds\n\t"
    "buffer_load_dwordx4 v7, s[52:55], s61 offen offset:2048 nt lds\n\t"
    "s_add_u32 m0, %[rbeg], 9216\n\t"
    "s_add_u32 s61, %[wb], 589824\n\t"
    "buffer_load_dwordx4 v7, s[52:55], s61 offen offset:0 nt lds\n\t"
    "buffer_load_dwordx4 v7, s[52:55], s61 offen offset:1024 nt lds\n\t"
    "buffer_load_dwordx4 v7, s[52:55], s61 offen offset:2048 nt lds\n\t"
    "s_mov_b64 exec, 0\n\t"
    "s_cmp_eq_u32 %[pair], 0\n\t"
    "s_cbranch_scc1 .Lh4_%=\n\t"
    "s_branch .Lh1_%=\n\t"
    ".LrM_%=:\n\t"
    "s_mov_b64 exec, -1\n\t"
    ".Ltx3_%=:\n\t"
    "s_memrealtime s[70:71]\n\t"
    "s_waitcnt lgkmcnt(0)\n\t"
    "s_sub_u32 s70, s70, s72\n\t"
    "s_cmp_lt_u32 s70, 115\n\t"
    "s_cbranch_scc1 .Ltx3_%=\n\t"
    "s_add_u32 m0, %[rbeg], 12288\n\t"
    "s_add_u32 s61, %[wb], 786432\n\t"
    "buffer_load_dwordx4 v7, s[52:55], s61 offen offset:0 nt lds\n\t"
    "buffer_load_dwordx4 v7, s[52:55], s61 offen offset:1024 nt lds\n\t"
    "buffer_load_dwordx4 v7, s[52:55], s61 offen offset:2048 nt lds\n\t"
    "s_add_u32 m0, %[rbeg], 15360\n\t"
    "s_add_u32 s61, %[wb], 983040\n\t"
    "buffer_load_dwordx4 v7, s[52:55], s61 offen offset:0 nt lds\n\t"
    "buffer_load_dwordx4 v7, s[52:55], s61 offen offset:1024 nt lds\n\t"
    "buffer_load_dwordx4 v7, s[52:55], s61 offen offset:2048 nt lds\n\t"
    "s_add_u32 m0, %[rbeg], 18432\n\t"
    "s_add_u32 s61, %[wb], 1179648\n\t"
    "buffer_load_dwordx4 v7, s[52:55], s61 offen offset:0 nt lds\n\t"
    "buffer_load_dwordx4 v7, s[52:55], s61 offen offset:1024 nt lds\n\t"
    "buffer_load_dwordx4 v7, s[52:55], s61 offen offset:2048 nt lds\n\t"
    "s_add_u32 m0, %[rbeg], 21504\n\t"
    "s_add_u32 s61, %[wb], 1376256\n\t"
    "buffer_load_dwordx4 v7, s[52:55], s61 offen offset:0 nt lds\n\t"
    "buffer_load_dwordx4 v7, s[52:55], s61 offen offset:1024 nt lds\n\t"
    "buffer_load_dwordx4 v7, s[52:55], s61 offen offset:2048 nt lds\n\t"
    "s_waitcnt vmcnt(0)\n\t"
    "s_barrier\n\t"
    "ds_read_b128 v[188:191], v2 offset:6144\n\t"
    "ds_read_b128 v[192:195], v1 offset:6144\n\t"
    "ds_read_b128 v[196:199], v0 offset:6144\n\t"
    "ds_read_b128 v[172:175], v0 offset:12288\n\t"
    "ds_read_b128 v[168:171], v1 offset:12288\n\t"
    "ds_read_b128 v[164:167], v2 offset:12288\n\t"
    "ds_read_b128 v[140:143], v2 offset:18432\n\t"
    "ds_read_b128 v[144:147], v1 offset:18432\n\t"
    "ds_read_b128 v[148:151], v0 offset:18432\n\t"
    "ds_read_b128 v[184:187], v3 offset:6144\n\t"
    "ds_read_b128 v[180:183], v4 offset:6144\n\t"
    "ds_read_b128 v[176:179], v5 offset:6144\n\t"
    "ds_read_b128 v[208:211], v3 offset:0\n\t"
    "ds_read_b128 v[204:207], v4 offset:0\n\t"
    "ds_read_b128 v[200:203], v5 offset:0\n\t"
    "ds_read_b128 v[160:163], v3 offset:12288\n\t"
    "ds_read_b128 v[156:159], v4 offset:12288\n\t"
    "ds_read_b128 v[152:155], v5 offset:12288\n\t"
    "ds_read_b128 v[128:131], v5 offset:18432\n\t"
    "ds_read_b128 v[132:135], v4 offset:18432\n\t"
    "ds_read_b128 v[136:139], v3 offset:18432\n\t"
    "ds_read_b128 v[220:223], v0 offset:0\n\t"
    "ds_read_b128 v[216:219], v1 offset:0\n\t"
    "ds_read_b128 v[212:215], v2 offset:0\n\t"
    "ds_read_b128 v[28:31], v3 offset:0\n\t"
    "ds_read_b128 v[32:35], v4 offset:0\n\t"
    "ds_read_b128 v[40:43], v5 offset:0\n\t"
    "s_add_i32 s63, %[rbeg], 24576\n\t"
    "s_add_i32 s62, %[rbeg], 49152\n\t"
    "s_barrier\n\t"
    "ds_read_b128 a[0:3], v8 offset:0\n\t"
    "ds_read_b128 a[8:11], v8 offset:1024\n\t"
    "ds_read_b128 a[16:19], v8 offset:2048\n\t"
    "ds_read_b128 a[24:27], v8 offset:3072\n\t"
    "ds_read_b128 a[32:35], v8 offset:4096\n\t"
    "ds_read_b128 a[40:43], v8 offset:5120\n\t"
    "ds_read_b128 a[44:47], v9 offset:5120\n\t"
    "ds_read_b128 a[36:39], v9 offset:4096\n\t"
    "ds_read_b128 a[28:31], v9 offset:3072\n\t"
    "ds_read_b128 a[20:23], v9 offset:2048\n\t"
    "ds_read_b128 a[12:15], v9 offset:1024\n\t"
    "s_waitcnt lgkmcnt(10)\n\t"
    "v_mfma_f32_16x16x32_bf16 v[64:67], a[0:3], v[220:223], 0\n\t"
    "ds_read_b128 a[4:7], v9 offset:0\n\t"
    "s_waitcnt lgkmcnt(10)\n\t"
    "v_mfma_f32_16x16x32_bf16 v[64:67], a[8:11], v[216:219], v[64:67]\n\t"
    "s_barrier\n\t"
    "ds_read_b128 a[48:51], v8 offset:6144\n\t"
    "s_waitcnt lgkmcnt(10)\n\t"
    "v_mfma_f32_16x16x32_bf16 v[64:67], a[16:19], v[212:215], v[64:67]\n\t"
    "ds_read_b128 a[56:59], v8 offset:7168\n\t"
    "s_waitcnt lgkmcnt(10)\n\t"
    "v_mfma_f32_16x16x32_bf16 v[64:67], a[24:27], v[28:31], v[64:67]\n\t"
    "ds_read_b128 a[64:67], v8 offset:8192\n\t"
    "s_waitcnt lgkmcnt(4)\n\t"
    "v_mfma_f32_16x16x32_bf16 v[64:67], a[32:35], v[32:35], v[64:67]\n\t"
    "ds_read_b128 a[72:75], v8 offset:9216\n\t"
    "v_mfma_f32_16x16x32_bf16 v[64:67], a[40:43], v[40:43], v[64:67]\n\t"
    "ds_read_b128 a[80:83], v8 offset:10240\n\t"
    "v_mfma_f32_16x16x32_bf16 v[56:59], a[44:47], v[40:43], 0\n\t"
    "ds_read_b128 a[88:91], v8 offset:11264\n\t"
    "v_mfma_f32_16x16x32_bf16 v[56:59], a[36:39], v[32:35], v[56:59]\n\t"
    "ds_read_b128 a[92:95], v9 offset:11264\n\t"
    "v_mfma_f32_16x16x32_bf16 v[56:59], a[28:31], v[28:31], v[56:59]\n\t"
    "ds_read_b128 a[84:87], v9 offset:10240\n\t"
    "v_mfma_f32_16x16x32_bf16 v[56:59], a[20:23], v[212:215], v[56:59]\n\t"
    "ds_read_b128 a[76:79], v9 offset:9216\n\t"
    "v_mfma_f32_16x16x32_bf16 v[56:59], a[12:15], v[216:219], v[56:59]\n\t"
    "v_cvt_pk_bf16_f32 v44, v64, v65\n\t"
    "v_cvt_pk_bf16_f32 v45, v66, v67\n\t"
    "ds_read_b128 a[68:71], v9 offset:8192\n\t"
    "s_waitcnt lgkmcnt(4)\n\t"
    "v_mfma_f32_16x16x32_bf16 v[56:59], a[4:7], v[220:223], v[56:59]\n\t"
    "ds_read_b128 a[60:63], v9 offset:7168\n\t"
    "v_mfma_f32_16x16x32_bf16 v[36:39], a[48:51], v[220:223], 0\n\t"
    "ds_read_b128 a[52:55], v9 offset:6144\n\t"
    "v_mfma_f32_16x16x32_bf16 v[36:39], a[56:59], v[216:219], v[36:39]\n\t"
    "s_barrier\n\t"
    "ds_read_b128 a[96:99], v8 offset:12288\n\t"
    "v_mfma_f32_16x16x32_bf16 v[36:39], a[64:67], v[212:215], v[36:39]\n\t"
    "ds_read_b128 a[104:107], v8 offset:13312\n\t"
    "v_mfma_f32_16x16x32_bf16 v[36:39], a[72:75], v[28:31], v[36:39]\n\t"
    "v_cvt_pk_bf16_f32 v46, v56, v57\n\t"
    "ds_read_b128 a[112:115], v8 offset:14336\n\t"
    "v_mfma_f32_16x16x32_bf16 v[36:39], a[80:83], v[32:35], v[36:39]\n\t"
    "v_cvt_pk_bf16_f32 v47, v58, v59\n\t"
    "ds_read_b128 a[120:123], v8 offset:15360\n\t"
    "v_mfma_f32_16x16x32_bf16 v[36:39], a[88:91], v[40:43], v[36:39]\n\t"
    "ds_read_b128 a[128:131], v8 offset:16384\n\t"
    "s_waitcnt lgkmcnt(4)\n\t"
    "v_mfma_f32_16x16x32_bf16 v[48:51], a[92:95], v[40:43], 0\n\t"
    "ds_read_b128 a[136:139], v8 offset:17408\n\t"
    "v_mfma_f32_16x16x32_bf16 v[48:51], a[84:87], v[32:35], v[48:51]\n\t"
    "ds_read_b128 a[140:143], v9 offset:17408\n\t"
    "v_mfma_f32_16x16x32_bf16 v[48:51], a[76:79], v[28:31], v[48:51]\n\t"
    "ds_read_b128 a[132:135], v9 offset:16384\n\t"
    "v_mfma_f32_16x16x32_bf16 v[48:51], a[68:71], v[212:215], v[48:51]\n\t"
    "v_cvt_pk_bf16_f32 v56, v36, v37\n\t"
    "ds_read_b128 a[124:127], v9 offset:15360\n\t"
    "v_mfma_f32_16x16x32_bf16 v[48:51], a[60:63], v[216:219], v[48:51]\n\t"
    "v_cvt_pk_bf16_f32 v57, v38, v39\n\t"
    "ds_read_b128 a[116:119], v9 offset:14336\n\t"
    "v_mfma_f32_16x16x32_bf16 v[48:51], a[52:55], v[220:223], v[48:51]\n\t"
    "ds_read_b128 a[108:111], v9 offset:13312\n\t"
    "v_mfma_f32_16x16x32_bf16 v[60:63], a[96:99], v[220:223], 0\n\t"
    "ds_read_b128 a[100:103], v9 offset:12288\n\t"
    "s_waitcnt lgkmcnt(4)\n\t"
    "v_mfma_f32_16x16x32_bf16 v[60:63], a[104:107], v[216:219], v[60:63]\n\t"
    "ds_read_b128 a[144:147], v8 offset:18432\n\t"
    "v_mfma_f32_16x16x32_bf16 v[60:63], a[112:115], v[212:215], v[60:63]\n\t"
    "ds_read_b128 a[152:155], v8 offset:19456\n\t"
    "v_mfma_f32_16x16x32_bf16 v[60:63], a[120:123], v[28:31], v[60:63]\n\t"
    "v_cvt_pk_bf16_f32 v58, v48, v49\n\t"
    "v_cvt_pk_bf16_f32 v59, v50, v51\n\t"
    "ds_read_b128 a[160:163], v8 offset:20480\n\t"
    "v_mfma_f32_16x16x32_bf16 v[60:63], a[128:131], v[32:35], v[60:63]\n\t"
    "ds_read_b128 a[168:171], v8 offset:21504\n\t"
    "v_mfma_f32_16x16x32_bf16 v[60:63], a[136:139], v[40:43], v[60:63]\n\t"
    "ds_read_b128 a[176:179], v8 offset:22528\n\t"
    "v_mfma_f32_16x16x32_bf16 v[36:39], a[140:143], v[40:43], 0\n\t"
    "ds_read_b128 a[184:187], v8 offset:23552\n\t"
    "v_mfma_f32_16x16x32_bf16 v[36:39], a[132:135], v[32:35], v[36:39]\n\t"
    "s_waitcnt lgkmcnt(3)\n\t"
    "v_mfma_f32_16x16x32_bf16 v[36:39], a[124:127], v[28:31], v[36:39]\n\t"
    "v_mfma_f32_16x16x32_bf16 v[36:39], a[116:119], v[212:215], v[36:39]\n\t"
    "v_cvt_pk_bf16_f32 v48, v60, v61\n\t"
    "v_cvt_pk_bf16_f32 v49, v62, v63\n\t"
    "v_mfma_f32_16x16x32_bf16 v[36:39], a[108:111], v[216:219], v[36:39]\n\t"
    "v_mfma_f32_16x16x32_bf16 v[36:39], a[100:103], v[220:223], v[36:39]\n\t"
    "v_mfma_f32_16x16x32_bf16 v[60:63], a[144:147], v[220:223], 0\n\t"
    "v_mfma_f32_16x16x32_bf16 v[60:63], a[152:155], v[216:219], v[60:63]\n\t"
    "v_mfma_f32_16x16x32_bf16 v[60:63], a[160:163], v[212:215], v[60:63]\n\t"
    "s_waitcnt lgkmcnt(0)\n\t"
    "v_mfma_f32_16x16x32_bf16 v[60:63], a[168:171], v[28:31], v[60:63]\n\t"
    "v_cvt_pk_bf16_f32 v50, v36, v37\n\t"
    "v_mfma_f32_16x16x32_bf16 v[60:63], a[176:179], v[32:35], v[60:63]\n\t"
    "v_cvt_pk_bf16_f32 v51, v38, v39\n\t"
    "v_mfma_f32_16x16x32_bf16 v[60:63], a[184:187], v[40:43], v[60:63]\n\t"
    "v_mfma_f32_16x16x32_bf16 v[64:67], v[220:223], v[44:47], 0\n\t"
    "v_mfma_f32_16x16x32_bf16 v[64:67], v[216:219], v[56:59], v[64:67]\n\t"
    "ds_read_b128 a[188:191], v9 offset:23552\n\t"
    "v_mfma_f32_16x16x32_bf16 v[64:67], v[212:215], v[48:51], v[64:67]\n\t"
    "ds_read_b128 a[180:183], v9 offset:22528\n\t"
    "v_mfma_f32_16x16x32_bf16 v[36:39], v[188:191], v[48:51], 0\n\t"
    "ds_read_b128 a[172:175], v9 offset:21504\n\t"
    "v_mfma_f32_16x16x32_bf16 v[36:39], v[192:195], v[56:59], v[36:39]\n\t"
    "ds_read_b128 a[164:167], v9 offset:20480\n\t"
    "v_mfma_f32_16x16x32_bf16 v[36:39], v[196:199], v[44:47], v[36:39]\n\t"
    "ds_read_b128 a[156:159], v9 offset:19456\n\t"
    "v_mfma_f32_16x16x32_bf16 v[68:71], v[172:175], v[44:47], 0\n\t"
    "ds_read_b128 a[148:151], v9 offset:18432\n\t"
    "v_mfma_f32_16x16x32_bf16 v[68:71], v[168:171], v[56:59], v[68:71]\n\t"
    "s_barrier\n\t"
    "ds_read_b128 a[192:195], v8 offset:24576\n\t"
    "v_mfma_f32_16x16x32_bf16 v[68:71], v[164:167], v[48:51], v[68:71]\n\t"
    "ds_read_b128 a[200:203], v8 offset:25600\n\t"
    "v_mfma_f32_16x16x32_bf16 v[72:75], v[140:143], v[48:51], 0\n\t"
    "ds_read_b128 a[208:211], v8 offset:26624\n\t"
    "v_mfma_f32_16x16x32_bf16 v[72:75], v[144:147], v[56:59], v[72:75]\n\t"
    "ds_read_b128 a[216:219], v8 offset:27648\n\t"
    "v_mfma_f32_16x16x32_bf16 v[72:75], v[148:151], v[44:47], v[72:75]\n\t"
    "ds_read_b128 a[224:227], v8 offset:28672\n\t"
    "s_waitcnt lgkmcnt(4)\n\t"
    "v_mfma_f32_16x16x32_bf16 v[48:51], a[188:191], v[40:43], 0\n\t"
    "ds_read_b128 a[232:235], v8 offset:29696\n\t"
    "v_mfma_f32_16x16x32_bf16 v[48:51], a[180:183], v[32:35], v[48:51]\n\t"
    "ds_read_b128 a[236:239], v9 offset:29696\n\t"
    "v_mfma_f32_16x16x32_bf16 v[48:51], a[172:175], v[28:31], v[48:51]\n\t"
    "ds_read_b128 a[228:231], v9 offset:28672\n\t"
    "v_mfma_f32_16x16x32_bf16 v[48:51], a[164:167], v[212:215], v[48:51]\n\t"
    "ds_read_b128 a[220:223], v9 offset:27648\n\t"
    "v_mfma_f32_16x16x32_bf16 v[48:51], a[156:159], v[216:219], v[48:51]\n\t"
    "ds_read_b128 a[212:215], v9 offset:26624\n\t"
    "v_mfma_f32_16x16x32_bf16 v[48:51], a[148:151], v[220:223], v[48:51]\n\t"
    "ds_read_b128 a[204:207], v9 offset:25600\n\t"
    "v_mfma_f32_16x16x32_bf16 v[44:47], a[192:195], v[220:223], 0\n\t"
    "ds_read_b128 a[196:199], v9 offset:24576\n\t"
    "s_waitcnt lgkmcnt(4)\n\t"
    "v_mfma_f32_16x16x32_bf16 v[44:47], a[200:203], v[216:219], v[44:47]\n\t"
    "v_cvt_pk_bf16_f32 v56, v60, v61\n\t"
    "ds_read_b128 a[240:243], v8 offset:30720\n\t"
    "v_mfma_f32_16x16x32_bf16 v[44:47], a[208:211], v[212:215], v[44:47]\n\t"
    "ds_read_b128 a[248:251], v8 offset:31744\n\t"
    "v_mfma_f32_16x16x32_bf16 v[44:47], a[216:219], v[28:31], v[44:47]\n\t"
    "v_cvt_pk_bf16_f32 v58, v48, v49\n\t"
    "ds_read_b128 v[224:227], v8 offset:32768\n\t"
    "v_mfma_f32_16x16x32_bf16 v[44:47], a[224:227], v[32:35], v[44:47]\n\t"
    "v_cvt_pk_bf16_f32 v59, v50, v51\n\t"
    "ds_read_b128 v[232:235], v8 offset:33792\n\t"
    "v_mfma_f32_16x16x32_bf16 v[44:47], a[232:235], v[40:43], v[44:47]\n\t"
    "v_cvt_pk_bf16_f32 v57, v62, v63\n\t"
    "ds_read_b128 v[240:243], v8 offset:34816\n\t"
    "v_mfma_f32_16x16x32_bf16 v[76:79], a[236:239], v[40:43], 0\n\t"
    "ds_read_b128 v[248:251], v8 offset:35840\n\t"
    "v_mfma_f32_16x16x32_bf16 v[76:79], a[228:231], v[32:35], v[76:79]\n\t"
    "ds_read_b128 v[252:255], v9 offset:35840\n\t"
    "s_waitcnt lgkmcnt(4)\n\t"
    "v_mfma_f32_16x16x32_bf16 v[76:79], a[220:223], v[28:31], v[76:79]\n\t"
    "ds_read_b128 v[244:247], v9 offset:34816\n\t"
    "v_mfma_f32_16x16x32_bf16 v[76:79], a[212:215], v[212:215], v[76:79]\n\t"
    "v_cvt_pk_bf16_f32 v48, v44, v45\n\t"
    "ds_read_b128 v[236:239], v9 offset:33792\n\t"
    "v_mfma_f32_16x16x32_bf16 v[76:79], a[204:207], v[216:219], v[76:79]\n\t"
    "v_cvt_pk_bf16_f32 v49, v46, v47\n\t"
    "ds_read_b128 v[228:231], v9 offset:32768\n\t"
    "v_mfma_f32_16x16x32_bf16 v[76:79], a[196:199], v[220:223], v[76:79]\n\t"
    "ds_read_b128 a[252:255], v9 offset:31744\n\t"
    "v_mfma_f32_16x16x32_bf16 v[80:83], a[240:243], v[220:223], 0\n\t"
    "ds_read_b128 a[244:247], v9 offset:30720\n\t"
    "v_mfma_f32_16x16x32_bf16 v[80:83], a[248:251], v[216:219], v[80:83]\n\t"
    "v_mfma_f32_16x16x32_bf16 v[80:83], v[224:227], v[212:215], v[80:83]\n\t"
    "s_waitcnt lgkmcnt(2)\n\t"
    "v_mfma_f32_16x16x32_bf16 v[80:83], v[232:235], v[28:31], v[80:83]\n\t"
    "v_cvt_pk_bf16_f32 v50, v76, v77\n\t"
    "v_cvt_pk_bf16_f32 v51, v78, v79\n\t"
    "v_mfma_f32_16x16x32_bf16 v[80:83], v[240:243], v[32:35], v[80:83]\n\t"
    "v_mfma_f32_16x16x32_bf16 v[80:83], v[248:251], v[40:43], v[80:83]\n\t"
    "v_mfma_f32_16x16x32_bf16 v[12:15], v[252:255], v[40:43], 0\n\t"
    "v_mfma_f32_16x16x32_bf16 v[12:15], v[244:247], v[32:35], v[12:15]\n\t"
    "v_mfma_f32_16x16x32_bf16 v[12:15], v[236:239], v[28:31], v[12:15]\n\t"
    "v_mfma_f32_16x16x32_bf16 v[12:15], v[228:231], v[212:215], v[12:15]\n\t"
    "v_cvt_pk_bf16_f32 v16, v80, v81\n\t"
    "v_cvt_pk_bf16_f32 v17, v82, v83\n\t"
    "s_waitcnt lgkmcnt(0)\n\t"
    "v_mfma_f32_16x16x32_bf16 v[12:15], a[252:255], v[216:219], v[12:15]\n\t"
    "v_mfma_f32_16x16x32_bf16 v[12:15], a[244:247], v[220:223], v[12:15]\n\t"
    "v_mfma_f32_16x16x32_bf16 v[20:23], a[0:3], v[196:199], 0\n\t"
    "v_mfma_f32_16x16x32_bf16 v[20:23], a[8:11], v[192:195], v[20:23]\n\t"
    "v_mfma_f32_16x16x32_bf16 v[20:23], a[16:19], v[188:191], v[20:23]\n\t"
    "v_mfma_f32_16x16x32_bf16 v[20:23], a[24:27], v[184:187], v[20:23]\n\t"
    "v_cvt_pk_bf16_f32 v18, v12, v13\n\t"
    "v_mfma_f32_16x16x32_bf16 v[20:23], a[32:35], v[180:183], v[20:23]\n\t"
    "v_mfma_f32_16x16x32_bf16 v[20:23], a[40:43], v[176:179], v[20:23]\n\t"
    "v_cvt_pk_bf16_f32 v19, v14, v15\n\t"
    "v_mfma_f32_16x16x32_bf16 v[64:67], v[208:211], v[56:59], v[64:67]\n\t"
    "v_mfma_f32_16x16x32_bf16 v[64:67], v[204:207], v[48:51], v[64:67]\n\t"
    "v_mfma_f32_16x16x32_bf16 v[64:67], v[200:203], v[16:19], v[64:67]\n\t"
    "v_mfma_f32_16x16x32_bf16 v[36:39], v[176:179], v[16:19], v[36:39]\n\t"
    "v_cvt_pk_bf16_f32 v12, v20, v21\n\t"
    "v_cvt_pk_bf16_f32 v13, v22, v23\n\t"
    "v_mfma_f32_16x16x32_bf16 v[36:39], v[180:183], v[48:51], v[36:39]\n\t"
    "s_barrier\n\t"
    "v_mfma_f32_16x16x32_bf16 v[36:39], v[184:187], v[56:59], v[36:39]\n\t"
    "v_mfma_f32_16x16x32_bf16 v[68:71], v[160:163], v[56:59], v[68:71]\n\t"
    "v_max3_f32 v20, v64, v65, v66\n\t"
    "v_mfma_f32_16x16x32_bf16 v[68:71], v[156:159], v[48:51], v[68:71]\n\t"
    "v_mfma_f32_16x16x32_bf16 v[68:71], v[152:155], v[16:19], v[68:71]\n\t"
    "v_mfma_f32_16x16x32_bf16 v[72:75], v[128:131], v[16:19], v[72:75]\n\t"
    "v_max3_f32 v21, v67, v36, v37\n\t"
    "v_max3_f32 v20, v20, v38, v39\n\t"
    "v_mfma_f32_16x16x32_bf16 v[72:75], v[132:135], v[48:51], v[72:75]\n\t"
    "v_mfma_f32_16x16x32_bf16 v[72:75], v[136:139], v[56:59], v[72:75]\n\t"
    "v_mfma_f32_16x16x32_bf16 v[16:19], a[44:47], v[176:179], 0\n\t"
    "v_max3_f32 v21, v21, v68, v69\n\t"
    "v_max3_f32 v20, v20, v70, v71\n\t"
    "v_mfma_f32_16x16x32_bf16 v[16:19], a[36:39], v[180:183], v[16:19]\n\t"
    "v_mfma_f32_16x16x32_bf16 v[16:19], a[28:31], v[184:187], v[16:19]\n\t"
    "v_mfma_f32_16x16x32_bf16 v[16:19], a[20:23], v[188:191], v[16:19]\n\t"
    "v_max3_f32 v21, v21, v72, v73\n\t"
    "v_max3_f32 v20, v20, v74, v75\n\t"
    "v_mfma_f32_16x16x32_bf16 v[16:19], a[12:15], v[192:195], v[16:19]\n\t"
    "v_max_f32_e32 v20, v20, v21\n\t"
    "v_mov_b32_e32 v21, v20\n\t"
    "v_mfma_f32_16x16x32_bf16 v[16:19], a[4:7], v[196:199], v[16:19]\n\t"
    "v_mfma_f32_16x16x32_bf16 v[24:27], a[48:51], v[196:199], 0\n\t"
    "v_permlane32_swap_b32 v20, v21\n\t"
    "v_max_f32_e32 v20, v20, v21\n\t"
    "v_mfma_f32_16x16x32_bf16 v[24:27], a[56:59], v[192:195], v[24:27]\n\t"
    "v_mov_b32_e32 v21, v20\n\t"
    "v_mfma_f32_16x16x32_bf16 v[24:27], a[64:67], v[188:191], v[24:27]\n\t"
    "v_mfma_f32_16x16x32_bf16 v[24:27], a[72:75], v[184:187], v[24:27]\n\t"
    "v_cvt_pk_bf16_f32 v14, v16, v17\n\t"
    "v_cvt_pk_bf16_f32 v15, v18, v19\n\t"
    "v_mfma_f32_16x16x32_bf16 v[24:27], a[80:83], v[180:183], v[24:27]\n\t"
    "v_permlane16_swap_b32 v20, v21\n\t"
    "v_max_f32_e32 v20, v20, v21\n\t"
    "v_mfma_f32_16x16x32_bf16 v[24:27], a[88:91], v[176:179], v[24:27]\n\t"
    "v_sub_f32_e32 v64, v64, v20\n\t"
    "v_sub_f32_e32 v65, v65, v20\n\t"
    "v_mfma_f32_16x16x32_bf16 v[16:19], a[92:95], v[176:179], 0\n\t"
    "v_exp_f32_e32 v64, v64\n\t"
    "v_mfma_f32_16x16x32_bf16 v[16:19], a[84:87], v[180:183], v[16:19]\n\t"
    "v_exp_f32_e32 v65, v65\n\t"
    "v_mfma_f32_16x16x32_bf16 v[16:19], a[76:79], v[184:187], v[16:19]\n\t"
    "v_add_f32_e32 v22, v64, v65\n\t"
    "v_sub_f32_e32 v66, v66, v20\n\t"
    "v_mfma_f32_16x16x32_bf16 v[16:19], a[68:71], v[188:191], v[16:19]\n\t"
    "v_cvt_pk_bf16_f32 v28, v24, v25\n\t"
    "v_cvt_pk_bf16_f32 v29, v26, v27\n\t"
    "v_mfma_f32_16x16x32_bf16 v[16:19], a[60:63], v[192:195], v[16:19]\n\t"
    "v_exp_f32_e32 v66, v66\n\t"
    "v_mfma_f32_16x16x32_bf16 v[16:19], a[52:55], v[196:199], v[16:19]\n\t"
    "v_sub_f32_e32 v67, v67, v20\n\t"
    "v_sub_f32_e32 v36, v36, v20\n\t"
    "v_mfma_f32_16x16x32_bf16 v[24:27], a[96:99], v[196:199], 0\n\t"
    "v_exp_f32_e32 v67, v67\n\t"
    "v_mfma_f32_16x16x32_bf16 v[24:27], a[104:107], v[192:195], v[24:27]\n\t"
    "v_add_f32_e32 v23, v66, v67\n\t"
    "v_sub_f32_e32 v37, v37, v20\n\t"
    "v_mfma_f32_16x16x32_bf16 v[24:27], a[112:115], v[188:191], v[24:27]\n\t"
    "v_exp_f32_e32 v36, v36\n\t"
    "v_mfma_f32_16x16x32_bf16 v[24:27], a[120:123], v[184:187], v[24:27]\n\t"
    "v_cvt_pk_bf16_f32 v30, v16, v17\n\t"
    "v_cvt_pk_bf16_f32 v31, v18, v19\n\t"
    "v_mfma_f32_16x16x32_bf16 v[24:27], a[128:131], v[180:183], v[24:27]\n\t"
    "v_add_f32_e32 v22, v22, v36\n\t"
    "v_sub_f32_e32 v38, v38, v20\n\t"
    "v_mfma_f32_16x16x32_bf16 v[24:27], a[136:139], v[176:179], v[24:27]\n\t"
    "v_exp_f32_e32 v37, v37\n\t"
    "v_mfma_f32_16x16x32_bf16 v[16:19], a[140:143], v[176:179], 0\n\t"
    "v_add_f32_e32 v22, v22, v37\n\t"
    "v_sub_f32_e32 v39, v39, v20\n\t"
    "v_mfma_f32_16x16x32_bf16 v[16:19], a[132:135], v[180:183], v[16:19]\n\t"
    "v_exp_f32_e32 v38, v38\n\t"
    "v_mfma_f32_16x16x32_bf16 v[16:19], a[124:127], v[184:187], v[16:19]\n\t"
    "v_add_f32_e32 v23, v23, v38\n\t"
    "v_sub_f32_e32 v68, v68, v20\n\t"
    "v_mfma_f32_16x16x32_bf16 v[16:19], a[116:119], v[188:191], v[16:19]\n\t"
    "v_cvt_pk_bf16_f32 v32, v24, v25\n\t"
    "v_cvt_pk_bf16_f32 v33, v26, v27\n\t"
    "v_mfma_f32_16x16x32_bf16 v[16:19], a[108:111], v[192:195], v[16:19]\n\t"
    "v_exp_f32_e32 v39, v39\n\t"
    "v_mfma_f32_16x16x32_bf16 v[16:19], a[100:103], v[196:199], v[16:19]\n\t"
    "v_add_f32_e32 v23, v23, v39\n\t"
    "v_sub_f32_e32 v69, v69, v20\n\t"
    "v_mfma_f32_16x16x32_bf16 v[24:27], a[144:147], v[196:199], 0\n\t"
    "v_exp_f32_e32 v68, v68\n\t"
    "v_mfma_f32_16x16x32_bf16 v[24:27], a[152:155], v[192:195], v[24:27]\n\t"
    "v_add_f32_e32 v22, v22, v68\n\t"
    "v_sub_f32_e32 v70, v70, v20\n\t"
    "v_mfma_f32_16x16x32_bf16 v[24:27], a[160:163], v[188:191], v[24:27]\n\t"
    "v_sub_f32_e32 v71, v71, v20\n\t"
    "v_mfma_f32_16x16x32_bf16 v[24:27], a[168:171], v[184:187], v[24:27]\n\t"
    "v_cvt_pk_bf16_f32 v34, v16, v17\n\t"
    "v_mfma_f32_16x16x32_bf16 v[24:27], a[176:179], v[180:183], v[24:27]\n\t"
    "v_cvt_pk_bf16_f32 v35, v18, v19\n\t"
    "v_sub_f32_e32 v72, v72, v20\n\t"
    "v_mfma_f32_16x16x32_bf16 v[24:27], a[184:187], v[176:179], v[24:27]\n\t"
    "v_sub_f32_e32 v73, v73, v20\n\t"
    "v_mfma_f32_16x16x32_bf16 v[40:43], v[220:223], v[12:15], 0\n\t"
    "v_exp_f32_e32 v69, v69\n\t"
    "v_mfma_f32_16x16x32_bf16 v[40:43], v[216:219], v[28:31], v[40:43]\n\t"
    "v_add_f32_e32 v22, v22, v69\n\t"
    "v_mfma_f32_16x16x32_bf16 v[40:43], v[212:215], v[32:35], v[40:43]\n\t"
    "v_sub_f32_e32 v74, v74, v20\n\t"
    "v_mfma_f32_16x16x32_bf16 v[16:19], v[188:191], v[32:35], 0\n\t"
    "v_exp_f32_e32 v70, v70\n\t"
    "v_mfma_f32_16x16x32_bf16 v[16:19], v[192:195], v[28:31], v[16:19]\n\t"
    "v_add_f32_e32 v23, v23, v70\n\t"
    "v_sub_f32_e32 v75, v75, v20\n\t"
    "v_mfma_f32_16x16x32_bf16 v[16:19], v[196:199], v[12:15], v[16:19]\n\t"
    "v_exp_f32_e32 v71, v71\n\t"
    "v_mfma_f32_16x16x32_bf16 v[44:47], v[172:175], v[12:15], 0\n\t"
    "v_add_f32_e32 v23, v23, v71\n\t"
    "v_cvt_pk_bf16_f32 v48, v24, v25\n\t"
    "v_mfma_f32_16x16x32_bf16 v[44:47], v[168:171], v[28:31], v[44:47]\n\t"
    "v_exp_f32_e32 v72, v72\n\t"
    "v_mfma_f32_16x16x32_bf16 v[44:47], v[164:167], v[32:35], v[44:47]\n\t"
    "v_add_f32_e32 v22, v22, v72\n\t"
    "v_cvt_pk_bf16_f32 v49, v26, v27\n\t"
    "v_mfma_f32_16x16x32_bf16 v[52:55], v[140:143], v[32:35], 0\n\t"
    "v_exp_f32_e32 v73, v73\n\t"
    "v_mfma_f32_16x16x32_bf16 v[52:55], v[144:147], v[28:31], v[52:55]\n\t"
    "v_add_f32_e32 v22, v22, v73\n\t"
    "v_mfma_f32_16x16x32_bf16 v[52:55], v[148:151], v[12:15], v[52:55]\n\t"
    "v_exp_f32_e32 v74, v74\n\t"
    "v_mfma_f32_16x16x32_bf16 v[24:27], a[188:191], v[176:179], 0\n\t"
    "v_add_f32_e32 v23, v23, v74\n\t"
    "v_mfma_f32_16x16x32_bf16 v[24:27], a[180:183], v[180:183], v[24:27]\n\t"
    "v_exp_f32_e32 v75, v75\n\t"
    "v_mfma_f32_16x16x32_bf16 v[24:27], a[172:175], v[184:187], v[24:27]\n\t"
    "v_add_f32_e32 v23, v23, v75\n\t"
    "v_add_f32_e32 v22, v22, v23\n\t"
    "v_mfma_f32_16x16x32_bf16 v[24:27], a[164:167], v[188:191], v[24:27]\n\t"
    "v_mov_b32_e32 v23, v22\n\t"
    "v_mfma_f32_16x16x32_bf16 v[24:27], a[156:159], v[192:195], v[24:27]\n\t"
    "s_add_u32 m0, %[rbeg], 24576\n\t"
    "s_add_u32 s61, %[wb], 12582912\n\t"
    "v_mfma_f32_16x16x32_bf16 v[24:27], a[148:151], v[196:199], v[24:27]\n\t"
    "v_permlane32_swap_b32 v22, v23\n\t"
    "v_add_f32_e32 v22, v22, v23\n\t"
    "v_mfma_f32_16x16x32_bf16 v[12:15], a[192:195], v[196:199], 0\n\t"
    "v_mov_b32_e32 v23, v22\n\t"
    "v_mfma_f32_16x16x32_bf16 v[12:15], a[200:203], v[192:195], v[12:15]\n\t"
    "buffer_load_dwordx4 v7, s[52:55], s61 offen offset:0 nt lds\n\t"
    "v_mfma_f32_16x16x32_bf16 v[12:15], a[208:211], v[188:191], v[12:15]\n\t"
    "v_permlane16_swap_b32 v22, v23\n\t"
    "v_add_f32_e32 v22, v22, v23\n\t"
    "v_mfma_f32_16x16x32_bf16 v[12:15], a[216:219], v[184:187], v[12:15]\n\t"
    "v_cvt_pk_bf16_f32 v50, v24, v25\n\t"
    "v_cvt_pk_bf16_f32 v51, v26, v27\n\t"
    "v_mfma_f32_16x16x32_bf16 v[12:15], a[224:227], v[180:183], v[12:15]\n\t"
    "v_rcp_f32_e32 v22, v22\n\t"
    "v_mfma_f32_16x16x32_bf16 v[12:15], a[232:235], v[176:179], v[12:15]\n\t"
    "v_mul_f32_e32 v64, v64, v22\n\t"
    "v_mul_f32_e32 v65, v65, v22\n\t"
    "v_mfma_f32_16x16x32_bf16 v[28:31], a[236:239], v[176:179], 0\n\t"
    "v_cvt_pk_bf16_f32 v24, v64, v65\n\t"
    "v_mul_f32_e32 v66, v66, v22\n\t"
    "buffer_load_dwordx4 v7, s[52:55], s61 offen offset:1024 nt lds\n\t"
    "v_mfma_f32_16x16x32_bf16 v[28:31], a[228:231], v[180:183], v[28:31]\n\t"
    "v_mul_f32_e32 v67, v67, v22\n\t"
    "v_cvt_pk_bf16_f32 v25, v66, v67\n\t"
    "v_mfma_f32_16x16x32_bf16 v[28:31], a[220:223], v[184:187], v[28:31]\n\t"
    "v_mul_f32_e32 v36, v36, v22\n\t"
    "v_mul_f32_e32 v37, v37, v22\n\t"
    "v_mfma_f32_16x16x32_bf16 v[28:31], a[212:215], v[188:191], v[28:31]\n\t"
    "v_cvt_pk_bf16_f32 v32, v12, v13\n\t"
    "v_cvt_pk_bf16_f32 v33, v14, v15\n\t"
    "v_mfma_f32_16x16x32_bf16 v[28:31], a[204:207], v[192:195], v[28:31]\n\t"
    "v_cvt_pk_bf16_f32 v26, v36, v37\n\t"
    "v_mul_f32_e32 v38, v38, v22\n\t"
    "v_mfma_f32_16x16x32_bf16 v[28:31], a[196:199], v[196:199], v[28:31]\n\t"
    "v_mul_f32_e32 v39, v39, v22\n\t"
    "v_cvt_pk_bf16_f32 v27, v38, v39\n\t"
    "buffer_load_dwordx4 v7, s[52:55], s61 offen offset:2048 nt lds\n\t"
    "v_mfma_f32_16x16x32_bf16 v[12:15], a[240:243], v[196:199], 0\n\t"
    "v_mul_f32_e32 v68, v68, v22\n\t"
    "v_mul_f32_e32 v69, v69, v22\n\t"
    "v_mfma_f32_16x16x32_bf16 v[12:15], a[248:251], v[192:195], v[12:15]\n\t"
    "v_cvt_pk_bf16_f32 v56, v68, v69\n\t"
    "v_mul_f32_e32 v70, v70, v22\n\t"
    "v_mfma_f32_16x16x32_bf16 v[12:15], v[224:227], v[188:191], v[12:15]\n\t"
    "v_mul_f32_e32 v71, v71, v22\n\t"
    "v_cvt_pk_bf16_f32 v57, v70, v71\n\t"
    "v_mfma_f32_16x16x32_bf16 v[12:15], v[232:235], v[184:187], v[12:15]\n\t"
    "v_cvt_pk_bf16_f32 v34, v28, v29\n\t"
    "v_cvt_pk_bf16_f32 v35, v30, v31\n\t"
    "s_add_u32 m0, %[rbeg], 27648\n\t"
    "s_add_u32 s61, %[wb], 12779520\n\t"
    "v_mfma_f32_16x16x32_bf16 v[12:15], v[240:243], v[180:183], v[12:15]\n\t"
    "v_mul_f32_e32 v72, v72, v22\n\t"
    "v_mul_f32_e32 v73, v73, v22\n\t"
    "v_mfma_f32_16x16x32_bf16 v[12:15], v[248:251], v[176:179], v[12:15]\n\t"
    "v_cvt_pk_bf16_f32 v58, v72, v73\n\t"
    "v_mul_f32_e32 v74, v74, v22\n\t"
    "v_mfma_f32_16x16x32_bf16 v[28:31], v[252:255], v[176:179], 0\n\t"
    "v_mul_f32_e32 v75, v75, v22\n\t"
    "v_cvt_pk_bf16_f32 v59, v74, v75\n\t"
    "v_mfma_f32_16x16x32_bf16 v[28:31], v[244:247], v[180:183], v[28:31]\n\t"
    "v_mfma_f32_16x16x32_bf16 v[28:31], v[236:239], v[184:187], v[28:31]\n\t"
    "buffer_load_dwordx4 v7, s[52:55], s61 offen offset:0 nt lds\n\t"
    "v_mfma_f32_16x16x32_bf16 v[28:31], v[228:231], v[188:191], v[28:31]\n\t"
    "v_cvt_pk_bf16_f32 v20, v12, v13\n\t"
    "v_cvt_pk_bf16_f32 v21, v14, v15\n\t"
    "v_mfma_f32_16x16x32_bf16 v[28:31], a[252:255], v[192:195], v[28:31]\n\t"
    "v_mfma_f32_16x16x32_bf16 v[28:31], a[244:247], v[196:199], v[28:31]\n\t"
    "v_mfma_f32_16x16x32_bf16 v[12:15], a[0:3], v[172:175], 0\n\t"
    "ds_write_b128 v6, v[24:27] offset:0\n\t"
    "v_mfma_f32_16x16x32_bf16 v[12:15], a[8:11], v[168:171], v[12:15]\n\t"
    "buffer_load_dwordx4 v7, s[52:55], s61 offen offset:1024 nt lds\n\t"
    "v_mfma_f32_16x16x32_bf16 v[12:15], a[16:19], v[164:167], v[12:15]\n\t"
    "v_mfma_f32_16x16x32_bf16 v[12:15], a[24:27], v[160:163], v[12:15]\n\t"
    "v_cvt_pk_bf16_f32 v22, v28, v29\n\t"
    "v_mfma_f32_16x16x32_bf16 v[12:15], a[32:35], v[156:159], v[12:15]\n\t"
    "ds_write_b128 v6, v[56:59] offset:1024\n\t"
    "v_mfma_f32_16x16x32_bf16 v[12:15], a[40:43], v[152:155], v[12:15]\n\t"
    "v_cvt_pk_bf16_f32 v23, v30, v31\n\t"
    "v_mfma_f32_16x16x32_bf16 v[40:43], v[208:211], v[48:51], v[40:43]\n\t"
    "buffer_load_dwordx4 v7, s[52:55], s61 offen offset:2048 nt lds\n\t"
    "v_mfma_f32_16x16x32_bf16 v[40:43], v[204:207], v[32:35], v[40:43]\n\t"
    "v_mfma_f32_16x16x32_bf16 v[40:43], v[200:203], v[20:23], v[40:43]\n\t"
    "s_add_i32 s64, s62, 24576\n\t"
    "v_mfma_f32_16x16x32_bf16 v[16:19], v[176:179], v[20:23], v[16:19]\n\t"
    "s_cmp_eq_u32 s64, %[rend]\n\t"
    "s_cselect_b32 s64, %[rbeg], s64\n\t"
    "v_mfma_f32_16x16x32_bf16 v[16:19], v[180:183], v[32:35], v[16:19]\n\t"
    "s_sub_i32 s65, s63, s64\n\t"
    "v_cvt_pk_bf16_f32 v24, v12, v13\n\t"
    "s_add_u32 m0, %[rbeg], 30720\n\t"
    "s_add_u32 s61, %[wb], 12976128\n\t"
    "v_mfma_f32_16x16x32_bf16 v[16:19], v[184:187], v[48:51], v[16:19]\n\t"
    "v_add_u32_e32 v0, s65, v0\n\t"
    "v_add_u32_e32 v1, s65, v1\n\t"
    "v_mfma_f32_16x16x32_bf16 v[44:47], v[160:163], v[48:51], v[44:47]\n\t"
    "v_add_u32_e32 v2, s65, v2\n\t"
    "v_add_u32_e32 v3, s65, v3\n\t"
    "v_mfma_f32_16x16x32_bf16 v[44:47], v[156:159], v[32:35], v[44:47]\n\t"
    "v_add_u32_e32 v4, s65, v4\n\t"
    "v_add_u32_e32 v5, s65, v5\n\t"
    "v_mfma_f32_16x16x32_bf16 v[44:47], v[152:155], v[20:23], v[44:47]\n\t"
    "v_max3_f32 v28, v40, v41, v42\n\t"
    "v_cvt_pk_bf16_f32 v25, v14, v15\n\t"
    "v_mfma_f32_16x16x32_bf16 v[52:55], v[128:131], v[20:23], v[52:55]\n\t"
    "v_max3_f32 v29, v43, v16, v17\n\t"
    ".Lh4_%=:\n\t"
    "s_cbranch_execz .Lh5_%=\n\t"
    "v_max3_f32 v28, v28, v18, v19\n\t"
    "v_mfma_f32_16x16x32_bf16 v[52:55], v[132:135], v[32:35], v[52:55]\n\t"
    "buffer_load_dwordx4 v7, s[52:55], s61 offen offset:0 nt lds\n\t"
    "v_mfma_f32_16x16x32_bf16 v[52:55], v[136:139], v[48:51], v[52:55]\n\t"
    "v_mfma_f32_16x16x32_bf16 v[12:15], a[44:47], v[152:155], 0\n\t"
    "v_max3_f32 v29, v29, v44, v45\n\t"
    "v_max3_f32 v28, v28, v46, v47\n\t"
    "v_mfma_f32_16x16x32_bf16 v[12:15], a[36:39], v[156:159], v[12:15]\n\t"
    "v_mfma_f32_16x16x32_bf16 v[12:15], a[28:31], v[160:163], v[12:15]\n\t"
    "buffer_load_dwordx4 v7, s[52:55], s61 offen offset:1024 nt lds\n\t"
    "v_mfma_f32_16x16x32_bf16 v[12:15], a[20:23], v[164:167], v[12:15]\n\t"
    "v_max3_f32 v29, v29, v52, v53\n\t"
    "v_max3_f32 v28, v28, v54, v55\n\t"
    "v_mfma_f32_16x16x32_bf16 v[12:15], a[12:15], v[168:171], v[12:15]\n\t"
    "v_max_f32_e32 v28, v28, v29\n\t"
    "v_mov_b32_e32 v29, v28\n\t"
    "v_mfma_f32_16x16x32_bf16 v[12:15], a[4:7], v[172:175], v[12:15]\n\t"
    "v_mfma_f32_16x16x32_bf16 v[20:23], a[48:51], v[172:175], 0\n\t"
    "v_permlane32_swap_b32 v28, v29\n\t"
    "v_max_f32_e32 v28, v28, v29\n\t"
    "v_mfma_f32_16x16x32_bf16 v[20:23], a[56:59], v[168:171], v[20:23]\n\t"
    "v_mov_b32_e32 v29, v28\n\t"
    "buffer_load_dwordx4 v7, s[52:55], s61 offen offset:2048 nt lds\n\t"
    "v_mfma_f32_16x16x32_bf16 v[20:23], a[64:67], v[164:167], v[20:23]\n\t"
    "v_mfma_f32_16x16x32_bf16 v[20:23], a[72:75], v[160:163], v[20:23]\n\t"
    "v_cvt_pk_bf16_f32 v26, v12, v13\n\t"
    "v_cvt_pk_bf16_f32 v27, v14, v15\n\t"
    "v_mfma_f32_16x16x32_bf16 v[20:23], a[80:83], v[156:159], v[20:23]\n\t"
    "v_permlane16_swap_b32 v28, v29\n\t"
    "v_max_f32_e32 v28, v28, v29\n\t"
    "v_mfma_f32_16x16x32_bf16 v[20:23], a[88:91], v[152:155], v[20:23]\n\t"
    "v_sub_f32_e32 v40, v40, v28\n\t"
    "v_sub_f32_e32 v41, v41, v28\n\t"
    "s_add_u32 m0, %[rbeg], 33792\n\t"
    "s_add_u32 s61, %[wb], 13172736\n\t"
    "v_mfma_f32_16x16x32_bf16 v[12:15], a[92:95], v[152:155], 0\n\t"
    "v_exp_f32_e32 v40, v40\n\t"
    "v_mfma_f32_16x16x32_bf16 v[12:15], a[84:87], v[156:159], v[12:15]\n\t"
    "v_exp_f32_e32 v41, v41\n\t"
    "v_mfma_f32_16x16x32_bf16 v[12:15], a[76:79], v[160:163], v[12:15]\n\t"
    "v_add_f32_e32 v30, v40, v41\n\t"
    "v_sub_f32_e32 v42, v42, v28\n\t"
    "v_mfma_f32_16x16x32_bf16 v[12:15], a[68:71], v[164:167], v[12:15]\n\t"
    "v_cvt_pk_bf16_f32 v32, v20, v21\n\t"
    "v_cvt_pk_bf16_f32 v33, v22, v23\n\t"
    "v_mfma_f32_16x16x32_bf16 v[12:15], a[60:63], v[168:171], v[12:15]\n\t"
    "v_exp_f32_e32 v42, v42\n\t"
    "buffer_load_dwordx4 v7, s[52:55], s61 offen offset:0 nt lds\n\t"
    "v_mfma_f32_16x16x32_bf16 v[12:15], a[52:55], v[172:175], v[12:15]\n\t"
    "v_sub_f32_e32 v43, v43, v28\n\t"
    "v_sub_f32_e32 v16, v16, v28\n\t"
    "v_mfma_f32_16x16x32_bf16 v[20:23], a[96:99], v[172:175], 0\n\t"
    "v_exp_f32_e32 v43, v43\n\t"
    "v_mfma_f32_16x16x32_bf16 v[20:23], a[104:107], v[168:171], v[20:23]\n\t"
    "v_add_f32_e32 v31, v42, v43\n\t"
    "v_sub_f32_e32 v17, v17, v28\n\t"
    "v_mfma_f32_16x16x32_bf16 v[20:23], a[112:115], v[164:167], v[20:23]\n\t"
    "v_exp_f32_e32 v16, v16\n\t"
    "v_mfma_f32_16x16x32_bf16 v[20:23], a[120:123], v[160:163], v[20:23]\n\t"
    "v_cvt_pk_bf16_f32 v34, v12, v13\n\t"
    "v_cvt_pk_bf16_f32 v35, v14, v15\n\t"
    "buffer_load_dwordx4 v7, s[52:55], s61 offen offset:1024 nt lds\n\t"
    "v_mfma_f32_16x16x32_bf16 v[20:23], a[128:131], v[156:159], v[20:23]\n\t"
    "v_add_f32_e32 v30, v30, v16\n\t"
    "v_sub_f32_e32 v18, v18, v28\n\t"
    "v_mfma_f32_16x16x32_bf16 v[20:23], a[136:139], v[152:155], v[20:23]\n\t"
    "v_exp_f32_e32 v17, v17\n\t"
    "v_mfma_f32_16x16x32_bf16 v[12:15], a[140:143], v[152:155], 0\n\t"
    "v_add_f32_e32 v30, v30, v17\n\t"
    "v_sub_f32_e32 v19, v19, v28\n\t"
    "v_mfma_f32_16x16x32_bf16 v[12:15], a[132:135], v[156:159], v[12:15]\n\t"
    "v_exp_f32_e32 v18, v18\n\t"
    "buffer_load_dwordx4 v7, s[52:55], s61 offen offset:2048 nt lds\n\t"
    "v_mfma_f32_16x16x32_bf16 v[12:15], a[124:127], v[160:163], v[12:15]\n\t"
    "v_add_f32_e32 v31, v31, v18\n\t"
    "v_sub_f32_e32 v44, v44, v28\n\t"
    "v_mfma_f32_16x16x32_bf16 v[12:15], a[116:119], v[164:167], v[12:15]\n\t"
    "v_cvt_pk_bf16_f32 v36, v20, v21\n\t"
    "v_cvt_pk_bf16_f32 v37, v22, v23\n\t"
    "v_mfma_f32_16x16x32_bf16 v[12:15], a[108:111], v[168:171], v[12:15]\n\t"
    "v_exp_f32_e32 v19, v19\n\t"
    "v_mfma_f32_16x16x32_bf16 v[12:15], a[100:103], v[172:175], v[12:15]\n\t"
    "v_add_f32_e32 v31, v31, v19\n\t"
    "v_sub_f32_e32 v45, v45, v28\n\t"
    "v_mfma_f32_16x16x32_bf16 v[20:23], a[144:147], v[172:175], 0\n\t"
    "v_exp_f32_e32 v44, v44\n\t"
    "v_mfma_f32_16x16x32_bf16 v[20:23], a[152:155], v[168:171], v[20:23]\n\t"
    "v_add_f32_e32 v30, v30, v44\n\t"
    "v_sub_f32_e32 v46, v46, v28\n\t"
    "v_mfma_f32_16x16x32_bf16 v[20:23], a[160:163], v[164:167], v[20:23]\n\t"
    "v_sub_f32_e32 v47, v47, v28\n\t"
    "s_add_u32 m0, %[rbeg], 36864\n\t"
    "s_add_u32 s61, %[wb], 13369344\n\t"
    "v_mfma_f32_16x16x32_bf16 v[20:23], a[168:171], v[160:163], v[20:23]\n\t"
    "v_cvt_pk_bf16_f32 v38, v12, v13\n\t"
    "v_mfma_f32_16x16x32_bf16 v[20:23], a[176:179], v[156:159], v[20:23]\n\t"
    "v_cvt_pk_bf16_f32 v39, v14, v15\n\t"
    "v_sub_f32_e32 v52, v52, v28\n\t"
    "v_mfma_f32_16x16x32_bf16 v[20:23], a[184:187], v[152:155], v[20:23]\n\t"
    "v_sub_f32_e32 v53, v53, v28\n\t"
    "buffer_load_dwordx4 v7, s[52:55], s61 offen offset:0 nt lds\n\t"
    "v_mfma_f32_16x16x32_bf16 v[56:59], v[220:223], v[24:27], 0\n\t"
    "v_exp_f32_e32 v45, v45\n\t"
    "v_mfma_f32_16x16x32_bf16 v[56:59], v[216:219], v[32:35], v[56:59]\n\t"
    "v_add_f32_e32 v30, v30, v45\n\t"
    "v_mfma_f32_16x16x32_bf16 v[56:59], v[212:215], v[36:39], v[56:59]\n\t"
    "v_sub_f32_e32 v54, v54, v28\n\t"
    "v_mfma_f32_16x16x32_bf16 v[60:63], v[188:191], v[36:39], 0\n\t"
    "v_exp_f32_e32 v46, v46\n\t"
    "v_mfma_f32_16x16x32_bf16 v[60:63], v[192:195], v[32:35], v[60:63]\n\t"
    "v_add_f32_e32 v31, v31, v46\n\t"
    "v_sub_f32_e32 v55, v55, v28\n\t"
    "buffer_load_dwordx4 v7, s[52:55], s61 offen offset:1024 nt lds\n\t"
    "v_mfma_f32_16x16x32_bf16 v[60:63], v[196:199], v[24:27], v[60:63]\n\t"
    "v_exp_f32_e32 v47, v47\n\t"
    "v_mfma_f32_16x16x32_bf16 v[64:67], v[172:175], v[24:27], 0\n\t"
    "v_add_f32_e32 v31, v31, v47\n\t"
    "v_cvt_pk_bf16_f32 v12, v20, v21\n\t"
    "v_mfma_f32_16x16x32_bf16 v[64:67], v[168:171], v[32:35], v[64:67]\n\t"
    "v_exp_f32_e32 v52, v52\n\t"
    "v_mfma_f32_16x16x32_bf16 v[64:67], v[164:167], v[36:39], v[64:67]\n\t"
    "v_add_f32_e32 v30, v30, v52\n\t"
    "v_cvt_pk_bf16_f32 v13, v22, v23\n\t"
    "v_mfma_f32_16x16x32_bf16 v[68:71], v[140:143], v[36:39], 0\n\t"
    "v_exp_f32_e32 v53, v53\n\t"
    "v_mfma_f32_16x16x32_bf16 v[68:71], v[144:147], v[32:35], v[68:71]\n\t"
    "v_add_f32_e32 v30, v30, v53\n\t"
    "buffer_load_dwordx4 v7, s[52:55], s61 offen offset:2048 nt lds\n\t"
    "v_mfma_f32_16x16x32_bf16 v[68:71], v[148:151], v[24:27], v[68:71]\n\t"
    "v_exp_f32_e32 v54, v54\n\t"
    "v_mfma_f32_16x16x32_bf16 v[20:23], a[188:191], v[152:155], 0\n\t"
    "v_add_f32_e32 v31, v31, v54\n\t"
    "v_mfma_f32_16x16x32_bf16 v[20:23], a[180:183], v[156:159], v[20:23]\n\t"
    "v_exp_f32_e32 v55, v55\n\t"
    "v_mfma_f32_16x16x32_bf16 v[20:23], a[172:175], v[160:163], v[20:23]\n\t"
    "v_add_f32_e32 v31, v31, v55\n\t"
    "v_add_f32_e32 v30, v30, v31\n\t"
    "v_mfma_f32_16x16x32_bf16 v[20:23], a[164:167], v[164:167], v[20:23]\n\t"
    "v_mov_b32_e32 v31, v30\n\t"
    "v_mfma_f32_16x16x32_bf16 v[20:23], a[156:159], v[168:171], v[20:23]\n\t"
    "s_add_u32 m0, %[rbeg], 39936\n\t"
    "s_add_u32 s61, %[wb], 13565952\n\t"
    "v_mfma_f32_16x16x32_bf16 v[20:23], a[148:151], v[172:175], v[20:23]\n\t"
    "v_permlane32_swap_b32 v30, v31\n\t"
    "v_add_f32_e32 v30, v30, v31\n\t"
    "v_mfma_f32_16x16x32_bf16 v[24:27], a[192:195], v[172:175], 0\n\t"
    "v_mov_b32_e32 v31, v30\n\t"
    "buffer_load_dwordx4 v7, s[52:55], s61 offen offset:0 nt lds\n\t"
    "v_mfma_f32_16x16x32_bf16 v[24:27], a[200:203], v[168:171], v[24:27]\n\t"
    "v_mfma_f32_16x16x32_bf16 v[24:27], a[208:211], v[164:167], v[24:27]\n\t"
    "v_permlane16_swap_b32 v30, v31\n\t"
    "v_add_f32_e32 v30, v30, v31\n\t"
    "v_mfma_f32_16x16x32_bf16 v[24:27], a[216:219], v[160:163], v[24:27]\n\t"
    "v_cvt_pk_bf16_f32 v14, v20, v21\n\t"
    "v_cvt_pk_bf16_f32 v15, v22, v23\n\t"
    "v_mfma_f32_16x16x32_bf16 v[24:27], a[224:227], v[156:159], v[24:27]\n\t"
    "v_rcp_f32_e32 v30, v30\n\t"
    "buffer_load_dwordx4 v7, s[52:55], s61 offen offset:1024 nt lds\n\t"
    "v_mfma_f32_16x16x32_bf16 v[24:27], a[232:235], v[152:155], v[24:27]\n\t"
    "v_mul_f32_e32 v40, v40, v30\n\t"
    "v_mul_f32_e32 v41, v41, v30\n\t"
    "v_mfma_f32_16x16x32_bf16 v[32:35], a[236:239], v[152:155], 0\n\t"
    "v_cvt_pk_bf16_f32 v20, v40, v41\n\t"
    "v_mul_f32_e32 v42, v42, v30\n\t"
    "v_mfma_f32_16x16x32_bf16 v[32:35], a[228:231], v[156:159], v[32:35]\n\t"
    "v_mul_f32_e32 v43, v43, v30\n\t"
    "v_cvt_pk_bf16_f32 v21, v42, v43\n\t"
    "v_mfma_f32_16x16x32_bf16 v[32:35], a[220:223], v[160:163], v[32:35]\n\t"
    "v_mul_f32_e32 v16, v16, v30\n\t"
    "v_mul_f32_e32 v17, v17, v30\n\t"
    "v_mfma_f32_16x16x32_bf16 v[32:35], a[212:215], v[164:167], v[32:35]\n\t"
    "v_cvt_pk_bf16_f32 v36, v24, v25\n\t"
    "v_cvt_pk_bf16_f32 v37, v26, v27\n\t"
    "buffer_load_dwordx4 v7, s[52:55], s61 offen offset:2048 nt lds\n\t"
    "v_mfma_f32_16x16x32_bf16 v[32:35], a[204:207], v[168:171], v[32:35]\n\t"
    "v_cvt_pk_bf16_f32 v22, v16, v17\n\t"
    "v_mul_f32_e32 v18, v18, v30\n\t"
    "v_mfma_f32_16x16x32_bf16 v[32:35], a[196:199], v[172:175], v[32:35]\n\t"
    "v_mul_f32_e32 v19, v19, v30\n\t"
    "v_cvt_pk_bf16_f32 v23, v18, v19\n\t"
    "v_mfma_f32_16x16x32_bf16 v[24:27], a[240:243], v[172:175], 0\n\t"
    "v_mul_f32_e32 v44, v44, v30\n\t"
    "v_mul_f32_e32 v45, v45, v30\n\t"
    "v_mfma_f32_16x16x32_bf16 v[24:27], a[248:251], v[168:171], v[24:27]\n\t"
    "v_cvt_pk_bf16_f32 v40, v44, v45\n\t"
    "v_mul_f32_e32 v46, v46, v30\n\t"
    "v_mfma_f32_16x16x32_bf16 v[24:27], v[224:227], v[164:167], v[24:27]\n\t"
    "v_mul_f32_e32 v47, v47, v30\n\t"
    "v_cvt_pk_bf16_f32 v41, v46, v47\n\t"
    "s_add_u32 m0, %[rbeg], 43008\n\t"
    "s_add_u32 s61, %[wb], 13762560\n\t"
    "v_mfma_f32_16x16x32_bf16 v[24:27], v[232:235], v[160:163], v[24:27]\n\t"
    "v_cvt_pk_bf16_f32 v38, v32, v33\n\t"
    "v_cvt_pk_bf16_f32 v39, v34, v35\n\t"
    "v_mfma_f32_16x16x32_bf16 v[24:27], v[240:243], v[156:159], v[24:27]\n\t"
    "v_mul_f32_e32 v52, v52, v30\n\t"
    "v_mul_f32_e32 v53, v53, v30\n\t"
    "v_mfma_f32_16x16x32_bf16 v[24:27], v[248:251], v[152:155], v[24:27]\n\t"
    "v_cvt_pk_bf16_f32 v42, v52, v53\n\t"
    "v_mul_f32_e32 v54, v54, v30\n\t"
    "v_mfma_f32_16x16x32_bf16 v[16:19], v[252:255], v[152:155], 0\n\t"
    "v_mul_f32_e32 v55, v55, v30\n\t"
    "v_cvt_pk_bf16_f32 v43, v54, v55\n\t"
    "v_mfma_f32_16x16x32_bf16 v[16:19], v[244:247], v[156:159], v[16:19]\n\t"
    "buffer_load_dwordx4 v7, s[52:55], s61 offen offset:0 nt lds\n\t"
    "v_mfma_f32_16x16x32_bf16 v[16:19], v[236:239], v[160:163], v[16:19]\n\t"
    "v_mfma_f32_16x16x32_bf16 v[16:19], v[228:231], v[164:167], v[16:19]\n\t"
    "v_cvt_pk_bf16_f32 v28, v24, v25\n\t"
    "v_cvt_pk_bf16_f32 v29, v26, v27\n\t"
    "v_mfma_f32_16x16x32_bf16 v[16:19], a[252:255], v[168:171], v[16:19]\n\t"
    "v_mfma_f32_16x16x32_bf16 v[16:19], a[244:247], v[172:175], v[16:19]\n\t"
    "buffer_load_dwordx4 v7, s[52:55], s61 offen offset:1024 nt lds\n\t"
    "v_mfma_f32_16x16x32_bf16 v[24:27], a[0:3], v[148:151], 0\n\t"
    "ds_write_b128 v6, v[20:23] offset:2048\n\t"
    "v_mfma_f32_16x16x32_bf16 v[24:27], a[8:11], v[144:147], v[24:27]\n\t"
    "v_mfma_f32_16x16x32_bf16 v[24:27], a[16:19], v[140:143], v[24:27]\n\t"
    "v_mfma_f32_16x16x32_bf16 v[24:27], a[24:27], v[136:139], v[24:27]\n\t"
    "v_cvt_pk_bf16_f32 v30, v16, v17\n\t"
    "v_mfma_f32_16x16x32_bf16 v[24:27], a[32:35], v[132:135], v[24:27]\n\t"
    "ds_write_b128 v6, v[40:43] offset:3072\n\t"
    "v_mfma_f32_16x16x32_bf16 v[24:27], a[40:43], v[128:131], v[24:27]\n\t"
    "v_cvt_pk_bf16_f32 v31, v18, v19\n\t"
    "buffer_load_dwordx4 v7, s[52:55], s61 offen offset:2048 nt lds\n\t"
    "v_mfma_f32_16x16x32_bf16 v[56:59], v[208:211], v[12:15], v[56:59]\n\t"
    "v_mfma_f32_16x16x32_bf16 v[56:59], v[204:207], v[36:39], v[56:59]\n\t"
    "v_mfma_f32_16x16x32_bf16 v[56:59], v[200:203], v[28:31], v[56:59]\n\t"
    "v_mfma_f32_16x16x32_bf16 v[60:63], v[176:179], v[28:31], v[60:63]\n\t"
    "v_cvt_pk_bf16_f32 v40, v24, v25\n\t"
    "v_cvt_pk_bf16_f32 v41, v26, v27\n\t"
    "v_mfma_f32_16x16x32_bf16 v[60:63], v[180:183], v[36:39], v[60:63]\n\t"
    "s_add_u32 m0, %[rbeg], 46080\n\t"
    "s_add_u32 s61, %[wb], 13959168\n\t"
    "v_mfma_f32_16x16x32_bf16 v[60:63], v[184:187], v[12:15], v[60:63]\n\t"
    "v_mfma_f32_16x16x32_bf16 v[64:67], v[160:163], v[12:15], v[64:67]\n\t"
    "v_max3_f32 v72, v56, v57, v58\n\t"
    "v_mfma_f32_16x16x32_bf16 v[64:67], v[156:159], v[36:39], v[64:67]\n\t"
    "buffer_load_dwordx4 v7, s[52:55], s61 offen offset:0 nt lds\n\t"
    "v_mfma_f32_16x16x32_bf16 v[64:67], v[152:155], v[28:31], v[64:67]\n\t"
    "v_mfma_f32_16x16x32_bf16 v[68:71], v[128:131], v[28:31], v[68:71]\n\t"
    "v_max3_f32 v73, v59, v60, v61\n\t"
    "v_max3_f32 v72, v72, v62, v63\n\t"
    "v_mfma_f32_16x16x32_bf16 v[68:71], v[132:135], v[36:39], v[68:71]\n\t"
    "v_mfma_f32_16x16x32_bf16 v[68:71], v[136:139], v[12:15], v[68:71]\n\t"
    "v_mfma_f32_16x16x32_bf16 v[16:19], a[44:47], v[128:131], 0\n\t"
    "v_max3_f32 v73, v73, v64, v65\n\t"
    "v_max3_f32 v72, v72, v66, v67\n\t"
    "v_mfma_f32_16x16x32_bf16 v[16:19], a[36:39], v[132:135], v[16:19]\n\t"
    "buffer_load_dwordx4 v7, s[52:55], s61 offen offset:1024 nt lds\n\t"
    "v_mfma_f32_16x16x32_bf16 v[16:19], a[28:31], v[136:139], v[16:19]\n\t"
    "v_mfma_f32_16x16x32_bf16 v[16:19], a[20:23], v[140:143], v[16:19]\n\t"
    "v_max3_f32 v73, v73, v68, v69\n\t"
    "v_max3_f32 v72, v72, v70, v71\n\t"
    "v_mfma_f32_16x16x32_bf16 v[16:19], a[12:15], v[144:147], v[16:19]\n\t"
    "v_max_f32_e32 v72, v72, v73\n\t"
    "v_mov_b32_e32 v73, v72\n\t"
    "v_mfma_f32_16x16x32_bf16 v[16:19], a[4:7], v[148:151], v[16:19]\n\t"
    "buffer_load_dwordx4 v7, s[52:55], s61 offen offset:2048 nt lds\n\t"
    "v_mfma_f32_16x16x32_bf16 v[12:15], a[48:51], v[148:151], 0\n\t"
    "v_permlane32_swap_b32 v72, v73\n\t"
    "v_max_f32_e32 v72, v72, v73\n\t"
    "v_mfma_f32_16x16x32_bf16 v[12:15], a[56:59], v[144:147], v[12:15]\n\t"
    "v_mov_b32_e32 v73, v72\n\t"
    "v_mfma_f32_16x16x32_bf16 v[12:15], a[64:67], v[140:143], v[12:15]\n\t"
    "v_mfma_f32_16x16x32_bf16 v[12:15], a[72:75], v[136:139], v[12:15]\n\t"
    "v_cvt_pk_bf16_f32 v42, v16, v17\n\t"
    "v_cvt_pk_bf16_f32 v43, v18, v19\n\t"
    "v_mfma_f32_16x16x32_bf16 v[12:15], a[80:83], v[132:135], v[12:15]\n\t"
    "v_permlane16_swap_b32 v72, v73\n\t"
    "v_max_f32_e32 v72, v72, v73\n\t"
    "s_add_u32 m0, %[rbeg], 49152\n\t"
    "s_add_u32 s61, %[wb], 25165824\n\t"
    "v_mfma_f32_16x16x32_bf16 v[12:15], a[88:91], v[128:131], v[12:15]\n\t"
    "v_sub_f32_e32 v56, v56, v72\n\t"
    "v_sub_f32_e32 v57, v57, v72\n\t"
    "v_mfma_f32_16x16x32_bf16 v[16:19], a[92:95], v[128:131], 0\n\t"
    "v_exp_f32_e32 v56, v56\n\t"
    "v_mfma_f32_16x16x32_bf16 v[16:19], a[84:87], v[132:135], v[16:19]\n\t"
    "v_exp_f32_e32 v57, v57\n\t"
    "v_mfma_f32_16x16x32_bf16 v[16:19], a[76:79], v[136:139], v[16:19]\n\t"
    "v_add_f32_e32 v74, v56, v57\n\t"
    "v_sub_f32_e32 v58, v58, v72\n\t"
    "buffer_load_dwordx4 v7, s[52:55], s61 offen offset:0 nt lds\n\t"
    "v_mfma_f32_16x16x32_bf16 v[16:19], a[68:71], v[140:143], v[16:19]\n\t"
    "v_cvt_pk_bf16_f32 v44, v12, v13\n\t"
    "v_cvt_pk_bf16_f32 v45, v14, v15\n\t"
    "v_mfma_f32_16x16x32_bf16 v[16:19], a[60:63], v[144:147], v[16:19]\n\t"
    "v_exp_f32_e32 v58, v58\n\t"
    "v_mfma_f32_16x16x32_bf16 v[16:19], a[52:55], v[148:151], v[16:19]\n\t"
    "v_sub_f32_e32 v59, v59, v72\n\t"
    "v_sub_f32_e32 v60, v60, v72\n\t"
    "v_mfma_f32_16x16x32_bf16 v[12:15], a[96:99], v[148:151], 0\n\t"
    "v_exp_f32_e32 v59, v59\n\t"
    "v_mfma_f32_16x16x32_bf16 v[12:15], a[104:107], v[144:147], v[12:15]\n\t"
    "v_add_f32_e32 v75, v58, v59\n\t"
    "v_sub_f32_e32 v61, v61, v72\n\t"
    "buffer_load_dwordx4 v7, s[52:55], s61 offen offset:1024 nt lds\n\t"
    "v_mfma_f32_16x16x32_bf16 v[12:15], a[112:115], v[140:143], v[12:15]\n\t"
    "v_exp_f32_e32 v60, v60\n\t"
    "v_mfma_f32_16x16x32_bf16 v[12:15], a[120:123], v[136:139], v[12:15]\n\t"
    "v_cvt_pk_bf16_f32 v46, v16, v17\n\t"
    "v_cvt_pk_bf16_f32 v47, v18, v19\n\t"
    "v_mfma_f32_16x16x32_bf16 v[12:15], a[128:131], v[132:135], v[12:15]\n\t"
    "v_add_f32_e32 v74, v74, v60\n\t"
    "v_sub_f32_e32 v62, v62, v72\n\t"
    "v_mfma_f32_16x16x32_bf16 v[12:15], a[136:139], v[128:131], v[12:15]\n\t"
    "v_exp_f32_e32 v61, v61\n\t"
    "v_mfma_f32_16x16x32_bf16 v[16:19], a[140:143], v[128:131], 0\n\t"
    "v_add_f32_e32 v74, v74, v61\n\t"
    "v_sub_f32_e32 v63, v63, v72\n\t"
    "buffer_load_dwordx4 v7, s[52:55], s61 offen offset:2048 nt lds\n\t"
    "v_mfma_f32_16x16x32_bf16 v[16:19], a[132:135], v[132:135], v[16:19]\n\t"
    "v_exp_f32_e32 v62, v62\n\t"
    "v_mfma_f32_16x16x32_bf16 v[16:19], a[124:127], v[136:139], v[16:19]\n\t"
    "v_add_f32_e32 v75, v75, v62\n\t"
    "v_sub_f32_e32 v64, v64, v72\n\t"
    "v_mfma_f32_16x16x32_bf16 v[16:19], a[116:119], v[140:143], v[16:19]\n\t"
    "v_cvt_pk_bf16_f32 v32, v12, v13\n\t"
    "v_cvt_pk_bf16_f32 v33, v14, v15\n\t"
    "v_mfma_f32_16x16x32_bf16 v[16:19], a[108:111], v[144:147], v[16:19]\n\t"
    "v_exp_f32_e32 v63, v63\n\t"
    "v_mfma_f32_16x16x32_bf16 v[16:19], a[100:103], v[148:151], v[16:19]\n\t"
    "v_add_f32_e32 v75, v75, v63\n\t"
    "v_sub_f32_e32 v65, v65, v72\n\t"
    "v_mfma_f32_16x16x32_bf16 v[48:51], a[144:147], v[148:151], 0\n\t"
    "v_exp_f32_e32 v64, v64\n\t"
    "v_mfma_f32_16x16x32_bf16 v[48:51], a[152:155], v[144:147], v[48:51]\n\t"
    "v_add_f32_e32 v74, v74, v64\n\t"
    "v_sub_f32_e32 v66, v66, v72\n\t"
    "s_mov_b32 s60, 8\n\t"
    "s_add_u32 s61, %[wb], 25362432\n\t"
    ".Lmid_%=:\n\t"
    "v_mfma_f32_16x16x32_bf16 v[48:51], a[160:163], v[140:143], v[48:51]\n\t"
    "s_add_u32 m0, s62, 3072\n\t"
    "v_sub_f32_e32 v67, v67, v72\n\t"
    "v_mfma_f32_16x16x32_bf16 v[48:51], a[168:171], v[136:139], v[48:51]\n\t"
    "buffer_load_dwordx4 v7, s[52:55], s61 offen offset:0 nt lds\n\t"
    "v_cvt_pk_bf16_f32 v34, v16, v17\n\t"
    "v_mfma_f32_16x16x32_bf16 v[48:51], a[176:179], v[132:135], v[48:51]\n\t"
    "v_cvt_pk_bf16_f32 v35, v18, v19\n\t"
    "v_sub_f32_e32 v68, v68, v72\n\t"
    "v_mfma_f32_16x16x32_bf16 v[48:51], a[184:187], v[128:131], v[48:51]\n\t"
    "buffer_load_dwordx4 v7, s[52:55], s61 offen offset:1024 nt lds\n\t"
    "v_sub_f32_e32 v69, v69, v72\n\t"
    "v_mfma_f32_16x16x32_bf16 v[12:15], v[220:223], v[40:43], 0\n\t"
    "v_exp_f32_e32 v65, v65\n\t"
    "v_mfma_f32_16x16x32_bf16 v[12:15], v[216:219], v[44:47], v[12:15]\n\t"
    "buffer_load_dwordx4 v7, s[52:55], s61 offen offset:2048 nt lds\n\t"
    "v_add_f32_e32 v74, v74, v65\n\t"
    "v_mfma_f32_16x16x32_bf16 v[12:15], v[212:215], v[32:35], v[12:15]\n\t"
    "s_waitcnt vmcnt(24)\n\t"
    "s_add_u32 s61, s61, 196608\n\t"
    "v_mfma_f32_16x16x32_bf16 v[16:19], v[188:191], v[32:35], 0\n\t"
    "v_exp_f32_e32 v66, v66\n\t"
    "v_mfma_f32_16x16x32_bf16 v[16:19], v[192:195], v[44:47], v[16:19]\n\t"
    "ds_read_b128 v[220:223], v0 offset:0\n\t"
    "v_add_f32_e32 v75, v75, v66\n\t"
    "v_mfma_f32_16x16x32_bf16 v[16:19], v[196:199], v[40:43], v[16:19]\n\t"
    "v_exp_f32_e32 v67, v67\n\t"
    "v_mfma_f32_16x16x32_bf16 v[20:23], v[172:175], v[40:43], 0\n\t"
    "ds_read_b128 v[216:219], v1 offset:0\n\t"
    "v_add_f32_e32 v75, v75, v67\n\t"
    "v_mfma_f32_16x16x32_bf16 v[20:23], v[168:171], v[44:47], v[20:23]\n\t"
    "v_exp_f32_e32 v68, v68\n\t"
    "v_mfma_f32_16x16x32_bf16 v[20:23], v[164:167], v[32:35], v[20:23]\n\t"
    "ds_read_b128 v[212:215], v2 offset:0\n\t"
    "v_add_f32_e32 v74, v74, v68\n\t"
    "v_mfma_f32_16x16x32_bf16 v[24:27], v[140:143], v[32:35], 0\n\t"
    "v_exp_f32_e32 v69, v69\n\t"
    "v_mfma_f32_16x16x32_bf16 v[24:27], v[144:147], v[44:47], v[24:27]\n\t"
    "ds_read_b128 v[28:31], v3 offset:0\n\t"
    "v_add_f32_e32 v74, v74, v69\n\t"
    "v_mfma_f32_16x16x32_bf16 v[24:27], v[148:151], v[40:43], v[24:27]\n\t"
    "v_sub_f32_e32 v70, v70, v72\n\t"
    "v_sub_f32_e32 v71, v71, v72\n\t"
    "v_mfma_f32_16x16x32_bf16 v[52:55], a[188:191], v[128:131], 0\n\t"
    "ds_read_b128 v[32:35], v4 offset:0\n\t"
    "v_cvt_pk_bf16_f32 v36, v48, v49\n\t"
    "v_mfma_f32_16x16x32_bf16 v[52:55], a[180:183], v[132:135], v[52:55]\n\t"
    "v_exp_f32_e32 v70, v70\n\t"
    "v_mfma_f32_16x16x32_bf16 v[52:55], a[172:175], v[136:139], v[52:55]\n\t"
    "ds_read_b128 v[40:43], v5 offset:0\n\t"
    "v_add_f32_e32 v75, v75, v70\n\t"
    "v_mfma_f32_16x16x32_bf16 v[52:55], a[164:167], v[140:143], v[52:55]\n\t"
    "v_exp_f32_e32 v71, v71\n\t"
    "v_mfma_f32_16x16x32_bf16 v[52:55], a[156:159], v[144:147], v[52:55]\n\t"
    "v_add_f32_e32 v75, v75, v71\n\t"
    "v_add_f32_e32 v74, v74, v75\n\t"
    "v_mfma_f32_16x16x32_bf16 v[52:55], a[148:151], v[148:151], v[52:55]\n\t"
    "v_mov_b32_e32 v75, v74\n\t"
    "v_cvt_pk_bf16_f32 v37, v50, v51\n\t"
    "v_mfma_f32_16x16x32_bf16 v[76:79], a[192:195], v[148:151], 0\n\t"
    "v_mfma_f32_16x16x32_bf16 v[76:79], a[200:203], v[144:147], v[76:79]\n\t"
    "v_permlane32_swap_b32 v74, v75\n\t"
    "v_add_f32_e32 v74, v74, v75\n\t"
    "v_mfma_f32_16x16x32_bf16 v[76:79], a[208:211], v[140:143], v[76:79]\n\t"
    "v_mov_b32_e32 v75, v74\n\t"
    "v_mfma_f32_16x16x32_bf16 v[76:79], a[216:219], v[136:139], v[76:79]\n\t"
    "v_cvt_pk_bf16_f32 v38, v52, v53\n\t"
    "v_cvt_pk_bf16_f32 v39, v54, v55\n\t"
    "v_mfma_f32_16x16x32_bf16 v[76:79], a[224:227], v[132:135], v[76:79]\n\t"
    "v_permlane16_swap_b32 v74, v75\n\t"
    "v_add_f32_e32 v74, v74, v75\n\t"
    "v_mfma_f32_16x16x32_bf16 v[76:79], a[232:235], v[128:131], v[76:79]\n\t"
    "v_rcp_f32_e32 v74, v74\n\t"
    "v_mfma_f32_16x16x32_bf16 v[80:83], a[236:239], v[128:131], 0\n\t"
    "v_mul_f32_e32 v56, v56, v74\n\t"
    "v_mul_f32_e32 v57, v57, v74\n\t"
    "v_mfma_f32_16x16x32_bf16 v[80:83], a[228:231], v[132:135], v[80:83]\n\t"
    "v_cvt_pk_bf16_f32 v44, v56, v57\n\t"
    "v_mul_f32_e32 v58, v58, v74\n\t"
    "v_mfma_f32_16x16x32_bf16 v[80:83], a[220:223], v[136:139], v[80:83]\n\t"
    "v_mul_f32_e32 v59, v59, v74\n\t"
    "v_cvt_pk_bf16_f32 v45, v58, v59\n\t"
    "v_mfma_f32_16x16x32_bf16 v[80:83], a[212:215], v[140:143], v[80:83]\n\t"
    "v_cvt_pk_bf16_f32 v48, v76, v77\n\t"
    "v_cvt_pk_bf16_f32 v49, v78, v79\n\t"
    "v_mfma_f32_16x16x32_bf16 v[80:83], a[204:207], v[144:147], v[80:83]\n\t"
    "v_mul_f32_e32 v60, v60, v74\n\t"
    "v_mul_f32_e32 v61, v61, v74\n\t"
    "v_mfma_f32_16x16x32_bf16 v[80:83], a[196:199], v[148:151], v[80:83]\n\t"
    "v_cvt_pk_bf16_f32 v46, v60, v61\n\t"
    "v_mul_f32_e32 v62, v62, v74\n\t"
    "v_mfma_f32_16x16x32_bf16 v[76:79], a[240:243], v[148:151], 0\n\t"
    "v_mul_f32_e32 v63, v63, v74\n\t"
    "v_cvt_pk_bf16_f32 v47, v62, v63\n\t"
    "v_mfma_f32_16x16x32_bf16 v[76:79], a[248:251], v[144:147], v[76:79]\n\t"
    "v_mul_f32_e32 v64, v64, v74\n\t"
    "v_mul_f32_e32 v65, v65, v74\n\t"
    "v_mfma_f32_16x16x32_bf16 v[76:79], v[224:227], v[140:143], v[76:79]\n\t"
    "v_cvt_pk_bf16_f32 v52, v64, v65\n\t"
    "v_mul_f32_e32 v66, v66, v74\n\t"
    "v_mfma_f32_16x16x32_bf16 v[76:79], v[232:235], v[136:139], v[76:79]\n\t"
    "v_cvt_pk_bf16_f32 v50, v80, v81\n\t"
    "v_cvt_pk_bf16_f32 v51, v82, v83\n\t"
    "v_mfma_f32_16x16x32_bf16 v[76:79], v[240:243], v[132:135], v[76:79]\n\t"
    "v_mul_f32_e32 v67, v67, v74\n\t"
    "v_cvt_pk_bf16_f32 v53, v66, v67\n\t"
    "v_mfma_f32_16x16x32_bf16 v[76:79], v[248:251], v[128:131], v[76:79]\n\t"
    "v_mul_f32_e32 v68, v68, v74\n\t"
    "v_mul_f32_e32 v69, v69, v74\n\t"
    "v_mfma_f32_16x16x32_bf16 v[56:59], v[252:255], v[128:131], 0\n\t"
    "v_cvt_pk_bf16_f32 v54, v68, v69\n\t"
    "v_mul_f32_e32 v70, v70, v74\n\t"
    "v_mfma_f32_16x16x32_bf16 v[56:59], v[244:247], v[132:135], v[56:59]\n\t"
    "v_mul_f32_e32 v71, v71, v74\n\t"
    "v_cvt_pk_bf16_f32 v55, v70, v71\n\t"
    "v_mfma_f32_16x16x32_bf16 v[56:59], v[236:239], v[136:139], v[56:59]\n\t"
    "s_sub_u32 s60, s60, 1\n\t"
    "s_cmp_lg_u32 s60, 0\n\t"
    "v_mfma_f32_16x16x32_bf16 v[56:59], v[228:231], v[140:143], v[56:59]\n\t"
    "v_cvt_pk_bf16_f32 v60, v76, v77\n\t"
    "v_cvt_pk_bf16_f32 v61, v78, v79\n\t"
    "v_mfma_f32_16x16x32_bf16 v[56:59], a[252:255], v[144:147], v[56:59]\n\t"
    "s_mov_b32 s63, s62\n\t"
    "s_mov_b32 s62, s64\n\t"
    "v_mfma_f32_16x16x32_bf16 v[56:59], a[244:247], v[148:151], v[56:59]\n\t"
    "s_cbranch_scc0 .Ltail_%=\n\t"
    "s_waitcnt lgkmcnt(0)\n\t"
    "v_mfma_f32_16x16x32_bf16 v[64:67], a[0:3], v[220:223], 0\n\t"
    "ds_write_b128 v6, v[44:47] offset:4096\n\t"
    "v_mfma_f32_16x16x32_bf16 v[64:67], a[8:11], v[216:219], v[64:67]\n\t"
    "v_mfma_f32_16x16x32_bf16 v[64:67], a[16:19], v[212:215], v[64:67]\n\t"
    "s_add_u32 m0, s63, 6144\n\t"
    "v_mfma_f32_16x16x32_bf16 v[64:67], a[24:27], v[28:31], v[64:67]\n\t"
    "buffer_load_dwordx4 v7, s[52:55], s61 offen offset:0 nt lds\n\t"
    "v_cvt_pk_bf16_f32 v62, v56, v57\n\t"
    "v_mfma_f32_16x16x32_bf16 v[64:67], a[32:35], v[32:35], v[64:67]\n\t"
    "ds_write_b128 v6, v[52:55] offset:5120\n\t"
    "v_mfma_f32_16x16x32_bf16 v[64:67], a[40:43], v[40:43], v[64:67]\n\t"
    "buffer_load_dwordx4 v7, s[52:55], s61 offen offset:1024 nt lds\n\t"
    "v_cvt_pk_bf16_f32 v63, v58, v59\n\t"
    "v_mfma_f32_16x16x32_bf16 v[12:15], v[208:211], v[36:39], v[12:15]\n\t"
    "v_mfma_f32_16x16x32_bf16 v[12:15], v[204:207], v[48:51], v[12:15]\n\t"
    "buffer_load_dwordx4 v7, s[52:55], s61 offen offset:2048 nt lds\n\t"
    "v_mfma_f32_16x16x32_bf16 v[12:15], v[200:203], v[60:63], v[12:15]\n\t"
    "s_add_u32 s61, s61, 196608\n\t"
    "v_mfma_f32_16x16x32_bf16 v[16:19], v[176:179], v[60:63], v[16:19]\n\t"
    "v_cvt_pk_bf16_f32 v44, v64, v65\n\t"
    "v_cvt_pk_bf16_f32 v45, v66, v67\n\t"
    "v_mfma_f32_16x16x32_bf16 v[16:19], v[180:183], v[48:51], v[16:19]\n\t"
    "s_waitcnt lgkmcnt(0)\n\t"
    "s_barrier\n\t"
    "v_mfma_f32_16x16x32_bf16 v[16:19], v[184:187], v[36:39], v[16:19]\n\t"
    "v_mfma_f32_16x16x32_bf16 v[20:23], v[160:163], v[36:39], v[20:23]\n\t"
    "v_max3_f32 v52, v12, v13, v14\n\t"
    "v_mfma_f32_16x16x32_bf16 v[20:23], v[156:159], v[48:51], v[20:23]\n\t"
    "v_mfma_f32_16x16x32_bf16 v[20:23], v[152:155], v[60:63], v[20:23]\n\t"
    "v_mfma_f32_16x16x32_bf16 v[24:27], v[128:131], v[60:63], v[24:27]\n\t"
    "v_max3_f32 v53, v15, v16, v17\n\t"
    "v_max3_f32 v52, v52, v18, v19\n\t"
    "v_mfma_f32_16x16x32_bf16 v[24:27], v[132:135], v[48:51], v[24:27]\n\t"
    "s_waitcnt vmcnt(9)\n\t"
    "v_mfma_f32_16x16x32_bf16 v[24:27], v[136:139], v[36:39], v[24:27]\n\t"
    "ds_read_b128 v[188:191], v2 offset:6144\n\t"
    "v_mfma_f32_16x16x32_bf16 v[56:59], a[44:47], v[40:43], 0\n\t"
    "v_max3_f32 v53, v53, v20, v21\n\t"
    "v_max3_f32 v52, v52, v22, v23\n\t"
    "v_mfma_f32_16x16x32_bf16 v[56:59], a[36:39], v[32:35], v[56:59]\n\t"
    "ds_read_b128 v[192:195], v1 offset:6144\n\t"
    "v_mfma_f32_16x16x32_bf16 v[56:59], a[28:31], v[28:31], v[56:59]\n\t"
    "v_mfma_f32_16x16x32_bf16 v[56:59], a[20:23], v[212:215], v[56:59]\n\t"
    "ds_read_b128 v[196:199], v0 offset:6144\n\t"
    "v_max3_f32 v53, v53, v24, v25\n\t"
    "v_mfma_f32_16x16x32_bf16 v[56:59], a[12:15], v[216:219], v[56:59]\n\t"
    "v_max3_f32 v52, v52, v26, v27\n\t"
    "v_max_f32_e32 v52, v52, v53\n\t"
    "v_mfma_f32_16x16x32_bf16 v[56:59], a[4:7], v[220:223], v[56:59]\n\t"
    "ds_read_b128 v[172:175], v0 offset:12288\n\t"
    "v_mov_b32_e32 v53, v52\n\t"
    "v_mfma_f32_16x16x32_bf16 v[36:39], a[48:51], v[220:223], 0\n\t"
    "v_mfma_f32_16x16x32_bf16 v[36:39], a[56:59], v[216:219], v[36:39]\n\t"
    "ds_read_b128 v[168:171], v1 offset:12288\n\t"
    "v_permlane32_swap_b32 v52, v53\n\t"
    "v_mfma_f32_16x16x32_bf16 v[36:39], a[64:67], v[212:215], v[36:39]\n\t"
    "v_max_f32_e32 v52, v52, v53\n\t"
    "v_mov_b32_e32 v53, v52\n\t"
    "v_mfma_f32_16x16x32_bf16 v[36:39], a[72:75], v[28:31], v[36:39]\n\t"
    "ds_read_b128 v[164:167], v2 offset:12288\n\t"
    "v_cvt_pk_bf16_f32 v46, v56, v57\n\t"
    "v_mfma_f32_16x16x32_bf16 v[36:39], a[80:83], v[32:35], v[36:39]\n\t"
    "v_cvt_pk_bf16_f32 v47, v58, v59\n\t"
    "v_permlane16_swap_b32 v52, v53\n\t"
    "v_mfma_f32_16x16x32_bf16 v[36:39], a[88:91], v[40:43], v[36:39]\n\t"
    "ds_read_b128 v[140:143], v2 offset:18432\n\t"
    "v_max_f32_e32 v52, v52, v53\n\t"
    "v_mfma_f32_16x16x32_bf16 v[48:51], a[92:95], v[40:43], 0\n\t"
    "v_sub_f32_e32 v12, v12, v52\n\t"
    "v_sub_f32_e32 v13, v13, v52\n\t"
    "v_mfma_f32_16x16x32_bf16 v[48:51], a[84:87], v[32:35], v[48:51]\n\t"
    "ds_read_b128 v[144:147], v1 offset:18432\n\t"
    "v_sub_f32_e32 v14, v14, v52\n\t"
    "v_mfma_f32_16x16x32_bf16 v[48:51], a[76:79], v[28:31], v[48:51]\n\t"
    "v_exp_f32_e32 v12, v12\n\t"
    "v_mfma_f32_16x16x32_bf16 v[48:51], a[68:71], v[212:215], v[48:51]\n\t"
    "ds_read_b128 v[148:151], v0 offset:18432\n\t"
    "v_cvt_pk_bf16_f32 v56, v36, v37\n\t"
    "v_mfma_f32_16x16x32_bf16 v[48:51], a[60:63], v[216:219], v[48:51]\n\t"
    "v_cvt_pk_bf16_f32 v57, v38, v39\n\t"
    "v_sub_f32_e32 v15, v15, v52\n\t"
    "v_mfma_f32_16x16x32_bf16 v[48:51], a[52:55], v[220:223], v[48:51]\n\t"
    "v_exp_f32_e32 v13, v13\n\t"
    "v_mfma_f32_16x16x32_bf16 v[60:63], a[96:99], v[220:223], 0\n\t"
    "v_add_f32_e32 v54, v12, v13\n\t"
    "v_sub_f32_e32 v16, v16, v52\n\t"
    "v_mfma_f32_16x16x32_bf16 v[60:63], a[104:107], v[216:219], v[60:63]\n\t"
    "v_exp_f32_e32 v14, v14\n\t"
    "v_mfma_f32_16x16x32_bf16 v[60:63], a[112:115], v[212:215], v[60:63]\n\t"
    "v_exp_f32_e32 v15, v15\n\t"
    "v_mfma_f32_16x16x32_bf16 v[60:63], a[120:123], v[28:31], v[60:63]\n\t"
    "v_cvt_pk_bf16_f32 v58, v48, v49\n\t"
    "v_cvt_pk_bf16_f32 v59, v50, v51\n\t"
    "v_mfma_f32_16x16x32_bf16 v[60:63], a[128:131], v[32:35], v[60:63]\n\t"
    "v_add_f32_e32 v55, v14, v15\n\t"
    "v_sub_f32_e32 v17, v17, v52\n\t"
    "v_mfma_f32_16x16x32_bf16 v[60:63], a[136:139], v[40:43], v[60:63]\n\t"
    "v_exp_f32_e32 v16, v16\n\t"
    "v_mfma_f32_16x16x32_bf16 v[36:39], a[140:143], v[40:43], 0\n\t"
    "v_add_f32_e32 v54, v54, v16\n\t"
    "v_sub_f32_e32 v18, v18, v52\n\t"
    "v_mfma_f32_16x16x32_bf16 v[36:39], a[132:135], v[32:35], v[36:39]\n\t"
    "v_exp_f32_e32 v17, v17\n\t"
    "v_mfma_f32_16x16x32_bf16 v[36:39], a[124:127], v[28:31], v[36:39]\n\t"
    "v_add_f32_e32 v54, v54, v17\n\t"
    "v_sub_f32_e32 v19, v19, v52\n\t"
    "v_mfma_f32_16x16x32_bf16 v[36:39], a[116:119], v[212:215], v[36:39]\n\t"
    "v_cvt_pk_bf16_f32 v48, v60, v61\n\t"
    "v_cvt_pk_bf16_f32 v49, v62, v63\n\t"
    "v_mfma_f32_16x16x32_bf16 v[36:39], a[108:111], v[216:219], v[36:39]\n\t"
    "v_exp_f32_e32 v18, v18\n\t"
    "v_mfma_f32_16x16x32_bf16 v[36:39], a[100:103], v[220:223], v[36:39]\n\t"
    "v_add_f32_e32 v55, v55, v18\n\t"
    "v_sub_f32_e32 v20, v20, v52\n\t"
    "v_mfma_f32_16x16x32_bf16 v[60:63], a[144:147], v[220:223], 0\n\t"
    "v_exp_f32_e32 v19, v19\n\t"
    "v_mfma_f32_16x16x32_bf16 v[60:63], a[152:155], v[216:219], v[60:63]\n\t"
    "v_add_f32_e32 v55, v55, v19\n\t"
    "v_exp_f32_e32 v20, v20\n\t"
    "v_mfma_f32_16x16x32_bf16 v[60:63], a[160:163], v[212:215], v[60:63]\n\t"
    "s_add_u32 m0, s63, 9216\n\t"
    "v_add_f32_e32 v54, v54, v20\n\t"
    "v_mfma_f32_16x16x32_bf16 v[60:63], a[168:171], v[28:31], v[60:63]\n\t"
    "buffer_load_dwordx4 v7, s[52:55], s61 offen offset:0 nt lds\n\t"
    "v_cvt_pk_bf16_f32 v50, v36, v37\n\t"
    "v_mfma_f32_16x16x32_bf16 v[60:63], a[176:179], v[32:35], v[60:63]\n\t"
    "v_cvt_pk_bf16_f32 v51, v38, v39\n\t"
    "v_sub_f32_e32 v21, v21, v52\n\t"
    "v_mfma_f32_16x16x32_bf16 v[60:63], a[184:187], v[40:43], v[60:63]\n\t"
    "buffer_load_dwordx4 v7, s[52:55], s61 offen offset:1024 nt lds\n\t"
    "v_sub_f32_e32 v22, v22, v52\n\t"
    "v_mfma_f32_16x16x32_bf16 v[64:67], v[220:223], v[44:47], 0\n\t"
    "v_exp_f32_e32 v21, v21\n\t"
    "v_mfma_f32_16x16x32_bf16 v[64:67], v[216:219], v[56:59], v[64:67]\n\t"
    "buffer_load_dwordx4 v7, s[52:55], s61 offen offset:2048 nt lds\n\t"
    "v_add_f32_e32 v54, v54, v21\n\t"
    "v_mfma_f32_16x16x32_bf16 v[64:67], v[212:215], v[48:51], v[64:67]\n\t"
    "s_add_u32 s61, s61, 196608\n\t"
    "s_waitcnt lgkmcnt(0)\n\t"
    "v_mfma_f32_16x16x32_bf16 v[36:39], v[188:191], v[48:51], 0\n\t"
    "v_exp_f32_e32 v22, v22\n\t"
    "v_mfma_f32_16x16x32_bf16 v[36:39], v[192:195], v[56:59], v[36:39]\n\t"
    "v_add_f32_e32 v55, v55, v22\n\t"
    "v_sub_f32_e32 v23, v23, v52\n\t"
    "v_mfma_f32_16x16x32_bf16 v[36:39], v[196:199], v[44:47], v[36:39]\n\t"
    "ds_read_b128 v[184:187], v3 offset:6144\n\t"
    "v_sub_f32_e32 v24, v24, v52\n\t"
    "v_mfma_f32_16x16x32_bf16 v[68:71], v[172:175], v[44:47], 0\n\t"
    "v_exp_f32_e32 v23, v23\n\t"
    "v_mfma_f32_16x16x32_bf16 v[68:71], v[168:171], v[56:59], v[68:71]\n\t"
    "ds_read_b128 v[180:183], v4 offset:6144\n\t"
    "v_add_f32_e32 v55, v55, v23\n\t"
    "v_mfma_f32_16x16x32_bf16 v[68:71], v[164:167], v[48:51], v[68:71]\n\t"
    "v_exp_f32_e32 v24, v24\n\t"
    "v_mfma_f32_16x16x32_bf16 v[72:75], v[140:143], v[48:51], 0\n\t"
    "ds_read_b128 v[176:179], v5 offset:6144\n\t"
    "v_add_f32_e32 v54, v54, v24\n\t"
    "v_mfma_f32_16x16x32_bf16 v[72:75], v[144:147], v[56:59], v[72:75]\n\t"
    "v_sub_f32_e32 v25, v25, v52\n\t"
    "v_sub_f32_e32 v26, v26, v52\n\t"
    "v_mfma_f32_16x16x32_bf16 v[72:75], v[148:151], v[44:47], v[72:75]\n\t"
    ".Lh5_%=:\n\t"
    "s_cbranch_execz .LrM_%=\n\t"
    "ds_read_b128 v[208:211], v3 offset:0\n\t"
    "v_sub_f32_e32 v27, v27, v52\n\t"
    "v_mfma_f32_16x16x32_bf16 v[48:51], a[188:191], v[40:43], 0\n\t"
    "v_exp_f32_e32 v25, v25\n\t"
    "v_mfma_f32_16x16x32_bf16 v[48:51], a[180:183], v[32:35], v[48:51]\n\t"
    "ds_read_b128 v[204:207], v4 offset:0\n\t"
    "v_add_f32_e32 v54, v54, v25\n\t"
    "v_mfma_f32_16x16x32_bf16 v[48:51], a[172:175], v[28:31], v[48:51]\n\t"
    "v_exp_f32_e32 v26, v26\n\t"
    "v_mfma_f32_16x16x32_bf16 v[48:51], a[164:167], v[212:215], v[48:51]\n\t"
    "ds_read_b128 v[200:203], v5 offset:0\n\t"
    "v_add_f32_e32 v55, v55, v26\n\t"
    "v_mfma_f32_16x16x32_bf16 v[48:51], a[156:159], v[216:219], v[48:51]\n\t"
    "v_exp_f32_e32 v27, v27\n\t"
    "v_mfma_f32_16x16x32_bf16 v[48:51], a[148:151], v[220:223], v[48:51]\n\t"
    "ds_read_b128 v[160:163], v3 offset:12288\n\t"
    "v_add_f32_e32 v55, v55, v27\n\t"
    "v_mfma_f32_16x16x32_bf16 v[44:47], a[192:195], v[220:223], 0\n\t"
    "v_add_f32_e32 v54, v54, v55\n\t"
    "v_mov_b32_e32 v55, v54\n\t"
    "v_mfma_f32_16x16x32_bf16 v[44:47], a[200:203], v[216:219], v[44:47]\n\t"
    "ds_read_b128 v[156:159], v4 offset:12288\n\t"
    "v_cvt_pk_bf16_f32 v56, v60, v61\n\t"
    "v_mfma_f32_16x16x32_bf16 v[44:47], a[208:211], v[212:215], v[44:47]\n\t"
    "v_permlane32_swap_b32 v54, v55\n\t"
    "v_add_f32_e32 v54, v54, v55\n\t"
    "v_mfma_f32_16x16x32_bf16 v[44:47], a[216:219], v[28:31], v[44:47]\n\t"
    "ds_read_b128 v[152:155], v5 offset:12288\n\t"
    "v_cvt_pk_bf16_f32 v58, v48, v49\n\t"
    "v_mfma_f32_16x16x32_bf16 v[44:47], a[224:227], v[32:35], v[44:47]\n\t"
    "v_cvt_pk_bf16_f32 v59, v50, v51\n\t"
    "v_mov_b32_e32 v55, v54\n\t"
    "v_mfma_f32_16x16x32_bf16 v[44:47], a[232:235], v[40:43], v[44:47]\n\t"
    "ds_read_b128 v[128:131], v5 offset:18432\n\t"
    "v_cvt_pk_bf16_f32 v57, v62, v63\n\t"
    "v_mfma_f32_16x16x32_bf16 v[76:79], a[236:239], v[40:43], 0\n\t"
    "v_permlane16_swap_b32 v54, v55\n\t"
    "v_add_f32_e32 v54, v54, v55\n\t"
    "v_mfma_f32_16x16x32_bf16 v[76:79], a[228:231], v[32:35], v[76:79]\n\t"
    "ds_read_b128 v[132:135], v4 offset:18432\n\t"
    "v_mfma_f32_16x16x32_bf16 v[76:79], a[220:223], v[28:31], v[76:79]\n\t"
    "v_rcp_f32_e32 v54, v54\n\t"
    "v_mfma_f32_16x16x32_bf16 v[76:79], a[212:215], v[212:215], v[76:79]\n\t"
    "ds_read_b128 v[136:139], v3 offset:18432\n\t"
    "v_cvt_pk_bf16_f32 v48, v44, v45\n\t"
    "v_mfma_f32_16x16x32_bf16 v[76:79], a[204:207], v[216:219], v[76:79]\n\t"
    "v_cvt_pk_bf16_f32 v49, v46, v47\n\t"
    "v_mul_f32_e32 v12, v12, v54\n\t"
    "v_mfma_f32_16x16x32_bf16 v[76:79], a[196:199], v[220:223], v[76:79]\n\t"
    "v_mul_f32_e32 v13, v13, v54\n\t"
    "v_cvt_pk_bf16_f32 v60, v12, v13\n\t"
    "v_mfma_f32_16x16x32_bf16 v[80:83], a[240:243], v[220:223], 0\n\t"
    "v_mul_f32_e32 v14, v14, v54\n\t"
    "v_mul_f32_e32 v15, v15, v54\n\t"
    "v_mfma_f32_16x16x32_bf16 v[80:83], a[248:251], v[216:219], v[80:83]\n\t"
    "v_cvt_pk_bf16_f32 v61, v14, v15\n\t"
    "v_mul_f32_e32 v16, v16, v54\n\t"
    "v_mfma_f32_16x16x32_bf16 v[80:83], v[224:227], v[212:215], v[80:83]\n\t"
    "v_mul_f32_e32 v17, v17, v54\n\t"
    "v_cvt_pk_bf16_f32 v62, v16, v17\n\t"
    "v_mfma_f32_16x16x32_bf16 v[80:83], v[232:235], v[28:31], v[80:83]\n\t"
    "v_cvt_pk_bf16_f32 v50, v76, v77\n\t"
    "v_cvt_pk_bf16_f32 v51, v78, v79\n\t"
    "v_mfma_f32_16x16x32_bf16 v[80:83], v[240:243], v[32:35], v[80:83]\n\t"
    "v_mul_f32_e32 v18, v18, v54\n\t"
    "v_mul_f32_e32 v19, v19, v54\n\t"
    "v_mfma_f32_16x16x32_bf16 v[80:83], v[248:251], v[40:43], v[80:83]\n\t"
    "v_cvt_pk_bf16_f32 v63, v18, v19\n\t"
    "v_mul_f32_e32 v20, v20, v54\n\t"
    "v_mfma_f32_16x16x32_bf16 v[12:15], v[252:255], v[40:43], 0\n\t"
    "v_mul_f32_e32 v21, v21, v54\n\t"
    "v_cvt_pk_bf16_f32 v44, v20, v21\n\t"
    "v_mfma_f32_16x16x32_bf16 v[12:15], v[244:247], v[32:35], v[12:15]\n\t"
    "v_mul_f32_e32 v22, v22, v54\n\t"
    "v_mul_f32_e32 v23, v23, v54\n\t"
    "v_mfma_f32_16x16x32_bf16 v[12:15], v[236:239], v[28:31], v[12:15]\n\t"
    "v_cvt_pk_bf16_f32 v45, v22, v23\n\t"
    "v_mul_f32_e32 v24, v24, v54\n\t"
    "v_mfma_f32_16x16x32_bf16 v[12:15], v[228:231], v[212:215], v[12:15]\n\t"
    "v_cvt_pk_bf16_f32 v16, v80, v81\n\t"
    "v_cvt_pk_bf16_f32 v17, v82, v83\n\t"
    "v_mfma_f32_16x16x32_bf16 v[12:15], a[252:255], v[216:219], v[12:15]\n\t"
    "v_mul_f32_e32 v25, v25, v54\n\t"
    "v_cvt_pk_bf16_f32 v46, v24, v25\n\t"
    "v_mfma_f32_16x16x32_bf16 v[12:15], a[244:247], v[220:223], v[12:15]\n\t"
    "v_mul_f32_e32 v26, v26, v54\n\t"
    "v_mul_f32_e32 v27, v27, v54\n\t"
    "v_mfma_f32_16x16x32_bf16 v[20:23], a[0:3], v[196:199], 0\n\t"
    "ds_write_b128 v6, v[60:63] offset:6144\n\t"
    "v_mfma_f32_16x16x32_bf16 v[20:23], a[8:11], v[192:195], v[20:23]\n\t"
    "v_cvt_pk_bf16_f32 v47, v26, v27\n\t"
    "v_mfma_f32_16x16x32_bf16 v[20:23], a[16:19], v[188:191], v[20:23]\n\t"
    "s_add_u32 m0, s63, 12288\n\t"
    "s_waitcnt lgkmcnt(1)\n\t"
    "v_mfma_f32_16x16x32_bf16 v[20:23], a[24:27], v[184:187], v[20:23]\n\t"
    "buffer_load_dwordx4 v7, s[52:55], s61 offen offset:0 nt lds\n\t"
    "v_cvt_pk_bf16_f32 v18, v12, v13\n\t"
    "v_mfma_f32_16x16x32_bf16 v[20:23], a[32:35], v[180:183], v[20:23]\n\t"
    "ds_write_b128 v6, v[44:47] offset:7168\n\t"
    "v_mfma_f32_16x16x32_bf16 v[20:23], a[40:43], v[176:179], v[20:23]\n\t"
    "buffer_load_dwordx4 v7, s[52:55], s61 offen offset:1024 nt lds\n\t"
    "v_cvt_pk_bf16_f32 v19, v14, v15\n\t"
    "v_mfma_f32_16x16x32_bf16 v[64:67], v[208:211], v[56:59], v[64:67]\n\t"
    "v_mfma_f32_16x16x32_bf16 v[64:67], v[204:207], v[48:51], v[64:67]\n\t"
    "buffer_load_dwordx4 v7, s[52:55], s61 offen offset:2048 nt lds\n\t"
    "v_mfma_f32_16x16x32_bf16 v[64:67], v[200:203], v[16:19], v[64:67]\n\t"
    "s_add_u32 s61, s61, 196608\n\t"
    "v_mfma_f32_16x16x32_bf16 v[36:39], v[176:179], v[16:19], v[36:39]\n\t"
    "v_cvt_pk_bf16_f32 v12, v20, v21\n\t"
    "v_cvt_pk_bf16_f32 v13, v22, v23\n\t"
    "v_mfma_f32_16x16x32_bf16 v[36:39], v[180:183], v[48:51], v[36:39]\n\t"
    "s_waitcnt lgkmcnt(0)\n\t"
    "s_barrier\n\t"
    "v_mfma_f32_16x16x32_bf16 v[36:39], v[184:187], v[56:59], v[36:39]\n\t"
    "v_mfma_f32_16x16x32_bf16 v[68:71], v[160:163], v[56:59], v[68:71]\n\t"
    "v_max3_f32 v20, v64, v65, v66\n\t"
    "v_mfma_f32_16x16x32_bf16 v[68:71], v[156:159], v[48:51], v[68:71]\n\t"
    "v_mfma_f32_16x16x32_bf16 v[68:71], v[152:155], v[16:19], v[68:71]\n\t"
    "v_mfma_f32_16x16x32_bf16 v[72:75], v[128:131], v[16:19], v[72:75]\n\t"
    "v_max3_f32 v21, v67, v36, v37\n\t"
    "v_max3_f32 v20, v20, v38, v39\n\t"
    "v_mfma_f32_16x16x32_bf16 v[72:75], v[132:135], v[48:51], v[72:75]\n\t"
    "v_mfma_f32_16x16x32_bf16 v[72:75], v[136:139], v[56:59], v[72:75]\n\t"
    "v_mfma_f32_16x16x32_bf16 v[16:19], a[44:47], v[176:179], 0\n\t"
    "v_max3_f32 v21, v21, v68, v69\n\t"
    "v_max3_f32 v20, v20, v70, v71\n\t"
    "v_mfma_f32_16x16x32_bf16 v[16:19], a[36:39], v[180:183], v[16:19]\n\t"
    "v_mfma_f32_16x16x32_bf16 v[16:19], a[28:31], v[184:187], v[16:19]\n\t"
    "v_mfma_f32_16x16x32_bf16 v[16:19], a[20:23], v[188:191], v[16:19]\n\t"
    "v_max3_f32 v21, v21, v72, v73\n\t"
    "v_max3_f32 v20, v20, v74, v75\n\t"
    "v_mfma_f32_16x16x32_bf16 v[16:19], a[12:15], v[192:195], v[16:19]\n\t"
    "v_max_f32_e32 v20, v20, v21\n\t"
    "v_mov_b32_e32 v21, v20\n\t"
    "v_mfma_f32_16x16x32_bf16 v[16:19], a[4:7], v[196:199], v[16:19]\n\t"
    "v_mfma_f32_16x16x32_bf16 v[24:27], a[48:51], v[196:199], 0\n\t"
    "v_permlane32_swap_b32 v20, v21\n\t"
    "v_max_f32_e32 v20, v20, v21\n\t"
    "v_mfma_f32_16x16x32_bf16 v[24:27], a[56:59], v[192:195], v[24:27]\n\t"
    "v_mov_b32_e32 v21, v20\n\t"
    "v_mfma_f32_16x16x32_bf16 v[24:27], a[64:67], v[188:191], v[24:27]\n\t"
    "v_mfma_f32_16x16x32_bf16 v[24:27], a[72:75], v[184:187], v[24:27]\n\t"
    "v_cvt_pk_bf16_f32 v14, v16, v17\n\t"
    "v_cvt_pk_bf16_f32 v15, v18, v19\n\t"
    "v_mfma_f32_16x16x32_bf16 v[24:27], a[80:83], v[180:183], v[24:27]\n\t"
    "v_permlane16_swap_b32 v20, v21\n\t"
    "v_max_f32_e32 v20, v20, v21\n\t"
    "v_mfma_f32_16x16x32_bf16 v[24:27], a[88:91], v[176:179], v[24:27]\n\t"
    "v_sub_f32_e32 v64, v64, v20\n\t"
    "v_sub_f32_e32 v65, v65, v20\n\t"
    "v_mfma_f32_16x16x32_bf16 v[16:19], a[92:95], v[176:179], 0\n\t"
    "v_exp_f32_e32 v64, v64\n\t"
    "v_mfma_f32_16x16x32_bf16 v[16:19], a[84:87], v[180:183], v[16:19]\n\t"
    "v_exp_f32_e32 v65, v65\n\t"
    "v_mfma_f32_16x16x32_bf16 v[16:19], a[76:79], v[184:187], v[16:19]\n\t"
    "v_add_f32_e32 v22, v64, v65\n\t"
    "v_sub_f32_e32 v66, v66, v20\n\t"
    "v_mfma_f32_16x16x32_bf16 v[16:19], a[68:71], v[188:191], v[16:19]\n\t"
    "v_cvt_pk_bf16_f32 v28, v24, v25\n\t"
    "v_cvt_pk_bf16_f32 v29, v26, v27\n\t"
    "v_mfma_f32_16x16x32_bf16 v[16:19], a[60:63], v[192:195], v[16:19]\n\t"
    "v_exp_f32_e32 v66, v66\n\t"
    "v_mfma_f32_16x16x32_bf16 v[16:19], a[52:55], v[196:199], v[16:19]\n\t"
    "v_sub_f32_e32 v67, v67, v20\n\t"
    "v_sub_f32_e32 v36, v36, v20\n\t"
    "v_mfma_f32_16x16x32_bf16 v[24:27], a[96:99], v[196:199], 0\n\t"
    "v_exp_f32_e32 v67, v67\n\t"
    "v_mfma_f32_16x16x32_bf16 v[24:27], a[104:107], v[192:195], v[24:27]\n\t"
    "v_add_f32_e32 v23, v66, v67\n\t"
    "v_sub_f32_e32 v37, v37, v20\n\t"
    "v_mfma_f32_16x16x32_bf16 v[24:27], a[112:115], v[188:191], v[24:27]\n\t"
    "v_exp_f32_e32 v36, v36\n\t"
    "v_mfma_f32_16x16x32_bf16 v[24:27], a[120:123], v[184:187], v[24:27]\n\t"
    "v_cvt_pk_bf16_f32 v30, v16, v17\n\t"
    "v_cvt_pk_bf16_f32 v31, v18, v19\n\t"
    "v_mfma_f32_16x16x32_bf16 v[24:27], a[128:131], v[180:183], v[24:27]\n\t"
    "v_add_f32_e32 v22, v22, v36\n\t"
    "v_sub_f32_e32 v38, v38, v20\n\t"
    "v_mfma_f32_16x16x32_bf16 v[24:27], a[136:139], v[176:179], v[24:27]\n\t"
    "v_exp_f32_e32 v37, v37\n\t"
    "v_mfma_f32_16x16x32_bf16 v[16:19], a[140:143], v[176:179], 0\n\t"
    "v_add_f32_e32 v22, v22, v37\n\t"
    "v_sub_f32_e32 v39, v39, v20\n\t"
    "v_mfma_f32_16x16x32_bf16 v[16:19], a[132:135], v[180:183], v[16:19]\n\t"
    "v_exp_f32_e32 v38, v38\n\t"
    "v_mfma_f32_16x16x32_bf16 v[16:19], a[124:127], v[184:187], v[16:19]\n\t"
    "v_add_f32_e32 v23, v23, v38\n\t"
    "v_sub_f32_e32 v68, v68, v20\n\t"
    "v_mfma_f32_16x16x32_bf16 v[16:19], a[116:119], v[188:191], v[16:19]\n\t"
    "v_cvt_pk_bf16_f32 v32, v24, v25\n\t"
    "v_cvt_pk_bf16_f32 v33, v26, v27\n\t"
    "v_mfma_f32_16x16x32_bf16 v[16:19], a[108:111], v[192:195], v[16:19]\n\t"
    "v_exp_f32_e32 v39, v39\n\t"
    "v_mfma_f32_16x16x32_bf16 v[16:19], a[100:103], v[196:199], v[16:19]\n\t"
    "v_add_f32_e32 v23, v23, v39\n\t"
    "v_sub_f32_e32 v69, v69, v20\n\t"
    "v_mfma_f32_16x16x32_bf16 v[24:27], a[144:147], v[196:199], 0\n\t"
    "v_exp_f32_e32 v68, v68\n\t"
    "v_mfma_f32_16x16x32_bf16 v[24:27], a[152:155], v[192:195], v[24:27]\n\t"
    "v_add_f32_e32 v22, v22, v68\n\t"
    "v_sub_f32_e32 v70, v70, v20\n\t"
    "v_mfma_f32_16x16x32_bf16 v[24:27], a[160:163], v[188:191], v[24:27]\n\t"
    "s_add_u32 m0, s63, 15360\n\t"
    "v_sub_f32_e32 v71, v71, v20\n\t"
    "v_mfma_f32_16x16x32_bf16 v[24:27], a[168:171], v[184:187], v[24:27]\n\t"
    "buffer_load_dwordx4 v7, s[52:55], s61 offen offset:0 nt lds\n\t"
    "v_cvt_pk_bf16_f32 v34, v16, v17\n\t"
    "v_mfma_f32_16x16x32_bf16 v[24:27], a[176:179], v[180:183], v[24:27]\n\t"
    "v_cvt_pk_bf16_f32 v35, v18, v19\n\t"
    "v_sub_f32_e32 v72, v72, v20\n\t"
    "v_mfma_f32_16x16x32_bf16 v[24:27], a[184:187], v[176:179], v[24:27]\n\t"
    "buffer_load_dwordx4 v7, s[52:55], s61 offen offset:1024 nt lds\n\t"
    "v_sub_f32_e32 v73, v73, v20\n\t"
    "v_mfma_f32_16x16x32_bf16 v[40:43], v[220:223], v[12:15], 0\n\t"
    "v_exp_f32_e32 v69, v69\n\t"
    "v_mfma_f32_16x16x32_bf16 v[40:43], v[216:219], v[28:31], v[40:43]\n\t"
    "buffer_load_dwordx4 v7, s[52:55], s61 offen offset:2048 nt lds\n\t"
    "v_add_f32_e32 v22, v22, v69\n\t"
    "v_mfma_f32_16x16x32_bf16 v[40:43], v[212:215], v[32:35], v[40:43]\n\t"
    "s_add_u32 s61, s61, 196608\n\t"
    "v_sub_f32_e32 v74, v74, v20\n\t"
    "v_mfma_f32_16x16x32_bf16 v[16:19], v[188:191], v[32:35], 0\n\t"
    "v_exp_f32_e32 v70, v70\n\t"
    "v_mfma_f32_16x16x32_bf16 v[16:19], v[192:195], v[28:31], v[16:19]\n\t"
    "v_add_f32_e32 v23, v23, v70\n\t"
    "v_sub_f32_e32 v75, v75, v20\n\t"
    "v_mfma_f32_16x16x32_bf16 v[16:19], v[196:199], v[12:15], v[16:19]\n\t"
    "v_exp_f32_e32 v71, v71\n\t"
    "v_mfma_f32_16x16x32_bf16 v[44:47], v[172:175], v[12:15], 0\n\t"
    "v_add_f32_e32 v23, v23, v71\n\t"
    "v_cvt_pk_bf16_f32 v48, v24, v25\n\t"
    "v_mfma_f32_16x16x32_bf16 v[44:47], v[168:171], v[28:31], v[44:47]\n\t"
    "v_exp_f32_e32 v72, v72\n\t"
    "v_mfma_f32_16x16x32_bf16 v[44:47], v[164:167], v[32:35], v[44:47]\n\t"
    "v_add_f32_e32 v22, v22, v72\n\t"
    "v_cvt_pk_bf16_f32 v49, v26, v27\n\t"
    "v_mfma_f32_16x16x32_bf16 v[52:55], v[140:143], v[32:35], 0\n\t"
    "v_exp_f32_e32 v73, v73\n\t"
    "v_mfma_f32_16x16x32_bf16 v[52:55], v[144:147], v[28:31], v[52:55]\n\t"
    "v_add_f32_e32 v22, v22, v73\n\t"
    "v_mfma_f32_16x16x32_bf16 v[52:55], v[148:151], v[12:15], v[52:55]\n\t"
    "v_exp_f32_e32 v74, v74\n\t"
    "v_mfma_f32_16x16x32_bf16 v[24:27], a[188:191], v[176:179], 0\n\t"
    "v_add_f32_e32 v23, v23, v74\n\t"
    "v_mfma_f32_16x16x32_bf16 v[24:27], a[180:183], v[180:183], v[24:27]\n\t"
    "v_exp_f32_e32 v75, v75\n\t"
    "v_mfma_f32_16x16x32_bf16 v[24:27], a[172:175], v[184:187], v[24:27]\n\t"
    "v_add_f32_e32 v23, v23, v75\n\t"
    "v_add_f32_e32 v22, v22, v23\n\t"
    "v_mfma_f32_16x16x32_bf16 v[24:27], a[164:167], v[188:191], v[24:27]\n\t"
    "v_mov_b32_e32 v23, v22\n\t"
    "v_mfma_f32_16x16x32_bf16 v[24:27], a[156:159], v[192:195], v[24:27]\n\t"
    "v_mfma_f32_16x16x32_bf16 v[24:27], a[148:151], v[196:199], v[24:27]\n\t"
    "v_permlane32_swap_b32 v22, v23\n\t"
    "v_add_f32_e32 v22, v22, v23\n\t"
    "v_mfma_f32_16x16x32_bf16 v[12:15], a[192:195], v[196:199], 0\n\t"
    "v_mov_b32_e32 v23, v22\n\t"
    "v_mfma_f32_16x16x32_bf16 v[12:15], a[200:203], v[192:195], v[12:15]\n\t"
    "v_mfma_f32_16x16x32_bf16 v[12:15], a[208:211], v[188:191], v[12:15]\n\t"
    "v_permlane16_swap_b32 v22, v23\n\t"
    "v_add_f32_e32 v22, v22, v23\n\t"
    "v_mfma_f32_16x16x32_bf16 v[12:15], a[216:219], v[184:187], v[12:15]\n\t"
    "v_cvt_pk_bf16_f32 v50, v24, v25\n\t"
    "v_cvt_pk_bf16_f32 v51, v26, v27\n\t"
    "v_mfma_f32_16x16x32_bf16 v[12:15], a[224:227], v[180:183], v[12:15]\n\t"
    "v_rcp_f32_e32 v22, v22\n\t"
    "v_mfma_f32_16x16x32_bf16 v[12:15], a[232:235], v[176:179], v[12:15]\n\t"
    "v_mul_f32_e32 v64, v64, v22\n\t"
    "v_mul_f32_e32 v65, v65, v22\n\t"
    "v_mfma_f32_16x16x32_bf16 v[28:31], a[236:239], v[176:179], 0\n\t"
    "v_cvt_pk_bf16_f32 v24, v64, v65\n\t"
    "v_mul_f32_e32 v66, v66, v22\n\t"
    "v_mfma_f32_16x16x32_bf16 v[28:31], a[228:231], v[180:183], v[28:31]\n\t"
    "v_mul_f32_e32 v67, v67, v22\n\t"
    "v_cvt_pk_bf16_f32 v25, v66, v67\n\t"
    "v_mfma_f32_16x16x32_bf16 v[28:31], a[220:223], v[184:187], v[28:31]\n\t"
    "v_mul_f32_e32 v36, v36, v22\n\t"
    "v_mul_f32_e32 v37, v37, v22\n\t"
    "v_mfma_f32_16x16x32_bf16 v[28:31], a[212:215], v[188:191], v[28:31]\n\t"
    "v_cvt_pk_bf16_f32 v32, v12, v13\n\t"
    "v_cvt_pk_bf16_f32 v33, v14, v15\n\t"
    "v_mfma_f32_16x16x32_bf16 v[28:31], a[204:207], v[192:195], v[28:31]\n\t"
    "v_cvt_pk_bf16_f32 v26, v36, v37\n\t"
    "v_mul_f32_e32 v38, v38, v22\n\t"
    "v_mfma_f32_16x16x32_bf16 v[28:31], a[196:199], v[196:199], v[28:31]\n\t"
    "v_mul_f32_e32 v39, v39, v22\n\t"
    "v_cvt_pk_bf16_f32 v27, v38, v39\n\t"
    "v_mfma_f32_16x16x32_bf16 v[12:15], a[240:243], v[196:199], 0\n\t"
    "v_mul_f32_e32 v68, v68, v22\n\t"
    "v_mul_f32_e32 v69, v69, v22\n\t"
    "v_mfma_f32_16x16x32_bf16 v[12:15], a[248:251], v[192:195], v[12:15]\n\t"
    "v_cvt_pk_bf16_f32 v56, v68, v69\n\t"
    "v_mul_f32_e32 v70, v70, v22\n\t"
    "v_mfma_f32_16x16x32_bf16 v[12:15], v[224:227], v[188:191], v[12:15]\n\t"
    "v_mul_f32_e32 v71, v71, v22\n\t"
    "v_cvt_pk_bf16_f32 v57, v70, v71\n\t"
    "v_mfma_f32_16x16x32_bf16 v[12:15], v[232:235], v[184:187], v[12:15]\n\t"
    "v_cvt_pk_bf16_f32 v34, v28, v29\n\t"
    "v_cvt_pk_bf16_f32 v35, v30, v31\n\t"
    "v_mfma_f32_16x16x32_bf16 v[12:15], v[240:243], v[180:183], v[12:15]\n\t"
    "v_mul_f32_e32 v72, v72, v22\n\t"
    "v_mul_f32_e32 v73, v73, v22\n\t"
    "v_mfma_f32_16x16x32_bf16 v[12:15], v[248:251], v[176:179], v[12:15]\n\t"
    "v_cvt_pk_bf16_f32 v58, v72, v73\n\t"
    "v_mul_f32_e32 v74, v74, v22\n\t"
    "v_mfma_f32_16x16x32_bf16 v[28:31], v[252:255], v[176:179], 0\n\t"
    "v_mul_f32_e32 v75, v75, v22\n\t"
    "v_cvt_pk_bf16_f32 v59, v74, v75\n\t"
    "v_mfma_f32_16x16x32_bf16 v[28:31], v[244:247], v[180:183], v[28:31]\n\t"
    "v_mfma_f32_16x16x32_bf16 v[28:31], v[236:239], v[184:187], v[28:31]\n\t"
    "v_mfma_f32_16x16x32_bf16 v[28:31], v[228:231], v[188:191], v[28:31]\n\t"
    "v_cvt_pk_bf16_f32 v20, v12, v13\n\t"
    "v_cvt_pk_bf16_f32 v21, v14, v15\n\t"
    "v_mfma_f32_16x16x32_bf16 v[28:31], a[252:255], v[192:195], v[28:31]\n\t"
    "v_mfma_f32_16x16x32_bf16 v[28:31], a[244:247], v[196:199], v[28:31]\n\t"
    "v_mfma_f32_16x16x32_bf16 v[12:15], a[0:3], v[172:175], 0\n\t"
    "ds_write_b128 v6, v[24:27] offset:0\n\t"
    "v_mfma_f32_16x16x32_bf16 v[12:15], a[8:11], v[168:171], v[12:15]\n\t"
    "v_mfma_f32_16x16x32_bf16 v[12:15], a[16:19], v[164:167], v[12:15]\n\t"
    "s_add_u32 m0, s63, 18432\n\t"
    "v_mfma_f32_16x16x32_bf16 v[12:15], a[24:27], v[160:163], v[12:15]\n\t"
    "buffer_load_dwordx4 v7, s[52:55], s61 offen offset:0 nt lds\n\t"
    "v_cvt_pk_bf16_f32 v22, v28, v29\n\t"
    "v_mfma_f32_16x16x32_bf16 v[12:15], a[32:35], v[156:159], v[12:15]\n\t"
    "ds_write_b128 v6, v[56:59] offset:1024\n\t"
    "v_mfma_f32_16x16x32_bf16 v[12:15], a[40:43], v[152:155], v[12:15]\n\t"
    "buffer_load_dwordx4 v7, s[52:55], s61 offen offset:1024 nt lds\n\t"
    "v_cvt_pk_bf16_f32 v23, v30, v31\n\t"
    "v_mfma_f32_16x16x32_bf16 v[40:43], v[208:211], v[48:51], v[40:43]\n\t"
    "v_mfma_f32_16x16x32_bf16 v[40:43], v[204:207], v[32:35], v[40:43]\n\t"
    "buffer_load_dwordx4 v7, s[52:55], s61 offen offset:2048 nt lds\n\t"
    "v_mfma_f32_16x16x32_bf16 v[40:43], v[200:203], v[20:23], v[40:43]\n\t"
    "s_add_u32 s61, s61, 196608\n\t"
    "s_add_i32 s64, s62, 24576\n\t"
    "v_mfma_f32_16x16x32_bf16 v[16:19], v[176:179], v[20:23], v[16:19]\n\t"
    "s_cmp_eq_u32 s64, %[rend]\n\t"
    "s_cselect_b32 s64, %[rbeg], s64\n\t"
    "v_mfma_f32_16x16x32_bf16 v[16:19], v[180:183], v[32:35], v[16:19]\n\t"
    "s_sub_i32 s65, s63, s64\n\t"
    "v_cvt_pk_bf16_f32 v24, v12, v13\n\t"
    "v_mfma_f32_16x16x32_bf16 v[16:19], v[184:187], v[48:51], v[16:19]\n\t"
    "v_add_u32_e32 v0, s65, v0\n\t"
    "v_add_u32_e32 v1, s65, v1\n\t"
    "v_mfma_f32_16x16x32_bf16 v[44:47], v[160:163], v[48:51], v[44:47]\n\t"
    "v_add_u32_e32 v2, s65, v2\n\t"
    "v_add_u32_e32 v3, s65, v3\n\t"
    "v_mfma_f32_16x16x32_bf16 v[44:47], v[156:159], v[32:35], v[44:47]\n\t"
    "v_add_u32_e32 v4, s65, v4\n\t"
    "v_add_u32_e32 v5, s65, v5\n\t"
    "v_mfma_f32_16x16x32_bf16 v[44:47], v[152:155], v[20:23], v[44:47]\n\t"
    "v_max3_f32 v28, v40, v41, v42\n\t"
    "v_cvt_pk_bf16_f32 v25, v14, v15\n\t"
    "v_mfma_f32_16x16x32_bf16 v[52:55], v[128:131], v[20:23], v[52:55]\n\t"
    "v_max3_f32 v29, v43, v16, v17\n\t"
    "v_max3_f32 v28, v28, v18, v19\n\t"
    "v_mfma_f32_16x16x32_bf16 v[52:55], v[132:135], v[32:35], v[52:55]\n\t"
    "v_mfma_f32_16x16x32_bf16 v[52:55], v[136:139], v[48:51], v[52:55]\n\t"
    "v_mfma_f32_16x16x32_bf16 v[12:15], a[44:47], v[152:155], 0\n\t"
    "v_max3_f32 v29, v29, v44, v45\n\t"
    "v_max3_f32 v28, v28, v46, v47\n\t"
    "v_mfma_f32_16x16x32_bf16 v[12:15], a[36:39], v[156:159], v[12:15]\n\t"
    "v_mfma_f32_16x16x32_bf16 v[12:15], a[28:31], v[160:163], v[12:15]\n\t"
    "v_mfma_f32_16x16x32_bf16 v[12:15], a[20:23], v[164:167], v[12:15]\n\t"
    "v_max3_f32 v29, v29, v52, v53\n\t"
    "v_max3_f32 v28, v28, v54, v55\n\t"
    "v_mfma_f32_16x16x32_bf16 v[12:15], a[12:15], v[168:171], v[12:15]\n\t"
    "v_max_f32_e32 v28, v28, v29\n\t"
    "v_mov_b32_e32 v29, v28\n\t"
    "v_mfma_f32_16x16x32_bf16 v[12:15], a[4:7], v[172:175], v[12:15]\n\t"
    "v_mfma_f32_16x16x32_bf16 v[20:23], a[48:51], v[172:175], 0\n\t"
    "v_permlane32_swap_b32 v28, v29\n\t"
    "v_max_f32_e32 v28, v28, v29\n\t"
    "v_mfma_f32_16x16x32_bf16 v[20:23], a[56:59], v[168:171], v[20:23]\n\t"
    "v_mov_b32_e32 v29, v28\n\t"
    "v_mfma_f32_16x16x32_bf16 v[20:23], a[64:67], v[164:167], v[20:23]\n\t"
    "v_mfma_f32_16x16x32_bf16 v[20:23], a[72:75], v[160:163], v[20:23]\n\t"
    "v_cvt_pk_bf16_f32 v26, v12, v13\n\t"
    "v_cvt_pk_bf16_f32 v27, v14, v15\n\t"
    "v_mfma_f32_16x16x32_bf16 v[20:23], a[80:83], v[156:159], v[20:23]\n\t"
    "v_permlane16_swap_b32 v28, v29\n\t"
    "v_max_f32_e32 v28, v28, v29\n\t"
    "v_mfma_f32_16x16x32_bf16 v[20:23], a[88:91], v[152:155], v[20:23]\n\t"
    "v_sub_f32_e32 v40, v40, v28\n\t"
    "v_sub_f32_e32 v41, v41, v28\n\t"
    "v_mfma_f32_16x16x32_bf16 v[12:15], a[92:95], v[152:155], 0\n\t"
    "v_exp_f32_e32 v40, v40\n\t"
    "v_mfma_f32_16x16x32_bf16 v[12:15], a[84:87], v[156:159], v[12:15]\n\t"
    "v_exp_f32_e32 v41, v41\n\t"
    "v_mfma_f32_16x16x32_bf16 v[12:15], a[76:79], v[160:163], v[12:15]\n\t"
    "v_add_f32_e32 v30, v40, v41\n\t"
    "v_sub_f32_e32 v42, v42, v28\n\t"
    "v_mfma_f32_16x16x32_bf16 v[12:15], a[68:71], v[164:167], v[12:15]\n\t"
    "v_cvt_pk_bf16_f32 v32, v20, v21\n\t"
    "v_cvt_pk_bf16_f32 v33, v22, v23\n\t"
    "v_mfma_f32_16x16x32_bf16 v[12:15], a[60:63], v[168:171], v[12:15]\n\t"
    "v_exp_f32_e32 v42, v42\n\t"
    "v_mfma_f32_16x16x32_bf16 v[12:15], a[52:55], v[172:175], v[12:15]\n\t"
    "v_sub_f32_e32 v43, v43, v28\n\t"
    "v_sub_f32_e32 v16, v16, v28\n\t"
    "v_mfma_f32_16x16x32_bf16 v[20:23], a[96:99], v[172:175], 0\n\t"
    "v_exp_f32_e32 v43, v43\n\t"
    "v_mfma_f32_16x16x32_bf16 v[20:23], a[104:107], v[168:171], v[20:23]\n\t"
    "v_add_f32_e32 v31, v42, v43\n\t"
    "v_sub_f32_e32 v17, v17, v28\n\t"
    "v_mfma_f32_16x16x32_bf16 v[20:23], a[112:115], v[164:167], v[20:23]\n\t"
    "v_exp_f32_e32 v16, v16\n\t"
    "v_mfma_f32_16x16x32_bf16 v[20:23], a[120:123], v[160:163], v[20:23]\n\t"
    "v_cvt_pk_bf16_f32 v34, v12, v13\n\t"
    "v_cvt_pk_bf16_f32 v35, v14, v15\n\t"
    "v_mfma_f32_16x16x32_bf16 v[20:23], a[128:131], v[156:159], v[20:23]\n\t"
    "v_add_f32_e32 v30, v30, v16\n\t"
    "v_sub_f32_e32 v18, v18, v28\n\t"
    "v_mfma_f32_16x16x32_bf16 v[20:23], a[136:139], v[152:155], v[20:23]\n\t"
    "v_exp_f32_e32 v17, v17\n\t"
    "v_mfma_f32_16x16x32_bf16 v[12:15], a[140:143], v[152:155], 0\n\t"
    "v_add_f32_e32 v30, v30, v17\n\t"
    "v_sub_f32_e32 v19, v19, v28\n\t"
    "v_mfma_f32_16x16x32_bf16 v[12:15], a[132:135], v[156:159], v[12:15]\n\t"
    "v_exp_f32_e32 v18, v18\n\t"
    "v_mfma_f32_16x16x32_bf16 v[12:15], a[124:127], v[160:163], v[12:15]\n\t"
    "v_add_f32_e32 v31, v31, v18\n\t"
    "v_sub_f32_e32 v44, v44, v28\n\t"
    "v_mfma_f32_16x16x32_bf16 v[12:15], a[116:119], v[164:167], v[12:15]\n\t"
    "v_cvt_pk_bf16_f32 v36, v20, v21\n\t"
    "v_cvt_pk_bf16_f32 v37, v22, v23\n\t"
    "v_mfma_f32_16x16x32_bf16 v[12:15], a[108:111], v[168:171], v[12:15]\n\t"
    "v_exp_f32_e32 v19, v19\n\t"
    "v_mfma_f32_16x16x32_bf16 v[12:15], a[100:103], v[172:175], v[12:15]\n\t"
    "v_add_f32_e32 v31, v31, v19\n\t"
    "v_sub_f32_e32 v45, v45, v28\n\t"
    "v_mfma_f32_16x16x32_bf16 v[20:23], a[144:147], v[172:175], 0\n\t"
    "v_exp_f32_e32 v44, v44\n\t"
    "v_mfma_f32_16x16x32_bf16 v[20:23], a[152:155], v[168:171], v[20:23]\n\t"
    "v_add_f32_e32 v30, v30, v44\n\t"
    "v_sub_f32_e32 v46, v46, v28\n\t"
    "v_mfma_f32_16x16x32_bf16 v[20:23], a[160:163], v[164:167], v[20:23]\n\t"
    "s_add_u32 m0, s63, 21504\n\t"
    "v_sub_f32_e32 v47, v47, v28\n\t"
    "v_mfma_f32_16x16x32_bf16 v[20:23], a[168:171], v[160:163], v[20:23]\n\t"
    "buffer_load_dwordx4 v7, s[52:55], s61 offen offset:0 nt lds\n\t"
    "v_cvt_pk_bf16_f32 v38, v12, v13\n\t"
    "v_mfma_f32_16x16x32_bf16 v[20:23], a[176:179], v[156:159], v[20:23]\n\t"
    "v_cvt_pk_bf16_f32 v39, v14, v15\n\t"
    "v_sub_f32_e32 v52, v52, v28\n\t"
    "v_mfma_f32_16x16x32_bf16 v[20:23], a[184:187], v[152:155], v[20:23]\n\t"
    "buffer_load_dwordx4 v7, s[52:55], s61 offen offset:1024 nt lds\n\t"
    "v_sub_f32_e32 v53, v53, v28\n\t"
    "v_mfma_f32_16x16x32_bf16 v[56:59], v[220:223], v[24:27], 0\n\t"
    "v_exp_f32_e32 v45, v45\n\t"
    "v_mfma_f32_16x16x32_bf16 v[56:59], v[216:219], v[32:35], v[56:59]\n\t"
    "buffer_load_dwordx4 v7, s[52:55], s61 offen offset:2048 nt lds\n\t"
    "v_add_f32_e32 v30, v30, v45\n\t"
    "v_mfma_f32_16x16x32_bf16 v[56:59], v[212:215], v[36:39], v[56:59]\n\t"
    "s_add_u32 s61, s61, 11206656\n\t"
    "v_sub_f32_e32 v54, v54, v28\n\t"
    "v_mfma_f32_16x16x32_bf16 v[60:63], v[188:191], v[36:39], 0\n\t"
    "v_exp_f32_e32 v46, v46\n\t"
    "v_mfma_f32_16x16x32_bf16 v[60:63], v[192:195], v[32:35], v[60:63]\n\t"
    "v_add_f32_e32 v31, v31, v46\n\t"
    "v_sub_f32_e32 v55, v55, v28\n\t"
    "v_mfma_f32_16x16x32_bf16 v[60:63], v[196:199], v[24:27], v[60:63]\n\t"
    "v_exp_f32_e32 v47, v47\n\t"
    "v_mfma_f32_16x16x32_bf16 v[64:67], v[172:175], v[24:27], 0\n\t"
    "v_add_f32_e32 v31, v31, v47\n\t"
    "v_cvt_pk_bf16_f32 v12, v20, v21\n\t"
    "v_mfma_f32_16x16x32_bf16 v[64:67], v[168:171], v[32:35], v[64:67]\n\t"
    "v_exp_f32_e32 v52, v52\n\t"
    "v_mfma_f32_16x16x32_bf16 v[64:67], v[164:167], v[36:39], v[64:67]\n\t"
    "v_add_f32_e32 v30, v30, v52\n\t"
    "v_cvt_pk_bf16_f32 v13, v22, v23\n\t"
    "v_mfma_f32_16x16x32_bf16 v[68:71], v[140:143], v[36:39], 0\n\t"
    "v_exp_f32_e32 v53, v53\n\t"
    "v_mfma_f32_16x16x32_bf16 v[68:71], v[144:147], v[32:35], v[68:71]\n\t"
    "v_add_f32_e32 v30, v30, v53\n\t"
    "v_mfma_f32_16x16x32_bf16 v[68:71], v[148:151], v[24:27], v[68:71]\n\t"
    "v_exp_f32_e32 v54, v54\n\t"
    "v_mfma_f32_16x16x32_bf16 v[20:23], a[188:191], v[152:155], 0\n\t"
    "v_add_f32_e32 v31, v31, v54\n\t"
    "v_mfma_f32_16x16x32_bf16 v[20:23], a[180:183], v[156:159], v[20:23]\n\t"
    "v_exp_f32_e32 v55, v55\n\t"
    "v_mfma_f32_16x16x32_bf16 v[20:23], a[172:175], v[160:163], v[20:23]\n\t"
    "v_add_f32_e32 v31, v31, v55\n\t"
    "v_add_f32_e32 v30, v30, v31\n\t"
    "v_mfma_f32_16x16x32_bf16 v[20:23], a[164:167], v[164:167], v[20:23]\n\t"
    "v_mov_b32_e32 v31, v30\n\t"
    "v_mfma_f32_16x16x32_bf16 v[20:23], a[156:159], v[168:171], v[20:23]\n\t"
    "v_mfma_f32_16x16x32_bf16 v[20:23], a[148:151], v[172:175], v[20:23]\n\t"
    "v_permlane32_swap_b32 v30, v31\n\t"
    "v_add_f32_e32 v30, v30, v31\n\t"
    "v_mfma_f32_16x16x32_bf16 v[24:27], a[192:195], v[172:175], 0\n\t"
    "v_mov_b32_e32 v31, v30\n\t"
    "v_mfma_f32_16x16x32_bf16 v[24:27], a[200:203], v[168:171], v[24:27]\n\t"
    "v_mfma_f32_16x16x32_bf16 v[24:27], a[208:211], v[164:167], v[24:27]\n\t"
    "v_permlane16_swap_b32 v30, v31\n\t"
    "v_add_f32_e32 v30, v30, v31\n\t"
    "v_mfma_f32_16x16x32_bf16 v[24:27], a[216:219], v[160:163], v[24:27]\n\t"
    "v_cvt_pk_bf16_f32 v14, v20, v21\n\t"
    "v_cvt_pk_bf16_f32 v15, v22, v23\n\t"
    "v_mfma_f32_16x16x32_bf16 v[24:27], a[224:227], v[156:159], v[24:27]\n\t"
    "v_rcp_f32_e32 v30, v30\n\t"
    "v_mfma_f32_16x16x32_bf16 v[24:27], a[232:235], v[152:155], v[24:27]\n\t"
    "v_mul_f32_e32 v40, v40, v30\n\t"
    "v_mul_f32_e32 v41, v41, v30\n\t"
    "v_mfma_f32_16x16x32_bf16 v[32:35], a[236:239], v[152:155], 0\n\t"
    "v_cvt_pk_bf16_f32 v20, v40, v41\n\t"
    "v_mul_f32_e32 v42, v42, v30\n\t"
    "v_mfma_f32_16x16x32_bf16 v[32:35], a[228:231], v[156:159], v[32:35]\n\t"
    "v_mul_f32_e32 v43, v43, v30\n\t"
    "v_cvt_pk_bf16_f32 v21, v42, v43\n\t"
    "v_mfma_f32_16x16x32_bf16 v[32:35], a[220:223], v[160:163], v[32:35]\n\t"
    "v_mul_f32_e32 v16, v16, v30\n\t"
    "v_mul_f32_e32 v17, v17, v30\n\t"
    "v_mfma_f32_16x16x32_bf16 v[32:35], a[212:215], v[164:167], v[32:35]\n\t"
    "v_cvt_pk_bf16_f32 v36, v24, v25\n\t"
    "v_cvt_pk_bf16_f32 v37, v26, v27\n\t"
    "v_mfma_f32_16x16x32_bf16 v[32:35], a[204:207], v[168:171], v[32:35]\n\t"
    "v_cvt_pk_bf16_f32 v22, v16, v17\n\t"
    "v_mul_f32_e32 v18, v18, v30\n\t"
    "v_mfma_f32_16x16x32_bf16 v[32:35], a[196:199], v[172:175], v[32:35]\n\t"
    "v_mul_f32_e32 v19, v19, v30\n\t"
    "v_cvt_pk_bf16_f32 v23, v18, v19\n\t"
    "v_mfma_f32_16x16x32_bf16 v[24:27], a[240:243], v[172:175], 0\n\t"
    "v_mul_f32_e32 v44, v44, v30\n\t"
    "v_mul_f32_e32 v45, v45, v30\n\t"
    "v_mfma_f32_16x16x32_bf16 v[24:27], a[248:251], v[168:171], v[24:27]\n\t"
    "v_cvt_pk_bf16_f32 v40, v44, v45\n\t"
    "v_mul_f32_e32 v46, v46, v30\n\t"
    "v_mfma_f32_16x16x32_bf16 v[24:27], v[224:227], v[164:167], v[24:27]\n\t"
    "v_mul_f32_e32 v47, v47, v30\n\t"
    "v_cvt_pk_bf16_f32 v41, v46, v47\n\t"
    "v_mfma_f32_16x16x32_bf16 v[24:27], v[232:235], v[160:163], v[24:27]\n\t"
    "v_cvt_pk_bf16_f32 v38, v32, v33\n\t"
    "v_cvt_pk_bf16_f32 v39, v34, v35\n\t"
    "v_mfma_f32_16x16x32_bf16 v[24:27], v[240:243], v[156:159], v[24:27]\n\t"
    "v_mul_f32_e32 v52, v52, v30\n\t"
    "v_mul_f32_e32 v53, v53, v30\n\t"
    "v_mfma_f32_16x16x32_bf16 v[24:27], v[248:251], v[152:155], v[24:27]\n\t"
    "v_cvt_pk_bf16_f32 v42, v52, v53\n\t"
    "v_mul_f32_e32 v54, v54, v30\n\t"
    "v_mfma_f32_16x16x32_bf16 v[16:19], v[252:255], v[152:155], 0\n\t"
    "v_mul_f32_e32 v55, v55, v30\n\t"
    "v_cvt_pk_bf16_f32 v43, v54, v55\n\t"
    "v_mfma_f32_16x16x32_bf16 v[16:19], v[244:247], v[156:159], v[16:19]\n\t"
    "v_mfma_f32_16x16x32_bf16 v[16:19], v[236:239], v[160:163], v[16:19]\n\t"
    "v_mfma_f32_16x16x32_bf16 v[16:19], v[228:231], v[164:167], v[16:19]\n\t"
    "v_cvt_pk_bf16_f32 v28, v24, v25\n\t"
    "v_cvt_pk_bf16_f32 v29, v26, v27\n\t"
    "v_mfma_f32_16x16x32_bf16 v[16:19], a[252:255], v[168:171], v[16:19]\n\t"
    "v_mfma_f32_16x16x32_bf16 v[16:19], a[244:247], v[172:175], v[16:19]\n\t"
    "v_mfma_f32_16x16x32_bf16 v[24:27], a[0:3], v[148:151], 0\n\t"
    "ds_write_b128 v6, v[20:23] offset:2048\n\t"
    "v_mfma_f32_16x16x32_bf16 v[24:27], a[8:11], v[144:147], v[24:27]\n\t"
    "v_mfma_f32_16x16x32_bf16 v[24:27], a[16:19], v[140:143], v[24:27]\n\t"
    "s_add_u32 m0, s62, 0\n\t"
    "v_mfma_f32_16x16x32_bf16 v[24:27], a[24:27], v[136:139], v[24:27]\n\t"
    "buffer_load_dwordx4 v7, s[52:55], s61 offen offset:0 nt lds\n\t"
    "v_cvt_pk_bf16_f32 v30, v16, v17\n\t"
    "v_mfma_f32_16x16x32_bf16 v[24:27], a[32:35], v[132:135], v[24:27]\n\t"
    "ds_write_b128 v6, v[40:43] offset:3072\n\t"
    "v_mfma_f32_16x16x32_bf16 v[24:27], a[40:43], v[128:131], v[24:27]\n\t"
    "buffer_load_dwordx4 v7, s[52:55], s61 offen offset:1024 nt lds\n\t"
    "v_cvt_pk_bf16_f32 v31, v18, v19\n\t"
    "v_mfma_f32_16x16x32_bf16 v[56:59], v[208:211], v[12:15], v[56:59]\n\t"
    "v_mfma_f32_16x16x32_bf16 v[56:59], v[204:207], v[36:39], v[56:59]\n\t"
    "buffer_load_dwordx4 v7, s[52:55], s61 offen offset:2048 nt lds\n\t"
    "v_mfma_f32_16x16x32_bf16 v[56:59], v[200:203], v[28:31], v[56:59]\n\t"
    "s_add_u32 s61, s61, 196608\n\t"
    "v_mfma_f32_16x16x32_bf16 v[60:63], v[176:179], v[28:31], v[60:63]\n\t"
    "v_cvt_pk_bf16_f32 v40, v24, v25\n\t"
    "v_cvt_pk_bf16_f32 v41, v26, v27\n\t"
    "v_mfma_f32_16x16x32_bf16 v[60:63], v[180:183], v[36:39], v[60:63]\n\t"
    "v_mfma_f32_16x16x32_bf16 v[60:63], v[184:187], v[12:15], v[60:63]\n\t"
    "v_mfma_f32_16x16x32_bf16 v[64:67], v[160:163], v[12:15], v[64:67]\n\t"
    "v_max3_f32 v72, v56, v57, v58\n\t"
    "v_mfma_f32_16x16x32_bf16 v[64:67], v[156:159], v[36:39], v[64:67]\n\t"
    "v_mfma_f32_16x16x32_bf16 v[64:67], v[152:155], v[28:31], v[64:67]\n\t"
    "v_mfma_f32_16x16x32_bf16 v[68:71], v[128:131], v[28:31], v[68:71]\n\t"
    "v_max3_f32 v73, v59, v60, v61\n\t"
    "v_max3_f32 v72, v72, v62, v63\n\t"
    "v_mfma_f32_16x16x32_bf16 v[68:71], v[132:135], v[36:39], v[68:71]\n\t"
    "v_mfma_f32_16x16x32_bf16 v[68:71], v[136:139], v[12:15], v[68:71]\n\t"
    "v_mfma_f32_16x16x32_bf16 v[16:19], a[44:47], v[128:131], 0\n\t"
    "v_max3_f32 v73, v73, v64, v65\n\t"
    "v_max3_f32 v72, v72, v66, v67\n\t"
    "v_mfma_f32_16x16x32_bf16 v[16:19], a[36:39], v[132:135], v[16:19]\n\t"
    "v_mfma_f32_16x16x32_bf16 v[16:19], a[28:31], v[136:139], v[16:19]\n\t"
    "v_mfma_f32_16x16x32_bf16 v[16:19], a[20:23], v[140:143], v[16:19]\n\t"
    "v_max3_f32 v73, v73, v68, v69\n\t"
    "v_max3_f32 v72, v72, v70, v71\n\t"
    "v_mfma_f32_16x16x32_bf16 v[16:19], a[12:15], v[144:147], v[16:19]\n\t"
    "v_max_f32_e32 v72, v72, v73\n\t"
    "v_mov_b32_e32 v73, v72\n\t"
    "v_mfma_f32_16x16x32_bf16 v[16:19], a[4:7], v[148:151], v[16:19]\n\t"
    "v_mfma_f32_16x16x32_bf16 v[12:15], a[48:51], v[148:151], 0\n\t"
    "v_permlane32_swap_b32 v72, v73\n\t"
    "v_max_f32_e32 v72, v72, v73\n\t"
    "v_mfma_f32_16x16x32_bf16 v[12:15], a[56:59], v[144:147], v[12:15]\n\t"
    "v_mov_b32_e32 v73, v72\n\t"
    "v_mfma_f32_16x16x32_bf16 v[12:15], a[64:67], v[140:143], v[12:15]\n\t"
    "v_mfma_f32_16x16x32_bf16 v[12:15], a[72:75], v[136:139], v[12:15]\n\t"
    "v_cvt_pk_bf16_f32 v42, v16, v17\n\t"
    "v_cvt_pk_bf16_f32 v43, v18, v19\n\t"
    "v_mfma_f32_16x16x32_bf16 v[12:15], a[80:83], v[132:135], v[12:15]\n\t"
    "v_permlane16_swap_b32 v72, v73\n\t"
    "v_max_f32_e32 v72, v72, v73\n\t"
    "v_mfma_f32_16x16x32_bf16 v[12:15], a[88:91], v[128:131], v[12:15]\n\t"
    "v_sub_f32_e32 v56, v56, v72\n\t"
    "v_sub_f32_e32 v57, v57, v72\n\t"
    "v_mfma_f32_16x16x32_bf16 v[16:19], a[92:95], v[128:131], 0\n\t"
    "v_exp_f32_e32 v56, v56\n\t"
    "v_mfma_f32_16x16x32_bf16 v[16:19], a[84:87], v[132:135], v[16:19]\n\t"
    "v_exp_f32_e32 v57, v57\n\t"
    "v_mfma_f32_16x16x32_bf16 v[16:19], a[76:79], v[136:139], v[16:19]\n\t"
    "v_add_f32_e32 v74, v56, v57\n\t"
    "v_sub_f32_e32 v58, v58, v72\n\t"
    "v_mfma_f32_16x16x32_bf16 v[16:19], a[68:71], v[140:143], v[16:19]\n\t"
    "v_cvt_pk_bf16_f32 v44, v12, v13\n\t"
    ".Lh6_%=:\n\t"
    "s_cbranch_execz .LrN_%=\n\t"
    "v_cvt_pk_bf16_f32 v45, v14, v15\n\t"
    "v_mfma_f32_16x16x32_bf16 v[16:19], a[60:63], v[144:147], v[16:19]\n\t"
    "v_exp_f32_e32 v58, v58\n\t"
    "v_mfma_f32_16x16x32_bf16 v[16:19], a[52:55], v[148:151], v[16:19]\n\t"
    "v_sub_f32_e32 v59, v59, v72\n\t"
    "v_sub_f32_e32 v60, v60, v72\n\t"
    "v_mfma_f32_16x16x32_bf16 v[12:15], a[96:99], v[148:151], 0\n\t"
    "v_exp_f32_e32 v59, v59\n\t"
    "v_mfma_f32_16x16x32_bf16 v[12:15], a[104:107], v[144:147], v[12:15]\n\t"
    "v_add_f32_e32 v75, v58, v59\n\t"
    "v_sub_f32_e32 v61, v61, v72\n\t"
    "v_mfma_f32_16x16x32_bf16 v[12:15], a[112:115], v[140:143], v[12:15]\n\t"
    "v_exp_f32_e32 v60, v60\n\t"
    "v_mfma_f32_16x16x32_bf16 v[12:15], a[120:123], v[136:139], v[12:15]\n\t"
    "v_cvt_pk_bf16_f32 v46, v16, v17\n\t"
    "v_cvt_pk_bf16_f32 v47, v18, v19\n\t"
    "v_mfma_f32_16x16x32_bf16 v[12:15], a[128:131], v[132:135], v[12:15]\n\t"
    "v_add_f32_e32 v74, v74, v60\n\t"
    "v_sub_f32_e32 v62, v62, v72\n\t"
    "v_mfma_f32_16x16x32_bf16 v[12:15], a[136:139], v[128:131], v[12:15]\n\t"
    "v_exp_f32_e32 v61, v61\n\t"
    "v_mfma_f32_16x16x32_bf16 v[16:19], a[140:143], v[128:131], 0\n\t"
    "v_add_f32_e32 v74, v74, v61\n\t"
    "v_sub_f32_e32 v63, v63, v72\n\t"
    "v_mfma_f32_16x16x32_bf16 v[16:19], a[132:135], v[132:135], v[16:19]\n\t"
    "v_exp_f32_e32 v62, v62\n\t"
    "v_mfma_f32_16x16x32_bf16 v[16:19], a[124:127], v[136:139], v[16:19]\n\t"
    "v_add_f32_e32 v75, v75, v62\n\t"
    "v_sub_f32_e32 v64, v64, v72\n\t"
    "v_mfma_f32_16x16x32_bf16 v[16:19], a[116:119], v[140:143], v[16:19]\n\t"
    "v_cvt_pk_bf16_f32 v32, v12, v13\n\t"
    "v_cvt_pk_bf16_f32 v33, v14, v15\n\t"
    "v_mfma_f32_16x16x32_bf16 v[16:19], a[108:111], v[144:147], v[16:19]\n\t"
    "v_exp_f32_e32 v63, v63\n\t"
    "v_mfma_f32_16x16x32_bf16 v[16:19], a[100:103], v[148:151], v[16:19]\n\t"
    "v_add_f32_e32 v75, v75, v63\n\t"
    "v_sub_f32_e32 v65, v65, v72\n\t"
    "v_mfma_f32_16x16x32_bf16 v[48:51], a[144:147], v[148:151], 0\n\t"
    "v_exp_f32_e32 v64, v64\n\t"
    "v_mfma_f32_16x16x32_bf16 v[48:51], a[152:155], v[144:147], v[48:51]\n\t"
    "v_add_f32_e32 v74, v74, v64\n\t"
    "v_sub_f32_e32 v66, v66, v72\n\t"
    "s_branch .Lmid_%=\n\t"
    ".Ltail_%=:\n\t"
    "v_mbcnt_lo_u32_b32 v8, -1, 0\n\t"
    "v_mbcnt_hi_u32_b32 v8, -1, v8\n\t"
    "v_lshlrev_b32_e32 v8, 5, v8\n\t"
    "s_nop 7\n\t"
    "s_nop 1\n\t"
    "ds_write_b128 v6, v[44:47] offset:4096\n\t"
    "v_cvt_pk_bf16_f32 v62, v56, v57\n\t"
    "ds_write_b128 v6, v[52:55] offset:5120\n\t"
    "v_cvt_pk_bf16_f32 v63, v58, v59\n\t"
    "s_nop 1\n\t"
    "v_mfma_f32_16x16x32_bf16 v[12:15], v[208:211], v[36:39], v[12:15]\n\t"
    "s_nop 1\n\t"
    "v_mfma_f32_16x16x32_bf16 v[12:15], v[204:207], v[48:51], v[12:15]\n\t"
    "s_nop 1\n\t"
    "v_mfma_f32_16x16x32_bf16 v[12:15], v[200:203], v[60:63], v[12:15]\n\t"
    "s_nop 1\n\t"
    "v_mfma_f32_16x16x32_bf16 v[16:19], v[176:179], v[60:63], v[16:19]\n\t"
    "s_nop 1\n\t"
    "v_mfma_f32_16x16x32_bf16 v[16:19], v[180:183], v[48:51], v[16:19]\n\t"
    "s_waitcnt lgkmcnt(0)\n\t"
    "s_barrier\n\t"
    "s_nop 1\n\t"
    "v_mfma_f32_16x16x32_bf16 v[16:19], v[184:187], v[36:39], v[16:19]\n\t"
    "s_nop 1\n\t"
    "v_mfma_f32_16x16x32_bf16 v[20:23], v[160:163], v[36:39], v[20:23]\n\t"
    "v_max3_f32 v52, v12, v13, v14\n\t"
    "s_nop 1\n\t"
    "v_mfma_f32_16x16x32_bf16 v[20:23], v[156:159], v[48:51], v[20:23]\n\t"
    "s_nop 1\n\t"
    "v_mfma_f32_16x16x32_bf16 v[20:23], v[152:155], v[60:63], v[20:23]\n\t"
    "s_nop 1\n\t"
    "v_mfma_f32_16x16x32_bf16 v[24:27], v[128:131], v[60:63], v[24:27]\n\t"
    "v_max3_f32 v53, v15, v16, v17\n\t"
    "v_max3_f32 v52, v52, v18, v19\n\t"
    "s_nop 1\n\t"
    "v_mfma_f32_16x16x32_bf16 v[24:27], v[132:135], v[48:51], v[24:27]\n\t"
    "s_nop 1\n\t"
    "v_mfma_f32_16x16x32_bf16 v[24:27], v[136:139], v[36:39], v[24:27]\n\t"
    "s_nop 7\n\t"
    "s_nop 1\n\t"
    "v_max3_f32 v53, v53, v20, v21\n\t"
    "v_max3_f32 v52, v52, v22, v23\n\t"
    "v_max3_f32 v53, v53, v24, v25\n\t"
    "v_max3_f32 v52, v52, v26, v27\n\t"
    "v_max_f32_e32 v52, v52, v53\n\t"
    "v_mov_b32_e32 v53, v52\n\t"
    "s_nop 1\n\t"
    "v_permlane32_swap_b32 v52, v53\n\t"
    "v_max_f32_e32 v52, v52, v53\n\t"
    "v_mov_b32_e32 v53, v52\n\t"
    "s_nop 1\n\t"
    "v_permlane16_swap_b32 v52, v53\n\t"
    "v_max_f32_e32 v52, v52, v53\n\t"
    "v_sub_f32_e32 v12, v12, v52\n\t"
    "v_sub_f32_e32 v13, v13, v52\n\t"
    "v_sub_f32_e32 v14, v14, v52\n\t"
    "v_exp_f32_e32 v12, v12\n\t"
    "s_nop 0\n\t"
    "v_sub_f32_e32 v15, v15, v52\n\t"
    "v_exp_f32_e32 v13, v13\n\t"
    "s_nop 0\n\t"
    "v_add_f32_e32 v54, v12, v13\n\t"
    "v_sub_f32_e32 v16, v16, v52\n\t"
    "v_exp_f32_e32 v14, v14\n\t"
    "s_nop 0\n\t"
    "v_exp_f32_e32 v15, v15\n\t"
    "s_nop 0\n\t"
    "v_add_f32_e32 v55, v14, v15\n\t"
    "v_sub_f32_e32 v17, v17, v52\n\t"
    "v_exp_f32_e32 v16, v16\n\t"
    "s_nop 0\n\t"
    "v_add_f32_e32 v54, v54, v16\n\t"
    "v_sub_f32_e32 v18, v18, v52\n\t"
    "v_exp_f32_e32 v17, v17\n\t"
    "s_nop 0\n\t"
    "v_add_f32_e32 v54, v54, v17\n\t"
    "v_sub_f32_e32 v19, v19, v52\n\t"
    "v_exp_f32_e32 v18, v18\n\t"
    "s_nop 0\n\t"
    "v_add_f32_e32 v55, v55, v18\n\t"
    "v_sub_f32_e32 v20, v20, v52\n\t"
    "v_exp_f32_e32 v19, v19\n\t"
    "s_nop 0\n\t"
    "v_add_f32_e32 v55, v55, v19\n\t"
    "v_exp_f32_e32 v20, v20\n\t"
    "s_nop 0\n\t"
    "v_add_f32_e32 v54, v54, v20\n\t"
    "v_sub_f32_e32 v21, v21, v52\n\t"
    "v_sub_f32_e32 v22, v22, v52\n\t"
    "v_exp_f32_e32 v21, v21\n\t"
    "s_nop 0\n\t"
    "v_add_f32_e32 v54, v54, v21\n\t"
    "v_exp_f32_e32 v22, v22\n\t"
    "s_nop 0\n\t"
    "v_add_f32_e32 v55, v55, v22\n\t"
    "v_sub_f32_e32 v23, v23, v52\n\t"
    "v_sub_f32_e32 v24, v24, v52\n\t"
    "v_exp_f32_e32 v23, v23\n\t"
    "s_nop 0\n\t"
    "v_add_f32_e32 v55, v55, v23\n\t"
    "v_exp_f32_e32 v24, v24\n\t"
    "s_nop 0\n\t"
    "v_add_f32_e32 v54, v54, v24\n\t"
    "v_sub_f32_e32 v25, v25, v52\n\t"
    "v_sub_f32_e32 v26, v26, v52\n\t"
    "v_sub_f32_e32 v27, v27, v52\n\t"
    "v_exp_f32_e32 v25, v25\n\t"
    "s_nop 0\n\t"
    "v_add_f32_e32 v54, v54, v25\n\t"
    "v_exp_f32_e32 v26, v26\n\t"
    "s_nop 0\n\t"
    "v_add_f32_e32 v55, v55, v26\n\t"
    "v_exp_f32_e32 v27, v27\n\t"
    "s_nop 0\n\t"
    "v_add_f32_e32 v55, v55, v27\n\t"
    "v_add_f32_e32 v54, v54, v55\n\t"
    "v_mov_b32_e32 v55, v54\n\t"
    "s_nop 1\n\t"
    "v_permlane32_swap_b32 v54, v55\n\t"
    "v_add_f32_e32 v54, v54, v55\n\t"
    "v_mov_b32_e32 v55, v54\n\t"
    "s_nop 1\n\t"
    "v_permlane16_swap_b32 v54, v55\n\t"
    "v_add_f32_e32 v54, v54, v55\n\t"
    "v_rcp_f32_e32 v54, v54\n\t"
    "s_nop 0\n\t"
    "v_mul_f32_e32 v12, v12, v54\n\t"
    "v_mul_f32_e32 v13, v13, v54\n\t"
    "v_cvt_pk_bf16_f32 v60, v12, v13\n\t"
    "v_mul_f32_e32 v14, v14, v54\n\t"
    "v_mul_f32_e32 v15, v15, v54\n\t"
    "v_cvt_pk_bf16_f32 v61, v14, v15\n\t"
    "v_mul_f32_e32 v16, v16, v54\n\t"
    "v_mul_f32_e32 v17, v17, v54\n\t"
    "v_cvt_pk_bf16_f32 v62, v16, v17\n\t"
    "v_mul_f32_e32 v18, v18, v54\n\t"
    "v_mul_f32_e32 v19, v19, v54\n\t"
    "v_cvt_pk_bf16_f32 v63, v18, v19\n\t"
    "v_mul_f32_e32 v20, v20, v54\n\t"
    "v_mul_f32_e32 v21, v21, v54\n\t"
    "v_cvt_pk_bf16_f32 v44, v20, v21\n\t"
    "v_mul_f32_e32 v22, v22, v54\n\t"
    "v_mul_f32_e32 v23, v23, v54\n\t"
    "v_cvt_pk_bf16_f32 v45, v22, v23\n\t"
    "v_mul_f32_e32 v24, v24, v54\n\t"
    "v_mul_f32_e32 v25, v25, v54\n\t"
    "v_cvt_pk_bf16_f32 v46, v24, v25\n\t"
    "v_mul_f32_e32 v26, v26, v54\n\t"
    "v_mul_f32_e32 v27, v27, v54\n\t"
    "ds_write_b128 v6, v[60:63] offset:6144\n\t"
    "v_cvt_pk_bf16_f32 v47, v26, v27\n\t"
    "ds_write_b128 v6, v[44:47] offset:7168\n\t"
    "s_waitcnt lgkmcnt(0)\n\t"
    "s_barrier\n\t"
    "s_cmp_lg_u32 %[closer], 0\n\t"
    "s_cbranch_scc1 .Lx2_%=\n\t"
    "s_mov_b32 s60, 0x10000\n\t"
    "buffer_load_dwordx2 v[10:11], off, s[56:59], %[eoff] sc1\n\t"
    ".Lp3_%=:\n\t"
    "buffer_load_dwordx4 v[12:15], v8, s[56:59], %[aoff] offen sc1\n\t"
    "buffer_load_dwordx4 v[16:19], v8, s[56:59], %[aoff] offen offset:16 sc1\n\t"
    "s_waitcnt vmcnt(0)\n\t"
    "v_add_co_u32_e32 v20, vcc, 0x7f4a7c15, v10\n\t"
    "s_nop 1\n\t"
    "v_mov_b32_e32 v9, 0x9e3779b9\n\t"
    "v_addc_co_u32_e32 v21, vcc, v9, v11, vcc\n\t"
    "v_cmp_eq_u64_e32 vcc, v[12:13], v[20:21]\n\t"
    "v_cmp_eq_u64_e64 s[66:67], v[14:15], v[20:21]\n\t"
    "s_and_b64 vcc, vcc, s[66:67]\n\t"
    "v_cmp_eq_u64_e64 s[66:67], v[16:17], v[20:21]\n\t"
    "s_and_b64 vcc, vcc, s[66:67]\n\t"
    "v_cmp_eq_u64_e64 s[66:67], v[18:19], v[20:21]\n\t"
    "s_and_b64 vcc, vcc, s[66:67]\n\t"
    "s_cmp_eq_u64 vcc, exec\n\t"
    "s_cbranch_scc1 .Lk4_%=\n\t"
    "s_sleep 8\n\t"
    "s_sub_u32 s60, s60, 1\n\t"
    "s_cmp_lg_u32 s60, 0\n\t"
    "s_cbranch_scc1 .Lp3_%=\n\t"
    ".Lk4_%=:\n\t"
    "s_mov_b64 exec, 1\n\t"
    "buffer_store_dwordx2 v[20:21], off, s[56:59], %[eoff] sc1\n\t"
    "s_mov_b64 exec, -1\n\t"
    ".Lx2_%=:\n\t"
    "s_endpgm\n\t"
    ".Lend_%=:\n\t"
    :: "{v31}"(lane), "{s[36:43]}"(k8), "{s[44:47]}"(k4),
       [role] "s"(role), [pair] "s"(pair), [prod] "s"((fid - 144) >> 31), [closer] "s"(bid | wave), [fid] "s"(fid), [wb] "s"(wb),
       [rbeg] "s"(rbeg), [rend] "s"(rbeg + 3 * SLOT), [pl] "s"(ldsb + PB + pair * 8192), [amask] "s"((unsigned long long)pair),
       [tch] "s"(((bid >> 3) * 2 + pair) * 1152), [foff] "s"(F_OFF + 64 * fid), [aoff] "s"(A_OFF), [aown] "s"(A_OFF + 8 * bid), [eoff] "s"(E_OFF)
    : "memory", "scc", ACLOB, VCLOB, SCLOB);
}

extern "C" void kernel_launch(void* const* d_in, const int* in_sizes, int n_in,
                              void* d_out, int out_size, void* d_ws, size_t ws_size,
                              hipStream_t stream) {
  attn_kernel<<<256, 256, 0, stream>>>((const unsigned char*)d_in[0], (const unsigned char*)d_in[1], (const unsigned char*)d_in[2],
                                       (unsigned char*)d_ws, (unsigned char*)d_out
      );
}
```
